```python
import torch

def _softmax(x, axis=-1):
    return torch.softmax(x, dim=axis)

def _gelu(x):
    return torch.nn.functional.gelu(x, approximate='tanh')

def _silu(x):
    return torch.nn.functional.silu(x)

def _sigmoid(x):
    return torch.sigmoid(x)

def _layernorm(x, axis=-1, eps=1e-05):
    m = x.mean(dim=axis, keepdim=True)
    v = x.var(dim=axis, keepdim=True, unbiased=False)
    return (x - m) / torch.sqrt(v + eps)

def _scatter_add(tgt, idx, src, axis=0):
    return tgt.clone().index_add_(axis, idx, src)

def _segment_sum(src, seg, n):
    out = torch.zeros((n,) + src.shape[1:], dtype=src.dtype)
    return out.index_add_(0, seg, src)

def _pad(x, pw):
    flat = tuple((v for pair in reversed(pw) for v in pair))
    return torch.nn.functional.pad(x, flat)
kernel_name = 'standalone__window_attn_davit__vision'

def setup_inputs():

    _w = lambda a: (lambda t: t.to(torch.bfloat16) if t.is_floating_point() else t)(torch.as_tensor(a))
    HW = 512
    C0 = 192
    ws = 7
    nh = 4
    H = W = max(HW // ws * ws, ws)
    C = max(C0 // nh * nh, nh)
    k = 3
    return {'x': _w((lambda *s: torch.randn(*s, dtype=torch.bfloat16) * 1.6)(1, H, W, C)), 'dw_pe': _w((lambda *s: torch.randn(*s, dtype=torch.bfloat16) * 1.6)(k, k, C) * 0.1), 'w_qkv': _w((lambda *s: torch.randn(*s, dtype=torch.bfloat16) * 1.6)(C, 3 * C) * C ** (-0.5)), 'w_o': _w((lambda *s: torch.randn(*s, dtype=torch.bfloat16) * 0.4)(C, C) * C ** (-0.5))}

def reference(x, dw_pe, w_qkv, w_o):
    B, H, W, C = x.shape
    ws = 7
    nh = 4
    dh = C // nh
    kk = dw_pe.shape[0]
    pad = kk // 2
    xp = _pad(x, ((0, 0), (pad, pad), (pad, pad), (0, 0)))
    pe = torch.zeros_like(x)
    for i in range(kk):
        for j in range(kk):
            pe = pe + xp[:, i:i + H, j:j + W, :] * dw_pe[i, j][None, None, None, :]
    x0 = x + pe
    h = _layernorm(x0, axis=-1)
    hw = h.reshape(B, H // ws, ws, W // ws, ws, C)
    hw = (lambda x, *d: x.permute(*d))(hw, 0, 1, 3, 2, 4, 5)
    hw = hw.reshape(-1, ws * ws, C)
    qkv = (hw @ w_qkv).reshape(-1, ws * ws, 3, nh, dh)
    q = qkv[:, :, 0].clone()
    k = qkv[:, :, 1].clone()
    v = qkv[:, :, 2].clone()
    scale = dh ** (-0.5)
    attn = torch.einsum('wnhd,wmhd->whnm', q, k) * scale
    attn = _softmax(attn, axis=-1)
    out = torch.einsum('whnm,wmhd->wnhd', attn, v)
    out = out.reshape(-1, ws * ws, C) @ w_o
    out = out.reshape(B, H // ws, W // ws, ws, ws, C)
    out = (lambda x, *d: x.permute(*d))(out, 0, 1, 3, 2, 4, 5)
    out = out.reshape(B, H, W, C)
    return x0 + out
```

```cpp
#include <hip/hip_runtime.h>
#include <hip/hip_bf16.h>
#include <stdint.h>

typedef short bf16x8 __attribute__((ext_vector_type(8)));
typedef float f32x4 __attribute__((ext_vector_type(4)));
typedef float f32x2 __attribute__((ext_vector_type(2)));
typedef unsigned u32x2 __attribute__((ext_vector_type(2)));
typedef unsigned u32x4 __attribute__((ext_vector_type(4)));

constexpr int IMG = 511;
constexpr int CH = 192;
constexpr int NWX = 73;
constexpr int NWIN = NWX * NWX;
constexpr int GRID = 256;
constexpr int NSHORT = 21 * GRID - NWIN;
constexpr int ROWBYTES = IMG * CH * 2;
constexpr int XBIAS = (IMG + 1) * CH * 2;
constexpr unsigned OOB = 0x40000000u;

constexpr int X_OFF = 32768;
constexpr int X1_OFF = 53632;
constexpr int O_OFF = 74496;
constexpr int V_OFF = 92928;
constexpr int KQL_OFF = 111360;
constexpr int OL_OFF = 128256;
constexpr int AT_OFF = 137472;
constexpr int CT_OFF = AT_OFF + 19584;
constexpr int RS_OFF = CT_OFF + 4096;
constexpr int WL_OFF = RS_OFF + 2304 + 64;
constexpr int LDS_BYTES = WL_OFF + 128;
#define RS_BASE (RS_OFF + (wave < 4 ? wave * 384 : 768 + wave * 192))
constexpr int STL_OFF = X1_OFF + 18816;
#define LB_OFF(b) ((b) < 7 ? V_OFF + (b) * 6160 : X1_OFF + ((b) - 7) * 6160)
#define LPOS(wc) (((wc) & 1) ? 0 : 6)
constexpr unsigned CROW = 0x060a0230u;
constexpr unsigned CLANE = 0x00132C3Fu;

constexpr unsigned long long TOKMAP[6] = {0x20001c190f2a2202ull, 0x052517121f061a13ull, 0x181e08151d0b260cull, 0x1b2b2903102d0901ull, 0x04162e0e2824142cull, 0x110a072127232f0dull};
constexpr unsigned long long ROWMAP[8] = {0x08321b240f1c0006ull, 0x23310e2a09170d14ull, 0x210c2f030a273418ull, 0x1d2b11221f043739ull, 0x3234363a103e192dull, 0x291a20022c3f1605ull, 0x303a3c2628122e15ull, 0x39253d1e01070b13ull};

constexpr int WF_BYTES = 8 * 36 * 1024;
constexpr int EP_OFF = WF_BYTES;
constexpr int CN_OFF = EP_OFF + 256, FL_OFF = EP_OFF + 512;
constexpr int WS_BYTES = EP_OFF + 1024;
constexpr int SPIN_MAX = 1 << 20;

#define MFMA32(a, b, c) __builtin_amdgcn_mfma_f32_16x16x32_bf16((a), (b), (c), 0, 0, 0)
#define BC8(v) __builtin_bit_cast(bf16x8, (v))
#define BAR() asm volatile("s_waitcnt lgkmcnt(0)\n\ts_barrier" ::: "memory")
#define SB() __builtin_amdgcn_sched_barrier(0)
#define OPQ(x) asm volatile("" : "+v"(x))
#define ZERO4 ((f32x4){0.f, 0.f, 0.f, 0.f})

typedef __bf16 nbf16x2 __attribute__((ext_vector_type(2)));
__device__ __forceinline__ unsigned pk2(float a, float b) {
    f32x2 v = {a, b};
    nbf16x2 r = __builtin_convertvector(v, nbf16x2);
    return __builtin_bit_cast(unsigned, r);
}
__device__ __forceinline__ float bflo(unsigned u) { return __uint_as_float(u << 16); }
__device__ __forceinline__ float bfhi(unsigned u) { return __uint_as_float(u & 0xffff0000u); }

__device__ __forceinline__ float bf2f(unsigned short v) { return __uint_as_float((unsigned)v << 16); }
__device__ __forceinline__ unsigned byte_of(unsigned long long a, unsigned long long b, int i) {
    return (unsigned)(((i & 8) ? b : a) >> (8 * (i & 7))) & 0xFFu;
}
__device__ __forceinline__ void seq_to_win(int seq, int& wy, int& wx) {
    const bool last = seq >= 5110;
    const int s = last ? 35 : seq / 146, r = seq - 146 * s;
    const int kx = last ? r / 6 : r >> 2, pos = r - (last ? 6 : 4) * kx;
    const bool low = last && pos >= (kx == 36 ? 2 : 4);
    wy = low ? 72 : 2 * s + (pos & 1);
    wx = 2 * kx + (low ? (kx == 36 ? 0 : pos - 4) : pos >> 1);
}
__device__ __forceinline__ int win_to_seq(int wy, int wx) {
    const int kx = wx >> 1;
    return wy == 72 ? 5110 + 6 * kx + (kx == 36 ? 2 : 4 + (wx & 1)) : 146 * (wy >> 1) + (wy >= 70 ? 6 : 4) * kx + 2 * (wx & 1) + (wy & 1);
}
__device__ __forceinline__ int block_of(int wy, int wx) { return wy >= 70 ? 1295 + (wx & ~1) + (wy == 72) : 37 * (wy >> 1) + (wx >> 1); }
__device__ __forceinline__ void block_pos(int b, int& ky, int& kx) {
    if (b >= 1295) { kx = (b - 1295) >> 1; ky = 70 + 2 * ((b - 1295) & 1); }
    else { const int s = b / 37; kx = b - 37 * s; ky = 2 * s; }
}
__device__ __forceinline__ unsigned wl_entry(int seq) {
    int wy, wx;
    seq_to_win(seq, wy, wx);
    return (unsigned)((wy * 7 * IMG + wx * 7) * (CH * 2)) | (wy == 0 ? 0x08000000u : 0u) | (wx == 0 ? 0x10000000u : 0u) |
           (wx == NWX - 1 ? 0x20000000u : 0u) | ((unsigned)(wx & 1) << 30) | ((unsigned)(wy & 1) << 31);
}
__device__ __forceinline__ unsigned dma_off(int lane) {
    const int col = (lane * 43) >> 8;
    return (unsigned)(col * (CH * 2 - 96) + 16 * lane) | (col == 0 ? 0x10000000u : 0u) | (col == 8 ? 0x20000000u : 0u);
}

__device__ __forceinline__ unsigned pf_off(int lane) {
    const int l = lane < 63 ? lane : 62, r = l / 21;
    return (unsigned)(r * ROWBYTES + (l - 21 * r) * 128) | (r == 0 ? 0x08000000u : 0u);
}
typedef __attribute__((address_space(1))) unsigned gu32;
typedef __attribute__((address_space(1))) unsigned long long gu64;
#define RLX_AGENT __ATOMIC_RELAXED, __HIP_MEMORY_SCOPE_AGENT
#define TAG_OF(e) (((unsigned long long)~((e) + 1u) << 32) | ((e) + 1u))

__device__ __forceinline__ float rstd_of(const char* p) {
    f32x4 p0 = *(const f32x4*)(p);
    f32x4 p1 = *(const f32x4*)(p + 1024);
    float s = (p0[0] + p0[2]) + (p1[0] + p1[2]);
    float q = (p0[1] + p0[3]) + (p1[1] + p1[3]);
    float mean = s * (1.f / 192.f);
    float var = __builtin_fmaf(q, 1.f / 192.f, -mean * mean);
    var = __builtin_fmaxf(var, 0.f);
    return __builtin_amdgcn_rsqf(var + 1e-5f);
}

#if defined(__HIP_DEVICE_COMPILE__)
#define NOPK __attribute__((target("no-packed-fp32-ops")))
#else
#define NOPK
#endif
__global__ __launch_bounds__(512, 2) NOPK void win_attn_kernel(const unsigned short* __restrict__ x, const unsigned short* __restrict__ dw,
                                                          const unsigned short* __restrict__ wqkv, const unsigned short* __restrict__ wo,
                                                          u32x4* wf, unsigned short* __restrict__ out) {
    __shared__ __attribute__((aligned(16))) char smem[LDS_BYTES];

    asm volatile("wk_code_begin:");
    const int tid = threadIdx.x;
    const int wave = __builtin_amdgcn_readfirstlane(tid >> 6);
    const int w = wave & 3;
#define RANGE_SETUP                                                                                \
    int bx = blockIdx.x;                                                                           \
    asm volatile("" : "+s"(bx));                                                                   \
    bx = __builtin_amdgcn_readfirstlane(bx);                                                       \
    const int q0 = (bx & 7) * 32 + (bx >> 3);                                                      \
    const int cnt = q0 < NSHORT ? 20 : 21;                                                         \
    const int start = q0 < NSHORT ? 20 * q0 : 21 * q0 - NSHORT;                                    \
    const int kq = q0;                                                                             \
    const auto wrsrc = __builtin_amdgcn_make_buffer_rsrc((void*)wf, 0, WS_BYTES, 0x00020000);
#define ROLE_SETUP                                                                                 \
    RANGE_SETUP                                                                                    \
    const unsigned en0 = __builtin_amdgcn_readfirstlane(wl_entry(start));                          \
    const auto xrsrc = __builtin_amdgcn_make_buffer_rsrc((void*)((const char*)x - XBIAS), 0, IMG * IMG * CH * 2 + XBIAS, 0x00020000);
    {
    RANGE_SETUP
    asm volatile("" :: "s"(x), "s"(dw), "s"(wqkv), "s"(wo), "s"(wf), "s"(out), "s"(cnt), "s"(kq));
    if (__builtin_expect(kq < 36, 1)) {
        int lane;
        asm volatile("v_mbcnt_lo_u32_b32 %0, -1, 0\n\tv_mbcnt_hi_u32_b32 %0, -1, %0" : "=v"(lane));
        const int n = lane & 15, g = lane >> 4;
        const bool isO = wave >= 6;
        const int fr = isO ? 210 + 2 * kq + wave : 6 * kq + wave;
        const int tile = (fr * 171) >> 10, ks = fr - 6 * tile, wv = (tile * 43) >> 8, mdt = tile - 6 * wv;
        const int ca = (mdt < 3 ? 192 : -48) + 48 * wv + 16 * mdt, cv = 96 * wv + 16 * mdt, co = 96 * wv - 576 + 32 * (mdt >> 1) + 4 * (mdt & 1);
        const int cb = isO ? co : (wv < 4 ? ca : cv), ldb = isO ? 384 : 1152;
        const auto srs = __builtin_amdgcn_make_buffer_rsrc((void*)(isO ? wo : wqkv), 0, 192 * ldb, 0x00020000);
        const int vo = (96 * (ks & 1) + 16 * (ks >> 1) + 48 * (g >> 1) + 8 * (g & 1)) * ldb + 2 * (cb + (isO ? 8 * (n >> 2) + (n & 3) : n));
        unsigned short u[8];
#pragma unroll
        for (int j = 0; j < 8; ++j) u[j] = __builtin_amdgcn_raw_buffer_load_b16(srs, vo, j * ldb, 0);
        SB();
        const unsigned ep = __hip_atomic_load((gu32*)((char*)wf + EP_OFF), RLX_AGENT);
        if (kq == 0 && wave == 7 && lane == 0) __hip_atomic_store((gu32*)((char*)wf + CN_OFF), 0u, RLX_AGENT);
        SB();
        float v[8];
#pragma unroll
        for (int j = 0; j < 8; ++j) v[j] = bf2f(u[j]);
        *(float*)(smem + O_OFF + n * 144 + (wave * 4 + g) * 4) = ((v[0] + v[1]) + (v[2] + v[3])) + ((v[4] + v[5]) + (v[6] + v[7]));
        BAR();
        f32x4 r[6];
#pragma unroll
        for (int i = 0; i < 6; ++i) r[i] = *(const f32x4*)(smem + O_OFF + n * 144 + i * 16);
        const f32x4 rs = ((r[0] + r[1]) + (r[2] + r[3])) + (r[4] + r[5]);
        const float mean = isO ? 0.f : ((rs[0] + rs[1]) + (rs[2] + rs[3])) * (1.f / 192.f);
        const u32x4 o = {pk2(v[0] - mean, v[1] - mean), pk2(v[2] - mean, v[3] - mean), pk2(v[4] - mean, v[5] - mean), pk2(v[6] - mean, v[7] - mean)};
        __builtin_amdgcn_raw_buffer_store_b128(o, wrsrc, lane * 16, fr * 1024, 16);
        asm volatile("s_waitcnt vmcnt(0)" ::: "memory");
        BAR();
        if (wave == 0 && lane == 0) __hip_atomic_store((gu64*)((char*)wf + FL_OFF) + kq, TAG_OF(ep), RLX_AGENT);
    }
    }
    bf16x8 W[36];
#define LOAD_W(j0, j1, k0, k1)                                                                     \
    _Pragma("unroll") for (int ks_ = (k0); ks_ < (k1); ++ks_)                                      \
        _Pragma("unroll") for (int j_ = (j0); j_ < (j1); ++j_)                                     \
            W[j_ * 6 + ks_] = BC8(__builtin_amdgcn_raw_buffer_load_b128(wrsrc, lane * 16, wave * 36864 + (j_ * 6 + ks_) * 1024, 16));

#define LOAD_WI(i0, i1)                                                                            \
    _Pragma("unroll") for (int i_ = (i0); i_ < (i1); ++i_)                                         \
        W[i_] = BC8(__builtin_amdgcn_raw_buffer_load_b128(wrsrc, lane * 16, wave * 36864 + i_ * 1024, 16));

#define LOAD_WL(i0, i1)                                                                            \
    _Pragma("unroll") for (int i_ = (i0); i_ < (i1); ++i_)                                         \
        W[i_] = BC8(__builtin_amdgcn_raw_buffer_load_b128(wrsrc, lane * 16, wl_ + i_ * 1024, 16));

#define LANE_SETUP                                                                                 \
    int lane;                                                                                      \
    asm volatile("v_mbcnt_lo_u32_b32 %0, -1, 0\n\tv_mbcnt_hi_u32_b32 %0, -1, %0" : "=v"(lane));    \
    __builtin_assume(lane >= 0 && lane < 64);                                                      \
    const int g = lane >> 4, l15 = lane & 15, sw = l15 >> 1;                                       \
    int fE = X_OFF + l15 * 384 + ((g ^ sw) << 4);                                                  \
    int fO = fE ^ 64;                                                                              \
    OPQ(fE); OPQ(fO);
#define FRAG(base, t, ks) (*(const bf16x8*)(smem + (((ks) & 1) ? fO : fE) + ((base) - X_OFF) + (t) * 6144 + ((ks) >> 1) * 128))
#define RSTD_PUBLISH(ln_, stoff)                                                                   \
    *(float*)(smem + ((ln_) < 48 ? RS_BASE : RS_OFF + 2304 - 192) + (ln_) * 4) = rstd_of(smem + (stoff) + (ln_) * 16);

    const unsigned ldsb = (unsigned)(size_t)smem;
#define DMA_SLICE(en)                                                                              \
        {                                                                                          \
            const unsigned vo_ = dma_off(lane) & (0x401fffffu | ((en) & 0x38000000u));             \
            const unsigned so_ = ((en) & 0x7ffffffu) + 96 * w;                                     \
            _Pragma("unroll") for (int r = 0; r < 9; ++r) {                                        \
                unsigned m0_ = ldsb + w * 8192 + r * 872;                                          \
                unsigned sr_ = (r == 0 && ((en) & 0x08000000u)) ? OOB : so_ + r * ROWBYTES;        \
                asm volatile("s_mov_b32 m0, %0\n\ts_mov_b32 exec_hi, 0x3fffff\n\tbuffer_load_dwordx4 %1, %2, %3 offen lds\n\ts_mov_b32 exec_hi, -1" \
                             :: "s"(m0_), "v"(vo_), "s"(xrsrc), "s"(sr_) : "m0", "memory");        \
            }                                                                                      \
        }
#define XPAT(r) (((r) < 48 ? (r) : 48) * 384 + (((2 * w) ^ (((r) >> 1) & 7)) << 4))
#define SPAT(r) ((w >> 1) * 1024 + (w & 1) * 8 + (r) * 16)
#define CONV_CONSTS(var) \
            const unsigned long long rlo = g == 0 ? ROWMAP[0] : (g == 1 ? ROWMAP[2] : (g == 2 ? ROWMAP[4] : ROWMAP[6])); \
            const unsigned long long rhi = g == 0 ? ROWMAP[1] : (g == 1 ? ROWMAP[3] : (g == 2 ? ROWMAP[5] : ROWMAP[7])); \
            int xrow = byte_of(rlo, rhi, l15); \
            if (lane == (int)((CLANE >> (8 * (var))) & 0xFF)) xrow = 48; \
            if (lane == 63) xrow = (CROW >> (8 * (var))) & 0xFF; \
            const unsigned pXw_ = XPAT(xrow); \
            const unsigned pSTw_ = (w >> 1) * 1024 + (w & 1) * 8 + xrow * 16; \
            const int oy = (0x5320 >> (4 * (l15 >> 2))) & 15, ox = (0x5320 >> (4 * (l15 & 3))) & 15; \
            const unsigned aB_ = ldsb + w * 8192 + (oy + (g >> 1)) * 872 + (ox + 2 * (g & 1)) * 96; \
            const int dyi = g - (l15 >> 3); \
            const unsigned aA_ = ldsb + AT_OFF + (l15 & 3) * 16 + \
                                 ((dyi < 0 || dyi > 2) ? 18816 : (dyi == 0 ? 0 : (dyi == 1 ? 6208 : 12544)) + ((l15 >> 2) & 1) * 3072 + w * 768); \
            const unsigned idx_ = (l15 & 3) * 0x5555u; \
            const unsigned se = (l15 == 4 * g) ? 0x3F803F80u : 0u; \
            const u32x4 sel4 = {se, se, se, se};

#define LANE_IDS int ln_ = lane; OPQ(ln_); const int l15_ = ln_ & 15;
#define B_PROLOGUE(ISV) \
        ROLE_SETUP \
        LANE_SETUP \
        const auto orsrc = __builtin_amdgcn_make_buffer_rsrc((void*)out, 0, IMG * IMG * CH * 2, 0x00020000); \
        __builtin_amdgcn_s_setprio(1); \
        const unsigned en1 = __builtin_amdgcn_readfirstlane(wl_entry(start + 1)); \
        unsigned ep = 0; \
        if (!(ISV)) ep = __hip_atomic_load((gu32*)((char*)wf + EP_OFF), RLX_AGENT); \
        int key0, nblk; \
        { \
            int wy, wx; \
            seq_to_win(start, wy, wx); \
            key0 = block_of(wy, wx); \
            seq_to_win(start + cnt - 1, wy, wx); \
            nblk = block_of(wy, wx) - key0 + 1; \
            asm volatile("s_barrier" ::: "memory");   \
            { \
                unsigned long long pc; \
                unsigned len, rel; \
                asm volatile("s_getpc_b64 %0\n.Lwk_here%=:\n\ts_mov_b32 %1, wk_code_end-wk_code_begin\n\ts_max_u32 %1, %1, wk_code_end1-wk_code_begin\n\ts_max_u32 %1, %1, wk_code_end2-wk_code_begin\n\ts_mov_b32 %2, .Lwk_here%=-wk_code_begin" : "=s"(pc), "=s"(len), "=s"(rel) :: "scc"); \
                pc -= rel; \
                const u32x4 cr = {(unsigned)pc, (unsigned)(pc >> 32) & 0xffffu, len, 0x00020000u}; \
                const int ci = (bx >> 3) * 4 + w; \
                unsigned m0_ = ldsb + O_OFF + 12288 + w * 1024, vo_ = lane * 16; \
                asm volatile("s_mov_b32 m0, %0\n\ts_nop 0\n\tbuffer_load_dwordx4 %1, %2, %3 offen lds" \
                             :: "s"(m0_), "v"(vo_), "s"(cr), "s"(ci * 1024) : "m0", "memory"); \
            } \
            _Pragma("unroll") for (int k = 0; k < 3; ++k) { \
                const int b = w + 4 * k; \
                if (b < nblk) { \
                    int ky, kx; \
                    block_pos(key0 + b, ky, kx); \
                    const unsigned org = (unsigned)(((7 * ky + 5) * IMG + 14 * kx + 5) * (CH * 2) + XBIAS); \
                    const unsigned org2 = (kx == 36) ? OOB : org; \
                    const unsigned vo_ = lane * 16; \
                    _Pragma("unroll") for (int r = 0; r < 4; ++r) { \
                        unsigned m0_ = ldsb + LB_OFF(b) + r * 1536; \
                        asm volatile("s_mov_b32 m0, %0\n\ts_mov_b64 exec, %5\n\tbuffer_load_dwordx4 %1, %2, %3 offen lds\n\t" \
                                     "buffer_load_dwordx4 %1, %2, %4 offen offset:768 lds\n\ts_mov_b64 exec, -1" \
                                     :: "s"(m0_), "v"(vo_), "s"(xrsrc), "s"(org + r * ROWBYTES), "s"(org2 + r * ROWBYTES), "s"(0xFFFFFFFFFFFFull) : "m0", "memory"); \
                    } \
                } \
            } \
        } \
        const int wl_ = (ISV) ? wave * 36864 : (wave - 2) * 36864 + 18432; \
        u32x4 xl[9]; \
        { \
            CONV_CONSTS(0) \
            const int b = l15 < nblk ? l15 : 0; \
            int ky, kx; \
            block_pos(key0 + b, ky, kx); \
            const int wy = ky + (g >> 1), wx = 2 * kx + (g & 1), i = win_to_seq(wy, wx) - start; \
            const bool ok = wy < NWX && wx < NWX && i >= 0 && i < cnt && l15 < nblk; \
            const int row = ok ? i : 47; \
            const unsigned lB = ldsb + LB_OFF(b) + (g >> 1) * 1536 + (g & 1) * 768 + 96 * w; \
            const unsigned lX = XPAT(row), lS = SPAT(row) + (unsigned)(STL_OFF - O_OFF - 18816); \
            const unsigned sol = ok ? (unsigned)(((7 * wy + LPOS(wy)) * IMG + 7 * wx + LPOS(wx)) * (CH * 2) + 96 * w) : OOB; \
            xl[6] = (u32x4){sol, 0u, 0u, 0u}; xl[7] = xl[8] = (u32x4){0u, 0u, 0u, 0u}; \
              \
            if (!(ISV) && w == 3) { \
                const unsigned long long tag = TAG_OF(ep); \
                gu64* fp = (gu64*)((char*)wf + FL_OFF) + (lane < 36 ? lane : 0); \
                _Pragma("unroll 1") \
                for (int it = 0; it < SPIN_MAX && __builtin_amdgcn_ballot_w64(__hip_atomic_load(fp, RLX_AGENT) != tag) != 0ull; ++it) __builtin_amdgcn_s_sleep(1); \
                __builtin_amdgcn_fence(__ATOMIC_ACQUIRE, "wavefront"); \
            } \
            asm volatile("s_waitcnt vmcnt(0)" ::: "memory");   \
            asm volatile( \
            "s_waitcnt lgkmcnt(0)\n\t" \
            "s_barrier\n\t" \
            "s_nop 7\n\t" \
            "ds_read_b128 v[168:171], v254\n\t" \
            "ds_read_b64 v[144:145], v253\n\t" \
            "ds_read_b64 v[146:147], v253 offset:384\n\t" \
            "ds_read_b64 v[148:149], v253 offset:3072\n\t" \
            "ds_read_b64 v[150:151], v253 offset:3456\n\t" \
            "ds_read_b128 v[172:175], v254 offset:64\n\t" \
            "ds_read_b64 v[152:153], v253 offset:8\n\t" \
            "ds_read_b64 v[154:155], v253 offset:392\n\t" \
            "ds_read_b64 v[156:157], v253 offset:3080\n\t" \
            "ds_read_b64 v[158:159], v253 offset:3464\n\t" \
            "ds_read_b64 v[160:161], v253 offset:16\n\t" \
            "ds_read_b64 v[162:163], v253 offset:400\n\t" \
            "ds_read_b64 v[164:165], v253 offset:3088\n\t" \
            "ds_read_b64 v[166:167], v253 offset:3472\n\t" \
            "v_mov_b64 v[176:177], 0\n\t" \
            "v_mov_b64 v[178:179], 0\n\t" \
            "v_mov_b64 v[180:181], 0\n\t" \
            "v_mov_b64 v[182:183], 0\n\t" \
            "v_mov_b64 v[184:185], 0\n\t" \
            "v_mov_b64 v[186:187], 0\n\t" \
            "v_mov_b64 v[188:189], 0\n\t" \
            "v_mov_b64 v[190:191], 0\n\t" \
            "v_add_u32 v236, s67, v250\n\t" \
            "v_xor_b32 v237, 16, v236\n\t" \
            "v_add_u32 v248, s67, v251\n\t" \
            "s_waitcnt lgkmcnt(9)\n\t" \
            "v_smfmac_f32_16x16x64_bf16 v[176:179], v[168:171], v[144:151], v255\n\t" \
            "ds_read_b128 v[168:171], v254 offset:128\n\t" \
            "ds_read_b64 v[144:145], v253 offset:24\n\t" \
            "s_waitcnt lgkmcnt(6)\n\t" \
            "v_smfmac_f32_16x16x64_bf16 v[180:183], v[172:175], v[152:159], v255\n\t" \
            "ds_read_b64 v[146:147], v253 offset:408\n\t" \
            "ds_read_b64 v[148:149], v253 offset:3096\n\t" \
            "ds_read_b64 v[150:151], v253 offset:3480\n\t" \
            "ds_read_b128 v[172:175], v254 offset:192\n\t" \
            "ds_read_b64 v[152:153], v253 offset:32\n\t" \
            "ds_read_b64 v[154:155], v253 offset:416\n\t" \
            "ds_read_b64 v[156:157], v253 offset:3104\n\t" \
            "ds_read_b64 v[158:159], v253 offset:3488\n\t" \
            "v_cvt_pk_bf16_f32 v200, v176, v177\n\t" \
            "v_cvt_pk_bf16_f32 v201, v178, v179\n\t" \
            "v_mov_b64 v[176:177], 0\n\t" \
            "v_mov_b64 v[178:179], 0\n\t" \
            "v_mfma_f32_4x4x4_16b_bf16 v[196:199], v[200:201], v[200:201], 0\n\t" \
            "v_cvt_pk_bf16_f32 v202, v180, v181\n\t" \
            "v_cvt_pk_bf16_f32 v203, v182, v183\n\t" \
            "v_mov_b64 v[180:181], 0\n\t" \
            "v_mov_b64 v[182:183], 0\n\t" \
            "v_mfma_f32_4x4x4_16b_bf16 v[196:199], v[202:203], v[202:203], v[196:199]\n\t" \
            "ds_write_b128 v236, v[200:203]\n\t" \
            "v_mfma_f32_16x16x32_bf16 v[192:195], v[238:241], v[200:203], 0\n\t" \
            "s_waitcnt lgkmcnt(10)\n\t" \
            "v_smfmac_f32_16x16x64_bf16 v[184:187], v[168:171], v[160:167], v255\n\t" \
            "ds_read_b128 v[168:171], v254 offset:256\n\t" \
            "ds_read_b64 v[160:161], v253 offset:40\n\t" \
            "ds_read_b64 v[162:163], v253 offset:424\n\t" \
            "ds_read_b64 v[164:165], v253 offset:3112\n\t" \
            "s_waitcnt lgkmcnt(9)\n\t" \
            "v_smfmac_f32_16x16x64_bf16 v[188:191], v[172:175], v[144:151], v255\n\t" \
            "ds_read_b64 v[166:167], v253 offset:3496\n\t" \
            "ds_read_b128 v[172:175], v254 offset:320\n\t" \
            "ds_read_b64 v[144:145], v253 offset:48\n\t" \
            "ds_read_b64 v[146:147], v253 offset:432\n\t" \
            "ds_read_b64 v[148:149], v253 offset:3120\n\t" \
            "ds_read_b64 v[150:151], v253 offset:3504\n\t" \
            "v_cvt_pk_bf16_f32 v204, v184, v185\n\t" \
            "v_cvt_pk_bf16_f32 v205, v186, v187\n\t" \
            "v_mov_b64 v[184:185], 0\n\t" \
            "v_mov_b64 v[186:187], 0\n\t" \
            "v_mfma_f32_4x4x4_16b_bf16 v[196:199], v[204:205], v[204:205], v[196:199]\n\t" \
            "v_cvt_pk_bf16_f32 v206, v188, v189\n\t" \
            "v_cvt_pk_bf16_f32 v207, v190, v191\n\t" \
            "v_mov_b64 v[188:189], 0\n\t" \
            "v_mov_b64 v[190:191], 0\n\t" \
            "v_mfma_f32_4x4x4_16b_bf16 v[196:199], v[206:207], v[206:207], v[196:199]\n\t" \
            "s_waitcnt lgkmcnt(14)\n\t" \
            "ds_write_b128 v237, v[204:207]\n\t" \
            "v_mfma_f32_16x16x32_bf16 v[192:195], v[238:241], v[204:207], v[192:195]\n\t" \
            "s_waitcnt lgkmcnt(10)\n\t" \
            "v_smfmac_f32_16x16x64_bf16 v[176:179], v[168:171], v[152:159], v255\n\t" \
            "ds_read_b128 v[168:171], v254 offset:384\n\t" \
            "ds_read_b64 v[152:153], v253 offset:56\n\t" \
            "ds_read_b64 v[154:155], v253 offset:440\n\t" \
            "ds_read_b64 v[156:157], v253 offset:3128\n\t" \
            "s_waitcnt lgkmcnt(9)\n\t" \
            "v_smfmac_f32_16x16x64_bf16 v[180:183], v[172:175], v[160:167], v255\n\t" \
            "ds_read_b64 v[158:159], v253 offset:3512\n\t" \
            "ds_read_b128 v[172:175], v254 offset:448\n\t" \
            "ds_read_b64 v[160:161], v253 offset:64\n\t" \
            "ds_read_b64 v[162:163], v253 offset:448\n\t" \
            "ds_read_b64 v[164:165], v253 offset:3136\n\t" \
            "ds_read_b64 v[166:167], v253 offset:3520\n\t" \
            "v_cvt_pk_bf16_f32 v208, v176, v177\n\t" \
            "v_cvt_pk_bf16_f32 v209, v178, v179\n\t" \
            "v_mov_b64 v[176:177], 0\n\t" \
            "v_mov_b64 v[178:179], 0\n\t" \
            "v_mfma_f32_4x4x4_16b_bf16 v[196:199], v[208:209], v[208:209], v[196:199]\n\t" \
            "v_cvt_pk_bf16_f32 v210, v180, v181\n\t" \
            "v_cvt_pk_bf16_f32 v211, v182, v183\n\t" \
            "v_mov_b64 v[180:181], 0\n\t" \
            "v_mov_b64 v[182:183], 0\n\t" \
            "v_mfma_f32_4x4x4_16b_bf16 v[196:199], v[210:211], v[210:211], v[196:199]\n\t" \
            "s_waitcnt lgkmcnt(14)\n\t" \
            "ds_write_b128 v236, v[208:211] offset:128\n\t" \
            "v_mfma_f32_16x16x32_bf16 v[192:195], v[238:241], v[208:211], v[192:195]\n\t" \
            "s_waitcnt lgkmcnt(10)\n\t" \
            "v_smfmac_f32_16x16x64_bf16 v[184:187], v[168:171], v[144:151], v255\n\t" \
            "ds_read_b128 v[168:171], v254 offset:512\n\t" \
            "ds_read_b64 v[144:145], v253 offset:72\n\t" \
            "ds_read_b64 v[146:147], v253 offset:456\n\t" \
            "ds_read_b64 v[148:149], v253 offset:3144\n\t" \
            "s_waitcnt lgkmcnt(9)\n\t" \
            "v_smfmac_f32_16x16x64_bf16 v[188:191], v[172:175], v[152:159], v255\n\t" \
            "ds_read_b64 v[150:151], v253 offset:3528\n\t" \
            "ds_read_b128 v[172:175], v254 offset:576\n\t" \
            "ds_read_b64 v[152:153], v253 offset:80\n\t" \
            "ds_read_b64 v[154:155], v253 offset:464\n\t" \
            "ds_read_b64 v[156:157], v253 offset:3152\n\t" \
            "ds_read_b64 v[158:159], v253 offset:3536\n\t" \
            "v_cvt_pk_bf16_f32 v212, v184, v185\n\t" \
            "v_cvt_pk_bf16_f32 v213, v186, v187\n\t" \
            "v_mov_b64 v[184:185], 0\n\t" \
            "v_mov_b64 v[186:187], 0\n\t" \
            "v_mfma_f32_4x4x4_16b_bf16 v[196:199], v[212:213], v[212:213], v[196:199]\n\t" \
            "v_cvt_pk_bf16_f32 v214, v188, v189\n\t" \
            "v_cvt_pk_bf16_f32 v215, v190, v191\n\t" \
            "v_mov_b64 v[188:189], 0\n\t" \
            "v_mov_b64 v[190:191], 0\n\t" \
            "v_mfma_f32_4x4x4_16b_bf16 v[196:199], v[214:215], v[214:215], v[196:199]\n\t" \
            "s_waitcnt lgkmcnt(14)\n\t" \
            "ds_write_b128 v237, v[212:215] offset:128\n\t" \
            "v_mfma_f32_16x16x32_bf16 v[192:195], v[238:241], v[212:215], v[192:195]\n\t" \
            "s_waitcnt lgkmcnt(10)\n\t" \
            "v_smfmac_f32_16x16x64_bf16 v[176:179], v[168:171], v[160:167], v255\n\t" \
            "ds_read_b128 v[168:171], v254 offset:640\n\t" \
            "ds_read_b64 v[160:161], v253 offset:88\n\t" \
            "ds_read_b64 v[162:163], v253 offset:472\n\t" \
            "ds_read_b64 v[164:165], v253 offset:3160\n\t" \
            "s_waitcnt lgkmcnt(9)\n\t" \
            "v_smfmac_f32_16x16x64_bf16 v[180:183], v[172:175], v[144:151], v255\n\t" \
            "ds_read_b64 v[166:167], v253 offset:3544\n\t" \
            "ds_read_b128 v[172:175], v254 offset:704\n\t" \
            "v_cvt_pk_bf16_f32 v216, v176, v177\n\t" \
            "v_cvt_pk_bf16_f32 v217, v178, v179\n\t" \
            "s_nop 1\n\t" \
            "v_mfma_f32_4x4x4_16b_bf16 v[196:199], v[216:217], v[216:217], v[196:199]\n\t" \
            "s_nop 0\n\t" \
            "v_cvt_pk_bf16_f32 v218, v180, v181\n\t" \
            "v_cvt_pk_bf16_f32 v219, v182, v183\n\t" \
            "s_nop 1\n\t" \
            "v_mfma_f32_4x4x4_16b_bf16 v[196:199], v[218:219], v[218:219], v[196:199]\n\t" \
            "ds_write_b128 v236, v[216:219] offset:256\n\t" \
            "v_mfma_f32_16x16x32_bf16 v[192:195], v[238:241], v[216:219], v[192:195]\n\t" \
            "s_waitcnt lgkmcnt(6)\n\t" \
            "v_smfmac_f32_16x16x64_bf16 v[184:187], v[168:171], v[152:159], v255\n\t" \
            "s_waitcnt lgkmcnt(1)\n\t" \
            "v_smfmac_f32_16x16x64_bf16 v[188:191], v[172:175], v[160:167], v255\n\t" \
            "s_nop 5\n\t" \
            "v_cvt_pk_bf16_f32 v220, v184, v185\n\t" \
            "v_cvt_pk_bf16_f32 v221, v186, v187\n\t" \
            "s_nop 1\n\t" \
            "v_mfma_f32_4x4x4_16b_bf16 v[196:199], v[220:221], v[220:221], v[196:199]\n\t" \
            "v_cvt_pk_bf16_f32 v222, v188, v189\n\t" \
            "v_cvt_pk_bf16_f32 v223, v190, v191\n\t" \
            "s_nop 1\n\t" \
            "v_mfma_f32_4x4x4_16b_bf16 v[196:199], v[222:223], v[222:223], v[196:199]\n\t" \
            "ds_write_b128 v237, v[220:223] offset:256\n\t" \
            "v_mfma_f32_16x16x32_bf16 v[192:195], v[238:241], v[220:223], v[192:195]\n\t" \
            "s_nop 7\n\t" \
            "v_cndmask_b32_e64 v193, v196, v197, s[60:61]\n\t" \
            "v_cndmask_b32_e64 v193, v193, v198, s[62:63]\n\t" \
            "v_cndmask_b32_e64 v193, v193, v199, s[64:65]\n\t" \
            "ds_write_b64 v248, v[192:193] offset:18816\n\t" \
            "s_waitcnt lgkmcnt(0)\n\t" \
            "s_nop 7\n\t" \
            : "={v[200:203]}"(xl[0]), "={v[204:207]}"(xl[1]), "={v[208:211]}"(xl[2]), "={v[212:215]}"(xl[3]), "={v[216:219]}"(xl[4]), "={v[220:223]}"(xl[5]) \
            : "{v250}"(lX), "{v251}"(lS), "{v253}"(lB), "{v254}"(aA_), "{v255}"(idx_), "{v[238:241]}"(sel4), "{s67}"(ldsb + O_OFF), \
              "{s[60:61]}"(0x2222222222222222ull), "{s[62:63]}"(0x4444444444444444ull), "{s[64:65]}"(0x8888888888888888ull) \
            : "v144", "v145", "v146", "v147", "v148", "v149", "v150", "v151", "v152", "v153", "v154", "v155", "v156", "v157", "v158", "v159", "v160", "v161", "v162", "v163", "v164", "v165", "v166", "v167", "v168", "v169", "v170", "v171", "v172", "v173", "v174", "v175", "v176", "v177", "v178", "v179", "v180", "v181", "v182", "v183", "v184", "v185", "v186", "v187", "v188", "v189", "v190", "v191", "v192", "v193", "v194", "v195", "v196", "v197", "v198", "v199", "v236", "v237", "v248", "v249", "memory"); \
        } \
          \
        LOAD_WL(0, 18) \
        SB(); \
        BAR();   \
        { \
            LANE_IDS \
            const int hd = (ISV) ? 2 * w : 2 * w - 3; \
            bf16x8 hl[12]; \
            _Pragma("unroll") for (int i = 0; i < 12; ++i) hl[i] = FRAG(O_OFF, i / 6, i % 6); \
            RSTD_PUBLISH(ln_, STL_OFF) \
            float rsl[2]; \
            _Pragma("unroll") for (int lt = 0; lt < 2; ++lt) rsl[lt] = *(const float*)(smem + RS_BASE + l15_ * 4 + lt * 64); \
            __builtin_amdgcn_s_waitcnt(0x0F70);   \
            if (ISV) { LOAD_WI(18, 36) } \
            DMA_SLICE(en1) \
            SB(); \
            _Pragma("unroll") for (int lt = 0; lt < 2; ++lt) { \
                f32x4 acc[3]; \
                _Pragma("unroll") for (int ks = 0; ks < 6; ++ks) \
                    _Pragma("unroll") for (int j = 0; j < 3; ++j) acc[j] = MFMA32(W[j * 6 + ks], hl[lt * 6 + ks], ks == 0 ? ZERO4 : acc[j]); \
                const int i = 16 * lt + l15_, g_ = ln_ >> 4; \
                const int vp = i < cnt ? OL_OFF + i * 384 + (((2 * hd + (g_ >> 1)) ^ ((i >> 1) & 7)) << 4) + 8 * (g_ & 1) : OL_OFF + 22 * 384 + g_ * 8; \
                _Pragma("unroll") for (int j = 0; j < 3; ++j) \
                    *(u32x2*)(smem + vp + j * 128) = (u32x2){pk2(acc[j][0] * rsl[lt], acc[j][1] * rsl[lt]), pk2(acc[j][2] * rsl[lt], acc[j][3] * rsl[lt])}; \
            } \
            SB(); \
        } \
          BAR();     \
        _Pragma("unroll") for (int p = 0; p < 6; ++p) __builtin_amdgcn_raw_buffer_store_b128(xl[p], orsrc, xl[6][0] + 16 * p, 0, 0);
#define B_SCALARS \
            CONV_CONSTS(0) \
            const unsigned aCA_ = ldsb + CT_OFF + lane * 16; \
            const unsigned long long ob = (unsigned long long)out, xb = (unsigned long long)((const char*)x - XBIAS), wb = (unsigned long long)wf; \
            const u32x4 orr = {(unsigned)ob, (unsigned)(ob >> 32) & 0xffffu, (unsigned)(IMG * IMG * CH * 2), 0x00020000u}; \
            const u32x4 xr = {(unsigned)xb, (unsigned)(xb >> 32) & 0xffffu, (unsigned)(IMG * IMG * CH * 2 + XBIAS), 0x00020000u}; \
            const u32x4 wr = {(unsigned)wb, (unsigned)(wb >> 32) & 0xffffu, (unsigned)WS_BYTES, 0x00020000u}; \
            unsigned s45 = OOB, s46 = (en0 & 0x7ffffffu) + 96 * w, s47 = (en1 & 0x7ffffffu) + 96 * w; \
            unsigned s48 = ldsb + X_OFF, s67 = ldsb + X1_OFF, s76 = ldsb + WL_OFF + 8; \
            unsigned s56 = __builtin_amdgcn_readfirstlane((en0 >> 30) * 4 + (en1 >> 30)); \
            unsigned long long xp = 0, sp = 0; \
            _Pragma("unroll") for (int v = 0; v < 4; ++v) { \
                const unsigned r = (CROW >> (8 * v)) & 0xFF; \
                xp |= (unsigned long long)XPAT(r) << (16 * v); \
                sp |= (unsigned long long)SPAT(r) << (16 * v); \
            }
#define B_SIO "+{s45}"(s45), "+{s46}"(s46), "+{s47}"(s47), "+{s48}"(s48), "+{s67}"(s67), "+{s76}"(s76), "+{s56}"(s56)
#define B_SIN "{v[238:241]}"(sel4), "{s[40:43]}"(orr), "{s[72:75]}"(xr), "{s39}"(0x401fffffu), "{s[28:31]}"(wr), "{s33}"(wave * 36864), "{s78}"(96 * w), \
              "{s27}"(96 * w - 384), "{s[50:51]}"(xp), "{s[52:53]}"(sp), "{s54}"(CLANE), "{s55}"(CROW & 0x0F0F0F0Fu), "{s[34:35]}"(0x0001000100010001ull), \
              "{s[60:61]}"(0x2222222222222222ull), "{s[62:63]}"(0x4444444444444444ull), "{s[64:65]}"(0x8888888888888888ull), \
              "{s77}"(w == 2 ? 768 : 6 * ROWBYTES + 768), "{s25}"(w == 2 ? 3 * ROWBYTES : OOB), "{s32}"(ldsb + X_OFF + 18432), "{s24}"(ldsb + WL_OFF + 12 + 4 * (cnt - 5)), "{s66}"(ldsb + w * 8192), "{s71}"((ldsb + X_OFF) ^ (ldsb + X1_OFF))
#define B_SCLOB "s26", "s44", "s70", "s68", "s69", "s79", "s36", "s37", "s38", "s57", "s58", "s59", "s49", "vcc", "scc", "m0", "memory"

    if (wave < 4) {
        ROLE_SETUP
        LANE_SETUP
        __builtin_amdgcn_s_setprio(3);
        {
            DMA_SLICE(en0)
            asm volatile("s_barrier" ::: "memory");
            unsigned tp[9];
            {
                const unsigned short* dp = dw + 48 * w + (lane < 48 ? lane : 0);
                asm volatile("global_load_ushort %0, %9, off\n\tglobal_load_ushort %1, %9, off offset:384\n\tglobal_load_ushort %2, %9, off offset:768\n\t"
                             "global_load_ushort %3, %9, off offset:1152\n\tglobal_load_ushort %4, %9, off offset:1536\n\tglobal_load_ushort %5, %9, off offset:1920\n\t"
                             "global_load_ushort %6, %9, off offset:2304\n\tglobal_load_ushort %7, %9, off offset:2688\n\tglobal_load_ushort %8, %9, off offset:3072"
                             : "=&v"(tp[0]), "=&v"(tp[1]), "=&v"(tp[2]), "=&v"(tp[3]), "=&v"(tp[4]), "=&v"(tp[5]), "=&v"(tp[6]), "=&v"(tp[7]), "=&v"(tp[8])
                             : "v"(dp) : "memory");
            }
            if (w == 0 && lane < 32) *(unsigned*)(smem + WL_OFF + lane * 4) = lane < cnt ? wl_entry(start + lane) : 0x7ffffffu;
            if (lane < 48) *(u32x4*)(smem + AT_OFF + 18816 + lane * 16) = (u32x4){0u, 0u, 0u, 0u};
            if (w == 1) {
                u32x4 so;
#pragma unroll
                for (int e = 0; e < 3; ++e) {
                    const int tk = byte_of(TOKMAP[2 * e], TOKMAP[2 * e + 1], l15), ty = tk / 7, tx = tk - 7 * ty;
                    so[e] = (unsigned)(ty * ROWBYTES + tx * (CH * 2)) + 16 * g;
                }
                so[3] = 0;
                *(u32x4*)(smem + CT_OFF + lane * 16) = so;
            }
            if (w == 2) *(u32x2*)(smem + CT_OFF + 1024 + lane * 16) = (u32x2){dma_off(lane), pf_off(lane)};
            asm volatile("s_waitcnt vmcnt(0)" : "+v"(tp[0]), "+v"(tp[1]), "+v"(tp[2]), "+v"(tp[3]), "+v"(tp[4]), "+v"(tp[5]), "+v"(tp[6]), "+v"(tp[7]), "+v"(tp[8]));
            tp[4] |= 0x3F800000u;
            if (lane < 48) {
#pragma unroll
                for (int dy = 0; dy < 3; ++dy) {
                    char* p = smem + AT_OFF + (dy == 0 ? 0 : (dy == 1 ? 6208 : 12544)) + w * 768 + lane * 16;
                    *(u32x4*)p = (u32x4){tp[3 * dy], tp[3 * dy + 1], tp[3 * dy + 2], 0u};
                    *(u32x4*)(p + 3072) = (u32x4){0u, tp[3 * dy], tp[3 * dy + 1], tp[3 * dy + 2]};
                }
            }
            CONV_CONSTS(en0 >> 30)
            asm volatile(
            "s_waitcnt vmcnt(0) lgkmcnt(0)\n\t"
            "s_nop 7\n\t"
            "ds_read_b128 v[168:171], v254\n\t"
            "ds_read_b64 v[144:145], v253\n\t"
            "ds_read_b64 v[146:147], v253 offset:96\n\t"
            "ds_read_b64 v[148:149], v253 offset:1744\n\t"
            "ds_read_b64 v[150:151], v253 offset:1840\n\t"
            "ds_read_b128 v[172:175], v254 offset:64\n\t"
            "ds_read_b64 v[152:153], v253 offset:8\n\t"
            "ds_read_b64 v[154:155], v253 offset:104\n\t"
            "ds_read_b64 v[156:157], v253 offset:1752\n\t"
            "ds_read_b64 v[158:159], v253 offset:1848\n\t"
            "ds_read_b64 v[160:161], v253 offset:16\n\t"
            "ds_read_b64 v[162:163], v253 offset:112\n\t"
            "ds_read_b64 v[164:165], v253 offset:1760\n\t"
            "ds_read_b64 v[166:167], v253 offset:1856\n\t"
            "v_mov_b64 v[176:177], 0\n\t"
            "v_mov_b64 v[178:179], 0\n\t"
            "v_mov_b64 v[180:181], 0\n\t"
            "v_mov_b64 v[182:183], 0\n\t"
            "v_mov_b64 v[184:185], 0\n\t"
            "v_mov_b64 v[186:187], 0\n\t"
            "v_mov_b64 v[188:189], 0\n\t"
            "v_mov_b64 v[190:191], 0\n\t"
            "v_add_u32 v236, s67, v250\n\t"
            "v_xor_b32 v237, 16, v236\n\t"
            "v_add_u32 v248, s67, v251\n\t"
            "s_waitcnt lgkmcnt(9)\n\t"
            "v_smfmac_f32_16x16x64_bf16 v[176:179], v[168:171], v[144:151], v255\n\t"
            "ds_read_b128 v[168:171], v254 offset:128\n\t"
            "ds_read_b64 v[144:145], v253 offset:24\n\t"
            "s_waitcnt lgkmcnt(6)\n\t"
            "v_smfmac_f32_16x16x64_bf16 v[180:183], v[172:175], v[152:159], v255\n\t"
            "ds_read_b64 v[146:147], v253 offset:120\n\t"
            "ds_read_b64 v[148:149], v253 offset:1768\n\t"
            "ds_read_b64 v[150:151], v253 offset:1864\n\t"
            "ds_read_b128 v[172:175], v254 offset:192\n\t"
            "ds_read_b64 v[152:153], v253 offset:32\n\t"
            "ds_read_b64 v[154:155], v253 offset:128\n\t"
            "ds_read_b64 v[156:157], v253 offset:1776\n\t"
            "ds_read_b64 v[158:159], v253 offset:1872\n\t"
            "v_cvt_pk_bf16_f32 v176, v176, v177\n\t"
            "v_cvt_pk_bf16_f32 v177, v178, v179\n\t"
            "v_cvt_pk_bf16_f32 v178, v180, v181\n\t"
            "v_cvt_pk_bf16_f32 v179, v182, v183\n\t"
            "v_mfma_f32_4x4x4_16b_bf16 v[196:199], v[176:177], v[176:177], 0\n\t"
            "ds_write_b128 v236, v[176:179]\n\t"
            "v_mfma_f32_16x16x32_bf16 v[192:195], v[238:241], v[176:179], 0\n\t"
            "v_mov_b64 v[176:177], 0\n\t"
            "v_mov_b64 v[180:181], 0\n\t"
            "v_mfma_f32_4x4x4_16b_bf16 v[196:199], v[178:179], v[178:179], v[196:199]\n\t"
            "v_mov_b64 v[178:179], 0\n\t"
            "v_mov_b64 v[182:183], 0\n\t"
            "s_waitcnt lgkmcnt(10)\n\t"
            "v_smfmac_f32_16x16x64_bf16 v[184:187], v[168:171], v[160:167], v255\n\t"
            "ds_read_b128 v[168:171], v254 offset:256\n\t"
            "ds_read_b64 v[160:161], v253 offset:40\n\t"
            "ds_read_b64 v[162:163], v253 offset:136\n\t"
            "ds_read_b64 v[164:165], v253 offset:1784\n\t"
            "ds_read_b64 v[166:167], v253 offset:1880\n\t"
            "s_waitcnt lgkmcnt(10)\n\t"
            "v_smfmac_f32_16x16x64_bf16 v[188:191], v[172:175], v[144:151], v255\n\t"
            "ds_read_b128 v[172:175], v254 offset:320\n\t"
            "ds_read_b64 v[144:145], v253 offset:48\n\t"
            "ds_read_b64 v[146:147], v253 offset:144\n\t"
            "ds_read_b64 v[148:149], v253 offset:1792\n\t"
            "ds_read_b64 v[150:151], v253 offset:1888\n\t"
            "v_cvt_pk_bf16_f32 v184, v184, v185\n\t"
            "v_cvt_pk_bf16_f32 v185, v186, v187\n\t"
            "s_nop 0\n\t"
            "v_cvt_pk_bf16_f32 v186, v188, v189\n\t"
            "v_cvt_pk_bf16_f32 v187, v190, v191\n\t"
            "v_mfma_f32_4x4x4_16b_bf16 v[196:199], v[184:185], v[184:185], v[196:199]\n\t"
            "s_waitcnt lgkmcnt(14)\n\t"
            "ds_write_b128 v237, v[184:187]\n\t"
            "v_mfma_f32_16x16x32_bf16 v[192:195], v[238:241], v[184:187], v[192:195]\n\t"
            "v_mov_b64 v[184:185], 0\n\t"
            "v_mov_b64 v[188:189], 0\n\t"
            "v_mfma_f32_4x4x4_16b_bf16 v[196:199], v[186:187], v[186:187], v[196:199]\n\t"
            "v_mov_b64 v[186:187], 0\n\t"
            "v_mov_b64 v[190:191], 0\n\t"
            "s_waitcnt lgkmcnt(10)\n\t"
            "v_smfmac_f32_16x16x64_bf16 v[176:179], v[168:171], v[152:159], v255\n\t"
            "ds_read_b128 v[168:171], v254 offset:384\n\t"
            "ds_read_b64 v[152:153], v253 offset:56\n\t"
            "ds_read_b64 v[154:155], v253 offset:152\n\t"
            "ds_read_b64 v[156:157], v253 offset:1800\n\t"
            "ds_read_b64 v[158:159], v253 offset:1896\n\t"
            "s_waitcnt lgkmcnt(10)\n\t"
            "v_smfmac_f32_16x16x64_bf16 v[180:183], v[172:175], v[160:167], v255\n\t"
            "ds_read_b128 v[172:175], v254 offset:448\n\t"
            "ds_read_b64 v[160:161], v253 offset:64\n\t"
            "ds_read_b64 v[162:163], v253 offset:160\n\t"
            "ds_read_b64 v[164:165], v253 offset:1808\n\t"
            "ds_read_b64 v[166:167], v253 offset:1904\n\t"
            "v_cvt_pk_bf16_f32 v176, v176, v177\n\t"
            "v_cvt_pk_bf16_f32 v177, v178, v179\n\t"
            "s_nop 0\n\t"
            "v_cvt_pk_bf16_f32 v178, v180, v181\n\t"
            "v_cvt_pk_bf16_f32 v179, v182, v183\n\t"
            "v_mfma_f32_4x4x4_16b_bf16 v[196:199], v[176:177], v[176:177], v[196:199]\n\t"
            "s_waitcnt lgkmcnt(14)\n\t"
            "ds_write_b128 v236, v[176:179] offset:128\n\t"
            "v_mfma_f32_16x16x32_bf16 v[192:195], v[238:241], v[176:179], v[192:195]\n\t"
            "v_mov_b64 v[176:177], 0\n\t"
            "v_mov_b64 v[180:181], 0\n\t"
            "v_mfma_f32_4x4x4_16b_bf16 v[196:199], v[178:179], v[178:179], v[196:199]\n\t"
            "v_mov_b64 v[178:179], 0\n\t"
            "v_mov_b64 v[182:183], 0\n\t"
            "s_waitcnt lgkmcnt(10)\n\t"
            "v_smfmac_f32_16x16x64_bf16 v[184:187], v[168:171], v[144:151], v255\n\t"
            "ds_read_b128 v[168:171], v254 offset:512\n\t"
            "ds_read_b64 v[144:145], v253 offset:72\n\t"
            "ds_read_b64 v[146:147], v253 offset:168\n\t"
            "ds_read_b64 v[148:149], v253 offset:1816\n\t"
            "ds_read_b64 v[150:151], v253 offset:1912\n\t"
            "s_waitcnt lgkmcnt(10)\n\t"
            "v_smfmac_f32_16x16x64_bf16 v[188:191], v[172:175], v[152:159], v255\n\t"
            "ds_read_b128 v[172:175], v254 offset:576\n\t"
            "ds_read_b64 v[152:153], v253 offset:80\n\t"
            "ds_read_b64 v[154:155], v253 offset:176\n\t"
            "ds_read_b64 v[156:157], v253 offset:1824\n\t"
            "ds_read_b64 v[158:159], v253 offset:1920\n\t"
            "v_cvt_pk_bf16_f32 v184, v184, v185\n\t"
            "v_cvt_pk_bf16_f32 v185, v186, v187\n\t"
            "s_nop 0\n\t"
            "v_cvt_pk_bf16_f32 v186, v188, v189\n\t"
            "v_cvt_pk_bf16_f32 v187, v190, v191\n\t"
            "v_mfma_f32_4x4x4_16b_bf16 v[196:199], v[184:185], v[184:185], v[196:199]\n\t"
            "s_waitcnt lgkmcnt(14)\n\t"
            "ds_write_b128 v237, v[184:187] offset:128\n\t"
            "v_mfma_f32_16x16x32_bf16 v[192:195], v[238:241], v[184:187], v[192:195]\n\t"
            "v_mov_b64 v[184:185], 0\n\t"
            "v_mov_b64 v[188:189], 0\n\t"
            "v_mfma_f32_4x4x4_16b_bf16 v[196:199], v[186:187], v[186:187], v[196:199]\n\t"
            "v_mov_b64 v[186:187], 0\n\t"
            "v_mov_b64 v[190:191], 0\n\t"
            "s_waitcnt lgkmcnt(10)\n\t"
            "v_smfmac_f32_16x16x64_bf16 v[176:179], v[168:171], v[160:167], v255\n\t"
            "ds_read_b128 v[168:171], v254 offset:640\n\t"
            "ds_read_b64 v[160:161], v253 offset:88\n\t"
            "ds_read_b64 v[162:163], v253 offset:184\n\t"
            "ds_read_b64 v[164:165], v253 offset:1832\n\t"
            "ds_read_b64 v[166:167], v253 offset:1928\n\t"
            "s_waitcnt lgkmcnt(10)\n\t"
            "v_smfmac_f32_16x16x64_bf16 v[180:183], v[172:175], v[144:151], v255\n\t"
            "ds_read_b128 v[172:175], v254 offset:704\n\t"
            "v_cvt_pk_bf16_f32 v176, v176, v177\n\t"
            "v_cvt_pk_bf16_f32 v177, v178, v179\n\t"
            "s_nop 1\n\t"
            "v_mfma_f32_4x4x4_16b_bf16 v[196:199], v[176:177], v[176:177], v[196:199]\n\t"
            "s_nop 1\n\t"
            "v_cvt_pk_bf16_f32 v178, v180, v181\n\t"
            "v_cvt_pk_bf16_f32 v179, v182, v183\n\t"
            "s_nop 1\n\t"
            "v_mfma_f32_4x4x4_16b_bf16 v[196:199], v[178:179], v[178:179], v[196:199]\n\t"
            "ds_write_b128 v236, v[176:179] offset:256\n\t"
            "s_waitcnt lgkmcnt(6)\n\t"
            "v_smfmac_f32_16x16x64_bf16 v[184:187], v[168:171], v[152:159], v255\n\t"
            "v_mfma_f32_16x16x32_bf16 v[192:195], v[238:241], v[176:179], v[192:195]\n\t"
            "s_waitcnt lgkmcnt(1)\n\t"
            "v_smfmac_f32_16x16x64_bf16 v[188:191], v[172:175], v[160:167], v255\n\t"
            "s_nop 4\n\t"
            "v_cvt_pk_bf16_f32 v184, v184, v185\n\t"
            "v_cvt_pk_bf16_f32 v185, v186, v187\n\t"
            "s_nop 1\n\t"
            "v_mfma_f32_4x4x4_16b_bf16 v[196:199], v[184:185], v[184:185], v[196:199]\n\t"
            "v_cvt_pk_bf16_f32 v186, v188, v189\n\t"
            "v_cvt_pk_bf16_f32 v187, v190, v191\n\t"
            "s_nop 1\n\t"
            "v_mfma_f32_4x4x4_16b_bf16 v[196:199], v[186:187], v[186:187], v[196:199]\n\t"
            "ds_write_b128 v237, v[184:187] offset:256\n\t"
            "v_mfma_f32_16x16x32_bf16 v[192:195], v[238:241], v[184:187], v[192:195]\n\t"
            "s_nop 7\n\t"
            "v_cndmask_b32_e64 v193, v196, v197, s[60:61]\n\t"
            "v_cndmask_b32_e64 v193, v193, v198, s[62:63]\n\t"
            "v_cndmask_b32_e64 v193, v193, v199, s[64:65]\n\t"
            "ds_write_b64 v248, v[192:193] offset:18816\n\t"
            "s_waitcnt lgkmcnt(0)\n\t"
            "s_nop 7\n\t"
            :: "{v250}"(pXw_), "{v251}"(pSTw_), "{v253}"(aB_), "{v254}"(aA_), "{v255}"(idx_), "{v[238:241]}"(sel4), "{s67}"(ldsb + X_OFF), "{s[60:61]}"(0x2222222222222222ull),
               "{s[62:63]}"(0x4444444444444444ull), "{s[64:65]}"(0x8888888888888888ull)
            : "v144", "v145", "v146", "v147", "v148", "v149", "v150", "v151", "v152", "v153", "v154", "v155", "v156", "v157", "v158", "v159", "v160", "v161", "v162", "v163", "v164", "v165", "v166", "v167", "v168", "v169", "v170", "v171", "v172", "v173", "v174", "v175", "v176", "v177", "v178", "v179", "v180", "v181", "v182", "v183", "v184", "v185", "v186", "v187", "v188", "v189", "v190", "v191", "v192", "v193", "v194", "v195", "v196", "v197", "v198", "v199", "v236", "v237", "v248", "v249", "memory");
        }
        __builtin_amdgcn_s_setprio(0);
        BAR();
        LOAD_WI(0, 36)
        __builtin_amdgcn_s_setprio(2);
        int dW = X_OFF + l15 * 384 + (((2 * w + (g >> 1)) ^ sw) << 4) + 8 * (g & 1);
        int vA = V_OFF + w * 4608 + lane * 16, vB = V_OFF + w * 4608 + 1024 + lane * 8;
        int rsR = RS_BASE + l15 * 4;
        OPQ(dW); OPQ(vA); OPQ(vB); OPQ(rsR);
        const float SC = 0.14433756729740643f * 1.4426950408889634f;
        BAR();
        {
            bf16x8 hl[12];
#pragma unroll
            for (int i = 0; i < 12; ++i) hl[i] = FRAG(O_OFF, i / 6, i % 6);
            RSTD_PUBLISH(lane, STL_OFF)
            float rsl[2];
#pragma unroll
            for (int lt = 0; lt < 2; ++lt) rsl[lt] = *(const float*)(smem + rsR + lt * 64);
            SB();
            f32x4 acc[2][6];
#pragma unroll
            for (int lt = 0; lt < 2; ++lt)
#pragma unroll
                for (int ks = 0; ks < 6; ++ks)
#pragma unroll
                    for (int j = 0; j < 6; ++j) acc[lt][j] = MFMA32(W[j * 6 + ks], hl[lt * 6 + ks], ks == 0 ? ZERO4 : acc[lt][j]);
#pragma unroll
            for (int lt = 0; lt < 2; ++lt) {
                const int i = 16 * lt + l15;
                const float rs = rsl[lt];
                char* p = smem + (i < cnt ? KQL_OFF + i * 800 + w * 192 : X1_OFF + w * 192);
                const u32x4 k01 = {pk2(acc[lt][0][0] * rs, acc[lt][0][1] * rs), pk2(acc[lt][0][2] * rs, acc[lt][0][3] * rs),
                                   pk2(acc[lt][1][0] * rs, acc[lt][1][1] * rs), pk2(acc[lt][1][2] * rs, acc[lt][1][3] * rs)};
                const u32x4 q01 = {pk2(acc[lt][3][0], acc[lt][3][1]), pk2(acc[lt][3][2], acc[lt][3][3]),
                                   pk2(acc[lt][4][0], acc[lt][4][1]), pk2(acc[lt][4][2], acc[lt][4][3])};
                const u32x4 k2 = {pk2(acc[lt][2][0] * rs, acc[lt][2][1] * rs), pk2(acc[lt][2][2] * rs, acc[lt][2][3] * rs), 0u, 0u};
                const u32x4 q2 = {pk2(acc[lt][5][0], acc[lt][5][1]), pk2(acc[lt][5][2], acc[lt][5][3]), 0u, 0u};
                *(u32x4*)(p + g * 16) = k01;
                *(u32x4*)(p + 64 + g * 16) = q01;
                *(u32x2*)(p + 128 + g * 8) = (u32x2){k2[0], k2[1]};
                *(u32x2*)(p + 160 + g * 8) = (u32x2){q2[0], q2[1]};
                f32x4 dd = MFMA32(BC8(k2), BC8(q2), ZERO4);
                dd = MFMA32(BC8(k01), BC8(q01), dd);
                float sll = (l15 & 3) == 1 ? dd[1] : dd[0];
                sll = (l15 & 3) == 2 ? dd[2] : sll;
                sll = (l15 & 3) == 3 ? dd[3] : sll;
                const bool dg = g == (l15 >> 2);
                if (dg && i < cnt) { *(float*)(smem + KQL_OFF + i * 800 + 768 + w * 4) = sll; *(float*)(smem + KQL_OFF + i * 800 + 784) = rs * SC; }
            }
        }

        {
            const unsigned rsb = ldsb + RS_BASE;
            const unsigned pE_ = fE - X_OFF, aO_ = ldsb + dW + (O_OFF - X_OFF), sel_ = (l15 & 3) ? 0u : 1u, aVB_ = ldsb + vB;
            const unsigned pST_ = lane * 16, aRSw_ = rsb + lane * 4, aPLw_ = rsb + lane * 2, aRSr_ = ldsb + rsR;
            const unsigned g16_ = (l15 & 3) ? ldsb + AT_OFF + 18816 : g * 16, g8_ = (l15 & 3) ? ldsb + AT_OFF + 18816 : g * 8;
            const unsigned loVL_ = ((2 * w + (l15 >> 3)) << 4) + (l15 & 7) * 2, loOL_ = ((2 * w + (g >> 1)) << 4) + 8 * (g & 1);
            const unsigned on = (l15 & 3) ? 0u : 0x3F803F80u;
            const u32x4 ones4 = {on, on, on, on};
            unsigned s44 = cnt, s48 = ldsb + X_OFF, s70 = 0;
            unsigned s72 = ldsb + KQL_OFF + w * 192, s73 = ldsb + OL_OFF, s74 = ldsb + KQL_OFF + 784, s77 = ldsb + OL_OFF, s76 = ldsb + KQL_OFF + 768 + w * 4;
            if (w == 0 && lane == 0) __hip_atomic_fetch_add((gu32*)((char*)wf + CN_OFF), 1u, RLX_AGENT);
            unsigned long long s68 = 0;
            asm volatile(
            "s_waitcnt lgkmcnt(0)\n\t"
            "s_nop 7\n\t"
            "s_branch .Lwa_in%=\n\t"
            ".Lwa_loop%=:\n\t"
            "s_waitcnt lgkmcnt(2)\n\t"
            "v_mfma_f32_16x16x32_bf16 v[192:195], v[0:3], v[204:207], 0\n\t"
            "s_bfe_u32 s49, s70, 0x30001\n\t"
            "s_lshl_b32 s49, s49, 4\n\t"
            "s_or_b32 s73, s77, s49\n\t"
            "v_mfma_f32_16x16x32_bf16 v[148:151], v[24:27], v[204:207], 0\n\t"
            "v_mad_u32_u24 v220, v246, s72, v253\n\t"
            "v_mad_u32_u24 v216, v246, s72, v252\n\t"
            "v_mfma_f32_16x16x32_bf16 v[196:199], v[48:51], v[204:207], 0\n\t"
            "ds_read_b64 v[222:223], v220 offset:160\n\t"
            "ds_read_b64 v[220:221], v220 offset:128\n\t"
            "v_mfma_f32_16x16x32_bf16 v[200:203], v[72:75], v[204:207], 0\n\t"
            "ds_read_b128 v[224:227], v216 offset:64\n\t"
            "ds_read_b128 v[216:219], v216\n\t"
            "v_mfma_f32_16x16x32_bf16 v[160:163], v[96:99], v[204:207], 0\n\t"
            "v_mul_f32 v176, v237, v176\n\t"
            "v_mul_f32 v177, v237, v177\n\t"
            "v_mfma_f32_16x16x32_bf16 v[164:167], v[120:123], v[204:207], 0\n\t"
            "ds_read_b128 v[204:207], v233 offset:12416\n\t"
            "v_mul_f32 v178, v237, v178\n\t"
            "s_waitcnt lgkmcnt(6)\n\t"
            "v_mfma_f32_16x16x32_bf16 v[192:195], v[4:7], v[208:211], v[192:195]\n\t"
            "v_mul_f32 v179, v237, v179\n\t"
            "v_cvt_pk_bf16_f32 v176, v176, v177\n\t"
            "v_mfma_f32_16x16x32_bf16 v[148:151], v[28:31], v[208:211], v[148:151]\n\t"
            "v_mul_f32 v168, v237, v168\n\t"
            "v_mul_f32 v169, v237, v169\n\t"
            "v_mfma_f32_16x16x32_bf16 v[196:199], v[52:55], v[208:211], v[196:199]\n\t"
            "v_cvt_pk_bf16_f32 v177, v178, v179\n\t"
            "v_mul_f32 v170, v237, v170\n\t"
            "v_mfma_f32_16x16x32_bf16 v[200:203], v[76:79], v[208:211], v[200:203]\n\t"
            "v_mul_f32 v171, v237, v171\n\t"
            "v_cvt_pk_bf16_f32 v168, v168, v169\n\t"
            "v_mfma_f32_16x16x32_bf16 v[160:163], v[100:103], v[208:211], v[160:163]\n\t"
            "v_cvt_pk_bf16_f32 v180, v180, v181\n\t"
            "v_cvt_pk_bf16_f32 v178, v188, v189\n\t"
            "v_mfma_f32_16x16x32_bf16 v[164:167], v[124:127], v[208:211], v[164:167]\n\t"
            "ds_read_b128 v[208:211], v232 offset:12544\n\t"
            "v_cvt_pk_bf16_f32 v179, v190, v191\n\t"
            "s_waitcnt lgkmcnt(6)\n\t"
            "v_mfma_f32_16x16x32_bf16 v[192:195], v[8:11], v[212:215], v[192:195]\n\t"
            "v_mul_f32 v172, v237, v172\n\t"
            "v_mul_f32 v173, v237, v173\n\t"
            "v_mfma_f32_16x16x32_bf16 v[148:151], v[32:35], v[212:215], v[148:151]\n\t"
            "v_mul_f32 v174, v237, v174\n\t"
            "v_mul_f32 v175, v237, v175\n\t"
            "v_mfma_f32_16x16x32_bf16 v[196:199], v[56:59], v[212:215], v[196:199]\n\t"
            "v_cvt_pk_bf16_f32 v169, v170, v171\n\t"
            "v_cvt_pk_bf16_f32 v181, v182, v183\n\t"
            "v_mfma_f32_16x16x32_bf16 v[200:203], v[80:83], v[212:215], v[200:203]\n\t"
            "v_cvt_pk_bf16_f32 v170, v172, v173\n\t"
            "v_cvt_pk_bf16_f32 v171, v174, v175\n\t"
            "v_mfma_f32_16x16x32_bf16 v[160:163], v[104:107], v[212:215], v[160:163]\n\t"
            "v_cvt_pk_bf16_f32 v182, v184, v185\n\t"
            "v_cvt_pk_bf16_f32 v183, v186, v187\n\t"
            "v_mfma_f32_16x16x32_bf16 v[164:167], v[128:131], v[212:215], v[164:167]\n\t"
            "ds_read_b128 v[212:215], v233 offset:12544\n\t"
            "v_mov_b32 v230, s76\n\t"
            "s_waitcnt lgkmcnt(2)\n\t"
            "v_mfma_f32_16x16x32_bf16 v[192:195], v[12:15], v[204:207], v[192:195]\n\t"
            "ds_read_b32 v230, v230\n\t"
            "v_mov_b32 v239, s74\n\t"
            "v_mfma_f32_16x16x32_bf16 v[148:151], v[36:39], v[204:207], v[148:151]\n\t"
            "s_add_u32 s70, s70, 1\n\t"
            "s_add_u32 s72, s72, 800\n\t"
            "s_add_u32 s74, s74, 800\n\t"
            "s_add_u32 s76, s76, 800\n\t"
            "v_mfma_f32_16x16x32_bf16 v[196:199], v[60:63], v[204:207], v[196:199]\n\t"
            "s_add_u32 s77, s77, 384\n\t"
            "s_xor_b32 s48, s48, s47\n\t"
            "s_sub_u32 s44, s44, 1\n\t"
            "v_mfma_f32_16x16x32_bf16 v[200:203], v[84:87], v[204:207], v[200:203]\n\t"
            "s_mov_b32 s68, 0x10001\n\t"
            "s_mov_b32 s69, 0x10001\n\t"
            "ds_read_b32 v239, v239\n\t"
            "s_waitcnt lgkmcnt(3)\n\t"
            "v_mfma_f32_16x16x32_bf16 v[192:195], v[16:19], v[208:211], v[192:195]\n\t"
            "v_mul_f32 v237, 0x3e553b94, v237\n\t"
            "v_mfma_f32_16x16x32_bf16 v[160:163], v[108:111], v[204:207], v[160:163]\n\t"
            "v_mfma_f32_16x16x32_bf16 v[148:151], v[40:43], v[208:211], v[148:151]\n\t"
            "v_mfma_f32_16x16x32_bf16 v[164:167], v[132:135], v[204:207], v[164:167]\n\t"
            "v_mfma_f32_16x16x32_bf16 v[196:199], v[64:67], v[208:211], v[196:199]\n\t"
            "v_mfma_f32_16x16x32_bf16 v[200:203], v[88:91], v[208:211], v[200:203]\n\t"
            "s_waitcnt lgkmcnt(2)\n\t"
            "v_mfma_f32_16x16x32_bf16 v[192:195], v[20:23], v[212:215], v[192:195]\n\t"
            "v_mfma_f32_16x16x32_bf16 v[160:163], v[112:115], v[208:211], v[160:163]\n\t"
            "v_mfma_f32_16x16x32_bf16 v[148:151], v[44:47], v[212:215], v[148:151]\n\t"
            "v_mfma_f32_16x16x32_bf16 v[164:167], v[136:139], v[208:211], v[164:167]\n\t"
            "v_mfma_f32_16x16x32_bf16 v[196:199], v[68:71], v[212:215], v[196:199]\n\t"
            "v_mfma_f32_16x16x32_bf16 v[200:203], v[92:95], v[212:215], v[200:203]\n\t"
            "v_mfma_f32_16x16x32_bf16 v[160:163], v[116:119], v[212:215], v[160:163]\n\t"
            "s_nop 1\n\t"
            "v_mul_f32 v192, v238, v192\n\t"
            "v_mfma_f32_16x16x32_bf16 v[164:167], v[140:143], v[212:215], v[164:167]\n\t"
            "v_mul_f32 v193, v238, v193\n\t"
            "v_mul_f32 v194, v238, v194\n\t"
            "v_mfma_f32_16x16x16_bf16 v[172:175], v[220:221], v[154:155], 0\n\t"
            "v_mul_f32 v195, v238, v195\n\t"
            "v_cvt_pk_bf16_f32 v192, v192, v193\n\t"
            "v_mul_f32 v148, v238, v148\n\t"
            "v_mfma_f32_16x16x16_bf16 v[184:187], v[152:153], v[178:179], 0\n\t"
            "v_mul_f32 v149, v238, v149\n\t"
            "v_mul_f32 v150, v238, v150\n\t"
            "v_mul_f32 v151, v238, v151\n\t"
            "v_mfma_f32_16x16x16_bf16 v[208:211], v[220:221], v[178:179], 0\n\t"
            "v_cvt_pk_bf16_f32 v193, v194, v195\n\t"
            "v_mul_f32 v196, v238, v196\n\t"
            "v_mul_f32 v197, v238, v197\n\t"
            "v_mfma_f32_16x16x16_bf16 v[204:207], v[176:177], v[178:179], 0\n\t"
            "v_cvt_pk_bf16_f32 v194, v148, v149\n\t"
            "v_cvt_pk_bf16_f32 v195, v150, v151\n\t"
            "v_cvt_pk_bf16_f32 v200, v200, v201\n\t"
            "v_mfma_f32_16x16x16_bf16 v[148:151], v[152:153], v[154:155], 0\n\t"
            "v_mul_f32 v198, v238, v198\n\t"
            "v_mul_f32 v199, v238, v199\n\t"
            "v_cvt_pk_bf16_f32 v196, v196, v197\n\t"
            "v_mfma_f32_16x16x32_bf16 v[172:175], v[216:219], v[156:159], v[172:175]\n\t"
            "s_setprio 0\n\t"
            "v_cvt_pk_bf16_f32 v201, v202, v203\n\t"
            "v_cvt_pk_bf16_f32 v197, v198, v199\n\t"
            "s_setprio 3\n\t"
            "v_mfma_f32_16x16x32_bf16 v[148:151], v[144:147], v[156:159], v[148:151]\n\t"
            "v_cvt_pk_bf16_f32 v202, v160, v161\n\t"
            "v_cvt_pk_bf16_f32 v203, v162, v163\n\t"
            "v_mfma_f32_16x16x16_bf16 v[160:163], v[176:177], v[154:155], 0\n\t"
            "v_cvt_pk_bf16_f32 v198, v164, v165\n\t"
            "v_cvt_pk_bf16_f32 v199, v166, v167\n\t"
            "v_mul_f32 v238, 0x3e553b94, v238\n\t"
            "v_mfma_f32_16x16x16_bf16 v[164:167], v[196:197], v[154:155], 0\n\t"
            "s_nop 0\n\t"
            "v_max3_f32 v228, v172, v148, v149\n\t"
            "v_max3_f32 v228, v228, v150, v151\n\t"
            "v_mfma_f32_16x16x16_bf16 v[188:191], v[196:197], v[178:179], 0\n\t"
            "v_mfma_f32_16x16x32_bf16 v[184:187], v[144:147], v[180:183], v[184:187]\n\t"
            "v_mfma_f32_16x16x32_bf16 v[208:211], v[216:219], v[180:183], v[208:211]\n\t"
            "v_mfma_f32_16x16x32_bf16 v[160:163], v[168:171], v[156:159], v[160:163]\n\t"
            "v_mfma_f32_16x16x32_bf16 v[204:207], v[168:171], v[180:183], v[204:207]\n\t"
            "v_mfma_f32_16x16x32_bf16 v[164:167], v[192:195], v[156:159], v[164:167]\n\t"
            "s_nop 0\n\t"
            "v_mfma_f32_16x16x32_bf16 v[188:191], v[192:195], v[180:183], v[188:191]\n\t"
            "v_mfma_f32_16x16x16_bf16 v[212:215], v[222:223], v[152:153], 0\n\t"
            "v_mfma_f32_16x16x16_bf16 v[156:159], v[222:223], v[196:197], 0\n\t"
            "s_nop 0\n\t"
            "v_max3_f32 v229, v208, v184, v185\n\t"
            "v_mfma_f32_16x16x16_bf16 v[180:183], v[152:153], v[198:199], 0\n\t"
            "v_max3_f32 v229, v229, v186, v187\n\t"
            "v_max3_f32 v228, v228, v160, v161\n\t"
            "v_max3_f32 v229, v229, v204, v205\n\t"
            "v_mfma_f32_16x16x32_bf16 v[212:215], v[224:227], v[144:147], v[212:215]\n\t"
            "v_max3_f32 v228, v228, v162, v163\n\t"
            "v_max3_f32 v229, v229, v206, v207\n\t"
            "v_mfma_f32_16x16x32_bf16 v[156:159], v[224:227], v[192:195], v[156:159]\n\t"
            "v_max3_f32 v228, v228, v164, v165\n\t"
            "v_max3_f32 v229, v229, v188, v189\n\t"
            "v_mfma_f32_16x16x32_bf16 v[180:183], v[144:147], v[200:203], v[180:183]\n\t"
            "s_setprio 0\n\t"
            "v_max3_f32 v228, v228, v166, v167\n\t"
            "v_max3_f32 v229, v229, v190, v191\n\t"
            "s_nop 1\n\t"
            "v_permlane32_swap_b32 v228, v229\n\t"
            "v_max_f32 v228, v228, v229\n\t"
            "v_mov_b32 v231, v228\n\t"
            "s_nop 1\n\t"
            "v_permlane16_swap_b32 v228, v231\n\t"
            "v_max_f32 v228, v228, v231\n\t"
            "v_mov_b32 v229, v228\n\t"
            "s_nop 1\n\t"
            "v_permlane32_swap_b32 v228, v229\n\t"
            "v_mul_f32_e64 v228, -v228, v236\n\t"
            "v_fma_f32 v148, v148, v236, v228\n\t"
            "v_fma_f32 v149, v149, v236, v228\n\t"
            "v_exp_f32 v148, v148\n\t"
            "v_exp_f32 v149, v149\n\t"
            "v_fma_f32 v150, v150, v236, v228\n\t"
            "v_fma_f32 v151, v151, v236, v228\n\t"
            "v_exp_f32 v150, v150\n\t"
            "v_exp_f32 v151, v151\n\t"
            "v_fma_f32 v160, v160, v236, v228\n\t"
            "v_fma_f32 v161, v161, v236, v228\n\t"
            "v_fma_f32 v162, v162, v236, v228\n\t"
            "v_fma_f32 v163, v163, v236, v228\n\t"
            "v_exp_f32 v160, v160\n\t"
            "v_exp_f32 v161, v161\n\t"
            "v_exp_f32 v162, v162\n\t"
            "v_exp_f32 v163, v163\n\t"
            "v_cvt_pk_bf16_f32 v148, v148, v149\n\t"
            "v_cvt_pk_bf16_f32 v149, v150, v151\n\t"
            "v_cvt_pk_bf16_f32 v150, v160, v161\n\t"
            "v_cvt_pk_bf16_f32 v151, v162, v163\n\t"
            "s_setprio 3\n\t"
            "v_mfma_f32_16x16x16_bf16 v[160:163], v[222:223], v[176:177], 0\n\t"
            "s_setprio 0\n\t"
            "v_mul_f32_e64 v229, -v229, v237\n\t"
            "v_fma_f32 v164, v164, v236, v228\n\t"
            "v_fma_f32 v165, v165, v236, v228\n\t"
            "v_fma_f32 v184, v184, v237, v229\n\t"
            "v_fma_f32 v185, v185, v237, v229\n\t"
            "v_exp_f32 v164, v164\n\t"
            "s_setprio 3\n\t"
            "v_mfma_f32_16x16x32_bf16 v[160:163], v[224:227], v[168:171], v[160:163]\n\t"
            "s_setprio 0\n\t"
            "v_exp_f32 v165, v165\n\t"
            "v_fma_f32 v166, v166, v236, v228\n\t"
            "v_fma_f32 v167, v167, v236, v228\n\t"
            "v_fma_f32 v172, v172, v236, v228\n\t"
            "v_exp_f32 v184, v184\n\t"
            "v_exp_f32 v185, v185\n\t"
            "v_fma_f32 v186, v186, v237, v229\n\t"
            "v_fma_f32 v187, v187, v237, v229\n\t"
            "v_max3_f32 v234, v212, v160, v156\n\t"
            "s_waitcnt lgkmcnt(1)\n\t"
            "v_max_f32 v234, v234, v230\n\t"
            "v_exp_f32 v166, v166\n\t"
            "v_exp_f32 v167, v167\n\t"
            "v_max_f32_dpp v234, v234, v234 row_ror:8 row_mask:0xf bank_mask:0xf\n\t"
            "v_exp_f32 v172, v172\n\t"
            "v_exp_f32 v186, v186\n\t"
            "v_max_f32_dpp v234, v234, v234 row_ror:4 row_mask:0xf bank_mask:0xf\n\t"
            "v_exp_f32 v187, v187\n\t"
            "v_fma_f32 v204, v204, v237, v229\n\t"
            "v_fma_f32 v205, v205, v237, v229\n\t"
            "v_fma_f32 v206, v206, v237, v229\n\t"
            "v_fma_f32 v207, v207, v237, v229\n\t"
            "v_fma_f32 v188, v188, v237, v229\n\t"
            "v_fma_f32 v189, v189, v237, v229\n\t"
            "v_exp_f32 v204, v204\n\t"
            "v_exp_f32 v205, v205\n\t"
            "v_exp_f32 v206, v206\n\t"
            "v_exp_f32 v207, v207\n\t"
            "v_exp_f32 v188, v188\n\t"
            "v_exp_f32 v189, v189\n\t"
            "v_fma_f32 v190, v190, v237, v229\n\t"
            "v_fma_f32 v191, v191, v237, v229\n\t"
            "v_fma_f32 v208, v208, v237, v229\n\t"
            "v_max_f32_dpp v234, v234, v234 row_ror:2 row_mask:0xf bank_mask:0xf\n\t"
            "v_cvt_pk_bf16_f32 v164, v164, v165\n\t"
            "v_exp_f32 v190, v190\n\t"
            "v_exp_f32 v191, v191\n\t"
            "v_exp_f32 v208, v208\n\t"
            "v_cvt_pk_bf16_f32 v184, v184, v185\n\t"
            "v_cndmask_b32_e64 v235, v212, v160, s[60:61]\n\t"
            "v_cvt_pk_bf16_f32 v165, v166, v167\n\t"
            "v_cvt_pk_bf16_f32 v172, v172, 0\n\t"
            "v_cvt_pk_bf16_f32 v185, v186, v187\n\t"
            "v_cndmask_b32_e64 v235, v235, v156, s[62:63]\n\t"
            "v_cndmask_b32_e64 v166, 0, v172, s[58:59]\n\t"
            "s_setprio 3\n\t"
            "v_mfma_f32_16x16x16_bf16 v[172:175], v[196:197], v[198:199], 0\n\t"
            "v_cvt_pk_bf16_f32 v186, v204, v205\n\t"
            "v_cvt_pk_bf16_f32 v187, v206, v207\n\t"
            "v_cvt_pk_bf16_f32 v188, v188, v189\n\t"
            "v_mfma_f32_16x16x16_bf16 v[204:207], v[176:177], v[198:199], 0\n\t"
            "v_max_f32_dpp v234, v234, v234 row_ror:1 row_mask:0xf bank_mask:0xf\n\t"
            "v_cndmask_b32_e64 v235, v235, v230, s[64:65]\n\t"
            "v_cvt_pk_bf16_f32 v189, v190, v191\n\t"
            "v_mfma_f32_16x16x32_bf16 v[172:175], v[192:195], v[200:203], v[172:175]\n\t"
            "v_cvt_pk_bf16_f32 v208, v208, 0\n\t"
            "v_sub_f32 v235, v235, v234\n\t"
            "v_mfma_f32_16x16x32_bf16 v[204:207], v[168:171], v[200:203], v[204:207]\n\t"
            "s_setprio 0\n\t"
            "v_cndmask_b32_e64 v190, 0, v208, s[58:59]\n\t"
            "s_waitcnt lgkmcnt(0)\n\t"
            "v_mul_f32 v235, v239, v235\n\t"
            "v_mfma_f32_16x16x16_bf16 v[208:211], v[220:221], v[198:199], 0\n\t"
            "v_exp_f32 v235, v235\n\t"
            "v_mov_b32 v167, 0\n\t"
            "v_cvt_pk_bf16_f32 v235, v235, 0\n\t"
            "v_cndmask_b32_e64 v235, 0, v235, s[66:67]\n\t"
            "v_mov_b32 v191, 0\n\t"
            "s_nop 0\n\t"
            "v_mfma_f32_16x16x32_bf16 v[208:211], v[216:219], v[200:203], v[208:211]\n\t"
            "ds_write_b16 v250, v235 offset:256\n\t"
            "s_nop 6\n\t"
            "v_max3_f32 v230, v208, v180, v181\n\t"
            "v_max3_f32 v230, v230, v182, v183\n\t"
            "v_max3_f32 v230, v230, v204, v205\n\t"
            "v_max3_f32 v230, v230, v206, v207\n\t"
            "v_max3_f32 v230, v230, v172, v173\n\t"
            "v_max3_f32 v230, v230, v174, v175\n\t"
            "v_mov_b32 v231, v230\n\t"
            "s_nop 1\n\t"
            "v_permlane32_swap_b32 v230, v231\n\t"
            "v_max_f32 v230, v230, v231\n\t"
            "v_mov_b32 v231, v230\n\t"
            "s_nop 1\n\t"
            "v_permlane16_swap_b32 v230, v231\n\t"
            "v_max_f32 v230, v230, v231\n\t"
            "v_mul_f32_e64 v230, -v230, v238\n\t"
            "v_fma_f32 v180, v180, v238, v230\n\t"
            "v_fma_f32 v181, v181, v238, v230\n\t"
            "v_exp_f32 v180, v180\n\t"
            "v_exp_f32 v181, v181\n\t"
            "v_fma_f32 v182, v182, v238, v230\n\t"
            "v_fma_f32 v183, v183, v238, v230\n\t"
            "v_exp_f32 v182, v182\n\t"
            "v_exp_f32 v183, v183\n\t"
            "v_fma_f32 v204, v204, v238, v230\n\t"
            "v_fma_f32 v205, v205, v238, v230\n\t"
            "v_fma_f32 v206, v206, v238, v230\n\t"
            "v_fma_f32 v207, v207, v238, v230\n\t"
            "v_exp_f32 v204, v204\n\t"
            "v_exp_f32 v205, v205\n\t"
            "v_exp_f32 v206, v206\n\t"
            "v_exp_f32 v207, v207\n\t"
            "v_fma_f32 v172, v172, v238, v230\n\t"
            "v_fma_f32 v173, v173, v238, v230\n\t"
            "v_cvt_pk_bf16_f32 v180, v180, v181\n\t"
            "v_exp_f32 v172, v172\n\t"
            "v_exp_f32 v173, v173\n\t"
            "v_fma_f32 v174, v174, v238, v230\n\t"
            "v_fma_f32 v175, v175, v238, v230\n\t"
            "v_fma_f32 v208, v208, v238, v230\n\t"
            "v_cvt_pk_bf16_f32 v181, v182, v183\n\t"
            "v_exp_f32 v174, v174\n\t"
            "v_exp_f32 v175, v175\n\t"
            "v_exp_f32 v208, v208\n\t"
            "v_cvt_pk_bf16_f32 v182, v204, v205\n\t"
            "v_cvt_pk_bf16_f32 v183, v206, v207\n\t"
            "v_cvt_pk_bf16_f32 v172, v172, v173\n\t"
            "v_cvt_pk_bf16_f32 v173, v174, v175\n\t"
            "v_cvt_pk_bf16_f32 v208, v208, 0\n\t"
            "v_cndmask_b32_e64 v174, 0, v208, s[58:59]\n\t"
            ".Lwa_in%=:\n\t"
            "s_barrier\n\t"
            "s_setprio 2\n\t"
            "v_mfma_f32_16x16x32_bf16 v[200:203], v[240:243], v[148:151], 0\n\t"
            "ds_read_b64 v[196:197], v247 offset:1536\n\t"
            "ds_read_b64 v[176:177], v247\n\t"
            "v_mfma_f32_16x16x32_bf16 v[200:203], v[240:243], v[164:167], v[200:203]\n\t"
            "ds_read_b64 v[220:221], v247 offset:3072\n\t"
            "v_add_u32 v235, s45, v248\n\t"
            "v_xor_b32 v234, s73, v254\n\t"
            "v_mov_b32 v199, 0\n\t"
            "v_mov_b32 v179, 0\n\t"
            "v_mov_b32 v223, 0\n\t"
            "v_add_u32 v232, s48, v244\n\t"
            "v_xor_b32 v233, 64, v232\n\t"
            "v_rcp_f32 v201, v200\n\t"
            "v_mov_b32 v175, 0\n\t"
            "ds_read_b128 v[168:171], v235\n\t"
            "ds_read_b128 v[192:195], v235 offset:1536\n\t"
            "ds_read_b128 v[216:219], v235 offset:3072\n\t"
            "ds_read_u16 v198, v234 offset:128\n\t"
            "ds_read_u16 v178, v234\n\t"
            "ds_read_u16 v222, v234 offset:256\n\t"
            "ds_read_b128 v[204:207], v232\n\t"
            "ds_read_b128 v[208:211], v233\n\t"
            "v_add_u32 v235, s48, v248\n\t"
            "v_mad_u32_u24 v234, v246, s75, v253\n\t"
            "ds_read_b128 v[228:231], v235 offset:18816\n\t"
            "s_waitcnt lgkmcnt(8)\n\t"
            "v_mfma_f32_16x16x32_bf16 v[224:227], v[168:171], v[148:151], 0\n\t"
            "s_waitcnt lgkmcnt(7)\n\t"
            "v_mfma_f32_16x16x32_bf16 v[212:215], v[192:195], v[148:151], 0\n\t"
            "s_waitcnt lgkmcnt(6)\n\t"
            "v_mfma_f32_16x16x32_bf16 v[148:151], v[216:219], v[148:151], 0\n\t"
            "s_waitcnt lgkmcnt(5)\n\t"
            "v_mfma_f32_16x16x32_bf16 v[212:215], v[196:199], v[164:167], v[212:215]\n\t"
            "s_waitcnt lgkmcnt(0)\n\t"
            "v_add_f32 v236, v228, v230\n\t"
            "v_add_f32 v237, v229, v231\n\t"
            "v_mfma_f32_16x16x32_bf16 v[224:227], v[176:179], v[164:167], v[224:227]\n\t"
            "ds_read_b128 v[228:231], v235 offset:19840\n\t"
            "v_mfma_f32_16x16x32_bf16 v[148:151], v[220:223], v[164:167], v[148:151]\n\t"
            "v_mfma_f32_16x16x32_bf16 v[144:147], v[0:3], v[204:207], 0\n\t"
            "s_nop 0\n\t"
            "v_mul_f32 v212, v201, v212\n\t"
            "v_mfma_f32_16x16x32_bf16 v[152:155], v[48:51], v[204:207], 0\n\t"
            "v_mul_f32 v213, v201, v213\n\t"
            "v_mul_f32 v214, v201, v214\n\t"
            "v_mfma_f32_16x16x32_bf16 v[156:159], v[72:75], v[204:207], 0\n\t"
            "v_mul_f32 v215, v201, v215\n\t"
            "v_cvt_pk_bf16_f32 v212, v212, v213\n\t"
            "v_mfma_f32_16x16x32_bf16 v[160:163], v[96:99], v[204:207], 0\n\t"
            "v_mul_f32 v224, v201, v224\n\t"
            "v_mul_f32 v225, v201, v225\n\t"
            "v_mfma_f32_16x16x32_bf16 v[164:167], v[120:123], v[204:207], 0\n\t"
            "v_mul_f32 v226, v201, v226\n\t"
            "v_mul_f32 v227, v201, v227\n\t"
            "v_mfma_f32_16x16x32_bf16 v[144:147], v[4:7], v[208:211], v[144:147]\n\t"
            "v_cvt_pk_bf16_f32 v213, v214, v215\n\t"
            "v_mul_f32 v148, v201, v148\n\t"
            "v_mfma_f32_16x16x32_bf16 v[152:155], v[52:55], v[208:211], v[152:155]\n\t"
            "v_mul_f32 v149, v201, v149\n\t"
            "v_mul_f32 v150, v201, v150\n\t"
            "v_mfma_f32_16x16x32_bf16 v[156:159], v[76:79], v[208:211], v[156:159]\n\t"
            "v_mul_f32 v151, v201, v151\n\t"
            "v_cvt_pk_bf16_f32 v224, v224, v225\n\t"
            "v_mfma_f32_16x16x32_bf16 v[200:203], v[240:243], v[184:187], 0\n\t"
            "ds_write_b64 v245, v[212:213] offset:128\n\t"
            "v_mfma_f32_16x16x32_bf16 v[212:215], v[192:195], v[184:187], 0\n\t"
            "v_cvt_pk_bf16_f32 v225, v226, v227\n\t"
            "v_cvt_pk_bf16_f32 v148, v148, v149\n\t"
            "v_mfma_f32_16x16x32_bf16 v[200:203], v[240:243], v[188:191], v[200:203]\n\t"
            "ds_write_b64 v245, v[224:225]\n\t"
            "v_mfma_f32_16x16x32_bf16 v[212:215], v[196:199], v[188:191], v[212:215]\n\t"
            "v_cvt_pk_bf16_f32 v149, v150, v151\n\t"
            "s_waitcnt lgkmcnt(2)\n\t"
            "v_add_f32 v228, v228, v230\n\t"
            "v_mfma_f32_16x16x32_bf16 v[224:227], v[168:171], v[184:187], 0\n\t"
            "ds_write_b64 v245, v[148:149] offset:256\n\t"
            "v_mfma_f32_16x16x32_bf16 v[184:187], v[216:219], v[184:187], 0\n\t"
            "v_rcp_f32 v201, v200\n\t"
            "v_mfma_f32_16x16x32_bf16 v[224:227], v[176:179], v[188:191], v[224:227]\n\t"
            "v_mul_f32 v212, v201, v212\n\t"
            "v_mul_f32 v213, v201, v213\n\t"
            "v_mfma_f32_16x16x32_bf16 v[184:187], v[220:223], v[188:191], v[184:187]\n\t"
            "v_mul_f32 v214, v201, v214\n\t"
            "v_mul_f32 v215, v201, v215\n\t"
            "v_mfma_f32_16x16x32_bf16 v[188:191], v[192:195], v[180:183], 0\n\t"
            "v_cvt_pk_bf16_f32 v212, v212, v213\n\t"
            "v_cvt_pk_bf16_f32 v213, v214, v215\n\t"
            "v_mfma_f32_16x16x32_bf16 v[188:191], v[196:199], v[172:175], v[188:191]\n\t"
            "ds_write_b64 v245, v[212:213] offset:6272\n\t"
            "v_mfma_f32_16x16x32_bf16 v[148:151], v[24:27], v[204:207], 0\n\t"
            "ds_read_b128 v[212:215], v232 offset:128\n\t"
            "ds_read_b128 v[204:207], v233 offset:128\n\t"
            "v_mfma_f32_16x16x32_bf16 v[148:151], v[28:31], v[208:211], v[148:151]\n\t"
            "v_mul_f32 v224, v201, v224\n\t"
            "v_mul_f32 v225, v201, v225\n\t"
            "v_mfma_f32_16x16x32_bf16 v[160:163], v[100:103], v[208:211], v[160:163]\n\t"
            "v_mul_f32 v226, v201, v226\n\t"
            "v_mul_f32 v227, v201, v227\n\t"
            "v_mfma_f32_16x16x32_bf16 v[164:167], v[124:127], v[208:211], v[164:167]\n\t"
            "ds_read_b128 v[208:211], v232 offset:256\n\t"
            "v_mul_f32 v184, v201, v184\n\t"
            "v_mul_f32 v185, v201, v185\n\t"
            "v_mul_f32 v186, v201, v186\n\t"
            "v_mul_f32 v187, v201, v187\n\t"
            "v_mfma_f32_16x16x32_bf16 v[200:203], v[240:243], v[180:183], 0\n\t"
            "v_cvt_pk_bf16_f32 v184, v184, v185\n\t"
            "v_cvt_pk_bf16_f32 v185, v186, v187\n\t"
            "v_mfma_f32_16x16x32_bf16 v[200:203], v[240:243], v[172:175], v[200:203]\n\t"
            "ds_read_b64 v[186:187], v234 offset:288\n\t"
            "ds_write_b64 v245, v[184:185] offset:6400\n\t"
            "ds_read_b64 v[184:185], v234 offset:256\n\t"
            "v_cvt_pk_bf16_f32 v224, v224, v225\n\t"
            "v_cvt_pk_bf16_f32 v225, v226, v227\n\t"
            "v_add_f32 v236, v236, v228\n\t"
            "s_waitcnt lgkmcnt(5)\n\t"
            "v_mfma_f32_16x16x32_bf16 v[144:147], v[8:11], v[212:215], v[144:147]\n\t"
            "v_rcp_f32 v201, v200\n\t"
            "v_mfma_f32_16x16x32_bf16 v[148:151], v[32:35], v[212:215], v[148:151]\n\t"
            "v_mul_f32 v188, v201, v188\n\t"
            "v_mul_f32 v189, v201, v189\n\t"
            "v_mfma_f32_16x16x32_bf16 v[152:155], v[56:59], v[212:215], v[152:155]\n\t"
            "v_mul_f32 v190, v201, v190\n\t"
            "v_mul_f32 v191, v201, v191\n\t"
            "v_mfma_f32_16x16x32_bf16 v[156:159], v[80:83], v[212:215], v[156:159]\n\t"
            "v_cvt_pk_bf16_f32 v188, v188, v189\n\t"
            "v_cvt_pk_bf16_f32 v189, v190, v191\n\t"
            "v_mfma_f32_16x16x32_bf16 v[160:163], v[104:107], v[212:215], v[160:163]\n\t"
            "ds_read_b64 v[190:191], v234 offset:352\n\t"
            "ds_write_b64 v245, v[188:189] offset:12416\n\t"
            "v_mfma_f32_16x16x32_bf16 v[164:167], v[128:131], v[212:215], v[164:167]\n\t"
            "ds_read_b64 v[188:189], v234 offset:320\n\t"
            "ds_read_b128 v[212:215], v233 offset:256\n\t"
            "s_waitcnt lgkmcnt(8)\n\t"
            "v_mfma_f32_16x16x32_bf16 v[144:147], v[12:15], v[204:207], v[144:147]\n\t"
            "ds_write_b64 v245, v[224:225] offset:6144\n\t"
            "v_mfma_f32_16x16x32_bf16 v[224:227], v[168:171], v[180:183], 0\n\t"
            "v_add_f32 v229, v229, v231\n\t"
            "v_mul_f32 v236, 0x3baaaaab, v236\n\t"
            "v_mfma_f32_16x16x32_bf16 v[180:183], v[216:219], v[180:183], 0\n\t"
            "v_add_f32 v237, v237, v229\n\t"
            "v_mul_f32_e64 v236, v236, -v236\n\t"
            "v_mfma_f32_16x16x32_bf16 v[224:227], v[176:179], v[172:175], v[224:227]\n\t"
            "v_fmac_f32 v236, 0x3baaaaab, v237\n\t"
            "v_max_f32 v236, 0, v236\n\t"
            "v_mfma_f32_16x16x32_bf16 v[180:183], v[220:223], v[172:175], v[180:183]\n\t"
            "v_add_f32 v236, 0x3727c5ac, v236\n\t"
            "v_rsq_f32 v236, v236\n\t"
            "v_mfma_f32_16x16x32_bf16 v[148:151], v[36:39], v[204:207], v[148:151]\n\t"
            "ds_write_b32 v249, v236\n\t"
            "v_mfma_f32_16x16x32_bf16 v[152:155], v[60:63], v[204:207], v[152:155]\n\t"
            "ds_read_b32 v236, v251\n\t"
            "ds_read_b32 v237, v251 offset:64\n\t"
            "v_mfma_f32_16x16x32_bf16 v[156:159], v[84:87], v[204:207], v[156:159]\n\t"
            "ds_read_b32 v238, v251 offset:128\n\t"
            "v_mul_f32 v224, v201, v224\n\t"
            "v_mfma_f32_16x16x32_bf16 v[160:163], v[108:111], v[204:207], v[160:163]\n\t"
            "v_mul_f32 v225, v201, v225\n\t"
            "v_mul_f32 v226, v201, v226\n\t"
            "v_mfma_f32_16x16x32_bf16 v[164:167], v[132:135], v[204:207], v[164:167]\n\t"
            "ds_read_b128 v[204:207], v232 offset:6144\n\t"
            "v_mul_f32 v227, v201, v227\n\t"
            "s_waitcnt lgkmcnt(13)\n\t"
            "v_mfma_f32_16x16x32_bf16 v[144:147], v[16:19], v[208:211], v[144:147]\n\t"
            "v_mul_f32 v180, v201, v180\n\t"
            "v_mul_f32 v181, v201, v181\n\t"
            "v_mfma_f32_16x16x32_bf16 v[148:151], v[40:43], v[208:211], v[148:151]\n\t"
            "v_mul_f32 v182, v201, v182\n\t"
            "v_mul_f32 v183, v201, v183\n\t"
            "s_waitcnt lgkmcnt(10)\n\t"
            "v_mfma_f32_16x16x32_bf16 v[200:203], v[240:243], v[184:187], 0\n\t"
            "v_cvt_pk_bf16_f32 v180, v180, v181\n\t"
            "v_cvt_pk_bf16_f32 v181, v182, v183\n\t"
            "v_mfma_f32_16x16x32_bf16 v[152:155], v[64:67], v[208:211], v[152:155]\n\t"
            "ds_write_b64 v245, v[180:181] offset:12544\n\t"
            "v_mfma_f32_16x16x32_bf16 v[156:159], v[88:91], v[208:211], v[156:159]\n\t"
            "v_cvt_pk_bf16_f32 v224, v224, v225\n\t"
            "v_cvt_pk_bf16_f32 v225, v226, v227\n\t"
            "v_mfma_f32_16x16x32_bf16 v[160:163], v[112:115], v[208:211], v[160:163]\n\t"
            "ds_write_b64 v245, v[224:225] offset:12288\n\t"
            "v_mfma_f32_16x16x32_bf16 v[164:167], v[136:139], v[208:211], v[164:167]\n\t"
            "ds_read_b128 v[208:211], v233 offset:6144\n\t"
            "v_mfma_f32_16x16x32_bf16 v[168:171], v[168:171], v[184:187], 0\n\t"
            "v_mfma_f32_16x16x32_bf16 v[192:195], v[192:195], v[184:187], 0\n\t"
            "v_mfma_f32_16x16x32_bf16 v[216:219], v[216:219], v[184:187], 0\n\t"
            "s_waitcnt lgkmcnt(10)\n\t"
            "v_mfma_f32_16x16x32_bf16 v[200:203], v[240:243], v[188:191], v[200:203]\n\t"
            "s_waitcnt lgkmcnt(9)\n\t"
            "v_mfma_f32_16x16x32_bf16 v[144:147], v[20:23], v[212:215], v[144:147]\n\t"
            "v_mfma_f32_16x16x32_bf16 v[148:151], v[44:47], v[212:215], v[148:151]\n\t"
            "v_mfma_f32_16x16x32_bf16 v[152:155], v[68:71], v[212:215], v[152:155]\n\t"
            "v_mfma_f32_16x16x32_bf16 v[156:159], v[92:95], v[212:215], v[156:159]\n\t"
            "v_mfma_f32_16x16x32_bf16 v[160:163], v[116:119], v[212:215], v[160:163]\n\t"
            "v_mfma_f32_16x16x32_bf16 v[164:167], v[140:143], v[212:215], v[164:167]\n\t"
            "ds_read_b128 v[212:215], v232 offset:6272\n\t"
            "v_mfma_f32_16x16x32_bf16 v[168:171], v[176:179], v[188:191], v[168:171]\n\t"
            "v_rcp_f32 v201, v200\n\t"
            "v_mfma_f32_16x16x32_bf16 v[192:195], v[196:199], v[188:191], v[192:195]\n\t"
            "v_xor_b32 v202, s73, v255\n\t"
            "s_waitcnt lgkmcnt(7)\n\t"
            "v_mul_f32 v144, v236, v144\n\t"
            "v_mfma_f32_16x16x32_bf16 v[216:219], v[220:223], v[188:191], v[216:219]\n\t"
            "v_mul_f32 v145, v236, v145\n\t"
            "v_mul_f32 v152, v236, v152\n\t"
            "s_waitcnt lgkmcnt(4)\n\t"
            "v_mfma_f32_16x16x32_bf16 v[172:175], v[24:27], v[204:207], 0\n\t"
            "v_mul_f32 v168, v201, v168\n\t"
            "v_mul_f32 v169, v201, v169\n\t"
            "v_mfma_f32_16x16x32_bf16 v[176:179], v[48:51], v[204:207], 0\n\t"
            "v_mul_f32 v192, v201, v192\n\t"
            "v_mul_f32 v193, v201, v193\n\t"
            "v_mfma_f32_16x16x32_bf16 v[180:183], v[72:75], v[204:207], 0\n\t"
            "v_mul_f32 v216, v201, v216\n\t"
            "v_mul_f32 v217, v201, v217\n\t"
            "v_mfma_f32_16x16x32_bf16 v[184:187], v[96:99], v[204:207], 0\n\t"
            "v_mul_f32 v170, v201, v170\n\t"
            "v_mul_f32 v171, v201, v171\n\t"
            "v_mfma_f32_16x16x32_bf16 v[188:191], v[120:123], v[204:207], 0\n\t"
            "v_cvt_pk_bf16_f32 v168, v168, v169\n\t"
            "v_mul_f32 v194, v201, v194\n\t"
            "s_waitcnt lgkmcnt(1)\n\t"
            "v_mfma_f32_16x16x32_bf16 v[172:175], v[28:31], v[208:211], v[172:175]\n\t"
            "v_mul_f32 v195, v201, v195\n\t"
            "v_cvt_pk_bf16_f32 v192, v192, v193\n\t"
            "v_mfma_f32_16x16x32_bf16 v[176:179], v[52:55], v[208:211], v[176:179]\n\t"
            "v_mul_f32 v218, v201, v218\n\t"
            "v_mul_f32 v219, v201, v219\n\t"
            "v_mfma_f32_16x16x32_bf16 v[180:183], v[76:79], v[208:211], v[180:183]\n\t"
            "v_cvt_pk_bf16_f32 v216, v216, v217\n\t"
            "v_cvt_pk_bf16_f32 v169, v170, v171\n\t"
            "v_mfma_f32_16x16x32_bf16 v[184:187], v[100:103], v[208:211], v[184:187]\n\t"
            "v_cvt_pk_bf16_f32 v193, v194, v195\n\t"
            "v_cvt_pk_bf16_f32 v217, v218, v219\n\t"
            "v_mfma_f32_16x16x32_bf16 v[188:191], v[124:127], v[208:211], v[188:191]\n\t"
            "s_mov_b64 exec, s[68:69]\n\t"
            "ds_write_b64 v202, v[168:169]\n\t"
            "ds_write_b64 v202, v[192:193] offset:128\n\t"
            "ds_write_b64 v202, v[216:217] offset:256\n\t"
            "s_mov_b64 exec, -1\n\t"
            "v_mfma_f32_16x16x32_bf16 v[168:171], v[0:3], v[204:207], 0\n\t"
            "ds_read_b128 v[204:207], v233 offset:6272\n\t"
            "v_mul_f32 v153, v236, v153\n\t"
            "v_mfma_f32_16x16x32_bf16 v[168:171], v[4:7], v[208:211], v[168:171]\n\t"
            "ds_read_b128 v[208:211], v232 offset:6400\n\t"
            "v_mul_f32 v146, v236, v146\n\t"
            "s_waitcnt lgkmcnt(5)\n\t"
            "v_mfma_f32_16x16x32_bf16 v[168:171], v[8:11], v[212:215], v[168:171]\n\t"
            "v_mul_f32 v147, v236, v147\n\t"
            "v_mul_f32 v154, v236, v154\n\t"
            "v_mfma_f32_16x16x32_bf16 v[172:175], v[32:35], v[212:215], v[172:175]\n\t"
            "v_mul_f32 v155, v236, v155\n\t"
            "v_cvt_pk_bf16_f32 v144, v144, v145\n\t"
            "v_mfma_f32_16x16x32_bf16 v[176:179], v[56:59], v[212:215], v[176:179]\n\t"
            "v_cvt_pk_bf16_f32 v152, v152, v153\n\t"
            "v_cvt_pk_bf16_f32 v156, v156, v157\n\t"
            "v_mfma_f32_16x16x32_bf16 v[180:183], v[80:83], v[212:215], v[180:183]\n\t"
            "v_mul_f32 v148, v236, v148\n\t"
            "v_mul_f32 v149, v236, v149\n\t"
            "v_mfma_f32_16x16x32_bf16 v[184:187], v[104:107], v[212:215], v[184:187]\n\t"
            "v_mul_f32 v150, v236, v150\n\t"
            "v_mul_f32 v151, v236, v151\n\t"
            "v_mfma_f32_16x16x32_bf16 v[188:191], v[128:131], v[212:215], v[188:191]\n\t"
            "ds_read_b128 v[212:215], v233 offset:6400\n\t"
            "v_cvt_pk_bf16_f32 v145, v146, v147\n\t"
            "v_cvt_pk_bf16_f32 v153, v154, v155\n\t"
            "s_waitcnt lgkmcnt(2)\n\t"
            "v_mfma_f32_16x16x32_bf16 v[168:171], v[12:15], v[204:207], v[168:171]\n\t"
            "v_cvt_pk_bf16_f32 v157, v158, v159\n\t"
            "v_cvt_pk_bf16_f32 v146, v148, v149\n\t"
            "v_mfma_f32_16x16x32_bf16 v[172:175], v[36:39], v[204:207], v[172:175]\n\t"
            "v_cvt_pk_bf16_f32 v147, v150, v151\n\t"
            "v_cvt_pk_bf16_f32 v158, v160, v161\n\t"
            "v_mfma_f32_16x16x32_bf16 v[176:179], v[60:63], v[204:207], v[176:179]\n\t"
            "s_cmp_eq_u32 s44, 0\n\t"
            "s_barrier\n\t"
            "s_cbranch_scc1 .Lwa_done%=\n\t"
            "v_cvt_pk_bf16_f32 v159, v162, v163\n\t"
            "v_cvt_pk_bf16_f32 v154, v164, v165\n\t"
            "v_mfma_f32_16x16x32_bf16 v[180:183], v[84:87], v[204:207], v[180:183]\n\t"
            "v_cvt_pk_bf16_f32 v155, v166, v167\n\t"
            "v_mul_f32 v236, 0x3e553b94, v236\n\t"
            "v_mfma_f32_16x16x32_bf16 v[184:187], v[108:111], v[204:207], v[184:187]\n\t"
            "v_mfma_f32_16x16x32_bf16 v[188:191], v[132:135], v[204:207], v[188:191]\n\t"
            "ds_read_b128 v[204:207], v232 offset:12288\n\t"
            "s_waitcnt lgkmcnt(2)\n\t"
            "v_mfma_f32_16x16x32_bf16 v[168:171], v[16:19], v[208:211], v[168:171]\n\t"
            "v_mfma_f32_16x16x32_bf16 v[172:175], v[40:43], v[208:211], v[172:175]\n\t"
            "v_mfma_f32_16x16x32_bf16 v[176:179], v[64:67], v[208:211], v[176:179]\n\t"
            "v_mfma_f32_16x16x32_bf16 v[180:183], v[88:91], v[208:211], v[180:183]\n\t"
            "v_mfma_f32_16x16x32_bf16 v[184:187], v[112:115], v[208:211], v[184:187]\n\t"
            "v_mfma_f32_16x16x32_bf16 v[188:191], v[136:139], v[208:211], v[188:191]\n\t"
            "ds_read_b128 v[208:211], v233 offset:12288\n\t"
            "s_waitcnt lgkmcnt(2)\n\t"
            "v_mfma_f32_16x16x32_bf16 v[168:171], v[20:23], v[212:215], v[168:171]\n\t"
            "v_mfma_f32_16x16x32_bf16 v[172:175], v[44:47], v[212:215], v[172:175]\n\t"
            "v_mfma_f32_16x16x32_bf16 v[176:179], v[68:71], v[212:215], v[176:179]\n\t"
            "v_mfma_f32_16x16x32_bf16 v[180:183], v[92:95], v[212:215], v[180:183]\n\t"
            "v_mfma_f32_16x16x32_bf16 v[184:187], v[116:119], v[212:215], v[184:187]\n\t"
            "v_mfma_f32_16x16x32_bf16 v[188:191], v[140:143], v[212:215], v[188:191]\n\t"
            "ds_read_b128 v[212:215], v232 offset:12416\n\t"
            "s_branch .Lwa_loop%=\n\t"
            ".Lwa_done%=:\n\t"
            "s_waitcnt lgkmcnt(0)\n\t"
            "s_nop 7\n\t"
            : "+{v[0:3]}"(W[0]), "+{v[4:7]}"(W[1]), "+{v[8:11]}"(W[2]), "+{v[12:15]}"(W[3]), "+{v[16:19]}"(W[4]), "+{v[20:23]}"(W[5]), "+{v[24:27]}"(W[6]), "+{v[28:31]}"(W[7]), "+{v[32:35]}"(W[8]), "+{v[36:39]}"(W[9]), "+{v[40:43]}"(W[10]), "+{v[44:47]}"(W[11]), "+{v[48:51]}"(W[12]), "+{v[52:55]}"(W[13]), "+{v[56:59]}"(W[14]), "+{v[60:63]}"(W[15]), "+{v[64:67]}"(W[16]), "+{v[68:71]}"(W[17]), "+{v[72:75]}"(W[18]), "+{v[76:79]}"(W[19]), "+{v[80:83]}"(W[20]), "+{v[84:87]}"(W[21]), "+{v[88:91]}"(W[22]), "+{v[92:95]}"(W[23]), "+{v[96:99]}"(W[24]), "+{v[100:103]}"(W[25]), "+{v[104:107]}"(W[26]), "+{v[108:111]}"(W[27]), "+{v[112:115]}"(W[28]), "+{v[116:119]}"(W[29]), "+{v[120:123]}"(W[30]), "+{v[124:127]}"(W[31]), "+{v[128:131]}"(W[32]), "+{v[132:135]}"(W[33]), "+{v[136:139]}"(W[34]), "+{v[140:143]}"(W[35]),
              "+{s44}"(s44), "+{s48}"(s48), "+{s70}"(s70), "+{s72}"(s72), "+{s73}"(s73), "+{s74}"(s74), "+{s76}"(s76), "+{s77}"(s77), "+{s[68:69]}"(s68)
            : "{v244}"(pE_), "{v245}"(aO_), "{v246}"(sel_), "{v247}"(aVB_), "{v248}"(pST_), "{v249}"(aRSw_), "{v250}"(aPLw_), "{v251}"(aRSr_), "{v252}"(g16_), "{v253}"(g8_), "{v254}"(loVL_), "{v255}"(loOL_), "{v[240:243]}"(ones4), "{s47}"((ldsb + X_OFF) ^ (ldsb + X1_OFF)),
              "{s[58:59]}"(0xFFFFull), "{s[60:61]}"(0xFFFF0000ull), "{s[62:63]}"(0xFFFF00000000ull), "{s[64:65]}"(0xFFFF000000000000ull),
              "{s[66:67]}"(0x1FFFFFFFFFFFFull), "{s75}"(rsb), "{s45}"(ldsb + V_OFF + w * 4608)
            : "v144", "v145", "v146", "v147", "v148", "v149", "v150", "v151", "v152", "v153", "v154", "v155", "v156", "v157", "v158", "v159", "v160", "v161", "v162", "v163", "v164", "v165", "v166", "v167", "v168", "v169", "v170", "v171", "v172", "v173", "v174", "v175", "v176", "v177", "v178", "v179", "v180", "v181", "v182", "v183", "v184", "v185", "v186", "v187", "v188", "v189", "v190", "v191", "v192", "v193", "v194", "v195", "v196", "v197", "v198", "v199", "v200", "v201", "v202", "v203", "v204", "v205", "v206", "v207", "v208", "v209", "v210", "v211", "v212", "v213", "v214", "v215", "v216", "v217", "v218", "v219", "v220", "v221", "v222", "v223", "v224", "v225", "v226", "v227", "v228", "v229", "v230", "v231", "v232", "v233", "v234", "v235", "v236", "v237", "v238", "v239", "s49", "vcc", "scc", "memory");
        }
        asm volatile("wk_code_end:");

        if (kq == 40 && wave == 0) {
#pragma unroll 1
            for (int it = 0; it < SPIN_MAX && __hip_atomic_load((gu32*)((char*)wf + CN_OFF), RLX_AGENT) != (unsigned)GRID; ++it) __builtin_amdgcn_s_sleep(8);
            __hip_atomic_store((gu32*)((char*)wf + EP_OFF), __hip_atomic_load((gu32*)((char*)wf + EP_OFF), RLX_AGENT) + 1u, RLX_AGENT);
        }
    } else if (wave < 6) {
        B_PROLOGUE(true)
        {
            LANE_SETUP
            const unsigned rsb = ldsb + RS_BASE;
            const unsigned pE_ = fE - X_OFF;
            const unsigned aVAw_ = ldsb + V_OFF + w * 9216 + lane * 16, aVBw_ = ldsb + V_OFF + w * 9216 + 1024 + lane * 8;
            const unsigned pST_ = lane * 16, aRSw_ = (lane < 48 ? rsb : ldsb + RS_OFF + 2304 - 192) + lane * 4, aRSr_ = rsb + (lane & 48);
            B_SCALARS
            asm volatile(
            "s_waitcnt lgkmcnt(0)\n\t"
            "s_nop 7\n\t"
            "ds_read_b128 v[168:171], v254\n\t"
            "ds_read_b128 v[172:175], v254 offset:64\n\t"
            "s_waitcnt vmcnt(0)\n\t"
            "ds_read_b64 v[144:145], v253\n\t"
            "ds_read_b64 v[146:147], v253 offset:96\n\t"
            "ds_read_b64 v[148:149], v253 offset:1744\n\t"
            "ds_read_b64 v[150:151], v253 offset:1840\n\t"
            "ds_read_b64 v[152:153], v253 offset:8\n\t"
            "ds_read_b64 v[154:155], v253 offset:104\n\t"
            "ds_read_b64 v[156:157], v253 offset:1752\n\t"
            "ds_read_b64 v[158:159], v253 offset:1848\n\t"
            "ds_read_b64 v[160:161], v253 offset:16\n\t"
            "ds_read_b64 v[162:163], v253 offset:112\n\t"
            "ds_read_b64 v[164:165], v253 offset:1760\n\t"
            "ds_read_b64 v[166:167], v253 offset:1856\n\t"
            "s_waitcnt lgkmcnt(0)\n\t"
            "s_nop 7\n\t"
            ".Lwc_loop%=:\n\t"
            "s_setprio 1\n\t"
            "v_mov_b64 v[176:177], 0\n\t"
            "v_mov_b64 v[178:179], 0\n\t"
            "v_mov_b64 v[180:181], 0\n\t"
            "v_mov_b64 v[182:183], 0\n\t"
            "s_setprio 3\n\t"
            "v_smfmac_f32_16x16x64_bf16 v[176:179], v[168:171], v[144:151], v255\n\t"
            "ds_read_b128 v[168:171], v254 offset:128\n\t"
            "v_smfmac_f32_16x16x64_bf16 v[180:183], v[172:175], v[152:159], v255\n\t"
            "s_setprio 1\n\t"
            "ds_read_b64 v[144:145], v253 offset:24\n\t"
            "ds_read_b64 v[146:147], v253 offset:120\n\t"
            "ds_read_b64 v[148:149], v253 offset:1768\n\t"
            "ds_read_b64 v[150:151], v253 offset:1864\n\t"
            "ds_read_b128 v[172:175], v254 offset:192\n\t"
            "ds_read_b64 v[152:153], v253 offset:32\n\t"
            "ds_read_b64 v[154:155], v253 offset:128\n\t"
            "ds_read_b64 v[156:157], v253 offset:1776\n\t"
            "ds_read_b64 v[158:159], v253 offset:1872\n\t"
            "v_mov_b32 v249, s76\n\t"
            "v_mov_b64 v[184:185], 0\n\t"
            "v_mov_b64 v[186:187], 0\n\t"
            "v_mov_b64 v[188:189], 0\n\t"
            "v_mov_b64 v[190:191], 0\n\t"
            "v_cvt_pk_bf16_f32 v176, v176, v177\n\t"
            "v_add_u32 v236, s67, v250\n\t"
            "v_cvt_pk_bf16_f32 v177, v178, v179\n\t"
            "v_cvt_pk_bf16_f32 v178, v180, v181\n\t"
            "v_cvt_pk_bf16_f32 v179, v182, v183\n\t"
            "s_setprio 3\n\t"
            "v_mfma_f32_4x4x4_16b_bf16 v[196:199], v[176:177], v[176:177], 0\n\t"
            "ds_read_b32 v249, v249\n\t"
            "v_mfma_f32_16x16x32_bf16 v[192:195], v[238:241], v[176:179], 0\n\t"
            "v_mov_b64 v[180:181], 0\n\t"
            "v_mov_b64 v[182:183], 0\n\t"
            "v_mfma_f32_4x4x4_16b_bf16 v[196:199], v[178:179], v[178:179], v[196:199]\n\t"
            "v_add_u32 v234, s48, v245\n\t"
            "s_waitcnt lgkmcnt(10)\n\t"
            "v_smfmac_f32_16x16x64_bf16 v[184:187], v[168:171], v[160:167], v255\n\t"
            "ds_read_b128 v[168:171], v254 offset:256\n\t"
            "ds_read_b64 v[160:161], v253 offset:40\n\t"
            "ds_read_b64 v[162:163], v253 offset:136\n\t"
            "ds_read_b64 v[164:165], v253 offset:1784\n\t"
            "ds_read_b64 v[166:167], v253 offset:1880\n\t"
            "s_waitcnt lgkmcnt(10)\n\t"
            "v_smfmac_f32_16x16x64_bf16 v[188:191], v[172:175], v[144:151], v255\n\t"
            "s_setprio 1\n\t"
            "ds_read_b128 v[172:175], v254 offset:320\n\t"
            "ds_read_b64 v[144:145], v253 offset:48\n\t"
            "ds_read_b64 v[146:147], v253 offset:144\n\t"
            "ds_read_b64 v[148:149], v253 offset:1792\n\t"
            "ds_read_b64 v[150:151], v253 offset:1888\n\t"
            "s_waitcnt lgkmcnt(14)\n\t"
            "ds_read_b128 v[200:203], v234 offset:18816\n\t"
            "s_waitcnt lgkmcnt(14)\n\t"
            "ds_read_b128 v[204:207], v234 offset:19840\n\t"
            "v_cvt_pk_bf16_f32 v184, v184, v185\n\t"
            "v_cvt_pk_bf16_f32 v185, v186, v187\n\t"
            "v_cvt_pk_bf16_f32 v186, v188, v189\n\t"
            "v_cvt_pk_bf16_f32 v187, v190, v191\n\t"
            "s_setprio 3\n\t"
            "v_mfma_f32_4x4x4_16b_bf16 v[196:199], v[184:185], v[184:185], v[196:199]\n\t"
            "v_mov_b64 v[188:189], 0\n\t"
            "v_mfma_f32_16x16x32_bf16 v[192:195], v[238:241], v[184:187], v[192:195]\n\t"
            "s_waitcnt lgkmcnt(12)\n\t"
            "v_readfirstlane_b32 s26, v249\n\t"
            "v_mov_b64 v[190:191], 0\n\t"
            "v_mfma_f32_4x4x4_16b_bf16 v[196:199], v[186:187], v[186:187], v[196:199]\n\t"
            "ds_read_b32 v249, v252 offset:1024\n\t"
            "s_and_b32 s68, s26, 0x7ffffff\n\t"
            "s_add_u32 s68, s68, s78\n\t"
            "s_and_b32 s69, s26, 0x38000000\n\t"
            "s_or_b32 s69, s69, s39\n\t"
            "s_bitcmp1_b32 s26, 27\n\t"
            "s_cselect_b32 s79, 2.0, s68\n\t"
            "s_and_b32 s57, s56, 3\n\t"
            "s_lshl_b32 s58, s57, 3\n\t"
            "s_lshr_b32 m0, s54, s58\n\t"
            "s_lshl_b32 s58, s57, 4\n\t"
            "s_lshr_b64 s[36:37], s[50:51], s58\n\t"
            "s_and_b32 s57, s36, 0xffff\n\t"
            "s_add_u32 s57, s57, s67\n\t"
            "s_waitcnt lgkmcnt(7)\n\t"
            "v_smfmac_f32_16x16x64_bf16 v[180:183], v[172:175], v[160:167], v255\n\t"
            "s_setprio 1\n\t"
            "s_lshr_b64 s[36:37], s[52:53], s58\n\t"
            "s_and_b32 s58, s36, 0xffff\n\t"
            "s_add_u32 s58, s58, s67\n\t"
            "ds_read_b128 v[172:175], v254 offset:448\n\t"
            "ds_read_b64 v[160:161], v253 offset:64\n\t"
            "ds_read_b64 v[162:163], v253 offset:160\n\t"
            "ds_read_b64 v[164:165], v253 offset:1808\n\t"
            "ds_read_b64 v[166:167], v253 offset:1904\n\t"
            "s_and_b32 s59, s50, 0xffff\n\t"
            "s_add_u32 s59, s59, s67\n\t"
            "s_and_b32 s38, s52, 0xffff\n\t"
            "s_add_u32 s38, s38, s67\n\t"
            "v_writelane_b32 v236, s59, m0\n\t"
            "v_writelane_b32 v236, s57, 63\n\t"
            "v_xor_b32 v237, 16, v236\n\t"
            "v_add_u32 v248, s67, v251\n\t"
            "v_writelane_b32 v248, s38, m0\n\t"
            "v_add_u32 v232, s48, v242\n\t"
            "v_writelane_b32 v248, s58, 63\n\t"
            "v_xor_b32 v233, 64, v232\n\t"
            "s_waitcnt lgkmcnt(7)\n\t"
            "v_add_f32 v200, v200, v202\n\t"
            "s_waitcnt lgkmcnt(6)\n\t"
            "v_add_f32 v204, v204, v206\n\t"
            "v_add_f32 v200, v200, v204\n\t"
            "v_add_f32 v201, v201, v203\n\t"
            "v_add_f32 v205, v205, v207\n\t"
            "v_mul_f32 v200, 0x3baaaaab, v200\n\t"
            "v_add_f32 v201, v201, v205\n\t"
            "v_mul_f32_e64 v200, v200, -v200\n\t"
            "v_fmac_f32 v200, 0x3baaaaab, v201\n\t"
            "v_max_f32 v200, 0, v200\n\t"
            "v_add_f32 v200, 0x3727c5ac, v200\n\t"
            "v_rsq_f32 v200, v200\n\t"
            "s_waitcnt lgkmcnt(5)\n\t"
            "v_and_b32 v249, s69, v249\n\t"
            "ds_read_b128 v[224:227], v232\n\t"
            "ds_write_b128 v236, v[176:179]\n\t"
            "v_mov_b64 v[176:177], 0\n\t"
            "v_mov_b64 v[178:179], 0\n\t"
            "s_setprio 3\n\t"
            "s_nop 0\n\t"
            "v_smfmac_f32_16x16x64_bf16 v[176:179], v[168:171], v[152:159], v255\n\t"
            "s_setprio 1\n\t"
            "ds_read_b128 v[168:171], v254 offset:384\n\t"
            "ds_read_b64 v[152:153], v253 offset:56\n\t"
            "ds_read_b64 v[154:155], v253 offset:152\n\t"
            "ds_read_b64 v[156:157], v253 offset:1800\n\t"
            "ds_read_b64 v[158:159], v253 offset:1896\n\t"
            "ds_write_b128 v237, v[184:187]\n\t"
            "v_mov_b64 v[184:185], 0\n\t"
            "v_mov_b64 v[186:187], 0\n\t"
            "v_cvt_pk_bf16_f32 v176, v176, v177\n\t"
            "v_cvt_pk_bf16_f32 v177, v178, v179\n\t"
            "v_cvt_pk_bf16_f32 v178, v180, v181\n\t"
            "v_cvt_pk_bf16_f32 v179, v182, v183\n\t"
            "s_setprio 3\n\t"
            "v_mfma_f32_4x4x4_16b_bf16 v[196:199], v[176:177], v[176:177], v[196:199]\n\t"
            "ds_write_b128 v236, v[176:179] offset:128\n\t"
            "v_mfma_f32_16x16x32_bf16 v[192:195], v[238:241], v[176:179], v[192:195]\n\t"
            "v_mov_b64 v[176:177], 0\n\t"
            "v_mov_b64 v[180:181], 0\n\t"
            "v_mfma_f32_4x4x4_16b_bf16 v[196:199], v[178:179], v[178:179], v[196:199]\n\t"
            "v_mov_b64 v[178:179], 0\n\t"
            "s_waitcnt lgkmcnt(8)\n\t"
            "v_mfma_f32_16x16x32_bf16 v[204:207], v[224:227], v[24:27], 0\n\t"
            "v_mov_b64 v[182:183], 0\n\t"
            "s_waitcnt lgkmcnt(6)\n\t"
            "v_smfmac_f32_16x16x64_bf16 v[184:187], v[168:171], v[144:151], v255\n\t"
            "ds_read_b128 v[168:171], v254 offset:512\n\t"
            "s_waitcnt lgkmcnt(3)\n\t"
            "v_smfmac_f32_16x16x64_bf16 v[188:191], v[172:175], v[152:159], v255\n\t"
            "ds_read_b64 v[144:145], v253 offset:72\n\t"
            "v_mfma_f32_16x16x32_bf16 v[208:211], v[224:227], v[48:51], 0\n\t"
            "ds_read_b64 v[146:147], v253 offset:168\n\t"
            "ds_read_b64 v[148:149], v253 offset:1816\n\t"
            "v_mfma_f32_16x16x32_bf16 v[212:215], v[224:227], v[72:75], 0\n\t"
            "ds_read_b64 v[150:151], v253 offset:1912\n\t"
            "ds_read_b128 v[172:175], v254 offset:576\n\t"
            "v_mfma_f32_16x16x32_bf16 v[216:219], v[224:227], v[96:99], 0\n\t"
            "ds_read_b64 v[152:153], v253 offset:80\n\t"
            "ds_read_b64 v[154:155], v253 offset:176\n\t"
            "v_mfma_f32_16x16x32_bf16 v[220:223], v[224:227], v[120:123], 0\n\t"
            "s_setprio 1\n\t"
            "ds_read_b64 v[156:157], v253 offset:1824\n\t"
            "ds_read_b64 v[158:159], v253 offset:1920\n\t"
            "v_cvt_pk_bf16_f32 v184, v184, v185\n\t"
            "v_cvt_pk_bf16_f32 v185, v186, v187\n\t"
            "v_cvt_pk_bf16_f32 v186, v188, v189\n\t"
            "v_cvt_pk_bf16_f32 v187, v190, v191\n\t"
            "s_setprio 3\n\t"
            "v_mfma_f32_4x4x4_16b_bf16 v[196:199], v[184:185], v[184:185], v[196:199]\n\t"
            "ds_write_b128 v237, v[184:187] offset:128\n\t"
            "s_waitcnt lgkmcnt(10)\n\t"
            "v_smfmac_f32_16x16x64_bf16 v[176:179], v[168:171], v[160:167], v255\n\t"
            "ds_read_b128 v[168:171], v254 offset:640\n\t"
            "v_mfma_f32_16x16x32_bf16 v[192:195], v[238:241], v[184:187], v[192:195]\n\t"
            "s_barrier\n\t"
            "ds_read_b64 v[160:161], v253 offset:88\n\t"
            "ds_read_b64 v[162:163], v253 offset:184\n\t"
            "v_mfma_f32_4x4x4_16b_bf16 v[196:199], v[186:187], v[186:187], v[196:199]\n\t"
            "ds_read_b64 v[164:165], v253 offset:1832\n\t"
            "ds_read_b64 v[166:167], v253 offset:1928\n\t"
            "v_mov_b64 v[184:185], 0\n\t"
            "s_waitcnt lgkmcnt(10)\n\t"
            "v_smfmac_f32_16x16x64_bf16 v[180:183], v[172:175], v[144:151], v255\n\t"
            "s_setprio 1\n\t"
            "ds_read_b128 v[172:175], v254 offset:704\n\t"
            "v_mov_b64 v[186:187], 0\n\t"
            "v_cvt_pk_bf16_f32 v176, v176, v177\n\t"
            "v_mov_b64 v[188:189], 0\n\t"
            "v_mov_b64 v[190:191], 0\n\t"
            "v_cvt_pk_bf16_f32 v177, v178, v179\n\t"
            "s_setprio 3\n\t"
            "s_nop 0\n\t"
            "v_mfma_f32_4x4x4_16b_bf16 v[196:199], v[176:177], v[176:177], v[196:199]\n\t"
            "ds_write_b32 v246, v200\n\t"
            "v_mfma_f32_16x16x32_bf16 v[200:203], v[224:227], v[0:3], 0\n\t"
            "ds_read_b128 v[224:227], v233 offset:128\n\t"
            "ds_read_b128 v[228:231], v247\n\t"
            "v_cvt_pk_bf16_f32 v178, v180, v181\n\t"
            "v_cvt_pk_bf16_f32 v179, v182, v183\n\t"
            "s_waitcnt lgkmcnt(8)\n\t"
            "v_smfmac_f32_16x16x64_bf16 v[184:187], v[168:171], v[152:159], v255\n\t"
            "ds_write_b128 v236, v[176:179] offset:256\n\t"
            "v_mfma_f32_4x4x4_16b_bf16 v[196:199], v[178:179], v[178:179], v[196:199]\n\t"
            "v_mfma_f32_16x16x32_bf16 v[192:195], v[238:241], v[176:179], v[192:195]\n\t"
            "s_waitcnt lgkmcnt(4)\n\t"
            "v_smfmac_f32_16x16x64_bf16 v[188:191], v[172:175], v[160:167], v255\n\t"
            "s_setprio 1\n\t"
            "s_mov_b32 exec_hi, 0x3fffff\n\t"
            "s_add_u32 m0, s66, 0\n\t"
            "s_add_u32 s49, s79, 0\n\t"
            "buffer_load_dwordx4 v249, s[72:75], s49 offen lds\n\t"
            "s_mov_b32 exec_hi, -1\n\t"
            "v_cvt_pk_bf16_f32 v184, v184, v185\n\t"
            "v_cvt_pk_bf16_f32 v185, v186, v187\n\t"
            "v_cvt_pk_bf16_f32 v186, v188, v189\n\t"
            "v_cvt_pk_bf16_f32 v187, v190, v191\n\t"
            "s_setprio 3\n\t"
            "v_mfma_f32_4x4x4_16b_bf16 v[196:199], v[184:185], v[184:185], v[196:199]\n\t"
            "ds_write_b128 v237, v[184:187] offset:256\n\t"
            "v_mfma_f32_16x16x32_bf16 v[192:195], v[238:241], v[184:187], v[192:195]\n\t"
            "s_mov_b32 exec_hi, 0x3fffff\n\t"
            "s_add_u32 m0, s66, 872\n\t"
            "s_add_u32 s49, s68, 196224\n\t"
            "buffer_load_dwordx4 v249, s[72:75], s49 offen lds\n\t"
            "s_mov_b32 exec_hi, -1\n\t"
            "v_mfma_f32_4x4x4_16b_bf16 v[196:199], v[186:187], v[186:187], v[196:199]\n\t"
            "s_setprio 1\n\t"
            "s_mov_b32 exec_hi, 0x3fffff\n\t"
            "s_add_u32 m0, s66, 1744\n\t"
            "s_add_u32 s49, s68, 392448\n\t"
            "buffer_load_dwordx4 v249, s[72:75], s49 offen lds\n\t"
            "s_mov_b32 exec_hi, -1\n\t"
            "s_mov_b32 exec_hi, 0x3fffff\n\t"
            "s_add_u32 m0, s66, 2616\n\t"
            "s_add_u32 s49, s68, 588672\n\t"
            "buffer_load_dwordx4 v249, s[72:75], s49 offen lds\n\t"
            "s_mov_b32 exec_hi, -1\n\t"
            "v_cndmask_b32_e64 v193, v196, v197, s[60:61]\n\t"
            "v_cndmask_b32_e64 v193, v193, v198, s[62:63]\n\t"
            "v_cndmask_b32_e64 v193, v193, v199, s[64:65]\n\t"
            "s_setprio 1\n\t"
            "ds_read_b128 v[196:199], v232 offset:128\n\t"
            "ds_write_b64 v248, v[192:193] offset:18816\n\t"
            "ds_read_b128 v[192:195], v233\n\t"
            "s_mov_b32 exec_hi, 0x3fffff\n\t"
            "s_add_u32 m0, s66, 3488\n\t"
            "s_add_u32 s49, s68, 784896\n\t"
            "buffer_load_dwordx4 v249, s[72:75], s49 offen lds\n\t"
            "s_mov_b32 exec_hi, -1\n\t"
            "s_mov_b32 exec_hi, 0x3fffff\n\t"
            "s_add_u32 m0, s66, 4360\n\t"
            "s_add_u32 s49, s68, 981120\n\t"
            "buffer_load_dwordx4 v249, s[72:75], s49 offen lds\n\t"
            "s_mov_b32 exec_hi, -1\n\t"
            "s_mov_b32 exec_hi, 0x3fffff\n\t"
            "s_add_u32 m0, s66, 5232\n\t"
            "s_add_u32 s49, s68, 1177344\n\t"
            "buffer_load_dwordx4 v249, s[72:75], s49 offen lds\n\t"
            "s_mov_b32 exec_hi, -1\n\t"
            "s_waitcnt lgkmcnt(0)\n\t"
            "v_mfma_f32_16x16x32_bf16 v[200:203], v[192:195], v[4:7], v[200:203]\n\t"
            "s_mov_b32 exec_hi, 0x3fffff\n\t"
            "s_add_u32 m0, s66, 6104\n\t"
            "s_add_u32 s49, s68, 1373568\n\t"
            "buffer_load_dwordx4 v249, s[72:75], s49 offen lds\n\t"
            "s_mov_b32 exec_hi, -1\n\t"
            "v_mfma_f32_16x16x32_bf16 v[204:207], v[192:195], v[28:31], v[204:207]\n\t"
            "s_mov_b32 exec_hi, 0x3fffff\n\t"
            "s_add_u32 m0, s66, 6976\n\t"
            "s_add_u32 s49, s68, 1569792\n\t"
            "buffer_load_dwordx4 v249, s[72:75], s49 offen lds\n\t"
            "s_mov_b32 exec_hi, -1\n\t"
            "v_mfma_f32_16x16x32_bf16 v[208:211], v[192:195], v[52:55], v[208:211]\n\t"
            "s_add_u32 s45, s46, s27\n\t"
            "s_mov_b32 s46, s47\n\t"
            "s_mov_b32 s47, s68\n\t"
            "v_mfma_f32_16x16x32_bf16 v[212:215], v[192:195], v[76:79], v[212:215]\n\t"
            "s_xor_b32 s48, s48, s71\n\t"
            "s_xor_b32 s67, s67, s71\n\t"
            "s_add_u32 s76, s76, 4\n\t"
            "v_mfma_f32_16x16x32_bf16 v[216:219], v[192:195], v[100:103], v[216:219]\n\t"
            "s_lshl_b32 s56, s56, 2\n\t"
            "s_lshr_b32 s49, s26, 30\n\t"
            "s_or_b32 s56, s56, s49\n\t"
            "v_mfma_f32_16x16x32_bf16 v[220:223], v[192:195], v[124:127], v[220:223]\n\t"
            "ds_read_b128 v[192:195], v232 offset:256\n\t"
            "v_mfma_f32_16x16x32_bf16 v[200:203], v[196:199], v[8:11], v[200:203]\n\t"
            "v_mfma_f32_16x16x32_bf16 v[204:207], v[196:199], v[32:35], v[204:207]\n\t"
            "v_mfma_f32_16x16x32_bf16 v[208:211], v[196:199], v[56:59], v[208:211]\n\t"
            "v_mfma_f32_16x16x32_bf16 v[212:215], v[196:199], v[80:83], v[212:215]\n\t"
            "v_mfma_f32_16x16x32_bf16 v[216:219], v[196:199], v[104:107], v[216:219]\n\t"
            "v_mfma_f32_16x16x32_bf16 v[220:223], v[196:199], v[128:131], v[220:223]\n\t"
            "ds_read_b128 v[196:199], v233 offset:256\n\t"
            "v_mfma_f32_16x16x32_bf16 v[200:203], v[224:227], v[12:15], v[200:203]\n\t"
            "v_mfma_f32_16x16x32_bf16 v[204:207], v[224:227], v[36:39], v[204:207]\n\t"
            "v_mfma_f32_16x16x32_bf16 v[208:211], v[224:227], v[60:63], v[208:211]\n\t"
            "v_mfma_f32_16x16x32_bf16 v[212:215], v[224:227], v[84:87], v[212:215]\n\t"
            "v_mfma_f32_16x16x32_bf16 v[216:219], v[224:227], v[108:111], v[216:219]\n\t"
            "v_mfma_f32_16x16x32_bf16 v[220:223], v[224:227], v[132:135], v[220:223]\n\t"
            "ds_read_b128 v[224:227], v232 offset:6144\n\t"
            "s_waitcnt lgkmcnt(2)\n\t"
            "v_mfma_f32_16x16x32_bf16 v[200:203], v[192:195], v[16:19], v[200:203]\n\t"
            "v_mfma_f32_16x16x32_bf16 v[204:207], v[192:195], v[40:43], v[204:207]\n\t"
            "v_mfma_f32_16x16x32_bf16 v[208:211], v[192:195], v[64:67], v[208:211]\n\t"
            "v_mfma_f32_16x16x32_bf16 v[212:215], v[192:195], v[88:91], v[212:215]\n\t"
            "v_mfma_f32_16x16x32_bf16 v[216:219], v[192:195], v[112:115], v[216:219]\n\t"
            "v_mfma_f32_16x16x32_bf16 v[220:223], v[192:195], v[136:139], v[220:223]\n\t"
            "ds_read_b128 v[192:195], v233 offset:6144\n\t"
            "s_waitcnt lgkmcnt(2)\n\t"
            "v_mfma_f32_16x16x32_bf16 v[200:203], v[196:199], v[20:23], v[200:203]\n\t"
            "v_mfma_f32_16x16x32_bf16 v[204:207], v[196:199], v[44:47], v[204:207]\n\t"
            "v_mfma_f32_16x16x32_bf16 v[208:211], v[196:199], v[68:71], v[208:211]\n\t"
            "v_mfma_f32_16x16x32_bf16 v[212:215], v[196:199], v[92:95], v[212:215]\n\t"
            "v_mfma_f32_16x16x32_bf16 v[216:219], v[196:199], v[116:119], v[216:219]\n\t"
            "v_mfma_f32_16x16x32_bf16 v[220:223], v[196:199], v[140:143], v[220:223]\n\t"
            "ds_read_b128 v[196:199], v232 offset:6272\n\t"
            "s_waitcnt lgkmcnt(2)\n\t"
            "v_mfma_f32_16x16x32_bf16 v[144:147], v[224:227], v[0:3], 0\n\t"
            "v_mul_f32 v200, v228, v200\n\t"
            "v_mul_f32 v201, v229, v201\n\t"
            "v_mfma_f32_16x16x32_bf16 v[148:151], v[224:227], v[24:27], 0\n\t"
            "v_mul_f32 v202, v230, v202\n\t"
            "v_mul_f32 v203, v231, v203\n\t"
            "v_mfma_f32_16x16x32_bf16 v[168:171], v[224:227], v[48:51], 0\n\t"
            "v_mul_f32 v204, v228, v204\n\t"
            "v_mul_f32 v205, v229, v205\n\t"
            "v_mfma_f32_16x16x32_bf16 v[172:175], v[224:227], v[72:75], 0\n\t"
            "v_mul_f32 v206, v230, v206\n\t"
            "v_mul_f32 v207, v231, v207\n\t"
            "v_mfma_f32_16x16x32_bf16 v[152:155], v[224:227], v[96:99], 0\n\t"
            "v_mul_f32 v208, v228, v208\n\t"
            "v_mul_f32 v209, v229, v209\n\t"
            "v_mfma_f32_16x16x32_bf16 v[156:159], v[224:227], v[120:123], 0\n\t"
            "ds_read_b128 v[224:227], v233 offset:6272\n\t"
            "v_mul_f32 v210, v230, v210\n\t"
            "s_waitcnt lgkmcnt(2)\n\t"
            "v_mfma_f32_16x16x32_bf16 v[144:147], v[192:195], v[4:7], v[144:147]\n\t"
            "v_mul_f32 v211, v231, v211\n\t"
            "v_mul_f32 v212, v228, v212\n\t"
            "v_mfma_f32_16x16x32_bf16 v[148:151], v[192:195], v[28:31], v[148:151]\n\t"
            "v_mul_f32 v213, v229, v213\n\t"
            "v_mul_f32 v214, v230, v214\n\t"
            "v_mfma_f32_16x16x32_bf16 v[168:171], v[192:195], v[52:55], v[168:171]\n\t"
            "v_mul_f32 v215, v231, v215\n\t"
            "v_mul_f32 v216, v228, v216\n\t"
            "v_mfma_f32_16x16x32_bf16 v[172:175], v[192:195], v[76:79], v[172:175]\n\t"
            "v_mul_f32 v217, v229, v217\n\t"
            "v_mul_f32 v218, v230, v218\n\t"
            "v_mfma_f32_16x16x32_bf16 v[152:155], v[192:195], v[100:103], v[152:155]\n\t"
            "v_mul_f32 v219, v231, v219\n\t"
            "v_mul_f32 v220, v228, v220\n\t"
            "v_mfma_f32_16x16x32_bf16 v[156:159], v[192:195], v[124:127], v[156:159]\n\t"
            "ds_read_b128 v[192:195], v232 offset:6400\n\t"
            "v_mul_f32 v221, v229, v221\n\t"
            "s_waitcnt lgkmcnt(2)\n\t"
            "v_mfma_f32_16x16x32_bf16 v[144:147], v[196:199], v[8:11], v[144:147]\n\t"
            "v_mul_f32 v222, v230, v222\n\t"
            "v_mul_f32 v223, v231, v223\n\t"
            "v_mfma_f32_16x16x32_bf16 v[148:151], v[196:199], v[32:35], v[148:151]\n\t"
            "ds_read_b128 v[228:231], v247 offset:64\n\t"
            "v_cvt_pk_bf16_f32 v200, v200, v201\n\t"
            "v_mfma_f32_16x16x32_bf16 v[168:171], v[196:199], v[56:59], v[168:171]\n\t"
            "v_cvt_pk_bf16_f32 v204, v204, v205\n\t"
            "v_cvt_pk_bf16_f32 v208, v208, v209\n\t"
            "v_mfma_f32_16x16x32_bf16 v[172:175], v[196:199], v[80:83], v[172:175]\n\t"
            "v_cvt_pk_bf16_f32 v212, v212, v213\n\t"
            "v_cvt_pk_bf16_f32 v216, v216, v217\n\t"
            "v_mfma_f32_16x16x32_bf16 v[152:155], v[196:199], v[104:107], v[152:155]\n\t"
            "v_cvt_pk_bf16_f32 v220, v220, v221\n\t"
            "v_cvt_pk_bf16_f32 v201, v202, v203\n\t"
            "v_mfma_f32_16x16x32_bf16 v[156:159], v[196:199], v[128:131], v[156:159]\n\t"
            "ds_read_b128 v[196:199], v233 offset:6400\n\t"
            "v_cvt_pk_bf16_f32 v205, v206, v207\n\t"
            "s_waitcnt lgkmcnt(3)\n\t"
            "v_mfma_f32_16x16x32_bf16 v[144:147], v[224:227], v[12:15], v[144:147]\n\t"
            "v_cvt_pk_bf16_f32 v209, v210, v211\n\t"
            "v_cvt_pk_bf16_f32 v213, v214, v215\n\t"
            "v_mfma_f32_16x16x32_bf16 v[148:151], v[224:227], v[36:39], v[148:151]\n\t"
            "v_cvt_pk_bf16_f32 v217, v218, v219\n\t"
            "v_cvt_pk_bf16_f32 v221, v222, v223\n\t"
            "v_mfma_f32_16x16x32_bf16 v[168:171], v[224:227], v[60:63], v[168:171]\n\t"
            "v_mfma_f32_16x16x32_bf16 v[172:175], v[224:227], v[84:87], v[172:175]\n\t"
            "v_mfma_f32_16x16x32_bf16 v[152:155], v[224:227], v[108:111], v[152:155]\n\t"
            "v_mfma_f32_16x16x32_bf16 v[156:159], v[224:227], v[132:135], v[156:159]\n\t"
            "ds_read_b128 v[224:227], v232 offset:12288\n\t"
            "s_waitcnt lgkmcnt(3)\n\t"
            "v_mfma_f32_16x16x32_bf16 v[144:147], v[192:195], v[16:19], v[144:147]\n\t"
            "v_mfma_f32_16x16x32_bf16 v[148:151], v[192:195], v[40:43], v[148:151]\n\t"
            "v_mfma_f32_16x16x32_bf16 v[168:171], v[192:195], v[64:67], v[168:171]\n\t"
            "v_mfma_f32_16x16x32_bf16 v[172:175], v[192:195], v[88:91], v[172:175]\n\t"
            "v_mfma_f32_16x16x32_bf16 v[152:155], v[192:195], v[112:115], v[152:155]\n\t"
            "v_mfma_f32_16x16x32_bf16 v[156:159], v[192:195], v[136:139], v[156:159]\n\t"
            "ds_read_b128 v[192:195], v233 offset:12288\n\t"
            "s_waitcnt lgkmcnt(2)\n\t"
            "v_mfma_f32_16x16x32_bf16 v[144:147], v[196:199], v[20:23], v[144:147]\n\t"
            "v_mfma_f32_16x16x32_bf16 v[148:151], v[196:199], v[44:47], v[148:151]\n\t"
            "v_mfma_f32_16x16x32_bf16 v[168:171], v[196:199], v[68:71], v[168:171]\n\t"
            "v_mfma_f32_16x16x32_bf16 v[172:175], v[196:199], v[92:95], v[172:175]\n\t"
            "v_mfma_f32_16x16x32_bf16 v[152:155], v[196:199], v[116:119], v[152:155]\n\t"
            "v_mfma_f32_16x16x32_bf16 v[156:159], v[196:199], v[140:143], v[156:159]\n\t"
            "ds_read_b128 v[196:199], v232 offset:12416\n\t"
            "s_waitcnt lgkmcnt(2)\n\t"
            "v_mfma_f32_16x16x32_bf16 v[160:163], v[224:227], v[0:3], 0\n\t"
            "v_mul_f32 v144, v228, v144\n\t"
            "v_mul_f32 v145, v229, v145\n\t"
            "v_mfma_f32_16x16x32_bf16 v[164:167], v[224:227], v[24:27], 0\n\t"
            "v_mul_f32 v146, v230, v146\n\t"
            "v_mul_f32 v147, v231, v147\n\t"
            "v_mfma_f32_16x16x32_bf16 v[176:179], v[224:227], v[48:51], 0\n\t"
            "v_mul_f32 v148, v228, v148\n\t"
            "v_mul_f32 v149, v229, v149\n\t"
            "v_mfma_f32_16x16x32_bf16 v[180:183], v[224:227], v[72:75], 0\n\t"
            "v_mul_f32 v150, v230, v150\n\t"
            "v_mul_f32 v151, v231, v151\n\t"
            "v_mfma_f32_16x16x32_bf16 v[184:187], v[224:227], v[96:99], 0\n\t"
            "v_mul_f32 v168, v228, v168\n\t"
            "v_mul_f32 v169, v229, v169\n\t"
            "v_mfma_f32_16x16x32_bf16 v[188:191], v[224:227], v[120:123], 0\n\t"
            "ds_read_b128 v[224:227], v233 offset:12416\n\t"
            "v_mul_f32 v170, v230, v170\n\t"
            "s_waitcnt lgkmcnt(2)\n\t"
            "v_mfma_f32_16x16x32_bf16 v[160:163], v[192:195], v[4:7], v[160:163]\n\t"
            "v_mul_f32 v171, v231, v171\n\t"
            "v_mul_f32 v172, v228, v172\n\t"
            "v_mfma_f32_16x16x32_bf16 v[164:167], v[192:195], v[28:31], v[164:167]\n\t"
            "v_mul_f32 v173, v229, v173\n\t"
            "v_mul_f32 v174, v230, v174\n\t"
            "v_mfma_f32_16x16x32_bf16 v[176:179], v[192:195], v[52:55], v[176:179]\n\t"
            "v_mul_f32 v175, v231, v175\n\t"
            "v_mul_f32 v152, v228, v152\n\t"
            "v_mfma_f32_16x16x32_bf16 v[180:183], v[192:195], v[76:79], v[180:183]\n\t"
            "v_mul_f32 v153, v229, v153\n\t"
            "v_mul_f32 v154, v230, v154\n\t"
            "v_mfma_f32_16x16x32_bf16 v[184:187], v[192:195], v[100:103], v[184:187]\n\t"
            "v_mul_f32 v155, v231, v155\n\t"
            "v_mul_f32 v156, v228, v156\n\t"
            "v_mfma_f32_16x16x32_bf16 v[188:191], v[192:195], v[124:127], v[188:191]\n\t"
            "ds_read_b128 v[192:195], v232 offset:12544\n\t"
            "v_mul_f32 v157, v229, v157\n\t"
            "s_waitcnt lgkmcnt(2)\n\t"
            "v_mfma_f32_16x16x32_bf16 v[160:163], v[196:199], v[8:11], v[160:163]\n\t"
            "s_waitcnt vmcnt(0)\n\t"
            "v_mul_f32 v158, v230, v158\n\t"
            "v_mul_f32 v159, v231, v159\n\t"
            "v_mfma_f32_16x16x32_bf16 v[164:167], v[196:199], v[32:35], v[164:167]\n\t"
            "ds_read_b128 v[228:231], v247 offset:128\n\t"
            "v_cvt_pk_bf16_f32 v202, v144, v145\n\t"
            "v_mfma_f32_16x16x32_bf16 v[176:179], v[196:199], v[56:59], v[176:179]\n\t"
            "ds_read_b64 v[144:145], v253\n\t"
            "v_cvt_pk_bf16_f32 v203, v146, v147\n\t"
            "v_mfma_f32_16x16x32_bf16 v[180:183], v[196:199], v[80:83], v[180:183]\n\t"
            "ds_read_b64 v[146:147], v253 offset:96\n\t"
            "v_cvt_pk_bf16_f32 v206, v148, v149\n\t"
            "v_mfma_f32_16x16x32_bf16 v[184:187], v[196:199], v[104:107], v[184:187]\n\t"
            "ds_read_b64 v[148:149], v253 offset:1744\n\t"
            "v_cvt_pk_bf16_f32 v207, v150, v151\n\t"
            "v_mfma_f32_16x16x32_bf16 v[188:191], v[196:199], v[128:131], v[188:191]\n\t"
            "ds_read_b128 v[196:199], v233 offset:12544\n\t"
            "ds_read_b64 v[150:151], v253 offset:1840\n\t"
            "s_waitcnt lgkmcnt(7)\n\t"
            "v_mfma_f32_16x16x32_bf16 v[160:163], v[224:227], v[12:15], v[160:163]\n\t"
            "v_cvt_pk_bf16_f32 v210, v168, v169\n\t"
            "v_cvt_pk_bf16_f32 v211, v170, v171\n\t"
            "v_mfma_f32_16x16x32_bf16 v[164:167], v[224:227], v[36:39], v[164:167]\n\t"
            "ds_read_b128 v[168:171], v254\n\t"
            "v_cvt_pk_bf16_f32 v214, v172, v173\n\t"
            "v_mfma_f32_16x16x32_bf16 v[176:179], v[224:227], v[60:63], v[176:179]\n\t"
            "v_cvt_pk_bf16_f32 v215, v174, v175\n\t"
            "v_cvt_pk_bf16_f32 v218, v152, v153\n\t"
            "v_mfma_f32_16x16x32_bf16 v[180:183], v[224:227], v[84:87], v[180:183]\n\t"
            "ds_read_b128 v[172:175], v254 offset:64\n\t"
            "ds_read_b64 v[152:153], v253 offset:8\n\t"
            "v_mfma_f32_16x16x32_bf16 v[184:187], v[224:227], v[108:111], v[184:187]\n\t"
            "v_cvt_pk_bf16_f32 v219, v154, v155\n\t"
            "v_cvt_pk_bf16_f32 v222, v156, v157\n\t"
            "v_mfma_f32_16x16x32_bf16 v[188:191], v[224:227], v[132:135], v[188:191]\n\t"
            "ds_read_b64 v[154:155], v253 offset:104\n\t"
            "ds_read_b64 v[156:157], v253 offset:1752\n\t"
            "s_waitcnt lgkmcnt(11)\n\t"
            "v_mfma_f32_16x16x32_bf16 v[160:163], v[192:195], v[16:19], v[160:163]\n\t"
            "v_cvt_pk_bf16_f32 v223, v158, v159\n\t"
            "v_mfma_f32_16x16x32_bf16 v[164:167], v[192:195], v[40:43], v[164:167]\n\t"
            "ds_read_b64 v[158:159], v253 offset:1848\n\t"
            "ds_write_b128 v243, v[200:203]\n\t"
            "v_mfma_f32_16x16x32_bf16 v[176:179], v[192:195], v[64:67], v[176:179]\n\t"
            "ds_write_b128 v243, v[204:207] offset:1536\n\t"
            "v_mfma_f32_16x16x32_bf16 v[180:183], v[192:195], v[88:91], v[180:183]\n\t"
            "ds_write_b128 v243, v[208:211] offset:3072\n\t"
            "v_mfma_f32_16x16x32_bf16 v[184:187], v[192:195], v[112:115], v[184:187]\n\t"
            "s_waitcnt lgkmcnt(14)\n\t"
            "ds_write_b128 v243, v[212:215] offset:4608\n\t"
            "v_mfma_f32_16x16x32_bf16 v[188:191], v[192:195], v[136:139], v[188:191]\n\t"
            "s_waitcnt lgkmcnt(14)\n\t"
            "ds_write_b128 v243, v[216:219] offset:6144\n\t"
            "s_waitcnt lgkmcnt(12)\n\t"
            "v_mfma_f32_16x16x32_bf16 v[160:163], v[196:199], v[20:23], v[160:163]\n\t"
            "ds_write_b128 v243, v[220:223] offset:7680\n\t"
            "v_mfma_f32_16x16x32_bf16 v[164:167], v[196:199], v[44:47], v[164:167]\n\t"
            "v_mfma_f32_16x16x32_bf16 v[176:179], v[196:199], v[68:71], v[176:179]\n\t"
            "v_mfma_f32_16x16x32_bf16 v[180:183], v[196:199], v[92:95], v[180:183]\n\t"
            "v_mfma_f32_16x16x32_bf16 v[184:187], v[196:199], v[116:119], v[184:187]\n\t"
            "v_mfma_f32_16x16x32_bf16 v[188:191], v[196:199], v[140:143], v[188:191]\n\t"
            "s_nop 1\n\t"
            "v_mul_f32 v160, v228, v160\n\t"
            "v_mul_f32 v161, v229, v161\n\t"
            "v_mul_f32 v162, v230, v162\n\t"
            "v_mul_f32 v163, v231, v163\n\t"
            "v_mul_f32 v164, v228, v164\n\t"
            "v_mul_f32 v165, v229, v165\n\t"
            "v_mul_f32 v176, v228, v176\n\t"
            "v_mul_f32 v177, v229, v177\n\t"
            "v_mul_f32 v180, v228, v180\n\t"
            "v_mul_f32 v181, v229, v181\n\t"
            "v_mul_f32 v184, v228, v184\n\t"
            "v_mul_f32 v185, v229, v185\n\t"
            "v_mul_f32 v188, v228, v188\n\t"
            "v_mul_f32 v189, v229, v189\n\t"
            "v_cvt_pk_bf16_f32 v160, v160, v161\n\t"
            "v_mul_f32 v166, v230, v166\n\t"
            "v_mul_f32 v167, v231, v167\n\t"
            "v_cvt_pk_bf16_f32 v164, v164, v165\n\t"
            "v_mul_f32 v178, v230, v178\n\t"
            "v_mul_f32 v179, v231, v179\n\t"
            "v_cvt_pk_bf16_f32 v176, v176, v177\n\t"
            "v_mul_f32 v182, v230, v182\n\t"
            "v_mul_f32 v183, v231, v183\n\t"
            "v_cvt_pk_bf16_f32 v180, v180, v181\n\t"
            "v_mul_f32 v186, v230, v186\n\t"
            "v_mul_f32 v187, v231, v187\n\t"
            "v_cvt_pk_bf16_f32 v184, v184, v185\n\t"
            "v_mul_f32 v190, v230, v190\n\t"
            "v_mul_f32 v191, v231, v191\n\t"
            "v_cvt_pk_bf16_f32 v188, v188, v189\n\t"
            "v_cvt_pk_bf16_f32 v161, v162, v163\n\t"
            "v_cvt_pk_bf16_f32 v165, v166, v167\n\t"
            "v_cvt_pk_bf16_f32 v177, v178, v179\n\t"
            "v_cvt_pk_bf16_f32 v181, v182, v183\n\t"
            "v_cvt_pk_bf16_f32 v185, v186, v187\n\t"
            "v_cvt_pk_bf16_f32 v189, v190, v191\n\t"
            "ds_read_b64 v[162:163], v253 offset:112\n\t"
            "ds_read_b64 v[166:167], v253 offset:1856\n\t"
            "s_waitcnt lgkmcnt(14)\n\t"
            "ds_write_b64 v244, v[160:161]\n\t"
            "s_waitcnt lgkmcnt(14)\n\t"
            "ds_read_b64 v[160:161], v253 offset:16\n\t"
            "s_waitcnt lgkmcnt(14)\n\t"
            "ds_write_b64 v244, v[164:165] offset:1536\n\t"
            "s_waitcnt lgkmcnt(14)\n\t"
            "ds_read_b64 v[164:165], v253 offset:1760\n\t"
            "s_waitcnt lgkmcnt(14)\n\t"
            "ds_write_b64 v244, v[176:177] offset:3072\n\t"
            "s_waitcnt lgkmcnt(14)\n\t"
            "ds_write_b64 v244, v[180:181] offset:4608\n\t"
            "s_waitcnt lgkmcnt(14)\n\t"
            "ds_write_b64 v244, v[184:185] offset:6144\n\t"
            "s_waitcnt lgkmcnt(14)\n\t"
            "ds_write_b64 v244, v[188:189] offset:7680\n\t"
            "s_waitcnt lgkmcnt(0)\n\t"
            "s_barrier\n\t"
            "s_cmp_lt_u32 s46, 0x7000000\n\t"
            "s_cbranch_scc1 .Lwc_loop%=\n\t"
            "s_waitcnt lgkmcnt(0)\n\t"
            "s_nop 7\n\t"
            "s_barrier\n\t"
            : "+{v[0:3]}"(W[0]), "+{v[4:7]}"(W[1]), "+{v[8:11]}"(W[2]), "+{v[12:15]}"(W[3]), "+{v[16:19]}"(W[4]), "+{v[20:23]}"(W[5]), "+{v[24:27]}"(W[6]), "+{v[28:31]}"(W[7]), "+{v[32:35]}"(W[8]), "+{v[36:39]}"(W[9]), "+{v[40:43]}"(W[10]), "+{v[44:47]}"(W[11]), "+{v[48:51]}"(W[12]), "+{v[52:55]}"(W[13]), "+{v[56:59]}"(W[14]), "+{v[60:63]}"(W[15]), "+{v[64:67]}"(W[16]), "+{v[68:71]}"(W[17]), "+{v[72:75]}"(W[18]), "+{v[76:79]}"(W[19]), "+{v[80:83]}"(W[20]), "+{v[84:87]}"(W[21]), "+{v[88:91]}"(W[22]), "+{v[92:95]}"(W[23]), "+{v[96:99]}"(W[24]), "+{v[100:103]}"(W[25]), "+{v[104:107]}"(W[26]), "+{v[108:111]}"(W[27]), "+{v[112:115]}"(W[28]), "+{v[116:119]}"(W[29]), "+{v[120:123]}"(W[30]), "+{v[124:127]}"(W[31]), "+{v[128:131]}"(W[32]), "+{v[132:135]}"(W[33]), "+{v[136:139]}"(W[34]), "+{v[140:143]}"(W[35]), "+{v[200:203]}"(xl[0]), "+{v[204:207]}"(xl[1]), "+{v[208:211]}"(xl[2]), "+{v[212:215]}"(xl[3]), "+{v[216:219]}"(xl[4]), "+{v[220:223]}"(xl[5]), "+{v[224:227]}"(xl[6]), "+{v[228:231]}"(xl[7]), "+{v[232:235]}"(xl[8]), B_SIO
            : "{v242}"(pE_), "{v243}"(aVAw_), "{v244}"(aVBw_), "{v245}"(pST_), "{v246}"(aRSw_), "{v247}"(aRSr_), "{v250}"(pXw_), "{v251}"(pSTw_), "{v252}"(aCA_), "{v253}"(aB_), "{v254}"(aA_), "{v255}"(idx_), B_SIN
            : "v144", "v145", "v146", "v147", "v148", "v149", "v150", "v151", "v152", "v153", "v154", "v155", "v156", "v157", "v158", "v159", "v160", "v161", "v162", "v163", "v164", "v165", "v166", "v167", "v168", "v169", "v170", "v171", "v172", "v173", "v174", "v175", "v176", "v177", "v178", "v179", "v180", "v181", "v182", "v183", "v184", "v185", "v186", "v187", "v188", "v189", "v190", "v191", "v192", "v193", "v194", "v195", "v196", "v197", "v198", "v199", "v236", "v237", "v248", "v249", B_SCLOB);
        }
        asm volatile("wk_code_end1:");
    } else {
        B_PROLOGUE(false)
        unsigned t45, t56;
        {
            LANE_SETUP
            const unsigned oE_ = ldsb + fE + (O_OFF - X_OFF), oO_ = oE_ ^ 64, l16_ = lane * 16;
            unsigned pr[3];
#pragma unroll
            for (int k = 0; k < 3; ++k) {
                const int c8 = 12 * (w - 2) + 4 * k + g;
                const int ws = c8 / 6, r6 = c8 - 6 * ws;
                pr[k] = l15 * 384 + (r6 >> 1) * 128 + (((2 * ws + (r6 & 1)) ^ sw) << 4);
            }
            const unsigned pR0_ = pr[0], pR1_ = pr[1], pR2_ = pr[2];
            B_SCALARS
            asm volatile(
            "s_waitcnt lgkmcnt(0)\n\t"
            "s_nop 7\n\t"
            "s_setprio 1\n\t"
            "ds_read_b128 v[168:171], v254\n\t"
            "ds_read_b128 v[172:175], v254 offset:64\n\t"
            "s_waitcnt vmcnt(0)\n\t"
            "ds_read_b64 v[144:145], v253\n\t"
            "ds_read_b64 v[146:147], v253 offset:96\n\t"
            "ds_read_b64 v[148:149], v253 offset:1744\n\t"
            "ds_read_b64 v[150:151], v253 offset:1840\n\t"
            "ds_read_b64 v[152:153], v253 offset:8\n\t"
            "ds_read_b64 v[154:155], v253 offset:104\n\t"
            "ds_read_b64 v[156:157], v253 offset:1752\n\t"
            "ds_read_b64 v[158:159], v253 offset:1848\n\t"
            "ds_read_b64 v[160:161], v253 offset:16\n\t"
            "ds_read_b64 v[162:163], v253 offset:112\n\t"
            "ds_read_b64 v[164:165], v253 offset:1760\n\t"
            "ds_read_b64 v[166:167], v253 offset:1856\n\t"
            "v_mov_b64 v[176:177], 0\n\t"
            "v_mov_b64 v[178:179], 0\n\t"
            "v_mov_b64 v[180:181], 0\n\t"
            "v_mov_b64 v[182:183], 0\n\t"
            "v_mov_b32 v249, s76\n\t"
            "v_mov_b64 v[184:185], 0\n\t"
            "v_mov_b64 v[186:187], 0\n\t"
            "v_mov_b64 v[188:189], 0\n\t"
            "v_mov_b64 v[190:191], 0\n\t"
            "v_add_u32 v236, s67, v250\n\t"
            "v_add_u32 v248, s67, v251\n\t"
            "ds_read_b32 v249, v249\n\t"
            "s_waitcnt lgkmcnt(9)\n\t"
            "v_smfmac_f32_16x16x64_bf16 v[176:179], v[168:171], v[144:151], v255\n\t"
            "ds_read_b128 v[168:171], v254 offset:128\n\t"
            "s_waitcnt lgkmcnt(6)\n\t"
            "v_smfmac_f32_16x16x64_bf16 v[180:183], v[172:175], v[152:159], v255\n\t"
            "ds_read_b64 v[144:145], v253 offset:24\n\t"
            "ds_read_b64 v[146:147], v253 offset:120\n\t"
            "ds_read_b64 v[148:149], v253 offset:1768\n\t"
            "ds_read_b64 v[150:151], v253 offset:1864\n\t"
            "ds_read_b128 v[172:175], v254 offset:192\n\t"
            "ds_read_b64 v[152:153], v253 offset:32\n\t"
            "ds_read_b64 v[154:155], v253 offset:128\n\t"
            "ds_read_b64 v[156:157], v253 offset:1776\n\t"
            "ds_read_b64 v[158:159], v253 offset:1872\n\t"
            "v_cvt_pk_bf16_f32 v176, v176, v177\n\t"
            "v_cvt_pk_bf16_f32 v177, v178, v179\n\t"
            "v_cvt_pk_bf16_f32 v178, v180, v181\n\t"
            "v_cvt_pk_bf16_f32 v179, v182, v183\n\t"
            "v_mfma_f32_4x4x4_16b_bf16 v[196:199], v[176:177], v[176:177], 0\n\t"
            "v_mov_b64 v[180:181], 0\n\t"
            "v_mfma_f32_16x16x32_bf16 v[192:195], v[238:241], v[176:179], 0\n\t"
            "s_waitcnt lgkmcnt(10)\n\t"
            "v_readfirstlane_b32 s26, v249\n\t"
            "v_mov_b64 v[182:183], 0\n\t"
            "v_mfma_f32_4x4x4_16b_bf16 v[196:199], v[178:179], v[178:179], v[196:199]\n\t"
            "ds_read_b32 v249, v252 offset:1024\n\t"
            "s_and_b32 s68, s26, 0x7ffffff\n\t"
            "s_add_u32 s68, s68, s78\n\t"
            "s_and_b32 s69, s26, 0x38000000\n\t"
            "s_or_b32 s69, s69, s39\n\t"
            "s_bitcmp1_b32 s26, 27\n\t"
            "s_cselect_b32 s79, 2.0, s68\n\t"
            "s_waitcnt lgkmcnt(10)\n\t"
            "v_smfmac_f32_16x16x64_bf16 v[184:187], v[168:171], v[160:167], v255\n\t"
            "s_and_b32 s57, s56, 3\n\t"
            "s_lshl_b32 s58, s57, 3\n\t"
            "s_lshr_b32 m0, s54, s58\n\t"
            "ds_read_b128 v[168:171], v254 offset:256\n\t"
            "ds_read_b64 v[160:161], v253 offset:40\n\t"
            "ds_read_b64 v[162:163], v253 offset:136\n\t"
            "ds_read_b64 v[164:165], v253 offset:1784\n\t"
            "s_waitcnt lgkmcnt(9)\n\t"
            "v_smfmac_f32_16x16x64_bf16 v[188:191], v[172:175], v[144:151], v255\n\t"
            "s_lshl_b32 s58, s57, 4\n\t"
            "s_lshr_b64 s[36:37], s[50:51], s58\n\t"
            "s_and_b32 s57, s36, 0xffff\n\t"
            "s_add_u32 s57, s57, s67\n\t"
            "ds_read_b64 v[166:167], v253 offset:1880\n\t"
            "ds_read_b128 v[172:175], v254 offset:320\n\t"
            "ds_read_b64 v[144:145], v253 offset:48\n\t"
            "ds_read_b64 v[146:147], v253 offset:144\n\t"
            "ds_read_b64 v[148:149], v253 offset:1792\n\t"
            "ds_read_b64 v[150:151], v253 offset:1888\n\t"
            "s_lshr_b64 s[36:37], s[52:53], s58\n\t"
            "s_and_b32 s58, s36, 0xffff\n\t"
            "s_add_u32 s58, s58, s67\n\t"
            "s_and_b32 s59, s50, 0xffff\n\t"
            "s_add_u32 s59, s59, s67\n\t"
            "s_and_b32 s38, s52, 0xffff\n\t"
            "s_add_u32 s38, s38, s67\n\t"
            "v_writelane_b32 v236, s59, m0\n\t"
            "v_writelane_b32 v236, s57, 63\n\t"
            "v_cvt_pk_bf16_f32 v184, v184, v185\n\t"
            "v_cvt_pk_bf16_f32 v185, v186, v187\n\t"
            "v_xor_b32 v237, 16, v236\n\t"
            "v_cvt_pk_bf16_f32 v186, v188, v189\n\t"
            "v_mfma_f32_4x4x4_16b_bf16 v[196:199], v[184:185], v[184:185], v[196:199]\n\t"
            "s_waitcnt lgkmcnt(14)\n\t"
            "ds_write_b128 v236, v[176:179]\n\t"
            "v_mov_b64 v[176:177], 0\n\t"
            "v_mov_b64 v[178:179], 0\n\t"
            "v_cvt_pk_bf16_f32 v187, v190, v191\n\t"
            "v_mov_b64 v[188:189], 0\n\t"
            "s_waitcnt lgkmcnt(10)\n\t"
            "v_smfmac_f32_16x16x64_bf16 v[176:179], v[168:171], v[152:159], v255\n\t"
            "s_barrier\n\t"
            "ds_read_b128 v[168:171], v254 offset:384\n\t"
            "s_waitcnt lgkmcnt(6)\n\t"
            "v_smfmac_f32_16x16x64_bf16 v[180:183], v[172:175], v[160:167], v255\n\t"
            "ds_read_b64 v[152:153], v253 offset:56\n\t"
            "v_mfma_f32_16x16x32_bf16 v[192:195], v[238:241], v[184:187], v[192:195]\n\t"
            "ds_read_b64 v[154:155], v253 offset:152\n\t"
            "ds_read_b64 v[156:157], v253 offset:1800\n\t"
            "v_mfma_f32_4x4x4_16b_bf16 v[196:199], v[186:187], v[186:187], v[196:199]\n\t"
            "ds_read_b64 v[158:159], v253 offset:1896\n\t"
            "ds_read_b128 v[172:175], v254 offset:448\n\t"
            "ds_read_b64 v[160:161], v253 offset:64\n\t"
            "ds_read_b64 v[162:163], v253 offset:160\n\t"
            "ds_read_b64 v[164:165], v253 offset:1808\n\t"
            "ds_read_b64 v[166:167], v253 offset:1904\n\t"
            "s_waitcnt lgkmcnt(14)\n\t"
            "ds_write_b128 v237, v[184:187]\n\t"
            "v_mov_b64 v[184:185], 0\n\t"
            "v_mov_b64 v[186:187], 0\n\t"
            "v_mov_b64 v[190:191], 0\n\t"
            "v_cvt_pk_bf16_f32 v176, v176, v177\n\t"
            "v_cvt_pk_bf16_f32 v177, v178, v179\n\t"
            "v_cvt_pk_bf16_f32 v178, v180, v181\n\t"
            "v_cvt_pk_bf16_f32 v179, v182, v183\n\t"
            "v_mfma_f32_4x4x4_16b_bf16 v[196:199], v[176:177], v[176:177], v[196:199]\n\t"
            "s_waitcnt lgkmcnt(14)\n\t"
            "ds_write_b128 v236, v[176:179] offset:128\n\t"
            "s_waitcnt lgkmcnt(11)\n\t"
            "v_smfmac_f32_16x16x64_bf16 v[184:187], v[168:171], v[144:151], v255\n\t"
            "ds_read_b128 v[168:171], v254 offset:512\n\t"
            "v_mfma_f32_16x16x32_bf16 v[192:195], v[238:241], v[176:179], v[192:195]\n\t"
            "ds_read_b64 v[144:145], v253 offset:72\n\t"
            "ds_read_b64 v[146:147], v253 offset:168\n\t"
            "s_waitcnt lgkmcnt(9)\n\t"
            "v_smfmac_f32_16x16x64_bf16 v[188:191], v[172:175], v[152:159], v255\n\t"
            "ds_read_b64 v[148:149], v253 offset:1816\n\t"
            "v_mfma_f32_4x4x4_16b_bf16 v[196:199], v[178:179], v[178:179], v[196:199]\n\t"
            "ds_read_b64 v[150:151], v253 offset:1912\n\t"
            "ds_read_b128 v[172:175], v254 offset:576\n\t"
            "ds_read_b64 v[152:153], v253 offset:80\n\t"
            "ds_read_b64 v[154:155], v253 offset:176\n\t"
            "ds_read_b64 v[156:157], v253 offset:1824\n\t"
            "s_waitcnt lgkmcnt(14)\n\t"
            "ds_read_b64 v[158:159], v253 offset:1920\n\t"
            "v_mov_b64 v[176:177], 0\n\t"
            "v_mov_b64 v[178:179], 0\n\t"
            "v_mov_b64 v[180:181], 0\n\t"
            "v_mov_b64 v[182:183], 0\n\t"
            "v_cvt_pk_bf16_f32 v184, v184, v185\n\t"
            "v_cvt_pk_bf16_f32 v185, v186, v187\n\t"
            "v_cvt_pk_bf16_f32 v186, v188, v189\n\t"
            "v_cvt_pk_bf16_f32 v187, v190, v191\n\t"
            "v_mfma_f32_4x4x4_16b_bf16 v[196:199], v[184:185], v[184:185], v[196:199]\n\t"
            "s_waitcnt lgkmcnt(14)\n\t"
            "ds_write_b128 v237, v[184:187] offset:128\n\t"
            "v_mfma_f32_16x16x32_bf16 v[192:195], v[238:241], v[184:187], v[192:195]\n\t"
            "v_mov_b64 v[184:185], 0\n\t"
            "v_mov_b64 v[188:189], 0\n\t"
            "s_waitcnt lgkmcnt(10)\n\t"
            "v_smfmac_f32_16x16x64_bf16 v[176:179], v[168:171], v[160:167], v255\n\t"
            "ds_read_b128 v[168:171], v254 offset:640\n\t"
            "v_mfma_f32_4x4x4_16b_bf16 v[196:199], v[186:187], v[186:187], v[196:199]\n\t"
            "ds_read_b64 v[160:161], v253 offset:88\n\t"
            "ds_read_b64 v[162:163], v253 offset:184\n\t"
            "ds_read_b64 v[164:165], v253 offset:1832\n\t"
            "s_waitcnt lgkmcnt(9)\n\t"
            "v_smfmac_f32_16x16x64_bf16 v[180:183], v[172:175], v[144:151], v255\n\t"
            "ds_read_b64 v[166:167], v253 offset:1928\n\t"
            "ds_read_b128 v[172:175], v254 offset:704\n\t"
            "v_mov_b64 v[186:187], 0\n\t"
            "v_mov_b64 v[190:191], 0\n\t"
            "v_writelane_b32 v248, s38, m0\n\t"
            "v_and_b32 v249, s69, v249\n\t"
            "v_cvt_pk_bf16_f32 v176, v176, v177\n\t"
            "v_cvt_pk_bf16_f32 v177, v178, v179\n\t"
            "v_cvt_pk_bf16_f32 v178, v180, v181\n\t"
            "v_cvt_pk_bf16_f32 v179, v182, v183\n\t"
            "v_mfma_f32_4x4x4_16b_bf16 v[196:199], v[176:177], v[176:177], v[196:199]\n\t"
            "ds_write_b128 v236, v[176:179] offset:256\n\t"
            "v_mfma_f32_16x16x32_bf16 v[192:195], v[238:241], v[176:179], v[192:195]\n\t"
            "v_writelane_b32 v248, s58, 63\n\t"
            "v_add_u32 v236, s48, v244\n\t"
            "v_mfma_f32_4x4x4_16b_bf16 v[196:199], v[178:179], v[178:179], v[196:199]\n\t"
            "ds_read_b128 v[200:203], v236\n\t"
            "ds_read_b128 v[212:215], v236 offset:6144\n\t"
            "s_waitcnt lgkmcnt(8)\n\t"
            "v_smfmac_f32_16x16x64_bf16 v[184:187], v[168:171], v[152:159], v255\n\t"
            "ds_read_b128 v[224:227], v236 offset:12288\n\t"
            "s_waitcnt lgkmcnt(4)\n\t"
            "v_smfmac_f32_16x16x64_bf16 v[188:191], v[172:175], v[160:167], v255\n\t"
            "s_mov_b32 exec_hi, 0x3fffff\n\t"
            "s_add_u32 m0, s66, 0\n\t"
            "s_add_u32 s49, s79, 0\n\t"
            "buffer_load_dwordx4 v249, s[72:75], s49 offen lds\n\t"
            "s_mov_b32 exec_hi, -1\n\t"
            "s_mov_b32 exec_hi, 0x3fffff\n\t"
            "s_add_u32 m0, s66, 872\n\t"
            "s_add_u32 s49, s68, 196224\n\t"
            "buffer_load_dwordx4 v249, s[72:75], s49 offen lds\n\t"
            "s_mov_b32 exec_hi, -1\n\t"
            "s_mov_b32 exec_hi, 0x3fffff\n\t"
            "s_add_u32 m0, s66, 1744\n\t"
            "s_add_u32 s49, s68, 392448\n\t"
            "buffer_load_dwordx4 v249, s[72:75], s49 offen lds\n\t"
            "s_mov_b32 exec_hi, -1\n\t"
            "s_mov_b32 exec_hi, 0x3fffff\n\t"
            "s_add_u32 m0, s66, 2616\n\t"
            "s_add_u32 s49, s68, 588672\n\t"
            "buffer_load_dwordx4 v249, s[72:75], s49 offen lds\n\t"
            "s_mov_b32 exec_hi, -1\n\t"
            "s_mov_b32 exec_hi, 0x3fffff\n\t"
            "s_add_u32 m0, s66, 3488\n\t"
            "s_add_u32 s49, s68, 784896\n\t"
            "buffer_load_dwordx4 v249, s[72:75], s49 offen lds\n\t"
            "s_mov_b32 exec_hi, -1\n\t"
            "s_mov_b32 exec_hi, 0x3fffff\n\t"
            "s_add_u32 m0, s66, 4360\n\t"
            "s_add_u32 s49, s68, 981120\n\t"
            "buffer_load_dwordx4 v249, s[72:75], s49 offen lds\n\t"
            "s_mov_b32 exec_hi, -1\n\t"
            "s_mov_b32 exec_hi, 0x3fffff\n\t"
            "s_add_u32 m0, s66, 5232\n\t"
            "s_add_u32 s49, s68, 1177344\n\t"
            "buffer_load_dwordx4 v249, s[72:75], s49 offen lds\n\t"
            "s_mov_b32 exec_hi, -1\n\t"
            "s_mov_b32 exec_hi, 0x3fffff\n\t"
            "s_add_u32 m0, s66, 6104\n\t"
            "s_add_u32 s49, s68, 1373568\n\t"
            "buffer_load_dwordx4 v249, s[72:75], s49 offen lds\n\t"
            "s_mov_b32 exec_hi, -1\n\t"
            "s_mov_b32 exec_hi, 0x3fffff\n\t"
            "s_add_u32 m0, s66, 6976\n\t"
            "s_add_u32 s49, s68, 1569792\n\t"
            "buffer_load_dwordx4 v249, s[72:75], s49 offen lds\n\t"
            "s_mov_b32 exec_hi, -1\n\t"
            "s_add_u32 s49, s33, 0\n\t"
            "buffer_load_dwordx4 v[0:3], v247, s[28:31], s49 offen sc1\n\t"
            "s_add_u32 s49, s33, 1024\n\t"
            "buffer_load_dwordx4 v[4:7], v247, s[28:31], s49 offen sc1\n\t"
            "s_add_u32 s49, s33, 2048\n\t"
            "buffer_load_dwordx4 v[8:11], v247, s[28:31], s49 offen sc1\n\t"
            "s_add_u32 s49, s33, 3072\n\t"
            "buffer_load_dwordx4 v[12:15], v247, s[28:31], s49 offen sc1\n\t"
            "s_add_u32 s49, s33, 4096\n\t"
            "buffer_load_dwordx4 v[16:19], v247, s[28:31], s49 offen sc1\n\t"
            "s_add_u32 s49, s33, 5120\n\t"
            "buffer_load_dwordx4 v[20:23], v247, s[28:31], s49 offen sc1\n\t"
            "s_add_u32 s49, s33, 6144\n\t"
            "buffer_load_dwordx4 v[24:27], v247, s[28:31], s49 offen sc1\n\t"
            "s_add_u32 s49, s33, 7168\n\t"
            "buffer_load_dwordx4 v[28:31], v247, s[28:31], s49 offen sc1\n\t"
            "s_add_u32 s49, s33, 8192\n\t"
            "buffer_load_dwordx4 v[32:35], v247, s[28:31], s49 offen sc1\n\t"
            "s_add_u32 s49, s33, 9216\n\t"
            "buffer_load_dwordx4 v[36:39], v247, s[28:31], s49 offen sc1\n\t"
            "s_add_u32 s49, s33, 10240\n\t"
            "buffer_load_dwordx4 v[40:43], v247, s[28:31], s49 offen sc1\n\t"
            "s_add_u32 s49, s33, 11264\n\t"
            "buffer_load_dwordx4 v[44:47], v247, s[28:31], s49 offen sc1\n\t"
            "s_add_u32 s49, s33, 12288\n\t"
            "buffer_load_dwordx4 v[48:51], v247, s[28:31], s49 offen sc1\n\t"
            "s_add_u32 s49, s33, 13312\n\t"
            "buffer_load_dwordx4 v[52:55], v247, s[28:31], s49 offen sc1\n\t"
            "s_add_u32 s49, s33, 14336\n\t"
            "buffer_load_dwordx4 v[56:59], v247, s[28:31], s49 offen sc1\n\t"
            "s_add_u32 s49, s33, 15360\n\t"
            "buffer_load_dwordx4 v[60:63], v247, s[28:31], s49 offen sc1\n\t"
            "s_add_u32 s49, s33, 16384\n\t"
            "buffer_load_dwordx4 v[64:67], v247, s[28:31], s49 offen sc1\n\t"
            "s_add_u32 s49, s33, 17408\n\t"
            "buffer_load_dwordx4 v[68:71], v247, s[28:31], s49 offen sc1\n\t"
            "s_add_u32 s49, s33, 18432\n\t"
            "buffer_load_dwordx4 v[72:75], v247, s[28:31], s49 offen sc1\n\t"
            "s_add_u32 s49, s33, 19456\n\t"
            "buffer_load_dwordx4 v[76:79], v247, s[28:31], s49 offen sc1\n\t"
            "s_add_u32 s49, s33, 20480\n\t"
            "buffer_load_dwordx4 v[80:83], v247, s[28:31], s49 offen sc1\n\t"
            "s_add_u32 s49, s33, 21504\n\t"
            "buffer_load_dwordx4 v[84:87], v247, s[28:31], s49 offen sc1\n\t"
            "s_add_u32 s49, s33, 22528\n\t"
            "buffer_load_dwordx4 v[88:91], v247, s[28:31], s49 offen sc1\n\t"
            "s_add_u32 s49, s33, 23552\n\t"
            "buffer_load_dwordx4 v[92:95], v247, s[28:31], s49 offen sc1\n\t"
            "s_add_u32 s49, s33, 24576\n\t"
            "buffer_load_dwordx4 v[96:99], v247, s[28:31], s49 offen sc1\n\t"
            "s_add_u32 s49, s33, 25600\n\t"
            "buffer_load_dwordx4 v[100:103], v247, s[28:31], s49 offen sc1\n\t"
            "s_add_u32 s49, s33, 26624\n\t"
            "buffer_load_dwordx4 v[104:107], v247, s[28:31], s49 offen sc1\n\t"
            "s_add_u32 s49, s33, 27648\n\t"
            "buffer_load_dwordx4 v[108:111], v247, s[28:31], s49 offen sc1\n\t"
            "s_add_u32 s49, s33, 28672\n\t"
            "buffer_load_dwordx4 v[112:115], v247, s[28:31], s49 offen sc1\n\t"
            "s_add_u32 s49, s33, 29696\n\t"
            "buffer_load_dwordx4 v[116:119], v247, s[28:31], s49 offen sc1\n\t"
            "s_add_u32 s49, s33, 30720\n\t"
            "buffer_load_dwordx4 v[120:123], v247, s[28:31], s49 offen sc1\n\t"
            "v_cvt_pk_bf16_f32 v184, v184, v185\n\t"
            "v_cvt_pk_bf16_f32 v185, v186, v187\n\t"
            "v_cvt_pk_bf16_f32 v186, v188, v189\n\t"
            "v_cvt_pk_bf16_f32 v187, v190, v191\n\t"
            "v_mfma_f32_4x4x4_16b_bf16 v[196:199], v[184:185], v[184:185], v[196:199]\n\t"
            "s_add_u32 s49, s33, 31744\n\t"
            "buffer_load_dwordx4 v[124:127], v247, s[28:31], s49 offen sc1\n\t"
            "v_mfma_f32_4x4x4_16b_bf16 v[196:199], v[186:187], v[186:187], v[196:199]\n\t"
            "s_add_u32 s49, s33, 32768\n\t"
            "buffer_load_dwordx4 v[128:131], v247, s[28:31], s49 offen sc1\n\t"
            "v_mfma_f32_16x16x32_bf16 v[192:195], v[238:241], v[184:187], v[192:195]\n\t"
            "ds_write_b128 v237, v[184:187] offset:256\n\t"
            "v_add_u32 v237, s48, v245\n\t"
            "ds_read_b128 v[204:207], v237\n\t"
            "ds_read_b128 v[216:219], v237 offset:6144\n\t"
            "ds_read_b128 v[228:231], v237 offset:12288\n\t"
            "s_add_u32 s49, s33, 33792\n\t"
            "buffer_load_dwordx4 v[132:135], v247, s[28:31], s49 offen sc1\n\t"
            "s_add_u32 s49, s33, 34816\n\t"
            "buffer_load_dwordx4 v[136:139], v247, s[28:31], s49 offen sc1\n\t"
            "v_cndmask_b32_e64 v193, v196, v197, s[60:61]\n\t"
            "v_cndmask_b32_e64 v193, v193, v198, s[62:63]\n\t"
            "v_cndmask_b32_e64 v193, v193, v199, s[64:65]\n\t"
            "ds_write_b64 v248, v[192:193] offset:18816\n\t"
            "v_add_u32 v248, s48, v246\n\t"
            "ds_read_b128 v[208:211], v248\n\t"
            "ds_read_b128 v[220:223], v248 offset:6144\n\t"
            "ds_read_b128 v[232:235], v248 offset:12288\n\t"
            "s_add_u32 s49, s33, 35840\n\t"
            "buffer_load_dwordx4 v[140:143], v247, s[28:31], s49 offen sc1\n\t"
            "s_add_u32 s45, s46, s27\n\t"
            "s_mov_b32 s46, s47\n\t"
            "s_mov_b32 s47, s68\n\t"
            "s_xor_b32 s48, s48, s71\n\t"
            "s_xor_b32 s67, s67, s71\n\t"
            "s_add_u32 s76, s76, 4\n\t"
            "s_lshl_b32 s56, s56, 2\n\t"
            "s_lshr_b32 s49, s26, 30\n\t"
            "s_or_b32 s56, s56, s49\n\t"
            "s_cmp_ge_u32 s76, s24\n\t"
            "s_cselect_b64 vcc, -1, 0\n\t"
            "s_waitcnt lgkmcnt(0)\n\t"
            "s_barrier\n\t"
            "ds_read_b128 v[168:171], v254\n\t"
            "ds_read_b128 v[172:175], v254 offset:64\n\t"
            "s_waitcnt vmcnt(0)\n\t"
            "ds_read_b64 v[144:145], v253\n\t"
            "ds_read_b64 v[146:147], v253 offset:96\n\t"
            "ds_read_b64 v[148:149], v253 offset:1744\n\t"
            "ds_read_b64 v[150:151], v253 offset:1840\n\t"
            "ds_read_b64 v[152:153], v253 offset:8\n\t"
            "ds_read_b64 v[154:155], v253 offset:104\n\t"
            "ds_read_b64 v[156:157], v253 offset:1752\n\t"
            "ds_read_b64 v[158:159], v253 offset:1848\n\t"
            "ds_read_b64 v[160:161], v253 offset:16\n\t"
            "ds_read_b64 v[162:163], v253 offset:112\n\t"
            "ds_read_b64 v[164:165], v253 offset:1760\n\t"
            "ds_read_b64 v[166:167], v253 offset:1856\n\t"
            "s_waitcnt lgkmcnt(0)\n\t"
            "s_nop 7\n\t"
            ".Lwb_loop%=:\n\t"
            "s_setprio 1\n\t"
            "v_mov_b64 v[176:177], 0\n\t"
            "v_mov_b64 v[178:179], 0\n\t"
            "v_mov_b64 v[180:181], 0\n\t"
            "v_mov_b64 v[182:183], 0\n\t"
            "s_setprio 3\n\t"
            "v_smfmac_f32_16x16x64_bf16 v[176:179], v[168:171], v[144:151], v255\n\t"
            "ds_read_b128 v[168:171], v254 offset:128\n\t"
            "s_waitcnt lgkmcnt(5)\n\t"
            "v_smfmac_f32_16x16x64_bf16 v[180:183], v[172:175], v[152:159], v255\n\t"
            "s_setprio 1\n\t"
            "ds_read_b64 v[144:145], v253 offset:24\n\t"
            "ds_read_b64 v[146:147], v253 offset:120\n\t"
            "ds_read_b64 v[148:149], v253 offset:1768\n\t"
            "ds_read_b64 v[150:151], v253 offset:1864\n\t"
            "ds_read_b128 v[172:175], v254 offset:192\n\t"
            "ds_read_b64 v[152:153], v253 offset:32\n\t"
            "ds_read_b64 v[154:155], v253 offset:128\n\t"
            "ds_read_b64 v[156:157], v253 offset:1776\n\t"
            "ds_read_b64 v[158:159], v253 offset:1872\n\t"
            "v_mov_b32 v249, s76\n\t"
            "v_mov_b64 v[184:185], 0\n\t"
            "v_mov_b64 v[186:187], 0\n\t"
            "v_mov_b64 v[188:189], 0\n\t"
            "v_mov_b64 v[190:191], 0\n\t"
            "v_cvt_pk_bf16_f32 v176, v176, v177\n\t"
            "v_add_u32 v236, s67, v250\n\t"
            "v_cvt_pk_bf16_f32 v177, v178, v179\n\t"
            "v_cvt_pk_bf16_f32 v178, v180, v181\n\t"
            "v_cvt_pk_bf16_f32 v179, v182, v183\n\t"
            "s_setprio 3\n\t"
            "v_mfma_f32_4x4x4_16b_bf16 v[196:199], v[176:177], v[176:177], 0\n\t"
            "ds_read_b32 v249, v249\n\t"
            "v_mfma_f32_16x16x32_bf16 v[192:195], v[238:241], v[176:179], 0\n\t"
            "v_mov_b64 v[180:181], 0\n\t"
            "v_mov_b64 v[182:183], 0\n\t"
            "v_mfma_f32_4x4x4_16b_bf16 v[196:199], v[178:179], v[178:179], v[196:199]\n\t"
            "v_add_u32 v248, s67, v251\n\t"
            "s_waitcnt lgkmcnt(10)\n\t"
            "v_smfmac_f32_16x16x64_bf16 v[184:187], v[168:171], v[160:167], v255\n\t"
            "ds_read_b128 v[168:171], v254 offset:256\n\t"
            "ds_read_b64 v[160:161], v253 offset:40\n\t"
            "ds_read_b64 v[162:163], v253 offset:136\n\t"
            "ds_read_b64 v[164:165], v253 offset:1784\n\t"
            "ds_read_b64 v[166:167], v253 offset:1880\n\t"
            "s_waitcnt lgkmcnt(10)\n\t"
            "v_smfmac_f32_16x16x64_bf16 v[188:191], v[172:175], v[144:151], v255\n\t"
            "s_setprio 1\n\t"
            "ds_read_b128 v[172:175], v254 offset:320\n\t"
            "ds_read_b64 v[144:145], v253 offset:48\n\t"
            "ds_read_b64 v[146:147], v253 offset:144\n\t"
            "ds_read_b64 v[148:149], v253 offset:1792\n\t"
            "ds_read_b64 v[150:151], v253 offset:1888\n\t"
            "v_cvt_pk_bf16_f32 v184, v184, v185\n\t"
            "v_cvt_pk_bf16_f32 v185, v186, v187\n\t"
            "v_cvt_pk_bf16_f32 v186, v188, v189\n\t"
            "v_cvt_pk_bf16_f32 v187, v190, v191\n\t"
            "s_setprio 3\n\t"
            "v_mfma_f32_4x4x4_16b_bf16 v[196:199], v[184:185], v[184:185], v[196:199]\n\t"
            "v_mov_b64 v[188:189], 0\n\t"
            "v_mfma_f32_16x16x32_bf16 v[192:195], v[238:241], v[184:187], v[192:195]\n\t"
            "s_waitcnt lgkmcnt(10)\n\t"
            "v_readfirstlane_b32 s26, v249\n\t"
            "v_mov_b64 v[190:191], 0\n\t"
            "v_mfma_f32_4x4x4_16b_bf16 v[196:199], v[186:187], v[186:187], v[196:199]\n\t"
            "ds_read_b32 v249, v252 offset:1024\n\t"
            "s_and_b32 s68, s26, 0x7ffffff\n\t"
            "s_add_u32 s68, s68, s78\n\t"
            "s_and_b32 s69, s26, 0x38000000\n\t"
            "s_or_b32 s69, s69, s39\n\t"
            "s_bitcmp1_b32 s26, 27\n\t"
            "s_cselect_b32 s79, 2.0, s68\n\t"
            "s_and_b32 s57, s56, 3\n\t"
            "s_lshl_b32 s58, s57, 3\n\t"
            "s_lshr_b32 m0, s54, s58\n\t"
            "s_lshl_b32 s58, s57, 4\n\t"
            "s_lshr_b64 s[36:37], s[50:51], s58\n\t"
            "s_and_b32 s57, s36, 0xffff\n\t"
            "s_add_u32 s57, s57, s67\n\t"
            "s_waitcnt lgkmcnt(5)\n\t"
            "v_smfmac_f32_16x16x64_bf16 v[180:183], v[172:175], v[160:167], v255\n\t"
            "s_setprio 1\n\t"
            "s_lshr_b64 s[36:37], s[52:53], s58\n\t"
            "s_and_b32 s58, s36, 0xffff\n\t"
            "s_add_u32 s58, s58, s67\n\t"
            "ds_read_b128 v[172:175], v254 offset:448\n\t"
            "ds_read_b64 v[160:161], v253 offset:64\n\t"
            "ds_read_b64 v[162:163], v253 offset:160\n\t"
            "ds_read_b64 v[164:165], v253 offset:1808\n\t"
            "ds_read_b64 v[166:167], v253 offset:1904\n\t"
            "s_and_b32 s59, s50, 0xffff\n\t"
            "s_add_u32 s59, s59, s67\n\t"
            "s_and_b32 s38, s52, 0xffff\n\t"
            "s_add_u32 s38, s38, s67\n\t"
            "v_writelane_b32 v236, s59, m0\n\t"
            "v_writelane_b32 v236, s57, 63\n\t"
            "v_xor_b32 v237, 16, v236\n\t"
            "v_writelane_b32 v248, s38, m0\n\t"
            "v_writelane_b32 v248, s58, 63\n\t"
            "s_waitcnt lgkmcnt(5)\n\t"
            "v_and_b32 v249, s69, v249\n\t"
            "s_barrier\n\t"
            "ds_write_b128 v236, v[176:179]\n\t"
            "v_mov_b64 v[176:177], 0\n\t"
            "v_mov_b64 v[178:179], 0\n\t"
            "s_setprio 3\n\t"
            "s_nop 0\n\t"
            "v_smfmac_f32_16x16x64_bf16 v[176:179], v[168:171], v[152:159], v255\n\t"
            "s_setprio 1\n\t"
            "ds_read_b128 v[168:171], v254 offset:384\n\t"
            "ds_read_b64 v[152:153], v253 offset:56\n\t"
            "ds_read_b64 v[154:155], v253 offset:152\n\t"
            "ds_read_b64 v[156:157], v253 offset:1800\n\t"
            "ds_read_b64 v[158:159], v253 offset:1896\n\t"
            "ds_write_b128 v237, v[184:187]\n\t"
            "v_mov_b64 v[184:185], 0\n\t"
            "v_mov_b64 v[186:187], 0\n\t"
            "v_cvt_pk_bf16_f32 v176, v176, v177\n\t"
            "v_cvt_pk_bf16_f32 v177, v178, v179\n\t"
            "v_cvt_pk_bf16_f32 v178, v180, v181\n\t"
            "v_cvt_pk_bf16_f32 v179, v182, v183\n\t"
            "s_setprio 3\n\t"
            "v_mfma_f32_4x4x4_16b_bf16 v[196:199], v[176:177], v[176:177], v[196:199]\n\t"
            "ds_write_b128 v236, v[176:179] offset:128\n\t"
            "v_mfma_f32_16x16x32_bf16 v[192:195], v[238:241], v[176:179], v[192:195]\n\t"
            "v_mov_b64 v[176:177], 0\n\t"
            "v_mov_b64 v[180:181], 0\n\t"
            "v_mfma_f32_4x4x4_16b_bf16 v[196:199], v[178:179], v[178:179], v[196:199]\n\t"
            "v_mov_b64 v[178:179], 0\n\t"
            "v_mov_b64 v[182:183], 0\n\t"
            "s_waitcnt lgkmcnt(6)\n\t"
            "v_smfmac_f32_16x16x64_bf16 v[184:187], v[168:171], v[144:151], v255\n\t"
            "ds_read_b128 v[168:171], v254 offset:512\n\t"
            "s_waitcnt lgkmcnt(3)\n\t"
            "v_smfmac_f32_16x16x64_bf16 v[188:191], v[172:175], v[152:159], v255\n\t"
            "s_setprio 1\n\t"
            "ds_read_b64 v[144:145], v253 offset:72\n\t"
            "ds_read_b64 v[146:147], v253 offset:168\n\t"
            "ds_read_b64 v[148:149], v253 offset:1816\n\t"
            "ds_read_b64 v[150:151], v253 offset:1912\n\t"
            "ds_read_b128 v[172:175], v254 offset:576\n\t"
            "ds_read_b64 v[152:153], v253 offset:80\n\t"
            "ds_read_b64 v[154:155], v253 offset:176\n\t"
            "ds_read_b64 v[156:157], v253 offset:1824\n\t"
            "ds_read_b64 v[158:159], v253 offset:1920\n\t"
            "v_cvt_pk_bf16_f32 v184, v184, v185\n\t"
            "v_cvt_pk_bf16_f32 v185, v186, v187\n\t"
            "v_cvt_pk_bf16_f32 v186, v188, v189\n\t"
            "v_cvt_pk_bf16_f32 v187, v190, v191\n\t"
            "s_setprio 3\n\t"
            "v_mfma_f32_4x4x4_16b_bf16 v[196:199], v[184:185], v[184:185], v[196:199]\n\t"
            "ds_write_b128 v237, v[184:187] offset:128\n\t"
            "v_mfma_f32_16x16x32_bf16 v[192:195], v[238:241], v[184:187], v[192:195]\n\t"
            "v_mov_b64 v[184:185], 0\n\t"
            "v_mov_b64 v[188:189], 0\n\t"
            "v_mfma_f32_4x4x4_16b_bf16 v[196:199], v[186:187], v[186:187], v[196:199]\n\t"
            "v_mov_b64 v[186:187], 0\n\t"
            "v_mov_b64 v[190:191], 0\n\t"
            "s_waitcnt lgkmcnt(10)\n\t"
            "v_smfmac_f32_16x16x64_bf16 v[176:179], v[168:171], v[160:167], v255\n\t"
            "ds_read_b128 v[168:171], v254 offset:640\n\t"
            "ds_read_b64 v[160:161], v253 offset:88\n\t"
            "ds_read_b64 v[162:163], v253 offset:184\n\t"
            "ds_read_b64 v[164:165], v253 offset:1832\n\t"
            "ds_read_b64 v[166:167], v253 offset:1928\n\t"
            "s_waitcnt lgkmcnt(10)\n\t"
            "v_smfmac_f32_16x16x64_bf16 v[180:183], v[172:175], v[144:151], v255\n\t"
            "s_setprio 1\n\t"
            "ds_read_b128 v[172:175], v254 offset:704\n\t"
            "v_lshlrev_b32 v144, 16, v212\n\t"
            "v_cvt_pk_bf16_f32 v176, v176, v177\n\t"
            "v_cvt_pk_bf16_f32 v177, v178, v179\n\t"
            "v_and_b32 v145, 0xffff0000, v212\n\t"
            "v_lshlrev_b32 v146, 16, v213\n\t"
            "s_setprio 3\n\t"
            "v_mfma_f32_4x4x4_16b_bf16 v[196:199], v[176:177], v[176:177], v[196:199]\n\t"
            "s_setprio 1\n\t"
            "v_and_b32 v147, 0xffff0000, v213\n\t"
            "v_cvt_pk_bf16_f32 v178, v180, v181\n\t"
            "v_cvt_pk_bf16_f32 v179, v182, v183\n\t"
            "v_lshlrev_b32 v180, 16, v210\n\t"
            "v_and_b32 v181, 0xffff0000, v210\n\t"
            "s_setprio 3\n\t"
            "v_mfma_f32_4x4x4_16b_bf16 v[196:199], v[178:179], v[178:179], v[196:199]\n\t"
            "ds_write_b128 v236, v[176:179] offset:256\n\t"
            "v_mfma_f32_16x16x32_bf16 v[192:195], v[238:241], v[176:179], v[192:195]\n\t"
            "v_lshlrev_b32 v176, 16, v208\n\t"
            "v_and_b32 v177, 0xffff0000, v208\n\t"
            "v_lshlrev_b32 v178, 16, v209\n\t"
            "s_waitcnt lgkmcnt(6)\n\t"
            "v_smfmac_f32_16x16x64_bf16 v[184:187], v[168:171], v[152:159], v255\n\t"
            "s_setprio 1\n\t"
            "v_lshlrev_b32 v152, 16, v200\n\t"
            "v_and_b32 v153, 0xffff0000, v200\n\t"
            "v_lshlrev_b32 v154, 16, v201\n\t"
            "v_and_b32 v155, 0xffff0000, v201\n\t"
            "v_lshlrev_b32 v156, 16, v202\n\t"
            "s_setprio 3\n\t"
            "s_waitcnt lgkmcnt(1)\n\t"
            "v_smfmac_f32_16x16x64_bf16 v[188:191], v[172:175], v[160:167], v255\n\t"
            "s_setprio 1\n\t"
            "v_cvt_pk_bf16_f32 v184, v184, v185\n\t"
            "v_and_b32 v157, 0xffff0000, v202\n\t"
            "v_cvt_pk_bf16_f32 v185, v186, v187\n\t"
            "v_lshlrev_b32 v158, 16, v203\n\t"
            "v_and_b32 v159, 0xffff0000, v203\n\t"
            "s_setprio 3\n\t"
            "v_mfma_f32_4x4x4_16b_bf16 v[196:199], v[184:185], v[184:185], v[196:199]\n\t"
            "s_setprio 1\n\t"
            "s_mov_b32 exec_hi, 0x3fffff\n\t"
            "s_add_u32 m0, s66, 0\n\t"
            "s_add_u32 s49, s79, 0\n\t"
            "buffer_load_dwordx4 v249, s[72:75], s49 offen lds\n\t"
            "s_mov_b32 exec_hi, -1\n\t"
            "v_cvt_pk_bf16_f32 v186, v188, v189\n\t"
            "v_cvt_pk_bf16_f32 v187, v190, v191\n\t"
            "v_lshlrev_b32 v160, 16, v204\n\t"
            "v_and_b32 v161, 0xffff0000, v204\n\t"
            "s_setprio 3\n\t"
            "v_mfma_f32_4x4x4_16b_bf16 v[196:199], v[186:187], v[186:187], v[196:199]\n\t"
            "ds_read_b128 v[188:191], v243\n\t"
            "v_mfma_f32_16x16x32_bf16 v[192:195], v[238:241], v[184:187], v[192:195]\n\t"
            "s_setprio 1\n\t"
            "ds_write_b128 v237, v[184:187] offset:256\n\t"
            "ds_read_b128 v[184:187], v242\n\t"
            "s_mov_b32 exec_hi, 0x3fffff\n\t"
            "s_add_u32 m0, s66, 872\n\t"
            "s_add_u32 s49, s68, 196224\n\t"
            "buffer_load_dwordx4 v249, s[72:75], s49 offen lds\n\t"
            "s_mov_b32 exec_hi, -1\n\t"
            "v_lshlrev_b32 v162, 16, v205\n\t"
            "v_cndmask_b32_e64 v193, v196, v197, s[60:61]\n\t"
            "v_cndmask_b32_e64 v193, v193, v198, s[62:63]\n\t"
            "v_cndmask_b32_e64 v193, v193, v199, s[64:65]\n\t"
            "v_and_b32 v163, 0xffff0000, v205\n\t"
            "v_lshlrev_b32 v164, 16, v206\n\t"
            "v_and_b32 v165, 0xffff0000, v206\n\t"
            "v_lshlrev_b32 v166, 16, v207\n\t"
            "v_and_b32 v167, 0xffff0000, v207\n\t"
            "v_and_b32 v179, 0xffff0000, v209\n\t"
            "v_lshlrev_b32 v182, 16, v211\n\t"
            "v_and_b32 v183, 0xffff0000, v211\n\t"
            "v_lshlrev_b32 v148, 16, v214\n\t"
            "v_and_b32 v149, 0xffff0000, v214\n\t"
            "v_lshlrev_b32 v150, 16, v215\n\t"
            "v_and_b32 v151, 0xffff0000, v215\n\t"
            "v_lshlrev_b32 v168, 16, v216\n\t"
            "v_and_b32 v169, 0xffff0000, v216\n\t"
            "s_setprio 3\n\t"
            "s_waitcnt lgkmcnt(0)\n\t"
            "v_mfma_f32_16x16x32_bf16 v[152:155], v[0:3], v[184:187], v[152:155]\n\t"
            "s_setprio 1\n\t"
            "s_setprio 1\n\t"
            "ds_read_b128 v[196:199], v252\n\t"
            "ds_write_b64 v248, v[192:193] offset:18816\n\t"
            "v_mfma_f32_16x16x32_bf16 v[156:159], v[24:27], v[184:187], v[156:159]\n\t"
            "ds_read_b128 v[192:195], v242 offset:128\n\t"
            "s_mov_b32 exec_hi, 0x3fffff\n\t"
            "s_add_u32 m0, s66, 1744\n\t"
            "s_add_u32 s49, s68, 392448\n\t"
            "buffer_load_dwordx4 v249, s[72:75], s49 offen lds\n\t"
            "s_mov_b32 exec_hi, -1\n\t"
            "v_mfma_f32_16x16x32_bf16 v[160:163], v[48:51], v[184:187], v[160:163]\n\t"
            "s_mov_b32 exec_hi, 0x3fffff\n\t"
            "s_add_u32 m0, s66, 2616\n\t"
            "s_add_u32 s49, s68, 588672\n\t"
            "buffer_load_dwordx4 v249, s[72:75], s49 offen lds\n\t"
            "s_mov_b32 exec_hi, -1\n\t"
            "v_mfma_f32_16x16x32_bf16 v[164:167], v[72:75], v[184:187], v[164:167]\n\t"
            "s_mov_b32 exec_hi, 0x3fffff\n\t"
            "s_add_u32 m0, s66, 3488\n\t"
            "s_add_u32 s49, s68, 784896\n\t"
            "buffer_load_dwordx4 v249, s[72:75], s49 offen lds\n\t"
            "s_mov_b32 exec_hi, -1\n\t"
            "v_mfma_f32_16x16x32_bf16 v[176:179], v[96:99], v[184:187], v[176:179]\n\t"
            "s_mov_b32 exec_hi, 0x3fffff\n\t"
            "s_add_u32 m0, s66, 4360\n\t"
            "s_add_u32 s49, s68, 981120\n\t"
            "buffer_load_dwordx4 v249, s[72:75], s49 offen lds\n\t"
            "s_mov_b32 exec_hi, -1\n\t"
            "v_mfma_f32_16x16x32_bf16 v[180:183], v[120:123], v[184:187], v[180:183]\n\t"
            "ds_read_b128 v[184:187], v243 offset:128\n\t"
            "s_mov_b32 exec_hi, 0x3fffff\n\t"
            "s_add_u32 m0, s66, 5232\n\t"
            "s_add_u32 s49, s68, 1177344\n\t"
            "buffer_load_dwordx4 v249, s[72:75], s49 offen lds\n\t"
            "s_mov_b32 exec_hi, -1\n\t"
            "v_mfma_f32_16x16x32_bf16 v[152:155], v[4:7], v[188:191], v[152:155]\n\t"
            "v_lshlrev_b32 v170, 16, v217\n\t"
            "v_and_b32 v171, 0xffff0000, v217\n\t"
            "v_mfma_f32_16x16x32_bf16 v[156:159], v[28:31], v[188:191], v[156:159]\n\t"
            "v_lshlrev_b32 v172, 16, v218\n\t"
            "v_and_b32 v173, 0xffff0000, v218\n\t"
            "v_mfma_f32_16x16x32_bf16 v[160:163], v[52:55], v[188:191], v[160:163]\n\t"
            "v_lshlrev_b32 v174, 16, v219\n\t"
            "v_and_b32 v175, 0xffff0000, v219\n\t"
            "v_mfma_f32_16x16x32_bf16 v[164:167], v[76:79], v[188:191], v[164:167]\n\t"
            "s_mov_b32 exec_hi, 0x3fffff\n\t"
            "s_add_u32 m0, s66, 6104\n\t"
            "s_add_u32 s49, s68, 1373568\n\t"
            "buffer_load_dwordx4 v249, s[72:75], s49 offen lds\n\t"
            "s_mov_b32 exec_hi, -1\n\t"
            "v_mfma_f32_16x16x32_bf16 v[176:179], v[100:103], v[188:191], v[176:179]\n\t"
            "s_mov_b32 exec_hi, 0x3fffff\n\t"
            "s_add_u32 m0, s66, 6976\n\t"
            "s_add_u32 s49, s68, 1569792\n\t"
            "buffer_load_dwordx4 v249, s[72:75], s49 offen lds\n\t"
            "s_mov_b32 exec_hi, -1\n\t"
            "v_mfma_f32_16x16x32_bf16 v[180:183], v[124:127], v[188:191], v[180:183]\n\t"
            "s_bfe_u32 s57, s56, 0x20004\n\t"
            "s_lshl_b32 s58, s57, 3\n\t"
            "s_lshr_b32 s58, s55, s58\n\t"
            "s_lshl_b64 s[36:37], s[34:35], s58\n\t"
            "s_waitcnt lgkmcnt(1)\n\t"
            "v_mfma_f32_16x16x32_bf16 v[152:155], v[8:11], v[192:195], v[152:155]\n\t"
            "s_bitcmp1_b32 s57, 1\n\t"
            "s_cselect_b32 s58, 1177344, 0\n\t"
            "s_bitcmp1_b32 s57, 0\n\t"
            "s_cselect_b32 s57, 2304, 0\n\t"
            "s_add_u32 s57, s57, s58\n\t"
            "v_mfma_f32_16x16x32_bf16 v[156:159], v[32:35], v[192:195], v[156:159]\n\t"
            "ds_read_b128 v[188:191], v242 offset:256\n\t"
            "s_mov_b64 exec, s[36:37]\n\t"
            "v_add_u32 v196, s57, v196\n\t"
            "s_mov_b64 exec, -1\n\t"
            "v_mfma_f32_16x16x32_bf16 v[160:163], v[56:59], v[192:195], v[160:163]\n\t"
            "v_add_u32 v236, s48, v244\n\t"
            "v_add_u32 v237, s48, v245\n\t"
            "v_mfma_f32_16x16x32_bf16 v[164:167], v[80:83], v[192:195], v[164:167]\n\t"
            "ds_read_b128 v[200:203], v236\n\t"
            "ds_read_b128 v[204:207], v237\n\t"
            "v_mfma_f32_16x16x32_bf16 v[176:179], v[104:107], v[192:195], v[176:179]\n\t"
            "ds_read_b128 v[212:215], v236 offset:6144\n\t"
            "ds_read_b128 v[216:219], v237 offset:6144\n\t"
            "v_mfma_f32_16x16x32_bf16 v[180:183], v[128:131], v[192:195], v[180:183]\n\t"
            "ds_read_b128 v[192:195], v243 offset:256\n\t"
            "v_add_u32 v248, s48, v246\n\t"
            "s_waitcnt lgkmcnt(6)\n\t"
            "v_mfma_f32_16x16x32_bf16 v[152:155], v[12:15], v[184:187], v[152:155]\n\t"
            "ds_read_b128 v[208:211], v248\n\t"
            "v_mov_b32 v249, s76\n\t"
            "v_mfma_f32_16x16x32_bf16 v[156:159], v[36:39], v[184:187], v[156:159]\n\t"
            "ds_read_b32 v249, v249 offset:4\n\t"
            "v_mfma_f32_16x16x32_bf16 v[160:163], v[60:63], v[184:187], v[160:163]\n\t"
            "v_mfma_f32_16x16x32_bf16 v[164:167], v[84:87], v[184:187], v[164:167]\n\t"
            "v_mfma_f32_16x16x32_bf16 v[176:179], v[108:111], v[184:187], v[176:179]\n\t"
            "v_mfma_f32_16x16x32_bf16 v[180:183], v[132:135], v[184:187], v[180:183]\n\t"
            "ds_read_b128 v[184:187], v242 offset:6144\n\t"
            "s_waitcnt lgkmcnt(8)\n\t"
            "v_mfma_f32_16x16x32_bf16 v[152:155], v[16:19], v[188:191], v[152:155]\n\t"
            "v_mfma_f32_16x16x32_bf16 v[156:159], v[40:43], v[188:191], v[156:159]\n\t"
            "v_mfma_f32_16x16x32_bf16 v[160:163], v[64:67], v[188:191], v[160:163]\n\t"
            "v_mfma_f32_16x16x32_bf16 v[164:167], v[88:91], v[188:191], v[164:167]\n\t"
            "s_waitcnt lgkmcnt(1)\n\t"
            "v_readfirstlane_b32 s44, v249\n\t"
            "v_mfma_f32_16x16x32_bf16 v[176:179], v[112:115], v[188:191], v[176:179]\n\t"
            "s_and_b32 s70, s44, 0x7ffffff\n\t"
            "s_add_u32 s70, s70, s77\n\t"
            "s_and_b32 s44, s44, 0x08000000\n\t"
            "s_or_b32 s44, s44, s39\n\t"
            "v_mfma_f32_16x16x32_bf16 v[180:183], v[136:139], v[188:191], v[180:183]\n\t"
            "ds_read_b128 v[188:191], v243 offset:6144\n\t"
            "ds_read_b32 v249, v252 offset:1028\n\t"
            "v_mfma_f32_16x16x32_bf16 v[152:155], v[20:23], v[192:195], v[152:155]\n\t"
            "v_mfma_f32_16x16x32_bf16 v[156:159], v[44:47], v[192:195], v[156:159]\n\t"
            "v_mfma_f32_16x16x32_bf16 v[160:163], v[68:71], v[192:195], v[160:163]\n\t"
            "v_mfma_f32_16x16x32_bf16 v[164:167], v[92:95], v[192:195], v[164:167]\n\t"
            "v_mfma_f32_16x16x32_bf16 v[176:179], v[116:119], v[192:195], v[176:179]\n\t"
            "v_mfma_f32_16x16x32_bf16 v[180:183], v[140:143], v[192:195], v[180:183]\n\t"
            "ds_read_b128 v[192:195], v242 offset:6272\n\t"
            "s_waitcnt lgkmcnt(3)\n\t"
            "v_mfma_f32_16x16x32_bf16 v[144:147], v[0:3], v[184:187], v[144:147]\n\t"
            "v_cvt_pk_bf16_f32 v152, v152, v153\n\t"
            "v_cvt_pk_bf16_f32 v153, v154, v155\n\t"
            "v_mfma_f32_16x16x32_bf16 v[148:151], v[24:27], v[184:187], v[148:151]\n\t"
            "v_cvt_pk_bf16_f32 v160, v160, v161\n\t"
            "v_cvt_pk_bf16_f32 v161, v162, v163\n\t"
            "v_mfma_f32_16x16x32_bf16 v[168:171], v[48:51], v[184:187], v[168:171]\n\t"
            "v_cvt_pk_bf16_f32 v154, v156, v157\n\t"
            "v_cvt_pk_bf16_f32 v155, v158, v159\n\t"
            "v_mfma_f32_16x16x32_bf16 v[172:175], v[72:75], v[184:187], v[172:175]\n\t"
            "v_cvt_pk_bf16_f32 v162, v164, v165\n\t"
            "v_cvt_pk_bf16_f32 v163, v166, v167\n\t"
            "s_waitcnt lgkmcnt(2)\n\t"
            "v_mfma_f32_16x16x32_bf16 v[144:147], v[4:7], v[188:191], v[144:147]\n\t"
            "v_lshlrev_b32 v156, 16, v220\n\t"
            "v_and_b32 v157, 0xffff0000, v220\n\t"
            "v_mfma_f32_16x16x32_bf16 v[148:151], v[28:31], v[188:191], v[148:151]\n\t"
            "v_lshlrev_b32 v158, 16, v221\n\t"
            "v_and_b32 v159, 0xffff0000, v221\n\t"
            "v_mfma_f32_16x16x32_bf16 v[168:171], v[52:55], v[188:191], v[168:171]\n\t"
            "v_lshlrev_b32 v164, 16, v222\n\t"
            "v_and_b32 v165, 0xffff0000, v222\n\t"
            "v_mfma_f32_16x16x32_bf16 v[156:159], v[96:99], v[184:187], v[156:159]\n\t"
            "v_lshlrev_b32 v166, 16, v223\n\t"
            "v_and_b32 v167, 0xffff0000, v223\n\t"
            "v_mfma_f32_16x16x32_bf16 v[172:175], v[76:79], v[188:191], v[172:175]\n\t"
            "ds_read_b128 v[220:223], v248 offset:6144\n\t"
            "s_cbranch_vccnz .Lst0_%=\n\t"
            "buffer_store_dwordx4 v[152:155], v196, s[40:43], s45 offen\n\t"
            ".Lsr0_%=:\n\t"
            "v_mfma_f32_16x16x32_bf16 v[164:167], v[120:123], v[184:187], v[164:167]\n\t"
            "ds_read_b128 v[184:187], v243 offset:6272\n\t"
            "s_cbranch_vccnz .Lst1_%=\n\t"
            "buffer_store_dwordx4 v[160:163], v196, s[40:43], s45 offen offset:64\n\t"
            ".Lsr1_%=:\n\t"
            "v_mfma_f32_16x16x32_bf16 v[156:159], v[100:103], v[188:191], v[156:159]\n\t"
            "v_cvt_pk_bf16_f32 v176, v176, v177\n\t"
            "v_cvt_pk_bf16_f32 v177, v178, v179\n\t"
            "v_mfma_f32_16x16x32_bf16 v[164:167], v[124:127], v[188:191], v[164:167]\n\t"
            "ds_read_b128 v[188:191], v242 offset:6400\n\t"
            "v_cvt_pk_bf16_f32 v178, v180, v181\n\t"
            "s_waitcnt lgkmcnt(3)\n\t"
            "v_mfma_f32_16x16x32_bf16 v[144:147], v[8:11], v[192:195], v[144:147]\n\t"
            "v_cvt_pk_bf16_f32 v179, v182, v183\n\t"
            "v_lshlrev_b32 v152, 16, v224\n\t"
            "v_mfma_f32_16x16x32_bf16 v[148:151], v[32:35], v[192:195], v[148:151]\n\t"
            "s_cbranch_vccnz .Lst2_%=\n\t"
            "buffer_store_dwordx4 v[176:179], v196, s[40:43], s45 offen offset:128\n\t"
            ".Lsr2_%=:\n\t"
            "v_mfma_f32_16x16x32_bf16 v[168:171], v[56:59], v[192:195], v[168:171]\n\t"
            "v_and_b32 v153, 0xffff0000, v224\n\t"
            "v_lshlrev_b32 v154, 16, v225\n\t"
            "v_mfma_f32_16x16x32_bf16 v[172:175], v[80:83], v[192:195], v[172:175]\n\t"
            "v_and_b32 v155, 0xffff0000, v225\n\t"
            "v_lshlrev_b32 v180, 16, v226\n\t"
            "v_mfma_f32_16x16x32_bf16 v[156:159], v[104:107], v[192:195], v[156:159]\n\t"
            "v_and_b32 v181, 0xffff0000, v226\n\t"
            "v_lshlrev_b32 v182, 16, v227\n\t"
            "v_mfma_f32_16x16x32_bf16 v[164:167], v[128:131], v[192:195], v[164:167]\n\t"
            "ds_read_b128 v[192:195], v243 offset:6400\n\t"
            "v_and_b32 v183, 0xffff0000, v227\n\t"
            "s_waitcnt lgkmcnt(2)\n\t"
            "v_mfma_f32_16x16x32_bf16 v[144:147], v[12:15], v[184:187], v[144:147]\n\t"
            "ds_read_b128 v[224:227], v236 offset:12288\n\t"
            "v_lshlrev_b32 v160, 16, v228\n\t"
            "v_mfma_f32_16x16x32_bf16 v[148:151], v[36:39], v[184:187], v[148:151]\n\t"
            "v_and_b32 v161, 0xffff0000, v228\n\t"
            "v_lshlrev_b32 v162, 16, v229\n\t"
            "s_waitcnt lgkmcnt(2)\n\t"
            "v_mfma_f32_16x16x32_bf16 v[144:147], v[16:19], v[188:191], v[144:147]\n\t"
            "v_and_b32 v163, 0xffff0000, v229\n\t"
            "v_lshlrev_b32 v176, 16, v230\n\t"
            "v_mfma_f32_16x16x32_bf16 v[168:171], v[60:63], v[184:187], v[168:171]\n\t"
            "v_and_b32 v177, 0xffff0000, v230\n\t"
            "v_lshlrev_b32 v178, 16, v231\n\t"
            "v_mfma_f32_16x16x32_bf16 v[148:151], v[40:43], v[188:191], v[148:151]\n\t"
            "v_and_b32 v179, 0xffff0000, v231\n\t"
            "v_and_b32 v249, s44, v249\n\t"
            "v_mfma_f32_16x16x32_bf16 v[172:175], v[84:87], v[184:187], v[172:175]\n\t"
            "ds_read_b128 v[228:231], v237 offset:12288\n\t"
            "v_mfma_f32_16x16x32_bf16 v[168:171], v[64:67], v[188:191], v[168:171]\n\t"
            "v_mfma_f32_16x16x32_bf16 v[156:159], v[108:111], v[184:187], v[156:159]\n\t"
            "s_waitcnt lgkmcnt(2)\n\t"
            "v_mfma_f32_16x16x32_bf16 v[144:147], v[20:23], v[192:195], v[144:147]\n\t"
            "v_mfma_f32_16x16x32_bf16 v[148:151], v[44:47], v[192:195], v[148:151]\n\t"
            "v_mfma_f32_16x16x32_bf16 v[164:167], v[132:135], v[184:187], v[164:167]\n\t"
            "ds_read_b128 v[184:187], v242 offset:12288\n\t"
            "v_mfma_f32_16x16x32_bf16 v[172:175], v[88:91], v[188:191], v[172:175]\n\t"
            "v_mfma_f32_16x16x32_bf16 v[168:171], v[68:71], v[192:195], v[168:171]\n\t"
            "v_mfma_f32_16x16x32_bf16 v[156:159], v[112:115], v[188:191], v[156:159]\n\t"
            "s_nop 1\n\t"
            "v_cvt_pk_bf16_f32 v144, v144, v145\n\t"
            "v_mfma_f32_16x16x32_bf16 v[164:167], v[136:139], v[188:191], v[164:167]\n\t"
            "ds_read_b128 v[188:191], v243 offset:12288\n\t"
            "v_cvt_pk_bf16_f32 v145, v146, v147\n\t"
            "v_mfma_f32_16x16x32_bf16 v[172:175], v[92:95], v[192:195], v[172:175]\n\t"
            "v_cvt_pk_bf16_f32 v146, v148, v149\n\t"
            "v_cvt_pk_bf16_f32 v147, v150, v151\n\t"
            "v_mfma_f32_16x16x32_bf16 v[156:159], v[116:119], v[192:195], v[156:159]\n\t"
            "s_cbranch_vccnz .Lst3_%=\n\t"
            "buffer_store_dwordx4 v[144:147], v197, s[40:43], s45 offen\n\t"
            ".Lsr3_%=:\n\t"
            "v_mfma_f32_16x16x32_bf16 v[164:167], v[140:143], v[192:195], v[164:167]\n\t"
            "ds_read_b128 v[192:195], v242 offset:12416\n\t"
            "v_cvt_pk_bf16_f32 v168, v168, v169\n\t"
            "s_waitcnt lgkmcnt(2)\n\t"
            "v_mfma_f32_16x16x32_bf16 v[152:155], v[0:3], v[184:187], v[152:155]\n\t"
            "v_cvt_pk_bf16_f32 v169, v170, v171\n\t"
            "v_cvt_pk_bf16_f32 v170, v172, v173\n\t"
            "v_mfma_f32_16x16x32_bf16 v[180:183], v[24:27], v[184:187], v[180:183]\n\t"
            "v_cvt_pk_bf16_f32 v171, v174, v175\n\t"
            "v_cvt_pk_bf16_f32 v156, v156, v157\n\t"
            "v_mfma_f32_16x16x32_bf16 v[160:163], v[48:51], v[184:187], v[160:163]\n\t"
            "ds_read_b128 v[172:175], v254 offset:64\n\t"
            "s_cbranch_vccnz .Lst4_%=\n\t"
            "buffer_store_dwordx4 v[168:171], v197, s[40:43], s45 offen offset:64\n\t"
            ".Lsr4_%=:\n\t"
            "v_mfma_f32_16x16x32_bf16 v[176:179], v[72:75], v[184:187], v[176:179]\n\t"
            "s_nop 0\n\t"
            "ds_read_b128 v[168:171], v254\n\t"
            "v_cvt_pk_bf16_f32 v157, v158, v159\n\t"
            "s_waitcnt lgkmcnt(3)\n\t"
            "v_mfma_f32_16x16x32_bf16 v[152:155], v[4:7], v[188:191], v[152:155]\n\t"
            "v_cvt_pk_bf16_f32 v158, v164, v165\n\t"
            "v_cvt_pk_bf16_f32 v159, v166, v167\n\t"
            "v_mfma_f32_16x16x32_bf16 v[180:183], v[28:31], v[188:191], v[180:183]\n\t"
            "s_cbranch_vccnz .Lst5_%=\n\t"
            "buffer_store_dwordx4 v[156:159], v197, s[40:43], s45 offen offset:128\n\t"
            ".Lsr5_%=:\n\t"
            "v_mfma_f32_16x16x32_bf16 v[160:163], v[52:55], v[188:191], v[160:163]\n\t"
            "s_waitcnt vmcnt(6)\n\t"
            "ds_read_b64 v[144:145], v253\n\t"
            "ds_read_b64 v[146:147], v253 offset:96\n\t"
            "v_mfma_f32_16x16x32_bf16 v[176:179], v[76:79], v[188:191], v[176:179]\n\t"
            "ds_read_b64 v[148:149], v253 offset:1744\n\t"
            "ds_read_b64 v[150:151], v253 offset:1840\n\t"
            "s_waitcnt lgkmcnt(6)\n\t"
            "v_mfma_f32_16x16x32_bf16 v[152:155], v[8:11], v[192:195], v[152:155]\n\t"
            "v_lshlrev_b32 v164, 16, v232\n\t"
            "v_and_b32 v165, 0xffff0000, v232\n\t"
            "v_mfma_f32_16x16x32_bf16 v[180:183], v[32:35], v[192:195], v[180:183]\n\t"
            "v_lshlrev_b32 v166, 16, v233\n\t"
            "v_and_b32 v167, 0xffff0000, v233\n\t"
            "v_mfma_f32_16x16x32_bf16 v[160:163], v[56:59], v[192:195], v[160:163]\n\t"
            "v_lshlrev_b32 v156, 16, v234\n\t"
            "v_and_b32 v157, 0xffff0000, v234\n\t"
            "v_mfma_f32_16x16x32_bf16 v[164:167], v[96:99], v[184:187], v[164:167]\n\t"
            "v_lshlrev_b32 v158, 16, v235\n\t"
            "v_and_b32 v159, 0xffff0000, v235\n\t"
            "v_mfma_f32_16x16x32_bf16 v[164:167], v[100:103], v[188:191], v[164:167]\n\t"
            "ds_read_b128 v[232:235], v248 offset:12288\n\t"
            "v_mfma_f32_16x16x32_bf16 v[156:159], v[120:123], v[184:187], v[156:159]\n\t"
            "ds_read_b128 v[184:187], v243 offset:12416\n\t"
            "v_mfma_f32_16x16x32_bf16 v[156:159], v[124:127], v[188:191], v[156:159]\n\t"
            "ds_read_b128 v[188:191], v242 offset:12544\n\t"
            "v_mfma_f32_16x16x32_bf16 v[176:179], v[80:83], v[192:195], v[176:179]\n\t"
            "v_mfma_f32_16x16x32_bf16 v[164:167], v[104:107], v[192:195], v[164:167]\n\t"
            "v_mfma_f32_16x16x32_bf16 v[156:159], v[128:131], v[192:195], v[156:159]\n\t"
            "ds_read_b128 v[192:195], v243 offset:12544\n\t"
            "s_waitcnt lgkmcnt(2)\n\t"
            "v_mfma_f32_16x16x32_bf16 v[152:155], v[12:15], v[184:187], v[152:155]\n\t"
            "v_mfma_f32_16x16x32_bf16 v[180:183], v[36:39], v[184:187], v[180:183]\n\t"
            "s_waitcnt lgkmcnt(1)\n\t"
            "v_mfma_f32_16x16x32_bf16 v[152:155], v[16:19], v[188:191], v[152:155]\n\t"
            "v_mfma_f32_16x16x32_bf16 v[160:163], v[60:63], v[184:187], v[160:163]\n\t"
            "s_waitcnt lgkmcnt(0)\n\t"
            "s_barrier\n\t"
            "v_mfma_f32_16x16x32_bf16 v[180:183], v[40:43], v[188:191], v[180:183]\n\t"
            "v_mfma_f32_16x16x32_bf16 v[176:179], v[84:87], v[184:187], v[176:179]\n\t"
            "v_mfma_f32_16x16x32_bf16 v[152:155], v[20:23], v[192:195], v[152:155]\n\t"
            "v_mfma_f32_16x16x32_bf16 v[160:163], v[64:67], v[188:191], v[160:163]\n\t"
            "v_mfma_f32_16x16x32_bf16 v[180:183], v[44:47], v[192:195], v[180:183]\n\t"
            "v_mfma_f32_16x16x32_bf16 v[164:167], v[108:111], v[184:187], v[164:167]\n\t"
            "v_mfma_f32_16x16x32_bf16 v[176:179], v[88:91], v[188:191], v[176:179]\n\t"
            "v_mfma_f32_16x16x32_bf16 v[156:159], v[132:135], v[184:187], v[156:159]\n\t"
            "v_mfma_f32_16x16x32_bf16 v[160:163], v[68:71], v[192:195], v[160:163]\n\t"
            "s_nop 1\n\t"
            "v_cvt_pk_bf16_f32 v152, v152, v153\n\t"
            "v_mfma_f32_16x16x32_bf16 v[164:167], v[112:115], v[188:191], v[164:167]\n\t"
            "v_cvt_pk_bf16_f32 v153, v154, v155\n\t"
            "v_mfma_f32_16x16x32_bf16 v[176:179], v[92:95], v[192:195], v[176:179]\n\t"
            "v_cvt_pk_bf16_f32 v154, v180, v181\n\t"
            "v_cvt_pk_bf16_f32 v155, v182, v183\n\t"
            "v_mfma_f32_16x16x32_bf16 v[156:159], v[136:139], v[188:191], v[156:159]\n\t"
            "s_cbranch_vccnz .Lst6_%=\n\t"
            "buffer_store_dwordx4 v[152:155], v198, s[40:43], s45 offen\n\t"
            ".Lsr6_%=:\n\t"
            "v_mfma_f32_16x16x32_bf16 v[164:167], v[116:119], v[192:195], v[164:167]\n\t"
            "s_nop 0\n\t"
            "ds_read_b64 v[152:153], v253 offset:8\n\t"
            "ds_read_b64 v[154:155], v253 offset:104\n\t"
            "v_mfma_f32_16x16x32_bf16 v[156:159], v[140:143], v[192:195], v[156:159]\n\t"
            "v_cvt_pk_bf16_f32 v160, v160, v161\n\t"
            "v_cvt_pk_bf16_f32 v161, v162, v163\n\t"
            "v_cvt_pk_bf16_f32 v162, v176, v177\n\t"
            "v_cvt_pk_bf16_f32 v163, v178, v179\n\t"
            "v_cvt_pk_bf16_f32 v164, v164, v165\n\t"
            "v_cvt_pk_bf16_f32 v165, v166, v167\n\t"
            "s_cbranch_vccnz .Lst7_%=\n\t"
            "buffer_store_dwordx4 v[160:163], v198, s[40:43], s45 offen offset:64\n\t"
            ".Lsr7_%=:\n\t"
            "s_nop 0\n\t"
            "v_cvt_pk_bf16_f32 v166, v156, v157\n\t"
            "v_cvt_pk_bf16_f32 v167, v158, v159\n\t"
            "ds_read_b64 v[156:157], v253 offset:1752\n\t"
            "ds_read_b64 v[158:159], v253 offset:1848\n\t"
            "ds_read_b64 v[160:161], v253 offset:16\n\t"
            "ds_read_b64 v[162:163], v253 offset:112\n\t"
            "s_cbranch_vccnz .Lst8_%=\n\t"
            "buffer_store_dwordx4 v[164:167], v198, s[40:43], s45 offen offset:128\n\t"
            ".Lsr8_%=:\n\t"
            "s_mov_b32 m0, s32\n\t"
            "s_add_u32 s49, s70, s25\n\t"
            "buffer_load_dword v249, s[72:75], s70 offen lds\n\t"
            "buffer_load_dword v249, s[72:75], s49 offen lds\n\t"
            "ds_read_b64 v[164:165], v253 offset:1760\n\t"
            "ds_read_b64 v[166:167], v253 offset:1856\n\t"
            "s_add_u32 s45, s46, s27\n\t"
            "s_mov_b32 s46, s47\n\t"
            "s_mov_b32 s47, s68\n\t"
            "s_xor_b32 s48, s48, s71\n\t"
            "s_xor_b32 s67, s67, s71\n\t"
            "s_add_u32 s76, s76, 4\n\t"
            "s_lshl_b32 s56, s56, 2\n\t"
            "s_lshr_b32 s49, s26, 30\n\t"
            "s_or_b32 s56, s56, s49\n\t"
            "s_cmp_ge_u32 s76, s24\n\t"
            "s_cselect_b64 vcc, -1, 0\n\t"
            "s_cmp_lt_u32 s46, 0x7000000\n\t"
            "s_cbranch_scc1 .Lwb_loop%=\n\t"
            "s_branch .Lsx%=\n\t"
            ".Lst0_%=:\n\t"
            "buffer_store_dwordx4 v[152:155], v196, s[40:43], s45 offen sc1\n\t"
            "s_branch .Lsr0_%=\n\t"
            ".Lst1_%=:\n\t"
            "buffer_store_dwordx4 v[160:163], v196, s[40:43], s45 offen offset:64 sc1\n\t"
            "s_branch .Lsr1_%=\n\t"
            ".Lst2_%=:\n\t"
            "buffer_store_dwordx4 v[176:179], v196, s[40:43], s45 offen offset:128 sc1\n\t"
            "s_branch .Lsr2_%=\n\t"
            ".Lst3_%=:\n\t"
            "buffer_store_dwordx4 v[144:147], v197, s[40:43], s45 offen sc1\n\t"
            "s_branch .Lsr3_%=\n\t"
            ".Lst4_%=:\n\t"
            "buffer_store_dwordx4 v[168:171], v197, s[40:43], s45 offen offset:64 sc1\n\t"
            "s_branch .Lsr4_%=\n\t"
            ".Lst5_%=:\n\t"
            "buffer_store_dwordx4 v[156:159], v197, s[40:43], s45 offen offset:128 sc1\n\t"
            "s_branch .Lsr5_%=\n\t"
            ".Lst6_%=:\n\t"
            "buffer_store_dwordx4 v[152:155], v198, s[40:43], s45 offen sc1\n\t"
            "s_branch .Lsr6_%=\n\t"
            ".Lst7_%=:\n\t"
            "buffer_store_dwordx4 v[160:163], v198, s[40:43], s45 offen offset:64 sc1\n\t"
            "s_branch .Lsr7_%=\n\t"
            ".Lst8_%=:\n\t"
            "buffer_store_dwordx4 v[164:167], v198, s[40:43], s45 offen offset:128 sc1\n\t"
            "s_branch .Lsr8_%=\n\t"
            ".Lsx%=:\n\t"
            "s_waitcnt lgkmcnt(0)\n\t"
            "s_nop 7\n\t"
            : "={v[0:3]}"(W[0]), "={v[4:7]}"(W[1]), "={v[8:11]}"(W[2]), "={v[12:15]}"(W[3]), "={v[16:19]}"(W[4]), "={v[20:23]}"(W[5]), "={v[24:27]}"(W[6]), "={v[28:31]}"(W[7]), "={v[32:35]}"(W[8]), "={v[36:39]}"(W[9]), "={v[40:43]}"(W[10]), "={v[44:47]}"(W[11]), "={v[48:51]}"(W[12]), "={v[52:55]}"(W[13]), "={v[56:59]}"(W[14]), "={v[60:63]}"(W[15]), "={v[64:67]}"(W[16]), "={v[68:71]}"(W[17]), "={v[72:75]}"(W[18]), "={v[76:79]}"(W[19]), "={v[80:83]}"(W[20]), "={v[84:87]}"(W[21]), "={v[88:91]}"(W[22]), "={v[92:95]}"(W[23]), "={v[96:99]}"(W[24]), "={v[100:103]}"(W[25]), "={v[104:107]}"(W[26]), "={v[108:111]}"(W[27]), "={v[112:115]}"(W[28]), "={v[116:119]}"(W[29]), "={v[120:123]}"(W[30]), "={v[124:127]}"(W[31]), "={v[128:131]}"(W[32]), "={v[132:135]}"(W[33]), "={v[136:139]}"(W[34]), "={v[140:143]}"(W[35]), "+{v[200:203]}"(xl[0]), "+{v[204:207]}"(xl[1]), "+{v[208:211]}"(xl[2]), "+{v[212:215]}"(xl[3]), "+{v[216:219]}"(xl[4]), "+{v[220:223]}"(xl[5]), "+{v[224:227]}"(xl[6]), "+{v[228:231]}"(xl[7]), "+{v[232:235]}"(xl[8]), B_SIO
            : "{v242}"(oE_), "{v243}"(oO_), "{v244}"(pR0_), "{v245}"(pR1_), "{v246}"(pR2_), "{v247}"(l16_), "{v250}"(pXw_), "{v251}"(pSTw_), "{v252}"(aCA_), "{v253}"(aB_), "{v254}"(aA_), "{v255}"(idx_), B_SIN
            : "v144", "v145", "v146", "v147", "v148", "v149", "v150", "v151", "v152", "v153", "v154", "v155", "v156", "v157", "v158", "v159", "v160", "v161", "v162", "v163", "v164", "v165", "v166", "v167", "v168", "v169", "v170", "v171", "v172", "v173", "v174", "v175", "v176", "v177", "v178", "v179", "v180", "v181", "v182", "v183", "v184", "v185", "v186", "v187", "v188", "v189", "v190", "v191", "v192", "v193", "v194", "v195", "v196", "v197", "v198", "v199", "v236", "v237", "v248", "v249", B_SCLOB);
            t45 = s45; t56 = s56;
        }
        {
            int ln_;
            asm volatile("v_mbcnt_lo_u32_b32 %0, -1, 0\n\tv_mbcnt_hi_u32_b32 %0, -1, %0" : "=v"(ln_));
            const int l15_ = ln_ & 15;
            const int g_ = ln_ >> 4;
#pragma unroll
            for (int lt = 0; lt < 2; ++lt) {
                int i = 16 * lt + l15_;
                const unsigned en = *(const unsigned*)(smem + WL_OFF + i * 4);
                const bool ok = (en & 0x7ffffffu) != 0x7ffffffu;
                i = ok ? i : 0;
                const int lE = OL_OFF + i * 384 + ((g_ ^ ((i >> 1) & 7)) << 4);
                const unsigned o = ok ? (en & 0x7ffffffu) + LPOS(en >> 31) * ROWBYTES + LPOS(en >> 30) * (CH * 2) + 192 * (w - 2) + 16 * g_ : OOB;
                u32x4 rl[3];
#pragma unroll
                for (int k = 0; k < 3; ++k) rl[k] = __builtin_bit_cast(u32x4, __builtin_amdgcn_raw_buffer_load_b128(orsrc, o + 64 * k, 0, 0));
                if (lt == 1) {
                    const unsigned oE_ = ldsb + O_OFF + l15_ * 384 + ((g_ ^ (l15_ >> 1)) << 4), oO_ = oE_ ^ 64, aCA_ = ldsb + CT_OFF + ln_ * 16;
                    const unsigned long long ob = (unsigned long long)out;
                    const u32x4 orr = {(unsigned)ob, (unsigned)(ob >> 32) & 0xffffu, (unsigned)(IMG * IMG * CH * 2), 0x00020000u};
                    asm volatile(
            "s_waitcnt lgkmcnt(0)\n\t"
            "s_barrier\n\t"
            "s_nop 7\n\t"
            "ds_read_b128 v[184:187], v242\n\t"
            "ds_read_b128 v[188:191], v243\n\t"
            "ds_read_b128 v[192:195], v242 offset:128\n\t"
            "ds_read_b128 v[196:199], v252\n\t"
            "v_lshlrev_b32 v152, 16, v200\n\t"
            "v_and_b32 v153, 0xffff0000, v200\n\t"
            "v_lshlrev_b32 v154, 16, v201\n\t"
            "v_and_b32 v155, 0xffff0000, v201\n\t"
            "v_lshlrev_b32 v156, 16, v202\n\t"
            "v_and_b32 v157, 0xffff0000, v202\n\t"
            "v_lshlrev_b32 v158, 16, v203\n\t"
            "v_and_b32 v159, 0xffff0000, v203\n\t"
            "v_lshlrev_b32 v160, 16, v204\n\t"
            "v_and_b32 v161, 0xffff0000, v204\n\t"
            "v_lshlrev_b32 v162, 16, v205\n\t"
            "v_and_b32 v163, 0xffff0000, v205\n\t"
            "v_lshlrev_b32 v164, 16, v206\n\t"
            "v_and_b32 v165, 0xffff0000, v206\n\t"
            "v_lshlrev_b32 v166, 16, v207\n\t"
            "v_and_b32 v167, 0xffff0000, v207\n\t"
            "v_lshlrev_b32 v176, 16, v208\n\t"
            "v_and_b32 v177, 0xffff0000, v208\n\t"
            "v_lshlrev_b32 v178, 16, v209\n\t"
            "v_and_b32 v179, 0xffff0000, v209\n\t"
            "v_lshlrev_b32 v180, 16, v210\n\t"
            "v_and_b32 v181, 0xffff0000, v210\n\t"
            "v_lshlrev_b32 v182, 16, v211\n\t"
            "v_and_b32 v183, 0xffff0000, v211\n\t"
            "v_lshlrev_b32 v144, 16, v212\n\t"
            "v_and_b32 v145, 0xffff0000, v212\n\t"
            "v_lshlrev_b32 v146, 16, v213\n\t"
            "s_waitcnt lgkmcnt(3)\n\t"
            "v_mfma_f32_16x16x32_bf16 v[152:155], v[0:3], v[184:187], v[152:155]\n\t"
            "s_bfe_u32 s57, s56, 0x20004\n\t"
            "s_lshl_b32 s58, s57, 3\n\t"
            "s_lshr_b32 s58, s55, s58\n\t"
            "s_lshl_b64 s[36:37], s[34:35], s58\n\t"
            "v_mfma_f32_16x16x32_bf16 v[156:159], v[24:27], v[184:187], v[156:159]\n\t"
            "s_bitcmp1_b32 s57, 1\n\t"
            "s_cselect_b32 s58, 1177344, 0\n\t"
            "s_bitcmp1_b32 s57, 0\n\t"
            "s_cselect_b32 s57, 2304, 0\n\t"
            "s_add_u32 s57, s57, s58\n\t"
            "v_mfma_f32_16x16x32_bf16 v[160:163], v[48:51], v[184:187], v[160:163]\n\t"
            "v_and_b32 v147, 0xffff0000, v213\n\t"
            "v_lshlrev_b32 v148, 16, v214\n\t"
            "v_mfma_f32_16x16x32_bf16 v[164:167], v[72:75], v[184:187], v[164:167]\n\t"
            "v_and_b32 v149, 0xffff0000, v214\n\t"
            "v_lshlrev_b32 v150, 16, v215\n\t"
            "v_mfma_f32_16x16x32_bf16 v[176:179], v[96:99], v[184:187], v[176:179]\n\t"
            "v_and_b32 v151, 0xffff0000, v215\n\t"
            "v_lshlrev_b32 v168, 16, v216\n\t"
            "v_mfma_f32_16x16x32_bf16 v[180:183], v[120:123], v[184:187], v[180:183]\n\t"
            "ds_read_b128 v[184:187], v243 offset:128\n\t"
            "v_and_b32 v169, 0xffff0000, v216\n\t"
            "s_waitcnt lgkmcnt(3)\n\t"
            "v_mfma_f32_16x16x32_bf16 v[152:155], v[4:7], v[188:191], v[152:155]\n\t"
            "v_lshlrev_b32 v170, 16, v217\n\t"
            "v_and_b32 v171, 0xffff0000, v217\n\t"
            "v_mfma_f32_16x16x32_bf16 v[156:159], v[28:31], v[188:191], v[156:159]\n\t"
            "v_lshlrev_b32 v172, 16, v218\n\t"
            "v_and_b32 v173, 0xffff0000, v218\n\t"
            "v_mfma_f32_16x16x32_bf16 v[160:163], v[52:55], v[188:191], v[160:163]\n\t"
            "v_lshlrev_b32 v174, 16, v219\n\t"
            "v_and_b32 v175, 0xffff0000, v219\n\t"
            "v_mfma_f32_16x16x32_bf16 v[164:167], v[76:79], v[188:191], v[164:167]\n\t"
            "s_waitcnt lgkmcnt(1)\n\t"
            "s_mov_b64 exec, s[36:37]\n\t"
            "v_add_u32 v196, s57, v196\n\t"
            "s_mov_b64 exec, -1\n\t"
            "v_mfma_f32_16x16x32_bf16 v[176:179], v[100:103], v[188:191], v[176:179]\n\t"
            "v_mfma_f32_16x16x32_bf16 v[180:183], v[124:127], v[188:191], v[180:183]\n\t"
            "ds_read_b128 v[188:191], v242 offset:256\n\t"
            "v_mfma_f32_16x16x32_bf16 v[152:155], v[8:11], v[192:195], v[152:155]\n\t"
            "v_mfma_f32_16x16x32_bf16 v[156:159], v[32:35], v[192:195], v[156:159]\n\t"
            "v_mfma_f32_16x16x32_bf16 v[160:163], v[56:59], v[192:195], v[160:163]\n\t"
            "v_mfma_f32_16x16x32_bf16 v[164:167], v[80:83], v[192:195], v[164:167]\n\t"
            "v_mfma_f32_16x16x32_bf16 v[176:179], v[104:107], v[192:195], v[176:179]\n\t"
            "v_mfma_f32_16x16x32_bf16 v[180:183], v[128:131], v[192:195], v[180:183]\n\t"
            "ds_read_b128 v[192:195], v243 offset:256\n\t"
            "s_waitcnt lgkmcnt(2)\n\t"
            "v_mfma_f32_16x16x32_bf16 v[152:155], v[12:15], v[184:187], v[152:155]\n\t"
            "v_mfma_f32_16x16x32_bf16 v[156:159], v[36:39], v[184:187], v[156:159]\n\t"
            "v_mfma_f32_16x16x32_bf16 v[160:163], v[60:63], v[184:187], v[160:163]\n\t"
            "v_mfma_f32_16x16x32_bf16 v[164:167], v[84:87], v[184:187], v[164:167]\n\t"
            "v_mfma_f32_16x16x32_bf16 v[176:179], v[108:111], v[184:187], v[176:179]\n\t"
            "v_mfma_f32_16x16x32_bf16 v[180:183], v[132:135], v[184:187], v[180:183]\n\t"
            "ds_read_b128 v[184:187], v242 offset:6144\n\t"
            "s_waitcnt lgkmcnt(2)\n\t"
            "v_mfma_f32_16x16x32_bf16 v[152:155], v[16:19], v[188:191], v[152:155]\n\t"
            "v_mfma_f32_16x16x32_bf16 v[156:159], v[40:43], v[188:191], v[156:159]\n\t"
            "v_mfma_f32_16x16x32_bf16 v[160:163], v[64:67], v[188:191], v[160:163]\n\t"
            "v_mfma_f32_16x16x32_bf16 v[164:167], v[88:91], v[188:191], v[164:167]\n\t"
            "v_mfma_f32_16x16x32_bf16 v[176:179], v[112:115], v[188:191], v[176:179]\n\t"
            "v_mfma_f32_16x16x32_bf16 v[180:183], v[136:139], v[188:191], v[180:183]\n\t"
            "ds_read_b128 v[188:191], v243 offset:6144\n\t"
            "s_waitcnt lgkmcnt(2)\n\t"
            "v_mfma_f32_16x16x32_bf16 v[152:155], v[20:23], v[192:195], v[152:155]\n\t"
            "v_mfma_f32_16x16x32_bf16 v[156:159], v[44:47], v[192:195], v[156:159]\n\t"
            "v_mfma_f32_16x16x32_bf16 v[160:163], v[68:71], v[192:195], v[160:163]\n\t"
            "v_mfma_f32_16x16x32_bf16 v[164:167], v[92:95], v[192:195], v[164:167]\n\t"
            "v_mfma_f32_16x16x32_bf16 v[176:179], v[116:119], v[192:195], v[176:179]\n\t"
            "v_mfma_f32_16x16x32_bf16 v[180:183], v[140:143], v[192:195], v[180:183]\n\t"
            "ds_read_b128 v[192:195], v242 offset:6272\n\t"
            "s_waitcnt lgkmcnt(2)\n\t"
            "v_mfma_f32_16x16x32_bf16 v[144:147], v[0:3], v[184:187], v[144:147]\n\t"
            "v_cvt_pk_bf16_f32 v152, v152, v153\n\t"
            "v_cvt_pk_bf16_f32 v153, v154, v155\n\t"
            "v_mfma_f32_16x16x32_bf16 v[148:151], v[24:27], v[184:187], v[148:151]\n\t"
            "v_cvt_pk_bf16_f32 v160, v160, v161\n\t"
            "v_cvt_pk_bf16_f32 v161, v162, v163\n\t"
            "v_mfma_f32_16x16x32_bf16 v[168:171], v[48:51], v[184:187], v[168:171]\n\t"
            "v_cvt_pk_bf16_f32 v154, v156, v157\n\t"
            "v_cvt_pk_bf16_f32 v155, v158, v159\n\t"
            "v_mfma_f32_16x16x32_bf16 v[172:175], v[72:75], v[184:187], v[172:175]\n\t"
            "v_cvt_pk_bf16_f32 v162, v164, v165\n\t"
            "v_cvt_pk_bf16_f32 v163, v166, v167\n\t"
            "s_waitcnt lgkmcnt(1)\n\t"
            "v_mfma_f32_16x16x32_bf16 v[144:147], v[4:7], v[188:191], v[144:147]\n\t"
            "v_lshlrev_b32 v156, 16, v220\n\t"
            "v_and_b32 v157, 0xffff0000, v220\n\t"
            "v_mfma_f32_16x16x32_bf16 v[148:151], v[28:31], v[188:191], v[148:151]\n\t"
            "v_lshlrev_b32 v158, 16, v221\n\t"
            "v_and_b32 v159, 0xffff0000, v221\n\t"
            "v_mfma_f32_16x16x32_bf16 v[168:171], v[52:55], v[188:191], v[168:171]\n\t"
            "v_lshlrev_b32 v164, 16, v222\n\t"
            "v_and_b32 v165, 0xffff0000, v222\n\t"
            "v_mfma_f32_16x16x32_bf16 v[156:159], v[96:99], v[184:187], v[156:159]\n\t"
            "v_lshlrev_b32 v166, 16, v223\n\t"
            "v_and_b32 v167, 0xffff0000, v223\n\t"
            "v_mfma_f32_16x16x32_bf16 v[172:175], v[76:79], v[188:191], v[172:175]\n\t"
            "buffer_store_dwordx4 v[152:155], v196, s[40:43], s45 offen sc1\n\t"
            "v_mfma_f32_16x16x32_bf16 v[164:167], v[120:123], v[184:187], v[164:167]\n\t"
            "ds_read_b128 v[184:187], v243 offset:6272\n\t"
            "buffer_store_dwordx4 v[160:163], v196, s[40:43], s45 offen offset:64 sc1\n\t"
            "v_mfma_f32_16x16x32_bf16 v[156:159], v[100:103], v[188:191], v[156:159]\n\t"
            "v_cvt_pk_bf16_f32 v176, v176, v177\n\t"
            "v_cvt_pk_bf16_f32 v177, v178, v179\n\t"
            "v_mfma_f32_16x16x32_bf16 v[164:167], v[124:127], v[188:191], v[164:167]\n\t"
            "ds_read_b128 v[188:191], v242 offset:6400\n\t"
            "v_cvt_pk_bf16_f32 v178, v180, v181\n\t"
            "s_waitcnt lgkmcnt(2)\n\t"
            "v_mfma_f32_16x16x32_bf16 v[144:147], v[8:11], v[192:195], v[144:147]\n\t"
            "v_cvt_pk_bf16_f32 v179, v182, v183\n\t"
            "v_lshlrev_b32 v152, 16, v224\n\t"
            "v_mfma_f32_16x16x32_bf16 v[148:151], v[32:35], v[192:195], v[148:151]\n\t"
            "buffer_store_dwordx4 v[176:179], v196, s[40:43], s45 offen offset:128 sc1\n\t"
            "v_mfma_f32_16x16x32_bf16 v[168:171], v[56:59], v[192:195], v[168:171]\n\t"
            "v_and_b32 v153, 0xffff0000, v224\n\t"
            "v_lshlrev_b32 v154, 16, v225\n\t"
            "v_mfma_f32_16x16x32_bf16 v[172:175], v[80:83], v[192:195], v[172:175]\n\t"
            "v_and_b32 v155, 0xffff0000, v225\n\t"
            "v_lshlrev_b32 v180, 16, v226\n\t"
            "v_mfma_f32_16x16x32_bf16 v[156:159], v[104:107], v[192:195], v[156:159]\n\t"
            "v_and_b32 v181, 0xffff0000, v226\n\t"
            "v_lshlrev_b32 v182, 16, v227\n\t"
            "v_mfma_f32_16x16x32_bf16 v[164:167], v[128:131], v[192:195], v[164:167]\n\t"
            "ds_read_b128 v[192:195], v243 offset:6400\n\t"
            "v_and_b32 v183, 0xffff0000, v227\n\t"
            "s_waitcnt lgkmcnt(2)\n\t"
            "v_mfma_f32_16x16x32_bf16 v[144:147], v[12:15], v[184:187], v[144:147]\n\t"
            "v_lshlrev_b32 v160, 16, v228\n\t"
            "v_and_b32 v161, 0xffff0000, v228\n\t"
            "v_mfma_f32_16x16x32_bf16 v[148:151], v[36:39], v[184:187], v[148:151]\n\t"
            "v_lshlrev_b32 v162, 16, v229\n\t"
            "v_and_b32 v163, 0xffff0000, v229\n\t"
            "s_waitcnt lgkmcnt(1)\n\t"
            "v_mfma_f32_16x16x32_bf16 v[144:147], v[16:19], v[188:191], v[144:147]\n\t"
            "v_lshlrev_b32 v176, 16, v230\n\t"
            "v_and_b32 v177, 0xffff0000, v230\n\t"
            "v_mfma_f32_16x16x32_bf16 v[168:171], v[60:63], v[184:187], v[168:171]\n\t"
            "v_lshlrev_b32 v178, 16, v231\n\t"
            "v_and_b32 v179, 0xffff0000, v231\n\t"
            "v_mfma_f32_16x16x32_bf16 v[148:151], v[40:43], v[188:191], v[148:151]\n\t"
            "v_mfma_f32_16x16x32_bf16 v[172:175], v[84:87], v[184:187], v[172:175]\n\t"
            "v_mfma_f32_16x16x32_bf16 v[168:171], v[64:67], v[188:191], v[168:171]\n\t"
            "v_mfma_f32_16x16x32_bf16 v[156:159], v[108:111], v[184:187], v[156:159]\n\t"
            "s_waitcnt lgkmcnt(0)\n\t"
            "v_mfma_f32_16x16x32_bf16 v[144:147], v[20:23], v[192:195], v[144:147]\n\t"
            "v_mfma_f32_16x16x32_bf16 v[148:151], v[44:47], v[192:195], v[148:151]\n\t"
            "v_mfma_f32_16x16x32_bf16 v[164:167], v[132:135], v[184:187], v[164:167]\n\t"
            "ds_read_b128 v[184:187], v242 offset:12288\n\t"
            "v_mfma_f32_16x16x32_bf16 v[172:175], v[88:91], v[188:191], v[172:175]\n\t"
            "v_mfma_f32_16x16x32_bf16 v[168:171], v[68:71], v[192:195], v[168:171]\n\t"
            "v_mfma_f32_16x16x32_bf16 v[156:159], v[112:115], v[188:191], v[156:159]\n\t"
            "s_nop 1\n\t"
            "v_cvt_pk_bf16_f32 v144, v144, v145\n\t"
            "v_mfma_f32_16x16x32_bf16 v[164:167], v[136:139], v[188:191], v[164:167]\n\t"
            "ds_read_b128 v[188:191], v243 offset:12288\n\t"
            "v_cvt_pk_bf16_f32 v145, v146, v147\n\t"
            "v_mfma_f32_16x16x32_bf16 v[172:175], v[92:95], v[192:195], v[172:175]\n\t"
            "v_cvt_pk_bf16_f32 v146, v148, v149\n\t"
            "v_cvt_pk_bf16_f32 v147, v150, v151\n\t"
            "v_mfma_f32_16x16x32_bf16 v[156:159], v[116:119], v[192:195], v[156:159]\n\t"
            "buffer_store_dwordx4 v[144:147], v197, s[40:43], s45 offen sc1\n\t"
            "v_mfma_f32_16x16x32_bf16 v[164:167], v[140:143], v[192:195], v[164:167]\n\t"
            "ds_read_b128 v[192:195], v242 offset:12416\n\t"
            "v_cvt_pk_bf16_f32 v168, v168, v169\n\t"
            "s_waitcnt lgkmcnt(2)\n\t"
            "v_mfma_f32_16x16x32_bf16 v[152:155], v[0:3], v[184:187], v[152:155]\n\t"
            "v_cvt_pk_bf16_f32 v169, v170, v171\n\t"
            "v_cvt_pk_bf16_f32 v170, v172, v173\n\t"
            "v_mfma_f32_16x16x32_bf16 v[180:183], v[24:27], v[184:187], v[180:183]\n\t"
            "v_cvt_pk_bf16_f32 v171, v174, v175\n\t"
            "v_cvt_pk_bf16_f32 v156, v156, v157\n\t"
            "v_mfma_f32_16x16x32_bf16 v[160:163], v[48:51], v[184:187], v[160:163]\n\t"
            "buffer_store_dwordx4 v[168:171], v197, s[40:43], s45 offen offset:64 sc1\n\t"
            "v_mfma_f32_16x16x32_bf16 v[176:179], v[72:75], v[184:187], v[176:179]\n\t"
            "v_cvt_pk_bf16_f32 v157, v158, v159\n\t"
            "v_cvt_pk_bf16_f32 v158, v164, v165\n\t"
            "s_waitcnt lgkmcnt(1)\n\t"
            "v_mfma_f32_16x16x32_bf16 v[152:155], v[4:7], v[188:191], v[152:155]\n\t"
            "v_cvt_pk_bf16_f32 v159, v166, v167\n\t"
            "v_lshlrev_b32 v164, 16, v232\n\t"
            "v_mfma_f32_16x16x32_bf16 v[180:183], v[28:31], v[188:191], v[180:183]\n\t"
            "buffer_store_dwordx4 v[156:159], v197, s[40:43], s45 offen offset:128 sc1\n\t"
            "v_mfma_f32_16x16x32_bf16 v[160:163], v[52:55], v[188:191], v[160:163]\n\t"
            "v_and_b32 v165, 0xffff0000, v232\n\t"
            "v_lshlrev_b32 v166, 16, v233\n\t"
            "v_mfma_f32_16x16x32_bf16 v[176:179], v[76:79], v[188:191], v[176:179]\n\t"
            "v_and_b32 v167, 0xffff0000, v233\n\t"
            "v_lshlrev_b32 v156, 16, v234\n\t"
            "s_waitcnt lgkmcnt(0)\n\t"
            "v_mfma_f32_16x16x32_bf16 v[152:155], v[8:11], v[192:195], v[152:155]\n\t"
            "v_and_b32 v157, 0xffff0000, v234\n\t"
            "v_lshlrev_b32 v158, 16, v235\n\t"
            "v_mfma_f32_16x16x32_bf16 v[164:167], v[96:99], v[184:187], v[164:167]\n\t"
            "v_and_b32 v159, 0xffff0000, v235\n\t"
            "v_mfma_f32_16x16x32_bf16 v[164:167], v[100:103], v[188:191], v[164:167]\n\t"
            "s_nop 0\n\t"
            "v_mfma_f32_16x16x32_bf16 v[156:159], v[120:123], v[184:187], v[156:159]\n\t"
            "ds_read_b128 v[184:187], v243 offset:12416\n\t"
            "v_mfma_f32_16x16x32_bf16 v[156:159], v[124:127], v[188:191], v[156:159]\n\t"
            "ds_read_b128 v[188:191], v242 offset:12544\n\t"
            "v_mfma_f32_16x16x32_bf16 v[180:183], v[32:35], v[192:195], v[180:183]\n\t"
            "v_mfma_f32_16x16x32_bf16 v[160:163], v[56:59], v[192:195], v[160:163]\n\t"
            "v_mfma_f32_16x16x32_bf16 v[176:179], v[80:83], v[192:195], v[176:179]\n\t"
            "v_mfma_f32_16x16x32_bf16 v[164:167], v[104:107], v[192:195], v[164:167]\n\t"
            "v_mfma_f32_16x16x32_bf16 v[156:159], v[128:131], v[192:195], v[156:159]\n\t"
            "ds_read_b128 v[192:195], v243 offset:12544\n\t"
            "s_waitcnt lgkmcnt(2)\n\t"
            "v_mfma_f32_16x16x32_bf16 v[152:155], v[12:15], v[184:187], v[152:155]\n\t"
            "v_mfma_f32_16x16x32_bf16 v[180:183], v[36:39], v[184:187], v[180:183]\n\t"
            "s_waitcnt lgkmcnt(1)\n\t"
            "v_mfma_f32_16x16x32_bf16 v[152:155], v[16:19], v[188:191], v[152:155]\n\t"
            "v_mfma_f32_16x16x32_bf16 v[160:163], v[60:63], v[184:187], v[160:163]\n\t"
            "v_mfma_f32_16x16x32_bf16 v[180:183], v[40:43], v[188:191], v[180:183]\n\t"
            "v_mfma_f32_16x16x32_bf16 v[176:179], v[84:87], v[184:187], v[176:179]\n\t"
            "s_waitcnt lgkmcnt(0)\n\t"
            "v_mfma_f32_16x16x32_bf16 v[152:155], v[20:23], v[192:195], v[152:155]\n\t"
            "v_mfma_f32_16x16x32_bf16 v[160:163], v[64:67], v[188:191], v[160:163]\n\t"
            "v_mfma_f32_16x16x32_bf16 v[180:183], v[44:47], v[192:195], v[180:183]\n\t"
            "v_mfma_f32_16x16x32_bf16 v[164:167], v[108:111], v[184:187], v[164:167]\n\t"
            "v_mfma_f32_16x16x32_bf16 v[176:179], v[88:91], v[188:191], v[176:179]\n\t"
            "v_mfma_f32_16x16x32_bf16 v[156:159], v[132:135], v[184:187], v[156:159]\n\t"
            "v_mfma_f32_16x16x32_bf16 v[160:163], v[68:71], v[192:195], v[160:163]\n\t"
            "s_nop 1\n\t"
            "v_cvt_pk_bf16_f32 v152, v152, v153\n\t"
            "v_mfma_f32_16x16x32_bf16 v[164:167], v[112:115], v[188:191], v[164:167]\n\t"
            "v_cvt_pk_bf16_f32 v153, v154, v155\n\t"
            "v_mfma_f32_16x16x32_bf16 v[176:179], v[92:95], v[192:195], v[176:179]\n\t"
            "v_cvt_pk_bf16_f32 v154, v180, v181\n\t"
            "v_cvt_pk_bf16_f32 v155, v182, v183\n\t"
            "v_mfma_f32_16x16x32_bf16 v[156:159], v[136:139], v[188:191], v[156:159]\n\t"
            "buffer_store_dwordx4 v[152:155], v198, s[40:43], s45 offen sc1\n\t"
            "v_mfma_f32_16x16x32_bf16 v[164:167], v[116:119], v[192:195], v[164:167]\n\t"
            "v_cvt_pk_bf16_f32 v160, v160, v161\n\t"
            "v_cvt_pk_bf16_f32 v161, v162, v163\n\t"
            "v_mfma_f32_16x16x32_bf16 v[156:159], v[140:143], v[192:195], v[156:159]\n\t"
            "v_cvt_pk_bf16_f32 v162, v176, v177\n\t"
            "v_cvt_pk_bf16_f32 v163, v178, v179\n\t"
            "buffer_store_dwordx4 v[160:163], v198, s[40:43], s45 offen offset:64 sc1\n\t"
            "s_nop 1\n\t"
            "v_cvt_pk_bf16_f32 v164, v164, v165\n\t"
            "v_cvt_pk_bf16_f32 v165, v166, v167\n\t"
            "s_nop 0\n\t"
            "v_cvt_pk_bf16_f32 v166, v156, v157\n\t"
            "v_cvt_pk_bf16_f32 v167, v158, v159\n\t"
            "buffer_store_dwordx4 v[164:167], v198, s[40:43], s45 offen offset:128 sc1\n\t"
            "s_nop 7\n\t"
                    :: "{v[0:3]}"(W[0]), "{v[4:7]}"(W[1]), "{v[8:11]}"(W[2]), "{v[12:15]}"(W[3]), "{v[16:19]}"(W[4]), "{v[20:23]}"(W[5]), "{v[24:27]}"(W[6]), "{v[28:31]}"(W[7]), "{v[32:35]}"(W[8]), "{v[36:39]}"(W[9]), "{v[40:43]}"(W[10]), "{v[44:47]}"(W[11]), "{v[48:51]}"(W[12]), "{v[52:55]}"(W[13]), "{v[56:59]}"(W[14]), "{v[60:63]}"(W[15]), "{v[64:67]}"(W[16]), "{v[68:71]}"(W[17]), "{v[72:75]}"(W[18]), "{v[76:79]}"(W[19]), "{v[80:83]}"(W[20]), "{v[84:87]}"(W[21]), "{v[88:91]}"(W[22]), "{v[92:95]}"(W[23]), "{v[96:99]}"(W[24]), "{v[100:103]}"(W[25]), "{v[104:107]}"(W[26]), "{v[108:111]}"(W[27]), "{v[112:115]}"(W[28]), "{v[116:119]}"(W[29]), "{v[120:123]}"(W[30]), "{v[124:127]}"(W[31]), "{v[128:131]}"(W[32]), "{v[132:135]}"(W[33]), "{v[136:139]}"(W[34]), "{v[140:143]}"(W[35]), "{v[200:203]}"(xl[0]), "{v[204:207]}"(xl[1]), "{v[208:211]}"(xl[2]), "{v[212:215]}"(xl[3]), "{v[216:219]}"(xl[4]), "{v[220:223]}"(xl[5]), "{v[224:227]}"(xl[6]), "{v[228:231]}"(xl[7]), "{v[232:235]}"(xl[8]), "{v242}"(oE_), "{v243}"(oO_), "{v252}"(aCA_),
                       "{s[40:43]}"(orr), "{s45}"(t45), "{s56}"(t56), "{s55}"(CROW & 0x0F0F0F0Fu), "{s[34:35]}"(0x0001000100010001ull)
                    : "v144", "v145", "v146", "v147", "v148", "v149", "v150", "v151", "v152", "v153", "v154", "v155", "v156", "v157", "v158", "v159", "v160", "v161", "v162", "v163", "v164", "v165", "v166", "v167", "v168", "v169", "v170", "v171", "v172", "v173", "v174", "v175", "v176", "v177", "v178", "v179", "v180", "v181", "v182", "v183", "v184", "v185", "v186", "v187", "v188", "v189", "v190", "v191", "v192", "v193", "v194", "v195", "v196", "v197", "v198", "v199", "s36", "s37", "s57", "s58", "scc", "memory");
                }
                f32x4 acc[6];
#pragma unroll
                for (int ks = 0; ks < 6; ++ks) {
                    const bf16x8 of = *(const bf16x8*)(smem + ((ks & 1) ? (lE ^ 64) : lE) + (ks >> 1) * 128);
#pragma unroll
                    for (int j = 0; j < 6; ++j) acc[j] = MFMA32(W[j * 6 + ks], of, ks == 0 ? ZERO4 : acc[j]);
                }
#pragma unroll
                for (int k = 0; k < 3; ++k) {
                    const f32x4 a = acc[2 * k], b = acc[2 * k + 1];
                    const u32x4 ov = {pk2(a[0] + bflo(rl[k][0]), a[1] + bfhi(rl[k][0])), pk2(a[2] + bflo(rl[k][1]), a[3] + bfhi(rl[k][1])),
                                      pk2(b[0] + bflo(rl[k][2]), b[1] + bfhi(rl[k][2])), pk2(b[2] + bflo(rl[k][3]), b[3] + bfhi(rl[k][3]))};
                    __builtin_amdgcn_raw_buffer_store_b128(ov, orsrc, o + 64 * k, 0, 16);
                }
                SB();
            }
        }
        asm volatile("wk_code_end2:");

    }
}

extern "C" void kernel_launch(void* const* d_in, const int* in_sizes, int n_in,
                              void* d_out, int out_size, void* d_ws, size_t ws_size,
                              hipStream_t stream) {
    const unsigned short* x = (const unsigned short*)d_in[0];
    const unsigned short* dw = (const unsigned short*)d_in[1];
    const unsigned short* wqkv = (const unsigned short*)d_in[2];
    const unsigned short* wo = (const unsigned short*)d_in[3];
    unsigned short* out = (unsigned short*)d_out;
    u32x4* wf = (u32x4*)d_ws;
    win_attn_kernel<<<GRID, 512, 0, stream>>>(x, dw, wqkv, wo, wf, out);
}
```

```cpp
#include <hip/hip_runtime.h>
#include <hip/hip_bf16.h>
#include <stdint.h>

typedef short bf16x8 __attribute__((ext_vector_type(8)));
typedef float f32x4 __attribute__((ext_vector_type(4)));
typedef float f32x2 __attribute__((ext_vector_type(2)));
typedef unsigned u32x2 __attribute__((ext_vector_type(2)));
typedef unsigned u32x4 __attribute__((ext_vector_type(4)));

constexpr int IMG = 511;
constexpr int CH = 192;
constexpr int NWX = 73;
constexpr int NWIN = NWX * NWX;
constexpr int GRID = 256;
constexpr int NSHORT = 21 * GRID - NWIN;
constexpr int ROWBYTES = IMG * CH * 2;
constexpr int XBIAS = (IMG + 1) * CH * 2;
constexpr unsigned OOB = 0x40000000u;

constexpr int X_OFF = 32768;
constexpr int X1_OFF = 53632;
constexpr int O_OFF = 74496;
constexpr int V_OFF = 92928;
constexpr int KQL_OFF = 111360;
constexpr int OL_OFF = 128256;
constexpr int AT_OFF = 137472;
constexpr int CT_OFF = AT_OFF + 19584;
constexpr int RS_OFF = CT_OFF + 4096;
constexpr int WL_OFF = RS_OFF + 2304 + 64;
constexpr int LDS_BYTES = WL_OFF + 128;
#define RS_BASE (RS_OFF + (wave < 4 ? wave * 384 : 768 + wave * 192))
constexpr int TP_OFF = X1_OFF + 8192;
constexpr int WA = 12, WB = 4;
constexpr int STL_OFF = O_OFF + 12288;
#define LB_OFF(b) ((b) < 7 ? V_OFF + (b) * 6160 : X1_OFF + ((b) - 7) * 6160)
#define LPOS(wc) (((wc) & 1) ? 0 : 6)
constexpr unsigned CROW = 0x060a0230u;
constexpr unsigned CLANE = 0x00132C3Fu;

constexpr unsigned long long TOKMAP[6] = {0x20001c190f2a2202ull, 0x052517121f061a13ull, 0x181e08151d0b260cull, 0x1b2b2903102d0901ull, 0x04162e0e2824142cull, 0x110a072127232f0dull};
constexpr unsigned long long ROWMAP[8] = {0x08321b240f1c0006ull, 0x23310e2a09170d14ull, 0x210c2f030a273418ull, 0x1d2b11221f043739ull, 0x3234363a103e192dull, 0x291a20022c3f1605ull, 0x303a3c2628122e15ull, 0x39253d1e01070b13ull};

constexpr int WF_BYTES = 8 * 36 * 1024;
constexpr int EP_OFF = WF_BYTES;
constexpr int CN_OFF = EP_OFF + 256, FL_OFF = EP_OFF + 512;
constexpr int SH_OFF = EP_OFF + 1024;
constexpr int WS_BYTES = SH_OFF + GRID * 12288;
constexpr int GO_OFF = WL_OFF + 124;
constexpr int PG_OFF = WL_OFF + 96;
constexpr bool PF1 = false;
constexpr int SPIN_MAX = 1 << 20;

#define MFMA32(a, b, c) __builtin_amdgcn_mfma_f32_16x16x32_bf16((a), (b), (c), 0, 0, 0)
#define BC8(v) __builtin_bit_cast(bf16x8, (v))
#define BAR() asm volatile("s_waitcnt lgkmcnt(0)\n\ts_barrier" ::: "memory")
#define SB() __builtin_amdgcn_sched_barrier(0)
#define OPQ(x) asm volatile("" : "+v"(x))
#define ZERO4 ((f32x4){0.f, 0.f, 0.f, 0.f})

typedef __bf16 nbf16x2 __attribute__((ext_vector_type(2)));
__device__ __forceinline__ unsigned pk2(float a, float b) {
    f32x2 v = {a, b};
    nbf16x2 r = __builtin_convertvector(v, nbf16x2);
    return __builtin_bit_cast(unsigned, r);
}
__device__ __forceinline__ float bflo(unsigned u) { return __uint_as_float(u << 16); }
__device__ __forceinline__ float bfhi(unsigned u) { return __uint_as_float(u & 0xffff0000u); }

__device__ __forceinline__ float bf2f(unsigned short v) { return __uint_as_float((unsigned)v << 16); }
__host__ __device__ __forceinline__ constexpr unsigned byte_of(unsigned long long a, unsigned long long b, int i) {
    return (unsigned)(((i & 8) ? b : a) >> (8 * (i & 7))) & 0xFFu;
}
__host__ __device__ __forceinline__ constexpr void seq_to_win(int seq, int& wy, int& wx) {
    const bool last = seq >= 5110;
    const int s = last ? 35 : seq / 146, r = seq - 146 * s;
    const int kx = last ? r / 6 : r >> 2, pos = r - (last ? 6 : 4) * kx;
    const bool low = last && pos >= (kx == 36 ? 2 : 4);
    wy = low ? 72 : 2 * s + (pos & 1);
    wx = 2 * kx + (low ? (kx == 36 ? 0 : pos - 4) : pos >> 1);
}
__host__ __device__ __forceinline__ constexpr int win_to_seq(int wy, int wx) {
    const int kx = wx >> 1;
    return wy == 72 ? 5110 + 6 * kx + (kx == 36 ? 2 : 4 + (wx & 1)) : 146 * (wy >> 1) + (wy >= 70 ? 6 : 4) * kx + 2 * (wx & 1) + (wy & 1);
}
__host__ __device__ __forceinline__ constexpr int block_of(int wy, int wx) { return wy >= 70 ? 1295 + (wx & ~1) + (wy == 72) : 37 * (wy >> 1) + (wx >> 1); }
__host__ __device__ __forceinline__ constexpr void block_pos(int b, int& ky, int& kx) {
    if (b >= 1295) { kx = (b - 1295) >> 1; ky = 70 + 2 * ((b - 1295) & 1); }
    else { const int s = b / 37; kx = b - 37 * s; ky = 2 * s; }
}
__host__ __device__ __forceinline__ constexpr unsigned wl_entry(int seq) {
    int wy = 0, wx = 0;
    seq_to_win(seq, wy, wx);
    return (unsigned)((wy * 7 * IMG + wx * 7) * (CH * 2)) | (wy == 0 ? 0x08000000u : 0u) | (wx == 0 ? 0x10000000u : 0u) |
           (wx == NWX - 1 ? 0x20000000u : 0u) | ((unsigned)(wx & 1) << 30) | ((unsigned)(wy & 1) << 31);
}
__host__ __device__ __forceinline__ constexpr unsigned dma_off(int lane) {
    const int col = (lane * 43) >> 8;
    return (unsigned)(col * (CH * 2 - 96) + 16 * lane) | (col == 0 ? 0x10000000u : 0u) | (col == 8 ? 0x20000000u : 0u);
}

__host__ __device__ __forceinline__ constexpr unsigned pf_off(int lane) {
    const int l = lane < 63 ? lane : 62, r = l / 21;
    return (unsigned)(r * ROWBYTES + (l - 21 * r) * 128) | (r == 0 ? 0x08000000u : 0u);
}
constexpr int RECN = 80;
struct Tables { unsigned rec[GRID][RECN]; unsigned img[768]; };
constexpr Tables make_tables() {
    Tables t = {};
    for (int bx = 0; bx < GRID; ++bx) {
        unsigned* r = t.rec[bx];
        const int q0 = (bx & 7) * 32 + (bx >> 3), cnt = q0 < NSHORT ? 20 : 21, start = q0 < NSHORT ? 20 * q0 : 21 * q0 - NSHORT;
        for (int i = 0; i < 32; ++i) r[i] = i < cnt ? wl_entry(start + i) : 0x7ffffffu;
        int wy = 0, wx = 0;
        seq_to_win(start, wy, wx);
        const int key0 = block_of(wy, wx);
        seq_to_win(start + cnt - 1, wy, wx);
        const int nblk = block_of(wy, wx) - key0 + 1;
        for (int b = 0; b < 8; ++b) {
            int ky = 0, kx = 0;
            block_pos(key0 + b, ky, kx);
            r[64 + b] = b < nblk ? (unsigned)(((7 * ky + 5) * IMG + 14 * kx + 5) * (CH * 2) + XBIAS) | (kx == 36 ? 1u : 0u) : OOB;
            for (int c = 0; c < 4; ++c) {
                const int y = ky + (c >> 1), x = 2 * kx + (c & 1), i = win_to_seq(y, x) - start;
                const bool ok = b < nblk && y < NWX && x < NWX && i >= 0 && i < cnt;
                r[32 + 4 * b + c] = ok ? (unsigned)(((7 * y + LPOS(y)) * IMG + 7 * x + LPOS(x)) * (CH * 2) + i) : OOB + 47;
            }
        }
        r[72] = q0; r[73] = cnt;
    }
    for (int lane = 0; lane < 64; ++lane) {
        for (int e = 0; e < 3; ++e) {
            const int tk = byte_of(TOKMAP[2 * e], TOKMAP[2 * e + 1], lane & 15);
            t.img[192 + 4 * lane + e] = (unsigned)((tk / 7) * ROWBYTES + (tk % 7) * (CH * 2)) + 16 * (lane >> 4);
        }
        t.img[448 + 4 * lane] = dma_off(lane);
        t.img[449 + 4 * lane] = pf_off(lane);
    }
    return t;
}
static __constant__ const Tables TAB = make_tables();
typedef __attribute__((address_space(1))) unsigned gu32;
typedef __attribute__((address_space(1))) unsigned long long gu64;
#define RLX_AGENT __ATOMIC_RELAXED, __HIP_MEMORY_SCOPE_AGENT
#define TAG_OF(e) (((unsigned long long)~((e) + 1u) << 32) | ((e) + 1u))

__device__ __forceinline__ float rstd_of(const char* p) {
    f32x4 p0 = *(const f32x4*)(p);
    f32x4 p1 = *(const f32x4*)(p + 1024);
    float s = (p0[0] + p0[2]) + (p1[0] + p1[2]);
    float q = (p0[1] + p0[3]) + (p1[1] + p1[3]);
    float mean = s * (1.f / 192.f);
    float var = __builtin_fmaf(q, 1.f / 192.f, -mean * mean);
    var = __builtin_fmaxf(var, 0.f);
    return __builtin_amdgcn_rsqf(var + 1e-5f);
}

#if defined(__HIP_DEVICE_COMPILE__)
#define NOPK __attribute__((target("no-packed-fp32-ops")))
#else
#define NOPK
#endif
__global__ __launch_bounds__(512, 2) NOPK void win_attn_kernel(const unsigned short* __restrict__ x, const unsigned short* __restrict__ dw,
                                                          const unsigned short* __restrict__ wqkv, const unsigned short* __restrict__ wo,
                                                          u32x4* wf, unsigned short* __restrict__ out) {
    __shared__ __attribute__((aligned(16))) char smem[LDS_BYTES];

    asm volatile("wk_code_begin:");
    const int tid = threadIdx.x;
    const int wave = __builtin_amdgcn_readfirstlane(tid >> 6);
    const int w = wave & 3;
    const unsigned ldsb = (unsigned)(size_t)smem;
#define GET_LANE(l) int l; asm volatile("v_mbcnt_lo_u32_b32 %0, -1, 0\n\tv_mbcnt_hi_u32_b32 %0, -1, %0" : "=v"(l)); __builtin_assume(l >= 0 && l < 64);
#define ROLE_SETUP                                                                                 \
    int bx = blockIdx.x;                                                                           \
    asm volatile("" : "+s"(bx));                                                                   \
    bx = __builtin_amdgcn_readfirstlane(bx);                                                       \
    const unsigned* rec = TAB.rec[bx];                                                             \
    const unsigned en0 = rec[0], en1 = rec[1];                                                     \
    const int q0 = rec[72], cnt = rec[73];                                                         \
    const auto wrsrc = __builtin_amdgcn_make_buffer_rsrc((void*)wf, 0, WS_BYTES, 0x00020000);      \
    const auto xrsrc = __builtin_amdgcn_make_buffer_rsrc((void*)((const char*)x - XBIAS), 0, IMG * IMG * CH * 2 + XBIAS, 0x00020000);
#define DMA_SLICE(en)                                                                              \
        {                                                                                          \
            const unsigned vo_ = dma_off(lane) & (0x401fffffu | ((en) & 0x38000000u));             \
            unsigned so_ = ((en) & 0x7ffffffu) + 96 * w, n_ = 8;                                   \
            asm volatile("s_mov_b32 m0, %2\n\ts_mov_b32 exec_hi, 0x3fffff\n\tbuffer_load_dwordx4 %3, %4, %5 offen lds\n"     \
                         ".Lds%=:\n\ts_add_u32 m0, m0, 872\n\ts_add_u32 %0, %0, %6\n\ts_sub_u32 %1, %1, 1\n\t"            \
                         "buffer_load_dwordx4 %3, %4, %0 offen lds\n\ts_cmp_lg_u32 %1, 0\n\ts_cbranch_scc1 .Lds%=\n\ts_mov_b32 exec_hi, -1" \
                         : "+s"(so_), "+s"(n_) : "s"(ldsb + w * 8192), "v"(vo_), "s"(xrsrc), "s"(((en) & 0x08000000u) ? OOB : so_), "n"(ROWBYTES) \
                         : "m0", "scc", "memory");                                                 \
        }
    asm volatile(
        "s_load_dwordx8 s[24:31], %[ka], 0x0\n\t"
        "s_load_dwordx2 s[32:33], %[ka], 0x20\n\t"
        "s_mul_i32 s36, %[bx], %[recb]\n\t"
        "s_xor_b32 s37, %[wave], 4\n\t"
        "s_lshl2_add_u32 s38, s37, s36\n\t"
        "s_load_dword s38, %[tab], s38 offset:0x100\n\t"
        "s_load_dword s39, %[tab], s36\n\t"
        "v_mbcnt_lo_u32_b32 v1, -1, 0\n\t"
        "v_mbcnt_hi_u32_b32 v1, -1, v1\n\t"
        "s_and_b32 s44, %[bx], 7\n\t"
        "s_lshl_b32 s44, s44, 5\n\t"
        "s_lshr_b32 s45, %[bx], 3\n\t"
        "s_add_u32 s44, s44, s45\n\t"
        "v_lshlrev_b32 v2, 4, v1\n\t"
        "s_mov_b32 s43, 0x20000\n\t"
        "s_cmp_ge_u32 s44, 36\n\t"
        "s_cbranch_scc1 .Lnp%=\n\t"
        "s_mul_i32 s45, s44, 6\n\t"
        "s_lshl1_add_u32 s46, s44, 210\n\t"
        "s_cmp_ge_u32 %[wave], 6\n\t"
        "s_cselect_b64 vcc, -1, 0\n\t"
        "s_cselect_b32 s45, s46, s45\n\t"
        "s_movk_i32 s52, 1152\n\t"
        "s_cselect_b32 s52, 384, s52\n\t"
        "s_cselect_b32 s53, 0, 0xbbaaaaab\n\t"
        "s_add_u32 s45, s45, %[wave]\n\t"
        "s_mul_i32 s46, s45, 171\n\t"
        "s_lshr_b32 s46, s46, 10\n\t"
        "s_mul_i32 s47, s46, 6\n\t"
        "s_sub_u32 s47, s45, s47\n\t"
        "s_mul_i32 s48, s46, 43\n\t"
        "s_lshr_b32 s48, s48, 8\n\t"
        "s_mul_i32 s49, s48, 6\n\t"
        "s_sub_u32 s49, s46, s49\n\t"
        "s_mul_i32 s50, s48, 48\n\t"
        "s_cmp_lt_u32 s49, 3\n\t"
        "s_movk_i32 s51, -48\n\t"
        "s_cselect_b32 s51, 192, s51\n\t"
        "s_cmp_lt_u32 s48, 4\n\t"
        "s_cselect_b32 s51, s51, s50\n\t"
        "s_add_u32 s51, s51, s50\n\t"
        "s_lshl4_add_u32 s51, s49, s51\n\t"
        "s_and_b32 s49, s49, 1\n\t"
        "s_mul_i32 s49, s49, 12\n\t"
        "s_add_u32 s49, s49, 576\n\t"
        "s_cmp_ge_u32 %[wave], 6\n\t"
        "s_cselect_b32 s49, s49, 0\n\t"
        "s_sub_u32 s51, s51, s49\n\t"
        "s_and_b32 s49, s47, 1\n\t"
        "s_mul_i32 s49, s49, 88\n\t"
        "s_lshl3_add_u32 s47, s47, s49\n\t"
        "v_lshrrev_b32 v3, 5, v1\n\t"
        "v_mul_u32_u24 v3, 48, v3\n\t"
        "v_lshrrev_b32 v4, 1, v1\n\t"
        "v_and_or_b32 v3, v4, 8, v3\n\t"
        "v_add_u32 v3, s47, v3\n\t"
        "v_mul_u32_u24 v3, s52, v3\n\t"
        "v_and_b32 v4, 15, v1\n\t"
        "v_and_b32 v5, 12, v1\n\t"
        "v_cndmask_b32 v5, 0, v5, vcc\n\t"
        "v_add3_u32 v5, v4, v5, s51\n\t"
        "v_lshl_add_u32 v3, v5, 1, v3\n\t"
        "v_mov_b64 v[8:9], 0\n\t"
        "v_mov_b64 v[10:11], 0\n\t"
        "v_mov_b64 v[12:13], 0\n\t"
        "v_mov_b64 v[14:15], 0\n\t"
        "s_mul_i32 s42, s52, 192\n\t"
        "s_cmp_ge_u32 %[wave], 6\n\t"
        "s_waitcnt lgkmcnt(0)\n\t"
        "s_cselect_b64 s[40:41], s[30:31], s[28:29]\n\t"
        "s_and_b32 s41, s41, 0xffff\n\t"
        "buffer_load_short_d16_hi v8, v3, s[40:43], 0 offen\n\t"
        "buffer_load_short_d16_hi v9, v3, s[40:43], s52 offen\n\t"
        "s_lshl_b32 s46, s52, 1\n\t"
        "buffer_load_short_d16_hi v10, v3, s[40:43], s46 offen\n\t"
        "s_add_u32 s46, s46, s52\n\t"
        "buffer_load_short_d16_hi v11, v3, s[40:43], s46 offen\n\t"
        "s_add_u32 s46, s46, s52\n\t"
        "buffer_load_short_d16_hi v12, v3, s[40:43], s46 offen\n\t"
        "s_add_u32 s46, s46, s52\n\t"
        "buffer_load_short_d16_hi v13, v3, s[40:43], s46 offen\n\t"
        "s_add_u32 s46, s46, s52\n\t"
        "buffer_load_short_d16_hi v14, v3, s[40:43], s46 offen\n\t"
        "s_add_u32 s46, s46, s52\n\t"
        "buffer_load_short_d16_hi v15, v3, s[40:43], s46 offen\n\t"
        "v_mov_b32 v6, %[ep]\n\t"
        "global_load_dword v7, v6, s[32:33] sc1\n\t"
        "v_mov_b32 v5, %[red]\n\t"
        "v_mad_u32_u24 v4, v4, %[c144], v5\n\t"
        "v_lshrrev_b32 v5, 2, v1\n\t"
        "v_and_b32 v5, 12, v5\n\t"
        "s_lshl_b32 s46, %[wave], 4\n\t"
        "v_add3_u32 v5, v4, v5, s46\n\t"
        "s_mov_b32 s40, s32\n\t"
        "s_and_b32 s41, s33, 0xffff\n\t"
        "s_mov_b32 s42, %[wsb]\n\t"
        "s_lshl_b32 s45, s45, 10\n\t"
        "s_waitcnt vmcnt(1)\n\t"
        "v_add_f32 v16, v8, v10\n\t"
        "v_add_f32 v17, v9, v11\n\t"
        "v_add_f32 v18, v12, v14\n\t"
        "v_add_f32 v19, v13, v15\n\t"
        "v_add_f32 v16, v16, v18\n\t"
        "v_add_f32 v17, v17, v19\n\t"
        "v_add_f32 v16, v16, v17\n\t"
        "ds_write_b32 v5, v16\n\t"
        "s_waitcnt lgkmcnt(0)\n\t"
        "s_barrier\n\t"
        "ds_read_b128 v[16:19], v4\n\t"
        "ds_read_b128 v[20:23], v4 offset:16\n\t"
        "ds_read_b128 v[24:27], v4 offset:32\n\t"
        "ds_read_b128 v[28:31], v4 offset:48\n\t"
        "ds_read_b128 v[32:35], v4 offset:64\n\t"
        "ds_read_b128 v[36:39], v4 offset:80\n\t"
        "s_waitcnt lgkmcnt(4)\n\t"
        "v_add_f32 v16, v16, v18\n\t"
        "v_add_f32 v17, v17, v19\n\t"
        "v_add_f32 v20, v20, v22\n\t"
        "v_add_f32 v21, v21, v23\n\t"
        "s_waitcnt lgkmcnt(2)\n\t"
        "v_add_f32 v24, v24, v26\n\t"
        "v_add_f32 v25, v25, v27\n\t"
        "v_add_f32 v28, v28, v30\n\t"
        "v_add_f32 v29, v29, v31\n\t"
        "v_add_f32 v16, v16, v20\n\t"
        "v_add_f32 v17, v17, v21\n\t"
        "s_waitcnt lgkmcnt(0)\n\t"
        "v_add_f32 v32, v32, v34\n\t"
        "v_add_f32 v33, v33, v35\n\t"
        "v_add_f32 v36, v36, v38\n\t"
        "v_add_f32 v37, v37, v39\n\t"
        "v_add_f32 v24, v24, v28\n\t"
        "v_add_f32 v25, v25, v29\n\t"
        "v_add_f32 v32, v32, v36\n\t"
        "v_add_f32 v33, v33, v37\n\t"
        "v_add_f32 v16, v16, v24\n\t"
        "v_add_f32 v17, v17, v25\n\t"
        "v_add_f32 v16, v16, v32\n\t"
        "v_add_f32 v17, v17, v33\n\t"
        "v_add_f32 v16, v16, v17\n\t"
        "v_mul_f32 v16, s53, v16\n\t"
        "v_mov_b32 v17, v16\n\t"
        "v_add_f32 v8, v8, v16\n\t"
        "v_add_f32 v9, v9, v17\n\t"
        "v_add_f32 v10, v10, v16\n\t"
        "v_add_f32 v11, v11, v17\n\t"
        "v_add_f32 v12, v12, v16\n\t"
        "v_add_f32 v13, v13, v17\n\t"
        "v_add_f32 v14, v14, v16\n\t"
        "v_add_f32 v15, v15, v17\n\t"
        "v_cvt_pk_bf16_f32 v8, v8, v9\n\t"
        "v_cvt_pk_bf16_f32 v9, v10, v11\n\t"
        "v_cvt_pk_bf16_f32 v10, v12, v13\n\t"
        "v_cvt_pk_bf16_f32 v11, v14, v15\n\t"
        "buffer_store_dwordx4 v[8:11], v2, s[40:43], s45 offen sc1\n\t"
        "s_or_b32 s46, s44, s37\n\t"
        "s_cmp_lg_u32 s46, 0\n\t"
        "s_cbranch_scc1 .Lnc%=\n\t"
        "v_mov_b32 v3, 0\n\t"
        "global_store_dword v6, v3, s[32:33] offset:256 sc1\n\t"
        ".Lnc%=:\n\t"
        "v_lshl_add_u32 v6, s44, 3, v6\n\t"
        "s_waitcnt vmcnt(0)\n\t"
        "s_barrier\n\t"
        "s_cmp_lg_u32 %[wave], 0\n\t"
        "s_cbranch_scc1 .Lnp%=\n\t"
        "v_add_u32 v4, 1, v7\n\t"
        "v_not_b32 v5, v4\n\t"
        "s_mov_b64 exec, 1\n\t"
        "global_store_dwordx2 v6, v[4:5], s[32:33] offset:512 sc1\n\t"
        "s_mov_b64 exec, -1\n\t"
        ".Lnp%=:\n\t"
        "s_getpc_b64 s[40:41]\n\t"
        ".Lwk_here%=:\n\t"
        "s_sub_u32 s40, s40, .Lwk_here%=-wk_code_begin\n\t"
        "s_subb_u32 s41, s41, 0\n\t"
        "s_and_b32 s41, s41, 0xffff\n\t"
        "s_mov_b32 s42, wk_code_end-wk_code_begin\n\t"
        "s_max_u32 s42, s42, wk_code_end1-wk_code_begin\n\t"
        "s_max_u32 s42, s42, wk_code_end2-wk_code_begin\n\t"
        "s_add_u32 m0, %[lds], %[sink]\n\t"
        "v_lshlrev_b32 v3, 7, v1\n\t"
        "s_lshl_b32 s45, %[wave], 13\n\t"
        "buffer_load_dword v3, s[40:43], s45 offen lds\n\t"
        "s_mov_b64 s[40:41], %[tab]\n\t"
        "s_and_b32 s41, s41, 0xffff\n\t"
        "s_mov_b32 s42, %[tabb]\n\t"
        "s_lshl_b32 s46, %[wave], 10\n\t"
        "s_add_u32 s47, s46, %[imgs]\n\t"
        "s_add_u32 s48, s46, %[imgd]\n\t"
        "s_lshl_b32 s49, %[wave], 8\n\t"
        "s_add_u32 s49, s49, %[mapd]\n\t"
        "s_add_u32 s50, s36, 128\n\t"
        "s_mov_b64 vcc, -1\n\t"
        "s_cmp_eq_u32 %[wave], 0\n\t"
        "s_cselect_b32 s47, s36, s47\n\t"
        "s_cselect_b32 s48, %[wld], s48\n\t"
        "s_cselect_b64 vcc, 0xff, vcc\n\t"
        "s_cmp_ge_u32 %[wave], 4\n\t"
        "s_cselect_b32 s47, s50, s47\n\t"
        "s_cselect_b32 s48, s49, s48\n\t"
        "s_cselect_b64 exec, 0xff, vcc\n\t"
        "s_add_u32 m0, %[lds], s48\n\t"
        "s_and_b32 s49, %[wave], 3\n\t"
        "buffer_load_dwordx4 v2, s[40:43], s47 offen lds\n\t"
        "s_mov_b64 exec, -1\n\t"
        "v_mul_u32_u24 v3, 43, v1\n\t"
        "v_lshrrev_b32 v3, 8, v3\n\t"
        "v_mad_u32_u24 v4, v3, %[c288], v2\n\t"
        "s_mul_i32 s49, s49, 96\n\t"
        "s_mul_i32 s50, s37, 6160\n\t"
        "s_add_u32 s50, s50, %[lb0]\n\t"
        "s_cmp_eq_u32 s37, 7\n\t"
        "s_cselect_b32 s50, %[lb7], s50\n\t"
        "s_add_u32 s46, s46, %[tp]\n\t"
        "s_waitcnt lgkmcnt(0)\n\t"
        "s_cmp_ge_u32 %[wave], 4\n\t"
        "s_cbranch_scc1 .Lib%=\n\t"
        "s_mov_b32 s40, s26\n\t"
        "s_and_b32 s41, s27, 0xffff\n\t"
        "s_movk_i32 s42, 3456\n\t"
        "s_add_u32 m0, %[lds], s46\n\t"
        "s_nop 0\n\t"
        "buffer_load_dwordx4 v4, s[40:43], s49 offen lds\n\t"
        ".Lib%=:\n\t"
        "s_sub_u32 s40, s24, %[xb]\n\t"
        "s_subb_u32 s41, s25, 0\n\t"
        "s_and_b32 s41, s41, 0xffff\n\t"
        "s_mov_b32 s42, %[xsz]\n\t"
        "s_cmp_eq_u32 s38, 2.0\n\t"
        "s_cbranch_scc1 .Lic%=\n\t"
        "s_mov_b32 exec_hi, 0xffff\n\t"
        "s_add_u32 m0, %[lds], s50\n\t"
        "s_mov_b32 s46, 4\n\t"
        "s_bitcmp1_b32 s38, 0\n\t"
        "s_cselect_b32 s47, 2.0, s38\n\t"
        "s_bitset0_b32 s38, 0\n\t"
        ".Lid%=:\n\t"
        "buffer_load_dwordx4 v2, s[40:43], s38 offen lds\n\t"
        "buffer_load_dwordx4 v2, s[40:43], s47 offen offset:768 lds\n\t"
        "s_add_u32 m0, m0, 1536\n\t"
        "s_add_u32 s38, s38, %[rb]\n\t"
        "s_add_u32 s47, s47, %[rb]\n\t"
        "s_sub_u32 s46, s46, 1\n\t"
        "s_cmp_lg_u32 s46, 0\n\t"
        "s_cbranch_scc1 .Lid%=\n\t"
        ".Lic%=:\n\t"
        "s_cmp_lt_u32 %[wave], 4\n\t"
        "s_cbranch_scc1 .Lie%=\n\t"
        "s_mov_b32 exec_hi, 0x3fffff\n\t"
        "s_and_b32 s46, s39, 0x10000000\n\t"
        "s_and_b32 s47, s39, 0x20000000\n\t"
        "v_cmp_eq_u32 vcc, 0, v3\n\t"
        "v_mov_b32 v5, s46\n\t"
        "v_mov_b32 v6, s47\n\t"
        "v_cndmask_b32 v5, 0, v5, vcc\n\t"
        "v_cmp_eq_u32 vcc, 8, v3\n\t"
        "s_and_b32 s46, s39, 0x7ffffff\n\t"
        "s_add_u32 s46, s46, s49\n\t"
        "v_cndmask_b32 v6, 0, v6, vcc\n\t"
        "v_or3_b32 v4, v4, v5, v6\n\t"
        "s_bitcmp1_b32 s39, 27\n\t"
        "s_cselect_b32 s47, 2.0, s46\n\t"
        "s_lshl_b32 s45, s37, 13\n\t"
        "s_add_u32 m0, %[lds], s45\n\t"
        "s_mov_b32 s48, 8\n\t"
        "buffer_load_dwordx4 v4, s[40:43], s47 offen lds\n\t"
        ".Lia%=:\n\t"
        "s_add_u32 m0, m0, 872\n\t"
        "s_add_u32 s46, s46, %[rb]\n\t"
        "s_sub_u32 s48, s48, 1\n\t"
        "buffer_load_dwordx4 v4, s[40:43], s46 offen lds\n\t"
        "s_cmp_lg_u32 s48, 0\n\t"
        "s_cbranch_scc1 .Lia%=\n\t"
        ".Lie%=:\n\t"
        "s_mov_b64 exec, -1\n\t"
        :: [ka] "s"(__builtin_amdgcn_kernarg_segment_ptr()), [bx] "s"(blockIdx.x), [wave] "s"(wave), [tab] "s"(&TAB), [lds] "s"(ldsb),
           [red] "s"(ldsb + O_OFF), [c144] "s"(144), [c288] "s"(288), [recb] "n"(RECN * 4), [ep] "n"(EP_OFF), [wsb] "n"(WS_BYTES), [sink] "n"(X_OFF + 18432),
           [tabb] "n"(sizeof(Tables)), [imgs] "n"(GRID * RECN * 4 - 1024), [imgd] "n"(AT_OFF + 18816 - 1024), [mapd] "n"(TP_OFF + 3072), [wld] "n"(WL_OFF),
           [lb0] "n"(LB_OFF(0)), [lb7] "n"(LB_OFF(7)), [tp] "n"(TP_OFF), [xb] "n"(XBIAS), [xsz] "n"(IMG * IMG * CH * 2 + XBIAS), [rb] "n"(ROWBYTES)
        : "v1", "v2", "v3", "v4", "v5", "v6", "v7", "v8", "v9", "v10", "v11", "v12", "v13", "v14", "v15", "v16", "v17", "v18", "v19", "v20", "v21", "v22", "v23", "v24", "v25", "v26", "v27", "v28", "v29", "v30", "v31", "v32", "v33", "v34", "v35", "v36", "v37", "v38", "v39", "s24", "s25", "s26", "s27", "s28", "s29", "s30", "s31", "s32", "s33", "s36", "s37", "s38", "s39", "s40", "s41", "s42", "s43", "s44", "s45", "s46", "s47", "s48", "s49", "s50", "s51", "s52", "s53", "m0", "vcc", "scc", "memory");
#define WAIT_GO                                                                                    \
        {                                                                                          \
            unsigned go_;                                                                          \
            asm volatile(".Lgo%=:\n\tds_read_b32 %0, %1\n\ts_waitcnt lgkmcnt(0)\n\tv_cmp_ne_u32 vcc, -1, %0\n\ts_nop 4\n\ts_cbranch_vccnz .Lgo%=" \
                         : "=&v"(go_) : "v"(ldsb + GO_OFF) : "vcc", "memory");                     \
        }
    bf16x8 W[36];
#define LOAD_W(j0, j1, k0, k1)                                                                     \
    _Pragma("unroll") for (int ks_ = (k0); ks_ < (k1); ++ks_)                                      \
        _Pragma("unroll") for (int j_ = (j0); j_ < (j1); ++j_)                                     \
            W[j_ * 6 + ks_] = BC8(__builtin_amdgcn_raw_buffer_load_b128(wrsrc, lane * 16, wave * 36864 + (j_ * 6 + ks_) * 1024, 16));

#define LOAD_WI(i0, i1)                                                                            \
    _Pragma("unroll") for (int i_ = (i0); i_ < (i1); ++i_)                                         \
        W[i_] = BC8(__builtin_amdgcn_raw_buffer_load_b128(wrsrc, lane * 16, wave * 36864 + i_ * 1024, 16));

#define LOAD_WL(i0, i1)                                                                            \
    _Pragma("unroll") for (int i_ = (i0); i_ < (i1); ++i_)                                         \
        W[i_] = BC8(__builtin_amdgcn_raw_buffer_load_b128(wrsrc, lane * 16, wl_ + i_ * 1024, 16));

#define LANE_SETUP                                                                                 \
    int lane;                                                                                      \
    asm volatile("v_mbcnt_lo_u32_b32 %0, -1, 0\n\tv_mbcnt_hi_u32_b32 %0, -1, %0" : "=v"(lane));    \
    __builtin_assume(lane >= 0 && lane < 64);                                                      \
    const int g = lane >> 4, l15 = lane & 15, sw = l15 >> 1;                                       \
    int fE = X_OFF + l15 * 384 + ((g ^ sw) << 4);                                                  \
    int fO = fE ^ 64;                                                                              \
    OPQ(fE); OPQ(fO);
#define FRAG(base, t, ks) (*(const bf16x8*)(smem + (((ks) & 1) ? fO : fE) + ((base) - X_OFF) + (t) * 6144 + ((ks) >> 1) * 128))
#define RSTD_PUBLISH(ln_, stoff)                                                                   \
    *(float*)(smem + ((ln_) < 48 ? RS_BASE : RS_OFF + 2304 - 192) + (ln_) * 4) = rstd_of(smem + (stoff) + (ln_) * 16);

#define XPAT(r) (((r) < 48 ? (r) : 48) * 384 + (((2 * w) ^ (((r) >> 1) & 7)) << 4))
#define SPAT(r) ((w >> 1) * 1024 + (w & 1) * 8 + (r) * 16)
#define CONV_CONSTS(var) \
            const unsigned long long rlo = g == 0 ? ROWMAP[0] : (g == 1 ? ROWMAP[2] : (g == 2 ? ROWMAP[4] : ROWMAP[6])); \
            const unsigned long long rhi = g == 0 ? ROWMAP[1] : (g == 1 ? ROWMAP[3] : (g == 2 ? ROWMAP[5] : ROWMAP[7])); \
            int xrow = byte_of(rlo, rhi, l15); \
            if (lane == (int)((CLANE >> (8 * (var))) & 0xFF)) xrow = 48; \
            if (lane == 63) xrow = (CROW >> (8 * (var))) & 0xFF; \
            const unsigned pXw_ = XPAT(xrow); \
            const unsigned pSTw_ = (w >> 1) * 1024 + (w & 1) * 8 + xrow * 16; \
            const int oy = (0x5320 >> (4 * (l15 >> 2))) & 15, ox = (0x5320 >> (4 * (l15 & 3))) & 15; \
            const unsigned aB_ = ldsb + w * 8192 + (oy + (g >> 1)) * 872 + (ox + 2 * (g & 1)) * 96; \
            const int dyi = g - (l15 >> 3); \
            const unsigned aA_ = ldsb + AT_OFF + (l15 & 3) * 16 + \
                                 ((dyi < 0 || dyi > 2) ? 18816 : (dyi == 0 ? 0 : (dyi == 1 ? 6208 : 12544)) + ((l15 >> 2) & 1) * 3072 + w * 768); \
            const unsigned idx_ = (l15 & 3) * 0x5555u; \
            const unsigned se = (l15 == 4 * g) ? 0x3F803F80u : 0u; \
            const u32x4 sel4 = {se, se, se, se};
#define PIN_CONSTS asm volatile("" :: "v"(pXw_), "v"(pSTw_), "v"(aB_), "v"(aA_), "v"(idx_), "v"(se));

    if (wave < 4) {
        ROLE_SETUP
        LANE_SETUP
        asm volatile("s_waitcnt vmcnt(0)" ::: "memory");
        if (PF1) {
            const int li = w * 64 + lane, r = (li * 2428) >> 16;
            const unsigned vo = (r > 8 || (r == 0 && (en1 & 0x08000000u))) ? OOB : r * ROWBYTES + (li - 27 * r) * 128;
            asm volatile("s_mov_b32 m0, %0\n\ts_nop 0\n\tbuffer_load_dword %1, %2, %3 offen lds"
                         :: "s"(ldsb + X_OFF + 18432), "v"(vo), "s"(xrsrc), "s"(en1 & 0x7ffffffu) : "m0", "memory");
        }
        if (lane < 48) {
            unsigned tp[9];
#pragma unroll
            for (int k = 0; k < 9; ++k) tp[k] = *(const unsigned short*)(smem + TP_OFF + w * 1024 + k * 96 + lane * 2);
            tp[4] |= 0x3F800000u;
#pragma unroll
            for (int dy = 0; dy < 3; ++dy) {
                char* p = smem + AT_OFF + (dy == 0 ? 0 : (dy == 1 ? 6208 : 12544)) + w * 768 + lane * 16;
                *(u32x4*)p = (u32x4){tp[3 * dy], tp[3 * dy + 1], tp[3 * dy + 2], 0u};
                *(u32x4*)(p + 3072) = (u32x4){0u, tp[3 * dy], tp[3 * dy + 1], tp[3 * dy + 2]};
            }
        }
        BAR();
        {
            const int dW = X_OFF + l15 * 384 + (((2 * w + (g >> 1)) ^ sw) << 4) + 8 * (g & 1);
            const unsigned rsb = ldsb + RS_BASE;
            const unsigned pE_ = fE - X_OFF, aO_ = ldsb + dW + (O_OFF - X_OFF), sel_ = (l15 & 3) ? 0u : 1u, aVB_ = ldsb + V_OFF + w * 4608 + 1024 + lane * 8;
            const unsigned pST_ = lane * 16, aRSw_ = rsb + lane * 4, aPLw_ = rsb + lane * 2, aRSr_ = rsb + l15 * 4;
            const unsigned g16_ = (l15 & 3) ? ldsb + AT_OFF + 18816 : g * 16, g8_ = (l15 & 3) ? ldsb + AT_OFF + 18816 : g * 8;
            const unsigned loVL_ = ((2 * w + (l15 >> 3)) << 4) + (l15 & 7) * 2, loOL_ = ((2 * w + (g >> 1)) << 4) + 8 * (g & 1);
            const unsigned on = (l15 & 3) ? 0u : 0x3F803F80u;
            const u32x4 ones4 = {on, on, on, on};
            const unsigned snk = ldsb + O_OFF + 15360 + w * 192, kr = ldsb + KQL_OFF + l15 * 800;
            const bool ok1 = 16 + l15 < cnt, dg = g == (l15 >> 2);
            u32x4 pl = {kr + w * 192 + g * 16, (ok1 ? kr + 12800 + w * 192 : snk) + g * 16, dg ? kr + 768 + w * 4 : snk, dg && ok1 ? kr + 12800 + 768 + w * 4 : snk};
            unsigned pcl, pch, len;
            asm volatile("s_getpc_b64 s[36:37]\n.Lwk_h2%=:\n\ts_sub_u32 s36, s36, .Lwk_h2%=-wk_code_begin\n\ts_subb_u32 s37, s37, 0\n\t"
                         "s_mov_b32 %2, wk_code_end-wk_code_begin\n\ts_max_u32 %2, %2, wk_code_end1-wk_code_begin" : "={s36}"(pcl), "={s37}"(pch), "=s"(len) :: "scc");
            pcl = __builtin_amdgcn_readfirstlane(pcl); pch = __builtin_amdgcn_readfirstlane(pch); len = __builtin_amdgcn_readfirstlane(len);
            const unsigned long long tb = w == 0 ? ((unsigned long long)pch << 32) | pcl : (unsigned long long)wf;
            const u32x4 tr = {(unsigned)tb, (unsigned)(tb >> 32) & 0xffffu, w == 0 ? len : (w == 1 ? (unsigned)WF_BYTES : SH_OFF + bx * 12288 + 12288), 0x00020000u};
            const unsigned to = w == 0 ? ((bx >> 3) % 6) * 8192 : (w == 1 ? 6 * 36864 + ((bx >> 3) % 9) * 8192 : SH_OFF + bx * 12288 + (w - 2) * 8192);
            asm volatile("" :: "{v244}"(pE_), "{v245}"(aO_), "{v246}"(sel_), "{v247}"(aVB_), "{v248}"(pST_), "{v249}"(aRSw_), "{v250}"(aPLw_), "{v251}"(aRSr_), "{v252}"(g16_), "{v253}"(g8_), "{v254}"(loVL_), "{v255}"(loOL_), "{v[240:243]}"(ones4), "{v[232:235]}"(pl), "{s[36:39]}"(tr), "{s41}"(to));
            if (w == 0) {
                gu64* fp = (gu64*)((char*)wf + (lane < 36 ? FL_OFF + lane * 8 : EP_OFF));
#pragma unroll 1
                for (int it = 0; it < SPIN_MAX; ++it) {
                    const unsigned long long f = __hip_atomic_load(fp, RLX_AGENT);
                    const unsigned ep = __builtin_amdgcn_readlane((unsigned)f, 63);
                    if ((__builtin_amdgcn_ballot_w64(f != TAG_OF(ep)) & 0xfffffffffull) == 0ull) break;
                    __builtin_amdgcn_s_sleep(1);
                }
                __builtin_amdgcn_fence(__ATOMIC_ACQUIRE, "wavefront");
                asm volatile("ds_write_b32 %0, %1" :: "v"(ldsb + GO_OFF), "v"(~0u) : "memory");
            } else WAIT_GO
            __builtin_amdgcn_s_setprio(2);
            unsigned s44 = cnt, s48 = ldsb + X_OFF, s70 = 0;
            unsigned s72 = ldsb + KQL_OFF + w * 192, s73 = ldsb + OL_OFF, s74 = ldsb + KQL_OFF + 784, s77 = ldsb + OL_OFF, s76 = ldsb + KQL_OFF + 768 + w * 4;
            unsigned long long s68 = 0;
            const unsigned long long wb = (unsigned long long)wf;
            const u32x4 wr = {(unsigned)wb, (unsigned)(wb >> 32) & 0xffffu, (unsigned)WS_BYTES, 0x00020000u};
            asm volatile(
            "s_setprio 2\n\t"
            "s_add_u32 s49, s33, 0\n\t"
            "buffer_load_dwordx4 v[0:3], v248, s[28:31], s49 offen sc1\n\t"
            "s_add_u32 s49, s33, 1024\n\t"
            "buffer_load_dwordx4 v[4:7], v248, s[28:31], s49 offen sc1\n\t"
            "s_add_u32 s49, s33, 2048\n\t"
            "buffer_load_dwordx4 v[8:11], v248, s[28:31], s49 offen sc1\n\t"
            "s_add_u32 s49, s33, 3072\n\t"
            "buffer_load_dwordx4 v[12:15], v248, s[28:31], s49 offen sc1\n\t"
            "s_add_u32 s49, s33, 4096\n\t"
            "buffer_load_dwordx4 v[16:19], v248, s[28:31], s49 offen sc1\n\t"
            "s_add_u32 s49, s33, 5120\n\t"
            "buffer_load_dwordx4 v[20:23], v248, s[28:31], s49 offen sc1\n\t"
            "s_add_u32 s49, s33, 6144\n\t"
            "buffer_load_dwordx4 v[24:27], v248, s[28:31], s49 offen sc1\n\t"
            "s_add_u32 s49, s33, 7168\n\t"
            "buffer_load_dwordx4 v[28:31], v248, s[28:31], s49 offen sc1\n\t"
            "s_add_u32 s49, s33, 8192\n\t"
            "buffer_load_dwordx4 v[32:35], v248, s[28:31], s49 offen sc1\n\t"
            "s_add_u32 s49, s33, 9216\n\t"
            "buffer_load_dwordx4 v[36:39], v248, s[28:31], s49 offen sc1\n\t"
            "s_add_u32 s49, s33, 10240\n\t"
            "buffer_load_dwordx4 v[40:43], v248, s[28:31], s49 offen sc1\n\t"
            "s_add_u32 s49, s33, 11264\n\t"
            "buffer_load_dwordx4 v[44:47], v248, s[28:31], s49 offen sc1\n\t"
            "s_add_u32 s49, s33, 12288\n\t"
            "buffer_load_dwordx4 v[48:51], v248, s[28:31], s49 offen sc1\n\t"
            "s_add_u32 s49, s33, 13312\n\t"
            "buffer_load_dwordx4 v[52:55], v248, s[28:31], s49 offen sc1\n\t"
            "s_add_u32 s49, s33, 14336\n\t"
            "buffer_load_dwordx4 v[56:59], v248, s[28:31], s49 offen sc1\n\t"
            "s_add_u32 s49, s33, 15360\n\t"
            "buffer_load_dwordx4 v[60:63], v248, s[28:31], s49 offen sc1\n\t"
            "v_mov_b32 v224, s46\n\t"
            ".Ls1%=:\n\t"
            "ds_read_b128 v[228:231], v224\n\t"
            "s_waitcnt lgkmcnt(0)\n\t"
            "v_and_b32 v228, v228, v229\n\t"
            "v_and_b32 v230, v230, v231\n\t"
            "v_and_b32 v228, v228, v230\n\t"
            "s_nop 0\n\t"
            "v_readfirstlane_b32 s49, v228\n\t"
            "s_nop 3\n\t"
            "s_bitcmp1_b32 s49, 27\n\t"
            "s_cbranch_scc0 .Ls1%=\n\t"
            "s_nop 7\n\t"
            "v_add_u32 v237, s35, v248\n\t"
            "v_add_u32 v238, s56, v244\n\t"
            "v_xor_b32 v239, 64, v238\n\t"
            "v_add_u32 v236, s56, v248\n\t"
            "ds_read_b128 v[200:203], v237\n\t"
            "ds_read_b128 v[204:207], v237 offset:1024\n\t"
            "ds_read_b128 v[144:147], v238\n\t"
            "ds_read_b128 v[148:151], v239\n\t"
            "ds_read_b128 v[152:155], v238 offset:128\n\t"
            "ds_read_b128 v[156:159], v239 offset:128\n\t"
            "ds_read_b128 v[160:163], v238 offset:256\n\t"
            "ds_read_b128 v[164:167], v239 offset:256\n\t"
            "ds_read_b128 v[168:171], v238 offset:6144\n\t"
            "ds_read_b128 v[172:175], v239 offset:6144\n\t"
            "ds_read_b128 v[176:179], v238 offset:6272\n\t"
            "ds_read_b128 v[180:183], v239 offset:6272\n\t"
            "ds_read_b128 v[184:187], v238 offset:6400\n\t"
            "ds_read_b128 v[188:191], v239 offset:6400\n\t"
            "ds_read_b128 v[208:211], v237 offset:2048\n\t"
            "s_waitcnt lgkmcnt(14)\n\t"
            "ds_read_b128 v[224:227], v236 offset:12288\n\t"
            "s_waitcnt lgkmcnt(14)\n\t"
            "ds_read_b128 v[228:231], v236 offset:13312\n\t"
            "s_add_u32 s49, s33, 16384\n\t"
            "buffer_load_dwordx4 v[64:67], v248, s[28:31], s49 offen sc1\n\t"
            "s_add_u32 s49, s33, 17408\n\t"
            "buffer_load_dwordx4 v[68:71], v248, s[28:31], s49 offen sc1\n\t"
            "s_waitcnt vmcnt(12)\n\t"
            "s_waitcnt lgkmcnt(14)\n\t"
            "v_mfma_f32_16x16x32_bf16 v[192:195], v[0:3], v[144:147], 0\n\t"
            "buffer_store_dwordx4 v[200:203], v248, s[28:31], s34 offen\n\t"
            "s_waitcnt lgkmcnt(8)\n\t"
            "v_mfma_f32_16x16x32_bf16 v[212:215], v[0:3], v[168:171], 0\n\t"
            "buffer_store_dwordx4 v[204:207], v248, s[28:31], s34 offen offset:1024\n\t"
            "v_mfma_f32_16x16x32_bf16 v[192:195], v[4:7], v[148:151], v[192:195]\n\t"
            "s_waitcnt lgkmcnt(2)\n\t"
            "buffer_store_dwordx4 v[208:211], v248, s[28:31], s34 offen offset:2048\n\t"
            "v_mfma_f32_16x16x32_bf16 v[212:215], v[4:7], v[172:175], v[212:215]\n\t"
            "s_add_u32 s49, s33, 18432\n\t"
            "buffer_load_dwordx4 v[72:75], v248, s[28:31], s49 offen sc1\n\t"
            "v_mfma_f32_16x16x32_bf16 v[192:195], v[8:11], v[152:155], v[192:195]\n\t"
            "s_add_u32 s49, s33, 19456\n\t"
            "buffer_load_dwordx4 v[76:79], v248, s[28:31], s49 offen sc1\n\t"
            "v_mfma_f32_16x16x32_bf16 v[212:215], v[8:11], v[176:179], v[212:215]\n\t"
            "s_add_u32 s49, s33, 20480\n\t"
            "buffer_load_dwordx4 v[80:83], v248, s[28:31], s49 offen sc1\n\t"
            "v_mfma_f32_16x16x32_bf16 v[192:195], v[12:15], v[156:159], v[192:195]\n\t"
            "s_add_u32 s49, s33, 21504\n\t"
            "buffer_load_dwordx4 v[84:87], v248, s[28:31], s49 offen sc1\n\t"
            "v_mfma_f32_16x16x32_bf16 v[212:215], v[12:15], v[180:183], v[212:215]\n\t"
            "s_add_u32 s49, s33, 22528\n\t"
            "buffer_load_dwordx4 v[88:91], v248, s[28:31], s49 offen sc1\n\t"
            "v_mfma_f32_16x16x32_bf16 v[192:195], v[16:19], v[160:163], v[192:195]\n\t"
            "s_add_u32 s49, s33, 23552\n\t"
            "buffer_load_dwordx4 v[92:95], v248, s[28:31], s49 offen sc1\n\t"
            "v_mfma_f32_16x16x32_bf16 v[212:215], v[16:19], v[184:187], v[212:215]\n\t"
            "s_waitcnt lgkmcnt(1)\n\t"
            "v_add_f32 v224, v224, v226\n\t"
            "s_waitcnt lgkmcnt(0)\n\t"
            "v_add_f32 v228, v228, v230\n\t"
            "v_mfma_f32_16x16x32_bf16 v[192:195], v[20:23], v[164:167], v[192:195]\n\t"
            "v_add_f32 v224, v224, v228\n\t"
            "v_add_f32 v225, v225, v227\n\t"
            "v_mfma_f32_16x16x32_bf16 v[212:215], v[20:23], v[188:191], v[212:215]\n\t"
            "s_waitcnt vmcnt(15)\n\t"
            "s_add_u32 s49, s33, 24576\n\t"
            "buffer_load_dwordx4 v[96:99], v248, s[28:31], s49 offen sc1\n\t"
            "v_mfma_f32_16x16x32_bf16 v[196:199], v[24:27], v[144:147], 0\n\t"
            "s_add_u32 s49, s33, 25600\n\t"
            "buffer_load_dwordx4 v[100:103], v248, s[28:31], s49 offen sc1\n\t"
            "v_mfma_f32_16x16x32_bf16 v[216:219], v[24:27], v[168:171], 0\n\t"
            "s_add_u32 s49, s33, 26624\n\t"
            "buffer_load_dwordx4 v[104:107], v248, s[28:31], s49 offen sc1\n\t"
            "v_mfma_f32_16x16x32_bf16 v[196:199], v[28:31], v[148:151], v[196:199]\n\t"
            "s_add_u32 s49, s33, 27648\n\t"
            "buffer_load_dwordx4 v[108:111], v248, s[28:31], s49 offen sc1\n\t"
            "v_mfma_f32_16x16x32_bf16 v[216:219], v[28:31], v[172:175], v[216:219]\n\t"
            "s_add_u32 s49, s33, 28672\n\t"
            "buffer_load_dwordx4 v[112:115], v248, s[28:31], s49 offen sc1\n\t"
            "v_mfma_f32_16x16x32_bf16 v[196:199], v[32:35], v[152:155], v[196:199]\n\t"
            "s_add_u32 s49, s33, 29696\n\t"
            "buffer_load_dwordx4 v[116:119], v248, s[28:31], s49 offen sc1\n\t"
            "v_mfma_f32_16x16x32_bf16 v[216:219], v[32:35], v[176:179], v[216:219]\n\t"
            "v_add_f32 v229, v229, v231\n\t"
            "v_mul_f32 v224, 0x3baaaaab, v224\n\t"
            "v_mfma_f32_16x16x32_bf16 v[196:199], v[36:39], v[156:159], v[196:199]\n\t"
            "v_add_f32 v225, v225, v229\n\t"
            "v_mul_f32_e64 v224, v224, -v224\n\t"
            "v_mfma_f32_16x16x32_bf16 v[216:219], v[36:39], v[180:183], v[216:219]\n\t"
            "v_fmac_f32 v224, 0x3baaaaab, v225\n\t"
            "v_max_f32 v224, 0, v224\n\t"
            "v_mfma_f32_16x16x32_bf16 v[196:199], v[40:43], v[160:163], v[196:199]\n\t"
            "v_add_f32 v224, 0x3727c5ac, v224\n\t"
            "v_rsq_f32 v224, v224\n\t"
            "v_mfma_f32_16x16x32_bf16 v[216:219], v[40:43], v[184:187], v[216:219]\n\t"
            "ds_write_b32 v249, v224\n\t"
            "v_mfma_f32_16x16x32_bf16 v[196:199], v[44:47], v[164:167], v[196:199]\n\t"
            "ds_read_b32 v236, v251\n\t"
            "ds_read_b32 v237, v251 offset:64\n\t"
            "v_mfma_f32_16x16x32_bf16 v[216:219], v[44:47], v[188:191], v[216:219]\n\t"
            "s_waitcnt vmcnt(15)\n\t"
            "s_add_u32 s49, s33, 30720\n\t"
            "buffer_load_dwordx4 v[120:123], v248, s[28:31], s49 offen sc1\n\t"
            "v_mfma_f32_16x16x32_bf16 v[200:203], v[48:51], v[144:147], 0\n\t"
            "s_add_u32 s49, s33, 31744\n\t"
            "buffer_load_dwordx4 v[124:127], v248, s[28:31], s49 offen sc1\n\t"
            "v_mfma_f32_16x16x32_bf16 v[220:223], v[48:51], v[168:171], 0\n\t"
            "s_add_u32 s49, s33, 32768\n\t"
            "buffer_load_dwordx4 v[128:131], v248, s[28:31], s49 offen sc1\n\t"
            "v_mfma_f32_16x16x32_bf16 v[200:203], v[52:55], v[148:151], v[200:203]\n\t"
            "s_add_u32 s49, s33, 33792\n\t"
            "buffer_load_dwordx4 v[132:135], v248, s[28:31], s49 offen sc1\n\t"
            "v_mfma_f32_16x16x32_bf16 v[220:223], v[52:55], v[172:175], v[220:223]\n\t"
            "s_add_u32 s49, s33, 34816\n\t"
            "buffer_load_dwordx4 v[136:139], v248, s[28:31], s49 offen sc1\n\t"
            "v_mfma_f32_16x16x32_bf16 v[200:203], v[56:59], v[152:155], v[200:203]\n\t"
            "s_add_u32 s49, s33, 35840\n\t"
            "buffer_load_dwordx4 v[140:143], v248, s[28:31], s49 offen sc1\n\t"
            "s_setprio 0\n\t"
            "v_mfma_f32_16x16x32_bf16 v[220:223], v[56:59], v[176:179], v[220:223]\n\t"
            "s_waitcnt lgkmcnt(1)\n\t"
            "v_mul_f32 v192, v236, v192\n\t"
            "v_mul_f32 v193, v236, v193\n\t"
            "v_mfma_f32_16x16x32_bf16 v[200:203], v[60:63], v[156:159], v[200:203]\n\t"
            "s_waitcnt lgkmcnt(0)\n\t"
            "v_mul_f32 v212, v237, v212\n\t"
            "v_mul_f32 v213, v237, v213\n\t"
            "v_mfma_f32_16x16x32_bf16 v[220:223], v[60:63], v[180:183], v[220:223]\n\t"
            "v_mul_f32 v194, v236, v194\n\t"
            "v_mul_f32 v195, v236, v195\n\t"
            "v_mfma_f32_16x16x32_bf16 v[200:203], v[64:67], v[160:163], v[200:203]\n\t"
            "v_cvt_pk_bf16_f32 v192, v192, v193\n\t"
            "v_mul_f32 v214, v237, v214\n\t"
            "v_mfma_f32_16x16x32_bf16 v[220:223], v[64:67], v[184:187], v[220:223]\n\t"
            "v_mul_f32 v215, v237, v215\n\t"
            "v_cvt_pk_bf16_f32 v212, v212, v213\n\t"
            "v_mfma_f32_16x16x32_bf16 v[200:203], v[68:71], v[164:167], v[200:203]\n\t"
            "v_cvt_pk_bf16_f32 v193, v194, v195\n\t"
            "v_cvt_pk_bf16_f32 v213, v214, v215\n\t"
            "v_mfma_f32_16x16x32_bf16 v[220:223], v[68:71], v[188:191], v[220:223]\n\t"
            "s_waitcnt vmcnt(12)\n\t"
            "v_mul_f32 v196, v236, v196\n\t"
            "v_mfma_f32_16x16x32_bf16 v[204:207], v[72:75], v[144:147], 0\n\t"
            "v_mul_f32 v197, v236, v197\n\t"
            "v_mul_f32 v198, v236, v198\n\t"
            "v_mfma_f32_16x16x32_bf16 v[224:227], v[72:75], v[168:171], 0\n\t"
            "v_mul_f32 v199, v236, v199\n\t"
            "v_mul_f32 v216, v237, v216\n\t"
            "v_mfma_f32_16x16x32_bf16 v[204:207], v[76:79], v[148:151], v[204:207]\n\t"
            "v_mul_f32 v217, v237, v217\n\t"
            "v_mul_f32 v218, v237, v218\n\t"
            "v_mfma_f32_16x16x32_bf16 v[224:227], v[76:79], v[172:175], v[224:227]\n\t"
            "v_mul_f32 v219, v237, v219\n\t"
            "v_cvt_pk_bf16_f32 v194, v196, v197\n\t"
            "v_mfma_f32_16x16x32_bf16 v[204:207], v[80:83], v[152:155], v[204:207]\n\t"
            "v_cvt_pk_bf16_f32 v195, v198, v199\n\t"
            "v_cvt_pk_bf16_f32 v214, v216, v217\n\t"
            "v_mfma_f32_16x16x32_bf16 v[224:227], v[80:83], v[176:179], v[224:227]\n\t"
            "v_cvt_pk_bf16_f32 v215, v218, v219\n\t"
            "v_mul_f32 v200, v236, v200\n\t"
            "v_mfma_f32_16x16x32_bf16 v[204:207], v[84:87], v[156:159], v[204:207]\n\t"
            "v_mul_f32 v201, v236, v201\n\t"
            "v_mul_f32 v220, v237, v220\n\t"
            "v_mfma_f32_16x16x32_bf16 v[224:227], v[84:87], v[180:183], v[224:227]\n\t"
            "v_mul_f32 v221, v237, v221\n\t"
            "v_mul_f32 v202, v236, v202\n\t"
            "v_mfma_f32_16x16x32_bf16 v[204:207], v[88:91], v[160:163], v[204:207]\n\t"
            "v_mul_f32 v203, v236, v203\n\t"
            "v_cvt_pk_bf16_f32 v200, v200, v201\n\t"
            "v_mfma_f32_16x16x32_bf16 v[224:227], v[88:91], v[184:187], v[224:227]\n\t"
            "v_mul_f32 v222, v237, v222\n\t"
            "v_mul_f32 v223, v237, v223\n\t"
            "v_mfma_f32_16x16x32_bf16 v[204:207], v[92:95], v[164:167], v[204:207]\n\t"
            "v_cvt_pk_bf16_f32 v220, v220, v221\n\t"
            "v_cvt_pk_bf16_f32 v201, v202, v203\n\t"
            "v_mfma_f32_16x16x32_bf16 v[224:227], v[92:95], v[188:191], v[224:227]\n\t"
            "s_waitcnt vmcnt(6)\n\t"
            "v_cvt_pk_bf16_f32 v221, v222, v223\n\t"
            "v_mfma_f32_16x16x32_bf16 v[196:199], v[96:99], v[144:147], 0\n\t"
            "ds_write_b128 v232, v[192:195]\n\t"
            "v_mfma_f32_16x16x32_bf16 v[216:219], v[96:99], v[168:171], 0\n\t"
            "v_cvt_pk_bf16_f32 v204, v204, v205\n\t"
            "v_cvt_pk_bf16_f32 v205, v206, v207\n\t"
            "v_mfma_f32_16x16x32_bf16 v[196:199], v[100:103], v[148:151], v[196:199]\n\t"
            "v_cvt_pk_bf16_f32 v224, v224, v225\n\t"
            "v_cvt_pk_bf16_f32 v225, v226, v227\n\t"
            "v_mfma_f32_16x16x32_bf16 v[216:219], v[100:103], v[172:175], v[216:219]\n\t"
            "ds_write_b128 v233, v[212:215]\n\t"
            "v_mfma_f32_16x16x32_bf16 v[196:199], v[104:107], v[152:155], v[196:199]\n\t"
            "v_mul_f32 v236, 0x3e553b94, v236\n\t"
            "v_mul_f32 v237, 0x3e553b94, v237\n\t"
            "v_mfma_f32_16x16x32_bf16 v[216:219], v[104:107], v[176:179], v[216:219]\n\t"
            "v_mfma_f32_16x16x32_bf16 v[196:199], v[108:111], v[156:159], v[196:199]\n\t"
            "v_mfma_f32_16x16x32_bf16 v[216:219], v[108:111], v[180:183], v[216:219]\n\t"
            "v_mfma_f32_16x16x32_bf16 v[196:199], v[112:115], v[160:163], v[196:199]\n\t"
            "v_mfma_f32_16x16x32_bf16 v[216:219], v[112:115], v[184:187], v[216:219]\n\t"
            "v_mfma_f32_16x16x32_bf16 v[196:199], v[116:119], v[164:167], v[196:199]\n\t"
            "v_mfma_f32_16x16x32_bf16 v[216:219], v[116:119], v[188:191], v[216:219]\n\t"
            "s_waitcnt vmcnt(0)\n\t"
            "v_mfma_f32_16x16x32_bf16 v[208:211], v[120:123], v[144:147], 0\n\t"
            "v_mfma_f32_16x16x32_bf16 v[228:231], v[120:123], v[168:171], 0\n\t"
            "v_mfma_f32_16x16x32_bf16 v[208:211], v[124:127], v[148:151], v[208:211]\n\t"
            "v_mfma_f32_16x16x32_bf16 v[228:231], v[124:127], v[172:175], v[228:231]\n\t"
            "s_nop 1\n\t"
            "v_cvt_pk_bf16_f32 v206, v196, v197\n\t"
            "v_mfma_f32_16x16x32_bf16 v[208:211], v[128:131], v[152:155], v[208:211]\n\t"
            "v_cvt_pk_bf16_f32 v207, v198, v199\n\t"
            "v_cvt_pk_bf16_f32 v226, v216, v217\n\t"
            "v_mfma_f32_16x16x32_bf16 v[228:231], v[128:131], v[176:179], v[228:231]\n\t"
            "v_cvt_pk_bf16_f32 v227, v218, v219\n\t"
            "v_mfma_f32_16x16x32_bf16 v[208:211], v[132:135], v[156:159], v[208:211]\n\t"
            "ds_write_b128 v232, v[204:207] offset:64\n\t"
            "v_mfma_f32_16x16x32_bf16 v[228:231], v[132:135], v[180:183], v[228:231]\n\t"
            "ds_write_b128 v233, v[224:227] offset:64\n\t"
            "v_mfma_f32_16x16x32_bf16 v[208:211], v[136:139], v[160:163], v[208:211]\n\t"
            "v_mfma_f32_16x16x32_bf16 v[228:231], v[136:139], v[184:187], v[228:231]\n\t"
            "v_mfma_f32_16x16x32_bf16 v[208:211], v[140:143], v[164:167], v[208:211]\n\t"
            "v_mfma_f32_16x16x32_bf16 v[228:231], v[140:143], v[188:191], v[228:231]\n\t"
            "s_nop 6\n\t"
            "v_cvt_pk_bf16_f32 v202, v208, v209\n\t"
            "v_cvt_pk_bf16_f32 v203, v210, v211\n\t"
            "s_nop 1\n\t"
            "v_mfma_f32_16x16x16_bf16 v[196:199], v[200:201], v[202:203], 0\n\t"
            "v_lshrrev_b32 v208, 5, v248\n\t"
            "v_and_b32 v208, 24, v208\n\t"
            "v_sub_u32 v208, v232, v208\n\t"
            "v_cvt_pk_bf16_f32 v222, v228, v229\n\t"
            "v_cvt_pk_bf16_f32 v223, v230, v231\n\t"
            "v_lshrrev_b32 v228, 5, v248\n\t"
            "v_mfma_f32_16x16x32_bf16 v[196:199], v[192:195], v[204:207], v[196:199]\n\t"
            "v_and_b32 v228, 24, v228\n\t"
            "v_sub_u32 v228, v233, v228\n\t"
            "v_mfma_f32_16x16x16_bf16 v[216:219], v[220:221], v[222:223], 0\n\t"
            "ds_write_b64 v208, v[200:201] offset:128\n\t"
            "ds_write_b64 v228, v[220:221] offset:128\n\t"
            "ds_write_b64 v208, v[202:203] offset:160\n\t"
            "ds_write_b64 v228, v[222:223] offset:160\n\t"
            "s_nop 0\n\t"
            "v_cndmask_b32_e64 v196, v196, v197, s[50:51]\n\t"
            "v_cndmask_b32_e64 v196, v196, v198, s[52:53]\n\t"
            "v_mfma_f32_16x16x32_bf16 v[216:219], v[212:215], v[224:227], v[216:219]\n\t"
            "v_cndmask_b32_e64 v196, v196, v199, s[54:55]\n\t"
            "ds_write_b32 v234, v196\n\t"
            "v_add_u32 v234, s57, v234\n\t"
            "ds_write_b32 v234, v236\n\t"
            "s_nop 3\n\t"
            "v_cndmask_b32_e64 v216, v216, v217, s[50:51]\n\t"
            "v_cndmask_b32_e64 v216, v216, v218, s[52:53]\n\t"
            "v_cndmask_b32_e64 v216, v216, v219, s[54:55]\n\t"
            "ds_write_b32 v235, v216\n\t"
            "v_add_u32 v235, s57, v235\n\t"
            "ds_write_b32 v235, v237\n\t"
            "s_waitcnt lgkmcnt(0)\n\t"
            "s_nop 7\n\t"
            "s_cmp_lg_u32 s33, 0\n\t"
            "s_cbranch_scc1 .Lar%=\n\t"
            "s_mov_b64 exec, 1\n\t"
            "v_mov_b32 v220, 1\n\t"
            "s_mov_b32 s49, 295168\n\t"
            "buffer_atomic_add v220, off, s[28:31], s49\n\t"
            "s_mov_b64 exec, -1\n\t"
            ".Lar%=:\n\t"
            "v_mov_b32 v224, s46\n\t"
            ".Ls2%=:\n\t"
            "ds_read_b128 v[228:231], v224\n\t"
            "s_waitcnt lgkmcnt(0)\n\t"
            "v_and_b32 v228, v228, v229\n\t"
            "v_and_b32 v230, v230, v231\n\t"
            "v_and_b32 v228, v228, v230\n\t"
            "s_nop 0\n\t"
            "v_readfirstlane_b32 s49, v228\n\t"
            "s_nop 3\n\t"
            "s_bitcmp1_b32 s49, 31\n\t"
            "s_cbranch_scc0 .Ls2%=\n\t"
            "s_waitcnt lgkmcnt(0)\n\t"
            "s_nop 7\n\t"
            "s_setprio 2\n\t"
            "v_add_u32 v232, s48, v244\n\t"
            "v_xor_b32 v233, 64, v232\n\t"
            "v_add_u32 v235, s48, v248\n\t"
            "ds_read_b128 v[204:207], v232\n\t"
            "ds_read_b128 v[208:211], v233\n\t"
            "ds_read_b128 v[212:215], v232 offset:128\n\t"
            "ds_read_b128 v[228:231], v235 offset:18816\n\t"
            "s_waitcnt lgkmcnt(3)\n\t"
            "v_mfma_f32_16x16x32_bf16 v[144:147], v[0:3], v[204:207], 0\n\t"
            "s_waitcnt lgkmcnt(0)\n\t"
            "v_add_f32 v236, v228, v230\n\t"
            "v_mfma_f32_16x16x32_bf16 v[148:151], v[24:27], v[204:207], 0\n\t"
            "v_add_f32 v237, v229, v231\n\t"
            "v_mfma_f32_16x16x32_bf16 v[152:155], v[48:51], v[204:207], 0\n\t"
            "ds_read_b128 v[228:231], v235 offset:19840\n\t"
            "v_mfma_f32_16x16x32_bf16 v[156:159], v[72:75], v[204:207], 0\n\t"
            "v_mfma_f32_16x16x32_bf16 v[160:163], v[96:99], v[204:207], 0\n\t"
            "v_mfma_f32_16x16x32_bf16 v[164:167], v[120:123], v[204:207], 0\n\t"
            "ds_read_b128 v[204:207], v233 offset:128\n\t"
            "v_mfma_f32_16x16x32_bf16 v[144:147], v[4:7], v[208:211], v[144:147]\n\t"
            "v_mfma_f32_16x16x32_bf16 v[148:151], v[28:31], v[208:211], v[148:151]\n\t"
            "v_mfma_f32_16x16x32_bf16 v[152:155], v[52:55], v[208:211], v[152:155]\n\t"
            "v_mfma_f32_16x16x32_bf16 v[156:159], v[76:79], v[208:211], v[156:159]\n\t"
            "v_mfma_f32_16x16x32_bf16 v[160:163], v[100:103], v[208:211], v[160:163]\n\t"
            "s_waitcnt lgkmcnt(1)\n\t"
            "v_add_f32 v228, v228, v230\n\t"
            "v_add_f32 v236, v236, v228\n\t"
            "v_mfma_f32_16x16x32_bf16 v[164:167], v[124:127], v[208:211], v[164:167]\n\t"
            "ds_read_b128 v[208:211], v232 offset:256\n\t"
            "v_add_f32 v229, v229, v231\n\t"
            "v_mfma_f32_16x16x32_bf16 v[144:147], v[8:11], v[212:215], v[144:147]\n\t"
            "v_mul_f32 v236, 0x3baaaaab, v236\n\t"
            "v_add_f32 v237, v237, v229\n\t"
            "v_mfma_f32_16x16x32_bf16 v[148:151], v[32:35], v[212:215], v[148:151]\n\t"
            "v_mul_f32_e64 v236, v236, -v236\n\t"
            "v_fmac_f32 v236, 0x3baaaaab, v237\n\t"
            "v_mfma_f32_16x16x32_bf16 v[152:155], v[56:59], v[212:215], v[152:155]\n\t"
            "v_max_f32 v236, 0, v236\n\t"
            "v_add_f32 v236, 0x3727c5ac, v236\n\t"
            "v_mfma_f32_16x16x32_bf16 v[156:159], v[80:83], v[212:215], v[156:159]\n\t"
            "v_rsq_f32 v236, v236\n\t"
            "v_mfma_f32_16x16x32_bf16 v[160:163], v[104:107], v[212:215], v[160:163]\n\t"
            "ds_write_b32 v249, v236\n\t"
            "v_mfma_f32_16x16x32_bf16 v[164:167], v[128:131], v[212:215], v[164:167]\n\t"
            "ds_read_b128 v[212:215], v233 offset:256\n\t"
            "ds_read_b32 v236, v251\n\t"
            "s_waitcnt lgkmcnt(4)\n\t"
            "v_mfma_f32_16x16x32_bf16 v[144:147], v[12:15], v[204:207], v[144:147]\n\t"
            "ds_read_b32 v237, v251 offset:64\n\t"
            "ds_read_b32 v238, v251 offset:128\n\t"
            "v_mfma_f32_16x16x32_bf16 v[148:151], v[36:39], v[204:207], v[148:151]\n\t"
            "v_mfma_f32_16x16x32_bf16 v[152:155], v[60:63], v[204:207], v[152:155]\n\t"
            "v_mfma_f32_16x16x32_bf16 v[156:159], v[84:87], v[204:207], v[156:159]\n\t"
            "v_mfma_f32_16x16x32_bf16 v[160:163], v[108:111], v[204:207], v[160:163]\n\t"
            "v_mfma_f32_16x16x32_bf16 v[164:167], v[132:135], v[204:207], v[164:167]\n\t"
            "ds_read_b128 v[204:207], v232 offset:6144\n\t"
            "s_waitcnt lgkmcnt(6)\n\t"
            "v_mfma_f32_16x16x32_bf16 v[144:147], v[16:19], v[208:211], v[144:147]\n\t"
            "v_mfma_f32_16x16x32_bf16 v[148:151], v[40:43], v[208:211], v[148:151]\n\t"
            "v_mfma_f32_16x16x32_bf16 v[152:155], v[64:67], v[208:211], v[152:155]\n\t"
            "v_mfma_f32_16x16x32_bf16 v[156:159], v[88:91], v[208:211], v[156:159]\n\t"
            "v_mfma_f32_16x16x32_bf16 v[160:163], v[112:115], v[208:211], v[160:163]\n\t"
            "v_mfma_f32_16x16x32_bf16 v[164:167], v[136:139], v[208:211], v[164:167]\n\t"
            "ds_read_b128 v[208:211], v233 offset:6144\n\t"
            "s_waitcnt lgkmcnt(5)\n\t"
            "v_mfma_f32_16x16x32_bf16 v[144:147], v[20:23], v[212:215], v[144:147]\n\t"
            "v_mfma_f32_16x16x32_bf16 v[148:151], v[44:47], v[212:215], v[148:151]\n\t"
            "v_mfma_f32_16x16x32_bf16 v[152:155], v[68:71], v[212:215], v[152:155]\n\t"
            "v_mfma_f32_16x16x32_bf16 v[156:159], v[92:95], v[212:215], v[156:159]\n\t"
            "v_mfma_f32_16x16x32_bf16 v[160:163], v[116:119], v[212:215], v[160:163]\n\t"
            "v_mfma_f32_16x16x32_bf16 v[164:167], v[140:143], v[212:215], v[164:167]\n\t"
            "ds_read_b128 v[212:215], v232 offset:6272\n\t"
            "s_waitcnt lgkmcnt(2)\n\t"
            "v_mfma_f32_16x16x32_bf16 v[168:171], v[0:3], v[204:207], 0\n\t"
            "v_mul_f32 v144, v236, v144\n\t"
            "v_mul_f32 v145, v236, v145\n\t"
            "v_mfma_f32_16x16x32_bf16 v[172:175], v[24:27], v[204:207], 0\n\t"
            "v_mul_f32 v152, v236, v152\n\t"
            "v_mul_f32 v153, v236, v153\n\t"
            "v_mfma_f32_16x16x32_bf16 v[176:179], v[48:51], v[204:207], 0\n\t"
            "v_mul_f32 v146, v236, v146\n\t"
            "v_mul_f32 v147, v236, v147\n\t"
            "v_mfma_f32_16x16x32_bf16 v[180:183], v[72:75], v[204:207], 0\n\t"
            "v_mul_f32 v154, v236, v154\n\t"
            "v_mul_f32 v155, v236, v155\n\t"
            "v_mfma_f32_16x16x32_bf16 v[184:187], v[96:99], v[204:207], 0\n\t"
            "v_cvt_pk_bf16_f32 v144, v144, v145\n\t"
            "v_cvt_pk_bf16_f32 v152, v152, v153\n\t"
            "v_mfma_f32_16x16x32_bf16 v[188:191], v[120:123], v[204:207], 0\n\t"
            "ds_read_b128 v[204:207], v233 offset:6272\n\t"
            "v_cvt_pk_bf16_f32 v156, v156, v157\n\t"
            "s_waitcnt lgkmcnt(2)\n\t"
            "v_mfma_f32_16x16x32_bf16 v[168:171], v[4:7], v[208:211], v[168:171]\n\t"
            "v_mul_f32 v148, v236, v148\n\t"
            "v_mul_f32 v149, v236, v149\n\t"
            "v_mfma_f32_16x16x32_bf16 v[172:175], v[28:31], v[208:211], v[172:175]\n\t"
            "v_mul_f32 v150, v236, v150\n\t"
            "v_mul_f32 v151, v236, v151\n\t"
            "v_mfma_f32_16x16x32_bf16 v[176:179], v[52:55], v[208:211], v[176:179]\n\t"
            "v_cvt_pk_bf16_f32 v145, v146, v147\n\t"
            "v_cvt_pk_bf16_f32 v153, v154, v155\n\t"
            "v_mfma_f32_16x16x32_bf16 v[180:183], v[76:79], v[208:211], v[180:183]\n\t"
            "v_cvt_pk_bf16_f32 v157, v158, v159\n\t"
            "v_cvt_pk_bf16_f32 v146, v148, v149\n\t"
            "v_mfma_f32_16x16x32_bf16 v[184:187], v[100:103], v[208:211], v[184:187]\n\t"
            "v_cvt_pk_bf16_f32 v147, v150, v151\n\t"
            "v_cvt_pk_bf16_f32 v158, v160, v161\n\t"
            "v_mfma_f32_16x16x32_bf16 v[188:191], v[124:127], v[208:211], v[188:191]\n\t"
            "ds_read_b128 v[208:211], v232 offset:6400\n\t"
            "v_cvt_pk_bf16_f32 v159, v162, v163\n\t"
            "s_waitcnt lgkmcnt(2)\n\t"
            "v_mfma_f32_16x16x32_bf16 v[168:171], v[8:11], v[212:215], v[168:171]\n\t"
            "v_cvt_pk_bf16_f32 v154, v164, v165\n\t"
            "v_cvt_pk_bf16_f32 v155, v166, v167\n\t"
            "v_mfma_f32_16x16x32_bf16 v[172:175], v[32:35], v[212:215], v[172:175]\n\t"
            "v_mul_f32 v236, 0x3e553b94, v236\n\t"
            "v_mfma_f32_16x16x32_bf16 v[176:179], v[56:59], v[212:215], v[176:179]\n\t"
            "v_mfma_f32_16x16x32_bf16 v[180:183], v[80:83], v[212:215], v[180:183]\n\t"
            "v_mfma_f32_16x16x32_bf16 v[184:187], v[104:107], v[212:215], v[184:187]\n\t"
            "v_mfma_f32_16x16x32_bf16 v[188:191], v[128:131], v[212:215], v[188:191]\n\t"
            "ds_read_b128 v[212:215], v233 offset:6400\n\t"
            "s_waitcnt lgkmcnt(2)\n\t"
            "v_mfma_f32_16x16x32_bf16 v[168:171], v[12:15], v[204:207], v[168:171]\n\t"
            "v_mfma_f32_16x16x32_bf16 v[172:175], v[36:39], v[204:207], v[172:175]\n\t"
            "v_mfma_f32_16x16x32_bf16 v[176:179], v[60:63], v[204:207], v[176:179]\n\t"
            "v_mfma_f32_16x16x32_bf16 v[180:183], v[84:87], v[204:207], v[180:183]\n\t"
            "v_mfma_f32_16x16x32_bf16 v[184:187], v[108:111], v[204:207], v[184:187]\n\t"
            "v_mfma_f32_16x16x32_bf16 v[188:191], v[132:135], v[204:207], v[188:191]\n\t"
            "ds_read_b128 v[204:207], v232 offset:12288\n\t"
            "s_waitcnt lgkmcnt(2)\n\t"
            "v_mfma_f32_16x16x32_bf16 v[168:171], v[16:19], v[208:211], v[168:171]\n\t"
            "v_mfma_f32_16x16x32_bf16 v[172:175], v[40:43], v[208:211], v[172:175]\n\t"
            "v_mfma_f32_16x16x32_bf16 v[176:179], v[64:67], v[208:211], v[176:179]\n\t"
            "v_mfma_f32_16x16x32_bf16 v[180:183], v[88:91], v[208:211], v[180:183]\n\t"
            "v_mfma_f32_16x16x32_bf16 v[184:187], v[112:115], v[208:211], v[184:187]\n\t"
            "v_mfma_f32_16x16x32_bf16 v[188:191], v[136:139], v[208:211], v[188:191]\n\t"
            "ds_read_b128 v[208:211], v233 offset:12288\n\t"
            "s_waitcnt lgkmcnt(2)\n\t"
            "v_mfma_f32_16x16x32_bf16 v[168:171], v[20:23], v[212:215], v[168:171]\n\t"
            "v_mfma_f32_16x16x32_bf16 v[172:175], v[44:47], v[212:215], v[172:175]\n\t"
            "v_mfma_f32_16x16x32_bf16 v[176:179], v[68:71], v[212:215], v[176:179]\n\t"
            "v_mfma_f32_16x16x32_bf16 v[180:183], v[92:95], v[212:215], v[180:183]\n\t"
            "v_mfma_f32_16x16x32_bf16 v[184:187], v[116:119], v[212:215], v[184:187]\n\t"
            "v_mfma_f32_16x16x32_bf16 v[188:191], v[140:143], v[212:215], v[188:191]\n\t"
            "ds_read_b128 v[212:215], v232 offset:12416\n\t"
            "s_waitcnt lgkmcnt(0)\n\t"
            "s_setprio 2\n\t"
            "s_nop 7\n\t"
            ".Lwa_loop%=:\n\t"
            "s_waitcnt lgkmcnt(2)\n\t"
            "v_mfma_f32_16x16x32_bf16 v[192:195], v[0:3], v[204:207], 0\n\t"
            "s_bfe_u32 s49, s70, 0x30001\n\t"
            "s_lshl_b32 s49, s49, 4\n\t"
            "s_or_b32 s73, s77, s49\n\t"
            "v_mfma_f32_16x16x32_bf16 v[148:151], v[24:27], v[204:207], 0\n\t"
            "v_mad_u32_u24 v220, v246, s72, v253\n\t"
            "v_mad_u32_u24 v216, v246, s72, v252\n\t"
            "v_mfma_f32_16x16x32_bf16 v[196:199], v[48:51], v[204:207], 0\n\t"
            "ds_read_b64 v[222:223], v220 offset:160\n\t"
            "ds_read_b64 v[220:221], v220 offset:128\n\t"
            "v_mfma_f32_16x16x32_bf16 v[200:203], v[72:75], v[204:207], 0\n\t"
            "ds_read_b128 v[224:227], v216 offset:64\n\t"
            "ds_read_b128 v[216:219], v216\n\t"
            "v_mfma_f32_16x16x32_bf16 v[160:163], v[96:99], v[204:207], 0\n\t"
            "v_mul_f32 v176, v237, v176\n\t"
            "v_mul_f32 v177, v237, v177\n\t"
            "v_mfma_f32_16x16x32_bf16 v[164:167], v[120:123], v[204:207], 0\n\t"
            "ds_read_b128 v[204:207], v233 offset:12416\n\t"
            "v_mul_f32 v178, v237, v178\n\t"
            "s_waitcnt lgkmcnt(6)\n\t"
            "v_mfma_f32_16x16x32_bf16 v[192:195], v[4:7], v[208:211], v[192:195]\n\t"
            "v_mul_f32 v179, v237, v179\n\t"
            "v_cvt_pk_bf16_f32 v176, v176, v177\n\t"
            "v_mfma_f32_16x16x32_bf16 v[148:151], v[28:31], v[208:211], v[148:151]\n\t"
            "v_mul_f32 v168, v237, v168\n\t"
            "v_mul_f32 v169, v237, v169\n\t"
            "v_mfma_f32_16x16x32_bf16 v[196:199], v[52:55], v[208:211], v[196:199]\n\t"
            "v_cvt_pk_bf16_f32 v177, v178, v179\n\t"
            "v_mul_f32 v170, v237, v170\n\t"
            "v_mfma_f32_16x16x32_bf16 v[200:203], v[76:79], v[208:211], v[200:203]\n\t"
            "v_mul_f32 v171, v237, v171\n\t"
            "v_cvt_pk_bf16_f32 v168, v168, v169\n\t"
            "v_mfma_f32_16x16x32_bf16 v[160:163], v[100:103], v[208:211], v[160:163]\n\t"
            "v_cvt_pk_bf16_f32 v180, v180, v181\n\t"
            "v_cvt_pk_bf16_f32 v178, v188, v189\n\t"
            "v_mfma_f32_16x16x32_bf16 v[164:167], v[124:127], v[208:211], v[164:167]\n\t"
            "ds_read_b128 v[208:211], v232 offset:12544\n\t"
            "v_cvt_pk_bf16_f32 v179, v190, v191\n\t"
            "s_waitcnt lgkmcnt(6)\n\t"
            "v_mfma_f32_16x16x32_bf16 v[192:195], v[8:11], v[212:215], v[192:195]\n\t"
            "v_mul_f32 v172, v237, v172\n\t"
            "v_mul_f32 v173, v237, v173\n\t"
            "v_mfma_f32_16x16x32_bf16 v[148:151], v[32:35], v[212:215], v[148:151]\n\t"
            "v_mul_f32 v174, v237, v174\n\t"
            "v_mul_f32 v175, v237, v175\n\t"
            "v_mfma_f32_16x16x32_bf16 v[196:199], v[56:59], v[212:215], v[196:199]\n\t"
            "v_cvt_pk_bf16_f32 v169, v170, v171\n\t"
            "v_cvt_pk_bf16_f32 v181, v182, v183\n\t"
            "v_mfma_f32_16x16x32_bf16 v[200:203], v[80:83], v[212:215], v[200:203]\n\t"
            "v_cvt_pk_bf16_f32 v170, v172, v173\n\t"
            "v_cvt_pk_bf16_f32 v171, v174, v175\n\t"
            "v_mfma_f32_16x16x32_bf16 v[160:163], v[104:107], v[212:215], v[160:163]\n\t"
            "v_cvt_pk_bf16_f32 v182, v184, v185\n\t"
            "v_cvt_pk_bf16_f32 v183, v186, v187\n\t"
            "v_mfma_f32_16x16x32_bf16 v[164:167], v[128:131], v[212:215], v[164:167]\n\t"
            "ds_read_b128 v[212:215], v233 offset:12544\n\t"
            "v_mov_b32 v230, s76\n\t"
            "s_waitcnt lgkmcnt(2)\n\t"
            "v_mfma_f32_16x16x32_bf16 v[192:195], v[12:15], v[204:207], v[192:195]\n\t"
            "ds_read_b32 v230, v230\n\t"
            "v_mov_b32 v239, s74\n\t"
            "v_mfma_f32_16x16x32_bf16 v[148:151], v[36:39], v[204:207], v[148:151]\n\t"
            "s_add_u32 s70, s70, 1\n\t"
            "s_add_u32 s72, s72, 800\n\t"
            "s_add_u32 s74, s74, 800\n\t"
            "s_add_u32 s76, s76, 800\n\t"
            "v_mfma_f32_16x16x32_bf16 v[196:199], v[60:63], v[204:207], v[196:199]\n\t"
            "s_add_u32 s77, s77, 384\n\t"
            "s_xor_b32 s48, s48, s47\n\t"
            "s_sub_u32 s44, s44, 1\n\t"
            "v_mfma_f32_16x16x32_bf16 v[200:203], v[84:87], v[204:207], v[200:203]\n\t"
            "s_mov_b32 s68, 0x10001\n\t"
            "s_mov_b32 s69, 0x10001\n\t"
            "ds_read_b32 v239, v239\n\t"
            "s_waitcnt lgkmcnt(3)\n\t"
            "v_mfma_f32_16x16x32_bf16 v[192:195], v[16:19], v[208:211], v[192:195]\n\t"
            "v_mul_f32 v237, 0x3e553b94, v237\n\t"
            "v_mfma_f32_16x16x32_bf16 v[160:163], v[108:111], v[204:207], v[160:163]\n\t"
            "v_mfma_f32_16x16x32_bf16 v[148:151], v[40:43], v[208:211], v[148:151]\n\t"
            "v_mfma_f32_16x16x32_bf16 v[164:167], v[132:135], v[204:207], v[164:167]\n\t"
            "v_mfma_f32_16x16x32_bf16 v[196:199], v[64:67], v[208:211], v[196:199]\n\t"
            "v_mfma_f32_16x16x32_bf16 v[200:203], v[88:91], v[208:211], v[200:203]\n\t"
            "s_waitcnt lgkmcnt(2)\n\t"
            "v_mfma_f32_16x16x32_bf16 v[192:195], v[20:23], v[212:215], v[192:195]\n\t"
            "v_mfma_f32_16x16x32_bf16 v[160:163], v[112:115], v[208:211], v[160:163]\n\t"
            "v_mfma_f32_16x16x32_bf16 v[148:151], v[44:47], v[212:215], v[148:151]\n\t"
            "v_mfma_f32_16x16x32_bf16 v[164:167], v[136:139], v[208:211], v[164:167]\n\t"
            "v_mfma_f32_16x16x32_bf16 v[196:199], v[68:71], v[212:215], v[196:199]\n\t"
            "v_mfma_f32_16x16x32_bf16 v[200:203], v[92:95], v[212:215], v[200:203]\n\t"
            "v_mfma_f32_16x16x32_bf16 v[160:163], v[116:119], v[212:215], v[160:163]\n\t"
            "s_nop 1\n\t"
            "v_mul_f32 v192, v238, v192\n\t"
            "v_mfma_f32_16x16x32_bf16 v[164:167], v[140:143], v[212:215], v[164:167]\n\t"
            "v_mul_f32 v193, v238, v193\n\t"
            "v_mul_f32 v194, v238, v194\n\t"
            "v_mfma_f32_16x16x16_bf16 v[172:175], v[220:221], v[154:155], 0\n\t"
            "v_mul_f32 v195, v238, v195\n\t"
            "v_cvt_pk_bf16_f32 v192, v192, v193\n\t"
            "v_mul_f32 v148, v238, v148\n\t"
            "v_mfma_f32_16x16x16_bf16 v[184:187], v[152:153], v[178:179], 0\n\t"
            "v_mul_f32 v149, v238, v149\n\t"
            "v_mul_f32 v150, v238, v150\n\t"
            "v_mul_f32 v151, v238, v151\n\t"
            "v_mfma_f32_16x16x16_bf16 v[208:211], v[220:221], v[178:179], 0\n\t"
            "v_cvt_pk_bf16_f32 v193, v194, v195\n\t"
            "v_mul_f32 v196, v238, v196\n\t"
            "v_mul_f32 v197, v238, v197\n\t"
            "v_mfma_f32_16x16x16_bf16 v[204:207], v[176:177], v[178:179], 0\n\t"
            "v_cvt_pk_bf16_f32 v194, v148, v149\n\t"
            "v_cvt_pk_bf16_f32 v195, v150, v151\n\t"
            "v_cvt_pk_bf16_f32 v200, v200, v201\n\t"
            "v_mfma_f32_16x16x16_bf16 v[148:151], v[152:153], v[154:155], 0\n\t"
            "v_mul_f32 v198, v238, v198\n\t"
            "v_mul_f32 v199, v238, v199\n\t"
            "v_cvt_pk_bf16_f32 v196, v196, v197\n\t"
            "v_mfma_f32_16x16x32_bf16 v[172:175], v[216:219], v[156:159], v[172:175]\n\t"
            "s_setprio 0\n\t"
            "v_cvt_pk_bf16_f32 v201, v202, v203\n\t"
            "v_cvt_pk_bf16_f32 v197, v198, v199\n\t"
            "s_setprio 3\n\t"
            "v_mfma_f32_16x16x32_bf16 v[148:151], v[144:147], v[156:159], v[148:151]\n\t"
            "v_cvt_pk_bf16_f32 v202, v160, v161\n\t"
            "v_cvt_pk_bf16_f32 v203, v162, v163\n\t"
            "v_mfma_f32_16x16x16_bf16 v[160:163], v[176:177], v[154:155], 0\n\t"
            "v_cvt_pk_bf16_f32 v198, v164, v165\n\t"
            "v_cvt_pk_bf16_f32 v199, v166, v167\n\t"
            "v_mul_f32 v238, 0x3e553b94, v238\n\t"
            "v_mfma_f32_16x16x16_bf16 v[164:167], v[196:197], v[154:155], 0\n\t"
            "s_nop 0\n\t"
            "v_max3_f32 v228, v172, v148, v149\n\t"
            "v_max3_f32 v228, v228, v150, v151\n\t"
            "v_mfma_f32_16x16x16_bf16 v[188:191], v[196:197], v[178:179], 0\n\t"
            "v_mfma_f32_16x16x32_bf16 v[184:187], v[144:147], v[180:183], v[184:187]\n\t"
            "v_mfma_f32_16x16x32_bf16 v[208:211], v[216:219], v[180:183], v[208:211]\n\t"
            "v_mfma_f32_16x16x32_bf16 v[160:163], v[168:171], v[156:159], v[160:163]\n\t"
            "v_mfma_f32_16x16x32_bf16 v[204:207], v[168:171], v[180:183], v[204:207]\n\t"
            "v_mfma_f32_16x16x32_bf16 v[164:167], v[192:195], v[156:159], v[164:167]\n\t"
            "s_nop 0\n\t"
            "v_mfma_f32_16x16x32_bf16 v[188:191], v[192:195], v[180:183], v[188:191]\n\t"
            "v_mfma_f32_16x16x16_bf16 v[212:215], v[222:223], v[152:153], 0\n\t"
            "v_mfma_f32_16x16x16_bf16 v[156:159], v[222:223], v[196:197], 0\n\t"
            "s_nop 0\n\t"
            "v_max3_f32 v229, v208, v184, v185\n\t"
            "v_mfma_f32_16x16x16_bf16 v[180:183], v[152:153], v[198:199], 0\n\t"
            "v_max3_f32 v229, v229, v186, v187\n\t"
            "v_max3_f32 v228, v228, v160, v161\n\t"
            "v_max3_f32 v229, v229, v204, v205\n\t"
            "v_mfma_f32_16x16x32_bf16 v[212:215], v[224:227], v[144:147], v[212:215]\n\t"
            "v_max3_f32 v228, v228, v162, v163\n\t"
            "v_max3_f32 v229, v229, v206, v207\n\t"
            "v_mfma_f32_16x16x32_bf16 v[156:159], v[224:227], v[192:195], v[156:159]\n\t"
            "v_max3_f32 v228, v228, v164, v165\n\t"
            "v_max3_f32 v229, v229, v188, v189\n\t"
            "v_mfma_f32_16x16x32_bf16 v[180:183], v[144:147], v[200:203], v[180:183]\n\t"
            "s_setprio 0\n\t"
            "v_max3_f32 v228, v228, v166, v167\n\t"
            "v_max3_f32 v229, v229, v190, v191\n\t"
            "s_nop 1\n\t"
            "v_permlane32_swap_b32 v228, v229\n\t"
            "v_max_f32 v228, v228, v229\n\t"
            "v_mov_b32 v231, v228\n\t"
            "s_nop 1\n\t"
            "v_permlane16_swap_b32 v228, v231\n\t"
            "v_max_f32 v228, v228, v231\n\t"
            "v_mov_b32 v229, v228\n\t"
            "s_nop 1\n\t"
            "v_permlane32_swap_b32 v228, v229\n\t"
            "v_mul_f32_e64 v228, -v228, v236\n\t"
            "v_fma_f32 v148, v148, v236, v228\n\t"
            "v_fma_f32 v149, v149, v236, v228\n\t"
            "v_exp_f32 v148, v148\n\t"
            "v_exp_f32 v149, v149\n\t"
            "v_fma_f32 v150, v150, v236, v228\n\t"
            "v_fma_f32 v151, v151, v236, v228\n\t"
            "v_exp_f32 v150, v150\n\t"
            "v_exp_f32 v151, v151\n\t"
            "v_fma_f32 v160, v160, v236, v228\n\t"
            "v_fma_f32 v161, v161, v236, v228\n\t"
            "v_fma_f32 v162, v162, v236, v228\n\t"
            "v_fma_f32 v163, v163, v236, v228\n\t"
            "v_exp_f32 v160, v160\n\t"
            "v_exp_f32 v161, v161\n\t"
            "v_exp_f32 v162, v162\n\t"
            "v_exp_f32 v163, v163\n\t"
            "v_cvt_pk_bf16_f32 v148, v148, v149\n\t"
            "v_cvt_pk_bf16_f32 v149, v150, v151\n\t"
            "v_cvt_pk_bf16_f32 v150, v160, v161\n\t"
            "v_cvt_pk_bf16_f32 v151, v162, v163\n\t"
            "s_setprio 3\n\t"
            "v_mfma_f32_16x16x16_bf16 v[160:163], v[222:223], v[176:177], 0\n\t"
            "s_setprio 0\n\t"
            "v_mul_f32_e64 v229, -v229, v237\n\t"
            "v_fma_f32 v164, v164, v236, v228\n\t"
            "v_fma_f32 v165, v165, v236, v228\n\t"
            "v_fma_f32 v184, v184, v237, v229\n\t"
            "v_fma_f32 v185, v185, v237, v229\n\t"
            "v_exp_f32 v164, v164\n\t"
            "s_setprio 3\n\t"
            "v_mfma_f32_16x16x32_bf16 v[160:163], v[224:227], v[168:171], v[160:163]\n\t"
            "s_setprio 0\n\t"
            "v_exp_f32 v165, v165\n\t"
            "v_fma_f32 v166, v166, v236, v228\n\t"
            "v_fma_f32 v167, v167, v236, v228\n\t"
            "v_fma_f32 v172, v172, v236, v228\n\t"
            "v_exp_f32 v184, v184\n\t"
            "v_exp_f32 v185, v185\n\t"
            "v_fma_f32 v186, v186, v237, v229\n\t"
            "v_fma_f32 v187, v187, v237, v229\n\t"
            "v_max3_f32 v234, v212, v160, v156\n\t"
            "s_waitcnt lgkmcnt(1)\n\t"
            "v_max_f32 v234, v234, v230\n\t"
            "v_exp_f32 v166, v166\n\t"
            "v_exp_f32 v167, v167\n\t"
            "v_max_f32_dpp v234, v234, v234 row_ror:8 row_mask:0xf bank_mask:0xf\n\t"
            "v_exp_f32 v172, v172\n\t"
            "v_exp_f32 v186, v186\n\t"
            "v_max_f32_dpp v234, v234, v234 row_ror:4 row_mask:0xf bank_mask:0xf\n\t"
            "v_exp_f32 v187, v187\n\t"
            "v_fma_f32 v204, v204, v237, v229\n\t"
            "v_fma_f32 v205, v205, v237, v229\n\t"
            "v_fma_f32 v206, v206, v237, v229\n\t"
            "v_fma_f32 v207, v207, v237, v229\n\t"
            "v_fma_f32 v188, v188, v237, v229\n\t"
            "v_fma_f32 v189, v189, v237, v229\n\t"
            "v_exp_f32 v204, v204\n\t"
            "v_exp_f32 v205, v205\n\t"
            "v_exp_f32 v206, v206\n\t"
            "v_exp_f32 v207, v207\n\t"
            "v_exp_f32 v188, v188\n\t"
            "v_exp_f32 v189, v189\n\t"
            "v_fma_f32 v190, v190, v237, v229\n\t"
            "v_fma_f32 v191, v191, v237, v229\n\t"
            "v_fma_f32 v208, v208, v237, v229\n\t"
            "v_max_f32_dpp v234, v234, v234 row_ror:2 row_mask:0xf bank_mask:0xf\n\t"
            "v_cvt_pk_bf16_f32 v164, v164, v165\n\t"
            "v_exp_f32 v190, v190\n\t"
            "v_exp_f32 v191, v191\n\t"
            "v_exp_f32 v208, v208\n\t"
            "v_cvt_pk_bf16_f32 v184, v184, v185\n\t"
            "v_cndmask_b32_e64 v235, v212, v160, s[60:61]\n\t"
            "v_cvt_pk_bf16_f32 v165, v166, v167\n\t"
            "v_cvt_pk_bf16_f32 v172, v172, 0\n\t"
            "v_cvt_pk_bf16_f32 v185, v186, v187\n\t"
            "v_cndmask_b32_e64 v235, v235, v156, s[62:63]\n\t"
            "v_cndmask_b32_e64 v166, 0, v172, s[58:59]\n\t"
            "s_setprio 3\n\t"
            "v_mfma_f32_16x16x16_bf16 v[172:175], v[196:197], v[198:199], 0\n\t"
            "v_cvt_pk_bf16_f32 v186, v204, v205\n\t"
            "v_cvt_pk_bf16_f32 v187, v206, v207\n\t"
            "v_cvt_pk_bf16_f32 v188, v188, v189\n\t"
            "v_mfma_f32_16x16x16_bf16 v[204:207], v[176:177], v[198:199], 0\n\t"
            "v_max_f32_dpp v234, v234, v234 row_ror:1 row_mask:0xf bank_mask:0xf\n\t"
            "v_cndmask_b32_e64 v235, v235, v230, s[64:65]\n\t"
            "v_cvt_pk_bf16_f32 v189, v190, v191\n\t"
            "v_mfma_f32_16x16x32_bf16 v[172:175], v[192:195], v[200:203], v[172:175]\n\t"
            "v_cvt_pk_bf16_f32 v208, v208, 0\n\t"
            "v_sub_f32 v235, v235, v234\n\t"
            "v_mfma_f32_16x16x32_bf16 v[204:207], v[168:171], v[200:203], v[204:207]\n\t"
            "s_setprio 0\n\t"
            "v_cndmask_b32_e64 v190, 0, v208, s[58:59]\n\t"
            "s_waitcnt lgkmcnt(0)\n\t"
            "v_mul_f32 v235, v239, v235\n\t"
            "v_mfma_f32_16x16x16_bf16 v[208:211], v[220:221], v[198:199], 0\n\t"
            "v_exp_f32 v235, v235\n\t"
            "v_mov_b32 v167, 0\n\t"
            "v_cvt_pk_bf16_f32 v235, v235, 0\n\t"
            "v_cndmask_b32_e64 v235, 0, v235, s[66:67]\n\t"
            "v_mov_b32 v191, 0\n\t"
            "s_nop 0\n\t"
            "v_mfma_f32_16x16x32_bf16 v[208:211], v[216:219], v[200:203], v[208:211]\n\t"
            "ds_write_b16 v250, v235 offset:256\n\t"
            "s_nop 6\n\t"
            "v_max3_f32 v230, v208, v180, v181\n\t"
            "v_max3_f32 v230, v230, v182, v183\n\t"
            "v_max3_f32 v230, v230, v204, v205\n\t"
            "v_max3_f32 v230, v230, v206, v207\n\t"
            "v_max3_f32 v230, v230, v172, v173\n\t"
            "v_max3_f32 v230, v230, v174, v175\n\t"
            "v_mov_b32 v231, v230\n\t"
            "s_nop 1\n\t"
            "v_permlane32_swap_b32 v230, v231\n\t"
            "v_max_f32 v230, v230, v231\n\t"
            "v_mov_b32 v231, v230\n\t"
            "s_nop 1\n\t"
            "v_permlane16_swap_b32 v230, v231\n\t"
            "v_max_f32 v230, v230, v231\n\t"
            "v_mul_f32_e64 v230, -v230, v238\n\t"
            "v_fma_f32 v180, v180, v238, v230\n\t"
            "v_fma_f32 v181, v181, v238, v230\n\t"
            "v_exp_f32 v180, v180\n\t"
            "v_exp_f32 v181, v181\n\t"
            "v_fma_f32 v182, v182, v238, v230\n\t"
            "v_fma_f32 v183, v183, v238, v230\n\t"
            "v_exp_f32 v182, v182\n\t"
            "v_exp_f32 v183, v183\n\t"
            "v_fma_f32 v204, v204, v238, v230\n\t"
            "v_fma_f32 v205, v205, v238, v230\n\t"
            "v_fma_f32 v206, v206, v238, v230\n\t"
            "v_fma_f32 v207, v207, v238, v230\n\t"
            "v_exp_f32 v204, v204\n\t"
            "v_exp_f32 v205, v205\n\t"
            "v_exp_f32 v206, v206\n\t"
            "v_exp_f32 v207, v207\n\t"
            "v_fma_f32 v172, v172, v238, v230\n\t"
            "v_fma_f32 v173, v173, v238, v230\n\t"
            "v_cvt_pk_bf16_f32 v180, v180, v181\n\t"
            "v_exp_f32 v172, v172\n\t"
            "v_exp_f32 v173, v173\n\t"
            "v_fma_f32 v174, v174, v238, v230\n\t"
            "v_fma_f32 v175, v175, v238, v230\n\t"
            "v_fma_f32 v208, v208, v238, v230\n\t"
            "v_cvt_pk_bf16_f32 v181, v182, v183\n\t"
            "v_exp_f32 v174, v174\n\t"
            "v_exp_f32 v175, v175\n\t"
            "v_exp_f32 v208, v208\n\t"
            "v_cvt_pk_bf16_f32 v182, v204, v205\n\t"
            "v_cvt_pk_bf16_f32 v183, v206, v207\n\t"
            "v_cvt_pk_bf16_f32 v172, v172, v173\n\t"
            "v_cvt_pk_bf16_f32 v173, v174, v175\n\t"
            "v_cvt_pk_bf16_f32 v208, v208, 0\n\t"
            "v_cndmask_b32_e64 v174, 0, v208, s[58:59]\n\t"
            ".Lwa_in%=:\n\t"
            "s_barrier\n\t"
            "s_setprio 2\n\t"
            "s_cmp_le_u32 s44, 2\n\t"
            "s_cbranch_scc1 .Lwa_end%=\n\t"
            ".Lwa_go%=:\n\t"
            "v_mfma_f32_16x16x32_bf16 v[200:203], v[240:243], v[148:151], 0\n\t"
            "ds_read_b64 v[196:197], v247 offset:1536\n\t"
            "ds_read_b64 v[176:177], v247\n\t"
            "v_mfma_f32_16x16x32_bf16 v[200:203], v[240:243], v[164:167], v[200:203]\n\t"
            "ds_read_b64 v[220:221], v247 offset:3072\n\t"
            "v_add_u32 v235, s45, v248\n\t"
            "v_xor_b32 v234, s73, v254\n\t"
            "v_mov_b32 v199, 0\n\t"
            "v_mov_b32 v179, 0\n\t"
            "v_mov_b32 v223, 0\n\t"
            "v_add_u32 v232, s48, v244\n\t"
            "v_xor_b32 v233, 64, v232\n\t"
            "v_rcp_f32 v201, v200\n\t"
            "v_mov_b32 v175, 0\n\t"
            "ds_read_b128 v[168:171], v235\n\t"
            "ds_read_b128 v[192:195], v235 offset:1536\n\t"
            "ds_read_b128 v[216:219], v235 offset:3072\n\t"
            "ds_read_u16 v198, v234 offset:128\n\t"
            "ds_read_u16 v178, v234\n\t"
            "ds_read_u16 v222, v234 offset:256\n\t"
            "ds_read_b128 v[204:207], v232\n\t"
            "ds_read_b128 v[208:211], v233\n\t"
            "v_add_u32 v235, s48, v248\n\t"
            "v_mad_u32_u24 v234, v246, s75, v253\n\t"
            "ds_read_b128 v[228:231], v235 offset:18816\n\t"
            "s_waitcnt lgkmcnt(8)\n\t"
            "v_mfma_f32_16x16x32_bf16 v[224:227], v[168:171], v[148:151], 0\n\t"
            "s_waitcnt lgkmcnt(7)\n\t"
            "v_mfma_f32_16x16x32_bf16 v[212:215], v[192:195], v[148:151], 0\n\t"
            "s_waitcnt lgkmcnt(6)\n\t"
            "v_mfma_f32_16x16x32_bf16 v[148:151], v[216:219], v[148:151], 0\n\t"
            "s_waitcnt lgkmcnt(5)\n\t"
            "v_mfma_f32_16x16x32_bf16 v[212:215], v[196:199], v[164:167], v[212:215]\n\t"
            "s_waitcnt lgkmcnt(0)\n\t"
            "v_add_f32 v236, v228, v230\n\t"
            "v_add_f32 v237, v229, v231\n\t"
            "v_mfma_f32_16x16x32_bf16 v[224:227], v[176:179], v[164:167], v[224:227]\n\t"
            "ds_read_b128 v[228:231], v235 offset:19840\n\t"
            "v_mfma_f32_16x16x32_bf16 v[148:151], v[220:223], v[164:167], v[148:151]\n\t"
            "v_mfma_f32_16x16x32_bf16 v[144:147], v[0:3], v[204:207], 0\n\t"
            "s_nop 0\n\t"
            "v_mul_f32 v212, v201, v212\n\t"
            "v_mfma_f32_16x16x32_bf16 v[152:155], v[48:51], v[204:207], 0\n\t"
            "v_mul_f32 v213, v201, v213\n\t"
            "v_mul_f32 v214, v201, v214\n\t"
            "v_mfma_f32_16x16x32_bf16 v[156:159], v[72:75], v[204:207], 0\n\t"
            "v_mul_f32 v215, v201, v215\n\t"
            "v_cvt_pk_bf16_f32 v212, v212, v213\n\t"
            "v_mfma_f32_16x16x32_bf16 v[160:163], v[96:99], v[204:207], 0\n\t"
            "v_mul_f32 v224, v201, v224\n\t"
            "v_mul_f32 v225, v201, v225\n\t"
            "v_mfma_f32_16x16x32_bf16 v[164:167], v[120:123], v[204:207], 0\n\t"
            "v_mul_f32 v226, v201, v226\n\t"
            "v_mul_f32 v227, v201, v227\n\t"
            "v_mfma_f32_16x16x32_bf16 v[144:147], v[4:7], v[208:211], v[144:147]\n\t"
            "v_cvt_pk_bf16_f32 v213, v214, v215\n\t"
            "v_mul_f32 v148, v201, v148\n\t"
            "v_mfma_f32_16x16x32_bf16 v[152:155], v[52:55], v[208:211], v[152:155]\n\t"
            "v_mul_f32 v149, v201, v149\n\t"
            "v_mul_f32 v150, v201, v150\n\t"
            "v_mfma_f32_16x16x32_bf16 v[156:159], v[76:79], v[208:211], v[156:159]\n\t"
            "v_mul_f32 v151, v201, v151\n\t"
            "v_cvt_pk_bf16_f32 v224, v224, v225\n\t"
            "v_mfma_f32_16x16x32_bf16 v[200:203], v[240:243], v[184:187], 0\n\t"
            "ds_write_b64 v245, v[212:213] offset:128\n\t"
            "v_mfma_f32_16x16x32_bf16 v[212:215], v[192:195], v[184:187], 0\n\t"
            "v_cvt_pk_bf16_f32 v225, v226, v227\n\t"
            "v_cvt_pk_bf16_f32 v148, v148, v149\n\t"
            "v_mfma_f32_16x16x32_bf16 v[200:203], v[240:243], v[188:191], v[200:203]\n\t"
            "ds_write_b64 v245, v[224:225]\n\t"
            "v_mfma_f32_16x16x32_bf16 v[212:215], v[196:199], v[188:191], v[212:215]\n\t"
            "v_cvt_pk_bf16_f32 v149, v150, v151\n\t"
            "s_waitcnt lgkmcnt(2)\n\t"
            "v_add_f32 v228, v228, v230\n\t"
            "v_mfma_f32_16x16x32_bf16 v[224:227], v[168:171], v[184:187], 0\n\t"
            "ds_write_b64 v245, v[148:149] offset:256\n\t"
            "v_mfma_f32_16x16x32_bf16 v[184:187], v[216:219], v[184:187], 0\n\t"
            "v_rcp_f32 v201, v200\n\t"
            "v_mfma_f32_16x16x32_bf16 v[224:227], v[176:179], v[188:191], v[224:227]\n\t"
            "v_mul_f32 v212, v201, v212\n\t"
            "v_mul_f32 v213, v201, v213\n\t"
            "v_mfma_f32_16x16x32_bf16 v[184:187], v[220:223], v[188:191], v[184:187]\n\t"
            "v_mul_f32 v214, v201, v214\n\t"
            "v_mul_f32 v215, v201, v215\n\t"
            "v_mfma_f32_16x16x32_bf16 v[188:191], v[192:195], v[180:183], 0\n\t"
            "v_cvt_pk_bf16_f32 v212, v212, v213\n\t"
            "v_cvt_pk_bf16_f32 v213, v214, v215\n\t"
            "v_mfma_f32_16x16x32_bf16 v[188:191], v[196:199], v[172:175], v[188:191]\n\t"
            "ds_write_b64 v245, v[212:213] offset:6272\n\t"
            "v_mfma_f32_16x16x32_bf16 v[148:151], v[24:27], v[204:207], 0\n\t"
            "ds_read_b128 v[212:215], v232 offset:128\n\t"
            "ds_read_b128 v[204:207], v233 offset:128\n\t"
            "v_mfma_f32_16x16x32_bf16 v[148:151], v[28:31], v[208:211], v[148:151]\n\t"
            "v_mul_f32 v224, v201, v224\n\t"
            "v_mul_f32 v225, v201, v225\n\t"
            "v_mfma_f32_16x16x32_bf16 v[160:163], v[100:103], v[208:211], v[160:163]\n\t"
            "v_mul_f32 v226, v201, v226\n\t"
            "v_mul_f32 v227, v201, v227\n\t"
            "v_mfma_f32_16x16x32_bf16 v[164:167], v[124:127], v[208:211], v[164:167]\n\t"
            "ds_read_b128 v[208:211], v232 offset:256\n\t"
            "v_mul_f32 v184, v201, v184\n\t"
            "v_mul_f32 v185, v201, v185\n\t"
            "v_mul_f32 v186, v201, v186\n\t"
            "v_mul_f32 v187, v201, v187\n\t"
            "v_mfma_f32_16x16x32_bf16 v[200:203], v[240:243], v[180:183], 0\n\t"
            "v_cvt_pk_bf16_f32 v184, v184, v185\n\t"
            "v_cvt_pk_bf16_f32 v185, v186, v187\n\t"
            "v_mfma_f32_16x16x32_bf16 v[200:203], v[240:243], v[172:175], v[200:203]\n\t"
            "ds_read_b64 v[186:187], v234 offset:288\n\t"
            "ds_write_b64 v245, v[184:185] offset:6400\n\t"
            "ds_read_b64 v[184:185], v234 offset:256\n\t"
            "v_cvt_pk_bf16_f32 v224, v224, v225\n\t"
            "v_cvt_pk_bf16_f32 v225, v226, v227\n\t"
            "v_add_f32 v236, v236, v228\n\t"
            "s_waitcnt lgkmcnt(5)\n\t"
            "v_mfma_f32_16x16x32_bf16 v[144:147], v[8:11], v[212:215], v[144:147]\n\t"
            "v_rcp_f32 v201, v200\n\t"
            "v_mfma_f32_16x16x32_bf16 v[148:151], v[32:35], v[212:215], v[148:151]\n\t"
            "v_mul_f32 v188, v201, v188\n\t"
            "v_mul_f32 v189, v201, v189\n\t"
            "v_mfma_f32_16x16x32_bf16 v[152:155], v[56:59], v[212:215], v[152:155]\n\t"
            "v_mul_f32 v190, v201, v190\n\t"
            "v_mul_f32 v191, v201, v191\n\t"
            "v_mfma_f32_16x16x32_bf16 v[156:159], v[80:83], v[212:215], v[156:159]\n\t"
            "v_cvt_pk_bf16_f32 v188, v188, v189\n\t"
            "v_cvt_pk_bf16_f32 v189, v190, v191\n\t"
            "v_mfma_f32_16x16x32_bf16 v[160:163], v[104:107], v[212:215], v[160:163]\n\t"
            "ds_read_b64 v[190:191], v234 offset:352\n\t"
            "ds_write_b64 v245, v[188:189] offset:12416\n\t"
            "v_mfma_f32_16x16x32_bf16 v[164:167], v[128:131], v[212:215], v[164:167]\n\t"
            "ds_read_b64 v[188:189], v234 offset:320\n\t"
            "ds_read_b128 v[212:215], v233 offset:256\n\t"
            "s_waitcnt lgkmcnt(8)\n\t"
            "v_mfma_f32_16x16x32_bf16 v[144:147], v[12:15], v[204:207], v[144:147]\n\t"
            "ds_write_b64 v245, v[224:225] offset:6144\n\t"
            "v_mfma_f32_16x16x32_bf16 v[224:227], v[168:171], v[180:183], 0\n\t"
            "v_add_f32 v229, v229, v231\n\t"
            "v_mul_f32 v236, 0x3baaaaab, v236\n\t"
            "v_mfma_f32_16x16x32_bf16 v[180:183], v[216:219], v[180:183], 0\n\t"
            "v_add_f32 v237, v237, v229\n\t"
            "v_mul_f32_e64 v236, v236, -v236\n\t"
            "v_mfma_f32_16x16x32_bf16 v[224:227], v[176:179], v[172:175], v[224:227]\n\t"
            "v_fmac_f32 v236, 0x3baaaaab, v237\n\t"
            "v_max_f32 v236, 0, v236\n\t"
            "v_mfma_f32_16x16x32_bf16 v[180:183], v[220:223], v[172:175], v[180:183]\n\t"
            "v_add_f32 v236, 0x3727c5ac, v236\n\t"
            "v_rsq_f32 v236, v236\n\t"
            "v_mfma_f32_16x16x32_bf16 v[148:151], v[36:39], v[204:207], v[148:151]\n\t"
            "ds_write_b32 v249, v236\n\t"
            "v_mfma_f32_16x16x32_bf16 v[152:155], v[60:63], v[204:207], v[152:155]\n\t"
            "ds_read_b32 v236, v251\n\t"
            "ds_read_b32 v237, v251 offset:64\n\t"
            "v_mfma_f32_16x16x32_bf16 v[156:159], v[84:87], v[204:207], v[156:159]\n\t"
            "ds_read_b32 v238, v251 offset:128\n\t"
            "v_mul_f32 v224, v201, v224\n\t"
            "v_mfma_f32_16x16x32_bf16 v[160:163], v[108:111], v[204:207], v[160:163]\n\t"
            "v_mul_f32 v225, v201, v225\n\t"
            "v_mul_f32 v226, v201, v226\n\t"
            "v_mfma_f32_16x16x32_bf16 v[164:167], v[132:135], v[204:207], v[164:167]\n\t"
            "ds_read_b128 v[204:207], v232 offset:6144\n\t"
            "v_mul_f32 v227, v201, v227\n\t"
            "s_waitcnt lgkmcnt(13)\n\t"
            "v_mfma_f32_16x16x32_bf16 v[144:147], v[16:19], v[208:211], v[144:147]\n\t"
            "v_mul_f32 v180, v201, v180\n\t"
            "v_mul_f32 v181, v201, v181\n\t"
            "v_mfma_f32_16x16x32_bf16 v[148:151], v[40:43], v[208:211], v[148:151]\n\t"
            "v_mul_f32 v182, v201, v182\n\t"
            "v_mul_f32 v183, v201, v183\n\t"
            "s_waitcnt lgkmcnt(10)\n\t"
            "v_mfma_f32_16x16x32_bf16 v[200:203], v[240:243], v[184:187], 0\n\t"
            "v_cvt_pk_bf16_f32 v180, v180, v181\n\t"
            "v_cvt_pk_bf16_f32 v181, v182, v183\n\t"
            "v_mfma_f32_16x16x32_bf16 v[152:155], v[64:67], v[208:211], v[152:155]\n\t"
            "ds_write_b64 v245, v[180:181] offset:12544\n\t"
            "v_mfma_f32_16x16x32_bf16 v[156:159], v[88:91], v[208:211], v[156:159]\n\t"
            "v_cvt_pk_bf16_f32 v224, v224, v225\n\t"
            "v_cvt_pk_bf16_f32 v225, v226, v227\n\t"
            "v_mfma_f32_16x16x32_bf16 v[160:163], v[112:115], v[208:211], v[160:163]\n\t"
            "ds_write_b64 v245, v[224:225] offset:12288\n\t"
            "v_mfma_f32_16x16x32_bf16 v[164:167], v[136:139], v[208:211], v[164:167]\n\t"
            "ds_read_b128 v[208:211], v233 offset:6144\n\t"
            "v_mfma_f32_16x16x32_bf16 v[168:171], v[168:171], v[184:187], 0\n\t"
            "v_mfma_f32_16x16x32_bf16 v[192:195], v[192:195], v[184:187], 0\n\t"
            "v_mfma_f32_16x16x32_bf16 v[216:219], v[216:219], v[184:187], 0\n\t"
            "s_waitcnt lgkmcnt(10)\n\t"
            "v_mfma_f32_16x16x32_bf16 v[200:203], v[240:243], v[188:191], v[200:203]\n\t"
            "s_waitcnt lgkmcnt(9)\n\t"
            "v_mfma_f32_16x16x32_bf16 v[144:147], v[20:23], v[212:215], v[144:147]\n\t"
            "v_mfma_f32_16x16x32_bf16 v[148:151], v[44:47], v[212:215], v[148:151]\n\t"
            "v_mfma_f32_16x16x32_bf16 v[152:155], v[68:71], v[212:215], v[152:155]\n\t"
            "v_mfma_f32_16x16x32_bf16 v[156:159], v[92:95], v[212:215], v[156:159]\n\t"
            "v_mfma_f32_16x16x32_bf16 v[160:163], v[116:119], v[212:215], v[160:163]\n\t"
            "v_mfma_f32_16x16x32_bf16 v[164:167], v[140:143], v[212:215], v[164:167]\n\t"
            "ds_read_b128 v[212:215], v232 offset:6272\n\t"
            "v_mfma_f32_16x16x32_bf16 v[168:171], v[176:179], v[188:191], v[168:171]\n\t"
            "v_rcp_f32 v201, v200\n\t"
            "v_mfma_f32_16x16x32_bf16 v[192:195], v[196:199], v[188:191], v[192:195]\n\t"
            "v_xor_b32 v202, s73, v255\n\t"
            "s_waitcnt lgkmcnt(7)\n\t"
            "v_mul_f32 v144, v236, v144\n\t"
            "v_mfma_f32_16x16x32_bf16 v[216:219], v[220:223], v[188:191], v[216:219]\n\t"
            "v_mul_f32 v145, v236, v145\n\t"
            "v_mul_f32 v152, v236, v152\n\t"
            "s_waitcnt lgkmcnt(4)\n\t"
            "v_mfma_f32_16x16x32_bf16 v[172:175], v[24:27], v[204:207], 0\n\t"
            "v_mul_f32 v168, v201, v168\n\t"
            "v_mul_f32 v169, v201, v169\n\t"
            "v_mfma_f32_16x16x32_bf16 v[176:179], v[48:51], v[204:207], 0\n\t"
            "v_mul_f32 v192, v201, v192\n\t"
            "v_mul_f32 v193, v201, v193\n\t"
            "v_mfma_f32_16x16x32_bf16 v[180:183], v[72:75], v[204:207], 0\n\t"
            "v_mul_f32 v216, v201, v216\n\t"
            "v_mul_f32 v217, v201, v217\n\t"
            "v_mfma_f32_16x16x32_bf16 v[184:187], v[96:99], v[204:207], 0\n\t"
            "v_mul_f32 v170, v201, v170\n\t"
            "v_mul_f32 v171, v201, v171\n\t"
            "v_mfma_f32_16x16x32_bf16 v[188:191], v[120:123], v[204:207], 0\n\t"
            "v_cvt_pk_bf16_f32 v168, v168, v169\n\t"
            "v_mul_f32 v194, v201, v194\n\t"
            "s_waitcnt lgkmcnt(1)\n\t"
            "v_mfma_f32_16x16x32_bf16 v[172:175], v[28:31], v[208:211], v[172:175]\n\t"
            "v_mul_f32 v195, v201, v195\n\t"
            "v_cvt_pk_bf16_f32 v192, v192, v193\n\t"
            "v_mfma_f32_16x16x32_bf16 v[176:179], v[52:55], v[208:211], v[176:179]\n\t"
            "v_mul_f32 v218, v201, v218\n\t"
            "v_mul_f32 v219, v201, v219\n\t"
            "v_mfma_f32_16x16x32_bf16 v[180:183], v[76:79], v[208:211], v[180:183]\n\t"
            "v_cvt_pk_bf16_f32 v216, v216, v217\n\t"
            "v_cvt_pk_bf16_f32 v169, v170, v171\n\t"
            "v_mfma_f32_16x16x32_bf16 v[184:187], v[100:103], v[208:211], v[184:187]\n\t"
            "v_cvt_pk_bf16_f32 v193, v194, v195\n\t"
            "v_cvt_pk_bf16_f32 v217, v218, v219\n\t"
            "v_mfma_f32_16x16x32_bf16 v[188:191], v[124:127], v[208:211], v[188:191]\n\t"
            "s_mov_b64 exec, s[68:69]\n\t"
            "ds_write_b64 v202, v[168:169]\n\t"
            "ds_write_b64 v202, v[192:193] offset:128\n\t"
            "ds_write_b64 v202, v[216:217] offset:256\n\t"
            "s_mov_b64 exec, -1\n\t"
            "v_mfma_f32_16x16x32_bf16 v[168:171], v[0:3], v[204:207], 0\n\t"
            "ds_read_b128 v[204:207], v233 offset:6272\n\t"
            "v_mul_f32 v153, v236, v153\n\t"
            "v_mfma_f32_16x16x32_bf16 v[168:171], v[4:7], v[208:211], v[168:171]\n\t"
            "ds_read_b128 v[208:211], v232 offset:6400\n\t"
            "v_mul_f32 v146, v236, v146\n\t"
            "s_waitcnt lgkmcnt(5)\n\t"
            "v_mfma_f32_16x16x32_bf16 v[168:171], v[8:11], v[212:215], v[168:171]\n\t"
            "v_mul_f32 v147, v236, v147\n\t"
            "v_mul_f32 v154, v236, v154\n\t"
            "v_mfma_f32_16x16x32_bf16 v[172:175], v[32:35], v[212:215], v[172:175]\n\t"
            "v_mul_f32 v155, v236, v155\n\t"
            "v_cvt_pk_bf16_f32 v144, v144, v145\n\t"
            "v_mfma_f32_16x16x32_bf16 v[176:179], v[56:59], v[212:215], v[176:179]\n\t"
            "v_cvt_pk_bf16_f32 v152, v152, v153\n\t"
            "v_cvt_pk_bf16_f32 v156, v156, v157\n\t"
            "v_mfma_f32_16x16x32_bf16 v[180:183], v[80:83], v[212:215], v[180:183]\n\t"
            "v_mul_f32 v148, v236, v148\n\t"
            "v_mul_f32 v149, v236, v149\n\t"
            "v_mfma_f32_16x16x32_bf16 v[184:187], v[104:107], v[212:215], v[184:187]\n\t"
            "v_mul_f32 v150, v236, v150\n\t"
            "v_mul_f32 v151, v236, v151\n\t"
            "v_mfma_f32_16x16x32_bf16 v[188:191], v[128:131], v[212:215], v[188:191]\n\t"
            "ds_read_b128 v[212:215], v233 offset:6400\n\t"
            "v_cvt_pk_bf16_f32 v145, v146, v147\n\t"
            "v_cvt_pk_bf16_f32 v153, v154, v155\n\t"
            "s_waitcnt lgkmcnt(2)\n\t"
            "v_mfma_f32_16x16x32_bf16 v[168:171], v[12:15], v[204:207], v[168:171]\n\t"
            "v_cvt_pk_bf16_f32 v157, v158, v159\n\t"
            "v_cvt_pk_bf16_f32 v146, v148, v149\n\t"
            "v_mfma_f32_16x16x32_bf16 v[172:175], v[36:39], v[204:207], v[172:175]\n\t"
            "v_cvt_pk_bf16_f32 v147, v150, v151\n\t"
            "v_cvt_pk_bf16_f32 v158, v160, v161\n\t"
            "v_mfma_f32_16x16x32_bf16 v[176:179], v[60:63], v[204:207], v[176:179]\n\t"
            "s_barrier\n\t"
            "v_cvt_pk_bf16_f32 v159, v162, v163\n\t"
            "v_cvt_pk_bf16_f32 v154, v164, v165\n\t"
            "v_mfma_f32_16x16x32_bf16 v[180:183], v[84:87], v[204:207], v[180:183]\n\t"
            "v_cvt_pk_bf16_f32 v155, v166, v167\n\t"
            "v_mul_f32 v236, 0x3e553b94, v236\n\t"
            "v_mfma_f32_16x16x32_bf16 v[184:187], v[108:111], v[204:207], v[184:187]\n\t"
            "v_mfma_f32_16x16x32_bf16 v[188:191], v[132:135], v[204:207], v[188:191]\n\t"
            "ds_read_b128 v[204:207], v232 offset:12288\n\t"
            "s_waitcnt lgkmcnt(2)\n\t"
            "v_mfma_f32_16x16x32_bf16 v[168:171], v[16:19], v[208:211], v[168:171]\n\t"
            "v_mfma_f32_16x16x32_bf16 v[172:175], v[40:43], v[208:211], v[172:175]\n\t"
            "v_mfma_f32_16x16x32_bf16 v[176:179], v[64:67], v[208:211], v[176:179]\n\t"
            "v_mfma_f32_16x16x32_bf16 v[180:183], v[88:91], v[208:211], v[180:183]\n\t"
            "v_mfma_f32_16x16x32_bf16 v[184:187], v[112:115], v[208:211], v[184:187]\n\t"
            "v_mfma_f32_16x16x32_bf16 v[188:191], v[136:139], v[208:211], v[188:191]\n\t"
            "ds_read_b128 v[208:211], v233 offset:12288\n\t"
            "s_waitcnt lgkmcnt(2)\n\t"
            "v_mfma_f32_16x16x32_bf16 v[168:171], v[20:23], v[212:215], v[168:171]\n\t"
            "v_mfma_f32_16x16x32_bf16 v[172:175], v[44:47], v[212:215], v[172:175]\n\t"
            "v_mfma_f32_16x16x32_bf16 v[176:179], v[68:71], v[212:215], v[176:179]\n\t"
            "v_mfma_f32_16x16x32_bf16 v[180:183], v[92:95], v[212:215], v[180:183]\n\t"
            "v_mfma_f32_16x16x32_bf16 v[184:187], v[116:119], v[212:215], v[184:187]\n\t"
            "v_mfma_f32_16x16x32_bf16 v[188:191], v[140:143], v[212:215], v[188:191]\n\t"
            "ds_read_b128 v[212:215], v232 offset:12416\n\t"
            "s_branch .Lwa_loop%=\n\t"
            ".Lwa_end%=:\n\t"
            "s_cmp_eq_u32 s44, 0\n\t"
            "s_cbranch_scc1 .Lwa_done%=\n\t"
            "s_cmp_lg_u32 s44, 2\n\t"
            "s_cbranch_scc1 .Lwa_go%=\n\t"
            "s_mov_b32 m0, s40\n\t"
            "v_lshlrev_b32 v232, 3, v248\n\t"
            "buffer_load_dword v232, s[36:39], s41 offen lds\n\t"
            "s_branch .Lwa_go%=\n\t"
            ".Lwa_done%=:\n\t"
            "s_setprio 2\n\t"
            "s_waitcnt lgkmcnt(0)\n\t"
            "s_nop 7\n\t"
            "v_mfma_f32_16x16x32_bf16 v[200:203], v[240:243], v[148:151], 0\n\t"
            "ds_read_b64 v[176:177], v247\n\t"
            "ds_read_b64 v[196:197], v247 offset:1536\n\t"
            "v_mfma_f32_16x16x32_bf16 v[200:203], v[240:243], v[164:167], v[200:203]\n\t"
            "ds_read_b64 v[220:221], v247 offset:3072\n\t"
            "v_add_u32 v235, s45, v248\n\t"
            "v_xor_b32 v234, s73, v254\n\t"
            "v_mov_b32 v179, 0\n\t"
            "v_mov_b32 v199, 0\n\t"
            "v_mov_b32 v223, 0\n\t"
            "v_mov_b32 v175, 0\n\t"
            "s_nop 0\n\t"
            "v_rcp_f32 v201, v200\n\t"
            "ds_read_b128 v[168:171], v235\n\t"
            "ds_read_b128 v[192:195], v235 offset:1536\n\t"
            "ds_read_u16 v178, v234\n\t"
            "ds_read_u16 v198, v234 offset:128\n\t"
            "ds_read_b128 v[216:219], v235 offset:3072\n\t"
            "ds_read_u16 v222, v234 offset:256\n\t"
            "v_mad_u32_u24 v234, v246, s75, v253\n\t"
            "s_waitcnt lgkmcnt(5)\n\t"
            "v_mfma_f32_16x16x32_bf16 v[224:227], v[168:171], v[148:151], 0\n\t"
            "s_waitcnt lgkmcnt(4)\n\t"
            "v_mfma_f32_16x16x32_bf16 v[212:215], v[192:195], v[148:151], 0\n\t"
            "s_waitcnt lgkmcnt(1)\n\t"
            "v_mfma_f32_16x16x32_bf16 v[148:151], v[216:219], v[148:151], 0\n\t"
            "v_mfma_f32_16x16x32_bf16 v[224:227], v[176:179], v[164:167], v[224:227]\n\t"
            "v_mfma_f32_16x16x32_bf16 v[212:215], v[196:199], v[164:167], v[212:215]\n\t"
            "s_waitcnt lgkmcnt(0)\n\t"
            "v_mfma_f32_16x16x32_bf16 v[148:151], v[220:223], v[164:167], v[148:151]\n\t"
            "s_nop 4\n\t"
            "v_mul_f32 v224, v201, v224\n\t"
            "v_mul_f32 v225, v201, v225\n\t"
            "v_mul_f32 v226, v201, v226\n\t"
            "v_mul_f32 v227, v201, v227\n\t"
            "v_cvt_pk_bf16_f32 v224, v224, v225\n\t"
            "v_cvt_pk_bf16_f32 v225, v226, v227\n\t"
            "v_mul_f32 v212, v201, v212\n\t"
            "v_mul_f32 v213, v201, v213\n\t"
            "v_mul_f32 v214, v201, v214\n\t"
            "v_mul_f32 v215, v201, v215\n\t"
            "v_cvt_pk_bf16_f32 v212, v212, v213\n\t"
            "v_cvt_pk_bf16_f32 v213, v214, v215\n\t"
            "v_mul_f32 v148, v201, v148\n\t"
            "v_mul_f32 v149, v201, v149\n\t"
            "v_mul_f32 v150, v201, v150\n\t"
            "v_mul_f32 v151, v201, v151\n\t"
            "v_mfma_f32_16x16x32_bf16 v[200:203], v[240:243], v[184:187], 0\n\t"
            "ds_write_b64 v245, v[224:225]\n\t"
            "v_mfma_f32_16x16x32_bf16 v[224:227], v[168:171], v[184:187], 0\n\t"
            "ds_write_b64 v245, v[212:213] offset:128\n\t"
            "v_mfma_f32_16x16x32_bf16 v[212:215], v[192:195], v[184:187], 0\n\t"
            "v_cvt_pk_bf16_f32 v148, v148, v149\n\t"
            "v_cvt_pk_bf16_f32 v149, v150, v151\n\t"
            "v_mfma_f32_16x16x32_bf16 v[184:187], v[216:219], v[184:187], 0\n\t"
            "ds_write_b64 v245, v[148:149] offset:256\n\t"
            "v_mfma_f32_16x16x32_bf16 v[200:203], v[240:243], v[188:191], v[200:203]\n\t"
            "v_mfma_f32_16x16x32_bf16 v[224:227], v[176:179], v[188:191], v[224:227]\n\t"
            "v_mfma_f32_16x16x32_bf16 v[212:215], v[196:199], v[188:191], v[212:215]\n\t"
            "v_mfma_f32_16x16x32_bf16 v[184:187], v[220:223], v[188:191], v[184:187]\n\t"
            "v_mfma_f32_16x16x32_bf16 v[188:191], v[192:195], v[180:183], 0\n\t"
            "v_mfma_f32_16x16x32_bf16 v[188:191], v[196:199], v[172:175], v[188:191]\n\t"
            "s_nop 2\n\t"
            "v_rcp_f32 v201, v200\n\t"
            "s_nop 0\n\t"
            "v_mul_f32 v224, v201, v224\n\t"
            "v_mul_f32 v225, v201, v225\n\t"
            "v_mul_f32 v226, v201, v226\n\t"
            "v_mul_f32 v227, v201, v227\n\t"
            "v_mul_f32 v212, v201, v212\n\t"
            "v_mul_f32 v213, v201, v213\n\t"
            "v_mul_f32 v214, v201, v214\n\t"
            "v_mul_f32 v215, v201, v215\n\t"
            "v_mul_f32 v184, v201, v184\n\t"
            "v_mul_f32 v185, v201, v185\n\t"
            "v_mul_f32 v186, v201, v186\n\t"
            "v_mul_f32 v187, v201, v187\n\t"
            "v_mfma_f32_16x16x32_bf16 v[200:203], v[240:243], v[180:183], 0\n\t"
            "v_cvt_pk_bf16_f32 v184, v184, v185\n\t"
            "v_cvt_pk_bf16_f32 v185, v186, v187\n\t"
            "v_mfma_f32_16x16x32_bf16 v[200:203], v[240:243], v[172:175], v[200:203]\n\t"
            "ds_read_b64 v[186:187], v234 offset:288\n\t"
            "ds_write_b64 v245, v[184:185] offset:6400\n\t"
            "ds_read_b64 v[184:185], v234 offset:256\n\t"
            "v_cvt_pk_bf16_f32 v224, v224, v225\n\t"
            "v_cvt_pk_bf16_f32 v225, v226, v227\n\t"
            "v_cvt_pk_bf16_f32 v212, v212, v213\n\t"
            "v_cvt_pk_bf16_f32 v213, v214, v215\n\t"
            "s_nop 0\n\t"
            "v_rcp_f32 v201, v200\n\t"
            "s_nop 0\n\t"
            "v_mul_f32 v188, v201, v188\n\t"
            "v_mul_f32 v189, v201, v189\n\t"
            "v_mul_f32 v190, v201, v190\n\t"
            "v_mul_f32 v191, v201, v191\n\t"
            "v_cvt_pk_bf16_f32 v188, v188, v189\n\t"
            "v_cvt_pk_bf16_f32 v189, v190, v191\n\t"
            "ds_read_b64 v[190:191], v234 offset:352\n\t"
            "ds_write_b64 v245, v[188:189] offset:12416\n\t"
            "ds_read_b64 v[188:189], v234 offset:320\n\t"
            "ds_write_b64 v245, v[224:225] offset:6144\n\t"
            "v_mfma_f32_16x16x32_bf16 v[224:227], v[168:171], v[180:183], 0\n\t"
            "ds_write_b64 v245, v[212:213] offset:6272\n\t"
            "v_mfma_f32_16x16x32_bf16 v[180:183], v[216:219], v[180:183], 0\n\t"
            "v_mfma_f32_16x16x32_bf16 v[224:227], v[176:179], v[172:175], v[224:227]\n\t"
            "v_mfma_f32_16x16x32_bf16 v[180:183], v[220:223], v[172:175], v[180:183]\n\t"
            "s_waitcnt lgkmcnt(5)\n\t"
            "v_mfma_f32_16x16x32_bf16 v[168:171], v[168:171], v[184:187], 0\n\t"
            "v_mfma_f32_16x16x32_bf16 v[192:195], v[192:195], v[184:187], 0\n\t"
            "v_mfma_f32_16x16x32_bf16 v[216:219], v[216:219], v[184:187], 0\n\t"
            "s_waitcnt lgkmcnt(2)\n\t"
            "v_mfma_f32_16x16x32_bf16 v[168:171], v[176:179], v[188:191], v[168:171]\n\t"
            "v_mfma_f32_16x16x32_bf16 v[192:195], v[196:199], v[188:191], v[192:195]\n\t"
            "v_mul_f32 v224, v201, v224\n\t"
            "v_mfma_f32_16x16x32_bf16 v[216:219], v[220:223], v[188:191], v[216:219]\n\t"
            "v_mul_f32 v225, v201, v225\n\t"
            "v_mul_f32 v226, v201, v226\n\t"
            "v_mul_f32 v227, v201, v227\n\t"
            "v_mul_f32 v180, v201, v180\n\t"
            "v_mul_f32 v181, v201, v181\n\t"
            "v_mul_f32 v182, v201, v182\n\t"
            "v_mul_f32 v183, v201, v183\n\t"
            "v_mfma_f32_16x16x32_bf16 v[200:203], v[240:243], v[184:187], 0\n\t"
            "v_cvt_pk_bf16_f32 v224, v224, v225\n\t"
            "v_cvt_pk_bf16_f32 v180, v180, v181\n\t"
            "v_mfma_f32_16x16x32_bf16 v[200:203], v[240:243], v[188:191], v[200:203]\n\t"
            "v_cvt_pk_bf16_f32 v225, v226, v227\n\t"
            "v_cvt_pk_bf16_f32 v181, v182, v183\n\t"
            "ds_write_b64 v245, v[224:225] offset:12288\n\t"
            "ds_write_b64 v245, v[180:181] offset:12544\n\t"
            "s_nop 3\n\t"
            "v_rcp_f32 v201, v200\n\t"
            "s_nop 0\n\t"
            "v_mul_f32 v168, v201, v168\n\t"
            "v_mul_f32 v169, v201, v169\n\t"
            "v_cvt_pk_bf16_f32 v168, v168, v169\n\t"
            "v_mul_f32 v192, v201, v192\n\t"
            "v_mul_f32 v193, v201, v193\n\t"
            "v_mul_f32 v216, v201, v216\n\t"
            "v_mul_f32 v217, v201, v217\n\t"
            "v_mul_f32 v170, v201, v170\n\t"
            "v_mul_f32 v171, v201, v171\n\t"
            "v_mul_f32 v194, v201, v194\n\t"
            "v_mul_f32 v195, v201, v195\n\t"
            "v_cvt_pk_bf16_f32 v192, v192, v193\n\t"
            "v_mul_f32 v218, v201, v218\n\t"
            "v_mul_f32 v219, v201, v219\n\t"
            "v_cvt_pk_bf16_f32 v216, v216, v217\n\t"
            "v_xor_b32 v202, s73, v255\n\t"
            "v_cvt_pk_bf16_f32 v169, v170, v171\n\t"
            "v_cvt_pk_bf16_f32 v193, v194, v195\n\t"
            "v_cvt_pk_bf16_f32 v217, v218, v219\n\t"
            "s_mov_b64 exec, s[68:69]\n\t"
            "ds_write_b64 v202, v[168:169]\n\t"
            "ds_write_b64 v202, v[192:193] offset:128\n\t"
            "ds_write_b64 v202, v[216:217] offset:256\n\t"
            "s_mov_b64 exec, -1\n\t"
            "s_waitcnt lgkmcnt(0)\n\t"
            "v_mov_b32 v144, s42\n\t"
            "v_bfrev_b32 v145, -2\n\t"
            "ds_write_b32 v144, v145\n\t"
            "s_waitcnt lgkmcnt(0)\n\t"
            "s_nop 7\n\t"
            : "+{v[232:235]}"(pl),
              "+{s44}"(s44), "+{s48}"(s48), "+{s70}"(s70), "+{s72}"(s72), "+{s73}"(s73), "+{s74}"(s74), "+{s76}"(s76), "+{s77}"(s77), "+{s[68:69]}"(s68)
            : "{v244}"(pE_), "{v245}"(aO_), "{v246}"(sel_), "{v247}"(aVB_), "{v248}"(pST_), "{v249}"(aRSw_), "{v250}"(aPLw_), "{v251}"(aRSr_), "{v252}"(g16_), "{v253}"(g8_), "{v254}"(loVL_), "{v255}"(loOL_), "{v[240:243]}"(ones4), "{s47}"((ldsb + X_OFF) ^ (ldsb + X1_OFF)),
              "{s[58:59]}"(0xFFFFull), "{s[60:61]}"(0xFFFF0000ull), "{s[62:63]}"(0xFFFF00000000ull), "{s[64:65]}"(0xFFFF000000000000ull),
              "{s[66:67]}"(0x1FFFFFFFFFFFFull), "{s75}"(rsb), "{s45}"(ldsb + V_OFF + w * 4608),
              "{s[28:31]}"(wr), "{s33}"(wave * 36864), "{s46}"(ldsb + PG_OFF), "{s42}"(ldsb + PG_OFF + w * 4), "{s56}"(ldsb + O_OFF), "{s57}"(16 - 4 * w), "{s[36:39]}"(tr), "{s41}"(to), "{s40}"(ldsb + X_OFF + 18432), "{s34}"(SH_OFF + bx * 12288 + w * 3072), "{s35}"(ldsb + O_OFF + w * 3072),
              "{s[50:51]}"(0x2222222222222222ull), "{s[52:53]}"(0x4444444444444444ull), "{s[54:55]}"(0x8888888888888888ull)
            : "v0", "v1", "v2", "v3", "v4", "v5", "v6", "v7", "v8", "v9", "v10", "v11", "v12", "v13", "v14", "v15", "v16", "v17", "v18", "v19", "v20", "v21", "v22", "v23", "v24", "v25", "v26", "v27", "v28", "v29", "v30", "v31", "v32", "v33", "v34", "v35", "v36", "v37", "v38", "v39", "v40", "v41", "v42", "v43", "v44", "v45", "v46", "v47", "v48", "v49", "v50", "v51", "v52", "v53", "v54", "v55", "v56", "v57", "v58", "v59", "v60", "v61", "v62", "v63", "v64", "v65", "v66", "v67", "v68", "v69", "v70", "v71", "v72", "v73", "v74", "v75", "v76", "v77", "v78", "v79", "v80", "v81", "v82", "v83", "v84", "v85", "v86", "v87", "v88", "v89", "v90", "v91", "v92", "v93", "v94", "v95", "v96", "v97", "v98", "v99", "v100", "v101", "v102", "v103", "v104", "v105", "v106", "v107", "v108", "v109", "v110", "v111", "v112", "v113", "v114", "v115", "v116", "v117", "v118", "v119", "v120", "v121", "v122", "v123", "v124", "v125", "v126", "v127", "v128", "v129", "v130", "v131", "v132", "v133", "v134", "v135", "v136", "v137", "v138", "v139", "v140", "v141", "v142", "v143", "v144", "v145", "v146", "v147", "v148", "v149", "v150", "v151", "v152", "v153", "v154", "v155", "v156", "v157", "v158", "v159", "v160", "v161", "v162", "v163", "v164", "v165", "v166", "v167", "v168", "v169", "v170", "v171", "v172", "v173", "v174", "v175", "v176", "v177", "v178", "v179", "v180", "v181", "v182", "v183", "v184", "v185", "v186", "v187", "v188", "v189", "v190", "v191", "v192", "v193", "v194", "v195", "v196", "v197", "v198", "v199", "v200", "v201", "v202", "v203", "v204", "v205", "v206", "v207", "v208", "v209", "v210", "v211", "v212", "v213", "v214", "v215", "v216", "v217", "v218", "v219", "v220", "v221", "v222", "v223", "v224", "v225", "v226", "v227", "v228", "v229", "v230", "v231", "v236", "v237", "v238", "v239", "s49", "vcc", "scc", "m0", "memory");
        }
        asm volatile("wk_code_end:");

        if (q0 == 40 && wave == 0) {
#pragma unroll 1
            for (int it = 0; it < SPIN_MAX && __hip_atomic_load((gu32*)((char*)wf + CN_OFF), RLX_AGENT) != (unsigned)GRID; ++it) __builtin_amdgcn_s_sleep(8);
            __hip_atomic_store((gu32*)((char*)wf + EP_OFF), __hip_atomic_load((gu32*)((char*)wf + EP_OFF), RLX_AGENT) + 1u, RLX_AGENT);
        }
    } else {
        ROLE_SETUP
        {
            LANE_SETUP
            const bool isV = wave < 6;
            CONV_CONSTS(0)
            const unsigned aCA_ = ldsb + CT_OFF + lane * 16, rsb = ldsb + RS_BASE, pE = fE - X_OFF;
            const unsigned aRSw = (lane < 48 ? rsb : ldsb + RS_OFF + 2304 - 192) + lane * 4, aRSr = rsb + (lane & 48);
            unsigned pr[3];
#pragma unroll
            for (int k = 0; k < 3; ++k) {
                const int c8 = 12 * (w - 2) + 4 * k + g;
                const int ws = c8 / 6, r6 = c8 - 6 * ws;
                pr[k] = l15 * 384 + (r6 >> 1) * 128 + (((2 * ws + (r6 & 1)) ^ sw) << 4);
            }
            unsigned r242 = isV ? pE : ldsb + O_OFF + pE, r243 = isV ? ldsb + V_OFF + w * 9216 + lane * 16 : (ldsb + O_OFF + pE) ^ 64;
            u32x4 r244 = {isV ? ldsb + V_OFF + w * 9216 + 1024 + lane * 8 : pr[0], isV ? aRSr : pr[1], isV ? aRSw : pr[2], (unsigned)lane * 16};
            const int hd = isV ? 2 * w : 2 * w - 3;
            const unsigned vq = ldsb + OL_OFF + l15 * 384 + (((2 * hd + (g >> 1)) ^ sw) << 4) + 8 * (g & 1);
            u32x4 f1 = {16 + l15 < cnt ? vq + 6144 : ldsb + OL_OFF + 22 * 384 + g * 8, pE, ldsb + V_OFF + hd * 4608 + lane * 16, ldsb + V_OFF + hd * 4608 + 1024 + lane * 8};
            u32x4 f2 = {aRSw, aRSr, rsb + l15 * 4, 0u};
            const unsigned lB = ldsb + LB_OFF(l15 & 7) + (g >> 1) * 1536 + (g & 1) * 768 + 96 * w;
            const unsigned long long ob = (unsigned long long)out, xb = (unsigned long long)((const char*)x - XBIAS), wb = (unsigned long long)wf;
            const u32x4 orr = {(unsigned)ob, (unsigned)(ob >> 32) & 0xffffu, (unsigned)(IMG * IMG * CH * 2), 0x00020000u};
            const u32x4 xr = {(unsigned)xb, (unsigned)(xb >> 32) & 0xffffu, (unsigned)(IMG * IMG * CH * 2 + XBIAS), 0x00020000u};
            const u32x4 wr = {(unsigned)wb, (unsigned)(wb >> 32) & 0xffffu, (unsigned)WS_BYTES, 0x00020000u};
            unsigned s45 = OOB, s46 = OOB, s47 = (en0 & 0x7ffffffu) + 96 * w;
            unsigned s48 = ldsb + X1_OFF, s67 = ldsb + X_OFF, s76 = ldsb + WL_OFF + 4, s56 = en0 >> 30;
            unsigned s21 = SH_OFF + bx * 12288, s44 = ldsb + PG_OFF + w * 4, s70 = isV ? wave * 36864 : (wave - 2) * 36864 + 18432;
            unsigned long long xp = 0, sp = 0;
#pragma unroll
            for (int v = 0; v < 4; ++v) {
                const unsigned r = (CROW >> (8 * v)) & 0xFF;
                xp |= (unsigned long long)XPAT(r) << (16 * v);
                sp |= (unsigned long long)SPAT(r) << (16 * v);
            }
#define B_VIN "{v250}"(pXw_), "{v251}"(pSTw_), "{v252}"(aCA_), "{v253}"(aB_), "{v254}"(aA_), "{v255}"(idx_), "{v[238:241]}"(sel4)
#define B_SIN "{s[40:43]}"(orr), "{s[72:75]}"(xr), "{s39}"(0x401fffffu), "{s[28:31]}"(wr), "{s33}"(wave * 36864), "{s78}"(96 * w), \
              "{s27}"(96 * w - 384), "{s[50:51]}"(xp), "{s[52:53]}"(sp), "{s54}"(CLANE), "{s55}"(CROW & 0x0F0F0F0Fu), "{s[34:35]}"(0x0001000100010001ull), \
              "{s[60:61]}"(0x2222222222222222ull), "{s[62:63]}"(0x4444444444444444ull), "{s[64:65]}"(0x8888888888888888ull), \
              "{s77}"(w == 2 ? 768 : 6 * ROWBYTES + 768), "{s25}"(w == 2 ? 3 * ROWBYTES : OOB), "{s32}"(ldsb + X_OFF + 18432), \
              "{s24}"(ldsb + WL_OFF + 12 + 4 * (cnt - 5)), "{s66}"(ldsb + w * 8192), "{s71}"((ldsb + X_OFF) ^ (ldsb + X1_OFF)), \
              "{s23}"(ldsb + WL_OFF), "{s22}"(ldsb + O_OFF), \
              "{s20}"(6 * ROWBYTES + 2304 + 192 * (w & 1)), "{s19}"(2048 + (w & 1) * 1024)
            asm volatile("" :: B_VIN, "{v242}"(r242), "{v243}"(r243), "{v[244:247]}"(r244), "{v[132:135]}"(f1), "{v[136:139]}"(f2), "v"(lB), "v"(vq));
            asm volatile("" :: B_SIN);
            asm volatile("s_waitcnt vmcnt(9)" ::: "memory");
            unsigned lm = *(const unsigned*)(smem + TP_OFF + 4096 + w * 256 + ((l15 & 7) * 4 + g) * 4);
            lm = l15 < 8 ? lm : OOB + 47;
            const int row = lm & 63;
            u32x4 f0 = {XPAT(row) + (unsigned)(O_OFF - X_OFF), SPAT(row) + (unsigned)(STL_OFF - X_OFF - 18816), lB, vq};
            asm volatile(
            "s_setprio 3\n\t"
            "s_waitcnt lgkmcnt(0)\n\t"
            "s_barrier\n\t"
            "s_nop 7\n\t"
            "ds_read_b128 v[168:171], v254\n\t"
            "ds_read_b64 v[144:145], v130\n\t"
            "ds_read_b64 v[146:147], v130 offset:384\n\t"
            "ds_read_b64 v[148:149], v130 offset:3072\n\t"
            "ds_read_b64 v[150:151], v130 offset:3456\n\t"
            "ds_read_b128 v[172:175], v254 offset:64\n\t"
            "ds_read_b64 v[152:153], v130 offset:8\n\t"
            "ds_read_b64 v[154:155], v130 offset:392\n\t"
            "ds_read_b64 v[156:157], v130 offset:3080\n\t"
            "ds_read_b64 v[158:159], v130 offset:3464\n\t"
            "ds_read_b64 v[160:161], v130 offset:16\n\t"
            "ds_read_b64 v[162:163], v130 offset:400\n\t"
            "ds_read_b64 v[164:165], v130 offset:3088\n\t"
            "ds_read_b64 v[166:167], v130 offset:3472\n\t"
            "v_mov_b64 v[176:177], 0\n\t"
            "v_mov_b64 v[178:179], 0\n\t"
            "v_mov_b64 v[180:181], 0\n\t"
            "v_mov_b64 v[182:183], 0\n\t"
            "v_mov_b64 v[184:185], 0\n\t"
            "v_mov_b64 v[186:187], 0\n\t"
            "v_mov_b64 v[188:189], 0\n\t"
            "v_mov_b64 v[190:191], 0\n\t"
            "v_add_u32 v236, s67, v128\n\t"
            "v_xor_b32 v237, 16, v236\n\t"
            "v_add_u32 v248, s67, v129\n\t"
            "s_waitcnt lgkmcnt(9)\n\t"
            "v_smfmac_f32_16x16x64_bf16 v[176:179], v[168:171], v[144:151], v255\n\t"
            "ds_read_b128 v[168:171], v254 offset:128\n\t"
            "ds_read_b64 v[144:145], v130 offset:24\n\t"
            "s_waitcnt lgkmcnt(6)\n\t"
            "v_smfmac_f32_16x16x64_bf16 v[180:183], v[172:175], v[152:159], v255\n\t"
            "ds_read_b64 v[146:147], v130 offset:408\n\t"
            "ds_read_b64 v[148:149], v130 offset:3096\n\t"
            "ds_read_b64 v[150:151], v130 offset:3480\n\t"
            "ds_read_b128 v[172:175], v254 offset:192\n\t"
            "ds_read_b64 v[152:153], v130 offset:32\n\t"
            "ds_read_b64 v[154:155], v130 offset:416\n\t"
            "ds_read_b64 v[156:157], v130 offset:3104\n\t"
            "ds_read_b64 v[158:159], v130 offset:3488\n\t"
            "v_cvt_pk_bf16_f32 v176, v176, v177\n\t"
            "v_cvt_pk_bf16_f32 v177, v178, v179\n\t"
            "v_cvt_pk_bf16_f32 v178, v180, v181\n\t"
            "v_cvt_pk_bf16_f32 v179, v182, v183\n\t"
            "v_mfma_f32_4x4x4_16b_bf16 v[196:199], v[176:177], v[176:177], 0\n\t"
            "ds_write_b128 v236, v[176:179]\n\t"
            "v_mfma_f32_16x16x32_bf16 v[192:195], v[238:241], v[176:179], 0\n\t"
            "v_mov_b64 v[176:177], 0\n\t"
            "v_mov_b64 v[180:181], 0\n\t"
            "v_mfma_f32_4x4x4_16b_bf16 v[196:199], v[178:179], v[178:179], v[196:199]\n\t"
            "v_mov_b64 v[178:179], 0\n\t"
            "v_mov_b64 v[182:183], 0\n\t"
            "s_waitcnt lgkmcnt(10)\n\t"
            "v_smfmac_f32_16x16x64_bf16 v[184:187], v[168:171], v[160:167], v255\n\t"
            "ds_read_b128 v[168:171], v254 offset:256\n\t"
            "ds_read_b64 v[160:161], v130 offset:40\n\t"
            "ds_read_b64 v[162:163], v130 offset:424\n\t"
            "ds_read_b64 v[164:165], v130 offset:3112\n\t"
            "ds_read_b64 v[166:167], v130 offset:3496\n\t"
            "s_waitcnt lgkmcnt(10)\n\t"
            "v_smfmac_f32_16x16x64_bf16 v[188:191], v[172:175], v[144:151], v255\n\t"
            "ds_read_b128 v[172:175], v254 offset:320\n\t"
            "ds_read_b64 v[144:145], v130 offset:48\n\t"
            "ds_read_b64 v[146:147], v130 offset:432\n\t"
            "ds_read_b64 v[148:149], v130 offset:3120\n\t"
            "ds_read_b64 v[150:151], v130 offset:3504\n\t"
            "v_cvt_pk_bf16_f32 v184, v184, v185\n\t"
            "v_cvt_pk_bf16_f32 v185, v186, v187\n\t"
            "s_nop 0\n\t"
            "v_cvt_pk_bf16_f32 v186, v188, v189\n\t"
            "v_cvt_pk_bf16_f32 v187, v190, v191\n\t"
            "v_mfma_f32_4x4x4_16b_bf16 v[196:199], v[184:185], v[184:185], v[196:199]\n\t"
            "s_waitcnt lgkmcnt(14)\n\t"
            "ds_write_b128 v237, v[184:187]\n\t"
            "v_mfma_f32_16x16x32_bf16 v[192:195], v[238:241], v[184:187], v[192:195]\n\t"
            "v_mov_b64 v[184:185], 0\n\t"
            "v_mov_b64 v[188:189], 0\n\t"
            "v_mfma_f32_4x4x4_16b_bf16 v[196:199], v[186:187], v[186:187], v[196:199]\n\t"
            "v_mov_b64 v[186:187], 0\n\t"
            "v_mov_b64 v[190:191], 0\n\t"
            "s_waitcnt lgkmcnt(10)\n\t"
            "v_smfmac_f32_16x16x64_bf16 v[176:179], v[168:171], v[152:159], v255\n\t"
            "ds_read_b128 v[168:171], v254 offset:384\n\t"
            "ds_read_b64 v[152:153], v130 offset:56\n\t"
            "ds_read_b64 v[154:155], v130 offset:440\n\t"
            "ds_read_b64 v[156:157], v130 offset:3128\n\t"
            "ds_read_b64 v[158:159], v130 offset:3512\n\t"
            "s_waitcnt lgkmcnt(10)\n\t"
            "v_smfmac_f32_16x16x64_bf16 v[180:183], v[172:175], v[160:167], v255\n\t"
            "ds_read_b128 v[172:175], v254 offset:448\n\t"
            "ds_read_b64 v[160:161], v130 offset:64\n\t"
            "ds_read_b64 v[162:163], v130 offset:448\n\t"
            "ds_read_b64 v[164:165], v130 offset:3136\n\t"
            "ds_read_b64 v[166:167], v130 offset:3520\n\t"
            "v_cvt_pk_bf16_f32 v176, v176, v177\n\t"
            "v_cvt_pk_bf16_f32 v177, v178, v179\n\t"
            "s_nop 0\n\t"
            "v_cvt_pk_bf16_f32 v178, v180, v181\n\t"
            "v_cvt_pk_bf16_f32 v179, v182, v183\n\t"
            "v_mfma_f32_4x4x4_16b_bf16 v[196:199], v[176:177], v[176:177], v[196:199]\n\t"
            "s_waitcnt lgkmcnt(14)\n\t"
            "ds_write_b128 v236, v[176:179] offset:128\n\t"
            "v_mfma_f32_16x16x32_bf16 v[192:195], v[238:241], v[176:179], v[192:195]\n\t"
            "v_mov_b64 v[176:177], 0\n\t"
            "v_mov_b64 v[180:181], 0\n\t"
            "v_mfma_f32_4x4x4_16b_bf16 v[196:199], v[178:179], v[178:179], v[196:199]\n\t"
            "v_mov_b64 v[178:179], 0\n\t"
            "v_mov_b64 v[182:183], 0\n\t"
            "s_waitcnt lgkmcnt(10)\n\t"
            "v_smfmac_f32_16x16x64_bf16 v[184:187], v[168:171], v[144:151], v255\n\t"
            "ds_read_b128 v[168:171], v254 offset:512\n\t"
            "ds_read_b64 v[144:145], v130 offset:72\n\t"
            "ds_read_b64 v[146:147], v130 offset:456\n\t"
            "ds_read_b64 v[148:149], v130 offset:3144\n\t"
            "ds_read_b64 v[150:151], v130 offset:3528\n\t"
            "s_waitcnt lgkmcnt(10)\n\t"
            "v_smfmac_f32_16x16x64_bf16 v[188:191], v[172:175], v[152:159], v255\n\t"
            "ds_read_b128 v[172:175], v254 offset:576\n\t"
            "ds_read_b64 v[152:153], v130 offset:80\n\t"
            "ds_read_b64 v[154:155], v130 offset:464\n\t"
            "ds_read_b64 v[156:157], v130 offset:3152\n\t"
            "ds_read_b64 v[158:159], v130 offset:3536\n\t"
            "v_cvt_pk_bf16_f32 v184, v184, v185\n\t"
            "v_cvt_pk_bf16_f32 v185, v186, v187\n\t"
            "s_nop 0\n\t"
            "v_cvt_pk_bf16_f32 v186, v188, v189\n\t"
            "v_cvt_pk_bf16_f32 v187, v190, v191\n\t"
            "v_mfma_f32_4x4x4_16b_bf16 v[196:199], v[184:185], v[184:185], v[196:199]\n\t"
            "s_waitcnt lgkmcnt(14)\n\t"
            "ds_write_b128 v237, v[184:187] offset:128\n\t"
            "v_mfma_f32_16x16x32_bf16 v[192:195], v[238:241], v[184:187], v[192:195]\n\t"
            "v_mov_b64 v[184:185], 0\n\t"
            "v_mov_b64 v[188:189], 0\n\t"
            "v_mfma_f32_4x4x4_16b_bf16 v[196:199], v[186:187], v[186:187], v[196:199]\n\t"
            "v_mov_b64 v[186:187], 0\n\t"
            "v_mov_b64 v[190:191], 0\n\t"
            "s_waitcnt lgkmcnt(10)\n\t"
            "v_smfmac_f32_16x16x64_bf16 v[176:179], v[168:171], v[160:167], v255\n\t"
            "ds_read_b128 v[168:171], v254 offset:640\n\t"
            "ds_read_b64 v[160:161], v130 offset:88\n\t"
            "ds_read_b64 v[162:163], v130 offset:472\n\t"
            "ds_read_b64 v[164:165], v130 offset:3160\n\t"
            "ds_read_b64 v[166:167], v130 offset:3544\n\t"
            "s_waitcnt lgkmcnt(10)\n\t"
            "v_smfmac_f32_16x16x64_bf16 v[180:183], v[172:175], v[144:151], v255\n\t"
            "ds_read_b128 v[172:175], v254 offset:704\n\t"
            "v_cvt_pk_bf16_f32 v176, v176, v177\n\t"
            "v_cvt_pk_bf16_f32 v177, v178, v179\n\t"
            "s_nop 1\n\t"
            "v_mfma_f32_4x4x4_16b_bf16 v[196:199], v[176:177], v[176:177], v[196:199]\n\t"
            "s_nop 1\n\t"
            "v_cvt_pk_bf16_f32 v178, v180, v181\n\t"
            "v_cvt_pk_bf16_f32 v179, v182, v183\n\t"
            "s_nop 1\n\t"
            "v_mfma_f32_4x4x4_16b_bf16 v[196:199], v[178:179], v[178:179], v[196:199]\n\t"
            "ds_write_b128 v236, v[176:179] offset:256\n\t"
            "s_waitcnt lgkmcnt(6)\n\t"
            "v_smfmac_f32_16x16x64_bf16 v[184:187], v[168:171], v[152:159], v255\n\t"
            "v_mfma_f32_16x16x32_bf16 v[192:195], v[238:241], v[176:179], v[192:195]\n\t"
            "s_waitcnt lgkmcnt(1)\n\t"
            "v_smfmac_f32_16x16x64_bf16 v[188:191], v[172:175], v[160:167], v255\n\t"
            "s_nop 4\n\t"
            "v_cvt_pk_bf16_f32 v184, v184, v185\n\t"
            "v_cvt_pk_bf16_f32 v185, v186, v187\n\t"
            "s_nop 1\n\t"
            "v_mfma_f32_4x4x4_16b_bf16 v[196:199], v[184:185], v[184:185], v[196:199]\n\t"
            "v_cvt_pk_bf16_f32 v186, v188, v189\n\t"
            "v_cvt_pk_bf16_f32 v187, v190, v191\n\t"
            "s_nop 1\n\t"
            "v_mfma_f32_4x4x4_16b_bf16 v[196:199], v[186:187], v[186:187], v[196:199]\n\t"
            "ds_write_b128 v237, v[184:187] offset:256\n\t"
            "v_mfma_f32_16x16x32_bf16 v[192:195], v[238:241], v[184:187], v[192:195]\n\t"
            "s_nop 7\n\t"
            "v_cndmask_b32_e64 v193, v196, v197, s[60:61]\n\t"
            "v_cndmask_b32_e64 v193, v193, v198, s[62:63]\n\t"
            "v_cndmask_b32_e64 v193, v193, v199, s[64:65]\n\t"
            "ds_write_b64 v248, v[192:193] offset:18816\n\t"
            "s_waitcnt lgkmcnt(0)\n\t"
            "s_nop 7\n\t"
            "v_mov_b32 v144, s44\n\t"
            "v_mov_b32 v145, 0x0fffffff\n\t"
            "ds_write_b32 v144, v145\n\t"
            "s_nop 7\n\t"
            "ds_read_b128 v[168:171], v254\n\t"
            "ds_read_b128 v[172:175], v254 offset:64\n\t"
            "s_waitcnt vmcnt(0)\n\t"
            "ds_read_b64 v[144:145], v253\n\t"
            "ds_read_b64 v[146:147], v253 offset:96\n\t"
            "ds_read_b64 v[148:149], v253 offset:1744\n\t"
            "ds_read_b64 v[150:151], v253 offset:1840\n\t"
            "ds_read_b64 v[152:153], v253 offset:8\n\t"
            "ds_read_b64 v[154:155], v253 offset:104\n\t"
            "ds_read_b64 v[156:157], v253 offset:1752\n\t"
            "ds_read_b64 v[158:159], v253 offset:1848\n\t"
            "ds_read_b64 v[160:161], v253 offset:16\n\t"
            "ds_read_b64 v[162:163], v253 offset:112\n\t"
            "ds_read_b64 v[164:165], v253 offset:1760\n\t"
            "ds_read_b64 v[166:167], v253 offset:1856\n\t"
            "v_mov_b64 v[176:177], 0\n\t"
            "v_mov_b64 v[178:179], 0\n\t"
            "v_mov_b64 v[180:181], 0\n\t"
            "v_mov_b64 v[182:183], 0\n\t"
            "v_mov_b32 v249, s76\n\t"
            "v_mov_b64 v[184:185], 0\n\t"
            "v_mov_b64 v[186:187], 0\n\t"
            "v_mov_b64 v[188:189], 0\n\t"
            "v_mov_b64 v[190:191], 0\n\t"
            "v_add_u32 v236, s67, v250\n\t"
            "v_add_u32 v248, s67, v251\n\t"
            "ds_read_b32 v249, v249\n\t"
            "s_waitcnt lgkmcnt(9)\n\t"
            "v_smfmac_f32_16x16x64_bf16 v[176:179], v[168:171], v[144:151], v255\n\t"
            "ds_read_b128 v[168:171], v254 offset:128\n\t"
            "s_waitcnt lgkmcnt(6)\n\t"
            "v_smfmac_f32_16x16x64_bf16 v[180:183], v[172:175], v[152:159], v255\n\t"
            "ds_read_b64 v[144:145], v253 offset:24\n\t"
            "ds_read_b64 v[146:147], v253 offset:120\n\t"
            "ds_read_b64 v[148:149], v253 offset:1768\n\t"
            "ds_read_b64 v[150:151], v253 offset:1864\n\t"
            "ds_read_b128 v[172:175], v254 offset:192\n\t"
            "ds_read_b64 v[152:153], v253 offset:32\n\t"
            "ds_read_b64 v[154:155], v253 offset:128\n\t"
            "ds_read_b64 v[156:157], v253 offset:1776\n\t"
            "ds_read_b64 v[158:159], v253 offset:1872\n\t"
            "v_cvt_pk_bf16_f32 v176, v176, v177\n\t"
            "v_cvt_pk_bf16_f32 v177, v178, v179\n\t"
            "v_cvt_pk_bf16_f32 v178, v180, v181\n\t"
            "v_cvt_pk_bf16_f32 v179, v182, v183\n\t"
            "v_mfma_f32_4x4x4_16b_bf16 v[196:199], v[176:177], v[176:177], 0\n\t"
            "v_mov_b64 v[180:181], 0\n\t"
            "v_mfma_f32_16x16x32_bf16 v[192:195], v[238:241], v[176:179], 0\n\t"
            "s_waitcnt lgkmcnt(10)\n\t"
            "v_readfirstlane_b32 s26, v249\n\t"
            "v_mov_b64 v[182:183], 0\n\t"
            "v_mfma_f32_4x4x4_16b_bf16 v[196:199], v[178:179], v[178:179], v[196:199]\n\t"
            "s_and_b32 s68, s26, 0x7ffffff\n\t"
            "s_add_u32 s68, s68, s78\n\t"
            "s_and_b32 s69, s26, 0x38000000\n\t"
            "s_or_b32 s69, s69, s39\n\t"
            "s_bitcmp1_b32 s26, 27\n\t"
            "s_cselect_b32 s79, 2.0, s68\n\t"
            "s_and_b32 s57, s56, 3\n\t"
            "s_lshl_b32 s58, s57, 3\n\t"
            "s_lshr_b32 m0, s54, s58\n\t"
            "s_waitcnt lgkmcnt(9)\n\t"
            "v_smfmac_f32_16x16x64_bf16 v[184:187], v[168:171], v[160:167], v255\n\t"
            "s_lshl_b32 s58, s57, 4\n\t"
            "s_lshr_b64 s[36:37], s[50:51], s58\n\t"
            "s_and_b32 s57, s36, 0xffff\n\t"
            "s_add_u32 s57, s57, s67\n\t"
            "ds_read_b128 v[168:171], v254 offset:256\n\t"
            "s_waitcnt lgkmcnt(5)\n\t"
            "v_smfmac_f32_16x16x64_bf16 v[188:191], v[172:175], v[144:151], v255\n\t"
            "s_lshr_b64 s[36:37], s[52:53], s58\n\t"
            "s_and_b32 s58, s36, 0xffff\n\t"
            "s_add_u32 s58, s58, s67\n\t"
            "ds_read_b64 v[160:161], v253 offset:40\n\t"
            "ds_read_b64 v[162:163], v253 offset:136\n\t"
            "ds_read_b64 v[164:165], v253 offset:1784\n\t"
            "ds_read_b64 v[166:167], v253 offset:1880\n\t"
            "ds_read_b128 v[172:175], v254 offset:320\n\t"
            "ds_read_b64 v[144:145], v253 offset:48\n\t"
            "ds_read_b64 v[146:147], v253 offset:144\n\t"
            "ds_read_b64 v[148:149], v253 offset:1792\n\t"
            "ds_read_b64 v[150:151], v253 offset:1888\n\t"
            "s_and_b32 s59, s50, 0xffff\n\t"
            "s_add_u32 s59, s59, s67\n\t"
            "s_and_b32 s38, s52, 0xffff\n\t"
            "s_add_u32 s38, s38, s67\n\t"
            "v_writelane_b32 v236, s59, m0\n\t"
            "v_writelane_b32 v236, s57, 63\n\t"
            "v_cvt_pk_bf16_f32 v184, v184, v185\n\t"
            "v_cvt_pk_bf16_f32 v185, v186, v187\n\t"
            "v_xor_b32 v237, 16, v236\n\t"
            "v_cvt_pk_bf16_f32 v186, v188, v189\n\t"
            "v_mfma_f32_4x4x4_16b_bf16 v[196:199], v[184:185], v[184:185], v[196:199]\n\t"
            "ds_write_b128 v236, v[176:179]\n\t"
            "v_mov_b64 v[176:177], 0\n\t"
            "v_mov_b64 v[178:179], 0\n\t"
            "v_cvt_pk_bf16_f32 v187, v190, v191\n\t"
            "v_mov_b64 v[188:189], 0\n\t"
            "s_waitcnt lgkmcnt(10)\n\t"
            "v_smfmac_f32_16x16x64_bf16 v[176:179], v[168:171], v[152:159], v255\n\t"
            "ds_read_b128 v[168:171], v254 offset:384\n\t"
            "v_mfma_f32_16x16x32_bf16 v[192:195], v[238:241], v[184:187], v[192:195]\n\t"
            "ds_read_b64 v[152:153], v253 offset:56\n\t"
            "ds_read_b64 v[154:155], v253 offset:152\n\t"
            "s_waitcnt lgkmcnt(8)\n\t"
            "v_smfmac_f32_16x16x64_bf16 v[180:183], v[172:175], v[160:167], v255\n\t"
            "ds_read_b64 v[156:157], v253 offset:1800\n\t"
            "v_mfma_f32_4x4x4_16b_bf16 v[196:199], v[186:187], v[186:187], v[196:199]\n\t"
            "ds_read_b64 v[158:159], v253 offset:1896\n\t"
            "ds_read_b128 v[172:175], v254 offset:448\n\t"
            "ds_read_b64 v[160:161], v253 offset:64\n\t"
            "ds_read_b64 v[162:163], v253 offset:160\n\t"
            "ds_read_b64 v[164:165], v253 offset:1808\n\t"
            "ds_read_b64 v[166:167], v253 offset:1904\n\t"
            "s_waitcnt lgkmcnt(14)\n\t"
            "ds_write_b128 v237, v[184:187]\n\t"
            "v_mov_b64 v[184:185], 0\n\t"
            "v_mov_b64 v[186:187], 0\n\t"
            "v_mov_b64 v[190:191], 0\n\t"
            "v_cvt_pk_bf16_f32 v176, v176, v177\n\t"
            "v_cvt_pk_bf16_f32 v177, v178, v179\n\t"
            "v_cvt_pk_bf16_f32 v178, v180, v181\n\t"
            "v_cvt_pk_bf16_f32 v179, v182, v183\n\t"
            "v_mfma_f32_4x4x4_16b_bf16 v[196:199], v[176:177], v[176:177], v[196:199]\n\t"
            "s_waitcnt lgkmcnt(14)\n\t"
            "ds_write_b128 v236, v[176:179] offset:128\n\t"
            "s_waitcnt lgkmcnt(11)\n\t"
            "v_smfmac_f32_16x16x64_bf16 v[184:187], v[168:171], v[144:151], v255\n\t"
            "ds_read_b128 v[168:171], v254 offset:512\n\t"
            "v_mfma_f32_16x16x32_bf16 v[192:195], v[238:241], v[176:179], v[192:195]\n\t"
            "ds_read_b64 v[144:145], v253 offset:72\n\t"
            "ds_read_b64 v[146:147], v253 offset:168\n\t"
            "s_waitcnt lgkmcnt(9)\n\t"
            "v_smfmac_f32_16x16x64_bf16 v[188:191], v[172:175], v[152:159], v255\n\t"
            "ds_read_b64 v[148:149], v253 offset:1816\n\t"
            "v_mfma_f32_4x4x4_16b_bf16 v[196:199], v[178:179], v[178:179], v[196:199]\n\t"
            "ds_read_b64 v[150:151], v253 offset:1912\n\t"
            "ds_read_b128 v[172:175], v254 offset:576\n\t"
            "ds_read_b64 v[152:153], v253 offset:80\n\t"
            "ds_read_b64 v[154:155], v253 offset:176\n\t"
            "ds_read_b64 v[156:157], v253 offset:1824\n\t"
            "s_waitcnt lgkmcnt(14)\n\t"
            "ds_read_b64 v[158:159], v253 offset:1920\n\t"
            "v_mov_b64 v[176:177], 0\n\t"
            "v_mov_b64 v[178:179], 0\n\t"
            "v_mov_b64 v[180:181], 0\n\t"
            "v_mov_b64 v[182:183], 0\n\t"
            "v_cvt_pk_bf16_f32 v184, v184, v185\n\t"
            "v_cvt_pk_bf16_f32 v185, v186, v187\n\t"
            "v_cvt_pk_bf16_f32 v186, v188, v189\n\t"
            "v_cvt_pk_bf16_f32 v187, v190, v191\n\t"
            "v_mfma_f32_4x4x4_16b_bf16 v[196:199], v[184:185], v[184:185], v[196:199]\n\t"
            "s_waitcnt lgkmcnt(14)\n\t"
            "ds_write_b128 v237, v[184:187] offset:128\n\t"
            "v_mfma_f32_16x16x32_bf16 v[192:195], v[238:241], v[184:187], v[192:195]\n\t"
            "v_mov_b64 v[184:185], 0\n\t"
            "v_mov_b64 v[188:189], 0\n\t"
            "s_waitcnt lgkmcnt(10)\n\t"
            "v_smfmac_f32_16x16x64_bf16 v[176:179], v[168:171], v[160:167], v255\n\t"
            "ds_read_b128 v[168:171], v254 offset:640\n\t"
            "v_mfma_f32_4x4x4_16b_bf16 v[196:199], v[186:187], v[186:187], v[196:199]\n\t"
            "ds_read_b64 v[160:161], v253 offset:88\n\t"
            "ds_read_b64 v[162:163], v253 offset:184\n\t"
            "ds_read_b64 v[164:165], v253 offset:1832\n\t"
            "s_waitcnt lgkmcnt(9)\n\t"
            "v_smfmac_f32_16x16x64_bf16 v[180:183], v[172:175], v[144:151], v255\n\t"
            "ds_read_b64 v[166:167], v253 offset:1928\n\t"
            "ds_read_b128 v[172:175], v254 offset:704\n\t"
            "v_mov_b64 v[186:187], 0\n\t"
            "v_cvt_pk_bf16_f32 v176, v176, v177\n\t"
            "v_mov_b64 v[190:191], 0\n\t"
            "v_cvt_pk_bf16_f32 v177, v178, v179\n\t"
            "v_writelane_b32 v248, s38, m0\n\t"
            "v_writelane_b32 v248, s58, 63\n\t"
            "v_mfma_f32_4x4x4_16b_bf16 v[196:199], v[176:177], v[176:177], v[196:199]\n\t"
            "v_cvt_pk_bf16_f32 v178, v180, v181\n\t"
            "v_cvt_pk_bf16_f32 v179, v182, v183\n\t"
            "s_nop 1\n\t"
            "v_mfma_f32_4x4x4_16b_bf16 v[196:199], v[178:179], v[178:179], v[196:199]\n\t"
            "ds_write_b128 v236, v[176:179] offset:256\n\t"
            "v_mfma_f32_16x16x32_bf16 v[192:195], v[238:241], v[176:179], v[192:195]\n\t"
            "s_waitcnt lgkmcnt(6)\n\t"
            "v_smfmac_f32_16x16x64_bf16 v[184:187], v[168:171], v[152:159], v255\n\t"
            "s_waitcnt lgkmcnt(1)\n\t"
            "v_smfmac_f32_16x16x64_bf16 v[188:191], v[172:175], v[160:167], v255\n\t"
            "s_nop 5\n\t"
            "v_cvt_pk_bf16_f32 v184, v184, v185\n\t"
            "v_cvt_pk_bf16_f32 v185, v186, v187\n\t"
            "s_nop 1\n\t"
            "v_mfma_f32_4x4x4_16b_bf16 v[196:199], v[184:185], v[184:185], v[196:199]\n\t"
            "v_cvt_pk_bf16_f32 v186, v188, v189\n\t"
            "v_cvt_pk_bf16_f32 v187, v190, v191\n\t"
            "s_nop 1\n\t"
            "v_mfma_f32_4x4x4_16b_bf16 v[196:199], v[186:187], v[186:187], v[196:199]\n\t"
            "v_mfma_f32_16x16x32_bf16 v[192:195], v[238:241], v[184:187], v[192:195]\n\t"
            "ds_write_b128 v237, v[184:187] offset:256\n\t"
            "s_nop 6\n\t"
            "v_cndmask_b32_e64 v193, v196, v197, s[60:61]\n\t"
            "v_cndmask_b32_e64 v193, v193, v198, s[62:63]\n\t"
            "v_cndmask_b32_e64 v193, v193, v199, s[64:65]\n\t"
            "ds_write_b64 v248, v[192:193] offset:18816\n\t"
            "s_waitcnt lgkmcnt(0)\n\t"
            "s_nop 7\n\t"
            "v_mov_b32 v144, s44\n\t"
            "v_mov_b32 v145, -1\n\t"
            "ds_write_b32 v144, v145\n\t"
            "s_setprio 1\n\t"
            "v_mov_b32 v144, s23\n\t"
            ".Lgo%=:\n\t"
            "ds_read_b32 v145, v144 offset:124\n\t"
            "s_waitcnt lgkmcnt(0)\n\t"
            "v_cmp_ne_u32 vcc, -1, v145\n\t"
            "s_nop 4\n\t"
            "s_cbranch_vccnz .Lgo%=\n\t"
            "s_nop 7\n\t"
            "ds_read_b32 v249, v252 offset:1024\n\t"
            "s_add_u32 s49, s70, 0\n\t"
            "buffer_load_dwordx4 v[0:3], v247, s[28:31], s49 offen sc1\n\t"
            "s_add_u32 s49, s70, 1024\n\t"
            "buffer_load_dwordx4 v[4:7], v247, s[28:31], s49 offen sc1\n\t"
            "s_add_u32 s49, s70, 2048\n\t"
            "buffer_load_dwordx4 v[8:11], v247, s[28:31], s49 offen sc1\n\t"
            "s_add_u32 s49, s70, 3072\n\t"
            "buffer_load_dwordx4 v[12:15], v247, s[28:31], s49 offen sc1\n\t"
            "s_add_u32 s49, s70, 4096\n\t"
            "buffer_load_dwordx4 v[16:19], v247, s[28:31], s49 offen sc1\n\t"
            "s_add_u32 s49, s70, 5120\n\t"
            "buffer_load_dwordx4 v[20:23], v247, s[28:31], s49 offen sc1\n\t"
            "s_add_u32 s49, s70, 6144\n\t"
            "buffer_load_dwordx4 v[24:27], v247, s[28:31], s49 offen sc1\n\t"
            "s_add_u32 s49, s70, 7168\n\t"
            "buffer_load_dwordx4 v[28:31], v247, s[28:31], s49 offen sc1\n\t"
            "s_add_u32 s49, s70, 8192\n\t"
            "buffer_load_dwordx4 v[32:35], v247, s[28:31], s49 offen sc1\n\t"
            "s_add_u32 s49, s70, 9216\n\t"
            "buffer_load_dwordx4 v[36:39], v247, s[28:31], s49 offen sc1\n\t"
            "s_add_u32 s49, s70, 10240\n\t"
            "buffer_load_dwordx4 v[40:43], v247, s[28:31], s49 offen sc1\n\t"
            "s_add_u32 s49, s70, 11264\n\t"
            "buffer_load_dwordx4 v[44:47], v247, s[28:31], s49 offen sc1\n\t"
            "s_add_u32 s49, s70, 12288\n\t"
            "buffer_load_dwordx4 v[48:51], v247, s[28:31], s49 offen sc1\n\t"
            "s_add_u32 s49, s70, 13312\n\t"
            "buffer_load_dwordx4 v[52:55], v247, s[28:31], s49 offen sc1\n\t"
            "s_add_u32 s49, s70, 14336\n\t"
            "buffer_load_dwordx4 v[56:59], v247, s[28:31], s49 offen sc1\n\t"
            "s_add_u32 s49, s70, 15360\n\t"
            "buffer_load_dwordx4 v[60:63], v247, s[28:31], s49 offen sc1\n\t"
            "s_add_u32 s49, s70, 16384\n\t"
            "buffer_load_dwordx4 v[64:67], v247, s[28:31], s49 offen sc1\n\t"
            "s_add_u32 s49, s70, 17408\n\t"
            "buffer_load_dwordx4 v[68:71], v247, s[28:31], s49 offen sc1\n\t"
            "s_waitcnt lgkmcnt(0)\n\t"
            "v_and_b32 v249, s69, v249\n\t"
            "s_mov_b32 exec_hi, 0x3fffff\n\t"
            "s_add_u32 m0, s66, 0\n\t"
            "s_add_u32 s49, s79, 0\n\t"
            "buffer_load_dwordx4 v249, s[72:75], s49 offen lds\n\t"
            "s_mov_b32 exec_hi, -1\n\t"
            "s_mov_b32 exec_hi, 0x3fffff\n\t"
            "s_add_u32 m0, s66, 872\n\t"
            "s_add_u32 s49, s68, 196224\n\t"
            "buffer_load_dwordx4 v249, s[72:75], s49 offen lds\n\t"
            "s_mov_b32 exec_hi, -1\n\t"
            "s_mov_b32 exec_hi, 0x3fffff\n\t"
            "s_add_u32 m0, s66, 1744\n\t"
            "s_add_u32 s49, s68, 392448\n\t"
            "buffer_load_dwordx4 v249, s[72:75], s49 offen lds\n\t"
            "s_mov_b32 exec_hi, -1\n\t"
            "s_mov_b32 exec_hi, 0x3fffff\n\t"
            "s_add_u32 m0, s66, 2616\n\t"
            "s_add_u32 s49, s68, 588672\n\t"
            "buffer_load_dwordx4 v249, s[72:75], s49 offen lds\n\t"
            "s_mov_b32 exec_hi, -1\n\t"
            "s_mov_b32 exec_hi, 0x3fffff\n\t"
            "s_add_u32 m0, s66, 3488\n\t"
            "s_add_u32 s49, s68, 784896\n\t"
            "buffer_load_dwordx4 v249, s[72:75], s49 offen lds\n\t"
            "s_mov_b32 exec_hi, -1\n\t"
            "s_mov_b32 exec_hi, 0x3fffff\n\t"
            "s_add_u32 m0, s66, 4360\n\t"
            "s_add_u32 s49, s68, 981120\n\t"
            "buffer_load_dwordx4 v249, s[72:75], s49 offen lds\n\t"
            "s_mov_b32 exec_hi, -1\n\t"
            "s_mov_b32 exec_hi, 0x3fffff\n\t"
            "s_add_u32 m0, s66, 5232\n\t"
            "s_add_u32 s49, s68, 1177344\n\t"
            "buffer_load_dwordx4 v249, s[72:75], s49 offen lds\n\t"
            "s_mov_b32 exec_hi, -1\n\t"
            "s_mov_b32 exec_hi, 0x3fffff\n\t"
            "s_add_u32 m0, s66, 6104\n\t"
            "s_add_u32 s49, s68, 1373568\n\t"
            "buffer_load_dwordx4 v249, s[72:75], s49 offen lds\n\t"
            "s_mov_b32 exec_hi, -1\n\t"
            "s_mov_b32 exec_hi, 0x3fffff\n\t"
            "s_add_u32 m0, s66, 6976\n\t"
            "s_add_u32 s49, s68, 1569792\n\t"
            "buffer_load_dwordx4 v249, s[72:75], s49 offen lds\n\t"
            "s_mov_b32 exec_hi, -1\n\t"
            "s_add_u32 s45, s46, s27\n\t"
            "s_mov_b32 s46, s47\n\t"
            "s_mov_b32 s47, s68\n\t"
            "s_xor_b32 s48, s48, s71\n\t"
            "s_xor_b32 s67, s67, s71\n\t"
            "s_add_u32 s76, s76, 4\n\t"
            "s_lshl_b32 s56, s56, 2\n\t"
            "s_lshr_b32 s49, s26, 30\n\t"
            "s_or_b32 s56, s56, s49\n\t"
            "s_cmp_ge_u32 s76, s24\n\t"
            "s_cselect_b64 vcc, -1, 0\n\t"
            "s_nop 7\n\t"
            "v_mov_b32 v144, s23\n\t"
            ".Lpg%=:\n\t"
            "ds_read_b128 v[148:151], v144 offset:96\n\t"
            "s_waitcnt lgkmcnt(0)\n\t"
            "v_and_b32 v148, v148, v149\n\t"
            "v_and_b32 v150, v150, v151\n\t"
            "v_and_b32 v148, v148, v150\n\t"
            "v_cmp_ne_u32 vcc, -1, v148\n\t"
            "s_nop 4\n\t"
            "s_cbranch_vccnz .Lpg%=\n\t"
            "s_setprio 3\n\t"
            "s_nop 7\n\t"
            "ds_read_b128 v[168:171], v254\n\t"
            "ds_read_b128 v[172:175], v254 offset:64\n\t"
            "v_add_u32 v112, s22, v133\n\t"
            "v_xor_b32 v113, 64, v112\n\t"
            "v_add_u32 v114, s22, v247\n\t"
            "v_mov_b64 v[176:177], 0\n\t"
            "v_mov_b64 v[178:179], 0\n\t"
            "v_mov_b64 v[180:181], 0\n\t"
            "v_mov_b64 v[182:183], 0\n\t"
            "v_mov_b32 v249, s76\n\t"
            "v_mov_b64 v[184:185], 0\n\t"
            "v_mov_b64 v[186:187], 0\n\t"
            "v_mov_b64 v[188:189], 0\n\t"
            "v_mov_b64 v[190:191], 0\n\t"
            "v_add_u32 v236, s67, v250\n\t"
            "v_add_u32 v248, s67, v251\n\t"
            "ds_read_b128 v[120:123], v112\n\t"
            "ds_read_b128 v[124:127], v113\n\t"
            "ds_read_b128 v[224:227], v112 offset:128\n\t"
            "ds_read_b128 v[228:231], v114 offset:12288\n\t"
            "ds_read_b128 v[232:235], v114 offset:13312\n\t"
            "ds_read_b32 v249, v249\n\t"
            "s_waitcnt vmcnt(9)\n\t"
            "v_add_u32 v114, s48, v247\n\t"
            "s_waitcnt lgkmcnt(5)\n\t"
            "v_mfma_f32_16x16x32_bf16 v[200:203], v[0:3], v[120:123], 0\n\t"
            "s_waitcnt lgkmcnt(2)\n\t"
            "v_add_f32 v228, v228, v230\n\t"
            "v_mfma_f32_16x16x32_bf16 v[204:207], v[24:27], v[120:123], 0\n\t"
            "s_waitcnt lgkmcnt(0)\n\t"
            "v_readfirstlane_b32 s26, v249\n\t"
            "v_add_f32 v232, v232, v234\n\t"
            "v_mfma_f32_16x16x32_bf16 v[208:211], v[48:51], v[120:123], 0\n\t"
            "s_and_b32 s68, s26, 0x7ffffff\n\t"
            "s_add_u32 s68, s68, s78\n\t"
            "s_and_b32 s69, s26, 0x38000000\n\t"
            "s_or_b32 s69, s69, s39\n\t"
            "s_bitcmp1_b32 s26, 27\n\t"
            "s_cselect_b32 s79, 2.0, s68\n\t"
            "v_mfma_f32_16x16x32_bf16 v[200:203], v[4:7], v[124:127], v[200:203]\n\t"
            "s_and_b32 s57, s56, 3\n\t"
            "s_lshl_b32 s58, s57, 3\n\t"
            "s_lshr_b32 m0, s54, s58\n\t"
            "v_mfma_f32_16x16x32_bf16 v[204:207], v[28:31], v[124:127], v[204:207]\n\t"
            "s_lshl_b32 s58, s57, 4\n\t"
            "s_lshr_b64 s[36:37], s[50:51], s58\n\t"
            "s_and_b32 s57, s36, 0xffff\n\t"
            "s_add_u32 s57, s57, s67\n\t"
            "v_mfma_f32_16x16x32_bf16 v[208:211], v[52:55], v[124:127], v[208:211]\n\t"
            "s_lshr_b64 s[36:37], s[52:53], s58\n\t"
            "s_and_b32 s58, s36, 0xffff\n\t"
            "s_add_u32 s58, s58, s67\n\t"
            "v_mfma_f32_16x16x32_bf16 v[200:203], v[8:11], v[224:227], v[200:203]\n\t"
            "s_and_b32 s59, s50, 0xffff\n\t"
            "s_add_u32 s59, s59, s67\n\t"
            "s_and_b32 s38, s52, 0xffff\n\t"
            "s_add_u32 s38, s38, s67\n\t"
            "v_mfma_f32_16x16x32_bf16 v[204:207], v[32:35], v[224:227], v[204:207]\n\t"
            "ds_read_b128 v[120:123], v113 offset:128\n\t"
            "ds_read_b128 v[124:127], v112 offset:256\n\t"
            "v_mfma_f32_16x16x32_bf16 v[208:211], v[56:59], v[224:227], v[208:211]\n\t"
            "ds_read_b128 v[224:227], v113 offset:256\n\t"
            "ds_read_b32 v249, v252 offset:1024\n\t"
            "v_writelane_b32 v236, s59, m0\n\t"
            "v_writelane_b32 v236, s57, 63\n\t"
            "v_xor_b32 v237, 16, v236\n\t"
            "v_add_f32 v228, v228, v232\n\t"
            "v_add_f32 v229, v229, v231\n\t"
            "v_add_f32 v233, v233, v235\n\t"
            "v_mul_f32 v228, 0x3baaaaab, v228\n\t"
            "v_add_f32 v229, v229, v233\n\t"
            "v_mul_f32_e64 v228, v228, -v228\n\t"
            "v_fmac_f32 v228, 0x3baaaaab, v229\n\t"
            "v_max_f32 v228, 0, v228\n\t"
            "v_add_f32 v228, 0x3727c5ac, v228\n\t"
            "v_rsq_f32 v228, v228\n\t"
            "v_writelane_b32 v248, s38, m0\n\t"
            "v_writelane_b32 v248, s58, 63\n\t"
            "ds_read_b128 v[232:235], v114 offset:19840\n\t"
            "ds_write_b32 v136, v228\n\t"
            "ds_read_b32 v116, v138\n\t"
            "ds_read_b128 v[228:231], v114 offset:18816\n\t"
            "ds_read_b32 v117, v138 offset:64\n\t"
            "s_waitcnt lgkmcnt(8)\n\t"
            "v_mfma_f32_16x16x32_bf16 v[200:203], v[12:15], v[120:123], v[200:203]\n\t"
            "v_mfma_f32_16x16x32_bf16 v[204:207], v[36:39], v[120:123], v[204:207]\n\t"
            "s_waitcnt lgkmcnt(5)\n\t"
            "v_and_b32 v249, s69, v249\n\t"
            "v_mfma_f32_16x16x32_bf16 v[208:211], v[60:63], v[120:123], v[208:211]\n\t"
            "ds_read_b128 v[120:123], v112 offset:6144\n\t"
            "v_mfma_f32_16x16x32_bf16 v[200:203], v[16:19], v[124:127], v[200:203]\n\t"
            "v_mfma_f32_16x16x32_bf16 v[204:207], v[40:43], v[124:127], v[204:207]\n\t"
            "v_mfma_f32_16x16x32_bf16 v[208:211], v[64:67], v[124:127], v[208:211]\n\t"
            "ds_read_b128 v[124:127], v113 offset:6144\n\t"
            "v_mfma_f32_16x16x32_bf16 v[200:203], v[20:23], v[224:227], v[200:203]\n\t"
            "s_waitcnt lgkmcnt(6)\n\t"
            "v_add_f32 v232, v232, v234\n\t"
            "v_add_f32 v233, v233, v235\n\t"
            "v_mfma_f32_16x16x32_bf16 v[204:207], v[44:47], v[224:227], v[204:207]\n\t"
            "s_waitcnt lgkmcnt(3)\n\t"
            "v_add_f32 v228, v228, v230\n\t"
            "v_add_f32 v228, v228, v232\n\t"
            "v_mfma_f32_16x16x32_bf16 v[208:211], v[68:71], v[224:227], v[208:211]\n\t"
            "ds_read_b128 v[224:227], v112 offset:6272\n\t"
            "v_add_f32 v229, v229, v231\n\t"
            "v_mul_f32 v200, v116, v200\n\t"
            "v_mul_f32 v201, v116, v201\n\t"
            "v_mul_f32 v202, v116, v202\n\t"
            "v_mul_f32 v204, v116, v204\n\t"
            "v_mul_f32 v205, v116, v205\n\t"
            "s_waitcnt lgkmcnt(2)\n\t"
            "v_mfma_f32_16x16x32_bf16 v[220:223], v[48:51], v[120:123], 0\n\t"
            "v_mul_f32 v208, v116, v208\n\t"
            "v_mul_f32 v209, v116, v209\n\t"
            "v_mfma_f32_16x16x32_bf16 v[212:215], v[0:3], v[120:123], 0\n\t"
            "v_mul_f32 v203, v116, v203\n\t"
            "v_cvt_pk_bf16_f32 v200, v200, v201\n\t"
            "v_mfma_f32_16x16x32_bf16 v[216:219], v[24:27], v[120:123], 0\n\t"
            "ds_read_b128 v[120:123], v113 offset:6272\n\t"
            "v_mul_f32 v206, v116, v206\n\t"
            "s_waitcnt lgkmcnt(2)\n\t"
            "v_mfma_f32_16x16x32_bf16 v[220:223], v[52:55], v[124:127], v[220:223]\n\t"
            "s_waitcnt vmcnt(0)\n\t"
            "ds_read_b64 v[144:145], v253\n\t"
            "v_mfma_f32_16x16x32_bf16 v[212:215], v[4:7], v[124:127], v[212:215]\n\t"
            "ds_read_b64 v[146:147], v253 offset:96\n\t"
            "ds_read_b64 v[148:149], v253 offset:1744\n\t"
            "v_mfma_f32_16x16x32_bf16 v[216:219], v[28:31], v[124:127], v[216:219]\n\t"
            "ds_read_b64 v[150:151], v253 offset:1840\n\t"
            "ds_read_b64 v[152:153], v253 offset:8\n\t"
            "s_waitcnt lgkmcnt(6)\n\t"
            "v_mfma_f32_16x16x32_bf16 v[212:215], v[8:11], v[224:227], v[212:215]\n\t"
            "ds_read_b64 v[154:155], v253 offset:104\n\t"
            "ds_read_b64 v[156:157], v253 offset:1752\n\t"
            "v_mfma_f32_16x16x32_bf16 v[216:219], v[32:35], v[224:227], v[216:219]\n\t"
            "ds_read_b64 v[158:159], v253 offset:1848\n\t"
            "ds_read_b64 v[160:161], v253 offset:16\n\t"
            "v_mfma_f32_16x16x32_bf16 v[220:223], v[56:59], v[224:227], v[220:223]\n\t"
            "ds_read_b64 v[162:163], v253 offset:112\n\t"
            "ds_read_b64 v[164:165], v253 offset:1760\n\t"
            "ds_read_b64 v[166:167], v253 offset:1856\n\t"
            "ds_read_b128 v[124:127], v112 offset:6400\n\t"
            "ds_read_b128 v[224:227], v113 offset:6400\n\t"
            "v_add_u32 v112, s48, v133\n\t"
            "v_xor_b32 v113, 64, v112\n\t"
            "s_waitcnt lgkmcnt(14)\n\t"
            "v_mfma_f32_16x16x32_bf16 v[212:215], v[12:15], v[120:123], v[212:215]\n\t"
            "v_mul_f32 v207, v116, v207\n\t"
            "v_cvt_pk_bf16_f32 v204, v204, v205\n\t"
            "v_mfma_f32_16x16x32_bf16 v[216:219], v[36:39], v[120:123], v[216:219]\n\t"
            "v_mul_f32 v210, v116, v210\n\t"
            "v_mul_f32 v211, v116, v211\n\t"
            "v_mfma_f32_16x16x32_bf16 v[220:223], v[60:63], v[120:123], v[220:223]\n\t"
            "ds_read_b128 v[120:123], v112\n\t"
            "v_cvt_pk_bf16_f32 v208, v208, v209\n\t"
            "s_waitcnt lgkmcnt(11)\n\t"
            "v_smfmac_f32_16x16x64_bf16 v[176:179], v[168:171], v[144:151], v255\n\t"
            "ds_read_b128 v[168:171], v254 offset:128\n\t"
            "ds_read_b64 v[144:145], v253 offset:24\n\t"
            "ds_read_b64 v[146:147], v253 offset:120\n\t"
            "ds_read_b64 v[148:149], v253 offset:1768\n\t"
            "s_waitcnt lgkmcnt(14)\n\t"
            "ds_read_b64 v[150:151], v253 offset:1864\n\t"
            "s_waitcnt lgkmcnt(12)\n\t"
            "v_smfmac_f32_16x16x64_bf16 v[180:183], v[172:175], v[152:159], v255\n\t"
            "ds_read_b128 v[172:175], v254 offset:192\n\t"
            "ds_read_b64 v[152:153], v253 offset:32\n\t"
            "ds_read_b64 v[154:155], v253 offset:128\n\t"
            "s_waitcnt lgkmcnt(14)\n\t"
            "ds_read_b64 v[156:157], v253 offset:1776\n\t"
            "s_waitcnt lgkmcnt(11)\n\t"
            "v_mfma_f32_16x16x32_bf16 v[212:215], v[16:19], v[124:127], v[212:215]\n\t"
            "ds_read_b64 v[158:159], v253 offset:1872\n\t"
            "v_cvt_pk_bf16_f32 v176, v176, v177\n\t"
            "v_mfma_f32_16x16x32_bf16 v[216:219], v[40:43], v[124:127], v[216:219]\n\t"
            "v_cvt_pk_bf16_f32 v177, v178, v179\n\t"
            "v_cvt_pk_bf16_f32 v178, v180, v181\n\t"
            "v_mfma_f32_16x16x32_bf16 v[220:223], v[64:67], v[124:127], v[220:223]\n\t"
            "ds_read_b128 v[124:127], v113\n\t"
            "v_cvt_pk_bf16_f32 v179, v182, v183\n\t"
            "v_mfma_f32_4x4x4_16b_bf16 v[196:199], v[176:177], v[176:177], 0\n\t"
            "ds_write_b128 v236, v[176:179]\n\t"
            "v_mfma_f32_16x16x32_bf16 v[192:195], v[238:241], v[176:179], 0\n\t"
            "v_mov_b64 v[176:177], 0\n\t"
            "v_mov_b64 v[180:181], 0\n\t"
            "s_waitcnt lgkmcnt(11)\n\t"
            "v_smfmac_f32_16x16x64_bf16 v[184:187], v[168:171], v[160:167], v255\n\t"
            "ds_read_b128 v[168:171], v254 offset:256\n\t"
            "v_mfma_f32_4x4x4_16b_bf16 v[196:199], v[178:179], v[178:179], v[196:199]\n\t"
            "ds_read_b64 v[160:161], v253 offset:40\n\t"
            "s_waitcnt lgkmcnt(8)\n\t"
            "v_smfmac_f32_16x16x64_bf16 v[188:191], v[172:175], v[144:151], v255\n\t"
            "ds_read_b64 v[162:163], v253 offset:136\n\t"
            "v_mfma_f32_16x16x32_bf16 v[212:215], v[20:23], v[224:227], v[212:215]\n\t"
            "ds_read_b64 v[164:165], v253 offset:1784\n\t"
            "ds_read_b64 v[166:167], v253 offset:1880\n\t"
            "v_mfma_f32_16x16x32_bf16 v[216:219], v[44:47], v[224:227], v[216:219]\n\t"
            "ds_read_b128 v[172:175], v254 offset:320\n\t"
            "ds_read_b64 v[144:145], v253 offset:48\n\t"
            "v_mfma_f32_16x16x32_bf16 v[220:223], v[68:71], v[224:227], v[220:223]\n\t"
            "ds_read_b64 v[146:147], v253 offset:144\n\t"
            "ds_read_b64 v[148:149], v253 offset:1792\n\t"
            "s_waitcnt lgkmcnt(14)\n\t"
            "ds_read_b64 v[150:151], v253 offset:1888\n\t"
            "s_waitcnt lgkmcnt(14)\n\t"
            "ds_read_b128 v[224:227], v112 offset:128\n\t"
            "v_mov_b64 v[178:179], 0\n\t"
            "v_mov_b64 v[182:183], 0\n\t"
            "v_cvt_pk_bf16_f32 v184, v184, v185\n\t"
            "v_cvt_pk_bf16_f32 v185, v186, v187\n\t"
            "v_cvt_pk_bf16_f32 v186, v188, v189\n\t"
            "v_cvt_pk_bf16_f32 v187, v190, v191\n\t"
            "v_mfma_f32_4x4x4_16b_bf16 v[196:199], v[184:185], v[184:185], v[196:199]\n\t"
            "s_waitcnt lgkmcnt(14)\n\t"
            "ds_write_b128 v237, v[184:187]\n\t"
            "s_waitcnt lgkmcnt(11)\n\t"
            "v_smfmac_f32_16x16x64_bf16 v[176:179], v[168:171], v[152:159], v255\n\t"
            "ds_read_b128 v[168:171], v254 offset:384\n\t"
            "v_mfma_f32_16x16x32_bf16 v[192:195], v[238:241], v[184:187], v[192:195]\n\t"
            "ds_read_b64 v[152:153], v253 offset:56\n\t"
            "ds_read_b64 v[154:155], v253 offset:152\n\t"
            "v_mfma_f32_4x4x4_16b_bf16 v[196:199], v[186:187], v[186:187], v[196:199]\n\t"
            "ds_read_b64 v[156:157], v253 offset:1800\n\t"
            "s_waitcnt lgkmcnt(14)\n\t"
            "ds_read_b64 v[158:159], v253 offset:1896\n\t"
            "s_waitcnt lgkmcnt(11)\n\t"
            "v_smfmac_f32_16x16x64_bf16 v[180:183], v[172:175], v[160:167], v255\n\t"
            "ds_read_b128 v[172:175], v254 offset:448\n\t"
            "ds_read_b64 v[160:161], v253 offset:64\n\t"
            "ds_read_b64 v[162:163], v253 offset:160\n\t"
            "ds_read_b64 v[164:165], v253 offset:1808\n\t"
            "s_waitcnt lgkmcnt(14)\n\t"
            "ds_read_b64 v[166:167], v253 offset:1904\n\t"
            "v_mov_b64 v[184:185], 0\n\t"
            "v_mov_b64 v[186:187], 0\n\t"
            "v_mov_b64 v[188:189], 0\n\t"
            "v_mov_b64 v[190:191], 0\n\t"
            "v_cvt_pk_bf16_f32 v176, v176, v177\n\t"
            "v_cvt_pk_bf16_f32 v177, v178, v179\n\t"
            "v_cvt_pk_bf16_f32 v178, v180, v181\n\t"
            "v_cvt_pk_bf16_f32 v179, v182, v183\n\t"
            "v_mfma_f32_4x4x4_16b_bf16 v[196:199], v[176:177], v[176:177], v[196:199]\n\t"
            "s_waitcnt lgkmcnt(14)\n\t"
            "ds_write_b128 v236, v[176:179] offset:128\n\t"
            "v_mfma_f32_16x16x32_bf16 v[192:195], v[238:241], v[176:179], v[192:195]\n\t"
            "v_mov_b64 v[176:177], 0\n\t"
            "v_mov_b64 v[180:181], 0\n\t"
            "s_waitcnt lgkmcnt(10)\n\t"
            "v_smfmac_f32_16x16x64_bf16 v[184:187], v[168:171], v[144:151], v255\n\t"
            "ds_read_b128 v[168:171], v254 offset:512\n\t"
            "v_mfma_f32_4x4x4_16b_bf16 v[196:199], v[178:179], v[178:179], v[196:199]\n\t"
            "ds_read_b64 v[144:145], v253 offset:72\n\t"
            "ds_read_b64 v[146:147], v253 offset:168\n\t"
            "ds_read_b64 v[148:149], v253 offset:1816\n\t"
            "s_waitcnt lgkmcnt(9)\n\t"
            "v_smfmac_f32_16x16x64_bf16 v[188:191], v[172:175], v[152:159], v255\n\t"
            "ds_read_b64 v[150:151], v253 offset:1912\n\t"
            "ds_read_b128 v[172:175], v254 offset:576\n\t"
            "ds_read_b64 v[152:153], v253 offset:80\n\t"
            "ds_read_b64 v[154:155], v253 offset:176\n\t"
            "ds_read_b64 v[156:157], v253 offset:1824\n\t"
            "ds_read_b64 v[158:159], v253 offset:1920\n\t"
            "v_mov_b64 v[178:179], 0\n\t"
            "v_mov_b64 v[182:183], 0\n\t"
            "v_cvt_pk_bf16_f32 v184, v184, v185\n\t"
            "v_cvt_pk_bf16_f32 v185, v186, v187\n\t"
            "v_cvt_pk_bf16_f32 v186, v188, v189\n\t"
            "v_cvt_pk_bf16_f32 v187, v190, v191\n\t"
            "v_mfma_f32_4x4x4_16b_bf16 v[196:199], v[184:185], v[184:185], v[196:199]\n\t"
            "s_waitcnt lgkmcnt(14)\n\t"
            "ds_write_b128 v237, v[184:187] offset:128\n\t"
            "v_mfma_f32_16x16x32_bf16 v[192:195], v[238:241], v[184:187], v[192:195]\n\t"
            "v_mov_b64 v[184:185], 0\n\t"
            "v_mov_b64 v[188:189], 0\n\t"
            "v_mfma_f32_4x4x4_16b_bf16 v[196:199], v[186:187], v[186:187], v[196:199]\n\t"
            "v_mov_b64 v[186:187], 0\n\t"
            "s_waitcnt lgkmcnt(10)\n\t"
            "v_smfmac_f32_16x16x64_bf16 v[176:179], v[168:171], v[160:167], v255\n\t"
            "ds_read_b128 v[168:171], v254 offset:640\n\t"
            "ds_read_b64 v[160:161], v253 offset:88\n\t"
            "ds_read_b64 v[162:163], v253 offset:184\n\t"
            "ds_read_b64 v[164:165], v253 offset:1832\n\t"
            "ds_read_b64 v[166:167], v253 offset:1928\n\t"
            "s_waitcnt lgkmcnt(10)\n\t"
            "v_smfmac_f32_16x16x64_bf16 v[180:183], v[172:175], v[144:151], v255\n\t"
            "ds_read_b128 v[172:175], v254 offset:704\n\t"
            "v_mov_b64 v[190:191], 0\n\t"
            "v_cvt_pk_bf16_f32 v201, v202, v203\n\t"
            "v_cvt_pk_bf16_f32 v205, v206, v207\n\t"
            "v_cvt_pk_bf16_f32 v209, v210, v211\n\t"
            "v_mul_f32 v228, 0x3baaaaab, v228\n\t"
            "v_add_f32 v229, v229, v233\n\t"
            "v_mul_f32_e64 v228, v228, -v228\n\t"
            "v_fmac_f32 v228, 0x3baaaaab, v229\n\t"
            "v_mul_f32 v212, v117, v212\n\t"
            "v_mul_f32 v213, v117, v213\n\t"
            "v_mul_f32 v216, v117, v216\n\t"
            "v_mul_f32 v217, v117, v217\n\t"
            "v_mul_f32 v220, v117, v220\n\t"
            "v_mul_f32 v221, v117, v221\n\t"
            "v_max_f32 v228, 0, v228\n\t"
            "v_mul_f32 v214, v117, v214\n\t"
            "v_mul_f32 v215, v117, v215\n\t"
            "v_cvt_pk_bf16_f32 v212, v212, v213\n\t"
            "v_mul_f32 v218, v117, v218\n\t"
            "v_mul_f32 v219, v117, v219\n\t"
            "v_cvt_pk_bf16_f32 v216, v216, v217\n\t"
            "v_mul_f32 v222, v117, v222\n\t"
            "s_waitcnt lgkmcnt(5)\n\t"
            "v_smfmac_f32_16x16x64_bf16 v[184:187], v[168:171], v[152:159], v255\n\t"
            "ds_write_b64 v131, v[200:201]\n\t"
            "v_mfma_f32_16x16x32_bf16 v[200:203], v[120:123], v[0:3], 0\n\t"
            "ds_write_b64 v131, v[204:205] offset:128\n\t"
            "s_waitcnt lgkmcnt(2)\n\t"
            "v_smfmac_f32_16x16x64_bf16 v[188:191], v[172:175], v[160:167], v255\n\t"
            "ds_write_b64 v131, v[208:209] offset:256\n\t"
            "v_mfma_f32_16x16x32_bf16 v[204:207], v[120:123], v[24:27], 0\n\t"
            "s_mov_b32 exec_hi, 0x3fffff\n\t"
            "s_add_u32 m0, s66, 0\n\t"
            "s_add_u32 s49, s79, 0\n\t"
            "buffer_load_dwordx4 v249, s[72:75], s49 offen lds\n\t"
            "s_mov_b32 exec_hi, -1\n\t"
            "v_mfma_f32_16x16x32_bf16 v[208:211], v[120:123], v[48:51], 0\n\t"
            "ds_read_b128 v[120:123], v113 offset:128\n\t"
            "s_mov_b32 exec_hi, 0x3fffff\n\t"
            "s_add_u32 m0, s66, 872\n\t"
            "s_add_u32 s49, s68, 196224\n\t"
            "buffer_load_dwordx4 v249, s[72:75], s49 offen lds\n\t"
            "s_mov_b32 exec_hi, -1\n\t"
            "v_mfma_f32_16x16x32_bf16 v[200:203], v[124:127], v[4:7], v[200:203]\n\t"
            "s_mov_b32 exec_hi, 0x3fffff\n\t"
            "s_add_u32 m0, s66, 1744\n\t"
            "s_add_u32 s49, s68, 392448\n\t"
            "buffer_load_dwordx4 v249, s[72:75], s49 offen lds\n\t"
            "s_mov_b32 exec_hi, -1\n\t"
            "v_mfma_f32_16x16x32_bf16 v[204:207], v[124:127], v[28:31], v[204:207]\n\t"
            "s_mov_b32 exec_hi, 0x3fffff\n\t"
            "s_add_u32 m0, s66, 2616\n\t"
            "s_add_u32 s49, s68, 588672\n\t"
            "buffer_load_dwordx4 v249, s[72:75], s49 offen lds\n\t"
            "s_mov_b32 exec_hi, -1\n\t"
            "v_mfma_f32_16x16x32_bf16 v[208:211], v[124:127], v[52:55], v[208:211]\n\t"
            "ds_read_b128 v[124:127], v112 offset:256\n\t"
            "s_mov_b32 exec_hi, 0x3fffff\n\t"
            "s_add_u32 m0, s66, 3488\n\t"
            "s_add_u32 s49, s68, 784896\n\t"
            "buffer_load_dwordx4 v249, s[72:75], s49 offen lds\n\t"
            "s_mov_b32 exec_hi, -1\n\t"
            "v_mfma_f32_16x16x32_bf16 v[200:203], v[224:227], v[8:11], v[200:203]\n\t"
            "s_mov_b32 exec_hi, 0x3fffff\n\t"
            "s_add_u32 m0, s66, 4360\n\t"
            "s_add_u32 s49, s68, 981120\n\t"
            "buffer_load_dwordx4 v249, s[72:75], s49 offen lds\n\t"
            "s_mov_b32 exec_hi, -1\n\t"
            "v_mfma_f32_16x16x32_bf16 v[204:207], v[224:227], v[32:35], v[204:207]\n\t"
            "v_mul_f32 v223, v117, v223\n\t"
            "v_cvt_pk_bf16_f32 v220, v220, v221\n\t"
            "v_mfma_f32_16x16x32_bf16 v[208:211], v[224:227], v[56:59], v[208:211]\n\t"
            "ds_read_b128 v[224:227], v113 offset:256\n\t"
            "v_add_f32 v228, 0x3727c5ac, v228\n\t"
            "s_waitcnt lgkmcnt(2)\n\t"
            "v_mfma_f32_16x16x32_bf16 v[200:203], v[120:123], v[12:15], v[200:203]\n\t"
            "v_cvt_pk_bf16_f32 v213, v214, v215\n\t"
            "v_cvt_pk_bf16_f32 v217, v218, v219\n\t"
            "v_mfma_f32_16x16x32_bf16 v[204:207], v[120:123], v[36:39], v[204:207]\n\t"
            "v_cvt_pk_bf16_f32 v221, v222, v223\n\t"
            "v_rsq_f32 v228, v228\n\t"
            "v_mfma_f32_16x16x32_bf16 v[208:211], v[120:123], v[60:63], v[208:211]\n\t"
            "ds_read_b128 v[120:123], v112 offset:6144\n\t"
            "ds_write_b64 v132, v[212:213]\n\t"
            "s_waitcnt lgkmcnt(3)\n\t"
            "v_mfma_f32_16x16x32_bf16 v[200:203], v[124:127], v[16:19], v[200:203]\n\t"
            "ds_write_b64 v132, v[216:217] offset:128\n\t"
            "v_mfma_f32_16x16x32_bf16 v[204:207], v[124:127], v[40:43], v[204:207]\n\t"
            "ds_write_b64 v132, v[220:221] offset:256\n\t"
            "v_mfma_f32_16x16x32_bf16 v[208:211], v[124:127], v[64:67], v[208:211]\n\t"
            "ds_read_b128 v[124:127], v113 offset:6144\n\t"
            "ds_write_b32 v136, v228\n\t"
            "s_waitcnt lgkmcnt(6)\n\t"
            "v_mfma_f32_16x16x32_bf16 v[200:203], v[224:227], v[20:23], v[200:203]\n\t"
            "ds_read_b128 v[116:119], v137\n\t"
            "s_mov_b32 exec_hi, 0x3fffff\n\t"
            "s_add_u32 m0, s66, 5232\n\t"
            "s_add_u32 s49, s68, 1177344\n\t"
            "buffer_load_dwordx4 v249, s[72:75], s49 offen lds\n\t"
            "s_mov_b32 exec_hi, -1\n\t"
            "v_mfma_f32_16x16x32_bf16 v[204:207], v[224:227], v[44:47], v[204:207]\n\t"
            "s_mov_b32 exec_hi, 0x3fffff\n\t"
            "s_add_u32 m0, s66, 6104\n\t"
            "s_add_u32 s49, s68, 1373568\n\t"
            "buffer_load_dwordx4 v249, s[72:75], s49 offen lds\n\t"
            "s_mov_b32 exec_hi, -1\n\t"
            "v_mfma_f32_16x16x32_bf16 v[208:211], v[224:227], v[68:71], v[208:211]\n\t"
            "ds_read_b128 v[224:227], v112 offset:6272\n\t"
            "s_mov_b32 exec_hi, 0x3fffff\n\t"
            "s_add_u32 m0, s66, 6976\n\t"
            "s_add_u32 s49, s68, 1569792\n\t"
            "buffer_load_dwordx4 v249, s[72:75], s49 offen lds\n\t"
            "s_mov_b32 exec_hi, -1\n\t"
            "s_waitcnt lgkmcnt(7)\n\t"
            "v_mfma_f32_16x16x32_bf16 v[212:215], v[120:123], v[0:3], 0\n\t"
            "s_waitcnt lgkmcnt(1)\n\t"
            "v_mul_f32 v200, v116, v200\n\t"
            "v_mul_f32 v201, v117, v201\n\t"
            "v_mfma_f32_16x16x32_bf16 v[216:219], v[120:123], v[24:27], 0\n\t"
            "v_mul_f32 v202, v118, v202\n\t"
            "v_mul_f32 v203, v119, v203\n\t"
            "v_mfma_f32_16x16x32_bf16 v[220:223], v[120:123], v[48:51], 0\n\t"
            "ds_read_b128 v[120:123], v113 offset:6272\n\t"
            "v_mul_f32 v204, v116, v204\n\t"
            "v_mfma_f32_16x16x32_bf16 v[212:215], v[124:127], v[4:7], v[212:215]\n\t"
            "v_mul_f32 v205, v117, v205\n\t"
            "v_mul_f32 v206, v118, v206\n\t"
            "v_mfma_f32_16x16x32_bf16 v[216:219], v[124:127], v[28:31], v[216:219]\n\t"
            "v_mul_f32 v207, v119, v207\n\t"
            "v_mul_f32 v208, v116, v208\n\t"
            "v_mfma_f32_16x16x32_bf16 v[220:223], v[124:127], v[52:55], v[220:223]\n\t"
            "ds_read_b128 v[124:127], v112 offset:6400\n\t"
            "v_mul_f32 v209, v117, v209\n\t"
            "s_waitcnt lgkmcnt(2)\n\t"
            "v_mfma_f32_16x16x32_bf16 v[212:215], v[224:227], v[8:11], v[212:215]\n\t"
            "v_mul_f32 v210, v118, v210\n\t"
            "v_mul_f32 v211, v119, v211\n\t"
            "v_mfma_f32_16x16x32_bf16 v[216:219], v[224:227], v[32:35], v[216:219]\n\t"
            "ds_read_b128 v[116:119], v137 offset:64\n\t"
            "s_add_u32 s49, s33, 18432\n\t"
            "buffer_load_dwordx4 v[72:75], v247, s[28:31], s49 offen sc1\n\t"
            "v_mfma_f32_16x16x32_bf16 v[220:223], v[224:227], v[56:59], v[220:223]\n\t"
            "ds_read_b128 v[224:227], v113 offset:6400\n\t"
            "s_add_u32 s49, s33, 19456\n\t"
            "buffer_load_dwordx4 v[76:79], v247, s[28:31], s49 offen sc1\n\t"
            "s_waitcnt lgkmcnt(3)\n\t"
            "v_mfma_f32_16x16x32_bf16 v[212:215], v[120:123], v[12:15], v[212:215]\n\t"
            "s_add_u32 s49, s33, 20480\n\t"
            "buffer_load_dwordx4 v[80:83], v247, s[28:31], s49 offen sc1\n\t"
            "v_mfma_f32_16x16x32_bf16 v[216:219], v[120:123], v[36:39], v[216:219]\n\t"
            "v_cvt_pk_bf16_f32 v200, v200, v201\n\t"
            "v_cvt_pk_bf16_f32 v201, v202, v203\n\t"
            "v_mfma_f32_16x16x32_bf16 v[220:223], v[120:123], v[60:63], v[220:223]\n\t"
            "ds_read_b128 v[120:123], v112 offset:12288\n\t"
            "s_add_u32 s49, s33, 21504\n\t"
            "buffer_load_dwordx4 v[84:87], v247, s[28:31], s49 offen sc1\n\t"
            "s_waitcnt lgkmcnt(3)\n\t"
            "v_mfma_f32_16x16x32_bf16 v[212:215], v[124:127], v[16:19], v[212:215]\n\t"
            "s_add_u32 s49, s33, 22528\n\t"
            "buffer_load_dwordx4 v[88:91], v247, s[28:31], s49 offen sc1\n\t"
            "v_mfma_f32_16x16x32_bf16 v[216:219], v[124:127], v[40:43], v[216:219]\n\t"
            "s_add_u32 s49, s33, 23552\n\t"
            "buffer_load_dwordx4 v[92:95], v247, s[28:31], s49 offen sc1\n\t"
            "v_mfma_f32_16x16x32_bf16 v[220:223], v[124:127], v[64:67], v[220:223]\n\t"
            "ds_read_b128 v[124:127], v113 offset:12288\n\t"
            "v_cvt_pk_bf16_f32 v176, v176, v177\n\t"
            "s_waitcnt lgkmcnt(2)\n\t"
            "v_mfma_f32_16x16x32_bf16 v[212:215], v[224:227], v[20:23], v[212:215]\n\t"
            "v_cvt_pk_bf16_f32 v177, v178, v179\n\t"
            "v_cvt_pk_bf16_f32 v178, v180, v181\n\t"
            "v_mfma_f32_16x16x32_bf16 v[216:219], v[224:227], v[44:47], v[216:219]\n\t"
            "v_cvt_pk_bf16_f32 v179, v182, v183\n\t"
            "v_cvt_pk_bf16_f32 v184, v184, v185\n\t"
            "v_mfma_f32_16x16x32_bf16 v[220:223], v[224:227], v[68:71], v[220:223]\n\t"
            "ds_read_b128 v[224:227], v112 offset:12416\n\t"
            "s_add_u32 s49, s33, 24576\n\t"
            "buffer_load_dwordx4 v[96:99], v247, s[28:31], s49 offen sc1\n\t"
            "s_waitcnt lgkmcnt(2)\n\t"
            "v_mfma_f32_16x16x32_bf16 v[228:231], v[120:123], v[0:3], 0\n\t"
            "v_mul_f32 v212, v116, v212\n\t"
            "v_mul_f32 v213, v117, v213\n\t"
            "v_mfma_f32_16x16x32_bf16 v[232:235], v[120:123], v[24:27], 0\n\t"
            "v_mul_f32 v214, v118, v214\n\t"
            "v_mul_f32 v215, v119, v215\n\t"
            "s_waitcnt lgkmcnt(1)\n\t"
            "v_mfma_f32_16x16x32_bf16 v[228:231], v[124:127], v[4:7], v[228:231]\n\t"
            "v_cvt_pk_bf16_f32 v202, v212, v213\n\t"
            "v_cvt_pk_bf16_f32 v203, v214, v215\n\t"
            "v_mfma_f32_16x16x32_bf16 v[212:215], v[120:123], v[48:51], 0\n\t"
            "ds_read_b128 v[120:123], v113 offset:12416\n\t"
            "v_mul_f32 v216, v116, v216\n\t"
            "v_mfma_f32_16x16x32_bf16 v[232:235], v[124:127], v[28:31], v[232:235]\n\t"
            "v_mul_f32 v217, v117, v217\n\t"
            "v_mul_f32 v218, v118, v218\n\t"
            "v_mfma_f32_16x16x32_bf16 v[212:215], v[124:127], v[52:55], v[212:215]\n\t"
            "ds_read_b128 v[124:127], v112 offset:12544\n\t"
            "v_mul_f32 v219, v119, v219\n\t"
            "s_waitcnt lgkmcnt(2)\n\t"
            "v_mfma_f32_16x16x32_bf16 v[228:231], v[224:227], v[8:11], v[228:231]\n\t"
            "v_mul_f32 v220, v116, v220\n\t"
            "v_mul_f32 v221, v117, v221\n\t"
            "v_mfma_f32_16x16x32_bf16 v[232:235], v[224:227], v[32:35], v[232:235]\n\t"
            "v_mul_f32 v222, v118, v222\n\t"
            "v_mul_f32 v223, v119, v223\n\t"
            "v_mfma_f32_16x16x32_bf16 v[212:215], v[224:227], v[56:59], v[212:215]\n\t"
            "ds_read_b128 v[224:227], v113 offset:12544\n\t"
            "ds_read_b128 v[116:119], v137 offset:128\n\t"
            "v_mfma_f32_4x4x4_16b_bf16 v[196:199], v[176:177], v[176:177], v[196:199]\n\t"
            "v_cvt_pk_bf16_f32 v185, v186, v187\n\t"
            "v_mfma_f32_16x16x32_bf16 v[192:195], v[238:241], v[176:179], v[192:195]\n\t"
            "v_cvt_pk_bf16_f32 v186, v188, v189\n\t"
            "v_cvt_pk_bf16_f32 v187, v190, v191\n\t"
            "v_mfma_f32_4x4x4_16b_bf16 v[196:199], v[178:179], v[178:179], v[196:199]\n\t"
            "s_add_u32 s49, s33, 25600\n\t"
            "buffer_load_dwordx4 v[100:103], v247, s[28:31], s49 offen sc1\n\t"
            "s_waitcnt lgkmcnt(3)\n\t"
            "v_mfma_f32_16x16x32_bf16 v[228:231], v[120:123], v[12:15], v[228:231]\n\t"
            "v_cvt_pk_bf16_f32 v204, v204, v205\n\t"
            "v_cvt_pk_bf16_f32 v208, v208, v209\n\t"
            "v_mfma_f32_16x16x32_bf16 v[232:235], v[120:123], v[36:39], v[232:235]\n\t"
            "v_cvt_pk_bf16_f32 v205, v206, v207\n\t"
            "v_cvt_pk_bf16_f32 v209, v210, v211\n\t"
            "v_mfma_f32_16x16x32_bf16 v[212:215], v[120:123], v[60:63], v[212:215]\n\t"
            "v_cvt_pk_bf16_f32 v206, v216, v217\n\t"
            "v_cvt_pk_bf16_f32 v207, v218, v219\n\t"
            "v_mfma_f32_4x4x4_16b_bf16 v[196:199], v[184:185], v[184:185], v[196:199]\n\t"
            "v_cvt_pk_bf16_f32 v210, v220, v221\n\t"
            "s_waitcnt lgkmcnt(2)\n\t"
            "v_mfma_f32_16x16x32_bf16 v[228:231], v[124:127], v[16:19], v[228:231]\n\t"
            "v_cvt_pk_bf16_f32 v211, v222, v223\n\t"
            "v_mfma_f32_16x16x32_bf16 v[232:235], v[124:127], v[40:43], v[232:235]\n\t"
            "ds_write_b128 v236, v[176:179] offset:256\n\t"
            "v_mfma_f32_16x16x32_bf16 v[212:215], v[124:127], v[64:67], v[212:215]\n\t"
            "ds_write_b128 v237, v[184:187] offset:256\n\t"
            "v_mfma_f32_4x4x4_16b_bf16 v[196:199], v[186:187], v[186:187], v[196:199]\n\t"
            "s_add_u32 s49, s33, 26624\n\t"
            "buffer_load_dwordx4 v[104:107], v247, s[28:31], s49 offen sc1\n\t"
            "v_mfma_f32_16x16x32_bf16 v[192:195], v[238:241], v[184:187], v[192:195]\n\t"
            "ds_write_b128 v134, v[200:203]\n\t"
            "s_waitcnt lgkmcnt(4)\n\t"
            "v_mfma_f32_16x16x32_bf16 v[228:231], v[224:227], v[20:23], v[228:231]\n\t"
            "ds_write_b128 v134, v[204:207] offset:1536\n\t"
            "v_mfma_f32_16x16x32_bf16 v[232:235], v[224:227], v[44:47], v[232:235]\n\t"
            "ds_write_b128 v134, v[208:211] offset:3072\n\t"
            "v_mfma_f32_16x16x32_bf16 v[212:215], v[224:227], v[68:71], v[212:215]\n\t"
            "s_add_u32 s49, s33, 27648\n\t"
            "buffer_load_dwordx4 v[108:111], v247, s[28:31], s49 offen sc1\n\t"
            "v_cndmask_b32_e64 v193, v196, v197, s[60:61]\n\t"
            "v_cndmask_b32_e64 v193, v193, v198, s[62:63]\n\t"
            "s_waitcnt lgkmcnt(5)\n\t"
            "v_mul_f32 v228, v116, v228\n\t"
            "v_mul_f32 v229, v117, v229\n\t"
            "v_mul_f32 v232, v116, v232\n\t"
            "v_mul_f32 v233, v117, v233\n\t"
            "v_mul_f32 v212, v116, v212\n\t"
            "v_mul_f32 v213, v117, v213\n\t"
            "v_mul_f32 v230, v118, v230\n\t"
            "v_mul_f32 v231, v119, v231\n\t"
            "v_cvt_pk_bf16_f32 v228, v228, v229\n\t"
            "v_mul_f32 v234, v118, v234\n\t"
            "v_mul_f32 v235, v119, v235\n\t"
            "v_cvt_pk_bf16_f32 v232, v232, v233\n\t"
            "v_mul_f32 v214, v118, v214\n\t"
            "v_mul_f32 v215, v119, v215\n\t"
            "v_cvt_pk_bf16_f32 v212, v212, v213\n\t"
            "v_cndmask_b32_e64 v193, v193, v199, s[64:65]\n\t"
            "v_cvt_pk_bf16_f32 v229, v230, v231\n\t"
            "v_cvt_pk_bf16_f32 v233, v234, v235\n\t"
            "v_cvt_pk_bf16_f32 v213, v214, v215\n\t"
            "ds_write_b64 v248, v[192:193] offset:18816\n\t"
            "ds_write_b64 v135, v[228:229]\n\t"
            "ds_write_b64 v135, v[232:233] offset:1536\n\t"
            "ds_write_b64 v135, v[212:213] offset:3072\n\t"
            "s_waitcnt lgkmcnt(0)\n\t"
            "s_nop 7\n\t"
            "s_cmp_ge_u32 s33, 221184\n\t"
            "s_cbranch_scc1 .Lrp%=\n\t"
            "s_add_u32 s49, s33, 28672\n\t"
            "buffer_load_dwordx4 v[112:115], v247, s[28:31], s49 offen sc1\n\t"
            "s_add_u32 s49, s33, 29696\n\t"
            "buffer_load_dwordx4 v[116:119], v247, s[28:31], s49 offen sc1\n\t"
            "s_add_u32 s49, s33, 30720\n\t"
            "buffer_load_dwordx4 v[120:123], v247, s[28:31], s49 offen sc1\n\t"
            "s_add_u32 s49, s33, 31744\n\t"
            "buffer_load_dwordx4 v[124:127], v247, s[28:31], s49 offen sc1\n\t"
            "s_add_u32 s49, s33, 32768\n\t"
            "buffer_load_dwordx4 v[128:131], v247, s[28:31], s49 offen sc1\n\t"
            "s_add_u32 s49, s33, 33792\n\t"
            "buffer_load_dwordx4 v[132:135], v247, s[28:31], s49 offen sc1\n\t"
            "s_add_u32 s49, s33, 34816\n\t"
            "buffer_load_dwordx4 v[136:139], v247, s[28:31], s49 offen sc1\n\t"
            "s_add_u32 s49, s33, 35840\n\t"
            "buffer_load_dwordx4 v[140:143], v247, s[28:31], s49 offen sc1\n\t"
            "s_add_u32 s45, s46, s27\n\t"
            "s_mov_b32 s46, s47\n\t"
            "s_mov_b32 s47, s68\n\t"
            "s_xor_b32 s48, s48, s71\n\t"
            "s_xor_b32 s67, s67, s71\n\t"
            "s_add_u32 s76, s76, 4\n\t"
            "s_lshl_b32 s56, s56, 2\n\t"
            "s_lshr_b32 s49, s26, 30\n\t"
            "s_or_b32 s56, s56, s49\n\t"
            "s_barrier\n\t"
            "ds_read_b128 v[168:171], v254\n\t"
            "ds_read_b128 v[172:175], v254 offset:64\n\t"
            "s_waitcnt vmcnt(0)\n\t"
            "ds_read_b64 v[144:145], v253\n\t"
            "ds_read_b64 v[146:147], v253 offset:96\n\t"
            "ds_read_b64 v[148:149], v253 offset:1744\n\t"
            "ds_read_b64 v[150:151], v253 offset:1840\n\t"
            "ds_read_b64 v[152:153], v253 offset:8\n\t"
            "ds_read_b64 v[154:155], v253 offset:104\n\t"
            "ds_read_b64 v[156:157], v253 offset:1752\n\t"
            "ds_read_b64 v[158:159], v253 offset:1848\n\t"
            "ds_read_b64 v[160:161], v253 offset:16\n\t"
            "ds_read_b64 v[162:163], v253 offset:112\n\t"
            "ds_read_b64 v[164:165], v253 offset:1760\n\t"
            "ds_read_b64 v[166:167], v253 offset:1856\n\t"
            "s_waitcnt lgkmcnt(0)\n\t"
            "s_nop 7\n\t"
            ".Lwc_loop%=:\n\t"
            "s_setprio 1\n\t"
            "v_mov_b64 v[176:177], 0\n\t"
            "v_mov_b64 v[178:179], 0\n\t"
            "v_mov_b64 v[180:181], 0\n\t"
            "v_mov_b64 v[182:183], 0\n\t"
            "s_setprio 3\n\t"
            "v_smfmac_f32_16x16x64_bf16 v[176:179], v[168:171], v[144:151], v255\n\t"
            "ds_read_b128 v[168:171], v254 offset:128\n\t"
            "v_smfmac_f32_16x16x64_bf16 v[180:183], v[172:175], v[152:159], v255\n\t"
            "s_setprio 1\n\t"
            "ds_read_b64 v[144:145], v253 offset:24\n\t"
            "ds_read_b64 v[146:147], v253 offset:120\n\t"
            "ds_read_b64 v[148:149], v253 offset:1768\n\t"
            "ds_read_b64 v[150:151], v253 offset:1864\n\t"
            "ds_read_b128 v[172:175], v254 offset:192\n\t"
            "ds_read_b64 v[152:153], v253 offset:32\n\t"
            "ds_read_b64 v[154:155], v253 offset:128\n\t"
            "ds_read_b64 v[156:157], v253 offset:1776\n\t"
            "ds_read_b64 v[158:159], v253 offset:1872\n\t"
            "v_mov_b32 v249, s76\n\t"
            "v_mov_b64 v[184:185], 0\n\t"
            "v_mov_b64 v[186:187], 0\n\t"
            "v_mov_b64 v[188:189], 0\n\t"
            "v_mov_b64 v[190:191], 0\n\t"
            "v_cvt_pk_bf16_f32 v176, v176, v177\n\t"
            "v_add_u32 v236, s67, v250\n\t"
            "v_cvt_pk_bf16_f32 v177, v178, v179\n\t"
            "v_cvt_pk_bf16_f32 v178, v180, v181\n\t"
            "v_cvt_pk_bf16_f32 v179, v182, v183\n\t"
            "s_setprio 3\n\t"
            "v_mfma_f32_4x4x4_16b_bf16 v[196:199], v[176:177], v[176:177], 0\n\t"
            "ds_read_b32 v249, v249\n\t"
            "v_mfma_f32_16x16x32_bf16 v[192:195], v[238:241], v[176:179], 0\n\t"
            "v_mov_b64 v[180:181], 0\n\t"
            "v_mov_b64 v[182:183], 0\n\t"
            "v_mfma_f32_4x4x4_16b_bf16 v[196:199], v[178:179], v[178:179], v[196:199]\n\t"
            "v_add_u32 v234, s48, v247\n\t"
            "s_waitcnt lgkmcnt(10)\n\t"
            "v_smfmac_f32_16x16x64_bf16 v[184:187], v[168:171], v[160:167], v255\n\t"
            "ds_read_b128 v[168:171], v254 offset:256\n\t"
            "ds_read_b64 v[160:161], v253 offset:40\n\t"
            "ds_read_b64 v[162:163], v253 offset:136\n\t"
            "ds_read_b64 v[164:165], v253 offset:1784\n\t"
            "ds_read_b64 v[166:167], v253 offset:1880\n\t"
            "s_waitcnt lgkmcnt(10)\n\t"
            "v_smfmac_f32_16x16x64_bf16 v[188:191], v[172:175], v[144:151], v255\n\t"
            "s_setprio 1\n\t"
            "ds_read_b128 v[172:175], v254 offset:320\n\t"
            "ds_read_b64 v[144:145], v253 offset:48\n\t"
            "ds_read_b64 v[146:147], v253 offset:144\n\t"
            "ds_read_b64 v[148:149], v253 offset:1792\n\t"
            "ds_read_b64 v[150:151], v253 offset:1888\n\t"
            "s_waitcnt lgkmcnt(14)\n\t"
            "ds_read_b128 v[200:203], v234 offset:18816\n\t"
            "s_waitcnt lgkmcnt(14)\n\t"
            "ds_read_b128 v[204:207], v234 offset:19840\n\t"
            "v_cvt_pk_bf16_f32 v184, v184, v185\n\t"
            "v_cvt_pk_bf16_f32 v185, v186, v187\n\t"
            "v_cvt_pk_bf16_f32 v186, v188, v189\n\t"
            "v_cvt_pk_bf16_f32 v187, v190, v191\n\t"
            "s_setprio 3\n\t"
            "v_mfma_f32_4x4x4_16b_bf16 v[196:199], v[184:185], v[184:185], v[196:199]\n\t"
            "v_mov_b64 v[188:189], 0\n\t"
            "v_mfma_f32_16x16x32_bf16 v[192:195], v[238:241], v[184:187], v[192:195]\n\t"
            "s_waitcnt lgkmcnt(12)\n\t"
            "v_readfirstlane_b32 s26, v249\n\t"
            "v_mov_b64 v[190:191], 0\n\t"
            "v_mfma_f32_4x4x4_16b_bf16 v[196:199], v[186:187], v[186:187], v[196:199]\n\t"
            "ds_read_b32 v249, v252 offset:1024\n\t"
            "s_and_b32 s68, s26, 0x7ffffff\n\t"
            "s_add_u32 s68, s68, s78\n\t"
            "s_and_b32 s69, s26, 0x38000000\n\t"
            "s_or_b32 s69, s69, s39\n\t"
            "s_bitcmp1_b32 s26, 27\n\t"
            "s_cselect_b32 s79, 2.0, s68\n\t"
            "s_and_b32 s57, s56, 3\n\t"
            "s_lshl_b32 s58, s57, 3\n\t"
            "s_lshr_b32 m0, s54, s58\n\t"
            "s_lshl_b32 s58, s57, 4\n\t"
            "s_lshr_b64 s[36:37], s[50:51], s58\n\t"
            "s_and_b32 s57, s36, 0xffff\n\t"
            "s_add_u32 s57, s57, s67\n\t"
            "s_waitcnt lgkmcnt(7)\n\t"
            "v_smfmac_f32_16x16x64_bf16 v[180:183], v[172:175], v[160:167], v255\n\t"
            "s_setprio 1\n\t"
            "s_lshr_b64 s[36:37], s[52:53], s58\n\t"
            "s_and_b32 s58, s36, 0xffff\n\t"
            "s_add_u32 s58, s58, s67\n\t"
            "ds_read_b128 v[172:175], v254 offset:448\n\t"
            "ds_read_b64 v[160:161], v253 offset:64\n\t"
            "ds_read_b64 v[162:163], v253 offset:160\n\t"
            "ds_read_b64 v[164:165], v253 offset:1808\n\t"
            "ds_read_b64 v[166:167], v253 offset:1904\n\t"
            "s_and_b32 s59, s50, 0xffff\n\t"
            "s_add_u32 s59, s59, s67\n\t"
            "s_and_b32 s38, s52, 0xffff\n\t"
            "s_add_u32 s38, s38, s67\n\t"
            "v_writelane_b32 v236, s59, m0\n\t"
            "v_writelane_b32 v236, s57, 63\n\t"
            "v_xor_b32 v237, 16, v236\n\t"
            "v_add_u32 v248, s67, v251\n\t"
            "v_writelane_b32 v248, s38, m0\n\t"
            "v_add_u32 v232, s48, v242\n\t"
            "v_writelane_b32 v248, s58, 63\n\t"
            "v_xor_b32 v233, 64, v232\n\t"
            "s_waitcnt lgkmcnt(7)\n\t"
            "v_add_f32 v200, v200, v202\n\t"
            "s_waitcnt lgkmcnt(6)\n\t"
            "v_add_f32 v204, v204, v206\n\t"
            "v_add_f32 v200, v200, v204\n\t"
            "v_add_f32 v201, v201, v203\n\t"
            "v_add_f32 v205, v205, v207\n\t"
            "v_mul_f32 v200, 0x3baaaaab, v200\n\t"
            "v_add_f32 v201, v201, v205\n\t"
            "v_mul_f32_e64 v200, v200, -v200\n\t"
            "v_fmac_f32 v200, 0x3baaaaab, v201\n\t"
            "v_max_f32 v200, 0, v200\n\t"
            "v_add_f32 v200, 0x3727c5ac, v200\n\t"
            "v_rsq_f32 v200, v200\n\t"
            "s_waitcnt lgkmcnt(5)\n\t"
            "v_and_b32 v249, s69, v249\n\t"
            "ds_read_b128 v[224:227], v232\n\t"
            "ds_write_b128 v236, v[176:179]\n\t"
            "v_mov_b64 v[176:177], 0\n\t"
            "v_mov_b64 v[178:179], 0\n\t"
            "s_setprio 3\n\t"
            "s_nop 0\n\t"
            "v_smfmac_f32_16x16x64_bf16 v[176:179], v[168:171], v[152:159], v255\n\t"
            "s_setprio 1\n\t"
            "ds_read_b128 v[168:171], v254 offset:384\n\t"
            "ds_read_b64 v[152:153], v253 offset:56\n\t"
            "ds_read_b64 v[154:155], v253 offset:152\n\t"
            "ds_read_b64 v[156:157], v253 offset:1800\n\t"
            "ds_read_b64 v[158:159], v253 offset:1896\n\t"
            "ds_write_b128 v237, v[184:187]\n\t"
            "v_mov_b64 v[184:185], 0\n\t"
            "v_mov_b64 v[186:187], 0\n\t"
            "v_cvt_pk_bf16_f32 v176, v176, v177\n\t"
            "v_cvt_pk_bf16_f32 v177, v178, v179\n\t"
            "v_cvt_pk_bf16_f32 v178, v180, v181\n\t"
            "v_cvt_pk_bf16_f32 v179, v182, v183\n\t"
            "s_setprio 3\n\t"
            "v_mfma_f32_4x4x4_16b_bf16 v[196:199], v[176:177], v[176:177], v[196:199]\n\t"
            "ds_write_b128 v236, v[176:179] offset:128\n\t"
            "v_mfma_f32_16x16x32_bf16 v[192:195], v[238:241], v[176:179], v[192:195]\n\t"
            "v_mov_b64 v[176:177], 0\n\t"
            "v_mov_b64 v[180:181], 0\n\t"
            "v_mfma_f32_4x4x4_16b_bf16 v[196:199], v[178:179], v[178:179], v[196:199]\n\t"
            "v_mov_b64 v[178:179], 0\n\t"
            "s_waitcnt lgkmcnt(8)\n\t"
            "v_mfma_f32_16x16x32_bf16 v[204:207], v[224:227], v[24:27], 0\n\t"
            "v_mov_b64 v[182:183], 0\n\t"
            "s_waitcnt lgkmcnt(6)\n\t"
            "v_smfmac_f32_16x16x64_bf16 v[184:187], v[168:171], v[144:151], v255\n\t"
            "ds_read_b128 v[168:171], v254 offset:512\n\t"
            "s_waitcnt lgkmcnt(3)\n\t"
            "v_smfmac_f32_16x16x64_bf16 v[188:191], v[172:175], v[152:159], v255\n\t"
            "ds_read_b64 v[144:145], v253 offset:72\n\t"
            "v_mfma_f32_16x16x32_bf16 v[208:211], v[224:227], v[48:51], 0\n\t"
            "ds_read_b64 v[146:147], v253 offset:168\n\t"
            "ds_read_b64 v[148:149], v253 offset:1816\n\t"
            "v_mfma_f32_16x16x32_bf16 v[212:215], v[224:227], v[72:75], 0\n\t"
            "ds_read_b64 v[150:151], v253 offset:1912\n\t"
            "ds_read_b128 v[172:175], v254 offset:576\n\t"
            "v_mfma_f32_16x16x32_bf16 v[216:219], v[224:227], v[96:99], 0\n\t"
            "ds_read_b64 v[152:153], v253 offset:80\n\t"
            "ds_read_b64 v[154:155], v253 offset:176\n\t"
            "v_mfma_f32_16x16x32_bf16 v[220:223], v[224:227], v[120:123], 0\n\t"
            "s_setprio 1\n\t"
            "ds_read_b64 v[156:157], v253 offset:1824\n\t"
            "ds_read_b64 v[158:159], v253 offset:1920\n\t"
            "v_cvt_pk_bf16_f32 v184, v184, v185\n\t"
            "v_cvt_pk_bf16_f32 v185, v186, v187\n\t"
            "v_cvt_pk_bf16_f32 v186, v188, v189\n\t"
            "v_cvt_pk_bf16_f32 v187, v190, v191\n\t"
            "s_setprio 3\n\t"
            "v_mfma_f32_4x4x4_16b_bf16 v[196:199], v[184:185], v[184:185], v[196:199]\n\t"
            "ds_write_b128 v237, v[184:187] offset:128\n\t"
            "s_waitcnt lgkmcnt(10)\n\t"
            "v_smfmac_f32_16x16x64_bf16 v[176:179], v[168:171], v[160:167], v255\n\t"
            "ds_read_b128 v[168:171], v254 offset:640\n\t"
            "v_mfma_f32_16x16x32_bf16 v[192:195], v[238:241], v[184:187], v[192:195]\n\t"
            "s_barrier\n\t"
            "ds_read_b64 v[160:161], v253 offset:88\n\t"
            "ds_read_b64 v[162:163], v253 offset:184\n\t"
            "v_mfma_f32_4x4x4_16b_bf16 v[196:199], v[186:187], v[186:187], v[196:199]\n\t"
            "ds_read_b64 v[164:165], v253 offset:1832\n\t"
            "ds_read_b64 v[166:167], v253 offset:1928\n\t"
            "v_mov_b64 v[184:185], 0\n\t"
            "s_waitcnt lgkmcnt(10)\n\t"
            "v_smfmac_f32_16x16x64_bf16 v[180:183], v[172:175], v[144:151], v255\n\t"
            "s_setprio 1\n\t"
            "ds_read_b128 v[172:175], v254 offset:704\n\t"
            "v_mov_b64 v[186:187], 0\n\t"
            "v_cvt_pk_bf16_f32 v176, v176, v177\n\t"
            "v_mov_b64 v[188:189], 0\n\t"
            "v_mov_b64 v[190:191], 0\n\t"
            "v_cvt_pk_bf16_f32 v177, v178, v179\n\t"
            "s_setprio 3\n\t"
            "s_nop 0\n\t"
            "v_mfma_f32_4x4x4_16b_bf16 v[196:199], v[176:177], v[176:177], v[196:199]\n\t"
            "ds_write_b32 v246, v200\n\t"
            "v_mfma_f32_16x16x32_bf16 v[200:203], v[224:227], v[0:3], 0\n\t"
            "ds_read_b128 v[224:227], v233 offset:128\n\t"
            "ds_read_b128 v[228:231], v245\n\t"
            "v_cvt_pk_bf16_f32 v178, v180, v181\n\t"
            "v_cvt_pk_bf16_f32 v179, v182, v183\n\t"
            "s_waitcnt lgkmcnt(8)\n\t"
            "v_smfmac_f32_16x16x64_bf16 v[184:187], v[168:171], v[152:159], v255\n\t"
            "ds_write_b128 v236, v[176:179] offset:256\n\t"
            "v_mfma_f32_4x4x4_16b_bf16 v[196:199], v[178:179], v[178:179], v[196:199]\n\t"
            "v_mfma_f32_16x16x32_bf16 v[192:195], v[238:241], v[176:179], v[192:195]\n\t"
            "s_waitcnt lgkmcnt(4)\n\t"
            "v_smfmac_f32_16x16x64_bf16 v[188:191], v[172:175], v[160:167], v255\n\t"
            "s_setprio 1\n\t"
            "s_mov_b32 exec_hi, 0x3fffff\n\t"
            "s_add_u32 m0, s66, 0\n\t"
            "s_add_u32 s49, s79, 0\n\t"
            "buffer_load_dwordx4 v249, s[72:75], s49 offen lds\n\t"
            "s_mov_b32 exec_hi, -1\n\t"
            "v_cvt_pk_bf16_f32 v184, v184, v185\n\t"
            "v_cvt_pk_bf16_f32 v185, v186, v187\n\t"
            "v_cvt_pk_bf16_f32 v186, v188, v189\n\t"
            "v_cvt_pk_bf16_f32 v187, v190, v191\n\t"
            "s_setprio 3\n\t"
            "v_mfma_f32_4x4x4_16b_bf16 v[196:199], v[184:185], v[184:185], v[196:199]\n\t"
            "ds_write_b128 v237, v[184:187] offset:256\n\t"
            "v_mfma_f32_16x16x32_bf16 v[192:195], v[238:241], v[184:187], v[192:195]\n\t"
            "s_mov_b32 exec_hi, 0x3fffff\n\t"
            "s_add_u32 m0, s66, 872\n\t"
            "s_add_u32 s49, s68, 196224\n\t"
            "buffer_load_dwordx4 v249, s[72:75], s49 offen lds\n\t"
            "s_mov_b32 exec_hi, -1\n\t"
            "v_mfma_f32_4x4x4_16b_bf16 v[196:199], v[186:187], v[186:187], v[196:199]\n\t"
            "s_setprio 1\n\t"
            "s_mov_b32 exec_hi, 0x3fffff\n\t"
            "s_add_u32 m0, s66, 1744\n\t"
            "s_add_u32 s49, s68, 392448\n\t"
            "buffer_load_dwordx4 v249, s[72:75], s49 offen lds\n\t"
            "s_mov_b32 exec_hi, -1\n\t"
            "s_mov_b32 exec_hi, 0x3fffff\n\t"
            "s_add_u32 m0, s66, 2616\n\t"
            "s_add_u32 s49, s68, 588672\n\t"
            "buffer_load_dwordx4 v249, s[72:75], s49 offen lds\n\t"
            "s_mov_b32 exec_hi, -1\n\t"
            "v_cndmask_b32_e64 v193, v196, v197, s[60:61]\n\t"
            "v_cndmask_b32_e64 v193, v193, v198, s[62:63]\n\t"
            "v_cndmask_b32_e64 v193, v193, v199, s[64:65]\n\t"
            "s_setprio 1\n\t"
            "ds_read_b128 v[196:199], v232 offset:128\n\t"
            "ds_write_b64 v248, v[192:193] offset:18816\n\t"
            "ds_read_b128 v[192:195], v233\n\t"
            "s_mov_b32 exec_hi, 0x3fffff\n\t"
            "s_add_u32 m0, s66, 3488\n\t"
            "s_add_u32 s49, s68, 784896\n\t"
            "buffer_load_dwordx4 v249, s[72:75], s49 offen lds\n\t"
            "s_mov_b32 exec_hi, -1\n\t"
            "s_mov_b32 exec_hi, 0x3fffff\n\t"
            "s_add_u32 m0, s66, 4360\n\t"
            "s_add_u32 s49, s68, 981120\n\t"
            "buffer_load_dwordx4 v249, s[72:75], s49 offen lds\n\t"
            "s_mov_b32 exec_hi, -1\n\t"
            "s_mov_b32 exec_hi, 0x3fffff\n\t"
            "s_add_u32 m0, s66, 5232\n\t"
            "s_add_u32 s49, s68, 1177344\n\t"
            "buffer_load_dwordx4 v249, s[72:75], s49 offen lds\n\t"
            "s_mov_b32 exec_hi, -1\n\t"
            "s_waitcnt lgkmcnt(0)\n\t"
            "v_mfma_f32_16x16x32_bf16 v[200:203], v[192:195], v[4:7], v[200:203]\n\t"
            "s_mov_b32 exec_hi, 0x3fffff\n\t"
            "s_add_u32 m0, s66, 6104\n\t"
            "s_add_u32 s49, s68, 1373568\n\t"
            "buffer_load_dwordx4 v249, s[72:75], s49 offen lds\n\t"
            "s_mov_b32 exec_hi, -1\n\t"
            "v_mfma_f32_16x16x32_bf16 v[204:207], v[192:195], v[28:31], v[204:207]\n\t"
            "s_mov_b32 exec_hi, 0x3fffff\n\t"
            "s_add_u32 m0, s66, 6976\n\t"
            "s_add_u32 s49, s68, 1569792\n\t"
            "buffer_load_dwordx4 v249, s[72:75], s49 offen lds\n\t"
            "s_mov_b32 exec_hi, -1\n\t"
            "v_mfma_f32_16x16x32_bf16 v[208:211], v[192:195], v[52:55], v[208:211]\n\t"
            "s_add_u32 s45, s46, s27\n\t"
            "s_mov_b32 s46, s47\n\t"
            "s_mov_b32 s47, s68\n\t"
            "v_mfma_f32_16x16x32_bf16 v[212:215], v[192:195], v[76:79], v[212:215]\n\t"
            "s_xor_b32 s48, s48, s71\n\t"
            "s_xor_b32 s67, s67, s71\n\t"
            "s_add_u32 s76, s76, 4\n\t"
            "v_mfma_f32_16x16x32_bf16 v[216:219], v[192:195], v[100:103], v[216:219]\n\t"
            "s_lshl_b32 s56, s56, 2\n\t"
            "s_lshr_b32 s49, s26, 30\n\t"
            "s_or_b32 s56, s56, s49\n\t"
            "v_mfma_f32_16x16x32_bf16 v[220:223], v[192:195], v[124:127], v[220:223]\n\t"
            "ds_read_b128 v[192:195], v232 offset:256\n\t"
            "v_mfma_f32_16x16x32_bf16 v[200:203], v[196:199], v[8:11], v[200:203]\n\t"
            "v_mfma_f32_16x16x32_bf16 v[204:207], v[196:199], v[32:35], v[204:207]\n\t"
            "v_mfma_f32_16x16x32_bf16 v[208:211], v[196:199], v[56:59], v[208:211]\n\t"
            "v_mfma_f32_16x16x32_bf16 v[212:215], v[196:199], v[80:83], v[212:215]\n\t"
            "v_mfma_f32_16x16x32_bf16 v[216:219], v[196:199], v[104:107], v[216:219]\n\t"
            "v_mfma_f32_16x16x32_bf16 v[220:223], v[196:199], v[128:131], v[220:223]\n\t"
            "ds_read_b128 v[196:199], v233 offset:256\n\t"
            "v_mfma_f32_16x16x32_bf16 v[200:203], v[224:227], v[12:15], v[200:203]\n\t"
            "v_mfma_f32_16x16x32_bf16 v[204:207], v[224:227], v[36:39], v[204:207]\n\t"
            "v_mfma_f32_16x16x32_bf16 v[208:211], v[224:227], v[60:63], v[208:211]\n\t"
            "v_mfma_f32_16x16x32_bf16 v[212:215], v[224:227], v[84:87], v[212:215]\n\t"
            "v_mfma_f32_16x16x32_bf16 v[216:219], v[224:227], v[108:111], v[216:219]\n\t"
            "v_mfma_f32_16x16x32_bf16 v[220:223], v[224:227], v[132:135], v[220:223]\n\t"
            "ds_read_b128 v[224:227], v232 offset:6144\n\t"
            "s_waitcnt lgkmcnt(2)\n\t"
            "v_mfma_f32_16x16x32_bf16 v[200:203], v[192:195], v[16:19], v[200:203]\n\t"
            "v_mfma_f32_16x16x32_bf16 v[204:207], v[192:195], v[40:43], v[204:207]\n\t"
            "v_mfma_f32_16x16x32_bf16 v[208:211], v[192:195], v[64:67], v[208:211]\n\t"
            "v_mfma_f32_16x16x32_bf16 v[212:215], v[192:195], v[88:91], v[212:215]\n\t"
            "v_mfma_f32_16x16x32_bf16 v[216:219], v[192:195], v[112:115], v[216:219]\n\t"
            "v_mfma_f32_16x16x32_bf16 v[220:223], v[192:195], v[136:139], v[220:223]\n\t"
            "ds_read_b128 v[192:195], v233 offset:6144\n\t"
            "s_waitcnt lgkmcnt(2)\n\t"
            "v_mfma_f32_16x16x32_bf16 v[200:203], v[196:199], v[20:23], v[200:203]\n\t"
            "v_mfma_f32_16x16x32_bf16 v[204:207], v[196:199], v[44:47], v[204:207]\n\t"
            "v_mfma_f32_16x16x32_bf16 v[208:211], v[196:199], v[68:71], v[208:211]\n\t"
            "v_mfma_f32_16x16x32_bf16 v[212:215], v[196:199], v[92:95], v[212:215]\n\t"
            "v_mfma_f32_16x16x32_bf16 v[216:219], v[196:199], v[116:119], v[216:219]\n\t"
            "v_mfma_f32_16x16x32_bf16 v[220:223], v[196:199], v[140:143], v[220:223]\n\t"
            "ds_read_b128 v[196:199], v232 offset:6272\n\t"
            "s_waitcnt lgkmcnt(2)\n\t"
            "v_mfma_f32_16x16x32_bf16 v[144:147], v[224:227], v[0:3], 0\n\t"
            "v_mul_f32 v200, v228, v200\n\t"
            "v_mul_f32 v201, v229, v201\n\t"
            "v_mfma_f32_16x16x32_bf16 v[148:151], v[224:227], v[24:27], 0\n\t"
            "v_mul_f32 v202, v230, v202\n\t"
            "v_mul_f32 v203, v231, v203\n\t"
            "v_mfma_f32_16x16x32_bf16 v[168:171], v[224:227], v[48:51], 0\n\t"
            "v_mul_f32 v204, v228, v204\n\t"
            "v_mul_f32 v205, v229, v205\n\t"
            "v_mfma_f32_16x16x32_bf16 v[172:175], v[224:227], v[72:75], 0\n\t"
            "v_mul_f32 v206, v230, v206\n\t"
            "v_mul_f32 v207, v231, v207\n\t"
            "v_mfma_f32_16x16x32_bf16 v[152:155], v[224:227], v[96:99], 0\n\t"
            "v_mul_f32 v208, v228, v208\n\t"
            "v_mul_f32 v209, v229, v209\n\t"
            "v_mfma_f32_16x16x32_bf16 v[156:159], v[224:227], v[120:123], 0\n\t"
            "ds_read_b128 v[224:227], v233 offset:6272\n\t"
            "v_mul_f32 v210, v230, v210\n\t"
            "s_waitcnt lgkmcnt(2)\n\t"
            "v_mfma_f32_16x16x32_bf16 v[144:147], v[192:195], v[4:7], v[144:147]\n\t"
            "v_mul_f32 v211, v231, v211\n\t"
            "v_mul_f32 v212, v228, v212\n\t"
            "v_mfma_f32_16x16x32_bf16 v[148:151], v[192:195], v[28:31], v[148:151]\n\t"
            "v_mul_f32 v213, v229, v213\n\t"
            "v_mul_f32 v214, v230, v214\n\t"
            "v_mfma_f32_16x16x32_bf16 v[168:171], v[192:195], v[52:55], v[168:171]\n\t"
            "v_mul_f32 v215, v231, v215\n\t"
            "v_mul_f32 v216, v228, v216\n\t"
            "v_mfma_f32_16x16x32_bf16 v[172:175], v[192:195], v[76:79], v[172:175]\n\t"
            "v_mul_f32 v217, v229, v217\n\t"
            "v_mul_f32 v218, v230, v218\n\t"
            "v_mfma_f32_16x16x32_bf16 v[152:155], v[192:195], v[100:103], v[152:155]\n\t"
            "v_mul_f32 v219, v231, v219\n\t"
            "v_mul_f32 v220, v228, v220\n\t"
            "v_mfma_f32_16x16x32_bf16 v[156:159], v[192:195], v[124:127], v[156:159]\n\t"
            "ds_read_b128 v[192:195], v232 offset:6400\n\t"
            "v_mul_f32 v221, v229, v221\n\t"
            "s_waitcnt lgkmcnt(2)\n\t"
            "v_mfma_f32_16x16x32_bf16 v[144:147], v[196:199], v[8:11], v[144:147]\n\t"
            "v_mul_f32 v222, v230, v222\n\t"
            "v_mul_f32 v223, v231, v223\n\t"
            "v_mfma_f32_16x16x32_bf16 v[148:151], v[196:199], v[32:35], v[148:151]\n\t"
            "ds_read_b128 v[228:231], v245 offset:64\n\t"
            "v_cvt_pk_bf16_f32 v200, v200, v201\n\t"
            "v_mfma_f32_16x16x32_bf16 v[168:171], v[196:199], v[56:59], v[168:171]\n\t"
            "v_cvt_pk_bf16_f32 v204, v204, v205\n\t"
            "v_cvt_pk_bf16_f32 v208, v208, v209\n\t"
            "v_mfma_f32_16x16x32_bf16 v[172:175], v[196:199], v[80:83], v[172:175]\n\t"
            "v_cvt_pk_bf16_f32 v212, v212, v213\n\t"
            "v_cvt_pk_bf16_f32 v216, v216, v217\n\t"
            "v_mfma_f32_16x16x32_bf16 v[152:155], v[196:199], v[104:107], v[152:155]\n\t"
            "v_cvt_pk_bf16_f32 v220, v220, v221\n\t"
            "v_cvt_pk_bf16_f32 v201, v202, v203\n\t"
            "v_mfma_f32_16x16x32_bf16 v[156:159], v[196:199], v[128:131], v[156:159]\n\t"
            "ds_read_b128 v[196:199], v233 offset:6400\n\t"
            "v_cvt_pk_bf16_f32 v205, v206, v207\n\t"
            "s_waitcnt lgkmcnt(3)\n\t"
            "v_mfma_f32_16x16x32_bf16 v[144:147], v[224:227], v[12:15], v[144:147]\n\t"
            "v_cvt_pk_bf16_f32 v209, v210, v211\n\t"
            "v_cvt_pk_bf16_f32 v213, v214, v215\n\t"
            "v_mfma_f32_16x16x32_bf16 v[148:151], v[224:227], v[36:39], v[148:151]\n\t"
            "v_cvt_pk_bf16_f32 v217, v218, v219\n\t"
            "v_cvt_pk_bf16_f32 v221, v222, v223\n\t"
            "v_mfma_f32_16x16x32_bf16 v[168:171], v[224:227], v[60:63], v[168:171]\n\t"
            "v_mfma_f32_16x16x32_bf16 v[172:175], v[224:227], v[84:87], v[172:175]\n\t"
            "v_mfma_f32_16x16x32_bf16 v[152:155], v[224:227], v[108:111], v[152:155]\n\t"
            "v_mfma_f32_16x16x32_bf16 v[156:159], v[224:227], v[132:135], v[156:159]\n\t"
            "ds_read_b128 v[224:227], v232 offset:12288\n\t"
            "s_waitcnt lgkmcnt(3)\n\t"
            "v_mfma_f32_16x16x32_bf16 v[144:147], v[192:195], v[16:19], v[144:147]\n\t"
            "v_mfma_f32_16x16x32_bf16 v[148:151], v[192:195], v[40:43], v[148:151]\n\t"
            "v_mfma_f32_16x16x32_bf16 v[168:171], v[192:195], v[64:67], v[168:171]\n\t"
            "v_mfma_f32_16x16x32_bf16 v[172:175], v[192:195], v[88:91], v[172:175]\n\t"
            "v_mfma_f32_16x16x32_bf16 v[152:155], v[192:195], v[112:115], v[152:155]\n\t"
            "v_mfma_f32_16x16x32_bf16 v[156:159], v[192:195], v[136:139], v[156:159]\n\t"
            "ds_read_b128 v[192:195], v233 offset:12288\n\t"
            "s_waitcnt lgkmcnt(2)\n\t"
            "v_mfma_f32_16x16x32_bf16 v[144:147], v[196:199], v[20:23], v[144:147]\n\t"
            "v_mfma_f32_16x16x32_bf16 v[148:151], v[196:199], v[44:47], v[148:151]\n\t"
            "v_mfma_f32_16x16x32_bf16 v[168:171], v[196:199], v[68:71], v[168:171]\n\t"
            "v_mfma_f32_16x16x32_bf16 v[172:175], v[196:199], v[92:95], v[172:175]\n\t"
            "v_mfma_f32_16x16x32_bf16 v[152:155], v[196:199], v[116:119], v[152:155]\n\t"
            "v_mfma_f32_16x16x32_bf16 v[156:159], v[196:199], v[140:143], v[156:159]\n\t"
            "ds_read_b128 v[196:199], v232 offset:12416\n\t"
            "s_waitcnt lgkmcnt(2)\n\t"
            "v_mfma_f32_16x16x32_bf16 v[160:163], v[224:227], v[0:3], 0\n\t"
            "v_mul_f32 v144, v228, v144\n\t"
            "v_mul_f32 v145, v229, v145\n\t"
            "v_mfma_f32_16x16x32_bf16 v[164:167], v[224:227], v[24:27], 0\n\t"
            "v_mul_f32 v146, v230, v146\n\t"
            "v_mul_f32 v147, v231, v147\n\t"
            "v_mfma_f32_16x16x32_bf16 v[176:179], v[224:227], v[48:51], 0\n\t"
            "v_mul_f32 v148, v228, v148\n\t"
            "v_mul_f32 v149, v229, v149\n\t"
            "v_mfma_f32_16x16x32_bf16 v[180:183], v[224:227], v[72:75], 0\n\t"
            "v_mul_f32 v150, v230, v150\n\t"
            "v_mul_f32 v151, v231, v151\n\t"
            "v_mfma_f32_16x16x32_bf16 v[184:187], v[224:227], v[96:99], 0\n\t"
            "v_mul_f32 v168, v228, v168\n\t"
            "v_mul_f32 v169, v229, v169\n\t"
            "v_mfma_f32_16x16x32_bf16 v[188:191], v[224:227], v[120:123], 0\n\t"
            "ds_read_b128 v[224:227], v233 offset:12416\n\t"
            "v_mul_f32 v170, v230, v170\n\t"
            "s_waitcnt lgkmcnt(2)\n\t"
            "v_mfma_f32_16x16x32_bf16 v[160:163], v[192:195], v[4:7], v[160:163]\n\t"
            "v_mul_f32 v171, v231, v171\n\t"
            "v_mul_f32 v172, v228, v172\n\t"
            "v_mfma_f32_16x16x32_bf16 v[164:167], v[192:195], v[28:31], v[164:167]\n\t"
            "v_mul_f32 v173, v229, v173\n\t"
            "v_mul_f32 v174, v230, v174\n\t"
            "v_mfma_f32_16x16x32_bf16 v[176:179], v[192:195], v[52:55], v[176:179]\n\t"
            "v_mul_f32 v175, v231, v175\n\t"
            "v_mul_f32 v152, v228, v152\n\t"
            "v_mfma_f32_16x16x32_bf16 v[180:183], v[192:195], v[76:79], v[180:183]\n\t"
            "v_mul_f32 v153, v229, v153\n\t"
            "v_mul_f32 v154, v230, v154\n\t"
            "v_mfma_f32_16x16x32_bf16 v[184:187], v[192:195], v[100:103], v[184:187]\n\t"
            "v_mul_f32 v155, v231, v155\n\t"
            "v_mul_f32 v156, v228, v156\n\t"
            "v_mfma_f32_16x16x32_bf16 v[188:191], v[192:195], v[124:127], v[188:191]\n\t"
            "ds_read_b128 v[192:195], v232 offset:12544\n\t"
            "v_mul_f32 v157, v229, v157\n\t"
            "s_waitcnt lgkmcnt(2)\n\t"
            "v_mfma_f32_16x16x32_bf16 v[160:163], v[196:199], v[8:11], v[160:163]\n\t"
            "s_waitcnt vmcnt(0)\n\t"
            "v_mul_f32 v158, v230, v158\n\t"
            "v_mul_f32 v159, v231, v159\n\t"
            "v_mfma_f32_16x16x32_bf16 v[164:167], v[196:199], v[32:35], v[164:167]\n\t"
            "ds_read_b128 v[228:231], v245 offset:128\n\t"
            "v_cvt_pk_bf16_f32 v202, v144, v145\n\t"
            "v_mfma_f32_16x16x32_bf16 v[176:179], v[196:199], v[56:59], v[176:179]\n\t"
            "ds_read_b64 v[144:145], v253\n\t"
            "v_cvt_pk_bf16_f32 v203, v146, v147\n\t"
            "v_mfma_f32_16x16x32_bf16 v[180:183], v[196:199], v[80:83], v[180:183]\n\t"
            "ds_read_b64 v[146:147], v253 offset:96\n\t"
            "v_cvt_pk_bf16_f32 v206, v148, v149\n\t"
            "v_mfma_f32_16x16x32_bf16 v[184:187], v[196:199], v[104:107], v[184:187]\n\t"
            "ds_read_b64 v[148:149], v253 offset:1744\n\t"
            "v_cvt_pk_bf16_f32 v207, v150, v151\n\t"
            "v_mfma_f32_16x16x32_bf16 v[188:191], v[196:199], v[128:131], v[188:191]\n\t"
            "ds_read_b128 v[196:199], v233 offset:12544\n\t"
            "ds_read_b64 v[150:151], v253 offset:1840\n\t"
            "s_waitcnt lgkmcnt(7)\n\t"
            "v_mfma_f32_16x16x32_bf16 v[160:163], v[224:227], v[12:15], v[160:163]\n\t"
            "v_cvt_pk_bf16_f32 v210, v168, v169\n\t"
            "v_cvt_pk_bf16_f32 v211, v170, v171\n\t"
            "v_mfma_f32_16x16x32_bf16 v[164:167], v[224:227], v[36:39], v[164:167]\n\t"
            "ds_read_b128 v[168:171], v254\n\t"
            "v_cvt_pk_bf16_f32 v214, v172, v173\n\t"
            "v_mfma_f32_16x16x32_bf16 v[176:179], v[224:227], v[60:63], v[176:179]\n\t"
            "v_cvt_pk_bf16_f32 v215, v174, v175\n\t"
            "v_cvt_pk_bf16_f32 v218, v152, v153\n\t"
            "v_mfma_f32_16x16x32_bf16 v[180:183], v[224:227], v[84:87], v[180:183]\n\t"
            "ds_read_b128 v[172:175], v254 offset:64\n\t"
            "ds_read_b64 v[152:153], v253 offset:8\n\t"
            "v_mfma_f32_16x16x32_bf16 v[184:187], v[224:227], v[108:111], v[184:187]\n\t"
            "v_cvt_pk_bf16_f32 v219, v154, v155\n\t"
            "v_cvt_pk_bf16_f32 v222, v156, v157\n\t"
            "v_mfma_f32_16x16x32_bf16 v[188:191], v[224:227], v[132:135], v[188:191]\n\t"
            "ds_read_b64 v[154:155], v253 offset:104\n\t"
            "ds_read_b64 v[156:157], v253 offset:1752\n\t"
            "s_waitcnt lgkmcnt(11)\n\t"
            "v_mfma_f32_16x16x32_bf16 v[160:163], v[192:195], v[16:19], v[160:163]\n\t"
            "v_cvt_pk_bf16_f32 v223, v158, v159\n\t"
            "v_mfma_f32_16x16x32_bf16 v[164:167], v[192:195], v[40:43], v[164:167]\n\t"
            "ds_read_b64 v[158:159], v253 offset:1848\n\t"
            "ds_write_b128 v243, v[200:203]\n\t"
            "v_mfma_f32_16x16x32_bf16 v[176:179], v[192:195], v[64:67], v[176:179]\n\t"
            "ds_write_b128 v243, v[204:207] offset:1536\n\t"
            "v_mfma_f32_16x16x32_bf16 v[180:183], v[192:195], v[88:91], v[180:183]\n\t"
            "ds_write_b128 v243, v[208:211] offset:3072\n\t"
            "v_mfma_f32_16x16x32_bf16 v[184:187], v[192:195], v[112:115], v[184:187]\n\t"
            "s_waitcnt lgkmcnt(14)\n\t"
            "ds_write_b128 v243, v[212:215] offset:4608\n\t"
            "v_mfma_f32_16x16x32_bf16 v[188:191], v[192:195], v[136:139], v[188:191]\n\t"
            "s_waitcnt lgkmcnt(14)\n\t"
            "ds_write_b128 v243, v[216:219] offset:6144\n\t"
            "s_waitcnt lgkmcnt(12)\n\t"
            "v_mfma_f32_16x16x32_bf16 v[160:163], v[196:199], v[20:23], v[160:163]\n\t"
            "ds_write_b128 v243, v[220:223] offset:7680\n\t"
            "v_mfma_f32_16x16x32_bf16 v[164:167], v[196:199], v[44:47], v[164:167]\n\t"
            "v_mfma_f32_16x16x32_bf16 v[176:179], v[196:199], v[68:71], v[176:179]\n\t"
            "v_mfma_f32_16x16x32_bf16 v[180:183], v[196:199], v[92:95], v[180:183]\n\t"
            "v_mfma_f32_16x16x32_bf16 v[184:187], v[196:199], v[116:119], v[184:187]\n\t"
            "v_mfma_f32_16x16x32_bf16 v[188:191], v[196:199], v[140:143], v[188:191]\n\t"
            "s_nop 1\n\t"
            "v_mul_f32 v160, v228, v160\n\t"
            "v_mul_f32 v161, v229, v161\n\t"
            "v_mul_f32 v162, v230, v162\n\t"
            "v_mul_f32 v163, v231, v163\n\t"
            "v_mul_f32 v164, v228, v164\n\t"
            "v_mul_f32 v165, v229, v165\n\t"
            "v_mul_f32 v176, v228, v176\n\t"
            "v_mul_f32 v177, v229, v177\n\t"
            "v_mul_f32 v180, v228, v180\n\t"
            "v_mul_f32 v181, v229, v181\n\t"
            "v_mul_f32 v184, v228, v184\n\t"
            "v_mul_f32 v185, v229, v185\n\t"
            "v_mul_f32 v188, v228, v188\n\t"
            "v_mul_f32 v189, v229, v189\n\t"
            "v_cvt_pk_bf16_f32 v160, v160, v161\n\t"
            "v_mul_f32 v166, v230, v166\n\t"
            "v_mul_f32 v167, v231, v167\n\t"
            "v_cvt_pk_bf16_f32 v164, v164, v165\n\t"
            "v_mul_f32 v178, v230, v178\n\t"
            "v_mul_f32 v179, v231, v179\n\t"
            "v_cvt_pk_bf16_f32 v176, v176, v177\n\t"
            "v_mul_f32 v182, v230, v182\n\t"
            "v_mul_f32 v183, v231, v183\n\t"
            "v_cvt_pk_bf16_f32 v180, v180, v181\n\t"
            "v_mul_f32 v186, v230, v186\n\t"
            "v_mul_f32 v187, v231, v187\n\t"
            "v_cvt_pk_bf16_f32 v184, v184, v185\n\t"
            "v_mul_f32 v190, v230, v190\n\t"
            "v_mul_f32 v191, v231, v191\n\t"
            "v_cvt_pk_bf16_f32 v188, v188, v189\n\t"
            "v_cvt_pk_bf16_f32 v161, v162, v163\n\t"
            "v_cvt_pk_bf16_f32 v165, v166, v167\n\t"
            "v_cvt_pk_bf16_f32 v177, v178, v179\n\t"
            "v_cvt_pk_bf16_f32 v181, v182, v183\n\t"
            "v_cvt_pk_bf16_f32 v185, v186, v187\n\t"
            "v_cvt_pk_bf16_f32 v189, v190, v191\n\t"
            "ds_read_b64 v[162:163], v253 offset:112\n\t"
            "ds_read_b64 v[166:167], v253 offset:1856\n\t"
            "s_waitcnt lgkmcnt(14)\n\t"
            "ds_write_b64 v244, v[160:161]\n\t"
            "s_waitcnt lgkmcnt(14)\n\t"
            "ds_read_b64 v[160:161], v253 offset:16\n\t"
            "s_waitcnt lgkmcnt(14)\n\t"
            "ds_write_b64 v244, v[164:165] offset:1536\n\t"
            "s_waitcnt lgkmcnt(14)\n\t"
            "ds_read_b64 v[164:165], v253 offset:1760\n\t"
            "s_waitcnt lgkmcnt(14)\n\t"
            "ds_write_b64 v244, v[176:177] offset:3072\n\t"
            "s_waitcnt lgkmcnt(14)\n\t"
            "ds_write_b64 v244, v[180:181] offset:4608\n\t"
            "s_waitcnt lgkmcnt(14)\n\t"
            "ds_write_b64 v244, v[184:185] offset:6144\n\t"
            "s_waitcnt lgkmcnt(14)\n\t"
            "ds_write_b64 v244, v[188:189] offset:7680\n\t"
            "s_waitcnt lgkmcnt(0)\n\t"
            "s_barrier\n\t"
            "s_cmp_lt_u32 s46, 0x7000000\n\t"
            "s_cbranch_scc1 .Lwc_loop%=\n\t"
            "s_waitcnt lgkmcnt(0)\n\t"
            "s_nop 7\n\t"
            "s_setprio 3\n\t"
            "v_add_u32 v236, s19, v252\n\t"
            "ds_read_b128 v[244:247], v236\n\t"
            "s_add_u32 s70, s33, 73728\n\t"
            "s_waitcnt lgkmcnt(0)\n\t"
            "buffer_load_dwordx4 v[200:203], v244, s[28:31], s21 offen\n\t"
            "buffer_load_dwordx4 v[204:207], v245, s[28:31], s21 offen\n\t"
            "buffer_load_dwordx4 v[208:211], v246, s[28:31], s21 offen\n\t"
            "s_add_u32 s21, s21, 6144\n\t"
            "buffer_load_dwordx4 v[212:215], v244, s[28:31], s21 offen\n\t"
            "buffer_load_dwordx4 v[216:219], v245, s[28:31], s21 offen\n\t"
            "buffer_load_dwordx4 v[220:223], v246, s[28:31], s21 offen\n\t"
            "s_add_u32 s49, s70, 0\n\t"
            "buffer_load_dwordx4 v[0:3], v247, s[28:31], s49 offen sc1\n\t"
            "s_add_u32 s49, s70, 6144\n\t"
            "buffer_load_dwordx4 v[24:27], v247, s[28:31], s49 offen sc1\n\t"
            "s_add_u32 s49, s70, 12288\n\t"
            "buffer_load_dwordx4 v[48:51], v247, s[28:31], s49 offen sc1\n\t"
            "s_add_u32 s49, s70, 18432\n\t"
            "buffer_load_dwordx4 v[72:75], v247, s[28:31], s49 offen sc1\n\t"
            "s_add_u32 s49, s70, 24576\n\t"
            "buffer_load_dwordx4 v[96:99], v247, s[28:31], s49 offen sc1\n\t"
            "s_add_u32 s49, s70, 30720\n\t"
            "buffer_load_dwordx4 v[120:123], v247, s[28:31], s49 offen sc1\n\t"
            "s_add_u32 s49, s70, 1024\n\t"
            "buffer_load_dwordx4 v[4:7], v247, s[28:31], s49 offen sc1\n\t"
            "s_add_u32 s49, s70, 7168\n\t"
            "buffer_load_dwordx4 v[28:31], v247, s[28:31], s49 offen sc1\n\t"
            "s_add_u32 s49, s70, 13312\n\t"
            "buffer_load_dwordx4 v[52:55], v247, s[28:31], s49 offen sc1\n\t"
            "s_add_u32 s49, s70, 19456\n\t"
            "buffer_load_dwordx4 v[76:79], v247, s[28:31], s49 offen sc1\n\t"
            "s_add_u32 s49, s70, 25600\n\t"
            "buffer_load_dwordx4 v[100:103], v247, s[28:31], s49 offen sc1\n\t"
            "s_add_u32 s49, s70, 31744\n\t"
            "buffer_load_dwordx4 v[124:127], v247, s[28:31], s49 offen sc1\n\t"
            "s_add_u32 s49, s70, 2048\n\t"
            "buffer_load_dwordx4 v[8:11], v247, s[28:31], s49 offen sc1\n\t"
            "s_add_u32 s49, s70, 8192\n\t"
            "buffer_load_dwordx4 v[32:35], v247, s[28:31], s49 offen sc1\n\t"
            "s_add_u32 s49, s70, 14336\n\t"
            "buffer_load_dwordx4 v[56:59], v247, s[28:31], s49 offen sc1\n\t"
            "s_add_u32 s49, s70, 20480\n\t"
            "buffer_load_dwordx4 v[80:83], v247, s[28:31], s49 offen sc1\n\t"
            "s_add_u32 s49, s70, 26624\n\t"
            "buffer_load_dwordx4 v[104:107], v247, s[28:31], s49 offen sc1\n\t"
            "s_add_u32 s49, s70, 32768\n\t"
            "buffer_load_dwordx4 v[128:131], v247, s[28:31], s49 offen sc1\n\t"
            "s_add_u32 s49, s70, 3072\n\t"
            "buffer_load_dwordx4 v[12:15], v247, s[28:31], s49 offen sc1\n\t"
            "s_add_u32 s49, s70, 9216\n\t"
            "buffer_load_dwordx4 v[36:39], v247, s[28:31], s49 offen sc1\n\t"
            "s_add_u32 s49, s70, 15360\n\t"
            "buffer_load_dwordx4 v[60:63], v247, s[28:31], s49 offen sc1\n\t"
            "s_add_u32 s49, s70, 21504\n\t"
            "buffer_load_dwordx4 v[84:87], v247, s[28:31], s49 offen sc1\n\t"
            "s_add_u32 s49, s70, 27648\n\t"
            "buffer_load_dwordx4 v[108:111], v247, s[28:31], s49 offen sc1\n\t"
            "s_add_u32 s49, s70, 33792\n\t"
            "buffer_load_dwordx4 v[132:135], v247, s[28:31], s49 offen sc1\n\t"
            "s_add_u32 s49, s70, 4096\n\t"
            "buffer_load_dwordx4 v[16:19], v247, s[28:31], s49 offen sc1\n\t"
            "s_add_u32 s49, s70, 10240\n\t"
            "buffer_load_dwordx4 v[40:43], v247, s[28:31], s49 offen sc1\n\t"
            "s_add_u32 s49, s70, 16384\n\t"
            "buffer_load_dwordx4 v[64:67], v247, s[28:31], s49 offen sc1\n\t"
            "s_add_u32 s49, s70, 22528\n\t"
            "buffer_load_dwordx4 v[88:91], v247, s[28:31], s49 offen sc1\n\t"
            "s_add_u32 s49, s70, 28672\n\t"
            "buffer_load_dwordx4 v[112:115], v247, s[28:31], s49 offen sc1\n\t"
            "s_add_u32 s49, s70, 34816\n\t"
            "buffer_load_dwordx4 v[136:139], v247, s[28:31], s49 offen sc1\n\t"
            "s_add_u32 s49, s70, 5120\n\t"
            "buffer_load_dwordx4 v[20:23], v247, s[28:31], s49 offen sc1\n\t"
            "s_add_u32 s49, s70, 11264\n\t"
            "buffer_load_dwordx4 v[44:47], v247, s[28:31], s49 offen sc1\n\t"
            "s_add_u32 s49, s70, 17408\n\t"
            "buffer_load_dwordx4 v[68:71], v247, s[28:31], s49 offen sc1\n\t"
            "s_add_u32 s49, s70, 23552\n\t"
            "buffer_load_dwordx4 v[92:95], v247, s[28:31], s49 offen sc1\n\t"
            "s_add_u32 s49, s70, 29696\n\t"
            "buffer_load_dwordx4 v[116:119], v247, s[28:31], s49 offen sc1\n\t"
            "s_add_u32 s49, s70, 35840\n\t"
            "buffer_load_dwordx4 v[140:143], v247, s[28:31], s49 offen sc1\n\t"
            "s_nop 7\n\t"
            "v_bfe_u32 v248, v247, 2, 6\n\t"
            "v_add_u32 v248, s23, v248\n\t"
            "v_bfe_u32 v249, v247, 2, 6\n\t"
            "v_add_u32 v249, s23, v249\n\t"
            "v_lshrrev_b32 v236, 4, v247\n\t"
            "v_and_b32 v236, 48, v236\n\t"
            "v_add_u32 v236, s20, v236\n\t"
            "v_add_u32 v242, s22, v242\n\t"
            "v_xor_b32 v243, 64, v242\n\t"
            "ds_read_b32 v248, v248\n\t"
            "ds_read_b32 v249, v249 offset:64\n\t"
            "s_waitcnt lgkmcnt(1)\n\t"
            "v_ashrrev_i32 v237, 31, v248\n\t"
            "v_and_b32 v237, 1177344, v237\n\t"
            "v_sub_u32 v236, v236, v237\n\t"
            "v_bfe_i32 v237, v248, 30, 1\n\t"
            "v_and_b32 v237, 2304, v237\n\t"
            "v_sub_u32 v236, v236, v237\n\t"
            "v_and_b32 v248, 0x7ffffff, v248\n\t"
            "v_add_u32 v248, v248, v236\n\t"
            "v_lshrrev_b32 v236, 4, v247\n\t"
            "v_and_b32 v236, 48, v236\n\t"
            "s_waitcnt lgkmcnt(0)\n\t"
            "v_ashrrev_i32 v237, 31, v249\n\t"
            "v_add_u32 v236, s20, v236\n\t"
            "v_and_b32 v237, 1177344, v237\n\t"
            "v_sub_u32 v236, v236, v237\n\t"
            "v_bfe_i32 v237, v249, 30, 1\n\t"
            "v_and_b32 v237, 2304, v237\n\t"
            "v_sub_u32 v236, v236, v237\n\t"
            "v_and_b32 v249, 0x7ffffff, v249\n\t"
            "v_add_u32 v249, v249, v236\n\t"
            "s_nop 7\n\t"
            "v_mov_b32 v144, s23\n\t"
            ".Ldv%=:\n\t"
            "ds_read_b128 v[148:151], v144 offset:96\n\t"
            "s_waitcnt lgkmcnt(0)\n\t"
            "v_or_b32 v148, v148, v149\n\t"
            "v_or3_b32 v148, v148, v150, v151\n\t"
            "v_cmp_gt_i32 vcc, 0, v148\n\t"
            "s_nop 4\n\t"
            "s_cbranch_vccnz .Ldv%=\n\t"
            "s_waitcnt vmcnt(0)\n\t"
            "s_nop 7\n\t"
            "ds_read_b128 v[184:187], v242 offset:53760\n\t"
            "ds_read_b128 v[188:191], v243 offset:53760\n\t"
            "ds_read_b128 v[192:195], v242 offset:53888\n\t"
            "v_lshlrev_b32 v152, 16, v200\n\t"
            "v_and_b32 v153, 0xffff0000, v200\n\t"
            "v_lshlrev_b32 v154, 16, v201\n\t"
            "v_and_b32 v155, 0xffff0000, v201\n\t"
            "v_lshlrev_b32 v156, 16, v202\n\t"
            "v_and_b32 v157, 0xffff0000, v202\n\t"
            "v_lshlrev_b32 v158, 16, v203\n\t"
            "v_and_b32 v159, 0xffff0000, v203\n\t"
            "v_lshlrev_b32 v160, 16, v204\n\t"
            "v_and_b32 v161, 0xffff0000, v204\n\t"
            "v_lshlrev_b32 v162, 16, v205\n\t"
            "v_and_b32 v163, 0xffff0000, v205\n\t"
            "v_lshlrev_b32 v164, 16, v206\n\t"
            "v_and_b32 v165, 0xffff0000, v206\n\t"
            "v_lshlrev_b32 v166, 16, v207\n\t"
            "v_and_b32 v167, 0xffff0000, v207\n\t"
            "v_lshlrev_b32 v176, 16, v208\n\t"
            "v_and_b32 v177, 0xffff0000, v208\n\t"
            "v_lshlrev_b32 v178, 16, v209\n\t"
            "v_and_b32 v179, 0xffff0000, v209\n\t"
            "v_lshlrev_b32 v180, 16, v210\n\t"
            "v_and_b32 v181, 0xffff0000, v210\n\t"
            "v_lshlrev_b32 v182, 16, v211\n\t"
            "v_and_b32 v183, 0xffff0000, v211\n\t"
            "v_lshlrev_b32 v144, 16, v212\n\t"
            "v_and_b32 v145, 0xffff0000, v212\n\t"
            "v_lshlrev_b32 v146, 16, v213\n\t"
            "v_and_b32 v147, 0xffff0000, v213\n\t"
            "s_waitcnt lgkmcnt(2)\n\t"
            "v_mfma_f32_16x16x32_bf16 v[152:155], v[0:3], v[184:187], v[152:155]\n\t"
            "v_lshlrev_b32 v148, 16, v214\n\t"
            "v_and_b32 v149, 0xffff0000, v214\n\t"
            "v_mfma_f32_16x16x32_bf16 v[156:159], v[24:27], v[184:187], v[156:159]\n\t"
            "v_lshlrev_b32 v150, 16, v215\n\t"
            "v_and_b32 v151, 0xffff0000, v215\n\t"
            "v_mfma_f32_16x16x32_bf16 v[160:163], v[48:51], v[184:187], v[160:163]\n\t"
            "v_lshlrev_b32 v168, 16, v216\n\t"
            "v_and_b32 v169, 0xffff0000, v216\n\t"
            "v_mfma_f32_16x16x32_bf16 v[164:167], v[72:75], v[184:187], v[164:167]\n\t"
            "v_lshlrev_b32 v170, 16, v217\n\t"
            "v_and_b32 v171, 0xffff0000, v217\n\t"
            "v_mfma_f32_16x16x32_bf16 v[176:179], v[96:99], v[184:187], v[176:179]\n\t"
            "v_lshlrev_b32 v172, 16, v218\n\t"
            "v_and_b32 v173, 0xffff0000, v218\n\t"
            "v_mfma_f32_16x16x32_bf16 v[180:183], v[120:123], v[184:187], v[180:183]\n\t"
            "ds_read_b128 v[184:187], v243 offset:53888\n\t"
            "v_lshlrev_b32 v174, 16, v219\n\t"
            "s_waitcnt lgkmcnt(2)\n\t"
            "v_mfma_f32_16x16x32_bf16 v[152:155], v[4:7], v[188:191], v[152:155]\n\t"
            "v_and_b32 v175, 0xffff0000, v219\n\t"
            "v_mfma_f32_16x16x32_bf16 v[156:159], v[28:31], v[188:191], v[156:159]\n\t"
            "v_mfma_f32_16x16x32_bf16 v[160:163], v[52:55], v[188:191], v[160:163]\n\t"
            "v_mfma_f32_16x16x32_bf16 v[164:167], v[76:79], v[188:191], v[164:167]\n\t"
            "v_mfma_f32_16x16x32_bf16 v[176:179], v[100:103], v[188:191], v[176:179]\n\t"
            "v_mfma_f32_16x16x32_bf16 v[180:183], v[124:127], v[188:191], v[180:183]\n\t"
            "ds_read_b128 v[188:191], v242 offset:54016\n\t"
            "s_waitcnt lgkmcnt(2)\n\t"
            "v_mfma_f32_16x16x32_bf16 v[152:155], v[8:11], v[192:195], v[152:155]\n\t"
            "v_mfma_f32_16x16x32_bf16 v[156:159], v[32:35], v[192:195], v[156:159]\n\t"
            "v_mfma_f32_16x16x32_bf16 v[160:163], v[56:59], v[192:195], v[160:163]\n\t"
            "v_mfma_f32_16x16x32_bf16 v[164:167], v[80:83], v[192:195], v[164:167]\n\t"
            "v_mfma_f32_16x16x32_bf16 v[176:179], v[104:107], v[192:195], v[176:179]\n\t"
            "v_mfma_f32_16x16x32_bf16 v[180:183], v[128:131], v[192:195], v[180:183]\n\t"
            "ds_read_b128 v[192:195], v243 offset:54016\n\t"
            "s_waitcnt lgkmcnt(2)\n\t"
            "v_mfma_f32_16x16x32_bf16 v[152:155], v[12:15], v[184:187], v[152:155]\n\t"
            "v_mfma_f32_16x16x32_bf16 v[156:159], v[36:39], v[184:187], v[156:159]\n\t"
            "v_mfma_f32_16x16x32_bf16 v[160:163], v[60:63], v[184:187], v[160:163]\n\t"
            "v_mfma_f32_16x16x32_bf16 v[164:167], v[84:87], v[184:187], v[164:167]\n\t"
            "v_mfma_f32_16x16x32_bf16 v[176:179], v[108:111], v[184:187], v[176:179]\n\t"
            "v_mfma_f32_16x16x32_bf16 v[180:183], v[132:135], v[184:187], v[180:183]\n\t"
            "ds_read_b128 v[184:187], v242 offset:59904\n\t"
            "s_waitcnt lgkmcnt(2)\n\t"
            "v_mfma_f32_16x16x32_bf16 v[152:155], v[16:19], v[188:191], v[152:155]\n\t"
            "v_mfma_f32_16x16x32_bf16 v[156:159], v[40:43], v[188:191], v[156:159]\n\t"
            "v_mfma_f32_16x16x32_bf16 v[160:163], v[64:67], v[188:191], v[160:163]\n\t"
            "v_mfma_f32_16x16x32_bf16 v[164:167], v[88:91], v[188:191], v[164:167]\n\t"
            "v_mfma_f32_16x16x32_bf16 v[176:179], v[112:115], v[188:191], v[176:179]\n\t"
            "v_mfma_f32_16x16x32_bf16 v[180:183], v[136:139], v[188:191], v[180:183]\n\t"
            "ds_read_b128 v[188:191], v243 offset:59904\n\t"
            "s_waitcnt lgkmcnt(2)\n\t"
            "v_mfma_f32_16x16x32_bf16 v[152:155], v[20:23], v[192:195], v[152:155]\n\t"
            "v_mfma_f32_16x16x32_bf16 v[156:159], v[44:47], v[192:195], v[156:159]\n\t"
            "v_mfma_f32_16x16x32_bf16 v[160:163], v[68:71], v[192:195], v[160:163]\n\t"
            "v_mfma_f32_16x16x32_bf16 v[164:167], v[92:95], v[192:195], v[164:167]\n\t"
            "v_mfma_f32_16x16x32_bf16 v[176:179], v[116:119], v[192:195], v[176:179]\n\t"
            "v_mfma_f32_16x16x32_bf16 v[180:183], v[140:143], v[192:195], v[180:183]\n\t"
            "ds_read_b128 v[192:195], v242 offset:60032\n\t"
            "s_waitcnt lgkmcnt(2)\n\t"
            "v_mfma_f32_16x16x32_bf16 v[144:147], v[0:3], v[184:187], v[144:147]\n\t"
            "v_cvt_pk_bf16_f32 v152, v152, v153\n\t"
            "v_cvt_pk_bf16_f32 v153, v154, v155\n\t"
            "v_mfma_f32_16x16x32_bf16 v[148:151], v[24:27], v[184:187], v[148:151]\n\t"
            "v_cvt_pk_bf16_f32 v160, v160, v161\n\t"
            "v_cvt_pk_bf16_f32 v161, v162, v163\n\t"
            "v_mfma_f32_16x16x32_bf16 v[168:171], v[48:51], v[184:187], v[168:171]\n\t"
            "v_cvt_pk_bf16_f32 v154, v156, v157\n\t"
            "v_cvt_pk_bf16_f32 v155, v158, v159\n\t"
            "v_mfma_f32_16x16x32_bf16 v[172:175], v[72:75], v[184:187], v[172:175]\n\t"
            "v_cvt_pk_bf16_f32 v162, v164, v165\n\t"
            "v_cvt_pk_bf16_f32 v163, v166, v167\n\t"
            "s_waitcnt lgkmcnt(1)\n\t"
            "v_mfma_f32_16x16x32_bf16 v[144:147], v[4:7], v[188:191], v[144:147]\n\t"
            "v_lshlrev_b32 v156, 16, v220\n\t"
            "v_and_b32 v157, 0xffff0000, v220\n\t"
            "v_mfma_f32_16x16x32_bf16 v[148:151], v[28:31], v[188:191], v[148:151]\n\t"
            "v_lshlrev_b32 v158, 16, v221\n\t"
            "v_and_b32 v159, 0xffff0000, v221\n\t"
            "v_mfma_f32_16x16x32_bf16 v[168:171], v[52:55], v[188:191], v[168:171]\n\t"
            "v_lshlrev_b32 v164, 16, v222\n\t"
            "v_and_b32 v165, 0xffff0000, v222\n\t"
            "v_mfma_f32_16x16x32_bf16 v[156:159], v[96:99], v[184:187], v[156:159]\n\t"
            "v_lshlrev_b32 v166, 16, v223\n\t"
            "v_and_b32 v167, 0xffff0000, v223\n\t"
            "v_mfma_f32_16x16x32_bf16 v[172:175], v[76:79], v[188:191], v[172:175]\n\t"
            "buffer_store_dwordx4 v[152:155], v248, s[40:43], 0 offen sc1\n\t"
            "v_mfma_f32_16x16x32_bf16 v[164:167], v[120:123], v[184:187], v[164:167]\n\t"
            "ds_read_b128 v[184:187], v243 offset:60032\n\t"
            "buffer_store_dwordx4 v[160:163], v248, s[40:43], 0 offen offset:64 sc1\n\t"
            "v_mfma_f32_16x16x32_bf16 v[156:159], v[100:103], v[188:191], v[156:159]\n\t"
            "v_cvt_pk_bf16_f32 v176, v176, v177\n\t"
            "v_cvt_pk_bf16_f32 v177, v178, v179\n\t"
            "v_mfma_f32_16x16x32_bf16 v[164:167], v[124:127], v[188:191], v[164:167]\n\t"
            "ds_read_b128 v[188:191], v242 offset:60160\n\t"
            "v_cvt_pk_bf16_f32 v178, v180, v181\n\t"
            "s_waitcnt lgkmcnt(2)\n\t"
            "v_mfma_f32_16x16x32_bf16 v[144:147], v[8:11], v[192:195], v[144:147]\n\t"
            "v_cvt_pk_bf16_f32 v179, v182, v183\n\t"
            "v_mfma_f32_16x16x32_bf16 v[148:151], v[32:35], v[192:195], v[148:151]\n\t"
            "buffer_store_dwordx4 v[176:179], v248, s[40:43], 0 offen offset:128 sc1\n\t"
            "v_mfma_f32_16x16x32_bf16 v[168:171], v[56:59], v[192:195], v[168:171]\n\t"
            "v_mfma_f32_16x16x32_bf16 v[172:175], v[80:83], v[192:195], v[172:175]\n\t"
            "v_mfma_f32_16x16x32_bf16 v[156:159], v[104:107], v[192:195], v[156:159]\n\t"
            "v_mfma_f32_16x16x32_bf16 v[164:167], v[128:131], v[192:195], v[164:167]\n\t"
            "ds_read_b128 v[192:195], v243 offset:60160\n\t"
            "s_waitcnt lgkmcnt(2)\n\t"
            "v_mfma_f32_16x16x32_bf16 v[144:147], v[12:15], v[184:187], v[144:147]\n\t"
            "v_mfma_f32_16x16x32_bf16 v[148:151], v[36:39], v[184:187], v[148:151]\n\t"
            "s_waitcnt lgkmcnt(1)\n\t"
            "v_mfma_f32_16x16x32_bf16 v[144:147], v[16:19], v[188:191], v[144:147]\n\t"
            "v_mfma_f32_16x16x32_bf16 v[168:171], v[60:63], v[184:187], v[168:171]\n\t"
            "v_mfma_f32_16x16x32_bf16 v[148:151], v[40:43], v[188:191], v[148:151]\n\t"
            "v_mfma_f32_16x16x32_bf16 v[172:175], v[84:87], v[184:187], v[172:175]\n\t"
            "v_mfma_f32_16x16x32_bf16 v[168:171], v[64:67], v[188:191], v[168:171]\n\t"
            "v_mfma_f32_16x16x32_bf16 v[156:159], v[108:111], v[184:187], v[156:159]\n\t"
            "s_waitcnt lgkmcnt(0)\n\t"
            "v_mfma_f32_16x16x32_bf16 v[144:147], v[20:23], v[192:195], v[144:147]\n\t"
            "v_mfma_f32_16x16x32_bf16 v[148:151], v[44:47], v[192:195], v[148:151]\n\t"
            "v_mfma_f32_16x16x32_bf16 v[172:175], v[88:91], v[188:191], v[172:175]\n\t"
            "v_mfma_f32_16x16x32_bf16 v[164:167], v[132:135], v[184:187], v[164:167]\n\t"
            "v_mfma_f32_16x16x32_bf16 v[168:171], v[68:71], v[192:195], v[168:171]\n\t"
            "v_mfma_f32_16x16x32_bf16 v[156:159], v[112:115], v[188:191], v[156:159]\n\t"
            "v_mfma_f32_16x16x32_bf16 v[172:175], v[92:95], v[192:195], v[172:175]\n\t"
            "s_nop 1\n\t"
            "v_cvt_pk_bf16_f32 v144, v144, v145\n\t"
            "v_mfma_f32_16x16x32_bf16 v[164:167], v[136:139], v[188:191], v[164:167]\n\t"
            "v_cvt_pk_bf16_f32 v145, v146, v147\n\t"
            "v_cvt_pk_bf16_f32 v146, v148, v149\n\t"
            "v_mfma_f32_16x16x32_bf16 v[156:159], v[116:119], v[192:195], v[156:159]\n\t"
            "v_cvt_pk_bf16_f32 v147, v150, v151\n\t"
            "v_cvt_pk_bf16_f32 v168, v168, v169\n\t"
            "v_mfma_f32_16x16x32_bf16 v[164:167], v[140:143], v[192:195], v[164:167]\n\t"
            "buffer_store_dwordx4 v[144:147], v249, s[40:43], 0 offen sc1\n\t"
            "v_cvt_pk_bf16_f32 v169, v170, v171\n\t"
            "v_cvt_pk_bf16_f32 v170, v172, v173\n\t"
            "v_cvt_pk_bf16_f32 v171, v174, v175\n\t"
            "s_nop 0\n\t"
            "v_cvt_pk_bf16_f32 v156, v156, v157\n\t"
            "v_cvt_pk_bf16_f32 v157, v158, v159\n\t"
            "buffer_store_dwordx4 v[168:171], v249, s[40:43], 0 offen offset:64 sc1\n\t"
            "v_cvt_pk_bf16_f32 v158, v164, v165\n\t"
            "v_cvt_pk_bf16_f32 v159, v166, v167\n\t"
            "buffer_store_dwordx4 v[156:159], v249, s[40:43], 0 offen offset:128 sc1\n\t"
            "s_nop 7\n\t"
            "s_branch .Lre%=\n\t"
            ".Lrp%=:\n\t"
            "v_add_u32 v236, s19, v252\n\t"
            "ds_write_b128 v236, v[244:247]\n\t"
            "v_add_u32 v236, s48, v244\n\t"
            "v_add_u32 v237, s48, v245\n\t"
            "v_add_u32 v248, s48, v246\n\t"
            "ds_read_b128 v[200:203], v236\n\t"
            "ds_read_b128 v[204:207], v237\n\t"
            "ds_read_b128 v[208:211], v248\n\t"
            "ds_read_b128 v[212:215], v236 offset:6144\n\t"
            "ds_read_b128 v[216:219], v237 offset:6144\n\t"
            "ds_read_b128 v[220:223], v248 offset:6144\n\t"
            "ds_read_b128 v[224:227], v236 offset:12288\n\t"
            "ds_read_b128 v[228:231], v237 offset:12288\n\t"
            "ds_read_b128 v[232:235], v248 offset:12288\n\t"
            "s_add_u32 s49, s33, 0\n\t"
            "buffer_load_dwordx4 v[0:3], v247, s[28:31], s49 offen sc1\n\t"
            "s_add_u32 s49, s33, 1024\n\t"
            "buffer_load_dwordx4 v[4:7], v247, s[28:31], s49 offen sc1\n\t"
            "s_add_u32 s49, s33, 2048\n\t"
            "buffer_load_dwordx4 v[8:11], v247, s[28:31], s49 offen sc1\n\t"
            "s_add_u32 s49, s33, 3072\n\t"
            "buffer_load_dwordx4 v[12:15], v247, s[28:31], s49 offen sc1\n\t"
            "s_add_u32 s49, s33, 4096\n\t"
            "buffer_load_dwordx4 v[16:19], v247, s[28:31], s49 offen sc1\n\t"
            "s_add_u32 s49, s33, 5120\n\t"
            "buffer_load_dwordx4 v[20:23], v247, s[28:31], s49 offen sc1\n\t"
            "s_add_u32 s49, s33, 6144\n\t"
            "buffer_load_dwordx4 v[24:27], v247, s[28:31], s49 offen sc1\n\t"
            "s_add_u32 s49, s33, 7168\n\t"
            "buffer_load_dwordx4 v[28:31], v247, s[28:31], s49 offen sc1\n\t"
            "s_add_u32 s49, s33, 8192\n\t"
            "buffer_load_dwordx4 v[32:35], v247, s[28:31], s49 offen sc1\n\t"
            "s_add_u32 s49, s33, 9216\n\t"
            "buffer_load_dwordx4 v[36:39], v247, s[28:31], s49 offen sc1\n\t"
            "s_add_u32 s49, s33, 10240\n\t"
            "buffer_load_dwordx4 v[40:43], v247, s[28:31], s49 offen sc1\n\t"
            "s_add_u32 s49, s33, 11264\n\t"
            "buffer_load_dwordx4 v[44:47], v247, s[28:31], s49 offen sc1\n\t"
            "s_add_u32 s49, s33, 12288\n\t"
            "buffer_load_dwordx4 v[48:51], v247, s[28:31], s49 offen sc1\n\t"
            "s_add_u32 s49, s33, 13312\n\t"
            "buffer_load_dwordx4 v[52:55], v247, s[28:31], s49 offen sc1\n\t"
            "s_add_u32 s49, s33, 14336\n\t"
            "buffer_load_dwordx4 v[56:59], v247, s[28:31], s49 offen sc1\n\t"
            "s_add_u32 s49, s33, 15360\n\t"
            "buffer_load_dwordx4 v[60:63], v247, s[28:31], s49 offen sc1\n\t"
            "s_add_u32 s49, s33, 16384\n\t"
            "buffer_load_dwordx4 v[64:67], v247, s[28:31], s49 offen sc1\n\t"
            "s_add_u32 s49, s33, 17408\n\t"
            "buffer_load_dwordx4 v[68:71], v247, s[28:31], s49 offen sc1\n\t"
            "s_add_u32 s49, s33, 28672\n\t"
            "buffer_load_dwordx4 v[112:115], v247, s[28:31], s49 offen sc1\n\t"
            "s_add_u32 s49, s33, 29696\n\t"
            "buffer_load_dwordx4 v[116:119], v247, s[28:31], s49 offen sc1\n\t"
            "s_add_u32 s49, s33, 30720\n\t"
            "buffer_load_dwordx4 v[120:123], v247, s[28:31], s49 offen sc1\n\t"
            "s_add_u32 s49, s33, 31744\n\t"
            "buffer_load_dwordx4 v[124:127], v247, s[28:31], s49 offen sc1\n\t"
            "s_add_u32 s49, s33, 32768\n\t"
            "buffer_load_dwordx4 v[128:131], v247, s[28:31], s49 offen sc1\n\t"
            "s_add_u32 s49, s33, 33792\n\t"
            "buffer_load_dwordx4 v[132:135], v247, s[28:31], s49 offen sc1\n\t"
            "s_add_u32 s49, s33, 34816\n\t"
            "buffer_load_dwordx4 v[136:139], v247, s[28:31], s49 offen sc1\n\t"
            "s_add_u32 s49, s33, 35840\n\t"
            "buffer_load_dwordx4 v[140:143], v247, s[28:31], s49 offen sc1\n\t"
            "s_add_u32 s45, s46, s27\n\t"
            "s_mov_b32 s46, s47\n\t"
            "s_mov_b32 s47, s68\n\t"
            "s_xor_b32 s48, s48, s71\n\t"
            "s_xor_b32 s67, s67, s71\n\t"
            "s_add_u32 s76, s76, 4\n\t"
            "s_lshl_b32 s56, s56, 2\n\t"
            "s_lshr_b32 s49, s26, 30\n\t"
            "s_or_b32 s56, s56, s49\n\t"
            "s_cmp_ge_u32 s76, s24\n\t"
            "s_cselect_b64 vcc, -1, 0\n\t"
            "s_waitcnt lgkmcnt(0)\n\t"
            "s_barrier\n\t"
            "ds_read_b128 v[168:171], v254\n\t"
            "ds_read_b128 v[172:175], v254 offset:64\n\t"
            "s_waitcnt vmcnt(0)\n\t"
            "ds_read_b64 v[144:145], v253\n\t"
            "ds_read_b64 v[146:147], v253 offset:96\n\t"
            "ds_read_b64 v[148:149], v253 offset:1744\n\t"
            "ds_read_b64 v[150:151], v253 offset:1840\n\t"
            "ds_read_b64 v[152:153], v253 offset:8\n\t"
            "ds_read_b64 v[154:155], v253 offset:104\n\t"
            "ds_read_b64 v[156:157], v253 offset:1752\n\t"
            "ds_read_b64 v[158:159], v253 offset:1848\n\t"
            "ds_read_b64 v[160:161], v253 offset:16\n\t"
            "ds_read_b64 v[162:163], v253 offset:112\n\t"
            "ds_read_b64 v[164:165], v253 offset:1760\n\t"
            "ds_read_b64 v[166:167], v253 offset:1856\n\t"
            "s_waitcnt lgkmcnt(0)\n\t"
            "s_nop 7\n\t"
            ".Lwb_loop%=:\n\t"
            "s_setprio 1\n\t"
            "v_mov_b64 v[176:177], 0\n\t"
            "v_mov_b64 v[178:179], 0\n\t"
            "v_mov_b64 v[180:181], 0\n\t"
            "v_mov_b64 v[182:183], 0\n\t"
            "s_setprio 3\n\t"
            "v_smfmac_f32_16x16x64_bf16 v[176:179], v[168:171], v[144:151], v255\n\t"
            "ds_read_b128 v[168:171], v254 offset:128\n\t"
            "s_waitcnt lgkmcnt(5)\n\t"
            "v_smfmac_f32_16x16x64_bf16 v[180:183], v[172:175], v[152:159], v255\n\t"
            "s_setprio 1\n\t"
            "ds_read_b64 v[144:145], v253 offset:24\n\t"
            "ds_read_b64 v[146:147], v253 offset:120\n\t"
            "ds_read_b64 v[148:149], v253 offset:1768\n\t"
            "ds_read_b64 v[150:151], v253 offset:1864\n\t"
            "ds_read_b128 v[172:175], v254 offset:192\n\t"
            "ds_read_b64 v[152:153], v253 offset:32\n\t"
            "ds_read_b64 v[154:155], v253 offset:128\n\t"
            "ds_read_b64 v[156:157], v253 offset:1776\n\t"
            "ds_read_b64 v[158:159], v253 offset:1872\n\t"
            "v_mov_b32 v249, s76\n\t"
            "v_mov_b64 v[184:185], 0\n\t"
            "v_mov_b64 v[186:187], 0\n\t"
            "v_mov_b64 v[188:189], 0\n\t"
            "v_mov_b64 v[190:191], 0\n\t"
            "v_cvt_pk_bf16_f32 v176, v176, v177\n\t"
            "v_add_u32 v236, s67, v250\n\t"
            "v_cvt_pk_bf16_f32 v177, v178, v179\n\t"
            "v_cvt_pk_bf16_f32 v178, v180, v181\n\t"
            "v_cvt_pk_bf16_f32 v179, v182, v183\n\t"
            "s_setprio 3\n\t"
            "v_mfma_f32_4x4x4_16b_bf16 v[196:199], v[176:177], v[176:177], 0\n\t"
            "ds_read_b32 v249, v249\n\t"
            "v_mfma_f32_16x16x32_bf16 v[192:195], v[238:241], v[176:179], 0\n\t"
            "v_mov_b64 v[180:181], 0\n\t"
            "v_mov_b64 v[182:183], 0\n\t"
            "v_mfma_f32_4x4x4_16b_bf16 v[196:199], v[178:179], v[178:179], v[196:199]\n\t"
            "v_add_u32 v248, s67, v251\n\t"
            "s_waitcnt lgkmcnt(10)\n\t"
            "v_smfmac_f32_16x16x64_bf16 v[184:187], v[168:171], v[160:167], v255\n\t"
            "ds_read_b128 v[168:171], v254 offset:256\n\t"
            "ds_read_b64 v[160:161], v253 offset:40\n\t"
            "ds_read_b64 v[162:163], v253 offset:136\n\t"
            "ds_read_b64 v[164:165], v253 offset:1784\n\t"
            "ds_read_b64 v[166:167], v253 offset:1880\n\t"
            "s_waitcnt lgkmcnt(10)\n\t"
            "v_smfmac_f32_16x16x64_bf16 v[188:191], v[172:175], v[144:151], v255\n\t"
            "s_setprio 1\n\t"
            "ds_read_b128 v[172:175], v254 offset:320\n\t"
            "ds_read_b64 v[144:145], v253 offset:48\n\t"
            "ds_read_b64 v[146:147], v253 offset:144\n\t"
            "ds_read_b64 v[148:149], v253 offset:1792\n\t"
            "ds_read_b64 v[150:151], v253 offset:1888\n\t"
            "v_cvt_pk_bf16_f32 v184, v184, v185\n\t"
            "v_cvt_pk_bf16_f32 v185, v186, v187\n\t"
            "v_cvt_pk_bf16_f32 v186, v188, v189\n\t"
            "v_cvt_pk_bf16_f32 v187, v190, v191\n\t"
            "s_setprio 3\n\t"
            "v_mfma_f32_4x4x4_16b_bf16 v[196:199], v[184:185], v[184:185], v[196:199]\n\t"
            "v_mov_b64 v[188:189], 0\n\t"
            "v_mfma_f32_16x16x32_bf16 v[192:195], v[238:241], v[184:187], v[192:195]\n\t"
            "s_waitcnt lgkmcnt(10)\n\t"
            "v_readfirstlane_b32 s26, v249\n\t"
            "v_mov_b64 v[190:191], 0\n\t"
            "v_mfma_f32_4x4x4_16b_bf16 v[196:199], v[186:187], v[186:187], v[196:199]\n\t"
            "ds_read_b32 v249, v252 offset:1024\n\t"
            "s_and_b32 s68, s26, 0x7ffffff\n\t"
            "s_add_u32 s68, s68, s78\n\t"
            "s_and_b32 s69, s26, 0x38000000\n\t"
            "s_or_b32 s69, s69, s39\n\t"
            "s_bitcmp1_b32 s26, 27\n\t"
            "s_cselect_b32 s79, 2.0, s68\n\t"
            "s_and_b32 s57, s56, 3\n\t"
            "s_lshl_b32 s58, s57, 3\n\t"
            "s_lshr_b32 m0, s54, s58\n\t"
            "s_lshl_b32 s58, s57, 4\n\t"
            "s_lshr_b64 s[36:37], s[50:51], s58\n\t"
            "s_and_b32 s57, s36, 0xffff\n\t"
            "s_add_u32 s57, s57, s67\n\t"
            "s_waitcnt lgkmcnt(5)\n\t"
            "v_smfmac_f32_16x16x64_bf16 v[180:183], v[172:175], v[160:167], v255\n\t"
            "s_setprio 1\n\t"
            "s_lshr_b64 s[36:37], s[52:53], s58\n\t"
            "s_and_b32 s58, s36, 0xffff\n\t"
            "s_add_u32 s58, s58, s67\n\t"
            "ds_read_b128 v[172:175], v254 offset:448\n\t"
            "ds_read_b64 v[160:161], v253 offset:64\n\t"
            "ds_read_b64 v[162:163], v253 offset:160\n\t"
            "ds_read_b64 v[164:165], v253 offset:1808\n\t"
            "ds_read_b64 v[166:167], v253 offset:1904\n\t"
            "s_and_b32 s59, s50, 0xffff\n\t"
            "s_add_u32 s59, s59, s67\n\t"
            "s_and_b32 s38, s52, 0xffff\n\t"
            "s_add_u32 s38, s38, s67\n\t"
            "v_writelane_b32 v236, s59, m0\n\t"
            "v_writelane_b32 v236, s57, 63\n\t"
            "v_xor_b32 v237, 16, v236\n\t"
            "v_writelane_b32 v248, s38, m0\n\t"
            "v_writelane_b32 v248, s58, 63\n\t"
            "s_waitcnt lgkmcnt(5)\n\t"
            "v_and_b32 v249, s69, v249\n\t"
            "s_barrier\n\t"
            "ds_write_b128 v236, v[176:179]\n\t"
            "v_mov_b64 v[176:177], 0\n\t"
            "v_mov_b64 v[178:179], 0\n\t"
            "s_setprio 3\n\t"
            "s_nop 0\n\t"
            "v_smfmac_f32_16x16x64_bf16 v[176:179], v[168:171], v[152:159], v255\n\t"
            "s_setprio 1\n\t"
            "ds_read_b128 v[168:171], v254 offset:384\n\t"
            "ds_read_b64 v[152:153], v253 offset:56\n\t"
            "ds_read_b64 v[154:155], v253 offset:152\n\t"
            "ds_read_b64 v[156:157], v253 offset:1800\n\t"
            "ds_read_b64 v[158:159], v253 offset:1896\n\t"
            "ds_write_b128 v237, v[184:187]\n\t"
            "v_mov_b64 v[184:185], 0\n\t"
            "v_mov_b64 v[186:187], 0\n\t"
            "v_cvt_pk_bf16_f32 v176, v176, v177\n\t"
            "v_cvt_pk_bf16_f32 v177, v178, v179\n\t"
            "v_cvt_pk_bf16_f32 v178, v180, v181\n\t"
            "v_cvt_pk_bf16_f32 v179, v182, v183\n\t"
            "s_setprio 3\n\t"
            "v_mfma_f32_4x4x4_16b_bf16 v[196:199], v[176:177], v[176:177], v[196:199]\n\t"
            "ds_write_b128 v236, v[176:179] offset:128\n\t"
            "v_mfma_f32_16x16x32_bf16 v[192:195], v[238:241], v[176:179], v[192:195]\n\t"
            "v_mov_b64 v[176:177], 0\n\t"
            "v_mov_b64 v[180:181], 0\n\t"
            "v_mfma_f32_4x4x4_16b_bf16 v[196:199], v[178:179], v[178:179], v[196:199]\n\t"
            "v_mov_b64 v[178:179], 0\n\t"
            "v_mov_b64 v[182:183], 0\n\t"
            "s_waitcnt lgkmcnt(6)\n\t"
            "v_smfmac_f32_16x16x64_bf16 v[184:187], v[168:171], v[144:151], v255\n\t"
            "ds_read_b128 v[168:171], v254 offset:512\n\t"
            "s_waitcnt lgkmcnt(3)\n\t"
            "v_smfmac_f32_16x16x64_bf16 v[188:191], v[172:175], v[152:159], v255\n\t"
            "s_setprio 1\n\t"
            "ds_read_b64 v[144:145], v253 offset:72\n\t"
            "ds_read_b64 v[146:147], v253 offset:168\n\t"
            "ds_read_b64 v[148:149], v253 offset:1816\n\t"
            "ds_read_b64 v[150:151], v253 offset:1912\n\t"
            "ds_read_b128 v[172:175], v254 offset:576\n\t"
            "ds_read_b64 v[152:153], v253 offset:80\n\t"
            "ds_read_b64 v[154:155], v253 offset:176\n\t"
            "ds_read_b64 v[156:157], v253 offset:1824\n\t"
            "ds_read_b64 v[158:159], v253 offset:1920\n\t"
            "v_cvt_pk_bf16_f32 v184, v184, v185\n\t"
            "v_cvt_pk_bf16_f32 v185, v186, v187\n\t"
            "v_cvt_pk_bf16_f32 v186, v188, v189\n\t"
            "v_cvt_pk_bf16_f32 v187, v190, v191\n\t"
            "s_setprio 3\n\t"
            "v_mfma_f32_4x4x4_16b_bf16 v[196:199], v[184:185], v[184:185], v[196:199]\n\t"
            "ds_write_b128 v237, v[184:187] offset:128\n\t"
            "v_mfma_f32_16x16x32_bf16 v[192:195], v[238:241], v[184:187], v[192:195]\n\t"
            "v_mov_b64 v[184:185], 0\n\t"
            "v_mov_b64 v[188:189], 0\n\t"
            "v_mfma_f32_4x4x4_16b_bf16 v[196:199], v[186:187], v[186:187], v[196:199]\n\t"
            "v_mov_b64 v[186:187], 0\n\t"
            "v_mov_b64 v[190:191], 0\n\t"
            "s_waitcnt lgkmcnt(10)\n\t"
            "v_smfmac_f32_16x16x64_bf16 v[176:179], v[168:171], v[160:167], v255\n\t"
            "ds_read_b128 v[168:171], v254 offset:640\n\t"
            "ds_read_b64 v[160:161], v253 offset:88\n\t"
            "ds_read_b64 v[162:163], v253 offset:184\n\t"
            "ds_read_b64 v[164:165], v253 offset:1832\n\t"
            "ds_read_b64 v[166:167], v253 offset:1928\n\t"
            "s_waitcnt lgkmcnt(10)\n\t"
            "v_smfmac_f32_16x16x64_bf16 v[180:183], v[172:175], v[144:151], v255\n\t"
            "s_setprio 1\n\t"
            "ds_read_b128 v[172:175], v254 offset:704\n\t"
            "v_lshlrev_b32 v144, 16, v212\n\t"
            "v_cvt_pk_bf16_f32 v176, v176, v177\n\t"
            "v_cvt_pk_bf16_f32 v177, v178, v179\n\t"
            "v_and_b32 v145, 0xffff0000, v212\n\t"
            "v_lshlrev_b32 v146, 16, v213\n\t"
            "s_setprio 3\n\t"
            "v_mfma_f32_4x4x4_16b_bf16 v[196:199], v[176:177], v[176:177], v[196:199]\n\t"
            "s_setprio 1\n\t"
            "v_and_b32 v147, 0xffff0000, v213\n\t"
            "v_cvt_pk_bf16_f32 v178, v180, v181\n\t"
            "v_cvt_pk_bf16_f32 v179, v182, v183\n\t"
            "v_lshlrev_b32 v180, 16, v210\n\t"
            "v_and_b32 v181, 0xffff0000, v210\n\t"
            "s_setprio 3\n\t"
            "v_mfma_f32_4x4x4_16b_bf16 v[196:199], v[178:179], v[178:179], v[196:199]\n\t"
            "ds_write_b128 v236, v[176:179] offset:256\n\t"
            "v_mfma_f32_16x16x32_bf16 v[192:195], v[238:241], v[176:179], v[192:195]\n\t"
            "v_lshlrev_b32 v176, 16, v208\n\t"
            "v_and_b32 v177, 0xffff0000, v208\n\t"
            "v_lshlrev_b32 v178, 16, v209\n\t"
            "s_waitcnt lgkmcnt(6)\n\t"
            "v_smfmac_f32_16x16x64_bf16 v[184:187], v[168:171], v[152:159], v255\n\t"
            "s_setprio 1\n\t"
            "v_lshlrev_b32 v152, 16, v200\n\t"
            "v_and_b32 v153, 0xffff0000, v200\n\t"
            "v_lshlrev_b32 v154, 16, v201\n\t"
            "v_and_b32 v155, 0xffff0000, v201\n\t"
            "v_lshlrev_b32 v156, 16, v202\n\t"
            "s_setprio 3\n\t"
            "s_waitcnt lgkmcnt(1)\n\t"
            "v_smfmac_f32_16x16x64_bf16 v[188:191], v[172:175], v[160:167], v255\n\t"
            "s_setprio 1\n\t"
            "v_cvt_pk_bf16_f32 v184, v184, v185\n\t"
            "v_and_b32 v157, 0xffff0000, v202\n\t"
            "v_cvt_pk_bf16_f32 v185, v186, v187\n\t"
            "v_lshlrev_b32 v158, 16, v203\n\t"
            "v_and_b32 v159, 0xffff0000, v203\n\t"
            "s_setprio 3\n\t"
            "v_mfma_f32_4x4x4_16b_bf16 v[196:199], v[184:185], v[184:185], v[196:199]\n\t"
            "s_setprio 1\n\t"
            "s_mov_b32 exec_hi, 0x3fffff\n\t"
            "s_add_u32 m0, s66, 0\n\t"
            "s_add_u32 s49, s79, 0\n\t"
            "buffer_load_dwordx4 v249, s[72:75], s49 offen lds\n\t"
            "s_mov_b32 exec_hi, -1\n\t"
            "v_cvt_pk_bf16_f32 v186, v188, v189\n\t"
            "v_cvt_pk_bf16_f32 v187, v190, v191\n\t"
            "v_lshlrev_b32 v160, 16, v204\n\t"
            "v_and_b32 v161, 0xffff0000, v204\n\t"
            "s_setprio 3\n\t"
            "v_mfma_f32_4x4x4_16b_bf16 v[196:199], v[186:187], v[186:187], v[196:199]\n\t"
            "ds_read_b128 v[188:191], v243\n\t"
            "v_mfma_f32_16x16x32_bf16 v[192:195], v[238:241], v[184:187], v[192:195]\n\t"
            "s_setprio 1\n\t"
            "ds_write_b128 v237, v[184:187] offset:256\n\t"
            "ds_read_b128 v[184:187], v242\n\t"
            "s_mov_b32 exec_hi, 0x3fffff\n\t"
            "s_add_u32 m0, s66, 872\n\t"
            "s_add_u32 s49, s68, 196224\n\t"
            "buffer_load_dwordx4 v249, s[72:75], s49 offen lds\n\t"
            "s_mov_b32 exec_hi, -1\n\t"
            "v_lshlrev_b32 v162, 16, v205\n\t"
            "v_cndmask_b32_e64 v193, v196, v197, s[60:61]\n\t"
            "v_cndmask_b32_e64 v193, v193, v198, s[62:63]\n\t"
            "v_cndmask_b32_e64 v193, v193, v199, s[64:65]\n\t"
            "v_and_b32 v163, 0xffff0000, v205\n\t"
            "v_lshlrev_b32 v164, 16, v206\n\t"
            "v_and_b32 v165, 0xffff0000, v206\n\t"
            "v_lshlrev_b32 v166, 16, v207\n\t"
            "v_and_b32 v167, 0xffff0000, v207\n\t"
            "v_and_b32 v179, 0xffff0000, v209\n\t"
            "v_lshlrev_b32 v182, 16, v211\n\t"
            "v_and_b32 v183, 0xffff0000, v211\n\t"
            "v_lshlrev_b32 v148, 16, v214\n\t"
            "v_and_b32 v149, 0xffff0000, v214\n\t"
            "v_lshlrev_b32 v150, 16, v215\n\t"
            "v_and_b32 v151, 0xffff0000, v215\n\t"
            "v_lshlrev_b32 v168, 16, v216\n\t"
            "v_and_b32 v169, 0xffff0000, v216\n\t"
            "s_setprio 3\n\t"
            "s_waitcnt lgkmcnt(0)\n\t"
            "v_mfma_f32_16x16x32_bf16 v[152:155], v[0:3], v[184:187], v[152:155]\n\t"
            "s_setprio 1\n\t"
            "s_setprio 1\n\t"
            "ds_read_b128 v[196:199], v252\n\t"
            "ds_write_b64 v248, v[192:193] offset:18816\n\t"
            "v_mfma_f32_16x16x32_bf16 v[156:159], v[24:27], v[184:187], v[156:159]\n\t"
            "ds_read_b128 v[192:195], v242 offset:128\n\t"
            "s_mov_b32 exec_hi, 0x3fffff\n\t"
            "s_add_u32 m0, s66, 1744\n\t"
            "s_add_u32 s49, s68, 392448\n\t"
            "buffer_load_dwordx4 v249, s[72:75], s49 offen lds\n\t"
            "s_mov_b32 exec_hi, -1\n\t"
            "v_mfma_f32_16x16x32_bf16 v[160:163], v[48:51], v[184:187], v[160:163]\n\t"
            "s_mov_b32 exec_hi, 0x3fffff\n\t"
            "s_add_u32 m0, s66, 2616\n\t"
            "s_add_u32 s49, s68, 588672\n\t"
            "buffer_load_dwordx4 v249, s[72:75], s49 offen lds\n\t"
            "s_mov_b32 exec_hi, -1\n\t"
            "v_mfma_f32_16x16x32_bf16 v[164:167], v[72:75], v[184:187], v[164:167]\n\t"
            "s_mov_b32 exec_hi, 0x3fffff\n\t"
            "s_add_u32 m0, s66, 3488\n\t"
            "s_add_u32 s49, s68, 784896\n\t"
            "buffer_load_dwordx4 v249, s[72:75], s49 offen lds\n\t"
            "s_mov_b32 exec_hi, -1\n\t"
            "v_mfma_f32_16x16x32_bf16 v[176:179], v[96:99], v[184:187], v[176:179]\n\t"
            "s_mov_b32 exec_hi, 0x3fffff\n\t"
            "s_add_u32 m0, s66, 4360\n\t"
            "s_add_u32 s49, s68, 981120\n\t"
            "buffer_load_dwordx4 v249, s[72:75], s49 offen lds\n\t"
            "s_mov_b32 exec_hi, -1\n\t"
            "v_mfma_f32_16x16x32_bf16 v[180:183], v[120:123], v[184:187], v[180:183]\n\t"
            "ds_read_b128 v[184:187], v243 offset:128\n\t"
            "s_mov_b32 exec_hi, 0x3fffff\n\t"
            "s_add_u32 m0, s66, 5232\n\t"
            "s_add_u32 s49, s68, 1177344\n\t"
            "buffer_load_dwordx4 v249, s[72:75], s49 offen lds\n\t"
            "s_mov_b32 exec_hi, -1\n\t"
            "v_mfma_f32_16x16x32_bf16 v[152:155], v[4:7], v[188:191], v[152:155]\n\t"
            "v_lshlrev_b32 v170, 16, v217\n\t"
            "v_and_b32 v171, 0xffff0000, v217\n\t"
            "v_mfma_f32_16x16x32_bf16 v[156:159], v[28:31], v[188:191], v[156:159]\n\t"
            "v_lshlrev_b32 v172, 16, v218\n\t"
            "v_and_b32 v173, 0xffff0000, v218\n\t"
            "v_mfma_f32_16x16x32_bf16 v[160:163], v[52:55], v[188:191], v[160:163]\n\t"
            "v_lshlrev_b32 v174, 16, v219\n\t"
            "v_and_b32 v175, 0xffff0000, v219\n\t"
            "v_mfma_f32_16x16x32_bf16 v[164:167], v[76:79], v[188:191], v[164:167]\n\t"
            "s_mov_b32 exec_hi, 0x3fffff\n\t"
            "s_add_u32 m0, s66, 6104\n\t"
            "s_add_u32 s49, s68, 1373568\n\t"
            "buffer_load_dwordx4 v249, s[72:75], s49 offen lds\n\t"
            "s_mov_b32 exec_hi, -1\n\t"
            "v_mfma_f32_16x16x32_bf16 v[176:179], v[100:103], v[188:191], v[176:179]\n\t"
            "s_mov_b32 exec_hi, 0x3fffff\n\t"
            "s_add_u32 m0, s66, 6976\n\t"
            "s_add_u32 s49, s68, 1569792\n\t"
            "buffer_load_dwordx4 v249, s[72:75], s49 offen lds\n\t"
            "s_mov_b32 exec_hi, -1\n\t"
            "v_mfma_f32_16x16x32_bf16 v[180:183], v[124:127], v[188:191], v[180:183]\n\t"
            "s_bfe_u32 s57, s56, 0x20004\n\t"
            "s_lshl_b32 s58, s57, 3\n\t"
            "s_lshr_b32 s58, s55, s58\n\t"
            "s_lshl_b64 s[36:37], s[34:35], s58\n\t"
            "s_waitcnt lgkmcnt(1)\n\t"
            "v_mfma_f32_16x16x32_bf16 v[152:155], v[8:11], v[192:195], v[152:155]\n\t"
            "s_bitcmp1_b32 s57, 1\n\t"
            "s_cselect_b32 s58, 1177344, 0\n\t"
            "s_bitcmp1_b32 s57, 0\n\t"
            "s_cselect_b32 s57, 2304, 0\n\t"
            "s_add_u32 s57, s57, s58\n\t"
            "v_mfma_f32_16x16x32_bf16 v[156:159], v[32:35], v[192:195], v[156:159]\n\t"
            "ds_read_b128 v[188:191], v242 offset:256\n\t"
            "s_mov_b64 exec, s[36:37]\n\t"
            "v_add_u32 v196, s57, v196\n\t"
            "s_mov_b64 exec, -1\n\t"
            "v_mfma_f32_16x16x32_bf16 v[160:163], v[56:59], v[192:195], v[160:163]\n\t"
            "v_add_u32 v236, s48, v244\n\t"
            "v_add_u32 v237, s48, v245\n\t"
            "v_mfma_f32_16x16x32_bf16 v[164:167], v[80:83], v[192:195], v[164:167]\n\t"
            "ds_read_b128 v[200:203], v236\n\t"
            "ds_read_b128 v[204:207], v237\n\t"
            "v_mfma_f32_16x16x32_bf16 v[176:179], v[104:107], v[192:195], v[176:179]\n\t"
            "ds_read_b128 v[212:215], v236 offset:6144\n\t"
            "ds_read_b128 v[216:219], v237 offset:6144\n\t"
            "v_mfma_f32_16x16x32_bf16 v[180:183], v[128:131], v[192:195], v[180:183]\n\t"
            "ds_read_b128 v[192:195], v243 offset:256\n\t"
            "v_add_u32 v248, s48, v246\n\t"
            "s_waitcnt lgkmcnt(6)\n\t"
            "v_mfma_f32_16x16x32_bf16 v[152:155], v[12:15], v[184:187], v[152:155]\n\t"
            "ds_read_b128 v[208:211], v248\n\t"
            "v_mov_b32 v249, s76\n\t"
            "v_mfma_f32_16x16x32_bf16 v[156:159], v[36:39], v[184:187], v[156:159]\n\t"
            "ds_read_b32 v249, v249 offset:4\n\t"
            "v_mfma_f32_16x16x32_bf16 v[160:163], v[60:63], v[184:187], v[160:163]\n\t"
            "v_mfma_f32_16x16x32_bf16 v[164:167], v[84:87], v[184:187], v[164:167]\n\t"
            "v_mfma_f32_16x16x32_bf16 v[176:179], v[108:111], v[184:187], v[176:179]\n\t"
            "v_mfma_f32_16x16x32_bf16 v[180:183], v[132:135], v[184:187], v[180:183]\n\t"
            "ds_read_b128 v[184:187], v242 offset:6144\n\t"
            "s_waitcnt lgkmcnt(8)\n\t"
            "v_mfma_f32_16x16x32_bf16 v[152:155], v[16:19], v[188:191], v[152:155]\n\t"
            "v_mfma_f32_16x16x32_bf16 v[156:159], v[40:43], v[188:191], v[156:159]\n\t"
            "v_mfma_f32_16x16x32_bf16 v[160:163], v[64:67], v[188:191], v[160:163]\n\t"
            "v_mfma_f32_16x16x32_bf16 v[164:167], v[88:91], v[188:191], v[164:167]\n\t"
            "s_waitcnt lgkmcnt(1)\n\t"
            "v_readfirstlane_b32 s44, v249\n\t"
            "v_mfma_f32_16x16x32_bf16 v[176:179], v[112:115], v[188:191], v[176:179]\n\t"
            "s_and_b32 s70, s44, 0x7ffffff\n\t"
            "s_add_u32 s70, s70, s77\n\t"
            "s_and_b32 s44, s44, 0x08000000\n\t"
            "s_or_b32 s44, s44, s39\n\t"
            "v_mfma_f32_16x16x32_bf16 v[180:183], v[136:139], v[188:191], v[180:183]\n\t"
            "ds_read_b128 v[188:191], v243 offset:6144\n\t"
            "ds_read_b32 v249, v252 offset:1028\n\t"
            "v_mfma_f32_16x16x32_bf16 v[152:155], v[20:23], v[192:195], v[152:155]\n\t"
            "v_mfma_f32_16x16x32_bf16 v[156:159], v[44:47], v[192:195], v[156:159]\n\t"
            "v_mfma_f32_16x16x32_bf16 v[160:163], v[68:71], v[192:195], v[160:163]\n\t"
            "v_mfma_f32_16x16x32_bf16 v[164:167], v[92:95], v[192:195], v[164:167]\n\t"
            "v_mfma_f32_16x16x32_bf16 v[176:179], v[116:119], v[192:195], v[176:179]\n\t"
            "v_mfma_f32_16x16x32_bf16 v[180:183], v[140:143], v[192:195], v[180:183]\n\t"
            "ds_read_b128 v[192:195], v242 offset:6272\n\t"
            "s_waitcnt lgkmcnt(3)\n\t"
            "v_mfma_f32_16x16x32_bf16 v[144:147], v[0:3], v[184:187], v[144:147]\n\t"
            "v_cvt_pk_bf16_f32 v152, v152, v153\n\t"
            "v_cvt_pk_bf16_f32 v153, v154, v155\n\t"
            "v_mfma_f32_16x16x32_bf16 v[148:151], v[24:27], v[184:187], v[148:151]\n\t"
            "v_cvt_pk_bf16_f32 v160, v160, v161\n\t"
            "v_cvt_pk_bf16_f32 v161, v162, v163\n\t"
            "v_mfma_f32_16x16x32_bf16 v[168:171], v[48:51], v[184:187], v[168:171]\n\t"
            "v_cvt_pk_bf16_f32 v154, v156, v157\n\t"
            "v_cvt_pk_bf16_f32 v155, v158, v159\n\t"
            "v_mfma_f32_16x16x32_bf16 v[172:175], v[72:75], v[184:187], v[172:175]\n\t"
            "v_cvt_pk_bf16_f32 v162, v164, v165\n\t"
            "v_cvt_pk_bf16_f32 v163, v166, v167\n\t"
            "s_waitcnt lgkmcnt(2)\n\t"
            "v_mfma_f32_16x16x32_bf16 v[144:147], v[4:7], v[188:191], v[144:147]\n\t"
            "v_lshlrev_b32 v156, 16, v220\n\t"
            "v_and_b32 v157, 0xffff0000, v220\n\t"
            "v_mfma_f32_16x16x32_bf16 v[148:151], v[28:31], v[188:191], v[148:151]\n\t"
            "v_lshlrev_b32 v158, 16, v221\n\t"
            "v_and_b32 v159, 0xffff0000, v221\n\t"
            "v_mfma_f32_16x16x32_bf16 v[168:171], v[52:55], v[188:191], v[168:171]\n\t"
            "v_lshlrev_b32 v164, 16, v222\n\t"
            "v_and_b32 v165, 0xffff0000, v222\n\t"
            "v_mfma_f32_16x16x32_bf16 v[156:159], v[96:99], v[184:187], v[156:159]\n\t"
            "v_lshlrev_b32 v166, 16, v223\n\t"
            "v_and_b32 v167, 0xffff0000, v223\n\t"
            "v_mfma_f32_16x16x32_bf16 v[172:175], v[76:79], v[188:191], v[172:175]\n\t"
            "ds_read_b128 v[220:223], v248 offset:6144\n\t"
            "s_cbranch_vccnz .Lst0_%=\n\t"
            "buffer_store_dwordx4 v[152:155], v196, s[40:43], s45 offen\n\t"
            ".Lsr0_%=:\n\t"
            "v_mfma_f32_16x16x32_bf16 v[164:167], v[120:123], v[184:187], v[164:167]\n\t"
            "ds_read_b128 v[184:187], v243 offset:6272\n\t"
            "s_cbranch_vccnz .Lst1_%=\n\t"
            "buffer_store_dwordx4 v[160:163], v196, s[40:43], s45 offen offset:64\n\t"
            ".Lsr1_%=:\n\t"
            "v_mfma_f32_16x16x32_bf16 v[156:159], v[100:103], v[188:191], v[156:159]\n\t"
            "v_cvt_pk_bf16_f32 v176, v176, v177\n\t"
            "v_cvt_pk_bf16_f32 v177, v178, v179\n\t"
            "v_mfma_f32_16x16x32_bf16 v[164:167], v[124:127], v[188:191], v[164:167]\n\t"
            "ds_read_b128 v[188:191], v242 offset:6400\n\t"
            "v_cvt_pk_bf16_f32 v178, v180, v181\n\t"
            "s_waitcnt lgkmcnt(3)\n\t"
            "v_mfma_f32_16x16x32_bf16 v[144:147], v[8:11], v[192:195], v[144:147]\n\t"
            "v_cvt_pk_bf16_f32 v179, v182, v183\n\t"
            "v_lshlrev_b32 v152, 16, v224\n\t"
            "v_mfma_f32_16x16x32_bf16 v[148:151], v[32:35], v[192:195], v[148:151]\n\t"
            "s_cbranch_vccnz .Lst2_%=\n\t"
            "buffer_store_dwordx4 v[176:179], v196, s[40:43], s45 offen offset:128\n\t"
            ".Lsr2_%=:\n\t"
            "v_mfma_f32_16x16x32_bf16 v[168:171], v[56:59], v[192:195], v[168:171]\n\t"
            "v_and_b32 v153, 0xffff0000, v224\n\t"
            "v_lshlrev_b32 v154, 16, v225\n\t"
            "v_mfma_f32_16x16x32_bf16 v[172:175], v[80:83], v[192:195], v[172:175]\n\t"
            "v_and_b32 v155, 0xffff0000, v225\n\t"
            "v_lshlrev_b32 v180, 16, v226\n\t"
            "v_mfma_f32_16x16x32_bf16 v[156:159], v[104:107], v[192:195], v[156:159]\n\t"
            "v_and_b32 v181, 0xffff0000, v226\n\t"
            "v_lshlrev_b32 v182, 16, v227\n\t"
            "v_mfma_f32_16x16x32_bf16 v[164:167], v[128:131], v[192:195], v[164:167]\n\t"
            "ds_read_b128 v[192:195], v243 offset:6400\n\t"
            "v_and_b32 v183, 0xffff0000, v227\n\t"
            "s_waitcnt lgkmcnt(2)\n\t"
            "v_mfma_f32_16x16x32_bf16 v[144:147], v[12:15], v[184:187], v[144:147]\n\t"
            "ds_read_b128 v[224:227], v236 offset:12288\n\t"
            "v_lshlrev_b32 v160, 16, v228\n\t"
            "v_mfma_f32_16x16x32_bf16 v[148:151], v[36:39], v[184:187], v[148:151]\n\t"
            "v_and_b32 v161, 0xffff0000, v228\n\t"
            "v_lshlrev_b32 v162, 16, v229\n\t"
            "s_waitcnt lgkmcnt(2)\n\t"
            "v_mfma_f32_16x16x32_bf16 v[144:147], v[16:19], v[188:191], v[144:147]\n\t"
            "v_and_b32 v163, 0xffff0000, v229\n\t"
            "v_lshlrev_b32 v176, 16, v230\n\t"
            "v_mfma_f32_16x16x32_bf16 v[168:171], v[60:63], v[184:187], v[168:171]\n\t"
            "v_and_b32 v177, 0xffff0000, v230\n\t"
            "v_lshlrev_b32 v178, 16, v231\n\t"
            "v_mfma_f32_16x16x32_bf16 v[148:151], v[40:43], v[188:191], v[148:151]\n\t"
            "v_and_b32 v179, 0xffff0000, v231\n\t"
            "v_and_b32 v249, s44, v249\n\t"
            "v_mfma_f32_16x16x32_bf16 v[172:175], v[84:87], v[184:187], v[172:175]\n\t"
            "ds_read_b128 v[228:231], v237 offset:12288\n\t"
            "v_mfma_f32_16x16x32_bf16 v[168:171], v[64:67], v[188:191], v[168:171]\n\t"
            "v_mfma_f32_16x16x32_bf16 v[156:159], v[108:111], v[184:187], v[156:159]\n\t"
            "s_waitcnt lgkmcnt(2)\n\t"
            "v_mfma_f32_16x16x32_bf16 v[144:147], v[20:23], v[192:195], v[144:147]\n\t"
            "v_mfma_f32_16x16x32_bf16 v[148:151], v[44:47], v[192:195], v[148:151]\n\t"
            "v_mfma_f32_16x16x32_bf16 v[164:167], v[132:135], v[184:187], v[164:167]\n\t"
            "ds_read_b128 v[184:187], v242 offset:12288\n\t"
            "v_mfma_f32_16x16x32_bf16 v[172:175], v[88:91], v[188:191], v[172:175]\n\t"
            "v_mfma_f32_16x16x32_bf16 v[168:171], v[68:71], v[192:195], v[168:171]\n\t"
            "v_mfma_f32_16x16x32_bf16 v[156:159], v[112:115], v[188:191], v[156:159]\n\t"
            "s_nop 1\n\t"
            "v_cvt_pk_bf16_f32 v144, v144, v145\n\t"
            "v_mfma_f32_16x16x32_bf16 v[164:167], v[136:139], v[188:191], v[164:167]\n\t"
            "ds_read_b128 v[188:191], v243 offset:12288\n\t"
            "v_cvt_pk_bf16_f32 v145, v146, v147\n\t"
            "v_mfma_f32_16x16x32_bf16 v[172:175], v[92:95], v[192:195], v[172:175]\n\t"
            "v_cvt_pk_bf16_f32 v146, v148, v149\n\t"
            "v_cvt_pk_bf16_f32 v147, v150, v151\n\t"
            "v_mfma_f32_16x16x32_bf16 v[156:159], v[116:119], v[192:195], v[156:159]\n\t"
            "s_cbranch_vccnz .Lst3_%=\n\t"
            "buffer_store_dwordx4 v[144:147], v197, s[40:43], s45 offen\n\t"
            ".Lsr3_%=:\n\t"
            "v_mfma_f32_16x16x32_bf16 v[164:167], v[140:143], v[192:195], v[164:167]\n\t"
            "ds_read_b128 v[192:195], v242 offset:12416\n\t"
            "v_cvt_pk_bf16_f32 v168, v168, v169\n\t"
            "s_waitcnt lgkmcnt(2)\n\t"
            "v_mfma_f32_16x16x32_bf16 v[152:155], v[0:3], v[184:187], v[152:155]\n\t"
            "v_cvt_pk_bf16_f32 v169, v170, v171\n\t"
            "v_cvt_pk_bf16_f32 v170, v172, v173\n\t"
            "v_mfma_f32_16x16x32_bf16 v[180:183], v[24:27], v[184:187], v[180:183]\n\t"
            "v_cvt_pk_bf16_f32 v171, v174, v175\n\t"
            "v_cvt_pk_bf16_f32 v156, v156, v157\n\t"
            "v_mfma_f32_16x16x32_bf16 v[160:163], v[48:51], v[184:187], v[160:163]\n\t"
            "ds_read_b128 v[172:175], v254 offset:64\n\t"
            "s_cbranch_vccnz .Lst4_%=\n\t"
            "buffer_store_dwordx4 v[168:171], v197, s[40:43], s45 offen offset:64\n\t"
            ".Lsr4_%=:\n\t"
            "v_mfma_f32_16x16x32_bf16 v[176:179], v[72:75], v[184:187], v[176:179]\n\t"
            "s_nop 0\n\t"
            "ds_read_b128 v[168:171], v254\n\t"
            "v_cvt_pk_bf16_f32 v157, v158, v159\n\t"
            "s_waitcnt lgkmcnt(3)\n\t"
            "v_mfma_f32_16x16x32_bf16 v[152:155], v[4:7], v[188:191], v[152:155]\n\t"
            "v_cvt_pk_bf16_f32 v158, v164, v165\n\t"
            "v_cvt_pk_bf16_f32 v159, v166, v167\n\t"
            "v_mfma_f32_16x16x32_bf16 v[180:183], v[28:31], v[188:191], v[180:183]\n\t"
            "s_cbranch_vccnz .Lst5_%=\n\t"
            "buffer_store_dwordx4 v[156:159], v197, s[40:43], s45 offen offset:128\n\t"
            ".Lsr5_%=:\n\t"
            "v_mfma_f32_16x16x32_bf16 v[160:163], v[52:55], v[188:191], v[160:163]\n\t"
            "s_waitcnt vmcnt(6)\n\t"
            "ds_read_b64 v[144:145], v253\n\t"
            "ds_read_b64 v[146:147], v253 offset:96\n\t"
            "v_mfma_f32_16x16x32_bf16 v[176:179], v[76:79], v[188:191], v[176:179]\n\t"
            "ds_read_b64 v[148:149], v253 offset:1744\n\t"
            "ds_read_b64 v[150:151], v253 offset:1840\n\t"
            "s_waitcnt lgkmcnt(6)\n\t"
            "v_mfma_f32_16x16x32_bf16 v[152:155], v[8:11], v[192:195], v[152:155]\n\t"
            "v_lshlrev_b32 v164, 16, v232\n\t"
            "v_and_b32 v165, 0xffff0000, v232\n\t"
            "v_mfma_f32_16x16x32_bf16 v[180:183], v[32:35], v[192:195], v[180:183]\n\t"
            "v_lshlrev_b32 v166, 16, v233\n\t"
            "v_and_b32 v167, 0xffff0000, v233\n\t"
            "v_mfma_f32_16x16x32_bf16 v[160:163], v[56:59], v[192:195], v[160:163]\n\t"
            "v_lshlrev_b32 v156, 16, v234\n\t"
            "v_and_b32 v157, 0xffff0000, v234\n\t"
            "v_mfma_f32_16x16x32_bf16 v[164:167], v[96:99], v[184:187], v[164:167]\n\t"
            "v_lshlrev_b32 v158, 16, v235\n\t"
            "v_and_b32 v159, 0xffff0000, v235\n\t"
            "v_mfma_f32_16x16x32_bf16 v[164:167], v[100:103], v[188:191], v[164:167]\n\t"
            "ds_read_b128 v[232:235], v248 offset:12288\n\t"
            "v_mfma_f32_16x16x32_bf16 v[156:159], v[120:123], v[184:187], v[156:159]\n\t"
            "ds_read_b128 v[184:187], v243 offset:12416\n\t"
            "v_mfma_f32_16x16x32_bf16 v[156:159], v[124:127], v[188:191], v[156:159]\n\t"
            "ds_read_b128 v[188:191], v242 offset:12544\n\t"
            "v_mfma_f32_16x16x32_bf16 v[176:179], v[80:83], v[192:195], v[176:179]\n\t"
            "v_mfma_f32_16x16x32_bf16 v[164:167], v[104:107], v[192:195], v[164:167]\n\t"
            "v_mfma_f32_16x16x32_bf16 v[156:159], v[128:131], v[192:195], v[156:159]\n\t"
            "ds_read_b128 v[192:195], v243 offset:12544\n\t"
            "s_waitcnt lgkmcnt(2)\n\t"
            "v_mfma_f32_16x16x32_bf16 v[152:155], v[12:15], v[184:187], v[152:155]\n\t"
            "v_mfma_f32_16x16x32_bf16 v[180:183], v[36:39], v[184:187], v[180:183]\n\t"
            "s_waitcnt lgkmcnt(1)\n\t"
            "v_mfma_f32_16x16x32_bf16 v[152:155], v[16:19], v[188:191], v[152:155]\n\t"
            "v_mfma_f32_16x16x32_bf16 v[160:163], v[60:63], v[184:187], v[160:163]\n\t"
            "s_waitcnt lgkmcnt(0)\n\t"
            "s_barrier\n\t"
            "v_mfma_f32_16x16x32_bf16 v[180:183], v[40:43], v[188:191], v[180:183]\n\t"
            "v_mfma_f32_16x16x32_bf16 v[176:179], v[84:87], v[184:187], v[176:179]\n\t"
            "v_mfma_f32_16x16x32_bf16 v[152:155], v[20:23], v[192:195], v[152:155]\n\t"
            "v_mfma_f32_16x16x32_bf16 v[160:163], v[64:67], v[188:191], v[160:163]\n\t"
            "v_mfma_f32_16x16x32_bf16 v[180:183], v[44:47], v[192:195], v[180:183]\n\t"
            "v_mfma_f32_16x16x32_bf16 v[164:167], v[108:111], v[184:187], v[164:167]\n\t"
            "v_mfma_f32_16x16x32_bf16 v[176:179], v[88:91], v[188:191], v[176:179]\n\t"
            "v_mfma_f32_16x16x32_bf16 v[156:159], v[132:135], v[184:187], v[156:159]\n\t"
            "v_mfma_f32_16x16x32_bf16 v[160:163], v[68:71], v[192:195], v[160:163]\n\t"
            "s_nop 1\n\t"
            "v_cvt_pk_bf16_f32 v152, v152, v153\n\t"
            "v_mfma_f32_16x16x32_bf16 v[164:167], v[112:115], v[188:191], v[164:167]\n\t"
            "v_cvt_pk_bf16_f32 v153, v154, v155\n\t"
            "v_mfma_f32_16x16x32_bf16 v[176:179], v[92:95], v[192:195], v[176:179]\n\t"
            "v_cvt_pk_bf16_f32 v154, v180, v181\n\t"
            "v_cvt_pk_bf16_f32 v155, v182, v183\n\t"
            "v_mfma_f32_16x16x32_bf16 v[156:159], v[136:139], v[188:191], v[156:159]\n\t"
            "s_cbranch_vccnz .Lst6_%=\n\t"
            "buffer_store_dwordx4 v[152:155], v198, s[40:43], s45 offen\n\t"
            ".Lsr6_%=:\n\t"
            "v_mfma_f32_16x16x32_bf16 v[164:167], v[116:119], v[192:195], v[164:167]\n\t"
            "s_nop 0\n\t"
            "ds_read_b64 v[152:153], v253 offset:8\n\t"
            "ds_read_b64 v[154:155], v253 offset:104\n\t"
            "v_mfma_f32_16x16x32_bf16 v[156:159], v[140:143], v[192:195], v[156:159]\n\t"
            "v_cvt_pk_bf16_f32 v160, v160, v161\n\t"
            "v_cvt_pk_bf16_f32 v161, v162, v163\n\t"
            "v_cvt_pk_bf16_f32 v162, v176, v177\n\t"
            "v_cvt_pk_bf16_f32 v163, v178, v179\n\t"
            "v_cvt_pk_bf16_f32 v164, v164, v165\n\t"
            "v_cvt_pk_bf16_f32 v165, v166, v167\n\t"
            "s_cbranch_vccnz .Lst7_%=\n\t"
            "buffer_store_dwordx4 v[160:163], v198, s[40:43], s45 offen offset:64\n\t"
            ".Lsr7_%=:\n\t"
            "s_nop 0\n\t"
            "v_cvt_pk_bf16_f32 v166, v156, v157\n\t"
            "v_cvt_pk_bf16_f32 v167, v158, v159\n\t"
            "ds_read_b64 v[156:157], v253 offset:1752\n\t"
            "ds_read_b64 v[158:159], v253 offset:1848\n\t"
            "ds_read_b64 v[160:161], v253 offset:16\n\t"
            "ds_read_b64 v[162:163], v253 offset:112\n\t"
            "s_cbranch_vccnz .Lst8_%=\n\t"
            "buffer_store_dwordx4 v[164:167], v198, s[40:43], s45 offen offset:128\n\t"
            ".Lsr8_%=:\n\t"
            "s_mov_b32 m0, s32\n\t"
            "s_add_u32 s49, s70, s25\n\t"
            "buffer_load_dword v249, s[72:75], s70 offen lds\n\t"
            "buffer_load_dword v249, s[72:75], s49 offen lds\n\t"
            "ds_read_b64 v[164:165], v253 offset:1760\n\t"
            "ds_read_b64 v[166:167], v253 offset:1856\n\t"
            "s_add_u32 s45, s46, s27\n\t"
            "s_mov_b32 s46, s47\n\t"
            "s_mov_b32 s47, s68\n\t"
            "s_xor_b32 s48, s48, s71\n\t"
            "s_xor_b32 s67, s67, s71\n\t"
            "s_add_u32 s76, s76, 4\n\t"
            "s_lshl_b32 s56, s56, 2\n\t"
            "s_lshr_b32 s49, s26, 30\n\t"
            "s_or_b32 s56, s56, s49\n\t"
            "s_cmp_ge_u32 s76, s24\n\t"
            "s_cselect_b64 vcc, -1, 0\n\t"
            "s_cmp_lt_u32 s46, 0x7000000\n\t"
            "s_cbranch_scc1 .Lwb_loop%=\n\t"
            "s_branch .Lsx%=\n\t"
            ".Lst0_%=:\n\t"
            "buffer_store_dwordx4 v[152:155], v196, s[40:43], s45 offen sc1\n\t"
            "s_branch .Lsr0_%=\n\t"
            ".Lst1_%=:\n\t"
            "buffer_store_dwordx4 v[160:163], v196, s[40:43], s45 offen offset:64 sc1\n\t"
            "s_branch .Lsr1_%=\n\t"
            ".Lst2_%=:\n\t"
            "buffer_store_dwordx4 v[176:179], v196, s[40:43], s45 offen offset:128 sc1\n\t"
            "s_branch .Lsr2_%=\n\t"
            ".Lst3_%=:\n\t"
            "buffer_store_dwordx4 v[144:147], v197, s[40:43], s45 offen sc1\n\t"
            "s_branch .Lsr3_%=\n\t"
            ".Lst4_%=:\n\t"
            "buffer_store_dwordx4 v[168:171], v197, s[40:43], s45 offen offset:64 sc1\n\t"
            "s_branch .Lsr4_%=\n\t"
            ".Lst5_%=:\n\t"
            "buffer_store_dwordx4 v[156:159], v197, s[40:43], s45 offen offset:128 sc1\n\t"
            "s_branch .Lsr5_%=\n\t"
            ".Lst6_%=:\n\t"
            "buffer_store_dwordx4 v[152:155], v198, s[40:43], s45 offen sc1\n\t"
            "s_branch .Lsr6_%=\n\t"
            ".Lst7_%=:\n\t"
            "buffer_store_dwordx4 v[160:163], v198, s[40:43], s45 offen offset:64 sc1\n\t"
            "s_branch .Lsr7_%=\n\t"
            ".Lst8_%=:\n\t"
            "buffer_store_dwordx4 v[164:167], v198, s[40:43], s45 offen offset:128 sc1\n\t"
            "s_branch .Lsr8_%=\n\t"
            ".Lsx%=:\n\t"
            "s_waitcnt lgkmcnt(0)\n\t"
            "s_nop 7\n\t"
            "s_setprio 3\n\t"
            "s_nop 7\n\t"
            "ds_read_b128 v[196:199], v252\n\t"
            "s_bfe_u32 s57, s56, 0x20004\n\t"
            "s_lshl_b32 s58, s57, 3\n\t"
            "s_lshr_b32 s58, s55, s58\n\t"
            "s_lshl_b64 s[36:37], s[34:35], s58\n\t"
            "s_bitcmp1_b32 s57, 1\n\t"
            "s_cselect_b32 s58, 1177344, 0\n\t"
            "s_bitcmp1_b32 s57, 0\n\t"
            "s_cselect_b32 s57, 2304, 0\n\t"
            "s_add_u32 s57, s57, s58\n\t"
            "s_waitcnt lgkmcnt(0)\n\t"
            "s_mov_b64 exec, s[36:37]\n\t"
            "v_add_u32 v196, s57, v196\n\t"
            "s_mov_b64 exec, -1\n\t"
            "s_nop 7\n\t"
            "v_mov_b32 v144, s23\n\t"
            ".Ldp%=:\n\t"
            "ds_read_b128 v[148:151], v144 offset:96\n\t"
            "s_waitcnt lgkmcnt(0)\n\t"
            "v_or_b32 v148, v148, v149\n\t"
            "v_or3_b32 v148, v148, v150, v151\n\t"
            "v_cmp_gt_i32 vcc, 0, v148\n\t"
            "s_nop 4\n\t"
            "s_cbranch_vccnz .Ldp%=\n\t"
            "s_nop 7\n\t"
            "ds_read_b128 v[184:187], v242\n\t"
            "ds_read_b128 v[188:191], v243\n\t"
            "ds_read_b128 v[192:195], v242 offset:128\n\t"
            "v_lshlrev_b32 v152, 16, v200\n\t"
            "v_and_b32 v153, 0xffff0000, v200\n\t"
            "v_lshlrev_b32 v154, 16, v201\n\t"
            "v_and_b32 v155, 0xffff0000, v201\n\t"
            "v_lshlrev_b32 v156, 16, v202\n\t"
            "v_and_b32 v157, 0xffff0000, v202\n\t"
            "v_lshlrev_b32 v158, 16, v203\n\t"
            "v_and_b32 v159, 0xffff0000, v203\n\t"
            "v_lshlrev_b32 v160, 16, v204\n\t"
            "v_and_b32 v161, 0xffff0000, v204\n\t"
            "v_lshlrev_b32 v162, 16, v205\n\t"
            "v_and_b32 v163, 0xffff0000, v205\n\t"
            "v_lshlrev_b32 v164, 16, v206\n\t"
            "v_and_b32 v165, 0xffff0000, v206\n\t"
            "v_lshlrev_b32 v166, 16, v207\n\t"
            "v_and_b32 v167, 0xffff0000, v207\n\t"
            "v_lshlrev_b32 v176, 16, v208\n\t"
            "v_and_b32 v177, 0xffff0000, v208\n\t"
            "v_lshlrev_b32 v178, 16, v209\n\t"
            "v_and_b32 v179, 0xffff0000, v209\n\t"
            "v_lshlrev_b32 v180, 16, v210\n\t"
            "v_and_b32 v181, 0xffff0000, v210\n\t"
            "v_lshlrev_b32 v182, 16, v211\n\t"
            "v_and_b32 v183, 0xffff0000, v211\n\t"
            "v_lshlrev_b32 v144, 16, v212\n\t"
            "v_and_b32 v145, 0xffff0000, v212\n\t"
            "v_lshlrev_b32 v146, 16, v213\n\t"
            "v_and_b32 v147, 0xffff0000, v213\n\t"
            "s_waitcnt lgkmcnt(2)\n\t"
            "v_mfma_f32_16x16x32_bf16 v[152:155], v[0:3], v[184:187], v[152:155]\n\t"
            "v_lshlrev_b32 v148, 16, v214\n\t"
            "v_and_b32 v149, 0xffff0000, v214\n\t"
            "v_mfma_f32_16x16x32_bf16 v[156:159], v[24:27], v[184:187], v[156:159]\n\t"
            "v_lshlrev_b32 v150, 16, v215\n\t"
            "v_and_b32 v151, 0xffff0000, v215\n\t"
            "v_mfma_f32_16x16x32_bf16 v[160:163], v[48:51], v[184:187], v[160:163]\n\t"
            "v_lshlrev_b32 v168, 16, v216\n\t"
            "v_and_b32 v169, 0xffff0000, v216\n\t"
            "v_mfma_f32_16x16x32_bf16 v[164:167], v[72:75], v[184:187], v[164:167]\n\t"
            "v_lshlrev_b32 v170, 16, v217\n\t"
            "v_and_b32 v171, 0xffff0000, v217\n\t"
            "v_mfma_f32_16x16x32_bf16 v[176:179], v[96:99], v[184:187], v[176:179]\n\t"
            "v_lshlrev_b32 v172, 16, v218\n\t"
            "v_and_b32 v173, 0xffff0000, v218\n\t"
            "v_mfma_f32_16x16x32_bf16 v[180:183], v[120:123], v[184:187], v[180:183]\n\t"
            "ds_read_b128 v[184:187], v243 offset:128\n\t"
            "v_lshlrev_b32 v174, 16, v219\n\t"
            "s_waitcnt lgkmcnt(2)\n\t"
            "v_mfma_f32_16x16x32_bf16 v[152:155], v[4:7], v[188:191], v[152:155]\n\t"
            "v_and_b32 v175, 0xffff0000, v219\n\t"
            "v_mfma_f32_16x16x32_bf16 v[156:159], v[28:31], v[188:191], v[156:159]\n\t"
            "v_mfma_f32_16x16x32_bf16 v[160:163], v[52:55], v[188:191], v[160:163]\n\t"
            "v_mfma_f32_16x16x32_bf16 v[164:167], v[76:79], v[188:191], v[164:167]\n\t"
            "v_mfma_f32_16x16x32_bf16 v[176:179], v[100:103], v[188:191], v[176:179]\n\t"
            "v_mfma_f32_16x16x32_bf16 v[180:183], v[124:127], v[188:191], v[180:183]\n\t"
            "ds_read_b128 v[188:191], v242 offset:256\n\t"
            "s_waitcnt lgkmcnt(2)\n\t"
            "v_mfma_f32_16x16x32_bf16 v[152:155], v[8:11], v[192:195], v[152:155]\n\t"
            "v_mfma_f32_16x16x32_bf16 v[156:159], v[32:35], v[192:195], v[156:159]\n\t"
            "v_mfma_f32_16x16x32_bf16 v[160:163], v[56:59], v[192:195], v[160:163]\n\t"
            "v_mfma_f32_16x16x32_bf16 v[164:167], v[80:83], v[192:195], v[164:167]\n\t"
            "v_mfma_f32_16x16x32_bf16 v[176:179], v[104:107], v[192:195], v[176:179]\n\t"
            "v_mfma_f32_16x16x32_bf16 v[180:183], v[128:131], v[192:195], v[180:183]\n\t"
            "ds_read_b128 v[192:195], v243 offset:256\n\t"
            "s_waitcnt lgkmcnt(2)\n\t"
            "v_mfma_f32_16x16x32_bf16 v[152:155], v[12:15], v[184:187], v[152:155]\n\t"
            "v_mfma_f32_16x16x32_bf16 v[156:159], v[36:39], v[184:187], v[156:159]\n\t"
            "v_mfma_f32_16x16x32_bf16 v[160:163], v[60:63], v[184:187], v[160:163]\n\t"
            "v_mfma_f32_16x16x32_bf16 v[164:167], v[84:87], v[184:187], v[164:167]\n\t"
            "v_mfma_f32_16x16x32_bf16 v[176:179], v[108:111], v[184:187], v[176:179]\n\t"
            "v_mfma_f32_16x16x32_bf16 v[180:183], v[132:135], v[184:187], v[180:183]\n\t"
            "ds_read_b128 v[184:187], v242 offset:6144\n\t"
            "s_waitcnt lgkmcnt(2)\n\t"
            "v_mfma_f32_16x16x32_bf16 v[152:155], v[16:19], v[188:191], v[152:155]\n\t"
            "v_mfma_f32_16x16x32_bf16 v[156:159], v[40:43], v[188:191], v[156:159]\n\t"
            "v_mfma_f32_16x16x32_bf16 v[160:163], v[64:67], v[188:191], v[160:163]\n\t"
            "v_mfma_f32_16x16x32_bf16 v[164:167], v[88:91], v[188:191], v[164:167]\n\t"
            "v_mfma_f32_16x16x32_bf16 v[176:179], v[112:115], v[188:191], v[176:179]\n\t"
            "v_mfma_f32_16x16x32_bf16 v[180:183], v[136:139], v[188:191], v[180:183]\n\t"
            "ds_read_b128 v[188:191], v243 offset:6144\n\t"
            "s_waitcnt lgkmcnt(2)\n\t"
            "v_mfma_f32_16x16x32_bf16 v[152:155], v[20:23], v[192:195], v[152:155]\n\t"
            "v_mfma_f32_16x16x32_bf16 v[156:159], v[44:47], v[192:195], v[156:159]\n\t"
            "v_mfma_f32_16x16x32_bf16 v[160:163], v[68:71], v[192:195], v[160:163]\n\t"
            "v_mfma_f32_16x16x32_bf16 v[164:167], v[92:95], v[192:195], v[164:167]\n\t"
            "v_mfma_f32_16x16x32_bf16 v[176:179], v[116:119], v[192:195], v[176:179]\n\t"
            "v_mfma_f32_16x16x32_bf16 v[180:183], v[140:143], v[192:195], v[180:183]\n\t"
            "ds_read_b128 v[192:195], v242 offset:6272\n\t"
            "s_waitcnt lgkmcnt(2)\n\t"
            "v_mfma_f32_16x16x32_bf16 v[144:147], v[0:3], v[184:187], v[144:147]\n\t"
            "v_cvt_pk_bf16_f32 v152, v152, v153\n\t"
            "v_cvt_pk_bf16_f32 v153, v154, v155\n\t"
            "v_mfma_f32_16x16x32_bf16 v[148:151], v[24:27], v[184:187], v[148:151]\n\t"
            "v_cvt_pk_bf16_f32 v160, v160, v161\n\t"
            "v_cvt_pk_bf16_f32 v161, v162, v163\n\t"
            "v_mfma_f32_16x16x32_bf16 v[168:171], v[48:51], v[184:187], v[168:171]\n\t"
            "v_cvt_pk_bf16_f32 v154, v156, v157\n\t"
            "v_cvt_pk_bf16_f32 v155, v158, v159\n\t"
            "v_mfma_f32_16x16x32_bf16 v[172:175], v[72:75], v[184:187], v[172:175]\n\t"
            "v_cvt_pk_bf16_f32 v162, v164, v165\n\t"
            "v_cvt_pk_bf16_f32 v163, v166, v167\n\t"
            "s_waitcnt lgkmcnt(1)\n\t"
            "v_mfma_f32_16x16x32_bf16 v[144:147], v[4:7], v[188:191], v[144:147]\n\t"
            "v_lshlrev_b32 v156, 16, v220\n\t"
            "v_and_b32 v157, 0xffff0000, v220\n\t"
            "v_mfma_f32_16x16x32_bf16 v[148:151], v[28:31], v[188:191], v[148:151]\n\t"
            "v_lshlrev_b32 v158, 16, v221\n\t"
            "v_and_b32 v159, 0xffff0000, v221\n\t"
            "v_mfma_f32_16x16x32_bf16 v[168:171], v[52:55], v[188:191], v[168:171]\n\t"
            "v_lshlrev_b32 v164, 16, v222\n\t"
            "v_and_b32 v165, 0xffff0000, v222\n\t"
            "v_mfma_f32_16x16x32_bf16 v[156:159], v[96:99], v[184:187], v[156:159]\n\t"
            "v_lshlrev_b32 v166, 16, v223\n\t"
            "v_and_b32 v167, 0xffff0000, v223\n\t"
            "v_mfma_f32_16x16x32_bf16 v[172:175], v[76:79], v[188:191], v[172:175]\n\t"
            "buffer_store_dwordx4 v[152:155], v196, s[40:43], s45 offen sc1\n\t"
            "v_mfma_f32_16x16x32_bf16 v[164:167], v[120:123], v[184:187], v[164:167]\n\t"
            "ds_read_b128 v[184:187], v243 offset:6272\n\t"
            "buffer_store_dwordx4 v[160:163], v196, s[40:43], s45 offen offset:64 sc1\n\t"
            "v_mfma_f32_16x16x32_bf16 v[156:159], v[100:103], v[188:191], v[156:159]\n\t"
            "v_cvt_pk_bf16_f32 v176, v176, v177\n\t"
            "v_cvt_pk_bf16_f32 v177, v178, v179\n\t"
            "v_mfma_f32_16x16x32_bf16 v[164:167], v[124:127], v[188:191], v[164:167]\n\t"
            "ds_read_b128 v[188:191], v242 offset:6400\n\t"
            "v_cvt_pk_bf16_f32 v178, v180, v181\n\t"
            "s_waitcnt lgkmcnt(2)\n\t"
            "v_mfma_f32_16x16x32_bf16 v[144:147], v[8:11], v[192:195], v[144:147]\n\t"
            "v_cvt_pk_bf16_f32 v179, v182, v183\n\t"
            "v_lshlrev_b32 v152, 16, v224\n\t"
            "v_mfma_f32_16x16x32_bf16 v[148:151], v[32:35], v[192:195], v[148:151]\n\t"
            "buffer_store_dwordx4 v[176:179], v196, s[40:43], s45 offen offset:128 sc1\n\t"
            "v_mfma_f32_16x16x32_bf16 v[168:171], v[56:59], v[192:195], v[168:171]\n\t"
            "v_and_b32 v153, 0xffff0000, v224\n\t"
            "v_lshlrev_b32 v154, 16, v225\n\t"
            "v_mfma_f32_16x16x32_bf16 v[172:175], v[80:83], v[192:195], v[172:175]\n\t"
            "v_and_b32 v155, 0xffff0000, v225\n\t"
            "v_lshlrev_b32 v180, 16, v226\n\t"
            "v_mfma_f32_16x16x32_bf16 v[156:159], v[104:107], v[192:195], v[156:159]\n\t"
            "v_and_b32 v181, 0xffff0000, v226\n\t"
            "v_lshlrev_b32 v182, 16, v227\n\t"
            "v_mfma_f32_16x16x32_bf16 v[164:167], v[128:131], v[192:195], v[164:167]\n\t"
            "ds_read_b128 v[192:195], v243 offset:6400\n\t"
            "v_and_b32 v183, 0xffff0000, v227\n\t"
            "s_waitcnt lgkmcnt(2)\n\t"
            "v_mfma_f32_16x16x32_bf16 v[144:147], v[12:15], v[184:187], v[144:147]\n\t"
            "v_lshlrev_b32 v160, 16, v228\n\t"
            "v_and_b32 v161, 0xffff0000, v228\n\t"
            "v_mfma_f32_16x16x32_bf16 v[148:151], v[36:39], v[184:187], v[148:151]\n\t"
            "v_lshlrev_b32 v162, 16, v229\n\t"
            "v_and_b32 v163, 0xffff0000, v229\n\t"
            "s_waitcnt lgkmcnt(1)\n\t"
            "v_mfma_f32_16x16x32_bf16 v[144:147], v[16:19], v[188:191], v[144:147]\n\t"
            "v_lshlrev_b32 v176, 16, v230\n\t"
            "v_and_b32 v177, 0xffff0000, v230\n\t"
            "v_mfma_f32_16x16x32_bf16 v[168:171], v[60:63], v[184:187], v[168:171]\n\t"
            "v_lshlrev_b32 v178, 16, v231\n\t"
            "v_and_b32 v179, 0xffff0000, v231\n\t"
            "v_mfma_f32_16x16x32_bf16 v[148:151], v[40:43], v[188:191], v[148:151]\n\t"
            "v_mfma_f32_16x16x32_bf16 v[172:175], v[84:87], v[184:187], v[172:175]\n\t"
            "v_mfma_f32_16x16x32_bf16 v[168:171], v[64:67], v[188:191], v[168:171]\n\t"
            "v_mfma_f32_16x16x32_bf16 v[156:159], v[108:111], v[184:187], v[156:159]\n\t"
            "s_waitcnt lgkmcnt(0)\n\t"
            "v_mfma_f32_16x16x32_bf16 v[144:147], v[20:23], v[192:195], v[144:147]\n\t"
            "v_mfma_f32_16x16x32_bf16 v[148:151], v[44:47], v[192:195], v[148:151]\n\t"
            "v_mfma_f32_16x16x32_bf16 v[164:167], v[132:135], v[184:187], v[164:167]\n\t"
            "ds_read_b128 v[184:187], v242 offset:12288\n\t"
            "v_mfma_f32_16x16x32_bf16 v[172:175], v[88:91], v[188:191], v[172:175]\n\t"
            "v_mfma_f32_16x16x32_bf16 v[168:171], v[68:71], v[192:195], v[168:171]\n\t"
            "v_mfma_f32_16x16x32_bf16 v[156:159], v[112:115], v[188:191], v[156:159]\n\t"
            "s_nop 1\n\t"
            "v_cvt_pk_bf16_f32 v144, v144, v145\n\t"
            "v_mfma_f32_16x16x32_bf16 v[164:167], v[136:139], v[188:191], v[164:167]\n\t"
            "ds_read_b128 v[188:191], v243 offset:12288\n\t"
            "v_cvt_pk_bf16_f32 v145, v146, v147\n\t"
            "v_mfma_f32_16x16x32_bf16 v[172:175], v[92:95], v[192:195], v[172:175]\n\t"
            "v_cvt_pk_bf16_f32 v146, v148, v149\n\t"
            "v_cvt_pk_bf16_f32 v147, v150, v151\n\t"
            "v_mfma_f32_16x16x32_bf16 v[156:159], v[116:119], v[192:195], v[156:159]\n\t"
            "buffer_store_dwordx4 v[144:147], v197, s[40:43], s45 offen sc1\n\t"
            "v_mfma_f32_16x16x32_bf16 v[164:167], v[140:143], v[192:195], v[164:167]\n\t"
            "ds_read_b128 v[192:195], v242 offset:12416\n\t"
            "v_cvt_pk_bf16_f32 v168, v168, v169\n\t"
            "s_waitcnt lgkmcnt(2)\n\t"
            "v_mfma_f32_16x16x32_bf16 v[152:155], v[0:3], v[184:187], v[152:155]\n\t"
            "v_cvt_pk_bf16_f32 v169, v170, v171\n\t"
            "v_cvt_pk_bf16_f32 v170, v172, v173\n\t"
            "v_mfma_f32_16x16x32_bf16 v[180:183], v[24:27], v[184:187], v[180:183]\n\t"
            "v_cvt_pk_bf16_f32 v171, v174, v175\n\t"
            "v_cvt_pk_bf16_f32 v156, v156, v157\n\t"
            "v_mfma_f32_16x16x32_bf16 v[160:163], v[48:51], v[184:187], v[160:163]\n\t"
            "buffer_store_dwordx4 v[168:171], v197, s[40:43], s45 offen offset:64 sc1\n\t"
            "v_mfma_f32_16x16x32_bf16 v[176:179], v[72:75], v[184:187], v[176:179]\n\t"
            "v_cvt_pk_bf16_f32 v157, v158, v159\n\t"
            "v_cvt_pk_bf16_f32 v158, v164, v165\n\t"
            "s_waitcnt lgkmcnt(1)\n\t"
            "v_mfma_f32_16x16x32_bf16 v[152:155], v[4:7], v[188:191], v[152:155]\n\t"
            "v_cvt_pk_bf16_f32 v159, v166, v167\n\t"
            "v_lshlrev_b32 v164, 16, v232\n\t"
            "v_mfma_f32_16x16x32_bf16 v[180:183], v[28:31], v[188:191], v[180:183]\n\t"
            "buffer_store_dwordx4 v[156:159], v197, s[40:43], s45 offen offset:128 sc1\n\t"
            "v_mfma_f32_16x16x32_bf16 v[160:163], v[52:55], v[188:191], v[160:163]\n\t"
            "v_and_b32 v165, 0xffff0000, v232\n\t"
            "v_lshlrev_b32 v166, 16, v233\n\t"
            "v_mfma_f32_16x16x32_bf16 v[176:179], v[76:79], v[188:191], v[176:179]\n\t"
            "v_and_b32 v167, 0xffff0000, v233\n\t"
            "v_lshlrev_b32 v156, 16, v234\n\t"
            "s_waitcnt lgkmcnt(0)\n\t"
            "v_mfma_f32_16x16x32_bf16 v[152:155], v[8:11], v[192:195], v[152:155]\n\t"
            "v_and_b32 v157, 0xffff0000, v234\n\t"
            "v_lshlrev_b32 v158, 16, v235\n\t"
            "v_mfma_f32_16x16x32_bf16 v[164:167], v[96:99], v[184:187], v[164:167]\n\t"
            "v_and_b32 v159, 0xffff0000, v235\n\t"
            "v_mfma_f32_16x16x32_bf16 v[164:167], v[100:103], v[188:191], v[164:167]\n\t"
            "s_nop 0\n\t"
            "v_mfma_f32_16x16x32_bf16 v[156:159], v[120:123], v[184:187], v[156:159]\n\t"
            "ds_read_b128 v[184:187], v243 offset:12416\n\t"
            "v_mfma_f32_16x16x32_bf16 v[156:159], v[124:127], v[188:191], v[156:159]\n\t"
            "ds_read_b128 v[188:191], v242 offset:12544\n\t"
            "v_mfma_f32_16x16x32_bf16 v[180:183], v[32:35], v[192:195], v[180:183]\n\t"
            "v_mfma_f32_16x16x32_bf16 v[160:163], v[56:59], v[192:195], v[160:163]\n\t"
            "v_mfma_f32_16x16x32_bf16 v[176:179], v[80:83], v[192:195], v[176:179]\n\t"
            "v_mfma_f32_16x16x32_bf16 v[164:167], v[104:107], v[192:195], v[164:167]\n\t"
            "v_mfma_f32_16x16x32_bf16 v[156:159], v[128:131], v[192:195], v[156:159]\n\t"
            "ds_read_b128 v[192:195], v243 offset:12544\n\t"
            "s_waitcnt lgkmcnt(2)\n\t"
            "v_mfma_f32_16x16x32_bf16 v[152:155], v[12:15], v[184:187], v[152:155]\n\t"
            "v_mfma_f32_16x16x32_bf16 v[180:183], v[36:39], v[184:187], v[180:183]\n\t"
            "s_waitcnt lgkmcnt(1)\n\t"
            "v_mfma_f32_16x16x32_bf16 v[152:155], v[16:19], v[188:191], v[152:155]\n\t"
            "v_mfma_f32_16x16x32_bf16 v[160:163], v[60:63], v[184:187], v[160:163]\n\t"
            "v_mfma_f32_16x16x32_bf16 v[180:183], v[40:43], v[188:191], v[180:183]\n\t"
            "v_mfma_f32_16x16x32_bf16 v[176:179], v[84:87], v[184:187], v[176:179]\n\t"
            "s_waitcnt lgkmcnt(0)\n\t"
            "v_mfma_f32_16x16x32_bf16 v[152:155], v[20:23], v[192:195], v[152:155]\n\t"
            "v_mfma_f32_16x16x32_bf16 v[160:163], v[64:67], v[188:191], v[160:163]\n\t"
            "v_mfma_f32_16x16x32_bf16 v[180:183], v[44:47], v[192:195], v[180:183]\n\t"
            "v_mfma_f32_16x16x32_bf16 v[164:167], v[108:111], v[184:187], v[164:167]\n\t"
            "v_mfma_f32_16x16x32_bf16 v[176:179], v[88:91], v[188:191], v[176:179]\n\t"
            "v_mfma_f32_16x16x32_bf16 v[156:159], v[132:135], v[184:187], v[156:159]\n\t"
            "v_mfma_f32_16x16x32_bf16 v[160:163], v[68:71], v[192:195], v[160:163]\n\t"
            "s_nop 1\n\t"
            "v_cvt_pk_bf16_f32 v152, v152, v153\n\t"
            "v_mfma_f32_16x16x32_bf16 v[164:167], v[112:115], v[188:191], v[164:167]\n\t"
            "v_cvt_pk_bf16_f32 v153, v154, v155\n\t"
            "v_mfma_f32_16x16x32_bf16 v[176:179], v[92:95], v[192:195], v[176:179]\n\t"
            "v_cvt_pk_bf16_f32 v154, v180, v181\n\t"
            "v_cvt_pk_bf16_f32 v155, v182, v183\n\t"
            "v_mfma_f32_16x16x32_bf16 v[156:159], v[136:139], v[188:191], v[156:159]\n\t"
            "buffer_store_dwordx4 v[152:155], v198, s[40:43], s45 offen sc1\n\t"
            "v_mfma_f32_16x16x32_bf16 v[164:167], v[116:119], v[192:195], v[164:167]\n\t"
            "v_cvt_pk_bf16_f32 v160, v160, v161\n\t"
            "v_cvt_pk_bf16_f32 v161, v162, v163\n\t"
            "v_mfma_f32_16x16x32_bf16 v[156:159], v[140:143], v[192:195], v[156:159]\n\t"
            "v_cvt_pk_bf16_f32 v162, v176, v177\n\t"
            "v_cvt_pk_bf16_f32 v163, v178, v179\n\t"
            "buffer_store_dwordx4 v[160:163], v198, s[40:43], s45 offen offset:64 sc1\n\t"
            "s_nop 1\n\t"
            "v_cvt_pk_bf16_f32 v164, v164, v165\n\t"
            "v_cvt_pk_bf16_f32 v165, v166, v167\n\t"
            "s_nop 0\n\t"
            "v_cvt_pk_bf16_f32 v166, v156, v157\n\t"
            "v_cvt_pk_bf16_f32 v167, v158, v159\n\t"
            "buffer_store_dwordx4 v[164:167], v198, s[40:43], s45 offen offset:128 sc1\n\t"
            "s_nop 7\n\t"
            ".Lre%=:\n\t"
            : "+{v[128:131]}"(f0), "+{v[132:135]}"(f1), "+{v[136:139]}"(f2), "+{v242}"(r242), "+{v243}"(r243), "+{v[244:247]}"(r244),
              "+{s45}"(s45), "+{s46}"(s46), "+{s47}"(s47), "+{s48}"(s48), "+{s67}"(s67), "+{s76}"(s76), "+{s56}"(s56), "+{s44}"(s44), "+{s70}"(s70), "+{s21}"(s21)
            : B_VIN, B_SIN
            : "v0", "v1", "v2", "v3", "v4", "v5", "v6", "v7", "v8", "v9", "v10", "v11", "v12", "v13", "v14", "v15", "v16", "v17", "v18", "v19", "v20", "v21", "v22", "v23", "v24", "v25", "v26", "v27", "v28", "v29", "v30", "v31", "v32", "v33", "v34", "v35", "v36", "v37", "v38", "v39", "v40", "v41", "v42", "v43", "v44", "v45", "v46", "v47", "v48", "v49", "v50", "v51", "v52", "v53", "v54", "v55", "v56", "v57", "v58", "v59", "v60", "v61", "v62", "v63", "v64", "v65", "v66", "v67", "v68", "v69", "v70", "v71", "v72", "v73", "v74", "v75", "v76", "v77", "v78", "v79", "v80", "v81", "v82", "v83", "v84", "v85", "v86", "v87", "v88", "v89", "v90", "v91", "v92", "v93", "v94", "v95", "v96", "v97", "v98", "v99", "v100", "v101", "v102", "v103", "v104", "v105", "v106", "v107", "v108", "v109", "v110", "v111", "v112", "v113", "v114", "v115", "v116", "v117", "v118", "v119", "v120", "v121", "v122", "v123", "v124", "v125", "v126", "v127", "v140", "v141", "v142", "v143", "v144", "v145", "v146", "v147", "v148", "v149", "v150", "v151", "v152", "v153", "v154", "v155", "v156", "v157", "v158", "v159", "v160", "v161", "v162", "v163", "v164", "v165", "v166", "v167", "v168", "v169", "v170", "v171", "v172", "v173", "v174", "v175", "v176", "v177", "v178", "v179", "v180", "v181", "v182", "v183", "v184", "v185", "v186", "v187", "v188", "v189", "v190", "v191", "v192", "v193", "v194", "v195", "v196", "v197", "v198", "v199", "v200", "v201", "v202", "v203", "v204", "v205", "v206", "v207", "v208", "v209", "v210", "v211", "v212", "v213", "v214", "v215", "v216", "v217", "v218", "v219", "v220", "v221", "v222", "v223", "v224", "v225", "v226", "v227", "v228", "v229", "v230", "v231", "v232", "v233", "v234", "v235", "v236", "v237", "v248", "v249", "s26", "s68", "s69", "s79", "s36", "s37", "s38", "s57", "s58", "s59", "s49", "vcc", "scc", "m0", "memory");
        }
        asm volatile("wk_code_end1:");
        asm volatile("wk_code_end2:");

    }
}

extern "C" void kernel_launch(void* const* d_in, const int* in_sizes, int n_in,
                              void* d_out, int out_size, void* d_ws, size_t ws_size,
                              hipStream_t stream) {
    const unsigned short* x = (const unsigned short*)d_in[0];
    const unsigned short* dw = (const unsigned short*)d_in[1];
    const unsigned short* wqkv = (const unsigned short*)d_in[2];
    const unsigned short* wo = (const unsigned short*)d_in[3];
    unsigned short* out = (unsigned short*)d_out;
    u32x4* wf = (u32x4*)d_ws;
    win_attn_kernel<<<GRID, 512, 0, stream>>>(x, dw, wqkv, wo, wf, out);
}
```

```cpp
#include <hip/hip_runtime.h>
#include <hip/hip_bf16.h>
#include <stdint.h>

typedef short bf16x8 __attribute__((ext_vector_type(8)));
typedef float f32x4 __attribute__((ext_vector_type(4)));
typedef float f32x2 __attribute__((ext_vector_type(2)));
typedef unsigned u32x2 __attribute__((ext_vector_type(2)));
typedef unsigned u32x4 __attribute__((ext_vector_type(4)));

constexpr int IMG = 511;
constexpr int CH = 192;
constexpr int NWX = 73;
constexpr int NWIN = NWX * NWX;
constexpr int GRID = 256;
constexpr int NSHORT = 21 * GRID - NWIN;
constexpr int ROWBYTES = IMG * CH * 2;
constexpr int XBIAS = (IMG + 1) * CH * 2;
constexpr unsigned OOB = 0x40000000u;

constexpr int X_OFF = 32768;
constexpr int X1_OFF = 53632;
constexpr int O_OFF = 74496;
constexpr int V_OFF = 92928;
constexpr int KQL_OFF = 111360;
constexpr int OL_OFF = 128256;
constexpr int AT_OFF = 137472;
constexpr int CT_OFF = AT_OFF + 19584;
constexpr int RS_OFF = CT_OFF + 4096;
constexpr int WL_OFF = RS_OFF + 2304 + 64;
constexpr int LDS_BYTES = WL_OFF + 128;
#define RS_BASE (RS_OFF + (wave < 4 ? wave * 384 : 768 + wave * 192))
constexpr int TP_OFF = X1_OFF + 8192;
constexpr int WA = 12, WB = 4;
constexpr int STL_OFF = O_OFF + 12288;
#define LB_OFF(b) ((b) < 7 ? V_OFF + (b) * 6160 : X1_OFF + ((b) - 7) * 6160)
#define LPOS(wc) (((wc) & 1) ? 0 : 6)
constexpr unsigned CROW = 0x060a0230u;
constexpr unsigned CLANE = 0x00132C3Fu;

constexpr unsigned long long TOKMAP[6] = {0x20001c190f2a2202ull, 0x052517121f061a13ull, 0x181e08151d0b260cull, 0x1b2b2903102d0901ull, 0x04162e0e2824142cull, 0x110a072127232f0dull};
constexpr unsigned long long ROWMAP[8] = {0x08321b240f1c0006ull, 0x23310e2a09170d14ull, 0x210c2f030a273418ull, 0x1d2b11221f043739ull, 0x3234363a103e192dull, 0x291a20022c3f1605ull, 0x303a3c2628122e15ull, 0x39253d1e01070b13ull};

constexpr int WF_BYTES = 8 * 36 * 1024;
constexpr int EP_OFF = WF_BYTES;
constexpr int CN_OFF = EP_OFF + 256, FL_OFF = EP_OFF + 512;
constexpr int SH_OFF = EP_OFF + 1024;
constexpr int WS_BYTES = SH_OFF + GRID * 12288;
constexpr int GO_OFF = WL_OFF + 124;
constexpr int PG_OFF = WL_OFF + 96;
constexpr bool PF1 = false;
constexpr int SPIN_MAX = 1 << 20;

#define MFMA32(a, b, c) __builtin_amdgcn_mfma_f32_16x16x32_bf16((a), (b), (c), 0, 0, 0)
#define BC8(v) __builtin_bit_cast(bf16x8, (v))
#define BAR() asm volatile("s_waitcnt lgkmcnt(0)\n\ts_barrier" ::: "memory")
#define SB() __builtin_amdgcn_sched_barrier(0)
#define OPQ(x) asm volatile("" : "+v"(x))
#define ZERO4 ((f32x4){0.f, 0.f, 0.f, 0.f})

typedef __bf16 nbf16x2 __attribute__((ext_vector_type(2)));
__device__ __forceinline__ unsigned pk2(float a, float b) {
    f32x2 v = {a, b};
    nbf16x2 r = __builtin_convertvector(v, nbf16x2);
    return __builtin_bit_cast(unsigned, r);
}
__device__ __forceinline__ float bflo(unsigned u) { return __uint_as_float(u << 16); }
__device__ __forceinline__ float bfhi(unsigned u) { return __uint_as_float(u & 0xffff0000u); }

__device__ __forceinline__ float bf2f(unsigned short v) { return __uint_as_float((unsigned)v << 16); }
__host__ __device__ __forceinline__ constexpr unsigned byte_of(unsigned long long a, unsigned long long b, int i) {
    return (unsigned)(((i & 8) ? b : a) >> (8 * (i & 7))) & 0xFFu;
}
__host__ __device__ __forceinline__ constexpr void seq_to_win(int seq, int& wy, int& wx) {
    const bool last = seq >= 5110;
    const int s = last ? 35 : seq / 146, r = seq - 146 * s;
    const int kx = last ? r / 6 : r >> 2, pos = r - (last ? 6 : 4) * kx;
    const bool low = last && pos >= (kx == 36 ? 2 : 4);
    wy = low ? 72 : 2 * s + (pos & 1);
    wx = 2 * kx + (low ? (kx == 36 ? 0 : pos - 4) : pos >> 1);
}
__host__ __device__ __forceinline__ constexpr int win_to_seq(int wy, int wx) {
    const int kx = wx >> 1;
    return wy == 72 ? 5110 + 6 * kx + (kx == 36 ? 2 : 4 + (wx & 1)) : 146 * (wy >> 1) + (wy >= 70 ? 6 : 4) * kx + 2 * (wx & 1) + (wy & 1);
}
__host__ __device__ __forceinline__ constexpr int block_of(int wy, int wx) { return wy >= 70 ? 1295 + (wx & ~1) + (wy == 72) : 37 * (wy >> 1) + (wx >> 1); }
__host__ __device__ __forceinline__ constexpr void block_pos(int b, int& ky, int& kx) {
    if (b >= 1295) { kx = (b - 1295) >> 1; ky = 70 + 2 * ((b - 1295) & 1); }
    else { const int s = b / 37; kx = b - 37 * s; ky = 2 * s; }
}
__host__ __device__ __forceinline__ constexpr unsigned wl_entry(int seq) {
    int wy = 0, wx = 0;
    seq_to_win(seq, wy, wx);
    return (unsigned)((wy * 7 * IMG + wx * 7) * (CH * 2)) | (wy == 0 ? 0x08000000u : 0u) | (wx == 0 ? 0x10000000u : 0u) |
           (wx == NWX - 1 ? 0x20000000u : 0u) | ((unsigned)(wx & 1) << 30) | ((unsigned)(wy & 1) << 31);
}
__host__ __device__ __forceinline__ constexpr unsigned dma_off(int lane) {
    const int col = (lane * 43) >> 8;
    return (unsigned)(col * (CH * 2 - 96) + 16 * lane) | (col == 0 ? 0x10000000u : 0u) | (col == 8 ? 0x20000000u : 0u);
}

__host__ __device__ __forceinline__ constexpr unsigned pf_off(int lane) {
    const int l = lane < 63 ? lane : 62, r = l / 21;
    return (unsigned)(r * ROWBYTES + (l - 21 * r) * 128) | (r == 0 ? 0x08000000u : 0u);
}
constexpr int RECN = 80;
struct Tables { unsigned rec[GRID][RECN]; unsigned img[768]; };
constexpr Tables make_tables() {
    Tables t = {};
    for (int bx = 0; bx < GRID; ++bx) {
        unsigned* r = t.rec[bx];
        const int q0 = (bx & 7) * 32 + (bx >> 3), cnt = q0 < NSHORT ? 20 : 21, start = q0 < NSHORT ? 20 * q0 : 21 * q0 - NSHORT;
        for (int i = 0; i < 32; ++i) r[i] = i < cnt ? wl_entry(start + i) : 0x7ffffffu;
        int wy = 0, wx = 0;
        seq_to_win(start, wy, wx);
        const int key0 = block_of(wy, wx);
        seq_to_win(start + cnt - 1, wy, wx);
        const int nblk = block_of(wy, wx) - key0 + 1;
        for (int b = 0; b < 8; ++b) {
            int ky = 0, kx = 0;
            block_pos(key0 + b, ky, kx);
            r[64 + b] = b < nblk ? (unsigned)(((7 * ky + 5) * IMG + 14 * kx + 5) * (CH * 2) + XBIAS) | (kx == 36 ? 1u : 0u) : OOB;
            for (int c = 0; c < 4; ++c) {
                const int y = ky + (c >> 1), x = 2 * kx + (c & 1), i = win_to_seq(y, x) - start;
                const bool ok = b < nblk && y < NWX && x < NWX && i >= 0 && i < cnt;
                r[32 + 4 * b + c] = ok ? (unsigned)(((7 * y + LPOS(y)) * IMG + 7 * x + LPOS(x)) * (CH * 2) + i) : OOB + 47;
            }
        }
        r[72] = q0; r[73] = cnt;
    }
    for (int lane = 0; lane < 64; ++lane) {
        for (int e = 0; e < 3; ++e) {
            const int tk = byte_of(TOKMAP[2 * e], TOKMAP[2 * e + 1], lane & 15);
            t.img[192 + 4 * lane + e] = (unsigned)((tk / 7) * ROWBYTES + (tk % 7) * (CH * 2)) + 16 * (lane >> 4);
        }
        t.img[448 + 4 * lane] = dma_off(lane);
        t.img[449 + 4 * lane] = pf_off(lane);
    }
    return t;
}
static __constant__ const Tables TAB = make_tables();
typedef __attribute__((address_space(1))) unsigned gu32;
typedef __attribute__((address_space(1))) unsigned long long gu64;
#define RLX_AGENT __ATOMIC_RELAXED, __HIP_MEMORY_SCOPE_AGENT
#define TAG_OF(e) (((unsigned long long)~((e) + 1u) << 32) | ((e) + 1u))

__device__ __forceinline__ float rstd_of(const char* p) {
    f32x4 p0 = *(const f32x4*)(p);
    f32x4 p1 = *(const f32x4*)(p + 1024);
    float s = (p0[0] + p0[2]) + (p1[0] + p1[2]);
    float q = (p0[1] + p0[3]) + (p1[1] + p1[3]);
    float mean = s * (1.f / 192.f);
    float var = __builtin_fmaf(q, 1.f / 192.f, -mean * mean);
    var = __builtin_fmaxf(var, 0.f);
    return __builtin_amdgcn_rsqf(var + 1e-5f);
}

#if defined(__HIP_DEVICE_COMPILE__)
#define NOPK __attribute__((target("no-packed-fp32-ops")))
#else
#define NOPK
#endif
__global__ __launch_bounds__(512, 2) NOPK void win_attn_kernel(const unsigned short* __restrict__ x, const unsigned short* __restrict__ dw,
                                                          const unsigned short* __restrict__ wqkv, const unsigned short* __restrict__ wo,
                                                          u32x4* wf, unsigned short* __restrict__ out) {
    __shared__ __attribute__((aligned(16))) char smem[LDS_BYTES];

    asm volatile("wk_code_begin:");
    const int tid = threadIdx.x;
    const int wave = __builtin_amdgcn_readfirstlane(tid >> 6);
    const int w = wave & 3;
    const unsigned ldsb = (unsigned)(size_t)smem;
#define GET_LANE(l) int l; asm volatile("v_mbcnt_lo_u32_b32 %0, -1, 0\n\tv_mbcnt_hi_u32_b32 %0, -1, %0" : "=v"(l)); __builtin_assume(l >= 0 && l < 64);
#define ROLE_SETUP                                                                                 \
    int bx = blockIdx.x;                                                                           \
    asm volatile("" : "+s"(bx));                                                                   \
    bx = __builtin_amdgcn_readfirstlane(bx);                                                       \
    const unsigned* rec = TAB.rec[bx];                                                             \
    const unsigned en0 = rec[0], en1 = rec[1];                                                     \
    const int q0 = rec[72], cnt = rec[73];                                                         \
    const auto wrsrc = __builtin_amdgcn_make_buffer_rsrc((void*)wf, 0, WS_BYTES, 0x00020000);      \
    const auto xrsrc = __builtin_amdgcn_make_buffer_rsrc((void*)((const char*)x - XBIAS), 0, IMG * IMG * CH * 2 + XBIAS, 0x00020000);
#define DMA_SLICE(en)                                                                              \
        {                                                                                          \
            const unsigned vo_ = dma_off(lane) & (0x401fffffu | ((en) & 0x38000000u));             \
            unsigned so_ = ((en) & 0x7ffffffu) + 96 * w, n_ = 8;                                   \
            asm volatile("s_mov_b32 m0, %2\n\ts_mov_b32 exec_hi, 0x3fffff\n\tbuffer_load_dwordx4 %3, %4, %5 offen lds\n"     \
                         ".Lds%=:\n\ts_add_u32 m0, m0, 872\n\ts_add_u32 %0, %0, %6\n\ts_sub_u32 %1, %1, 1\n\t"            \
                         "buffer_load_dwordx4 %3, %4, %0 offen lds\n\ts_cmp_lg_u32 %1, 0\n\ts_cbranch_scc1 .Lds%=\n\ts_mov_b32 exec_hi, -1" \
                         : "+s"(so_), "+s"(n_) : "s"(ldsb + w * 8192), "v"(vo_), "s"(xrsrc), "s"(((en) & 0x08000000u) ? OOB : so_), "n"(ROWBYTES) \
                         : "m0", "scc", "memory");                                                 \
        }
    asm volatile(
        "s_load_dwordx8 s[24:31], %[ka], 0x0\n\t"
        "s_load_dwordx2 s[32:33], %[ka], 0x20\n\t"
        "s_mul_i32 s36, %[bx], %[recb]\n\t"
        "s_xor_b32 s37, %[wave], 4\n\t"
        "s_lshl2_add_u32 s38, s37, s36\n\t"
        "s_load_dword s38, %[tab], s38 offset:0x100\n\t"
        "s_load_dword s39, %[tab], s36\n\t"
        "v_mbcnt_lo_u32_b32 v1, -1, 0\n\t"
        "v_mbcnt_hi_u32_b32 v1, -1, v1\n\t"
        "s_and_b32 s44, %[bx], 7\n\t"
        "s_lshl_b32 s44, s44, 5\n\t"
        "s_lshr_b32 s45, %[bx], 3\n\t"
        "s_add_u32 s44, s44, s45\n\t"
        "v_lshlrev_b32 v2, 4, v1\n\t"
        "s_mov_b32 s43, 0x20000\n\t"
        "s_cmp_ge_u32 s44, 36\n\t"
        "s_cbranch_scc1 .Lnp%=\n\t"
        "s_mul_i32 s45, s44, 6\n\t"
        "s_lshl1_add_u32 s46, s44, 210\n\t"
        "s_cmp_ge_u32 %[wave], 6\n\t"
        "s_cselect_b64 vcc, -1, 0\n\t"
        "s_cselect_b32 s45, s46, s45\n\t"
        "s_movk_i32 s52, 1152\n\t"
        "s_cselect_b32 s52, 384, s52\n\t"
        "s_cselect_b32 s53, 0, 0xbbaaaaab\n\t"
        "s_add_u32 s45, s45, %[wave]\n\t"
        "s_mul_i32 s46, s45, 171\n\t"
        "s_lshr_b32 s46, s46, 10\n\t"
        "s_mul_i32 s47, s46, 6\n\t"
        "s_sub_u32 s47, s45, s47\n\t"
        "s_mul_i32 s48, s46, 43\n\t"
        "s_lshr_b32 s48, s48, 8\n\t"
        "s_mul_i32 s49, s48, 6\n\t"
        "s_sub_u32 s49, s46, s49\n\t"
        "s_mul_i32 s50, s48, 48\n\t"
        "s_cmp_lt_u32 s49, 3\n\t"
        "s_cselect_b32 s34, 1.0, 0x3e553b94\n\t"
        "s_movk_i32 s51, -48\n\t"
        "s_cselect_b32 s51, 192, s51\n\t"
        "s_cmp_lt_u32 s48, 4\n\t"
        "s_cselect_b32 s51, s51, s50\n\t"
        "s_cselect_b32 s34, s34, 1.0\n\t"
        "s_add_u32 s51, s51, s50\n\t"
        "s_lshl4_add_u32 s51, s49, s51\n\t"
        "s_and_b32 s49, s49, 1\n\t"
        "s_mul_i32 s49, s49, 12\n\t"
        "s_add_u32 s49, s49, 576\n\t"
        "s_cmp_ge_u32 %[wave], 6\n\t"
        "s_cselect_b32 s49, s49, 0\n\t"
        "s_sub_u32 s51, s51, s49\n\t"
        "s_and_b32 s49, s47, 1\n\t"
        "s_mul_i32 s49, s49, 88\n\t"
        "s_lshl3_add_u32 s47, s47, s49\n\t"
        "v_lshrrev_b32 v3, 5, v1\n\t"
        "v_mul_u32_u24 v3, 48, v3\n\t"
        "v_lshrrev_b32 v4, 1, v1\n\t"
        "v_and_or_b32 v3, v4, 8, v3\n\t"
        "v_add_u32 v3, s47, v3\n\t"
        "v_mul_u32_u24 v3, s52, v3\n\t"
        "v_and_b32 v4, 15, v1\n\t"
        "v_and_b32 v5, 12, v1\n\t"
        "v_cndmask_b32 v5, 0, v5, vcc\n\t"
        "v_add3_u32 v5, v4, v5, s51\n\t"
        "v_lshl_add_u32 v3, v5, 1, v3\n\t"
        "v_mov_b64 v[8:9], 0\n\t"
        "v_mov_b64 v[10:11], 0\n\t"
        "v_mov_b64 v[12:13], 0\n\t"
        "v_mov_b64 v[14:15], 0\n\t"
        "s_mul_i32 s42, s52, 192\n\t"
        "s_cmp_ge_u32 %[wave], 6\n\t"
        "s_waitcnt lgkmcnt(0)\n\t"
        "s_cselect_b64 s[40:41], s[30:31], s[28:29]\n\t"
        "s_and_b32 s41, s41, 0xffff\n\t"
        "buffer_load_short_d16_hi v8, v3, s[40:43], 0 offen\n\t"
        "buffer_load_short_d16_hi v9, v3, s[40:43], s52 offen\n\t"
        "s_lshl_b32 s46, s52, 1\n\t"
        "buffer_load_short_d16_hi v10, v3, s[40:43], s46 offen\n\t"
        "s_add_u32 s46, s46, s52\n\t"
        "buffer_load_short_d16_hi v11, v3, s[40:43], s46 offen\n\t"
        "s_add_u32 s46, s46, s52\n\t"
        "buffer_load_short_d16_hi v12, v3, s[40:43], s46 offen\n\t"
        "s_add_u32 s46, s46, s52\n\t"
        "buffer_load_short_d16_hi v13, v3, s[40:43], s46 offen\n\t"
        "s_add_u32 s46, s46, s52\n\t"
        "buffer_load_short_d16_hi v14, v3, s[40:43], s46 offen\n\t"
        "s_add_u32 s46, s46, s52\n\t"
        "buffer_load_short_d16_hi v15, v3, s[40:43], s46 offen\n\t"
        "v_mov_b32 v6, %[ep]\n\t"
        "global_load_dword v7, v6, s[32:33] sc1\n\t"
        "v_mov_b32 v5, %[red]\n\t"
        "v_mad_u32_u24 v4, v4, %[c144], v5\n\t"
        "v_lshrrev_b32 v5, 2, v1\n\t"
        "v_and_b32 v5, 12, v5\n\t"
        "s_lshl_b32 s46, %[wave], 4\n\t"
        "v_add3_u32 v5, v4, v5, s46\n\t"
        "s_mov_b32 s40, s32\n\t"
        "s_and_b32 s41, s33, 0xffff\n\t"
        "s_mov_b32 s42, %[wsb]\n\t"
        "s_lshl_b32 s45, s45, 10\n\t"
        "s_waitcnt vmcnt(1)\n\t"
        "v_add_f32 v16, v8, v10\n\t"
        "v_add_f32 v17, v9, v11\n\t"
        "v_add_f32 v18, v12, v14\n\t"
        "v_add_f32 v19, v13, v15\n\t"
        "v_add_f32 v16, v16, v18\n\t"
        "v_add_f32 v17, v17, v19\n\t"
        "v_add_f32 v16, v16, v17\n\t"
        "ds_write_b32 v5, v16\n\t"
        "s_waitcnt lgkmcnt(0)\n\t"
        "s_barrier\n\t"
        "ds_read_b128 v[16:19], v4\n\t"
        "ds_read_b128 v[20:23], v4 offset:16\n\t"
        "ds_read_b128 v[24:27], v4 offset:32\n\t"
        "ds_read_b128 v[28:31], v4 offset:48\n\t"
        "ds_read_b128 v[32:35], v4 offset:64\n\t"
        "ds_read_b128 v[36:39], v4 offset:80\n\t"
        "s_waitcnt lgkmcnt(4)\n\t"
        "v_add_f32 v16, v16, v18\n\t"
        "v_add_f32 v17, v17, v19\n\t"
        "v_add_f32 v20, v20, v22\n\t"
        "v_add_f32 v21, v21, v23\n\t"
        "s_waitcnt lgkmcnt(2)\n\t"
        "v_add_f32 v24, v24, v26\n\t"
        "v_add_f32 v25, v25, v27\n\t"
        "v_add_f32 v28, v28, v30\n\t"
        "v_add_f32 v29, v29, v31\n\t"
        "v_add_f32 v16, v16, v20\n\t"
        "v_add_f32 v17, v17, v21\n\t"
        "s_waitcnt lgkmcnt(0)\n\t"
        "v_add_f32 v32, v32, v34\n\t"
        "v_add_f32 v33, v33, v35\n\t"
        "v_add_f32 v36, v36, v38\n\t"
        "v_add_f32 v37, v37, v39\n\t"
        "v_add_f32 v24, v24, v28\n\t"
        "v_add_f32 v25, v25, v29\n\t"
        "v_add_f32 v32, v32, v36\n\t"
        "v_add_f32 v33, v33, v37\n\t"
        "v_add_f32 v16, v16, v24\n\t"
        "v_add_f32 v17, v17, v25\n\t"
        "v_add_f32 v16, v16, v32\n\t"
        "v_add_f32 v17, v17, v33\n\t"
        "v_add_f32 v16, v16, v17\n\t"
        "v_mul_f32 v16, s53, v16\n\t"
        "v_mul_f32 v16, s34, v16\n\t"
        "v_fma_f32 v8, v8, s34, v16\n\t"
        "v_fma_f32 v9, v9, s34, v16\n\t"
        "v_fma_f32 v10, v10, s34, v16\n\t"
        "v_fma_f32 v11, v11, s34, v16\n\t"
        "v_fma_f32 v12, v12, s34, v16\n\t"
        "v_fma_f32 v13, v13, s34, v16\n\t"
        "v_fma_f32 v14, v14, s34, v16\n\t"
        "v_fma_f32 v15, v15, s34, v16\n\t"
        "v_cvt_pk_bf16_f32 v8, v8, v9\n\t"
        "v_cvt_pk_bf16_f32 v9, v10, v11\n\t"
        "v_cvt_pk_bf16_f32 v10, v12, v13\n\t"
        "v_cvt_pk_bf16_f32 v11, v14, v15\n\t"
        "buffer_store_dwordx4 v[8:11], v2, s[40:43], s45 offen sc1\n\t"
        "s_or_b32 s46, s44, s37\n\t"
        "s_cmp_lg_u32 s46, 0\n\t"
        "s_cbranch_scc1 .Lnc%=\n\t"
        "v_mov_b32 v3, 0\n\t"
        "global_store_dword v6, v3, s[32:33] offset:256 sc1\n\t"
        ".Lnc%=:\n\t"
        "v_lshl_add_u32 v6, s44, 3, v6\n\t"
        "s_waitcnt vmcnt(0)\n\t"
        "s_barrier\n\t"
        "s_cmp_lg_u32 %[wave], 0\n\t"
        "s_cbranch_scc1 .Lnp%=\n\t"
        "v_add_u32 v4, 1, v7\n\t"
        "v_not_b32 v5, v4\n\t"
        "s_mov_b64 exec, 1\n\t"
        "global_store_dwordx2 v6, v[4:5], s[32:33] offset:512 sc1\n\t"
        "s_mov_b64 exec, -1\n\t"
        ".Lnp%=:\n\t"
        "s_getpc_b64 s[40:41]\n\t"
        ".Lwk_here%=:\n\t"
        "s_sub_u32 s40, s40, .Lwk_here%=-wk_code_begin\n\t"
        "s_subb_u32 s41, s41, 0\n\t"
        "s_and_b32 s41, s41, 0xffff\n\t"
        "s_mov_b32 s42, wk_code_end-wk_code_begin\n\t"
        "s_max_u32 s42, s42, wk_code_end1-wk_code_begin\n\t"
        "s_max_u32 s42, s42, wk_code_end2-wk_code_begin\n\t"
        "s_add_u32 m0, %[lds], %[sink]\n\t"
        "v_lshlrev_b32 v3, 7, v1\n\t"
        "s_lshl_b32 s45, %[wave], 13\n\t"
        "buffer_load_dword v3, s[40:43], s45 offen lds\n\t"
        "s_mov_b64 s[40:41], %[tab]\n\t"
        "s_and_b32 s41, s41, 0xffff\n\t"
        "s_mov_b32 s42, %[tabb]\n\t"
        "s_lshl_b32 s46, %[wave], 10\n\t"
        "s_add_u32 s47, s46, %[imgs]\n\t"
        "s_add_u32 s48, s46, %[imgd]\n\t"
        "s_lshl_b32 s49, %[wave], 8\n\t"
        "s_add_u32 s49, s49, %[mapd]\n\t"
        "s_add_u32 s50, s36, 128\n\t"
        "s_mov_b64 vcc, -1\n\t"
        "s_cmp_eq_u32 %[wave], 0\n\t"
        "s_cselect_b32 s47, s36, s47\n\t"
        "s_cselect_b32 s48, %[wld], s48\n\t"
        "s_cselect_b64 vcc, 0xff, vcc\n\t"
        "s_cmp_ge_u32 %[wave], 4\n\t"
        "s_cselect_b32 s47, s50, s47\n\t"
        "s_cselect_b32 s48, s49, s48\n\t"
        "s_cselect_b64 exec, 0xff, vcc\n\t"
        "s_add_u32 m0, %[lds], s48\n\t"
        "s_and_b32 s49, %[wave], 3\n\t"
        "buffer_load_dwordx4 v2, s[40:43], s47 offen lds\n\t"
        "s_mov_b64 exec, -1\n\t"
        "v_mul_u32_u24 v3, 43, v1\n\t"
        "v_lshrrev_b32 v3, 8, v3\n\t"
        "v_mad_u32_u24 v4, v3, %[c288], v2\n\t"
        "s_mul_i32 s49, s49, 96\n\t"
        "s_mul_i32 s50, s37, 6160\n\t"
        "s_add_u32 s50, s50, %[lb0]\n\t"
        "s_cmp_eq_u32 s37, 7\n\t"
        "s_cselect_b32 s50, %[lb7], s50\n\t"
        "s_add_u32 s46, s46, %[tp]\n\t"
        "s_waitcnt lgkmcnt(0)\n\t"
        "s_cmp_ge_u32 %[wave], 4\n\t"
        "s_cbranch_scc1 .Lib%=\n\t"
        "s_mov_b32 s40, s26\n\t"
        "s_and_b32 s41, s27, 0xffff\n\t"
        "s_movk_i32 s42, 3456\n\t"
        "s_add_u32 m0, %[lds], s46\n\t"
        "s_nop 0\n\t"
        "buffer_load_dwordx4 v4, s[40:43], s49 offen lds\n\t"
        ".Lib%=:\n\t"
        "s_sub_u32 s40, s24, %[xb]\n\t"
        "s_subb_u32 s41, s25, 0\n\t"
        "s_and_b32 s41, s41, 0xffff\n\t"
        "s_mov_b32 s42, %[xsz]\n\t"
        "s_cmp_eq_u32 s38, 2.0\n\t"
        "s_cbranch_scc1 .Lic%=\n\t"
        "s_mov_b32 exec_hi, 0xffff\n\t"
        "s_add_u32 m0, %[lds], s50\n\t"
        "s_mov_b32 s46, 4\n\t"
        "s_bitcmp1_b32 s38, 0\n\t"
        "s_cselect_b32 s47, 2.0, s38\n\t"
        "s_bitset0_b32 s38, 0\n\t"
        ".Lid%=:\n\t"
        "buffer_load_dwordx4 v2, s[40:43], s38 offen lds\n\t"
        "buffer_load_dwordx4 v2, s[40:43], s47 offen offset:768 lds\n\t"
        "s_add_u32 m0, m0, 1536\n\t"
        "s_add_u32 s38, s38, %[rb]\n\t"
        "s_add_u32 s47, s47, %[rb]\n\t"
        "s_sub_u32 s46, s46, 1\n\t"
        "s_cmp_lg_u32 s46, 0\n\t"
        "s_cbranch_scc1 .Lid%=\n\t"
        ".Lic%=:\n\t"
        "s_cmp_lt_u32 %[wave], 4\n\t"
        "s_cbranch_scc1 .Lie%=\n\t"
        "s_mov_b32 exec_hi, 0x3fffff\n\t"
        "s_and_b32 s46, s39, 0x10000000\n\t"
        "s_and_b32 s47, s39, 0x20000000\n\t"
        "v_cmp_eq_u32 vcc, 0, v3\n\t"
        "v_mov_b32 v5, s46\n\t"
        "v_mov_b32 v6, s47\n\t"
        "v_cndmask_b32 v5, 0, v5, vcc\n\t"
        "v_cmp_eq_u32 vcc, 8, v3\n\t"
        "s_and_b32 s46, s39, 0x7ffffff\n\t"
        "s_add_u32 s46, s46, s49\n\t"
        "v_cndmask_b32 v6, 0, v6, vcc\n\t"
        "v_or3_b32 v4, v4, v5, v6\n\t"
        "s_bitcmp1_b32 s39, 27\n\t"
        "s_cselect_b32 s47, 2.0, s46\n\t"
        "s_lshl_b32 s45, s37, 13\n\t"
        "s_add_u32 m0, %[lds], s45\n\t"
        "s_mov_b32 s48, 8\n\t"
        "buffer_load_dwordx4 v4, s[40:43], s47 offen lds\n\t"
        ".Lia%=:\n\t"
        "s_add_u32 m0, m0, 872\n\t"
        "s_add_u32 s46, s46, %[rb]\n\t"
        "s_sub_u32 s48, s48, 1\n\t"
        "buffer_load_dwordx4 v4, s[40:43], s46 offen lds\n\t"
        "s_cmp_lg_u32 s48, 0\n\t"
        "s_cbranch_scc1 .Lia%=\n\t"
        ".Lie%=:\n\t"
        "s_mov_b64 exec, -1\n\t"
        :: [ka] "s"(__builtin_amdgcn_kernarg_segment_ptr()), [bx] "s"(blockIdx.x), [wave] "s"(wave), [tab] "s"(&TAB), [lds] "s"(ldsb),
           [red] "s"(ldsb + O_OFF), [c144] "s"(144), [c288] "s"(288), [recb] "n"(RECN * 4), [ep] "n"(EP_OFF), [wsb] "n"(WS_BYTES), [sink] "n"(X_OFF + 18432),
           [tabb] "n"(sizeof(Tables)), [imgs] "n"(GRID * RECN * 4 - 1024), [imgd] "n"(AT_OFF + 18816 - 1024), [mapd] "n"(TP_OFF + 3072), [wld] "n"(WL_OFF),
           [lb0] "n"(LB_OFF(0)), [lb7] "n"(LB_OFF(7)), [tp] "n"(TP_OFF), [xb] "n"(XBIAS), [xsz] "n"(IMG * IMG * CH * 2 + XBIAS), [rb] "n"(ROWBYTES)
        : "v1", "v2", "v3", "v4", "v5", "v6", "v7", "v8", "v9", "v10", "v11", "v12", "v13", "v14", "v15", "v16", "v17", "v18", "v19", "v20", "v21", "v22", "v23", "v24", "v25", "v26", "v27", "v28", "v29", "v30", "v31", "v32", "v33", "v34", "v35", "v36", "v37", "v38", "v39", "s24", "s25", "s26", "s27", "s28", "s29", "s30", "s31", "s32", "s33", "s34", "s36", "s37", "s38", "s39", "s40", "s41", "s42", "s43", "s44", "s45", "s46", "s47", "s48", "s49", "s50", "s51", "s52", "s53", "m0", "vcc", "scc", "memory");
#define WAIT_GO                                                                                    \
        {                                                                                          \
            unsigned go_;                                                                          \
            asm volatile(".Lgo%=:\n\tds_read_b32 %0, %1\n\ts_waitcnt lgkmcnt(0)\n\tv_cmp_ne_u32 vcc, -1, %0\n\ts_nop 4\n\ts_cbranch_vccnz .Lgo%=" \
                         : "=&v"(go_) : "v"(ldsb + GO_OFF) : "vcc", "memory");                     \
        }
    bf16x8 W[36];
#define LOAD_W(j0, j1, k0, k1)                                                                     \
    _Pragma("unroll") for (int ks_ = (k0); ks_ < (k1); ++ks_)                                      \
        _Pragma("unroll") for (int j_ = (j0); j_ < (j1); ++j_)                                     \
            W[j_ * 6 + ks_] = BC8(__builtin_amdgcn_raw_buffer_load_b128(wrsrc, lane * 16, wave * 36864 + (j_ * 6 + ks_) * 1024, 16));

#define LOAD_WI(i0, i1)                                                                            \
    _Pragma("unroll") for (int i_ = (i0); i_ < (i1); ++i_)                                         \
        W[i_] = BC8(__builtin_amdgcn_raw_buffer_load_b128(wrsrc, lane * 16, wave * 36864 + i_ * 1024, 16));

#define LOAD_WL(i0, i1)                                                                            \
    _Pragma("unroll") for (int i_ = (i0); i_ < (i1); ++i_)                                         \
        W[i_] = BC8(__builtin_amdgcn_raw_buffer_load_b128(wrsrc, lane * 16, wl_ + i_ * 1024, 16));

#define LANE_SETUP                                                                                 \
    int lane;                                                                                      \
    asm volatile("v_mbcnt_lo_u32_b32 %0, -1, 0\n\tv_mbcnt_hi_u32_b32 %0, -1, %0" : "=v"(lane));    \
    __builtin_assume(lane >= 0 && lane < 64);                                                      \
    const int g = lane >> 4, l15 = lane & 15, sw = l15 >> 1;                                       \
    int fE = X_OFF + l15 * 384 + ((g ^ sw) << 4);                                                  \
    int fO = fE ^ 64;                                                                              \
    OPQ(fE); OPQ(fO);
#define FRAG(base, t, ks) (*(const bf16x8*)(smem + (((ks) & 1) ? fO : fE) + ((base) - X_OFF) + (t) * 6144 + ((ks) >> 1) * 128))
#define RSTD_PUBLISH(ln_, stoff)                                                                   \
    *(float*)(smem + ((ln_) < 48 ? RS_BASE : RS_OFF + 2304 - 192) + (ln_) * 4) = rstd_of(smem + (stoff) + (ln_) * 16);

#define XPAT(r) (((r) < 48 ? (r) : 48) * 384 + (((2 * w) ^ (((r) >> 1) & 7)) << 4))
#define SPAT(r) ((w >> 1) * 1024 + (w & 1) * 8 + (r) * 16)
#define CONV_CONSTS(var) \
            const unsigned long long rlo = g == 0 ? ROWMAP[0] : (g == 1 ? ROWMAP[2] : (g == 2 ? ROWMAP[4] : ROWMAP[6])); \
            const unsigned long long rhi = g == 0 ? ROWMAP[1] : (g == 1 ? ROWMAP[3] : (g == 2 ? ROWMAP[5] : ROWMAP[7])); \
            int xrow = byte_of(rlo, rhi, l15); \
            if (lane == (int)((CLANE >> (8 * (var))) & 0xFF)) xrow = 48; \
            if (lane == 63) xrow = (CROW >> (8 * (var))) & 0xFF; \
            const unsigned pXw_ = XPAT(xrow); \
            const unsigned pSTw_ = (w >> 1) * 1024 + (w & 1) * 8 + xrow * 16; \
            const int oy = (0x5320 >> (4 * (l15 >> 2))) & 15, ox = (0x5320 >> (4 * (l15 & 3))) & 15; \
            const unsigned aB_ = ldsb + w * 8192 + (oy + (g >> 1)) * 872 + (ox + 2 * (g & 1)) * 96; \
            const int dyi = g - (l15 >> 3); \
            const unsigned aA_ = ldsb + AT_OFF + (l15 & 3) * 16 + \
                                 ((dyi < 0 || dyi > 2) ? 18816 : (dyi == 0 ? 0 : (dyi == 1 ? 6208 : 12544)) + ((l15 >> 2) & 1) * 3072 + w * 768); \
            const unsigned idx_ = (l15 & 3) * 0x5555u; \
            const unsigned se = (l15 == 4 * g) ? 0x3F803F80u : 0u; \
            const u32x4 sel4 = {se, se, se, se};
#define PIN_CONSTS asm volatile("" :: "v"(pXw_), "v"(pSTw_), "v"(aB_), "v"(aA_), "v"(idx_), "v"(se));

    if (wave < 4) {
        ROLE_SETUP
        LANE_SETUP
        asm volatile("s_waitcnt vmcnt(0)" ::: "memory");
        if (PF1) {
            const int li = w * 64 + lane, r = (li * 2428) >> 16;
            const unsigned vo = (r > 8 || (r == 0 && (en1 & 0x08000000u))) ? OOB : r * ROWBYTES + (li - 27 * r) * 128;
            asm volatile("s_mov_b32 m0, %0\n\ts_nop 0\n\tbuffer_load_dword %1, %2, %3 offen lds"
                         :: "s"(ldsb + X_OFF + 18432), "v"(vo), "s"(xrsrc), "s"(en1 & 0x7ffffffu) : "m0", "memory");
        }
        if (lane < 48) {
            unsigned tp[9];
#pragma unroll
            for (int k = 0; k < 9; ++k) tp[k] = *(const unsigned short*)(smem + TP_OFF + w * 1024 + k * 96 + lane * 2);
            tp[4] |= 0x3F800000u;
#pragma unroll
            for (int dy = 0; dy < 3; ++dy) {
                char* p = smem + AT_OFF + (dy == 0 ? 0 : (dy == 1 ? 6208 : 12544)) + w * 768 + lane * 16;
                *(u32x4*)p = (u32x4){tp[3 * dy], tp[3 * dy + 1], tp[3 * dy + 2], 0u};
                *(u32x4*)(p + 3072) = (u32x4){0u, tp[3 * dy], tp[3 * dy + 1], tp[3 * dy + 2]};
            }
        }
        BAR();
        {
            const int dW = X_OFF + l15 * 384 + (((2 * w + (g >> 1)) ^ sw) << 4) + 8 * (g & 1);
            const unsigned rsb = ldsb + RS_BASE;
            const unsigned pE_ = fE - X_OFF, aO_ = ldsb + dW + (O_OFF - X_OFF), aZ_ = ldsb + AT_OFF + 18816, aVB_ = ldsb + V_OFF + w * 4608 + 1024 + lane * 8;
            unsigned aRSw_ = rsb + lane * 4;
            const unsigned pST_ = lane * 16, aPLw_ = rsb + lane * 2, aRSr_ = rsb + l15 * 4;
            const unsigned g16_ = g * 16, aPLr_ = (l15 & 3) ? aZ_ : rsb + g * 8;
            const unsigned loVL_ = ((2 * w + (l15 >> 3)) << 4) + (l15 & 7) * 2, loOL_ = ((2 * w + (g >> 1)) << 4) + 8 * (g & 1);
            const unsigned on = (l15 & 3) ? 0u : 0x3F803F80u;
            const u32x4 ones4 = {on, on, on, on};
            const unsigned snk = ldsb + O_OFF + 15360 + w * 192, kr = ldsb + KQL_OFF + l15 * 800;
            const bool ok1 = 16 + l15 < cnt, dg = g == (l15 >> 2);
            u32x4 pl = {kr + w * 192 + g * 16, (ok1 ? kr + 12800 + w * 192 : snk) + g * 16, dg ? kr + 768 + w * 4 : snk, dg && ok1 ? kr + 12800 + 768 + w * 4 : snk};
            unsigned pcl, pch, len;
            asm volatile("s_getpc_b64 s[36:37]\n.Lwk_h2%=:\n\ts_sub_u32 s36, s36, .Lwk_h2%=-wk_code_begin\n\ts_subb_u32 s37, s37, 0\n\t"
                         "s_mov_b32 %2, wk_code_end-wk_code_begin\n\ts_max_u32 %2, %2, wk_code_end1-wk_code_begin" : "={s36}"(pcl), "={s37}"(pch), "=s"(len) :: "scc");
            pcl = __builtin_amdgcn_readfirstlane(pcl); pch = __builtin_amdgcn_readfirstlane(pch); len = __builtin_amdgcn_readfirstlane(len);
            const unsigned long long tb = w == 0 ? ((unsigned long long)pch << 32) | pcl : (unsigned long long)wf;
            const u32x4 tr = {(unsigned)tb, (unsigned)(tb >> 32) & 0xffffu, w == 0 ? len : (w == 1 ? (unsigned)WF_BYTES : SH_OFF + bx * 12288 + 12288), 0x00020000u};
            const unsigned to = w == 0 ? ((bx >> 3) % 6) * 8192 : (w == 1 ? 6 * 36864 + ((bx >> 3) % 9) * 8192 : SH_OFF + bx * 12288 + (w - 2) * 8192);
            asm volatile("" :: "{v244}"(pE_), "{v245}"(aO_), "{v246}"(aZ_), "{v247}"(aVB_), "{v248}"(pST_), "{v249}"(aRSw_), "{v250}"(aPLw_), "{v251}"(aRSr_), "{v252}"(g16_), "{v253}"(aPLr_), "{v254}"(loVL_), "{v255}"(loOL_), "{v[240:243]}"(ones4), "{v[232:235]}"(pl), "{s[36:39]}"(tr), "{s41}"(to));
            if (w == 0) {
                gu64* fp = (gu64*)((char*)wf + (lane < 36 ? FL_OFF + lane * 8 : EP_OFF));
#pragma unroll 1
                for (int it = 0; it < SPIN_MAX; ++it) {
                    const unsigned long long f = __hip_atomic_load(fp, RLX_AGENT);
                    const unsigned ep = __builtin_amdgcn_readlane((unsigned)f, 63);
                    if ((__builtin_amdgcn_ballot_w64(f != TAG_OF(ep)) & 0xfffffffffull) == 0ull) break;
                    __builtin_amdgcn_s_sleep(1);
                }
                __builtin_amdgcn_fence(__ATOMIC_ACQUIRE, "wavefront");
                asm volatile("ds_write_b32 %0, %1" :: "v"(ldsb + GO_OFF), "v"(~0u) : "memory");
            } else WAIT_GO
            __builtin_amdgcn_s_setprio(2);
            unsigned s44 = cnt, s48 = ldsb + X_OFF, s70 = 0;
            unsigned s72 = ldsb + KQL_OFF + w * 192, s73 = ldsb + OL_OFF, s77 = ldsb + OL_OFF, s76 = ldsb + KQL_OFF + 768 + w * 4;
            const unsigned long long wb = (unsigned long long)wf;
            const u32x4 wr = {(unsigned)wb, (unsigned)(wb >> 32) & 0xffffu, (unsigned)WS_BYTES, 0x00020000u};
            asm volatile(
            "s_setprio 2\n\t"
            "s_add_u32 s49, s33, 0\n\t"
            "buffer_load_dwordx4 v[0:3], v248, s[28:31], s49 offen sc1\n\t"
            "s_add_u32 s49, s33, 1024\n\t"
            "buffer_load_dwordx4 v[4:7], v248, s[28:31], s49 offen sc1\n\t"
            "s_add_u32 s49, s33, 2048\n\t"
            "buffer_load_dwordx4 v[8:11], v248, s[28:31], s49 offen sc1\n\t"
            "s_add_u32 s49, s33, 3072\n\t"
            "buffer_load_dwordx4 v[12:15], v248, s[28:31], s49 offen sc1\n\t"
            "s_add_u32 s49, s33, 4096\n\t"
            "buffer_load_dwordx4 v[16:19], v248, s[28:31], s49 offen sc1\n\t"
            "s_add_u32 s49, s33, 5120\n\t"
            "buffer_load_dwordx4 v[20:23], v248, s[28:31], s49 offen sc1\n\t"
            "s_add_u32 s49, s33, 6144\n\t"
            "buffer_load_dwordx4 v[24:27], v248, s[28:31], s49 offen sc1\n\t"
            "s_add_u32 s49, s33, 7168\n\t"
            "buffer_load_dwordx4 v[28:31], v248, s[28:31], s49 offen sc1\n\t"
            "s_add_u32 s49, s33, 8192\n\t"
            "buffer_load_dwordx4 v[32:35], v248, s[28:31], s49 offen sc1\n\t"
            "s_add_u32 s49, s33, 9216\n\t"
            "buffer_load_dwordx4 v[36:39], v248, s[28:31], s49 offen sc1\n\t"
            "s_add_u32 s49, s33, 10240\n\t"
            "buffer_load_dwordx4 v[40:43], v248, s[28:31], s49 offen sc1\n\t"
            "s_add_u32 s49, s33, 11264\n\t"
            "buffer_load_dwordx4 v[44:47], v248, s[28:31], s49 offen sc1\n\t"
            "s_add_u32 s49, s33, 12288\n\t"
            "buffer_load_dwordx4 v[48:51], v248, s[28:31], s49 offen sc1\n\t"
            "s_add_u32 s49, s33, 13312\n\t"
            "buffer_load_dwordx4 v[52:55], v248, s[28:31], s49 offen sc1\n\t"
            "s_add_u32 s49, s33, 14336\n\t"
            "buffer_load_dwordx4 v[56:59], v248, s[28:31], s49 offen sc1\n\t"
            "s_add_u32 s49, s33, 15360\n\t"
            "buffer_load_dwordx4 v[60:63], v248, s[28:31], s49 offen sc1\n\t"
            "v_mov_b32 v224, s46\n\t"
            ".Ls1%=:\n\t"
            "ds_read_b128 v[220:223], v224\n\t"
            "s_waitcnt lgkmcnt(0)\n\t"
            "v_and_b32 v220, v220, v221\n\t"
            "v_and_b32 v222, v222, v223\n\t"
            "v_and_b32 v220, v220, v222\n\t"
            "s_nop 0\n\t"
            "v_readfirstlane_b32 s49, v220\n\t"
            "s_nop 3\n\t"
            "s_bitcmp1_b32 s49, 27\n\t"
            "s_cbranch_scc0 .Ls1%=\n\t"
            "s_nop 7\n\t"
            "v_add_u32 v237, s35, v248\n\t"
            "v_add_u32 v238, s56, v244\n\t"
            "v_xor_b32 v239, 64, v238\n\t"
            "v_add_u32 v236, s56, v248\n\t"
            "ds_read_b128 v[200:203], v237\n\t"
            "ds_read_b128 v[204:207], v237 offset:1024\n\t"
            "ds_read_b128 v[144:147], v238\n\t"
            "ds_read_b128 v[148:151], v239\n\t"
            "ds_read_b128 v[152:155], v238 offset:128\n\t"
            "ds_read_b128 v[156:159], v239 offset:128\n\t"
            "ds_read_b128 v[160:163], v238 offset:256\n\t"
            "ds_read_b128 v[164:167], v239 offset:256\n\t"
            "ds_read_b128 v[168:171], v238 offset:6144\n\t"
            "ds_read_b128 v[172:175], v239 offset:6144\n\t"
            "ds_read_b128 v[176:179], v238 offset:6272\n\t"
            "ds_read_b128 v[180:183], v239 offset:6272\n\t"
            "ds_read_b128 v[184:187], v238 offset:6400\n\t"
            "ds_read_b128 v[188:191], v239 offset:6400\n\t"
            "ds_read_b128 v[208:211], v237 offset:2048\n\t"
            "s_waitcnt lgkmcnt(14)\n\t"
            "ds_read_b128 v[224:227], v236 offset:12288\n\t"
            "s_waitcnt lgkmcnt(14)\n\t"
            "ds_read_b128 v[228:231], v236 offset:13312\n\t"
            "s_add_u32 s49, s33, 16384\n\t"
            "buffer_load_dwordx4 v[64:67], v248, s[28:31], s49 offen sc1\n\t"
            "s_add_u32 s49, s33, 17408\n\t"
            "buffer_load_dwordx4 v[68:71], v248, s[28:31], s49 offen sc1\n\t"
            "s_waitcnt vmcnt(12)\n\t"
            "s_waitcnt lgkmcnt(14)\n\t"
            "v_mfma_f32_16x16x32_bf16 v[192:195], v[0:3], v[144:147], 0\n\t"
            "buffer_store_dwordx4 v[200:203], v248, s[28:31], s34 offen\n\t"
            "s_waitcnt lgkmcnt(8)\n\t"
            "v_mfma_f32_16x16x32_bf16 v[212:215], v[0:3], v[168:171], 0\n\t"
            "buffer_store_dwordx4 v[204:207], v248, s[28:31], s34 offen offset:1024\n\t"
            "v_mfma_f32_16x16x32_bf16 v[192:195], v[4:7], v[148:151], v[192:195]\n\t"
            "s_waitcnt lgkmcnt(2)\n\t"
            "buffer_store_dwordx4 v[208:211], v248, s[28:31], s34 offen offset:2048\n\t"
            "v_mfma_f32_16x16x32_bf16 v[212:215], v[4:7], v[172:175], v[212:215]\n\t"
            "s_add_u32 s49, s33, 18432\n\t"
            "buffer_load_dwordx4 v[72:75], v248, s[28:31], s49 offen sc1\n\t"
            "v_mfma_f32_16x16x32_bf16 v[192:195], v[8:11], v[152:155], v[192:195]\n\t"
            "s_add_u32 s49, s33, 19456\n\t"
            "buffer_load_dwordx4 v[76:79], v248, s[28:31], s49 offen sc1\n\t"
            "v_mfma_f32_16x16x32_bf16 v[212:215], v[8:11], v[176:179], v[212:215]\n\t"
            "s_add_u32 s49, s33, 20480\n\t"
            "buffer_load_dwordx4 v[80:83], v248, s[28:31], s49 offen sc1\n\t"
            "v_mfma_f32_16x16x32_bf16 v[192:195], v[12:15], v[156:159], v[192:195]\n\t"
            "s_add_u32 s49, s33, 21504\n\t"
            "buffer_load_dwordx4 v[84:87], v248, s[28:31], s49 offen sc1\n\t"
            "v_mfma_f32_16x16x32_bf16 v[212:215], v[12:15], v[180:183], v[212:215]\n\t"
            "s_add_u32 s49, s33, 22528\n\t"
            "buffer_load_dwordx4 v[88:91], v248, s[28:31], s49 offen sc1\n\t"
            "v_mfma_f32_16x16x32_bf16 v[192:195], v[16:19], v[160:163], v[192:195]\n\t"
            "s_add_u32 s49, s33, 23552\n\t"
            "buffer_load_dwordx4 v[92:95], v248, s[28:31], s49 offen sc1\n\t"
            "v_mfma_f32_16x16x32_bf16 v[212:215], v[16:19], v[184:187], v[212:215]\n\t"
            "s_waitcnt lgkmcnt(1)\n\t"
            "v_add_f32 v224, v224, v226\n\t"
            "s_waitcnt lgkmcnt(0)\n\t"
            "v_add_f32 v228, v228, v230\n\t"
            "v_mfma_f32_16x16x32_bf16 v[192:195], v[20:23], v[164:167], v[192:195]\n\t"
            "v_add_f32 v224, v224, v228\n\t"
            "v_add_f32 v225, v225, v227\n\t"
            "v_mfma_f32_16x16x32_bf16 v[212:215], v[20:23], v[188:191], v[212:215]\n\t"
            "s_waitcnt vmcnt(15)\n\t"
            "s_add_u32 s49, s33, 24576\n\t"
            "buffer_load_dwordx4 v[96:99], v248, s[28:31], s49 offen sc1\n\t"
            "v_mfma_f32_16x16x32_bf16 v[196:199], v[24:27], v[144:147], 0\n\t"
            "s_add_u32 s49, s33, 25600\n\t"
            "buffer_load_dwordx4 v[100:103], v248, s[28:31], s49 offen sc1\n\t"
            "v_mfma_f32_16x16x32_bf16 v[216:219], v[24:27], v[168:171], 0\n\t"
            "s_add_u32 s49, s33, 26624\n\t"
            "buffer_load_dwordx4 v[104:107], v248, s[28:31], s49 offen sc1\n\t"
            "v_mfma_f32_16x16x32_bf16 v[196:199], v[28:31], v[148:151], v[196:199]\n\t"
            "s_add_u32 s49, s33, 27648\n\t"
            "buffer_load_dwordx4 v[108:111], v248, s[28:31], s49 offen sc1\n\t"
            "v_mfma_f32_16x16x32_bf16 v[216:219], v[28:31], v[172:175], v[216:219]\n\t"
            "s_add_u32 s49, s33, 28672\n\t"
            "buffer_load_dwordx4 v[112:115], v248, s[28:31], s49 offen sc1\n\t"
            "v_mfma_f32_16x16x32_bf16 v[196:199], v[32:35], v[152:155], v[196:199]\n\t"
            "s_add_u32 s49, s33, 29696\n\t"
            "buffer_load_dwordx4 v[116:119], v248, s[28:31], s49 offen sc1\n\t"
            "v_mfma_f32_16x16x32_bf16 v[216:219], v[32:35], v[176:179], v[216:219]\n\t"
            "v_add_f32 v229, v229, v231\n\t"
            "v_mul_f32 v224, 0x3baaaaab, v224\n\t"
            "v_mfma_f32_16x16x32_bf16 v[196:199], v[36:39], v[156:159], v[196:199]\n\t"
            "v_add_f32 v225, v225, v229\n\t"
            "v_mul_f32_e64 v224, v224, -v224\n\t"
            "v_mfma_f32_16x16x32_bf16 v[216:219], v[36:39], v[180:183], v[216:219]\n\t"
            "v_fmac_f32 v224, 0x3baaaaab, v225\n\t"
            "v_max_f32 v224, 0, v224\n\t"
            "v_mfma_f32_16x16x32_bf16 v[196:199], v[40:43], v[160:163], v[196:199]\n\t"
            "v_add_f32 v224, 0x3727c5ac, v224\n\t"
            "v_rsq_f32 v224, v224\n\t"
            "v_mfma_f32_16x16x32_bf16 v[216:219], v[40:43], v[184:187], v[216:219]\n\t"
            "ds_write_b32 v249, v224\n\t"
            "v_mfma_f32_16x16x32_bf16 v[196:199], v[44:47], v[164:167], v[196:199]\n\t"
            "ds_read_b32 v236, v251\n\t"
            "v_mfma_f32_16x16x32_bf16 v[216:219], v[44:47], v[188:191], v[216:219]\n\t"
            "ds_read_b32 v237, v251 offset:64\n\t"
            "s_waitcnt vmcnt(15)\n\t"
            "v_mfma_f32_16x16x32_bf16 v[200:203], v[48:51], v[144:147], 0\n\t"
            "s_add_u32 s49, s33, 30720\n\t"
            "buffer_load_dwordx4 v[120:123], v248, s[28:31], s49 offen sc1\n\t"
            "v_mfma_f32_16x16x32_bf16 v[220:223], v[48:51], v[168:171], 0\n\t"
            "s_add_u32 s49, s33, 31744\n\t"
            "buffer_load_dwordx4 v[124:127], v248, s[28:31], s49 offen sc1\n\t"
            "v_mfma_f32_16x16x32_bf16 v[200:203], v[52:55], v[148:151], v[200:203]\n\t"
            "s_add_u32 s49, s33, 32768\n\t"
            "buffer_load_dwordx4 v[128:131], v248, s[28:31], s49 offen sc1\n\t"
            "v_mfma_f32_16x16x32_bf16 v[220:223], v[52:55], v[172:175], v[220:223]\n\t"
            "s_add_u32 s49, s33, 33792\n\t"
            "buffer_load_dwordx4 v[132:135], v248, s[28:31], s49 offen sc1\n\t"
            "v_mfma_f32_16x16x32_bf16 v[200:203], v[56:59], v[152:155], v[200:203]\n\t"
            "s_add_u32 s49, s33, 34816\n\t"
            "buffer_load_dwordx4 v[136:139], v248, s[28:31], s49 offen sc1\n\t"
            "v_mfma_f32_16x16x32_bf16 v[220:223], v[56:59], v[176:179], v[220:223]\n\t"
            "s_add_u32 s49, s33, 35840\n\t"
            "buffer_load_dwordx4 v[140:143], v248, s[28:31], s49 offen sc1\n\t"
            "s_setprio 0\n\t"
            "v_mfma_f32_16x16x32_bf16 v[200:203], v[60:63], v[156:159], v[200:203]\n\t"
            "s_waitcnt lgkmcnt(1)\n\t"
            "v_mul_f32 v192, v236, v192\n\t"
            "v_mul_f32 v193, v236, v193\n\t"
            "v_mfma_f32_16x16x32_bf16 v[220:223], v[60:63], v[180:183], v[220:223]\n\t"
            "s_waitcnt lgkmcnt(0)\n\t"
            "v_mul_f32 v212, v237, v212\n\t"
            "v_mul_f32 v213, v237, v213\n\t"
            "v_mfma_f32_16x16x32_bf16 v[200:203], v[64:67], v[160:163], v[200:203]\n\t"
            "v_mul_f32 v194, v236, v194\n\t"
            "v_mul_f32 v195, v236, v195\n\t"
            "v_mfma_f32_16x16x32_bf16 v[220:223], v[64:67], v[184:187], v[220:223]\n\t"
            "v_cvt_pk_bf16_f32 v192, v192, v193\n\t"
            "v_mul_f32 v214, v237, v214\n\t"
            "v_mfma_f32_16x16x32_bf16 v[200:203], v[68:71], v[164:167], v[200:203]\n\t"
            "v_mul_f32 v215, v237, v215\n\t"
            "v_cvt_pk_bf16_f32 v212, v212, v213\n\t"
            "v_mfma_f32_16x16x32_bf16 v[220:223], v[68:71], v[188:191], v[220:223]\n\t"
            "s_waitcnt vmcnt(12)\n\t"
            "v_cvt_pk_bf16_f32 v193, v194, v195\n\t"
            "v_mfma_f32_16x16x32_bf16 v[204:207], v[72:75], v[144:147], 0\n\t"
            "v_cvt_pk_bf16_f32 v213, v214, v215\n\t"
            "v_mul_f32 v196, v236, v196\n\t"
            "v_mfma_f32_16x16x32_bf16 v[224:227], v[72:75], v[168:171], 0\n\t"
            "v_mul_f32 v197, v236, v197\n\t"
            "v_mul_f32 v198, v236, v198\n\t"
            "v_mfma_f32_16x16x32_bf16 v[204:207], v[76:79], v[148:151], v[204:207]\n\t"
            "v_mul_f32 v199, v236, v199\n\t"
            "v_mul_f32 v216, v237, v216\n\t"
            "v_mfma_f32_16x16x32_bf16 v[224:227], v[76:79], v[172:175], v[224:227]\n\t"
            "v_mul_f32 v217, v237, v217\n\t"
            "v_mul_f32 v218, v237, v218\n\t"
            "v_mfma_f32_16x16x32_bf16 v[204:207], v[80:83], v[152:155], v[204:207]\n\t"
            "v_mul_f32 v219, v237, v219\n\t"
            "v_cvt_pk_bf16_f32 v194, v196, v197\n\t"
            "v_mfma_f32_16x16x32_bf16 v[224:227], v[80:83], v[176:179], v[224:227]\n\t"
            "v_cvt_pk_bf16_f32 v195, v198, v199\n\t"
            "v_cvt_pk_bf16_f32 v214, v216, v217\n\t"
            "v_mfma_f32_16x16x32_bf16 v[204:207], v[84:87], v[156:159], v[204:207]\n\t"
            "v_cvt_pk_bf16_f32 v215, v218, v219\n\t"
            "v_mul_f32 v200, v236, v200\n\t"
            "v_mfma_f32_16x16x32_bf16 v[224:227], v[84:87], v[180:183], v[224:227]\n\t"
            "v_mul_f32 v201, v236, v201\n\t"
            "v_mul_f32 v220, v237, v220\n\t"
            "v_mfma_f32_16x16x32_bf16 v[204:207], v[88:91], v[160:163], v[204:207]\n\t"
            "v_mul_f32 v221, v237, v221\n\t"
            "v_mul_f32 v202, v236, v202\n\t"
            "v_mfma_f32_16x16x32_bf16 v[224:227], v[88:91], v[184:187], v[224:227]\n\t"
            "v_mul_f32 v203, v236, v203\n\t"
            "v_cvt_pk_bf16_f32 v200, v200, v201\n\t"
            "v_mfma_f32_16x16x32_bf16 v[204:207], v[92:95], v[164:167], v[204:207]\n\t"
            "v_mul_f32 v222, v237, v222\n\t"
            "v_mul_f32 v223, v237, v223\n\t"
            "v_mfma_f32_16x16x32_bf16 v[224:227], v[92:95], v[188:191], v[224:227]\n\t"
            "s_waitcnt vmcnt(6)\n\t"
            "v_cvt_pk_bf16_f32 v220, v220, v221\n\t"
            "v_mfma_f32_16x16x32_bf16 v[196:199], v[96:99], v[144:147], 0\n\t"
            "v_cvt_pk_bf16_f32 v201, v202, v203\n\t"
            "v_cvt_pk_bf16_f32 v221, v222, v223\n\t"
            "v_mfma_f32_16x16x32_bf16 v[216:219], v[96:99], v[168:171], 0\n\t"
            "v_mul_f32 v204, v236, v204\n\t"
            "v_mul_f32 v205, v236, v205\n\t"
            "v_mfma_f32_16x16x32_bf16 v[196:199], v[100:103], v[148:151], v[196:199]\n\t"
            "v_mul_f32 v224, v237, v224\n\t"
            "v_mul_f32 v225, v237, v225\n\t"
            "v_mfma_f32_16x16x32_bf16 v[216:219], v[100:103], v[172:175], v[216:219]\n\t"
            "v_mul_f32 v206, v236, v206\n\t"
            "v_mul_f32 v207, v236, v207\n\t"
            "v_mfma_f32_16x16x32_bf16 v[196:199], v[104:107], v[152:155], v[196:199]\n\t"
            "v_cvt_pk_bf16_f32 v204, v204, v205\n\t"
            "v_mul_f32 v226, v237, v226\n\t"
            "v_mfma_f32_16x16x32_bf16 v[216:219], v[104:107], v[176:179], v[216:219]\n\t"
            "v_mul_f32 v227, v237, v227\n\t"
            "v_cvt_pk_bf16_f32 v224, v224, v225\n\t"
            "v_mfma_f32_16x16x32_bf16 v[196:199], v[108:111], v[156:159], v[196:199]\n\t"
            "v_cvt_pk_bf16_f32 v205, v206, v207\n\t"
            "v_cvt_pk_bf16_f32 v225, v226, v227\n\t"
            "v_mfma_f32_16x16x32_bf16 v[216:219], v[108:111], v[180:183], v[216:219]\n\t"
            "ds_write_b128 v232, v[192:195]\n\t"
            "v_mfma_f32_16x16x32_bf16 v[196:199], v[112:115], v[160:163], v[196:199]\n\t"
            "ds_write_b128 v233, v[212:215]\n\t"
            "v_mfma_f32_16x16x32_bf16 v[216:219], v[112:115], v[184:187], v[216:219]\n\t"
            "v_mfma_f32_16x16x32_bf16 v[196:199], v[116:119], v[164:167], v[196:199]\n\t"
            "v_mfma_f32_16x16x32_bf16 v[216:219], v[116:119], v[188:191], v[216:219]\n\t"
            "s_waitcnt vmcnt(0)\n\t"
            "v_mfma_f32_16x16x32_bf16 v[208:211], v[120:123], v[144:147], 0\n\t"
            "v_mfma_f32_16x16x32_bf16 v[228:231], v[120:123], v[168:171], 0\n\t"
            "v_mfma_f32_16x16x32_bf16 v[208:211], v[124:127], v[148:151], v[208:211]\n\t"
            "v_mfma_f32_16x16x32_bf16 v[228:231], v[124:127], v[172:175], v[228:231]\n\t"
            "s_nop 1\n\t"
            "v_mul_f32 v196, v236, v196\n\t"
            "v_mfma_f32_16x16x32_bf16 v[208:211], v[128:131], v[152:155], v[208:211]\n\t"
            "v_mul_f32 v197, v236, v197\n\t"
            "v_mul_f32 v198, v236, v198\n\t"
            "v_mfma_f32_16x16x32_bf16 v[228:231], v[128:131], v[176:179], v[228:231]\n\t"
            "v_mul_f32 v199, v236, v199\n\t"
            "v_mul_f32 v216, v237, v216\n\t"
            "v_mfma_f32_16x16x32_bf16 v[208:211], v[132:135], v[156:159], v[208:211]\n\t"
            "v_mul_f32 v217, v237, v217\n\t"
            "v_mul_f32 v218, v237, v218\n\t"
            "v_mfma_f32_16x16x32_bf16 v[228:231], v[132:135], v[180:183], v[228:231]\n\t"
            "v_mul_f32 v219, v237, v219\n\t"
            "v_cvt_pk_bf16_f32 v206, v196, v197\n\t"
            "v_mfma_f32_16x16x32_bf16 v[208:211], v[136:139], v[160:163], v[208:211]\n\t"
            "v_cvt_pk_bf16_f32 v207, v198, v199\n\t"
            "v_cvt_pk_bf16_f32 v226, v216, v217\n\t"
            "v_mfma_f32_16x16x32_bf16 v[228:231], v[136:139], v[184:187], v[228:231]\n\t"
            "v_cvt_pk_bf16_f32 v227, v218, v219\n\t"
            "v_mfma_f32_16x16x32_bf16 v[208:211], v[140:143], v[164:167], v[208:211]\n\t"
            "ds_write_b128 v232, v[204:207] offset:64\n\t"
            "v_mfma_f32_16x16x32_bf16 v[228:231], v[140:143], v[188:191], v[228:231]\n\t"
            "ds_write_b128 v233, v[224:227] offset:64\n\t"
            "s_nop 4\n\t"
            "v_mul_f32 v208, v236, v208\n\t"
            "v_mul_f32 v209, v236, v209\n\t"
            "v_mul_f32 v210, v236, v210\n\t"
            "v_mul_f32 v211, v236, v211\n\t"
            "v_cvt_pk_bf16_f32 v202, v208, v209\n\t"
            "v_cvt_pk_bf16_f32 v203, v210, v211\n\t"
            "v_mul_f32 v228, v237, v228\n\t"
            "v_mul_f32 v229, v237, v229\n\t"
            "v_mfma_f32_16x16x16_bf16 v[196:199], v[200:201], v[202:203], 0\n\t"
            "v_mul_f32 v230, v237, v230\n\t"
            "v_mul_f32 v231, v237, v231\n\t"
            "v_cvt_pk_bf16_f32 v222, v228, v229\n\t"
            "v_cvt_pk_bf16_f32 v223, v230, v231\n\t"
            "s_nop 1\n\t"
            "v_mfma_f32_16x16x16_bf16 v[216:219], v[220:221], v[222:223], 0\n\t"
            "ds_write_b128 v232, v[200:203] offset:128\n\t"
            "v_mfma_f32_16x16x32_bf16 v[196:199], v[192:195], v[204:207], v[196:199]\n\t"
            "ds_write_b128 v233, v[220:223] offset:128\n\t"
            "s_nop 2\n\t"
            "v_mfma_f32_16x16x32_bf16 v[216:219], v[212:215], v[224:227], v[216:219]\n\t"
            "s_nop 2\n\t"
            "v_cndmask_b32_e64 v196, v196, v197, s[50:51]\n\t"
            "v_cndmask_b32_e64 v196, v196, v198, s[52:53]\n\t"
            "v_cndmask_b32_e64 v196, v196, v199, s[54:55]\n\t"
            "ds_write_b32 v234, v196\n\t"
            "s_nop 0\n\t"
            "v_cndmask_b32_e64 v216, v216, v217, s[50:51]\n\t"
            "v_cndmask_b32_e64 v216, v216, v218, s[52:53]\n\t"
            "v_cndmask_b32_e64 v216, v216, v219, s[54:55]\n\t"
            "ds_write_b32 v235, v216\n\t"
            "s_waitcnt lgkmcnt(0)\n\t"
            "s_nop 7\n\t"
            "s_cmp_lg_u32 s33, 0\n\t"
            "s_cbranch_scc1 .Lar%=\n\t"
            "s_mov_b64 exec, 1\n\t"
            "v_mov_b32 v216, 1\n\t"
            "s_mov_b32 s49, 295168\n\t"
            "buffer_atomic_add v216, off, s[28:31], s49\n\t"
            "s_mov_b64 exec, -1\n\t"
            ".Lar%=:\n\t"
            "v_mov_b32 v224, s46\n\t"
            ".Ls2%=:\n\t"
            "ds_read_b128 v[220:223], v224\n\t"
            "s_waitcnt lgkmcnt(0)\n\t"
            "v_and_b32 v220, v220, v221\n\t"
            "v_and_b32 v222, v222, v223\n\t"
            "v_and_b32 v220, v220, v222\n\t"
            "s_nop 0\n\t"
            "v_readfirstlane_b32 s49, v220\n\t"
            "s_nop 3\n\t"
            "s_bitcmp1_b32 s49, 31\n\t"
            "s_cbranch_scc0 .Ls2%=\n\t"
            "s_waitcnt lgkmcnt(0)\n\t"
            "s_nop 7\n\t"
            "s_setprio 2\n\t"
            "v_add_u32 v232, s48, v244\n\t"
            "v_add_u32 v208, s48, v248\n\t"
            "v_xor_b32 v233, 64, v232\n\t"
            "v_add_u32 v234, s72, v252\n\t"
            "v_cndmask_b32_e64 v235, v246, v234, s[50:51]\n\t"
            "v_cndmask_b32_e64 v234, v246, v234, s[26:27]\n\t"
            "v_mov_b32 v239, s76\n\t"
            "ds_read_b128 v[216:219], v232\n\t"
            "ds_read_b128 v[204:207], v208 offset:18816\n\t"
            "ds_read_b128 v[208:211], v208 offset:19840\n\t"
            "ds_read_b128 v[220:223], v233\n\t"
            "ds_read_b128 v[224:227], v232 offset:128\n\t"
            "ds_read_b128 v[188:191], v233 offset:6272\n\t"
            "ds_read_b128 v[192:195], v232 offset:6400\n\t"
            "ds_read_b128 v[200:203], v233 offset:6400\n\t"
            "ds_read_b32 v239, v239\n\t"
            "s_waitcnt lgkmcnt(8)\n\t"
            "v_mfma_f32_16x16x32_bf16 v[148:151], v[72:75], v[216:219], 0\n\t"
            "s_waitcnt lgkmcnt(7)\n\t"
            "v_add_f32 v204, v204, v206\n\t"
            "v_add_f32 v205, v205, v207\n\t"
            "s_waitcnt lgkmcnt(6)\n\t"
            "v_add_f32 v208, v208, v210\n\t"
            "v_add_f32 v209, v209, v211\n\t"
            "v_add_f32 v204, v204, v208\n\t"
            "v_add_f32 v205, v205, v209\n\t"
            "v_mfma_f32_16x16x32_bf16 v[208:211], v[96:99], v[216:219], 0\n\t"
            "v_mul_f32 v204, 0x3baaaaab, v204\n\t"
            "v_mul_f32_e64 v204, v204, -v204\n\t"
            "v_mfma_f32_16x16x32_bf16 v[212:215], v[120:123], v[216:219], 0\n\t"
            "v_fmac_f32 v204, 0x3baaaaab, v205\n\t"
            "v_max_f32 v204, 0, v204\n\t"
            "v_mfma_f32_16x16x32_bf16 v[144:147], v[0:3], v[216:219], 0\n\t"
            "v_add_f32 v204, 0x3727c5ac, v204\n\t"
            "v_rsq_f32 v204, v204\n\t"
            "s_nop 0\n\t"
            "ds_write_b32 v249, v204\n\t"
            "v_mfma_f32_16x16x32_bf16 v[204:207], v[24:27], v[216:219], 0\n\t"
            "ds_read_b32 v236, v251\n\t"
            "v_mfma_f32_16x16x32_bf16 v[152:155], v[48:51], v[216:219], 0\n\t"
            "ds_read_b128 v[216:219], v233 offset:128\n\t"
            "s_waitcnt lgkmcnt(8)\n\t"
            "v_mfma_f32_16x16x32_bf16 v[148:151], v[76:79], v[220:223], v[148:151]\n\t"
            "ds_read_b32 v237, v251 offset:64\n\t"
            "v_mfma_f32_16x16x32_bf16 v[208:211], v[100:103], v[220:223], v[208:211]\n\t"
            "ds_read_b32 v238, v251 offset:128\n\t"
            "v_mfma_f32_16x16x32_bf16 v[212:215], v[124:127], v[220:223], v[212:215]\n\t"
            "v_mfma_f32_16x16x32_bf16 v[144:147], v[4:7], v[220:223], v[144:147]\n\t"
            "v_mfma_f32_16x16x32_bf16 v[204:207], v[28:31], v[220:223], v[204:207]\n\t"
            "v_mfma_f32_16x16x32_bf16 v[152:155], v[52:55], v[220:223], v[152:155]\n\t"
            "ds_read_b128 v[220:223], v232 offset:256\n\t"
            "s_waitcnt lgkmcnt(10)\n\t"
            "v_mfma_f32_16x16x32_bf16 v[144:147], v[8:11], v[224:227], v[144:147]\n\t"
            "v_mfma_f32_16x16x32_bf16 v[204:207], v[32:35], v[224:227], v[204:207]\n\t"
            "v_mfma_f32_16x16x32_bf16 v[152:155], v[56:59], v[224:227], v[152:155]\n\t"
            "v_mfma_f32_16x16x32_bf16 v[148:151], v[80:83], v[224:227], v[148:151]\n\t"
            "v_mfma_f32_16x16x32_bf16 v[208:211], v[104:107], v[224:227], v[208:211]\n\t"
            "v_mfma_f32_16x16x32_bf16 v[212:215], v[128:131], v[224:227], v[212:215]\n\t"
            "ds_read_b128 v[224:227], v233 offset:256\n\t"
            "s_waitcnt lgkmcnt(4)\n\t"
            "v_mfma_f32_16x16x32_bf16 v[148:151], v[84:87], v[216:219], v[148:151]\n\t"
            "v_mfma_f32_16x16x32_bf16 v[208:211], v[108:111], v[216:219], v[208:211]\n\t"
            "v_mfma_f32_16x16x32_bf16 v[212:215], v[132:135], v[216:219], v[212:215]\n\t"
            "v_mfma_f32_16x16x32_bf16 v[144:147], v[12:15], v[216:219], v[144:147]\n\t"
            "v_mfma_f32_16x16x32_bf16 v[204:207], v[36:39], v[216:219], v[204:207]\n\t"
            "v_mfma_f32_16x16x32_bf16 v[152:155], v[60:63], v[216:219], v[152:155]\n\t"
            "ds_read_b128 v[216:219], v232 offset:6144\n\t"
            "s_waitcnt lgkmcnt(2)\n\t"
            "v_mfma_f32_16x16x32_bf16 v[148:151], v[88:91], v[220:223], v[148:151]\n\t"
            "v_mfma_f32_16x16x32_bf16 v[208:211], v[112:115], v[220:223], v[208:211]\n\t"
            "v_mfma_f32_16x16x32_bf16 v[212:215], v[136:139], v[220:223], v[212:215]\n\t"
            "v_mfma_f32_16x16x32_bf16 v[144:147], v[16:19], v[220:223], v[144:147]\n\t"
            "v_mfma_f32_16x16x32_bf16 v[204:207], v[40:43], v[220:223], v[204:207]\n\t"
            "v_mfma_f32_16x16x32_bf16 v[152:155], v[64:67], v[220:223], v[152:155]\n\t"
            "ds_read_b128 v[220:223], v233 offset:6144\n\t"
            "s_waitcnt lgkmcnt(2)\n\t"
            "v_mfma_f32_16x16x32_bf16 v[144:147], v[20:23], v[224:227], v[144:147]\n\t"
            "v_mfma_f32_16x16x32_bf16 v[204:207], v[44:47], v[224:227], v[204:207]\n\t"
            "v_mfma_f32_16x16x32_bf16 v[152:155], v[68:71], v[224:227], v[152:155]\n\t"
            "v_mfma_f32_16x16x32_bf16 v[148:151], v[92:95], v[224:227], v[148:151]\n\t"
            "v_mfma_f32_16x16x32_bf16 v[208:211], v[116:119], v[224:227], v[208:211]\n\t"
            "v_mfma_f32_16x16x32_bf16 v[212:215], v[140:143], v[224:227], v[212:215]\n\t"
            "ds_read_b128 v[224:227], v232 offset:6272\n\t"
            "s_waitcnt lgkmcnt(2)\n\t"
            "v_mfma_f32_16x16x32_bf16 v[160:163], v[72:75], v[216:219], 0\n\t"
            "v_mul_f32 v144, v236, v144\n\t"
            "v_mul_f32 v145, v236, v145\n\t"
            "v_mul_f32 v146, v236, v146\n\t"
            "s_waitcnt lgkmcnt(1)\n\t"
            "v_mfma_f32_16x16x32_bf16 v[160:163], v[76:79], v[220:223], v[160:163]\n\t"
            "v_mul_f32 v152, v236, v152\n\t"
            "v_mul_f32 v153, v236, v153\n\t"
            "v_mul_f32 v154, v236, v154\n\t"
            "v_mul_f32 v155, v236, v155\n\t"
            "v_cvt_pk_bf16_f32 v152, v152, v153\n\t"
            "v_cvt_pk_bf16_f32 v153, v154, v155\n\t"
            "v_mul_f32 v212, v236, v212\n\t"
            "v_mul_f32 v213, v236, v213\n\t"
            "v_mul_f32 v214, v236, v214\n\t"
            "v_mul_f32 v215, v236, v215\n\t"
            "v_cvt_pk_bf16_f32 v154, v212, v213\n\t"
            "v_cvt_pk_bf16_f32 v155, v214, v215\n\t"
            "v_mul_f32 v147, v236, v147\n\t"
            "v_cvt_pk_bf16_f32 v144, v144, v145\n\t"
            "v_mul_f32 v148, v236, v148\n\t"
            "v_mul_f32 v149, v236, v149\n\t"
            "v_cvt_pk_bf16_f32 v145, v146, v147\n\t"
            "v_mul_f32 v204, v236, v204\n\t"
            "v_mul_f32 v205, v236, v205\n\t"
            "v_mul_f32 v206, v236, v206\n\t"
            "v_mul_f32 v207, v236, v207\n\t"
            "v_mul_f32 v150, v236, v150\n\t"
            "s_waitcnt lgkmcnt(0)\n\t"
            "v_mfma_f32_16x16x32_bf16 v[160:163], v[80:83], v[224:227], v[160:163]\n\t"
            "ds_read_b128 v[212:215], v232 offset:12416\n\t"
            "v_mul_f32 v151, v236, v151\n\t"
            "v_mfma_f32_16x16x32_bf16 v[160:163], v[84:87], v[188:191], v[160:163]\n\t"
            "v_cvt_pk_bf16_f32 v148, v148, v149\n\t"
            "v_cvt_pk_bf16_f32 v146, v204, v205\n\t"
            "v_mfma_f32_16x16x32_bf16 v[160:163], v[88:91], v[192:195], v[160:163]\n\t"
            "v_cvt_pk_bf16_f32 v147, v206, v207\n\t"
            "v_cvt_pk_bf16_f32 v149, v150, v151\n\t"
            "v_mfma_f32_16x16x32_bf16 v[160:163], v[92:95], v[200:203], v[160:163]\n\t"
            "ds_read_b128 v[204:207], v232 offset:12288\n\t"
            "v_mul_f32 v208, v236, v208\n\t"
            "v_mfma_f32_16x16x16_bf16 v[180:183], v[152:153], v[154:155], 0\n\t"
            "v_mul_f32 v209, v236, v209\n\t"
            "v_mul_f32 v210, v236, v210\n\t"
            "v_mul_f32 v211, v236, v211\n\t"
            "v_mfma_f32_16x16x32_bf16 v[196:199], v[24:27], v[216:219], 0\n\t"
            "v_cvt_pk_bf16_f32 v150, v208, v209\n\t"
            "v_cvt_pk_bf16_f32 v151, v210, v211\n\t"
            "v_mfma_f32_16x16x32_bf16 v[196:199], v[28:31], v[220:223], v[196:199]\n\t"
            "ds_read_b128 v[208:211], v233 offset:12288\n\t"
            "v_mul_f32 v160, v237, v160\n\t"
            "v_mfma_f32_16x16x32_bf16 v[196:199], v[32:35], v[224:227], v[196:199]\n\t"
            "v_mul_f32 v161, v237, v161\n\t"
            "v_mul_f32 v162, v237, v162\n\t"
            "v_mfma_f32_16x16x32_bf16 v[196:199], v[36:39], v[188:191], v[196:199]\n\t"
            "v_mul_f32 v163, v237, v163\n\t"
            "v_cvt_pk_bf16_f32 v160, v160, v161\n\t"
            "v_mfma_f32_16x16x32_bf16 v[196:199], v[40:43], v[192:195], v[196:199]\n\t"
            "v_cvt_pk_bf16_f32 v161, v162, v163\n\t"
            "v_mfma_f32_16x16x32_bf16 v[196:199], v[44:47], v[200:203], v[196:199]\n\t"
            "v_mfma_f32_16x16x32_bf16 v[156:159], v[0:3], v[216:219], 0\n\t"
            "v_mfma_f32_16x16x32_bf16 v[156:159], v[4:7], v[220:223], v[156:159]\n\t"
            "v_mfma_f32_16x16x32_bf16 v[156:159], v[8:11], v[224:227], v[156:159]\n\t"
            "v_mfma_f32_16x16x32_bf16 v[156:159], v[12:15], v[188:191], v[156:159]\n\t"
            "v_mfma_f32_16x16x32_bf16 v[156:159], v[16:19], v[192:195], v[156:159]\n\t"
            "v_mfma_f32_16x16x32_bf16 v[156:159], v[20:23], v[200:203], v[156:159]\n\t"
            "s_nop 1\n\t"
            "v_mul_f32 v196, v237, v196\n\t"
            "v_mfma_f32_16x16x32_bf16 v[164:167], v[48:51], v[216:219], 0\n\t"
            "v_mul_f32 v197, v237, v197\n\t"
            "v_mul_f32 v198, v237, v198\n\t"
            "v_mfma_f32_16x16x32_bf16 v[164:167], v[52:55], v[220:223], v[164:167]\n\t"
            "v_mul_f32 v199, v237, v199\n\t"
            "v_mfma_f32_16x16x32_bf16 v[164:167], v[56:59], v[224:227], v[164:167]\n\t"
            "v_mul_f32 v156, v237, v156\n\t"
            "v_mul_f32 v157, v237, v157\n\t"
            "v_mfma_f32_16x16x32_bf16 v[164:167], v[60:63], v[188:191], v[164:167]\n\t"
            "v_mul_f32 v158, v237, v158\n\t"
            "v_mul_f32 v159, v237, v159\n\t"
            "v_mfma_f32_16x16x32_bf16 v[164:167], v[64:67], v[192:195], v[164:167]\n\t"
            "v_cvt_pk_bf16_f32 v156, v156, v157\n\t"
            "v_cvt_pk_bf16_f32 v157, v158, v159\n\t"
            "v_mfma_f32_16x16x32_bf16 v[164:167], v[68:71], v[200:203], v[164:167]\n\t"
            "v_cvt_pk_bf16_f32 v158, v196, v197\n\t"
            "v_cvt_pk_bf16_f32 v159, v198, v199\n\t"
            "v_mfma_f32_16x16x32_bf16 v[228:231], v[96:99], v[216:219], 0\n\t"
            "v_mfma_f32_16x16x32_bf16 v[228:231], v[100:103], v[220:223], v[228:231]\n\t"
            "v_mfma_f32_16x16x32_bf16 v[228:231], v[104:107], v[224:227], v[228:231]\n\t"
            "v_mfma_f32_16x16x32_bf16 v[228:231], v[108:111], v[188:191], v[228:231]\n\t"
            "s_nop 1\n\t"
            "v_mul_f32 v164, v237, v164\n\t"
            "v_mfma_f32_16x16x32_bf16 v[228:231], v[112:115], v[192:195], v[228:231]\n\t"
            "v_mul_f32 v165, v237, v165\n\t"
            "v_mul_f32 v166, v237, v166\n\t"
            "v_mfma_f32_16x16x32_bf16 v[228:231], v[116:119], v[200:203], v[228:231]\n\t"
            "v_mul_f32 v167, v237, v167\n\t"
            "v_cvt_pk_bf16_f32 v164, v164, v165\n\t"
            "v_mfma_f32_16x16x32_bf16 v[180:183], v[144:147], v[148:151], v[180:183]\n\t"
            "v_cvt_pk_bf16_f32 v165, v166, v167\n\t"
            "v_mfma_f32_16x16x32_bf16 v[196:199], v[120:123], v[216:219], 0\n\t"
            "ds_read_b128 v[216:219], v233 offset:12416\n\t"
            "v_mfma_f32_16x16x32_bf16 v[196:199], v[124:127], v[220:223], v[196:199]\n\t"
            "ds_read_b128 v[220:223], v232 offset:12544\n\t"
            "v_mul_f32 v228, v237, v228\n\t"
            "v_mfma_f32_16x16x32_bf16 v[196:199], v[128:131], v[224:227], v[196:199]\n\t"
            "ds_read_b128 v[224:227], v233 offset:12544\n\t"
            "v_mul_f32 v229, v237, v229\n\t"
            "v_mfma_f32_16x16x32_bf16 v[196:199], v[132:135], v[188:191], v[196:199]\n\t"
            "v_mul_f32 v230, v237, v230\n\t"
            "v_mul_f32 v231, v237, v231\n\t"
            "v_mfma_f32_16x16x32_bf16 v[196:199], v[136:139], v[192:195], v[196:199]\n\t"
            "v_cvt_pk_bf16_f32 v162, v228, v229\n\t"
            "v_cvt_pk_bf16_f32 v163, v230, v231\n\t"
            "v_mfma_f32_16x16x32_bf16 v[196:199], v[140:143], v[200:203], v[196:199]\n\t"
            "v_exp_f32 v180, v180\n\t"
            "s_waitcnt lgkmcnt(4)\n\t"
            "v_mfma_f32_16x16x32_bf16 v[176:179], v[48:51], v[204:207], 0\n\t"
            "v_exp_f32 v181, v181\n\t"
            "s_waitcnt lgkmcnt(3)\n\t"
            "v_mfma_f32_16x16x32_bf16 v[176:179], v[52:55], v[208:211], v[176:179]\n\t"
            "v_exp_f32 v182, v182\n\t"
            "v_mfma_f32_16x16x32_bf16 v[176:179], v[56:59], v[212:215], v[176:179]\n\t"
            "v_exp_f32 v183, v183\n\t"
            "s_waitcnt lgkmcnt(2)\n\t"
            "v_mfma_f32_16x16x32_bf16 v[176:179], v[60:63], v[216:219], v[176:179]\n\t"
            "v_mul_f32 v196, v237, v196\n\t"
            "v_mul_f32 v197, v237, v197\n\t"
            "s_waitcnt lgkmcnt(1)\n\t"
            "v_mfma_f32_16x16x32_bf16 v[176:179], v[64:67], v[220:223], v[176:179]\n\t"
            "v_mul_f32 v198, v237, v198\n\t"
            "v_mul_f32 v199, v237, v199\n\t"
            "s_waitcnt lgkmcnt(0)\n\t"
            "v_mfma_f32_16x16x32_bf16 v[176:179], v[68:71], v[224:227], v[176:179]\n\t"
            "v_cvt_pk_bf16_f32 v166, v196, v197\n\t"
            "v_cvt_pk_bf16_f32 v167, v198, v199\n\t"
            "v_cvt_pk_bf16_f32 v180, v180, v181\n\t"
            "v_cvt_pk_bf16_f32 v181, v182, v183\n\t"
            "v_mfma_f32_16x16x16_bf16 v[188:191], v[152:153], v[166:167], 0\n\t"
            "v_mfma_f32_16x16x32_bf16 v[168:171], v[0:3], v[204:207], 0\n\t"
            "s_nop 1\n\t"
            "v_mul_f32 v176, v238, v176\n\t"
            "v_mfma_f32_16x16x32_bf16 v[168:171], v[4:7], v[208:211], v[168:171]\n\t"
            "v_mul_f32 v177, v238, v177\n\t"
            "v_mul_f32 v178, v238, v178\n\t"
            "v_mfma_f32_16x16x32_bf16 v[168:171], v[8:11], v[212:215], v[168:171]\n\t"
            "v_mul_f32 v179, v238, v179\n\t"
            "v_cvt_pk_bf16_f32 v176, v176, v177\n\t"
            "v_mfma_f32_16x16x32_bf16 v[168:171], v[12:15], v[216:219], v[168:171]\n\t"
            "v_cvt_pk_bf16_f32 v177, v178, v179\n\t"
            "v_mfma_f32_16x16x32_bf16 v[168:171], v[16:19], v[220:223], v[168:171]\n\t"
            "v_mfma_f32_16x16x32_bf16 v[168:171], v[20:23], v[224:227], v[168:171]\n\t"
            "v_mfma_f32_16x16x16_bf16 v[192:195], v[164:165], v[166:167], 0\n\t"
            "v_mfma_f32_16x16x32_bf16 v[188:191], v[144:147], v[160:163], v[188:191]\n\t"
            "v_mfma_f32_16x16x16_bf16 v[184:187], v[164:165], v[154:155], 0\n\t"
            "s_nop 3\n\t"
            "v_mfma_f32_16x16x32_bf16 v[192:195], v[156:159], v[160:163], v[192:195]\n\t"
            "v_mfma_f32_16x16x32_bf16 v[172:175], v[72:75], v[204:207], 0\n\t"
            "v_mfma_f32_16x16x32_bf16 v[172:175], v[76:79], v[208:211], v[172:175]\n\t"
            "v_mul_f32 v168, v238, v168\n\t"
            "v_mfma_f32_16x16x32_bf16 v[172:175], v[80:83], v[212:215], v[172:175]\n\t"
            "v_mul_f32 v169, v238, v169\n\t"
            "v_mul_f32 v170, v238, v170\n\t"
            "v_mfma_f32_16x16x32_bf16 v[172:175], v[84:87], v[216:219], v[172:175]\n\t"
            "v_exp_f32 v188, v188\n\t"
            "v_mfma_f32_16x16x32_bf16 v[172:175], v[88:91], v[220:223], v[172:175]\n\t"
            "v_exp_f32 v189, v189\n\t"
            "v_mfma_f32_16x16x32_bf16 v[172:175], v[92:95], v[224:227], v[172:175]\n\t"
            "v_exp_f32 v190, v190\n\t"
            "v_mfma_f32_16x16x32_bf16 v[184:187], v[156:159], v[148:151], v[184:187]\n\t"
            "v_exp_f32 v191, v191\n\t"
            "v_mfma_f32_16x16x32_bf16 v[200:203], v[120:123], v[204:207], 0\n\t"
            "v_exp_f32 v192, v192\n\t"
            "v_mfma_f32_16x16x32_bf16 v[200:203], v[124:127], v[208:211], v[200:203]\n\t"
            "v_exp_f32 v193, v193\n\t"
            "v_mfma_f32_16x16x32_bf16 v[200:203], v[128:131], v[212:215], v[200:203]\n\t"
            "v_exp_f32 v194, v194\n\t"
            "v_mfma_f32_16x16x32_bf16 v[200:203], v[132:135], v[216:219], v[200:203]\n\t"
            "v_exp_f32 v184, v184\n\t"
            "v_mfma_f32_16x16x32_bf16 v[200:203], v[136:139], v[220:223], v[200:203]\n\t"
            "v_exp_f32 v185, v185\n\t"
            "v_mfma_f32_16x16x32_bf16 v[200:203], v[140:143], v[224:227], v[200:203]\n\t"
            "v_exp_f32 v186, v186\n\t"
            "v_exp_f32 v187, v187\n\t"
            "v_cvt_pk_bf16_f32 v182, v184, v185\n\t"
            "v_cvt_pk_bf16_f32 v183, v186, v187\n\t"
            "v_mfma_f32_16x16x16_bf16 v[184:187], v[176:177], v[154:155], 0\n\t"
            "v_exp_f32 v195, v195\n\t"
            "v_cvt_pk_bf16_f32 v188, v188, v189\n\t"
            "v_mfma_f32_16x16x32_bf16 v[196:199], v[24:27], v[204:207], 0\n\t"
            "v_cvt_pk_bf16_f32 v189, v190, v191\n\t"
            "v_cvt_pk_bf16_f32 v190, v192, v193\n\t"
            "v_mfma_f32_16x16x32_bf16 v[196:199], v[28:31], v[208:211], v[196:199]\n\t"
            "v_cvt_pk_bf16_f32 v191, v194, v195\n\t"
            "v_mul_f32 v171, v238, v171\n\t"
            "v_mfma_f32_16x16x32_bf16 v[196:199], v[32:35], v[212:215], v[196:199]\n\t"
            "v_cvt_pk_bf16_f32 v168, v168, v169\n\t"
            "v_cvt_pk_bf16_f32 v169, v170, v171\n\t"
            "v_mfma_f32_16x16x32_bf16 v[196:199], v[36:39], v[216:219], v[196:199]\n\t"
            "v_mul_f32 v172, v238, v172\n\t"
            "v_mul_f32 v173, v238, v173\n\t"
            "v_mfma_f32_16x16x32_bf16 v[196:199], v[40:43], v[220:223], v[196:199]\n\t"
            "v_mul_f32 v200, v238, v200\n\t"
            "v_mul_f32 v201, v238, v201\n\t"
            "v_mfma_f32_16x16x32_bf16 v[196:199], v[44:47], v[224:227], v[196:199]\n\t"
            "v_mul_f32 v202, v238, v202\n\t"
            "v_mul_f32 v203, v238, v203\n\t"
            "v_mfma_f32_16x16x32_bf16 v[228:231], v[96:99], v[204:207], 0\n\t"
            "ds_read_b64 v[204:205], v235 offset:136\n\t"
            "v_mul_f32 v174, v238, v174\n\t"
            "v_mfma_f32_16x16x32_bf16 v[228:231], v[100:103], v[208:211], v[228:231]\n\t"
            "ds_read_b64 v[206:207], v234 offset:128\n\t"
            "v_mul_f32 v175, v238, v175\n\t"
            "v_mfma_f32_16x16x32_bf16 v[228:231], v[104:107], v[212:215], v[228:231]\n\t"
            "ds_read_b128 v[208:211], v234\n\t"
            "v_mul_f32 v196, v238, v196\n\t"
            "v_mfma_f32_16x16x32_bf16 v[228:231], v[108:111], v[216:219], v[228:231]\n\t"
            "ds_read_b128 v[212:215], v235 offset:64\n\t"
            "v_mul_f32 v197, v238, v197\n\t"
            "v_mfma_f32_16x16x32_bf16 v[228:231], v[112:115], v[220:223], v[228:231]\n\t"
            "v_mul_f32 v198, v238, v198\n\t"
            "v_mul_f32 v199, v238, v199\n\t"
            "v_mfma_f32_16x16x32_bf16 v[228:231], v[116:119], v[224:227], v[228:231]\n\t"
            "v_cvt_pk_bf16_f32 v170, v196, v197\n\t"
            "v_cvt_pk_bf16_f32 v171, v198, v199\n\t"
            "v_mfma_f32_16x16x16_bf16 v[192:195], v[176:177], v[166:167], 0\n\t"
            "v_cvt_pk_bf16_f32 v172, v172, v173\n\t"
            "v_cvt_pk_bf16_f32 v178, v200, v201\n\t"
            "v_cvt_pk_bf16_f32 v179, v202, v203\n\t"
            "v_mfma_f32_16x16x32_bf16 v[184:187], v[168:171], v[148:151], v[184:187]\n\t"
            "v_cvt_pk_bf16_f32 v173, v174, v175\n\t"
            "v_mul_f32 v228, v238, v228\n\t"
            "v_mfma_f32_16x16x32_bf16 v[192:195], v[168:171], v[160:163], v[192:195]\n\t"
            "v_mul_f32 v229, v238, v229\n\t"
            "v_mul_f32 v230, v238, v230\n\t"
            "v_mul_f32 v231, v238, v231\n\t"
            "s_waitcnt lgkmcnt(2)\n\t"
            "v_mfma_f32_16x16x32_bf16 v[216:219], v[204:207], v[152:155], 0\n\t"
            "v_cvt_pk_bf16_f32 v174, v228, v229\n\t"
            "v_cvt_pk_bf16_f32 v175, v230, v231\n\t"
            "s_waitcnt lgkmcnt(1)\n\t"
            "v_mfma_f32_16x16x32_bf16 v[216:219], v[208:211], v[148:151], v[216:219]\n\t"
            "v_exp_f32 v184, v184\n\t"
            "s_waitcnt lgkmcnt(0)\n\t"
            "v_mfma_f32_16x16x32_bf16 v[216:219], v[212:215], v[144:147], v[216:219]\n\t"
            "v_exp_f32 v185, v185\n\t"
            "v_mfma_f32_16x16x32_bf16 v[220:223], v[204:207], v[164:167], 0\n\t"
            "v_exp_f32 v186, v186\n\t"
            "v_mfma_f32_16x16x32_bf16 v[220:223], v[208:211], v[160:163], v[220:223]\n\t"
            "v_exp_f32 v187, v187\n\t"
            "v_mfma_f32_16x16x32_bf16 v[220:223], v[212:215], v[156:159], v[220:223]\n\t"
            "v_cvt_pk_bf16_f32 v184, v184, v185\n\t"
            "v_cvt_pk_bf16_f32 v185, v186, v187\n\t"
            "v_mfma_f32_16x16x16_bf16 v[228:231], v[164:165], v[178:179], 0\n\t"
            "v_exp_f32 v216, v216\n\t"
            "v_mov_b32 v187, 0\n\t"
            "v_mfma_f32_16x16x16_bf16 v[200:203], v[176:177], v[178:179], 0\n\t"
            "v_cvt_pk_bf16_f32 v216, v216, 0\n\t"
            "v_cndmask_b32_e64 v186, 0, v216, s[58:59]\n\t"
            "v_exp_f32 v192, v192\n\t"
            "v_mfma_f32_16x16x32_bf16 v[224:227], v[204:207], v[176:179], 0\n\t"
            "v_exp_f32 v193, v193\n\t"
            "v_mfma_f32_16x16x32_bf16 v[224:227], v[208:211], v[172:175], v[224:227]\n\t"
            "v_exp_f32 v194, v194\n\t"
            "v_mfma_f32_16x16x32_bf16 v[224:227], v[212:215], v[168:171], v[224:227]\n\t"
            "v_exp_f32 v195, v195\n\t"
            "v_mfma_f32_16x16x16_bf16 v[196:199], v[152:153], v[178:179], 0\n\t"
            "v_exp_f32 v220, v220\n\t"
            "v_cvt_pk_bf16_f32 v192, v192, v193\n\t"
            "v_mfma_f32_16x16x32_bf16 v[204:207], v[240:243], v[180:183], 0\n\t"
            "v_cvt_pk_bf16_f32 v193, v194, v195\n\t"
            "v_cvt_pk_bf16_f32 v220, v220, 0\n\t"
            "v_mfma_f32_16x16x32_bf16 v[204:207], v[240:243], v[184:187], v[204:207]\n\t"
            "v_cndmask_b32_e64 v194, 0, v220, s[58:59]\n\t"
            "v_mov_b32 v195, 0\n\t"
            "v_mfma_f32_16x16x32_bf16 v[200:203], v[168:171], v[172:175], v[200:203]\n\t"
            "v_cndmask_b32_e64 v235, v217, v221, s[60:61]\n\t"
            "v_cndmask_b32_e64 v235, v235, v225, s[62:63]\n\t"
            "v_mfma_f32_16x16x32_bf16 v[208:211], v[240:243], v[188:191], 0\n\t"
            "v_cndmask_b32_e64 v235, v235, v239, s[64:65]\n\t"
            "v_cmp_nlt_f32_e64 s[24:25], |v235|, s23\n\t"
            "v_mfma_f32_16x16x32_bf16 v[208:211], v[240:243], v[192:195], v[208:211]\n\t"
            "v_exp_f32 v235, v235\n\t"
            "v_mfma_f32_16x16x32_bf16 v[196:199], v[144:147], v[172:175], v[196:199]\n\t"
            "v_exp_f32 v200, v200\n\t"
            "v_mfma_f32_16x16x32_bf16 v[228:231], v[156:159], v[172:175], v[228:231]\n\t"
            "v_exp_f32 v201, v201\n\t"
            "v_exp_f32 v202, v202\n\t"
            "v_exp_f32 v203, v203\n\t"
            "v_exp_f32 v224, v224\n\t"
            "v_cvt_pk_bf16_f32 v235, v235, 0\n\t"
            "v_cvt_pk_bf16_f32 v200, v200, v201\n\t"
            "v_exp_f32 v196, v196\n\t"
            "v_exp_f32 v197, v197\n\t"
            "v_exp_f32 v198, v198\n\t"
            "v_exp_f32 v199, v199\n\t"
            "v_exp_f32 v228, v228\n\t"
            "v_exp_f32 v229, v229\n\t"
            "v_exp_f32 v230, v230\n\t"
            "v_exp_f32 v231, v231\n\t"
            "v_cvt_pk_bf16_f32 v196, v196, v197\n\t"
            "v_cvt_pk_bf16_f32 v197, v198, v199\n\t"
            "v_cvt_pk_bf16_f32 v198, v228, v229\n\t"
            "v_cvt_pk_bf16_f32 v199, v230, v231\n\t"
            "v_cndmask_b32_e64 v235, 0, v235, s[66:67]\n\t"
            "v_cvt_pk_bf16_f32 v201, v202, v203\n\t"
            "v_mfma_f32_16x16x32_bf16 v[212:215], v[240:243], v[196:199], 0\n\t"
            "v_cvt_pk_bf16_f32 v224, v224, 0\n\t"
            "v_cndmask_b32_e64 v202, 0, v224, s[58:59]\n\t"
            "v_mov_b32 v203, 0\n\t"
            "v_add_u32 v205, 0xe0800000, v204\n\t"
            "v_add_u32 v209, 0xe0800000, v208\n\t"
            "v_mfma_f32_16x16x32_bf16 v[212:215], v[240:243], v[200:203], v[212:215]\n\t"
            "ds_write_b16 v250, v235 offset:256\n\t"
            "v_rcp_f32 v236, v204\n\t"
            "v_rcp_f32 v237, v208\n\t"
            "s_nop 4\n\t"
            "v_add_u32 v213, 0xe0800000, v212\n\t"
            "v_rcp_f32 v238, v212\n\t"
            "v_or3_b32 v205, v205, v209, v213\n\t"
            "v_cmp_le_u32 vcc, 2.0, v205\n\t"
            "s_waitcnt lgkmcnt(0)\n\t"
            "s_nop 7\n\t"
            "s_branch .Lwa_in%=\n\t"
            ".p2align 6\n\t"
            ".Lwa_loop%=:\n\t"
            "ds_read_b64 v[156:157], v247\n\t"
            "v_add_u32 v232, s48, v244\n\t"
            "v_add_u32 v208, s48, v248\n\t"
            "v_add_u32 v235, s45, v248\n\t"
            "v_xor_b32 v234, s73, v254\n\t"
            "v_xor_b32 v233, 64, v232\n\t"
            "v_mov_b32 v159, 0\n\t"
            "v_mov_b32 v167, 0\n\t"
            "v_mov_b32 v163, 0\n\t"
            "v_mov_b32 v239, s76\n\t"
            "ds_read_b128 v[216:219], v232\n\t"
            "ds_read_b128 v[204:207], v208 offset:18816\n\t"
            "ds_read_b128 v[208:211], v208 offset:19840\n\t"
            "ds_read_b128 v[168:171], v235\n\t"
            "ds_read_u16 v158, v234\n\t"
            "ds_read_b128 v[220:223], v233\n\t"
            "ds_read_b128 v[172:175], v235 offset:1536\n\t"
            "ds_read_b64 v[164:165], v247 offset:1536\n\t"
            "ds_read_u16 v166, v234 offset:128\n\t"
            "ds_read_b128 v[176:179], v235 offset:3072\n\t"
            "ds_read_b64 v[160:161], v247 offset:3072\n\t"
            "ds_read_u16 v162, v234 offset:256\n\t"
            "v_add_u32 v234, s72, v252\n\t"
            "v_cndmask_b32_e64 v235, v246, v234, s[50:51]\n\t"
            "v_cndmask_b32_e64 v234, v246, v234, s[26:27]\n\t"
            "ds_read_b128 v[224:227], v232 offset:128\n\t"
            "s_waitcnt lgkmcnt(14)\n\t"
            "ds_read_b32 v239, v239\n\t"
            "s_waitcnt lgkmcnt(13)\n\t"
            "v_mfma_f32_16x16x32_bf16 v[148:151], v[72:75], v[216:219], 0\n\t"
            "s_waitcnt lgkmcnt(12)\n\t"
            "v_add_f32 v204, v204, v206\n\t"
            "v_add_f32 v205, v205, v207\n\t"
            "s_waitcnt lgkmcnt(11)\n\t"
            "v_add_f32 v208, v208, v210\n\t"
            "v_add_f32 v209, v209, v211\n\t"
            "v_add_f32 v204, v204, v208\n\t"
            "v_add_f32 v205, v205, v209\n\t"
            "v_mfma_f32_16x16x32_bf16 v[208:211], v[96:99], v[216:219], 0\n\t"
            "v_mul_f32 v204, 0x3baaaaab, v204\n\t"
            "v_mul_f32_e64 v204, v204, -v204\n\t"
            "v_mfma_f32_16x16x32_bf16 v[212:215], v[120:123], v[216:219], 0\n\t"
            "v_fmac_f32 v204, 0x3baaaaab, v205\n\t"
            "v_max_f32 v204, 0, v204\n\t"
            "s_waitcnt lgkmcnt(10)\n\t"
            "v_mfma_f32_16x16x32_bf16 v[228:231], v[168:171], v[180:183], 0\n\t"
            "v_add_f32 v204, 0x3727c5ac, v204\n\t"
            "v_rsq_f32 v204, v204\n\t"
            "s_waitcnt lgkmcnt(9)\n\t"
            "v_mfma_f32_16x16x32_bf16 v[228:231], v[156:159], v[184:187], v[228:231]\n\t"
            "ds_write_b32 v249, v204\n\t"
            "v_mfma_f32_16x16x32_bf16 v[144:147], v[0:3], v[216:219], 0\n\t"
            "v_mfma_f32_16x16x32_bf16 v[204:207], v[24:27], v[216:219], 0\n\t"
            "v_mfma_f32_16x16x32_bf16 v[152:155], v[48:51], v[216:219], 0\n\t"
            "ds_read_b128 v[216:219], v233 offset:128\n\t"
            "s_waitcnt lgkmcnt(10)\n\t"
            "v_mfma_f32_16x16x32_bf16 v[148:151], v[76:79], v[220:223], v[148:151]\n\t"
            "s_nop 0\n\t"
            "v_mul_f32 v228, v236, v228\n\t"
            "v_mfma_f32_16x16x32_bf16 v[208:211], v[100:103], v[220:223], v[208:211]\n\t"
            "v_mul_f32 v229, v236, v229\n\t"
            "v_mul_f32 v230, v236, v230\n\t"
            "v_mfma_f32_16x16x32_bf16 v[212:215], v[124:127], v[220:223], v[212:215]\n\t"
            "v_mul_f32 v231, v236, v231\n\t"
            "v_cvt_pk_bf16_f32 v228, v228, v229\n\t"
            "v_mfma_f32_16x16x32_bf16 v[144:147], v[4:7], v[220:223], v[144:147]\n\t"
            "v_cvt_pk_bf16_f32 v229, v230, v231\n\t"
            "v_mfma_f32_16x16x32_bf16 v[204:207], v[28:31], v[220:223], v[204:207]\n\t"
            "ds_write_b64 v245, v[228:229]\n\t"
            "v_mfma_f32_16x16x32_bf16 v[152:155], v[52:55], v[220:223], v[152:155]\n\t"
            "ds_read_b128 v[220:223], v232 offset:256\n\t"
            "s_waitcnt lgkmcnt(11)\n\t"
            "v_mfma_f32_16x16x32_bf16 v[228:231], v[172:175], v[180:183], 0\n\t"
            "s_waitcnt lgkmcnt(9)\n\t"
            "v_mfma_f32_16x16x32_bf16 v[228:231], v[164:167], v[184:187], v[228:231]\n\t"
            "s_waitcnt lgkmcnt(8)\n\t"
            "v_mfma_f32_16x16x32_bf16 v[180:183], v[176:179], v[180:183], 0\n\t"
            "s_waitcnt lgkmcnt(6)\n\t"
            "v_mfma_f32_16x16x32_bf16 v[180:183], v[160:163], v[184:187], v[180:183]\n\t"
            "s_waitcnt lgkmcnt(5)\n\t"
            "v_mfma_f32_16x16x32_bf16 v[144:147], v[8:11], v[224:227], v[144:147]\n\t"
            "v_mfma_f32_16x16x32_bf16 v[204:207], v[32:35], v[224:227], v[204:207]\n\t"
            "v_mfma_f32_16x16x32_bf16 v[152:155], v[56:59], v[224:227], v[152:155]\n\t"
            "v_mfma_f32_16x16x32_bf16 v[148:151], v[80:83], v[224:227], v[148:151]\n\t"
            "v_mul_f32 v228, v236, v228\n\t"
            "v_mfma_f32_16x16x32_bf16 v[208:211], v[104:107], v[224:227], v[208:211]\n\t"
            "v_mul_f32 v229, v236, v229\n\t"
            "v_mul_f32 v230, v236, v230\n\t"
            "v_mfma_f32_16x16x32_bf16 v[212:215], v[128:131], v[224:227], v[212:215]\n\t"
            "ds_read_b128 v[224:227], v233 offset:256\n\t"
            "v_mul_f32 v231, v236, v231\n\t"
            "v_mfma_f32_16x16x32_bf16 v[184:187], v[168:171], v[188:191], 0\n\t"
            "v_cvt_pk_bf16_f32 v228, v228, v229\n\t"
            "v_cvt_pk_bf16_f32 v229, v230, v231\n\t"
            "v_mfma_f32_16x16x32_bf16 v[184:187], v[156:159], v[192:195], v[184:187]\n\t"
            "ds_write_b64 v245, v[228:229] offset:128\n\t"
            "v_mfma_f32_16x16x32_bf16 v[228:231], v[172:175], v[188:191], 0\n\t"
            "v_mul_f32 v180, v236, v180\n\t"
            "v_mul_f32 v181, v236, v181\n\t"
            "v_mfma_f32_16x16x32_bf16 v[228:231], v[164:167], v[192:195], v[228:231]\n\t"
            "v_mul_f32 v182, v236, v182\n\t"
            "v_mul_f32 v183, v236, v183\n\t"
            "s_waitcnt lgkmcnt(4)\n\t"
            "v_mfma_f32_16x16x32_bf16 v[148:151], v[84:87], v[216:219], v[148:151]\n\t"
            "ds_read_b32 v236, v251\n\t"
            "v_cvt_pk_bf16_f32 v180, v180, v181\n\t"
            "v_mfma_f32_16x16x32_bf16 v[208:211], v[108:111], v[216:219], v[208:211]\n\t"
            "v_cvt_pk_bf16_f32 v181, v182, v183\n\t"
            "v_mul_f32 v184, v237, v184\n\t"
            "v_mfma_f32_16x16x32_bf16 v[212:215], v[132:135], v[216:219], v[212:215]\n\t"
            "ds_write_b64 v245, v[180:181] offset:256\n\t"
            "v_mfma_f32_16x16x32_bf16 v[144:147], v[12:15], v[216:219], v[144:147]\n\t"
            "v_mul_f32 v185, v237, v185\n\t"
            "v_mul_f32 v186, v237, v186\n\t"
            "v_mfma_f32_16x16x32_bf16 v[204:207], v[36:39], v[216:219], v[204:207]\n\t"
            "v_mul_f32 v187, v237, v187\n\t"
            "v_cvt_pk_bf16_f32 v184, v184, v185\n\t"
            "v_mfma_f32_16x16x32_bf16 v[152:155], v[60:63], v[216:219], v[152:155]\n\t"
            "ds_read_b128 v[216:219], v232 offset:6144\n\t"
            "v_cvt_pk_bf16_f32 v185, v186, v187\n\t"
            "v_mfma_f32_16x16x32_bf16 v[180:183], v[168:171], v[196:199], 0\n\t"
            "ds_read_b64 v[186:187], v253 offset:352\n\t"
            "v_mul_f32 v228, v237, v228\n\t"
            "v_mfma_f32_16x16x32_bf16 v[180:183], v[156:159], v[200:203], v[180:183]\n\t"
            "ds_write_b64 v245, v[184:185] offset:6144\n\t"
            "s_waitcnt lgkmcnt(7)\n\t"
            "v_mfma_f32_16x16x32_bf16 v[148:151], v[88:91], v[220:223], v[148:151]\n\t"
            "ds_read_b64 v[184:185], v253 offset:320\n\t"
            "v_mul_f32 v229, v237, v229\n\t"
            "v_mfma_f32_16x16x32_bf16 v[208:211], v[112:115], v[220:223], v[208:211]\n\t"
            "v_mul_f32 v230, v237, v230\n\t"
            "v_mul_f32 v231, v237, v231\n\t"
            "v_mfma_f32_16x16x32_bf16 v[212:215], v[136:139], v[220:223], v[212:215]\n\t"
            "v_mul_f32 v180, v238, v180\n\t"
            "v_mul_f32 v181, v238, v181\n\t"
            "v_mfma_f32_16x16x32_bf16 v[188:191], v[176:179], v[188:191], 0\n\t"
            "v_mul_f32 v182, v238, v182\n\t"
            "v_mul_f32 v183, v238, v183\n\t"
            "v_mfma_f32_16x16x32_bf16 v[188:191], v[160:163], v[192:195], v[188:191]\n\t"
            "ds_read_b128 v[192:195], v232 offset:6400\n\t"
            "v_cvt_pk_bf16_f32 v180, v180, v181\n\t"
            "v_mfma_f32_16x16x32_bf16 v[144:147], v[16:19], v[220:223], v[144:147]\n\t"
            "v_cvt_pk_bf16_f32 v181, v182, v183\n\t"
            "v_cvt_pk_bf16_f32 v228, v228, v229\n\t"
            "v_mfma_f32_16x16x32_bf16 v[204:207], v[40:43], v[220:223], v[204:207]\n\t"
            "ds_read_b64 v[182:183], v253 offset:288\n\t"
            "v_cvt_pk_bf16_f32 v229, v230, v231\n\t"
            "v_mfma_f32_16x16x32_bf16 v[152:155], v[64:67], v[220:223], v[152:155]\n\t"
            "ds_read_b128 v[220:223], v233 offset:6144\n\t"
            "v_mul_f32 v188, v237, v188\n\t"
            "s_waitcnt lgkmcnt(10)\n\t"
            "v_mfma_f32_16x16x32_bf16 v[144:147], v[20:23], v[224:227], v[144:147]\n\t"
            "ds_write_b64 v245, v[180:181] offset:12288\n\t"
            "v_mfma_f32_16x16x32_bf16 v[204:207], v[44:47], v[224:227], v[204:207]\n\t"
            "ds_read_b64 v[180:181], v253 offset:256\n\t"
            "v_mul_f32 v189, v237, v189\n\t"
            "v_mfma_f32_16x16x32_bf16 v[152:155], v[68:71], v[224:227], v[152:155]\n\t"
            "ds_write_b64 v245, v[228:229] offset:6272\n\t"
            "v_mfma_f32_16x16x32_bf16 v[148:151], v[92:95], v[224:227], v[148:151]\n\t"
            "v_mul_f32 v190, v237, v190\n\t"
            "v_mul_f32 v191, v237, v191\n\t"
            "v_mfma_f32_16x16x32_bf16 v[208:211], v[116:119], v[224:227], v[208:211]\n\t"
            "ds_read_b32 v237, v251 offset:64\n\t"
            "v_cvt_pk_bf16_f32 v188, v188, v189\n\t"
            "v_mfma_f32_16x16x32_bf16 v[212:215], v[140:143], v[224:227], v[212:215]\n\t"
            "ds_read_b128 v[224:227], v232 offset:6272\n\t"
            "v_cvt_pk_bf16_f32 v189, v190, v191\n\t"
            "v_mfma_f32_16x16x32_bf16 v[228:231], v[172:175], v[196:199], 0\n\t"
            "s_waitcnt lgkmcnt(14)\n\t"
            "ds_write_b64 v245, v[188:189] offset:6400\n\t"
            "v_mfma_f32_16x16x32_bf16 v[228:231], v[164:167], v[200:203], v[228:231]\n\t"
            "s_waitcnt lgkmcnt(14)\n\t"
            "ds_read_b128 v[188:191], v233 offset:6272\n\t"
            "v_mul_f32 v152, v236, v152\n\t"
            "v_mfma_f32_16x16x32_bf16 v[196:199], v[176:179], v[196:199], 0\n\t"
            "v_mul_f32 v153, v236, v153\n\t"
            "v_mul_f32 v154, v236, v154\n\t"
            "v_mfma_f32_16x16x32_bf16 v[196:199], v[160:163], v[200:203], v[196:199]\n\t"
            "s_waitcnt lgkmcnt(14)\n\t"
            "ds_read_b128 v[200:203], v233 offset:6400\n\t"
            "v_mul_f32 v155, v236, v155\n\t"
            "s_waitcnt lgkmcnt(6)\n\t"
            "v_mfma_f32_16x16x32_bf16 v[168:171], v[168:171], v[180:183], 0\n\t"
            "v_mul_f32 v228, v238, v228\n\t"
            "v_mul_f32 v229, v238, v229\n\t"
            "v_mfma_f32_16x16x32_bf16 v[168:171], v[156:159], v[184:187], v[168:171]\n\t"
            "v_mul_f32 v230, v238, v230\n\t"
            "v_mul_f32 v231, v238, v231\n\t"
            "v_mfma_f32_16x16x32_bf16 v[176:179], v[176:179], v[180:183], 0\n\t"
            "v_cvt_pk_bf16_f32 v228, v228, v229\n\t"
            "v_cvt_pk_bf16_f32 v229, v230, v231\n\t"
            "v_mfma_f32_16x16x32_bf16 v[176:179], v[160:163], v[184:187], v[176:179]\n\t"
            "ds_write_b64 v245, v[228:229] offset:12416\n\t"
            "v_mfma_f32_16x16x32_bf16 v[228:231], v[240:243], v[180:183], 0\n\t"
            "v_cvt_pk_bf16_f32 v152, v152, v153\n\t"
            "v_cvt_pk_bf16_f32 v153, v154, v155\n\t"
            "v_mfma_f32_16x16x32_bf16 v[228:231], v[240:243], v[184:187], v[228:231]\n\t"
            "v_mul_f32 v212, v236, v212\n\t"
            "v_mul_f32 v213, v236, v213\n\t"
            "v_mfma_f32_16x16x32_bf16 v[160:163], v[72:75], v[216:219], 0\n\t"
            "v_mul_f32 v214, v236, v214\n\t"
            "v_mul_f32 v215, v236, v215\n\t"
            "v_mfma_f32_16x16x32_bf16 v[160:163], v[76:79], v[220:223], v[160:163]\n\t"
            "v_cvt_pk_bf16_f32 v154, v212, v213\n\t"
            "v_cvt_pk_bf16_f32 v155, v214, v215\n\t"
            "s_waitcnt lgkmcnt(4)\n\t"
            "v_mfma_f32_16x16x32_bf16 v[160:163], v[80:83], v[224:227], v[160:163]\n\t"
            "ds_read_b128 v[212:215], v232 offset:12416\n\t"
            "v_mul_f32 v196, v238, v196\n\t"
            "s_waitcnt lgkmcnt(3)\n\t"
            "v_mfma_f32_16x16x32_bf16 v[160:163], v[84:87], v[188:191], v[160:163]\n\t"
            "v_mul_f32 v197, v238, v197\n\t"
            "v_mul_f32 v198, v238, v198\n\t"
            "v_mfma_f32_16x16x32_bf16 v[160:163], v[88:91], v[192:195], v[160:163]\n\t"
            "v_mul_f32 v199, v238, v199\n\t"
            "v_cvt_pk_bf16_f32 v196, v196, v197\n\t"
            "s_waitcnt lgkmcnt(2)\n\t"
            "v_mfma_f32_16x16x32_bf16 v[160:163], v[92:95], v[200:203], v[160:163]\n\t"
            "ds_read_b32 v238, v251 offset:128\n\t"
            "v_cvt_pk_bf16_f32 v197, v198, v199\n\t"
            "v_mfma_f32_16x16x32_bf16 v[172:175], v[172:175], v[180:183], 0\n\t"
            "ds_write_b64 v245, v[196:197] offset:12544\n\t"
            "v_mfma_f32_16x16x32_bf16 v[172:175], v[164:167], v[184:187], v[172:175]\n\t"
            "v_rcp_f32 v229, v228\n\t"
            "v_mfma_f32_16x16x16_bf16 v[180:183], v[152:153], v[154:155], 0\n\t"
            "v_mul_f32 v168, v229, v168\n\t"
            "v_mul_f32 v169, v229, v169\n\t"
            "v_mul_f32 v176, v229, v176\n\t"
            "v_mfma_f32_16x16x32_bf16 v[196:199], v[24:27], v[216:219], 0\n\t"
            "v_mul_f32 v177, v229, v177\n\t"
            "v_mul_f32 v170, v229, v170\n\t"
            "v_mfma_f32_16x16x32_bf16 v[196:199], v[28:31], v[220:223], v[196:199]\n\t"
            "v_mul_f32 v172, v229, v172\n\t"
            "v_mul_f32 v173, v229, v173\n\t"
            "v_mfma_f32_16x16x32_bf16 v[196:199], v[32:35], v[224:227], v[196:199]\n\t"
            "v_mul_f32 v171, v229, v171\n\t"
            "v_cvt_pk_bf16_f32 v168, v168, v169\n\t"
            "v_mfma_f32_16x16x32_bf16 v[196:199], v[36:39], v[188:191], v[196:199]\n\t"
            "v_mul_f32 v174, v229, v174\n\t"
            "v_mul_f32 v175, v229, v175\n\t"
            "v_mfma_f32_16x16x32_bf16 v[196:199], v[40:43], v[192:195], v[196:199]\n\t"
            "v_cvt_pk_bf16_f32 v172, v172, v173\n\t"
            "v_mul_f32 v178, v229, v178\n\t"
            "v_mfma_f32_16x16x32_bf16 v[196:199], v[44:47], v[200:203], v[196:199]\n\t"
            "v_mul_f32 v179, v229, v179\n\t"
            "v_cvt_pk_bf16_f32 v176, v176, v177\n\t"
            "v_mfma_f32_16x16x32_bf16 v[156:159], v[0:3], v[216:219], 0\n\t"
            "v_xor_b32 v230, s73, v255\n\t"
            "v_cvt_pk_bf16_f32 v169, v170, v171\n\t"
            "v_mfma_f32_16x16x32_bf16 v[156:159], v[4:7], v[220:223], v[156:159]\n\t"
            "v_cvt_pk_bf16_f32 v173, v174, v175\n\t"
            "v_cvt_pk_bf16_f32 v177, v178, v179\n\t"
            "v_mfma_f32_16x16x32_bf16 v[156:159], v[8:11], v[224:227], v[156:159]\n\t"
            "s_mov_b64 exec, s[68:69]\n\t"
            "ds_write_b64 v230, v[168:169]\n\t"
            "ds_write_b64 v230, v[172:173] offset:128\n\t"
            "ds_write_b64 v230, v[176:177] offset:256\n\t"
            "s_mov_b64 exec, -1\n\t"
            "v_mfma_f32_16x16x32_bf16 v[156:159], v[12:15], v[188:191], v[156:159]\n\t"
            "v_mul_f32 v144, v236, v144\n\t"
            "v_mul_f32 v145, v236, v145\n\t"
            "v_mfma_f32_16x16x32_bf16 v[156:159], v[16:19], v[192:195], v[156:159]\n\t"
            "v_mul_f32 v146, v236, v146\n\t"
            "v_mul_f32 v147, v236, v147\n\t"
            "v_mfma_f32_16x16x32_bf16 v[156:159], v[20:23], v[200:203], v[156:159]\n\t"
            "v_cvt_pk_bf16_f32 v144, v144, v145\n\t"
            "v_mul_f32 v148, v236, v148\n\t"
            "v_mfma_f32_16x16x32_bf16 v[164:167], v[48:51], v[216:219], 0\n\t"
            "v_mul_f32 v149, v236, v149\n\t"
            "v_cvt_pk_bf16_f32 v145, v146, v147\n\t"
            "v_mfma_f32_16x16x32_bf16 v[164:167], v[52:55], v[220:223], v[164:167]\n\t"
            "v_mul_f32 v204, v236, v204\n\t"
            "v_mul_f32 v205, v236, v205\n\t"
            "v_mfma_f32_16x16x32_bf16 v[164:167], v[56:59], v[224:227], v[164:167]\n\t"
            "v_mul_f32 v206, v236, v206\n\t"
            "v_mul_f32 v207, v236, v207\n\t"
            "v_mfma_f32_16x16x32_bf16 v[164:167], v[60:63], v[188:191], v[164:167]\n\t"
            "s_waitcnt lgkmcnt(0)\n\t"
            "s_barrier\n\t"
            "s_setprio 2\n\t"
            "v_mul_f32 v150, v236, v150\n\t"
            "v_mul_f32 v151, v236, v151\n\t"
            "v_mfma_f32_16x16x32_bf16 v[164:167], v[64:67], v[192:195], v[164:167]\n\t"
            "v_cvt_pk_bf16_f32 v148, v148, v149\n\t"
            "v_cvt_pk_bf16_f32 v146, v204, v205\n\t"
            "v_mfma_f32_16x16x32_bf16 v[164:167], v[68:71], v[200:203], v[164:167]\n\t"
            "v_cvt_pk_bf16_f32 v147, v206, v207\n\t"
            "v_cvt_pk_bf16_f32 v149, v150, v151\n\t"
            "v_mfma_f32_16x16x32_bf16 v[228:231], v[96:99], v[216:219], 0\n\t"
            "ds_read_b128 v[204:207], v232 offset:12288\n\t"
            "v_mul_f32 v208, v236, v208\n\t"
            "v_mfma_f32_16x16x32_bf16 v[228:231], v[100:103], v[220:223], v[228:231]\n\t"
            "v_mul_f32 v209, v236, v209\n\t"
            "v_mul_f32 v210, v236, v210\n\t"
            "v_mfma_f32_16x16x32_bf16 v[228:231], v[104:107], v[224:227], v[228:231]\n\t"
            "v_mul_f32 v211, v236, v211\n\t"
            "v_cvt_pk_bf16_f32 v150, v208, v209\n\t"
            "v_mfma_f32_16x16x32_bf16 v[228:231], v[108:111], v[188:191], v[228:231]\n\t"
            "v_cvt_pk_bf16_f32 v151, v210, v211\n\t"
            "v_mul_f32 v156, v237, v156\n\t"
            "v_mfma_f32_16x16x32_bf16 v[228:231], v[112:115], v[192:195], v[228:231]\n\t"
            "ds_read_b128 v[208:211], v233 offset:12288\n\t"
            "v_mul_f32 v157, v237, v157\n\t"
            "v_mfma_f32_16x16x32_bf16 v[228:231], v[116:119], v[200:203], v[228:231]\n\t"
            "v_mul_f32 v158, v237, v158\n\t"
            "v_mul_f32 v159, v237, v159\n\t"
            "v_mfma_f32_16x16x32_bf16 v[180:183], v[144:147], v[148:151], v[180:183]\n\t"
            "v_cvt_pk_bf16_f32 v156, v156, v157\n\t"
            "v_cvt_pk_bf16_f32 v157, v158, v159\n\t"
            "v_mul_f32 v196, v237, v196\n\t"
            "v_mul_f32 v197, v237, v197\n\t"
            "v_mul_f32 v198, v237, v198\n\t"
            "v_mul_f32 v199, v237, v199\n\t"
            "v_cvt_pk_bf16_f32 v158, v196, v197\n\t"
            "v_cvt_pk_bf16_f32 v159, v198, v199\n\t"
            "v_mfma_f32_16x16x32_bf16 v[196:199], v[120:123], v[216:219], 0\n\t"
            "ds_read_b128 v[216:219], v233 offset:12416\n\t"
            "v_mul_f32 v164, v237, v164\n\t"
            "v_mfma_f32_16x16x32_bf16 v[196:199], v[124:127], v[220:223], v[196:199]\n\t"
            "ds_read_b128 v[220:223], v232 offset:12544\n\t"
            "v_mul_f32 v165, v237, v165\n\t"
            "v_mfma_f32_16x16x32_bf16 v[196:199], v[128:131], v[224:227], v[196:199]\n\t"
            "ds_read_b128 v[224:227], v233 offset:12544\n\t"
            "v_mul_f32 v166, v237, v166\n\t"
            "v_mfma_f32_16x16x32_bf16 v[196:199], v[132:135], v[188:191], v[196:199]\n\t"
            "v_mul_f32 v167, v237, v167\n\t"
            "v_cvt_pk_bf16_f32 v164, v164, v165\n\t"
            "v_mfma_f32_16x16x32_bf16 v[196:199], v[136:139], v[192:195], v[196:199]\n\t"
            "v_cvt_pk_bf16_f32 v165, v166, v167\n\t"
            "v_mul_f32 v160, v237, v160\n\t"
            "v_mfma_f32_16x16x32_bf16 v[196:199], v[140:143], v[200:203], v[196:199]\n\t"
            "v_mul_f32 v161, v237, v161\n\t"
            "v_mul_f32 v162, v237, v162\n\t"
            "s_waitcnt lgkmcnt(4)\n\t"
            "v_mfma_f32_16x16x32_bf16 v[176:179], v[48:51], v[204:207], 0\n\t"
            "v_mul_f32 v163, v237, v163\n\t"
            "v_cvt_pk_bf16_f32 v160, v160, v161\n\t"
            "s_waitcnt lgkmcnt(3)\n\t"
            "v_mfma_f32_16x16x32_bf16 v[176:179], v[52:55], v[208:211], v[176:179]\n\t"
            "v_cvt_pk_bf16_f32 v161, v162, v163\n\t"
            "v_mul_f32 v228, v237, v228\n\t"
            "v_mfma_f32_16x16x32_bf16 v[176:179], v[56:59], v[212:215], v[176:179]\n\t"
            "v_mul_f32 v196, v237, v196\n\t"
            "v_mul_f32 v197, v237, v197\n\t"
            "s_waitcnt lgkmcnt(2)\n\t"
            "v_mfma_f32_16x16x32_bf16 v[176:179], v[60:63], v[216:219], v[176:179]\n\t"
            "v_mul_f32 v198, v237, v198\n\t"
            "v_mul_f32 v199, v237, v199\n\t"
            "s_waitcnt lgkmcnt(1)\n\t"
            "v_mfma_f32_16x16x32_bf16 v[176:179], v[64:67], v[220:223], v[176:179]\n\t"
            "v_cvt_pk_bf16_f32 v166, v196, v197\n\t"
            "v_cvt_pk_bf16_f32 v167, v198, v199\n\t"
            "s_waitcnt lgkmcnt(0)\n\t"
            "v_mfma_f32_16x16x32_bf16 v[176:179], v[68:71], v[224:227], v[176:179]\n\t"
            "v_mul_f32 v229, v237, v229\n\t"
            "v_mul_f32 v230, v237, v230\n\t"
            "v_mfma_f32_16x16x16_bf16 v[188:191], v[152:153], v[166:167], 0\n\t"
            "v_mul_f32 v231, v237, v231\n\t"
            "v_cvt_pk_bf16_f32 v162, v228, v229\n\t"
            "v_cvt_pk_bf16_f32 v163, v230, v231\n\t"
            "v_mfma_f32_16x16x32_bf16 v[168:171], v[0:3], v[204:207], 0\n\t"
            "v_exp_f32 v180, v180\n\t"
            "v_mfma_f32_16x16x32_bf16 v[168:171], v[4:7], v[208:211], v[168:171]\n\t"
            "v_exp_f32 v181, v181\n\t"
            "v_mfma_f32_16x16x32_bf16 v[168:171], v[8:11], v[212:215], v[168:171]\n\t"
            "v_exp_f32 v182, v182\n\t"
            "v_mfma_f32_16x16x32_bf16 v[168:171], v[12:15], v[216:219], v[168:171]\n\t"
            "v_exp_f32 v183, v183\n\t"
            "v_mfma_f32_16x16x32_bf16 v[168:171], v[16:19], v[220:223], v[168:171]\n\t"
            "v_mul_f32 v176, v238, v176\n\t"
            "v_mul_f32 v177, v238, v177\n\t"
            "v_mfma_f32_16x16x32_bf16 v[168:171], v[20:23], v[224:227], v[168:171]\n\t"
            "v_mul_f32 v178, v238, v178\n\t"
            "v_mul_f32 v179, v238, v179\n\t"
            "v_mfma_f32_16x16x16_bf16 v[192:195], v[164:165], v[166:167], 0\n\t"
            "v_cvt_pk_bf16_f32 v176, v176, v177\n\t"
            "v_cvt_pk_bf16_f32 v180, v180, v181\n\t"
            "v_cvt_pk_bf16_f32 v177, v178, v179\n\t"
            "v_mfma_f32_16x16x32_bf16 v[188:191], v[144:147], v[160:163], v[188:191]\n\t"
            "v_cvt_pk_bf16_f32 v181, v182, v183\n\t"
            "v_mul_f32 v168, v238, v168\n\t"
            "v_mfma_f32_16x16x16_bf16 v[184:187], v[164:165], v[154:155], 0\n\t"
            "v_mul_f32 v169, v238, v169\n\t"
            "v_mul_f32 v170, v238, v170\n\t"
            "v_mul_f32 v171, v238, v171\n\t"
            "v_mfma_f32_16x16x32_bf16 v[192:195], v[156:159], v[160:163], v[192:195]\n\t"
            "s_nop 0\n\t"
            "v_exp_f32 v188, v188\n\t"
            "v_mfma_f32_16x16x32_bf16 v[172:175], v[72:75], v[204:207], 0\n\t"
            "v_exp_f32 v189, v189\n\t"
            "v_mfma_f32_16x16x32_bf16 v[172:175], v[76:79], v[208:211], v[172:175]\n\t"
            "v_exp_f32 v190, v190\n\t"
            "v_mfma_f32_16x16x32_bf16 v[172:175], v[80:83], v[212:215], v[172:175]\n\t"
            "v_exp_f32 v191, v191\n\t"
            "v_mfma_f32_16x16x32_bf16 v[172:175], v[84:87], v[216:219], v[172:175]\n\t"
            "v_exp_f32 v192, v192\n\t"
            "v_mfma_f32_16x16x32_bf16 v[172:175], v[88:91], v[220:223], v[172:175]\n\t"
            "v_exp_f32 v193, v193\n\t"
            "v_mfma_f32_16x16x32_bf16 v[172:175], v[92:95], v[224:227], v[172:175]\n\t"
            "v_exp_f32 v194, v194\n\t"
            "v_mfma_f32_16x16x32_bf16 v[184:187], v[156:159], v[148:151], v[184:187]\n\t"
            "v_exp_f32 v195, v195\n\t"
            "v_mfma_f32_16x16x32_bf16 v[200:203], v[120:123], v[204:207], 0\n\t"
            "v_cvt_pk_bf16_f32 v188, v188, v189\n\t"
            "v_cvt_pk_bf16_f32 v189, v190, v191\n\t"
            "v_mfma_f32_16x16x32_bf16 v[200:203], v[124:127], v[208:211], v[200:203]\n\t"
            "v_cvt_pk_bf16_f32 v190, v192, v193\n\t"
            "v_cvt_pk_bf16_f32 v191, v194, v195\n\t"
            "v_mfma_f32_16x16x32_bf16 v[200:203], v[128:131], v[212:215], v[200:203]\n\t"
            "v_exp_f32 v184, v184\n\t"
            "v_mfma_f32_16x16x32_bf16 v[200:203], v[132:135], v[216:219], v[200:203]\n\t"
            "v_exp_f32 v185, v185\n\t"
            "v_mfma_f32_16x16x32_bf16 v[200:203], v[136:139], v[220:223], v[200:203]\n\t"
            "v_exp_f32 v186, v186\n\t"
            "v_mfma_f32_16x16x32_bf16 v[200:203], v[140:143], v[224:227], v[200:203]\n\t"
            "v_exp_f32 v187, v187\n\t"
            "v_cvt_pk_bf16_f32 v182, v184, v185\n\t"
            "v_cvt_pk_bf16_f32 v183, v186, v187\n\t"
            "v_mfma_f32_16x16x16_bf16 v[184:187], v[176:177], v[154:155], 0\n\t"
            "v_cvt_pk_bf16_f32 v168, v168, v169\n\t"
            "v_cvt_pk_bf16_f32 v169, v170, v171\n\t"
            "v_mul_f32 v172, v238, v172\n\t"
            "v_mfma_f32_16x16x32_bf16 v[196:199], v[24:27], v[204:207], 0\n\t"
            "s_setprio 0\n\t"
            "v_mul_f32 v173, v238, v173\n\t"
            "v_mul_f32 v200, v238, v200\n\t"
            "v_mfma_f32_16x16x32_bf16 v[196:199], v[28:31], v[208:211], v[196:199]\n\t"
            "v_mul_f32 v201, v238, v201\n\t"
            "v_mul_f32 v202, v238, v202\n\t"
            "v_mfma_f32_16x16x32_bf16 v[196:199], v[32:35], v[212:215], v[196:199]\n\t"
            "v_mul_f32 v203, v238, v203\n\t"
            "v_mul_f32 v174, v238, v174\n\t"
            "v_mfma_f32_16x16x32_bf16 v[196:199], v[36:39], v[216:219], v[196:199]\n\t"
            "v_mul_f32 v175, v238, v175\n\t"
            "v_cvt_pk_bf16_f32 v172, v172, v173\n\t"
            "v_mfma_f32_16x16x32_bf16 v[196:199], v[40:43], v[220:223], v[196:199]\n\t"
            "v_cvt_pk_bf16_f32 v178, v200, v201\n\t"
            "v_cvt_pk_bf16_f32 v179, v202, v203\n\t"
            "v_mfma_f32_16x16x32_bf16 v[196:199], v[44:47], v[224:227], v[196:199]\n\t"
            "v_cvt_pk_bf16_f32 v173, v174, v175\n\t"
            "v_mfma_f32_16x16x32_bf16 v[228:231], v[96:99], v[204:207], 0\n\t"
            "ds_read_b64 v[204:205], v235 offset:136\n\t"
            "v_mfma_f32_16x16x32_bf16 v[228:231], v[100:103], v[208:211], v[228:231]\n\t"
            "ds_read_b64 v[206:207], v234 offset:128\n\t"
            "v_mfma_f32_16x16x32_bf16 v[228:231], v[104:107], v[212:215], v[228:231]\n\t"
            "ds_read_b128 v[208:211], v234\n\t"
            "v_mfma_f32_16x16x32_bf16 v[228:231], v[108:111], v[216:219], v[228:231]\n\t"
            "ds_read_b128 v[212:215], v235 offset:64\n\t"
            "v_mul_f32 v196, v238, v196\n\t"
            "v_mfma_f32_16x16x32_bf16 v[228:231], v[112:115], v[220:223], v[228:231]\n\t"
            "v_mul_f32 v197, v238, v197\n\t"
            "v_mul_f32 v198, v238, v198\n\t"
            "v_mfma_f32_16x16x32_bf16 v[228:231], v[116:119], v[224:227], v[228:231]\n\t"
            "v_mul_f32 v199, v238, v199\n\t"
            "v_cvt_pk_bf16_f32 v170, v196, v197\n\t"
            "v_mfma_f32_16x16x16_bf16 v[192:195], v[176:177], v[166:167], 0\n\t"
            "v_cvt_pk_bf16_f32 v171, v198, v199\n\t"
            "s_nop 1\n\t"
            "v_mfma_f32_16x16x32_bf16 v[184:187], v[168:171], v[148:151], v[184:187]\n\t"
            "s_nop 0\n\t"
            "v_mul_f32 v228, v238, v228\n\t"
            "v_mul_f32 v229, v238, v229\n\t"
            "v_mul_f32 v230, v238, v230\n\t"
            "v_mul_f32 v231, v238, v231\n\t"
            "v_mfma_f32_16x16x32_bf16 v[192:195], v[168:171], v[160:163], v[192:195]\n\t"
            "v_cvt_pk_bf16_f32 v174, v228, v229\n\t"
            "v_cvt_pk_bf16_f32 v175, v230, v231\n\t"
            "s_waitcnt lgkmcnt(2)\n\t"
            "v_mfma_f32_16x16x32_bf16 v[216:219], v[204:207], v[152:155], 0\n\t"
            "v_exp_f32 v184, v184\n\t"
            "s_waitcnt lgkmcnt(1)\n\t"
            "v_mfma_f32_16x16x32_bf16 v[216:219], v[208:211], v[148:151], v[216:219]\n\t"
            "v_exp_f32 v185, v185\n\t"
            "s_waitcnt lgkmcnt(0)\n\t"
            "v_mfma_f32_16x16x32_bf16 v[216:219], v[212:215], v[144:147], v[216:219]\n\t"
            "v_exp_f32 v186, v186\n\t"
            "v_mfma_f32_16x16x32_bf16 v[220:223], v[204:207], v[164:167], 0\n\t"
            "v_exp_f32 v187, v187\n\t"
            "v_mfma_f32_16x16x32_bf16 v[220:223], v[208:211], v[160:163], v[220:223]\n\t"
            "v_cvt_pk_bf16_f32 v184, v184, v185\n\t"
            "v_cvt_pk_bf16_f32 v185, v186, v187\n\t"
            "v_mfma_f32_16x16x32_bf16 v[220:223], v[212:215], v[156:159], v[220:223]\n\t"
            "s_nop 0\n\t"
            "v_exp_f32 v216, v216\n\t"
            "v_mfma_f32_16x16x16_bf16 v[228:231], v[164:165], v[178:179], 0\n\t"
            "v_cvt_pk_bf16_f32 v216, v216, 0\n\t"
            "v_cndmask_b32_e64 v186, 0, v216, s[58:59]\n\t"
            "v_mov_b32 v187, 0\n\t"
            "v_mfma_f32_16x16x16_bf16 v[200:203], v[176:177], v[178:179], 0\n\t"
            "v_exp_f32 v192, v192\n\t"
            "v_exp_f32 v193, v193\n\t"
            "v_mfma_f32_16x16x32_bf16 v[224:227], v[204:207], v[176:179], 0\n\t"
            "v_exp_f32 v194, v194\n\t"
            "v_mfma_f32_16x16x32_bf16 v[224:227], v[208:211], v[172:175], v[224:227]\n\t"
            "v_exp_f32 v195, v195\n\t"
            "v_mfma_f32_16x16x32_bf16 v[224:227], v[212:215], v[168:171], v[224:227]\n\t"
            "v_exp_f32 v220, v220\n\t"
            "v_mfma_f32_16x16x16_bf16 v[196:199], v[152:153], v[178:179], 0\n\t"
            "v_cvt_pk_bf16_f32 v192, v192, v193\n\t"
            "v_cvt_pk_bf16_f32 v193, v194, v195\n\t"
            "v_cvt_pk_bf16_f32 v220, v220, 0\n\t"
            "v_mfma_f32_16x16x32_bf16 v[204:207], v[240:243], v[180:183], 0\n\t"
            "v_cndmask_b32_e64 v194, 0, v220, s[58:59]\n\t"
            "v_mov_b32 v195, 0\n\t"
            "v_mfma_f32_16x16x32_bf16 v[204:207], v[240:243], v[184:187], v[204:207]\n\t"
            "v_cndmask_b32_e64 v235, v217, v221, s[60:61]\n\t"
            "v_cndmask_b32_e64 v235, v235, v225, s[62:63]\n\t"
            "v_mfma_f32_16x16x32_bf16 v[200:203], v[168:171], v[172:175], v[200:203]\n\t"
            "v_cndmask_b32_e64 v235, v235, v239, s[64:65]\n\t"
            "v_cmp_nlt_f32_e64 s[24:25], |v235|, s23\n\t"
            "v_mfma_f32_16x16x32_bf16 v[208:211], v[240:243], v[188:191], 0\n\t"
            "v_exp_f32 v235, v235\n\t"
            "v_mfma_f32_16x16x32_bf16 v[208:211], v[240:243], v[192:195], v[208:211]\n\t"
            "v_exp_f32 v224, v224\n\t"
            "v_mfma_f32_16x16x32_bf16 v[196:199], v[144:147], v[172:175], v[196:199]\n\t"
            "s_nop 0\n\t"
            "v_exp_f32 v200, v200\n\t"
            "v_mfma_f32_16x16x32_bf16 v[228:231], v[156:159], v[172:175], v[228:231]\n\t"
            "v_exp_f32 v201, v201\n\t"
            "v_exp_f32 v202, v202\n\t"
            "v_exp_f32 v203, v203\n\t"
            "v_cvt_pk_bf16_f32 v235, v235, 0\n\t"
            "v_cvt_pk_bf16_f32 v200, v200, v201\n\t"
            "v_cndmask_b32_e64 v235, 0, v235, s[66:67]\n\t"
            "v_exp_f32 v196, v196\n\t"
            "v_exp_f32 v197, v197\n\t"
            "v_exp_f32 v198, v198\n\t"
            "v_exp_f32 v199, v199\n\t"
            "v_exp_f32 v228, v228\n\t"
            "v_exp_f32 v229, v229\n\t"
            "v_exp_f32 v230, v230\n\t"
            "v_exp_f32 v231, v231\n\t"
            "v_cvt_pk_bf16_f32 v196, v196, v197\n\t"
            "v_cvt_pk_bf16_f32 v197, v198, v199\n\t"
            "v_cvt_pk_bf16_f32 v198, v228, v229\n\t"
            "v_cvt_pk_bf16_f32 v199, v230, v231\n\t"
            "v_cvt_pk_bf16_f32 v201, v202, v203\n\t"
            "v_cvt_pk_bf16_f32 v224, v224, 0\n\t"
            "v_mfma_f32_16x16x32_bf16 v[212:215], v[240:243], v[196:199], 0\n\t"
            "ds_write_b16 v250, v235 offset:256\n\t"
            "v_cndmask_b32_e64 v202, 0, v224, s[58:59]\n\t"
            "v_mov_b32 v203, 0\n\t"
            "v_add_u32 v205, 0xe0800000, v204\n\t"
            "v_add_u32 v209, 0xe0800000, v208\n\t"
            "v_mfma_f32_16x16x32_bf16 v[212:215], v[240:243], v[200:203], v[212:215]\n\t"
            "v_rcp_f32 v236, v204\n\t"
            "v_rcp_f32 v237, v208\n\t"
            "s_nop 5\n\t"
            "v_add_u32 v213, 0xe0800000, v212\n\t"
            "v_rcp_f32 v238, v212\n\t"
            "v_or3_b32 v205, v205, v209, v213\n\t"
            "v_cmp_le_u32 vcc, 2.0, v205\n\t"
            ".Lwa_in%=:\n\t"
            "s_or_b64 vcc, vcc, s[24:25]\n\t"
            "s_cbranch_scc1 .Lwa_slow%=\n\t"
            ".Lwa_ok%=:\n\t"
            "s_bfe_u32 s49, s70, 0x30001\n\t"
            "s_lshl_b32 s49, s49, 4\n\t"
            "s_or_b32 s73, s77, s49\n\t"
            "s_add_u32 s70, s70, 1\n\t"
            "s_add_u32 s72, s72, 800\n\t"
            "s_add_u32 s76, s76, 800\n\t"
            "s_add_u32 s77, s77, 384\n\t"
            "s_xor_b32 s48, s48, s47\n\t"
            "s_sub_u32 s44, s44, 1\n\t"
            "s_cmp_gt_u32 s44, 2\n\t"
            "s_barrier\n\t"
            "s_setprio 0\n\t"
            "s_cbranch_scc1 .Lwa_loop%=\n\t"
            "s_cmp_eq_u32 s44, 0\n\t"
            "s_cbranch_scc1 .Lwa_done%=\n\t"
            "s_cmp_lg_u32 s44, 2\n\t"
            "s_cbranch_scc1 .Lwa_loop%=\n\t"
            "s_mov_b32 m0, s40\n\t"
            "v_lshlrev_b32 v234, 3, v248\n\t"
            "buffer_load_dword v234, s[36:39], s41 offen lds\n\t"
            "s_branch .Lwa_loop%=\n\t"
            ".Lwa_slow%=:\n\t"
            "s_waitcnt lgkmcnt(0)\n\t"
            "s_nop 7\n\t"
            "v_mfma_f32_16x16x16_bf16 v[180:183], v[152:153], v[154:155], 0\n\t"
            "v_add_u32 v234, s72, v252\n\t"
            "v_cndmask_b32_e64 v235, v246, v234, s[50:51]\n\t"
            "v_cndmask_b32_e64 v234, v246, v234, s[26:27]\n\t"
            "v_mfma_f32_16x16x16_bf16 v[228:231], v[164:165], v[154:155], 0\n\t"
            "ds_read_b64 v[216:217], v235 offset:136\n\t"
            "ds_read_b64 v[218:219], v234 offset:128\n\t"
            "v_mfma_f32_16x16x16_bf16 v[184:187], v[176:177], v[154:155], 0\n\t"
            "ds_read_b128 v[220:223], v234\n\t"
            "ds_read_b128 v[224:227], v235 offset:64\n\t"
            "v_mfma_f32_16x16x32_bf16 v[180:183], v[144:147], v[148:151], v[180:183]\n\t"
            "v_mfma_f32_16x16x32_bf16 v[228:231], v[156:159], v[148:151], v[228:231]\n\t"
            "s_nop 1\n\t"
            "v_mfma_f32_16x16x32_bf16 v[184:187], v[168:171], v[148:151], v[184:187]\n\t"
            "v_mfma_f32_16x16x16_bf16 v[188:191], v[152:153], v[166:167], 0\n\t"
            "v_mfma_f32_16x16x16_bf16 v[192:195], v[176:177], v[166:167], 0\n\t"
            "v_mfma_f32_16x16x16_bf16 v[196:199], v[152:153], v[178:179], 0\n\t"
            "v_mfma_f32_16x16x16_bf16 v[200:203], v[176:177], v[178:179], 0\n\t"
            "s_waitcnt lgkmcnt(2)\n\t"
            "v_mfma_f32_16x16x32_bf16 v[204:207], v[216:219], v[152:155], 0\n\t"
            "s_waitcnt lgkmcnt(1)\n\t"
            "v_mfma_f32_16x16x32_bf16 v[204:207], v[220:223], v[148:151], v[204:207]\n\t"
            "s_waitcnt lgkmcnt(0)\n\t"
            "v_mfma_f32_16x16x32_bf16 v[204:207], v[224:227], v[144:147], v[204:207]\n\t"
            "v_mfma_f32_16x16x32_bf16 v[208:211], v[216:219], v[164:167], 0\n\t"
            "v_mfma_f32_16x16x32_bf16 v[208:211], v[220:223], v[160:163], v[208:211]\n\t"
            "v_mfma_f32_16x16x32_bf16 v[208:211], v[224:227], v[156:159], v[208:211]\n\t"
            "v_mfma_f32_16x16x32_bf16 v[188:191], v[144:147], v[160:163], v[188:191]\n\t"
            "v_mfma_f32_16x16x32_bf16 v[192:195], v[168:171], v[160:163], v[192:195]\n\t"
            "v_mfma_f32_16x16x32_bf16 v[212:215], v[216:219], v[176:179], 0\n\t"
            "s_nop 1\n\t"
            "v_max3_f32 v232, v204, v180, v181\n\t"
            "v_mfma_f32_16x16x32_bf16 v[212:215], v[220:223], v[172:175], v[212:215]\n\t"
            "v_max3_f32 v232, v232, v182, v183\n\t"
            "v_max3_f32 v232, v232, v228, v229\n\t"
            "v_mfma_f32_16x16x32_bf16 v[212:215], v[224:227], v[168:171], v[212:215]\n\t"
            "v_max3_f32 v232, v232, v230, v231\n\t"
            "v_max3_f32 v232, v232, v184, v185\n\t"
            "v_mfma_f32_16x16x32_bf16 v[196:199], v[144:147], v[172:175], v[196:199]\n\t"
            "v_max3_f32 v232, v232, v186, v187\n\t"
            "v_mov_b32 v233, v232\n\t"
            "v_mfma_f32_16x16x32_bf16 v[200:203], v[168:171], v[172:175], v[200:203]\n\t"
            "s_nop 0\n\t"
            "v_permlane32_swap_b32 v232, v233\n\t"
            "v_max_f32 v232, v232, v233\n\t"
            "v_mov_b32 v233, v232\n\t"
            "v_max3_f32 v234, v205, v209, v213\n\t"
            "v_max_f32 v234, v234, v239\n\t"
            "v_permlane16_swap_b32 v232, v233\n\t"
            "v_max_f32 v232, v232, v233\n\t"
            "v_sub_f32 v180, v180, v232\n\t"
            "v_sub_f32 v181, v181, v232\n\t"
            "v_exp_f32 v180, v180\n\t"
            "v_exp_f32 v181, v181\n\t"
            "v_sub_f32 v182, v182, v232\n\t"
            "v_sub_f32 v183, v183, v232\n\t"
            "v_exp_f32 v182, v182\n\t"
            "v_exp_f32 v183, v183\n\t"
            "v_sub_f32 v228, v228, v232\n\t"
            "v_sub_f32 v229, v229, v232\n\t"
            "v_sub_f32 v230, v230, v232\n\t"
            "v_sub_f32 v231, v231, v232\n\t"
            "v_exp_f32 v228, v228\n\t"
            "v_exp_f32 v229, v229\n\t"
            "v_exp_f32 v230, v230\n\t"
            "v_exp_f32 v231, v231\n\t"
            "v_cvt_pk_bf16_f32 v180, v180, v181\n\t"
            "v_cvt_pk_bf16_f32 v181, v182, v183\n\t"
            "v_cvt_pk_bf16_f32 v182, v228, v229\n\t"
            "v_cvt_pk_bf16_f32 v183, v230, v231\n\t"
            "v_mfma_f32_16x16x16_bf16 v[228:231], v[164:165], v[166:167], 0\n\t"
            "v_sub_f32 v184, v184, v232\n\t"
            "v_sub_f32 v185, v185, v232\n\t"
            "v_sub_f32 v186, v186, v232\n\t"
            "v_sub_f32 v187, v187, v232\n\t"
            "v_sub_f32 v204, v204, v232\n\t"
            "v_max3_f32 v232, v208, v188, v189\n\t"
            "v_max3_f32 v232, v232, v190, v191\n\t"
            "v_mfma_f32_16x16x32_bf16 v[228:231], v[156:159], v[160:163], v[228:231]\n\t"
            "v_max_f32_dpp v234, v234, v234 row_ror:8 row_mask:0xf bank_mask:0xf\n\t"
            "v_cndmask_b32_e64 v235, v205, v209, s[60:61]\n\t"
            "v_cndmask_b32_e64 v235, v235, v213, s[62:63]\n\t"
            "v_max_f32_dpp v234, v234, v234 row_ror:4 row_mask:0xf bank_mask:0xf\n\t"
            "v_cndmask_b32_e64 v235, v235, v239, s[64:65]\n\t"
            "v_exp_f32 v184, v184\n\t"
            "v_max_f32_dpp v234, v234, v234 row_ror:2 row_mask:0xf bank_mask:0xf\n\t"
            "v_exp_f32 v185, v185\n\t"
            "v_max3_f32 v232, v232, v228, v229\n\t"
            "v_max3_f32 v232, v232, v230, v231\n\t"
            "v_max3_f32 v232, v232, v192, v193\n\t"
            "v_max3_f32 v232, v232, v194, v195\n\t"
            "v_mov_b32 v233, v232\n\t"
            "v_max_f32_dpp v234, v234, v234 row_ror:1 row_mask:0xf bank_mask:0xf\n\t"
            "v_sub_f32 v235, v235, v234\n\t"
            "v_permlane32_swap_b32 v232, v233\n\t"
            "v_max_f32 v232, v232, v233\n\t"
            "v_mov_b32 v233, v232\n\t"
            "v_exp_f32 v235, v235\n\t"
            "v_exp_f32 v186, v186\n\t"
            "v_permlane16_swap_b32 v232, v233\n\t"
            "v_max_f32 v232, v232, v233\n\t"
            "v_sub_f32 v188, v188, v232\n\t"
            "v_sub_f32 v189, v189, v232\n\t"
            "v_exp_f32 v188, v188\n\t"
            "v_exp_f32 v189, v189\n\t"
            "v_sub_f32 v190, v190, v232\n\t"
            "v_sub_f32 v191, v191, v232\n\t"
            "v_exp_f32 v190, v190\n\t"
            "v_exp_f32 v191, v191\n\t"
            "v_sub_f32 v228, v228, v232\n\t"
            "v_sub_f32 v229, v229, v232\n\t"
            "v_sub_f32 v230, v230, v232\n\t"
            "v_sub_f32 v231, v231, v232\n\t"
            "v_exp_f32 v228, v228\n\t"
            "v_exp_f32 v229, v229\n\t"
            "v_exp_f32 v230, v230\n\t"
            "v_exp_f32 v231, v231\n\t"
            "v_cvt_pk_bf16_f32 v188, v188, v189\n\t"
            "v_cvt_pk_bf16_f32 v189, v190, v191\n\t"
            "v_cvt_pk_bf16_f32 v190, v228, v229\n\t"
            "v_cvt_pk_bf16_f32 v191, v230, v231\n\t"
            "v_mfma_f32_16x16x16_bf16 v[228:231], v[164:165], v[178:179], 0\n\t"
            "v_sub_f32 v192, v192, v232\n\t"
            "v_sub_f32 v193, v193, v232\n\t"
            "v_sub_f32 v194, v194, v232\n\t"
            "v_sub_f32 v195, v195, v232\n\t"
            "v_sub_f32 v208, v208, v232\n\t"
            "v_max3_f32 v232, v212, v196, v197\n\t"
            "v_max3_f32 v232, v232, v198, v199\n\t"
            "v_mfma_f32_16x16x32_bf16 v[228:231], v[156:159], v[172:175], v[228:231]\n\t"
            "v_exp_f32 v192, v192\n\t"
            "v_exp_f32 v193, v193\n\t"
            "v_exp_f32 v187, v187\n\t"
            "v_exp_f32 v204, v204\n\t"
            "v_exp_f32 v194, v194\n\t"
            "v_exp_f32 v195, v195\n\t"
            "v_exp_f32 v208, v208\n\t"
            "v_cvt_pk_bf16_f32 v184, v184, v185\n\t"
            "v_max3_f32 v232, v232, v228, v229\n\t"
            "v_max3_f32 v232, v232, v230, v231\n\t"
            "v_max3_f32 v232, v232, v200, v201\n\t"
            "v_max3_f32 v232, v232, v202, v203\n\t"
            "v_mov_b32 v233, v232\n\t"
            "v_cvt_pk_bf16_f32 v192, v192, v193\n\t"
            "v_cvt_pk_bf16_f32 v235, v235, 0\n\t"
            "v_permlane32_swap_b32 v232, v233\n\t"
            "v_max_f32 v232, v232, v233\n\t"
            "v_mov_b32 v233, v232\n\t"
            "v_cvt_pk_bf16_f32 v185, v186, v187\n\t"
            "v_cvt_pk_bf16_f32 v204, v204, 0\n\t"
            "v_permlane16_swap_b32 v232, v233\n\t"
            "v_max_f32 v232, v232, v233\n\t"
            "v_sub_f32 v196, v196, v232\n\t"
            "v_sub_f32 v197, v197, v232\n\t"
            "v_exp_f32 v196, v196\n\t"
            "v_exp_f32 v197, v197\n\t"
            "v_sub_f32 v198, v198, v232\n\t"
            "v_sub_f32 v199, v199, v232\n\t"
            "v_exp_f32 v198, v198\n\t"
            "v_exp_f32 v199, v199\n\t"
            "v_sub_f32 v228, v228, v232\n\t"
            "v_sub_f32 v229, v229, v232\n\t"
            "v_sub_f32 v230, v230, v232\n\t"
            "v_sub_f32 v231, v231, v232\n\t"
            "v_sub_f32 v200, v200, v232\n\t"
            "v_sub_f32 v201, v201, v232\n\t"
            "v_exp_f32 v228, v228\n\t"
            "v_exp_f32 v229, v229\n\t"
            "v_exp_f32 v230, v230\n\t"
            "v_exp_f32 v231, v231\n\t"
            "v_exp_f32 v200, v200\n\t"
            "v_exp_f32 v201, v201\n\t"
            "v_sub_f32 v202, v202, v232\n\t"
            "v_sub_f32 v203, v203, v232\n\t"
            "v_sub_f32 v212, v212, v232\n\t"
            "v_cvt_pk_bf16_f32 v196, v196, v197\n\t"
            "v_exp_f32 v202, v202\n\t"
            "v_exp_f32 v203, v203\n\t"
            "v_exp_f32 v212, v212\n\t"
            "v_cvt_pk_bf16_f32 v197, v198, v199\n\t"
            "v_cvt_pk_bf16_f32 v198, v228, v229\n\t"
            "v_cvt_pk_bf16_f32 v199, v230, v231\n\t"
            "v_cvt_pk_bf16_f32 v200, v200, v201\n\t"
            "v_cvt_pk_bf16_f32 v193, v194, v195\n\t"
            "v_cvt_pk_bf16_f32 v208, v208, 0\n\t"
            "v_cvt_pk_bf16_f32 v201, v202, v203\n\t"
            "v_cvt_pk_bf16_f32 v212, v212, 0\n\t"
            "v_cndmask_b32_e64 v235, 0, v235, s[66:67]\n\t"
            "v_cndmask_b32_e64 v186, 0, v204, s[58:59]\n\t"
            "v_mfma_f32_16x16x32_bf16 v[204:207], v[240:243], v[180:183], 0\n\t"
            "v_cndmask_b32_e64 v194, 0, v208, s[58:59]\n\t"
            "v_cndmask_b32_e64 v202, 0, v212, s[58:59]\n\t"
            "v_mfma_f32_16x16x32_bf16 v[208:211], v[240:243], v[188:191], 0\n\t"
            "ds_write_b16 v250, v235 offset:256\n\t"
            "v_mfma_f32_16x16x32_bf16 v[212:215], v[240:243], v[196:199], 0\n\t"
            "v_mov_b32 v187, 0\n\t"
            "v_mov_b32 v195, 0\n\t"
            "v_mov_b32 v203, 0\n\t"
            "v_mfma_f32_16x16x32_bf16 v[204:207], v[240:243], v[184:187], v[204:207]\n\t"
            "v_mfma_f32_16x16x32_bf16 v[208:211], v[240:243], v[192:195], v[208:211]\n\t"
            "v_mfma_f32_16x16x32_bf16 v[212:215], v[240:243], v[200:203], v[212:215]\n\t"
            "s_nop 5\n\t"
            "v_rcp_f32 v236, v204\n\t"
            "v_rcp_f32 v237, v208\n\t"
            "v_rcp_f32 v238, v212\n\t"
            "s_waitcnt lgkmcnt(0)\n\t"
            "s_nop 7\n\t"
            "s_branch .Lwa_ok%=\n\t"
            ".Lwa_done%=:\n\t"
            "s_setprio 2\n\t"
            "s_waitcnt lgkmcnt(0)\n\t"
            "s_nop 7\n\t"
            "ds_read_b64 v[156:157], v247\n\t"
            "v_add_u32 v235, s45, v248\n\t"
            "v_xor_b32 v234, s73, v254\n\t"
            "v_mov_b32 v159, 0\n\t"
            "v_mov_b32 v163, 0\n\t"
            "v_mov_b32 v167, 0\n\t"
            "ds_read_b128 v[168:171], v235\n\t"
            "ds_read_u16 v158, v234\n\t"
            "ds_read_b128 v[172:175], v235 offset:1536\n\t"
            "ds_read_b128 v[176:179], v235 offset:3072\n\t"
            "ds_read_b64 v[160:161], v247 offset:3072\n\t"
            "ds_read_u16 v162, v234 offset:256\n\t"
            "ds_read_b64 v[164:165], v247 offset:1536\n\t"
            "ds_read_u16 v166, v234 offset:128\n\t"
            "s_waitcnt lgkmcnt(7)\n\t"
            "v_mfma_f32_16x16x32_bf16 v[228:231], v[168:171], v[180:183], 0\n\t"
            "s_waitcnt lgkmcnt(6)\n\t"
            "v_mfma_f32_16x16x32_bf16 v[228:231], v[156:159], v[184:187], v[228:231]\n\t"
            "s_nop 7\n\t"
            "v_mul_f32 v228, v236, v228\n\t"
            "v_mul_f32 v229, v236, v229\n\t"
            "v_mul_f32 v230, v236, v230\n\t"
            "v_mul_f32 v231, v236, v231\n\t"
            "v_cvt_pk_bf16_f32 v228, v228, v229\n\t"
            "v_cvt_pk_bf16_f32 v229, v230, v231\n\t"
            "ds_write_b64 v245, v[228:229]\n\t"
            "s_waitcnt lgkmcnt(6)\n\t"
            "v_mfma_f32_16x16x32_bf16 v[228:231], v[172:175], v[180:183], 0\n\t"
            "s_waitcnt lgkmcnt(5)\n\t"
            "v_mfma_f32_16x16x32_bf16 v[180:183], v[176:179], v[180:183], 0\n\t"
            "s_waitcnt lgkmcnt(3)\n\t"
            "v_mfma_f32_16x16x32_bf16 v[180:183], v[160:163], v[184:187], v[180:183]\n\t"
            "s_waitcnt lgkmcnt(1)\n\t"
            "v_mfma_f32_16x16x32_bf16 v[228:231], v[164:167], v[184:187], v[228:231]\n\t"
            "v_mfma_f32_16x16x32_bf16 v[184:187], v[168:171], v[188:191], 0\n\t"
            "v_mfma_f32_16x16x32_bf16 v[184:187], v[156:159], v[192:195], v[184:187]\n\t"
            "s_nop 3\n\t"
            "v_mul_f32 v180, v236, v180\n\t"
            "v_mul_f32 v181, v236, v181\n\t"
            "v_mul_f32 v182, v236, v182\n\t"
            "v_mul_f32 v183, v236, v183\n\t"
            "v_cvt_pk_bf16_f32 v180, v180, v181\n\t"
            "v_cvt_pk_bf16_f32 v181, v182, v183\n\t"
            "v_mul_f32 v228, v236, v228\n\t"
            "v_mul_f32 v229, v236, v229\n\t"
            "v_mul_f32 v230, v236, v230\n\t"
            "v_mul_f32 v231, v236, v231\n\t"
            "v_cvt_pk_bf16_f32 v228, v228, v229\n\t"
            "v_cvt_pk_bf16_f32 v229, v230, v231\n\t"
            "v_mul_f32 v184, v237, v184\n\t"
            "v_mul_f32 v185, v237, v185\n\t"
            "v_mul_f32 v186, v237, v186\n\t"
            "v_mul_f32 v187, v237, v187\n\t"
            "v_cvt_pk_bf16_f32 v184, v184, v185\n\t"
            "v_cvt_pk_bf16_f32 v185, v186, v187\n\t"
            "ds_read_b64 v[186:187], v253 offset:352\n\t"
            "ds_write_b64 v245, v[180:181] offset:256\n\t"
            "v_mfma_f32_16x16x32_bf16 v[180:183], v[168:171], v[196:199], 0\n\t"
            "ds_write_b64 v245, v[228:229] offset:128\n\t"
            "v_mfma_f32_16x16x32_bf16 v[180:183], v[156:159], v[200:203], v[180:183]\n\t"
            "ds_write_b64 v245, v[184:185] offset:6144\n\t"
            "v_mfma_f32_16x16x32_bf16 v[228:231], v[172:175], v[188:191], 0\n\t"
            "ds_read_b64 v[184:185], v253 offset:320\n\t"
            "v_mfma_f32_16x16x32_bf16 v[228:231], v[164:167], v[192:195], v[228:231]\n\t"
            "v_mfma_f32_16x16x32_bf16 v[188:191], v[176:179], v[188:191], 0\n\t"
            "v_mfma_f32_16x16x32_bf16 v[188:191], v[160:163], v[192:195], v[188:191]\n\t"
            "s_nop 1\n\t"
            "v_mul_f32 v180, v238, v180\n\t"
            "v_mul_f32 v181, v238, v181\n\t"
            "v_mul_f32 v182, v238, v182\n\t"
            "v_mul_f32 v183, v238, v183\n\t"
            "v_cvt_pk_bf16_f32 v180, v180, v181\n\t"
            "v_cvt_pk_bf16_f32 v181, v182, v183\n\t"
            "v_mul_f32 v228, v237, v228\n\t"
            "v_mul_f32 v229, v237, v229\n\t"
            "v_mul_f32 v230, v237, v230\n\t"
            "v_mul_f32 v231, v237, v231\n\t"
            "v_cvt_pk_bf16_f32 v228, v228, v229\n\t"
            "v_cvt_pk_bf16_f32 v229, v230, v231\n\t"
            "v_mul_f32 v188, v237, v188\n\t"
            "v_mul_f32 v189, v237, v189\n\t"
            "v_mul_f32 v190, v237, v190\n\t"
            "v_mul_f32 v191, v237, v191\n\t"
            "v_cvt_pk_bf16_f32 v188, v188, v189\n\t"
            "v_cvt_pk_bf16_f32 v189, v190, v191\n\t"
            "ds_read_b64 v[182:183], v253 offset:288\n\t"
            "ds_write_b64 v245, v[180:181] offset:12288\n\t"
            "ds_read_b64 v[180:181], v253 offset:256\n\t"
            "ds_write_b64 v245, v[228:229] offset:6272\n\t"
            "v_mfma_f32_16x16x32_bf16 v[228:231], v[172:175], v[196:199], 0\n\t"
            "ds_write_b64 v245, v[188:189] offset:6400\n\t"
            "v_mfma_f32_16x16x32_bf16 v[228:231], v[164:167], v[200:203], v[228:231]\n\t"
            "v_mfma_f32_16x16x32_bf16 v[196:199], v[176:179], v[196:199], 0\n\t"
            "v_mfma_f32_16x16x32_bf16 v[196:199], v[160:163], v[200:203], v[196:199]\n\t"
            "s_nop 5\n\t"
            "v_mul_f32 v228, v238, v228\n\t"
            "s_waitcnt lgkmcnt(2)\n\t"
            "v_mfma_f32_16x16x32_bf16 v[168:171], v[168:171], v[180:183], 0\n\t"
            "v_mfma_f32_16x16x32_bf16 v[172:175], v[172:175], v[180:183], 0\n\t"
            "v_mfma_f32_16x16x32_bf16 v[176:179], v[176:179], v[180:183], 0\n\t"
            "v_mul_f32 v229, v238, v229\n\t"
            "v_mfma_f32_16x16x32_bf16 v[168:171], v[156:159], v[184:187], v[168:171]\n\t"
            "v_mul_f32 v230, v238, v230\n\t"
            "v_mul_f32 v231, v238, v231\n\t"
            "v_mfma_f32_16x16x32_bf16 v[172:175], v[164:167], v[184:187], v[172:175]\n\t"
            "v_cvt_pk_bf16_f32 v228, v228, v229\n\t"
            "v_cvt_pk_bf16_f32 v229, v230, v231\n\t"
            "v_mfma_f32_16x16x32_bf16 v[176:179], v[160:163], v[184:187], v[176:179]\n\t"
            "ds_write_b64 v245, v[228:229] offset:12416\n\t"
            "v_mfma_f32_16x16x32_bf16 v[228:231], v[240:243], v[180:183], 0\n\t"
            "v_mul_f32 v196, v238, v196\n\t"
            "v_mul_f32 v197, v238, v197\n\t"
            "v_mfma_f32_16x16x32_bf16 v[228:231], v[240:243], v[184:187], v[228:231]\n\t"
            "v_mul_f32 v198, v238, v198\n\t"
            "v_mul_f32 v199, v238, v199\n\t"
            "v_cvt_pk_bf16_f32 v196, v196, v197\n\t"
            "v_cvt_pk_bf16_f32 v197, v198, v199\n\t"
            "ds_write_b64 v245, v[196:197] offset:12544\n\t"
            "s_nop 2\n\t"
            "v_rcp_f32 v229, v228\n\t"
            "s_nop 0\n\t"
            "v_mul_f32 v168, v229, v168\n\t"
            "v_mul_f32 v169, v229, v169\n\t"
            "v_mul_f32 v172, v229, v172\n\t"
            "v_mul_f32 v173, v229, v173\n\t"
            "v_mul_f32 v176, v229, v176\n\t"
            "v_mul_f32 v177, v229, v177\n\t"
            "v_mul_f32 v170, v229, v170\n\t"
            "v_mul_f32 v171, v229, v171\n\t"
            "v_cvt_pk_bf16_f32 v168, v168, v169\n\t"
            "v_mul_f32 v174, v229, v174\n\t"
            "v_mul_f32 v175, v229, v175\n\t"
            "v_cvt_pk_bf16_f32 v172, v172, v173\n\t"
            "v_mul_f32 v178, v229, v178\n\t"
            "v_mul_f32 v179, v229, v179\n\t"
            "v_cvt_pk_bf16_f32 v176, v176, v177\n\t"
            "v_xor_b32 v230, s73, v255\n\t"
            "v_cvt_pk_bf16_f32 v169, v170, v171\n\t"
            "v_cvt_pk_bf16_f32 v173, v174, v175\n\t"
            "v_cvt_pk_bf16_f32 v177, v178, v179\n\t"
            "s_mov_b64 exec, s[68:69]\n\t"
            "ds_write_b64 v230, v[168:169]\n\t"
            "ds_write_b64 v230, v[172:173] offset:128\n\t"
            "ds_write_b64 v230, v[176:177] offset:256\n\t"
            "s_mov_b64 exec, -1\n\t"
            "s_waitcnt lgkmcnt(0)\n\t"
            "v_mov_b32 v144, s42\n\t"
            "v_bfrev_b32 v145, -2\n\t"
            "ds_write_b32 v144, v145\n\t"
            "s_waitcnt lgkmcnt(0)\n\t"
            "s_nop 7\n\t"
            : "+{v[232:235]}"(pl), "+{v249}"(aRSw_),
              "+{s44}"(s44), "+{s48}"(s48), "+{s70}"(s70), "+{s72}"(s72), "+{s73}"(s73), "+{s76}"(s76), "+{s77}"(s77)
            : "{v244}"(pE_), "{v245}"(aO_), "{v246}"(aZ_), "{v247}"(aVB_), "{v248}"(pST_), "{v250}"(aPLw_), "{v251}"(aRSr_), "{v252}"(g16_), "{v253}"(aPLr_), "{v254}"(loVL_), "{v255}"(loOL_), "{v[240:243]}"(ones4), "{s47}"((ldsb + X_OFF) ^ (ldsb + X1_OFF)),
              "{s[58:59]}"(0xFFFFull), "{s[60:61]}"(0xFFFF0000ull), "{s[62:63]}"(0xFFFF00000000ull), "{s[64:65]}"(0xFFFF000000000000ull),
              "{s[66:67]}"(0x1FFFFFFFFFFFFull), "{s[68:69]}"(0x0001000100010001ull), "{s[26:27]}"(0x1111111111111111ull), "{s23}"(64.f), "{s57}"(RS_OFF + 768 + (4 + (w >> 1)) * 192 - RS_BASE), "{s45}"(ldsb + V_OFF + w * 4608),
              "{s[28:31]}"(wr), "{s33}"(wave * 36864), "{s46}"(ldsb + PG_OFF), "{s42}"(ldsb + PG_OFF + w * 4), "{s56}"(ldsb + O_OFF), "{s[36:39]}"(tr), "{s41}"(to), "{s40}"(ldsb + X_OFF + 18432), "{s34}"(SH_OFF + bx * 12288 + w * 3072), "{s35}"(ldsb + O_OFF + w * 3072),
              "{s[50:51]}"(0x2222222222222222ull), "{s[52:53]}"(0x4444444444444444ull), "{s[54:55]}"(0x8888888888888888ull)
            : "s24", "s25", "v0", "v1", "v2", "v3", "v4", "v5", "v6", "v7", "v8", "v9", "v10", "v11", "v12", "v13", "v14", "v15", "v16", "v17", "v18", "v19", "v20", "v21", "v22", "v23", "v24", "v25", "v26", "v27", "v28", "v29", "v30", "v31", "v32", "v33", "v34", "v35", "v36", "v37", "v38", "v39", "v40", "v41", "v42", "v43", "v44", "v45", "v46", "v47", "v48", "v49", "v50", "v51", "v52", "v53", "v54", "v55", "v56", "v57", "v58", "v59", "v60", "v61", "v62", "v63", "v64", "v65", "v66", "v67", "v68", "v69", "v70", "v71", "v72", "v73", "v74", "v75", "v76", "v77", "v78", "v79", "v80", "v81", "v82", "v83", "v84", "v85", "v86", "v87", "v88", "v89", "v90", "v91", "v92", "v93", "v94", "v95", "v96", "v97", "v98", "v99", "v100", "v101", "v102", "v103", "v104", "v105", "v106", "v107", "v108", "v109", "v110", "v111", "v112", "v113", "v114", "v115", "v116", "v117", "v118", "v119", "v120", "v121", "v122", "v123", "v124", "v125", "v126", "v127", "v128", "v129", "v130", "v131", "v132", "v133", "v134", "v135", "v136", "v137", "v138", "v139", "v140", "v141", "v142", "v143", "v144", "v145", "v146", "v147", "v148", "v149", "v150", "v151", "v152", "v153", "v154", "v155", "v156", "v157", "v158", "v159", "v160", "v161", "v162", "v163", "v164", "v165", "v166", "v167", "v168", "v169", "v170", "v171", "v172", "v173", "v174", "v175", "v176", "v177", "v178", "v179", "v180", "v181", "v182", "v183", "v184", "v185", "v186", "v187", "v188", "v189", "v190", "v191", "v192", "v193", "v194", "v195", "v196", "v197", "v198", "v199", "v200", "v201", "v202", "v203", "v204", "v205", "v206", "v207", "v208", "v209", "v210", "v211", "v212", "v213", "v214", "v215", "v216", "v217", "v218", "v219", "v220", "v221", "v222", "v223", "v224", "v225", "v226", "v227", "v228", "v229", "v230", "v231", "v236", "v237", "v238", "v239", "s49", "vcc", "scc", "m0", "memory");
        }
        asm volatile("wk_code_end:");

        if (q0 == 40 && wave == 0) {
#pragma unroll 1
            for (int it = 0; it < SPIN_MAX && __hip_atomic_load((gu32*)((char*)wf + CN_OFF), RLX_AGENT) != (unsigned)GRID; ++it) __builtin_amdgcn_s_sleep(8);
            __hip_atomic_store((gu32*)((char*)wf + EP_OFF), __hip_atomic_load((gu32*)((char*)wf + EP_OFF), RLX_AGENT) + 1u, RLX_AGENT);
        }
    } else {
        ROLE_SETUP
        {
            LANE_SETUP
            const bool isV = wave < 6;
            CONV_CONSTS(0)
            const unsigned aCA_ = ldsb + CT_OFF + lane * 16, rsb = ldsb + RS_BASE, pE = fE - X_OFF;
            const unsigned aRSw = (lane < 48 ? rsb : ldsb + RS_OFF + 2304 - 192) + lane * 4, aRSr = rsb + (lane & 48);
            unsigned pr[3];
#pragma unroll
            for (int k = 0; k < 3; ++k) {
                const int c8 = 12 * (w - 2) + 4 * k + g;
                const int ws = c8 / 6, r6 = c8 - 6 * ws;
                pr[k] = l15 * 384 + (r6 >> 1) * 128 + (((2 * ws + (r6 & 1)) ^ sw) << 4);
            }
            unsigned r242 = isV ? pE : ldsb + O_OFF + pE, r243 = isV ? ldsb + V_OFF + w * 9216 + lane * 16 : (ldsb + O_OFF + pE) ^ 64;
            u32x4 r244 = {isV ? ldsb + V_OFF + w * 9216 + 1024 + lane * 8 : pr[0], isV ? aRSr : pr[1], isV ? aRSw : pr[2], (unsigned)lane * 16};
            const int hd = isV ? 2 * w : 2 * w - 3;
            const unsigned vq = ldsb + OL_OFF + l15 * 384 + (((2 * hd + (g >> 1)) ^ sw) << 4) + 8 * (g & 1);
            u32x4 f1 = {16 + l15 < cnt ? vq + 6144 : ldsb + OL_OFF + 22 * 384 + g * 8, pE, ldsb + V_OFF + hd * 4608 + lane * 16, ldsb + V_OFF + hd * 4608 + 1024 + lane * 8};
            u32x4 f2 = {aRSw, aRSr, rsb + l15 * 4, 0u};
            const unsigned lB = ldsb + LB_OFF(l15 & 7) + (g >> 1) * 1536 + (g & 1) * 768 + 96 * w;
            const unsigned long long ob = (unsigned long long)out, xb = (unsigned long long)((const char*)x - XBIAS), wb = (unsigned long long)wf;
            const u32x4 orr = {(unsigned)ob, (unsigned)(ob >> 32) & 0xffffu, (unsigned)(IMG * IMG * CH * 2), 0x00020000u};
            const u32x4 xr = {(unsigned)xb, (unsigned)(xb >> 32) & 0xffffu, (unsigned)(IMG * IMG * CH * 2 + XBIAS), 0x00020000u};
            const u32x4 wr = {(unsigned)wb, (unsigned)(wb >> 32) & 0xffffu, (unsigned)WS_BYTES, 0x00020000u};
            unsigned s45 = OOB, s46 = OOB, s47 = (en0 & 0x7ffffffu) + 96 * w;
            unsigned s48 = ldsb + X1_OFF, s67 = ldsb + X_OFF, s76 = ldsb + WL_OFF + 4, s56 = en0 >> 30;
            unsigned s21 = SH_OFF + bx * 12288, s44 = ldsb + PG_OFF + w * 4, s70 = isV ? wave * 36864 : (wave - 2) * 36864 + 18432;
            unsigned long long xp = 0, sp = 0;
#pragma unroll
            for (int v = 0; v < 4; ++v) {
                const unsigned r = (CROW >> (8 * v)) & 0xFF;
                xp |= (unsigned long long)XPAT(r) << (16 * v);
                sp |= (unsigned long long)SPAT(r) << (16 * v);
            }
#define B_VIN "{v250}"(pXw_), "{v251}"(pSTw_), "{v252}"(aCA_), "{v253}"(aB_), "{v254}"(aA_), "{v255}"(idx_), "{v[238:241]}"(sel4)
#define B_SIN "{s[40:43]}"(orr), "{s[72:75]}"(xr), "{s39}"(0x401fffffu), "{s[28:31]}"(wr), "{s33}"(wave * 36864), "{s78}"(96 * w), \
              "{s27}"(96 * w - 384), "{s[50:51]}"(xp), "{s[52:53]}"(sp), "{s54}"(CLANE), "{s55}"(CROW & 0x0F0F0F0Fu), "{s[34:35]}"(0x0001000100010001ull), \
              "{s[60:61]}"(0x2222222222222222ull), "{s[62:63]}"(0x4444444444444444ull), "{s[64:65]}"(0x8888888888888888ull), \
              "{s77}"(w == 2 ? 768 : 6 * ROWBYTES + 768), "{s25}"(w == 2 ? 3 * ROWBYTES : OOB), "{s32}"(ldsb + X_OFF + 18432), \
              "{s24}"(ldsb + WL_OFF + 12 + 4 * (cnt - 5)), "{s66}"(ldsb + w * 8192), "{s71}"((ldsb + X_OFF) ^ (ldsb + X1_OFF)), \
              "{s23}"(ldsb + WL_OFF), "{s22}"(ldsb + O_OFF), \
              "{s20}"(6 * ROWBYTES + 2304 + 192 * (w & 1)), "{s19}"(2048 + (w & 1) * 1024)
            asm volatile("" :: B_VIN, "{v242}"(r242), "{v243}"(r243), "{v[244:247]}"(r244), "{v[132:135]}"(f1), "{v[136:139]}"(f2), "v"(lB), "v"(vq));
            asm volatile("" :: B_SIN);
            asm volatile("s_waitcnt vmcnt(9)" ::: "memory");
            unsigned lm = *(const unsigned*)(smem + TP_OFF + 4096 + w * 256 + ((l15 & 7) * 4 + g) * 4);
            lm = l15 < 8 ? lm : OOB + 47;
            const int row = lm & 63;
            u32x4 f0 = {XPAT(row) + (unsigned)(O_OFF - X_OFF), SPAT(row) + (unsigned)(STL_OFF - X_OFF - 18816), lB, vq};
            asm volatile(
            "s_setprio 3\n\t"
            "s_waitcnt lgkmcnt(0)\n\t"
            "s_barrier\n\t"
            "s_nop 7\n\t"
            "ds_read_b128 v[168:171], v254\n\t"
            "v_mov_b64 v[176:177], 0\n\t"
            "v_mov_b64 v[178:179], 0\n\t"
            "v_mov_b64 v[180:181], 0\n\t"
            "v_mov_b64 v[182:183], 0\n\t"
            "v_mov_b64 v[184:185], 0\n\t"
            "v_mov_b64 v[186:187], 0\n\t"
            "v_mov_b64 v[188:189], 0\n\t"
            "v_mov_b64 v[190:191], 0\n\t"
            "v_add_u32 v236, s67, v128\n\t"
            "v_xor_b32 v237, 16, v236\n\t"
            "v_add_u32 v248, s67, v129\n\t"
            "ds_read_b64 v[144:145], v130\n\t"
            "ds_read_b64 v[146:147], v130 offset:384\n\t"
            "ds_read_b64 v[148:149], v130 offset:3072\n\t"
            "ds_read_b64 v[150:151], v130 offset:3456\n\t"
            "ds_read_b128 v[172:175], v254 offset:64\n\t"
            "ds_read_b64 v[152:153], v130 offset:8\n\t"
            "ds_read_b64 v[154:155], v130 offset:392\n\t"
            "ds_read_b64 v[156:157], v130 offset:3080\n\t"
            "ds_read_b64 v[158:159], v130 offset:3464\n\t"
            "ds_read_b64 v[160:161], v130 offset:16\n\t"
            "ds_read_b64 v[162:163], v130 offset:400\n\t"
            "ds_read_b64 v[164:165], v130 offset:3088\n\t"
            "ds_read_b64 v[166:167], v130 offset:3472\n\t"
            "s_waitcnt lgkmcnt(9)\n\t"
            "v_smfmac_f32_16x16x64_bf16 v[176:179], v[168:171], v[144:151], v255\n\t"
            "ds_read_b128 v[168:171], v254 offset:128\n\t"
            "ds_read_b64 v[144:145], v130 offset:24\n\t"
            "ds_read_b64 v[146:147], v130 offset:408\n\t"
            "ds_read_b64 v[148:149], v130 offset:3096\n\t"
            "s_waitcnt lgkmcnt(8)\n\t"
            "v_smfmac_f32_16x16x64_bf16 v[180:183], v[172:175], v[152:159], v255\n\t"
            "ds_read_b64 v[150:151], v130 offset:3480\n\t"
            "ds_read_b128 v[172:175], v254 offset:192\n\t"
            "ds_read_b64 v[152:153], v130 offset:32\n\t"
            "v_cvt_pk_bf16_f32 v176, v176, v177\n\t"
            "v_cvt_pk_bf16_f32 v177, v178, v179\n\t"
            "s_nop 1\n\t"
            "v_mfma_f32_4x4x4_16b_bf16 v[196:199], v[176:177], v[176:177], 0\n\t"
            "ds_read_b64 v[154:155], v130 offset:416\n\t"
            "ds_read_b64 v[156:157], v130 offset:3104\n\t"
            "v_cvt_pk_bf16_f32 v178, v180, v181\n\t"
            "v_cvt_pk_bf16_f32 v179, v182, v183\n\t"
            "v_mov_b64 v[180:181], 0\n\t"
            "v_mov_b64 v[182:183], 0\n\t"
            "v_mfma_f32_16x16x32_bf16 v[192:195], v[238:241], v[176:179], 0\n\t"
            "ds_read_b64 v[158:159], v130 offset:3488\n\t"
            "s_waitcnt lgkmcnt(9)\n\t"
            "v_smfmac_f32_16x16x64_bf16 v[184:187], v[168:171], v[160:167], v255\n\t"
            "ds_read_b128 v[168:171], v254 offset:256\n\t"
            "v_mfma_f32_4x4x4_16b_bf16 v[196:199], v[178:179], v[178:179], v[196:199]\n\t"
            "ds_read_b64 v[160:161], v130 offset:40\n\t"
            "ds_read_b64 v[162:163], v130 offset:424\n\t"
            "ds_read_b64 v[164:165], v130 offset:3112\n\t"
            "ds_read_b64 v[166:167], v130 offset:3496\n\t"
            "s_waitcnt lgkmcnt(9)\n\t"
            "v_smfmac_f32_16x16x64_bf16 v[188:191], v[172:175], v[144:151], v255\n\t"
            "ds_read_b128 v[172:175], v254 offset:320\n\t"
            "v_cvt_pk_bf16_f32 v184, v184, v185\n\t"
            "v_cvt_pk_bf16_f32 v185, v186, v187\n\t"
            "s_nop 1\n\t"
            "v_mfma_f32_4x4x4_16b_bf16 v[196:199], v[184:185], v[184:185], v[196:199]\n\t"
            "ds_read_b64 v[144:145], v130 offset:48\n\t"
            "ds_read_b64 v[146:147], v130 offset:432\n\t"
            "ds_read_b64 v[148:149], v130 offset:3120\n\t"
            "ds_read_b64 v[150:151], v130 offset:3504\n\t"
            "v_cvt_pk_bf16_f32 v186, v188, v189\n\t"
            "v_cvt_pk_bf16_f32 v187, v190, v191\n\t"
            "v_mov_b64 v[188:189], 0\n\t"
            "v_mov_b64 v[190:191], 0\n\t"
            "v_mfma_f32_16x16x32_bf16 v[192:195], v[238:241], v[184:187], v[192:195]\n\t"
            "ds_write_b128 v236, v[176:179]\n\t"
            "v_mfma_f32_4x4x4_16b_bf16 v[196:199], v[186:187], v[186:187], v[196:199]\n\t"
            "v_mov_b64 v[176:177], 0\n\t"
            "v_mov_b64 v[178:179], 0\n\t"
            "s_waitcnt lgkmcnt(10)\n\t"
            "s_nop 0\n\t"
            "v_smfmac_f32_16x16x64_bf16 v[176:179], v[168:171], v[152:159], v255\n\t"
            "ds_read_b128 v[168:171], v254 offset:384\n\t"
            "s_waitcnt lgkmcnt(6)\n\t"
            "v_smfmac_f32_16x16x64_bf16 v[180:183], v[172:175], v[160:167], v255\n\t"
            "ds_read_b64 v[152:153], v130 offset:56\n\t"
            "ds_read_b64 v[154:155], v130 offset:440\n\t"
            "ds_read_b64 v[156:157], v130 offset:3128\n\t"
            "ds_read_b64 v[158:159], v130 offset:3512\n\t"
            "ds_read_b128 v[172:175], v254 offset:448\n\t"
            "ds_read_b64 v[160:161], v130 offset:64\n\t"
            "v_cvt_pk_bf16_f32 v176, v176, v177\n\t"
            "v_cvt_pk_bf16_f32 v177, v178, v179\n\t"
            "v_cvt_pk_bf16_f32 v178, v180, v181\n\t"
            "v_cvt_pk_bf16_f32 v179, v182, v183\n\t"
            "v_mfma_f32_4x4x4_16b_bf16 v[196:199], v[176:177], v[176:177], v[196:199]\n\t"
            "ds_read_b64 v[162:163], v130 offset:448\n\t"
            "v_mfma_f32_16x16x32_bf16 v[192:195], v[238:241], v[176:179], v[192:195]\n\t"
            "ds_read_b64 v[164:165], v130 offset:3136\n\t"
            "v_mov_b64 v[180:181], 0\n\t"
            "v_mfma_f32_4x4x4_16b_bf16 v[196:199], v[178:179], v[178:179], v[196:199]\n\t"
            "ds_read_b64 v[166:167], v130 offset:3520\n\t"
            "v_mov_b64 v[182:183], 0\n\t"
            "s_waitcnt lgkmcnt(14)\n\t"
            "ds_write_b128 v237, v[184:187]\n\t"
            "v_mov_b64 v[184:185], 0\n\t"
            "v_mov_b64 v[186:187], 0\n\t"
            "s_waitcnt lgkmcnt(10)\n\t"
            "s_nop 0\n\t"
            "v_smfmac_f32_16x16x64_bf16 v[184:187], v[168:171], v[144:151], v255\n\t"
            "ds_read_b128 v[168:171], v254 offset:512\n\t"
            "s_waitcnt lgkmcnt(6)\n\t"
            "v_smfmac_f32_16x16x64_bf16 v[188:191], v[172:175], v[152:159], v255\n\t"
            "ds_read_b64 v[144:145], v130 offset:72\n\t"
            "ds_read_b64 v[146:147], v130 offset:456\n\t"
            "ds_read_b64 v[148:149], v130 offset:3144\n\t"
            "ds_read_b64 v[150:151], v130 offset:3528\n\t"
            "ds_read_b128 v[172:175], v254 offset:576\n\t"
            "ds_read_b64 v[152:153], v130 offset:80\n\t"
            "v_cvt_pk_bf16_f32 v184, v184, v185\n\t"
            "v_cvt_pk_bf16_f32 v185, v186, v187\n\t"
            "v_cvt_pk_bf16_f32 v186, v188, v189\n\t"
            "v_cvt_pk_bf16_f32 v187, v190, v191\n\t"
            "v_mfma_f32_4x4x4_16b_bf16 v[196:199], v[184:185], v[184:185], v[196:199]\n\t"
            "ds_read_b64 v[154:155], v130 offset:464\n\t"
            "v_mfma_f32_16x16x32_bf16 v[192:195], v[238:241], v[184:187], v[192:195]\n\t"
            "ds_read_b64 v[156:157], v130 offset:3152\n\t"
            "v_mov_b64 v[188:189], 0\n\t"
            "v_mfma_f32_4x4x4_16b_bf16 v[196:199], v[186:187], v[186:187], v[196:199]\n\t"
            "ds_read_b64 v[158:159], v130 offset:3536\n\t"
            "v_mov_b64 v[190:191], 0\n\t"
            "s_waitcnt lgkmcnt(14)\n\t"
            "ds_write_b128 v236, v[176:179] offset:128\n\t"
            "v_mov_b64 v[176:177], 0\n\t"
            "v_mov_b64 v[178:179], 0\n\t"
            "s_waitcnt lgkmcnt(10)\n\t"
            "s_nop 0\n\t"
            "v_smfmac_f32_16x16x64_bf16 v[176:179], v[168:171], v[160:167], v255\n\t"
            "ds_read_b128 v[168:171], v254 offset:640\n\t"
            "s_waitcnt lgkmcnt(6)\n\t"
            "v_smfmac_f32_16x16x64_bf16 v[180:183], v[172:175], v[144:151], v255\n\t"
            "ds_read_b64 v[160:161], v130 offset:88\n\t"
            "ds_read_b64 v[162:163], v130 offset:472\n\t"
            "ds_read_b64 v[164:165], v130 offset:3160\n\t"
            "ds_read_b64 v[166:167], v130 offset:3544\n\t"
            "ds_read_b128 v[172:175], v254 offset:704\n\t"
            "ds_write_b128 v237, v[184:187] offset:128\n\t"
            "v_mov_b64 v[184:185], 0\n\t"
            "v_mov_b64 v[186:187], 0\n\t"
            "v_cvt_pk_bf16_f32 v176, v176, v177\n\t"
            "v_cvt_pk_bf16_f32 v177, v178, v179\n\t"
            "v_cvt_pk_bf16_f32 v178, v180, v181\n\t"
            "v_cvt_pk_bf16_f32 v179, v182, v183\n\t"
            "v_mfma_f32_4x4x4_16b_bf16 v[196:199], v[176:177], v[176:177], v[196:199]\n\t"
            "ds_write_b128 v236, v[176:179] offset:256\n\t"
            "s_waitcnt lgkmcnt(7)\n\t"
            "v_smfmac_f32_16x16x64_bf16 v[184:187], v[168:171], v[152:159], v255\n\t"
            "v_mfma_f32_4x4x4_16b_bf16 v[196:199], v[178:179], v[178:179], v[196:199]\n\t"
            "v_mfma_f32_16x16x32_bf16 v[192:195], v[238:241], v[176:179], v[192:195]\n\t"
            "s_waitcnt lgkmcnt(2)\n\t"
            "v_smfmac_f32_16x16x64_bf16 v[188:191], v[172:175], v[160:167], v255\n\t"
            "s_nop 3\n\t"
            "v_cvt_pk_bf16_f32 v184, v184, v185\n\t"
            "v_cvt_pk_bf16_f32 v185, v186, v187\n\t"
            "s_nop 1\n\t"
            "v_mfma_f32_4x4x4_16b_bf16 v[196:199], v[184:185], v[184:185], v[196:199]\n\t"
            "v_cvt_pk_bf16_f32 v186, v188, v189\n\t"
            "v_cvt_pk_bf16_f32 v187, v190, v191\n\t"
            "s_nop 1\n\t"
            "v_mfma_f32_4x4x4_16b_bf16 v[196:199], v[186:187], v[186:187], v[196:199]\n\t"
            "ds_write_b128 v237, v[184:187] offset:256\n\t"
            "v_mfma_f32_16x16x32_bf16 v[192:195], v[238:241], v[184:187], v[192:195]\n\t"
            "s_nop 7\n\t"
            "v_cndmask_b32_e64 v193, v196, v197, s[60:61]\n\t"
            "v_cndmask_b32_e64 v193, v193, v198, s[62:63]\n\t"
            "v_cndmask_b32_e64 v193, v193, v199, s[64:65]\n\t"
            "ds_write_b64 v248, v[192:193] offset:18816\n\t"
            "s_waitcnt lgkmcnt(0)\n\t"
            "s_nop 7\n\t"
            "v_mov_b32 v144, s44\n\t"
            "v_mov_b32 v145, 0x0fffffff\n\t"
            "ds_write_b32 v144, v145\n\t"
            "s_nop 7\n\t"
            "ds_read_b128 v[168:171], v254\n\t"
            "s_waitcnt vmcnt(0)\n\t"
            "ds_read_b64 v[144:145], v253\n\t"
            "v_mov_b64 v[176:177], 0\n\t"
            "v_mov_b64 v[178:179], 0\n\t"
            "v_mov_b64 v[180:181], 0\n\t"
            "v_mov_b64 v[182:183], 0\n\t"
            "v_mov_b32 v249, s76\n\t"
            "v_mov_b64 v[184:185], 0\n\t"
            "v_mov_b64 v[186:187], 0\n\t"
            "v_mov_b64 v[188:189], 0\n\t"
            "v_mov_b64 v[190:191], 0\n\t"
            "v_add_u32 v236, s67, v250\n\t"
            "v_add_u32 v248, s67, v251\n\t"
            "ds_read_b64 v[146:147], v253 offset:96\n\t"
            "ds_read_b64 v[148:149], v253 offset:1744\n\t"
            "ds_read_b64 v[150:151], v253 offset:1840\n\t"
            "ds_read_b128 v[172:175], v254 offset:64\n\t"
            "ds_read_b64 v[152:153], v253 offset:8\n\t"
            "ds_read_b64 v[154:155], v253 offset:104\n\t"
            "ds_read_b64 v[156:157], v253 offset:1752\n\t"
            "ds_read_b64 v[158:159], v253 offset:1848\n\t"
            "ds_read_b64 v[160:161], v253 offset:16\n\t"
            "ds_read_b64 v[162:163], v253 offset:112\n\t"
            "ds_read_b64 v[164:165], v253 offset:1760\n\t"
            "ds_read_b64 v[166:167], v253 offset:1856\n\t"
            "ds_read_b32 v249, v249\n\t"
            "s_waitcnt lgkmcnt(10)\n\t"
            "v_smfmac_f32_16x16x64_bf16 v[176:179], v[168:171], v[144:151], v255\n\t"
            "ds_read_b128 v[168:171], v254 offset:128\n\t"
            "ds_read_b64 v[144:145], v253 offset:24\n\t"
            "ds_read_b64 v[146:147], v253 offset:120\n\t"
            "ds_read_b64 v[148:149], v253 offset:1768\n\t"
            "s_waitcnt lgkmcnt(9)\n\t"
            "v_smfmac_f32_16x16x64_bf16 v[180:183], v[172:175], v[152:159], v255\n\t"
            "ds_read_b64 v[150:151], v253 offset:1864\n\t"
            "ds_read_b128 v[172:175], v254 offset:192\n\t"
            "ds_read_b64 v[152:153], v253 offset:32\n\t"
            "v_cvt_pk_bf16_f32 v176, v176, v177\n\t"
            "v_cvt_pk_bf16_f32 v177, v178, v179\n\t"
            "s_waitcnt lgkmcnt(7)\n\t"
            "v_readfirstlane_b32 s26, v249\n\t"
            "v_mfma_f32_4x4x4_16b_bf16 v[196:199], v[176:177], v[176:177], 0\n\t"
            "ds_read_b64 v[154:155], v253 offset:128\n\t"
            "s_and_b32 s68, s26, 0x7ffffff\n\t"
            "s_add_u32 s68, s68, s78\n\t"
            "s_and_b32 s69, s26, 0x38000000\n\t"
            "s_or_b32 s69, s69, s39\n\t"
            "s_bitcmp1_b32 s26, 27\n\t"
            "s_cselect_b32 s79, 2.0, s68\n\t"
            "ds_read_b64 v[156:157], v253 offset:1776\n\t"
            "s_and_b32 s57, s56, 3\n\t"
            "s_lshl_b32 s58, s57, 3\n\t"
            "s_lshr_b32 m0, s54, s58\n\t"
            "s_waitcnt lgkmcnt(8)\n\t"
            "v_smfmac_f32_16x16x64_bf16 v[184:187], v[168:171], v[160:167], v255\n\t"
            "s_lshl_b32 s58, s57, 4\n\t"
            "s_lshr_b64 s[36:37], s[50:51], s58\n\t"
            "s_and_b32 s57, s36, 0xffff\n\t"
            "s_add_u32 s57, s57, s67\n\t"
            "ds_read_b64 v[158:159], v253 offset:1872\n\t"
            "s_lshr_b64 s[36:37], s[52:53], s58\n\t"
            "s_and_b32 s58, s36, 0xffff\n\t"
            "s_add_u32 s58, s58, s67\n\t"
            "ds_read_b128 v[168:171], v254 offset:256\n\t"
            "s_and_b32 s59, s50, 0xffff\n\t"
            "s_add_u32 s59, s59, s67\n\t"
            "s_and_b32 s38, s52, 0xffff\n\t"
            "s_add_u32 s38, s38, s67\n\t"
            "s_waitcnt lgkmcnt(5)\n\t"
            "v_smfmac_f32_16x16x64_bf16 v[188:191], v[172:175], v[144:151], v255\n\t"
            "ds_read_b64 v[160:161], v253 offset:40\n\t"
            "v_writelane_b32 v236, s59, m0\n\t"
            "v_writelane_b32 v236, s57, 63\n\t"
            "v_cvt_pk_bf16_f32 v178, v180, v181\n\t"
            "v_cvt_pk_bf16_f32 v179, v182, v183\n\t"
            "v_mov_b64 v[180:181], 0\n\t"
            "v_mov_b64 v[182:183], 0\n\t"
            "v_mfma_f32_16x16x32_bf16 v[192:195], v[238:241], v[176:179], 0\n\t"
            "ds_read_b64 v[162:163], v253 offset:136\n\t"
            "v_cvt_pk_bf16_f32 v184, v184, v185\n\t"
            "v_mfma_f32_4x4x4_16b_bf16 v[196:199], v[178:179], v[178:179], v[196:199]\n\t"
            "ds_read_b64 v[164:165], v253 offset:1784\n\t"
            "v_cvt_pk_bf16_f32 v185, v186, v187\n\t"
            "v_xor_b32 v237, 16, v236\n\t"
            "v_cvt_pk_bf16_f32 v186, v188, v189\n\t"
            "v_mfma_f32_4x4x4_16b_bf16 v[196:199], v[184:185], v[184:185], v[196:199]\n\t"
            "ds_read_b64 v[166:167], v253 offset:1880\n\t"
            "v_cvt_pk_bf16_f32 v187, v190, v191\n\t"
            "v_mov_b64 v[188:189], 0\n\t"
            "v_mov_b64 v[190:191], 0\n\t"
            "v_mfma_f32_16x16x32_bf16 v[192:195], v[238:241], v[184:187], v[192:195]\n\t"
            "ds_read_b128 v[172:175], v254 offset:320\n\t"
            "v_writelane_b32 v248, s38, m0\n\t"
            "v_mfma_f32_4x4x4_16b_bf16 v[196:199], v[186:187], v[186:187], v[196:199]\n\t"
            "ds_read_b64 v[144:145], v253 offset:48\n\t"
            "v_writelane_b32 v248, s58, 63\n\t"
            "ds_read_b64 v[146:147], v253 offset:144\n\t"
            "ds_read_b64 v[148:149], v253 offset:1792\n\t"
            "ds_read_b64 v[150:151], v253 offset:1888\n\t"
            "ds_write_b128 v236, v[176:179]\n\t"
            "v_mov_b64 v[176:177], 0\n\t"
            "v_mov_b64 v[178:179], 0\n\t"
            "s_waitcnt lgkmcnt(10)\n\t"
            "s_nop 0\n\t"
            "v_smfmac_f32_16x16x64_bf16 v[176:179], v[168:171], v[152:159], v255\n\t"
            "ds_read_b128 v[168:171], v254 offset:384\n\t"
            "ds_read_b64 v[152:153], v253 offset:56\n\t"
            "ds_read_b64 v[154:155], v253 offset:152\n\t"
            "s_waitcnt lgkmcnt(8)\n\t"
            "v_smfmac_f32_16x16x64_bf16 v[180:183], v[172:175], v[160:167], v255\n\t"
            "ds_read_b64 v[156:157], v253 offset:1800\n\t"
            "ds_read_b64 v[158:159], v253 offset:1896\n\t"
            "ds_read_b128 v[172:175], v254 offset:448\n\t"
            "ds_read_b64 v[160:161], v253 offset:64\n\t"
            "v_cvt_pk_bf16_f32 v176, v176, v177\n\t"
            "v_cvt_pk_bf16_f32 v177, v178, v179\n\t"
            "s_nop 1\n\t"
            "v_mfma_f32_4x4x4_16b_bf16 v[196:199], v[176:177], v[176:177], v[196:199]\n\t"
            "ds_read_b64 v[162:163], v253 offset:160\n\t"
            "v_cvt_pk_bf16_f32 v178, v180, v181\n\t"
            "v_cvt_pk_bf16_f32 v179, v182, v183\n\t"
            "v_mov_b64 v[180:181], 0\n\t"
            "v_mov_b64 v[182:183], 0\n\t"
            "v_mfma_f32_16x16x32_bf16 v[192:195], v[238:241], v[176:179], v[192:195]\n\t"
            "ds_read_b64 v[164:165], v253 offset:1808\n\t"
            "v_mfma_f32_4x4x4_16b_bf16 v[196:199], v[178:179], v[178:179], v[196:199]\n\t"
            "ds_read_b64 v[166:167], v253 offset:1904\n\t"
            "s_waitcnt lgkmcnt(14)\n\t"
            "ds_write_b128 v237, v[184:187]\n\t"
            "v_mov_b64 v[184:185], 0\n\t"
            "v_mov_b64 v[186:187], 0\n\t"
            "s_waitcnt lgkmcnt(10)\n\t"
            "s_nop 0\n\t"
            "v_smfmac_f32_16x16x64_bf16 v[184:187], v[168:171], v[144:151], v255\n\t"
            "ds_read_b128 v[168:171], v254 offset:512\n\t"
            "s_waitcnt lgkmcnt(6)\n\t"
            "v_smfmac_f32_16x16x64_bf16 v[188:191], v[172:175], v[152:159], v255\n\t"
            "ds_read_b64 v[144:145], v253 offset:72\n\t"
            "ds_read_b64 v[146:147], v253 offset:168\n\t"
            "ds_read_b64 v[148:149], v253 offset:1816\n\t"
            "ds_read_b64 v[150:151], v253 offset:1912\n\t"
            "ds_read_b128 v[172:175], v254 offset:576\n\t"
            "ds_read_b64 v[152:153], v253 offset:80\n\t"
            "v_cvt_pk_bf16_f32 v184, v184, v185\n\t"
            "v_cvt_pk_bf16_f32 v185, v186, v187\n\t"
            "v_cvt_pk_bf16_f32 v186, v188, v189\n\t"
            "v_cvt_pk_bf16_f32 v187, v190, v191\n\t"
            "v_mfma_f32_4x4x4_16b_bf16 v[196:199], v[184:185], v[184:185], v[196:199]\n\t"
            "ds_read_b64 v[154:155], v253 offset:176\n\t"
            "v_mfma_f32_16x16x32_bf16 v[192:195], v[238:241], v[184:187], v[192:195]\n\t"
            "ds_read_b64 v[156:157], v253 offset:1824\n\t"
            "v_mov_b64 v[188:189], 0\n\t"
            "v_mfma_f32_4x4x4_16b_bf16 v[196:199], v[186:187], v[186:187], v[196:199]\n\t"
            "ds_read_b64 v[158:159], v253 offset:1920\n\t"
            "v_mov_b64 v[190:191], 0\n\t"
            "s_waitcnt lgkmcnt(14)\n\t"
            "ds_write_b128 v236, v[176:179] offset:128\n\t"
            "v_mov_b64 v[176:177], 0\n\t"
            "v_mov_b64 v[178:179], 0\n\t"
            "s_waitcnt lgkmcnt(10)\n\t"
            "s_nop 0\n\t"
            "v_smfmac_f32_16x16x64_bf16 v[176:179], v[168:171], v[160:167], v255\n\t"
            "ds_read_b128 v[168:171], v254 offset:640\n\t"
            "s_waitcnt lgkmcnt(6)\n\t"
            "v_smfmac_f32_16x16x64_bf16 v[180:183], v[172:175], v[144:151], v255\n\t"
            "ds_read_b64 v[160:161], v253 offset:88\n\t"
            "ds_read_b64 v[162:163], v253 offset:184\n\t"
            "ds_read_b64 v[164:165], v253 offset:1832\n\t"
            "ds_read_b64 v[166:167], v253 offset:1928\n\t"
            "ds_read_b128 v[172:175], v254 offset:704\n\t"
            "ds_write_b128 v237, v[184:187] offset:128\n\t"
            "v_mov_b64 v[184:185], 0\n\t"
            "v_mov_b64 v[186:187], 0\n\t"
            "v_cvt_pk_bf16_f32 v176, v176, v177\n\t"
            "v_cvt_pk_bf16_f32 v177, v178, v179\n\t"
            "v_cvt_pk_bf16_f32 v178, v180, v181\n\t"
            "v_cvt_pk_bf16_f32 v179, v182, v183\n\t"
            "v_mfma_f32_4x4x4_16b_bf16 v[196:199], v[176:177], v[176:177], v[196:199]\n\t"
            "ds_write_b128 v236, v[176:179] offset:256\n\t"
            "s_waitcnt lgkmcnt(7)\n\t"
            "v_smfmac_f32_16x16x64_bf16 v[184:187], v[168:171], v[152:159], v255\n\t"
            "v_mfma_f32_4x4x4_16b_bf16 v[196:199], v[178:179], v[178:179], v[196:199]\n\t"
            "v_mfma_f32_16x16x32_bf16 v[192:195], v[238:241], v[176:179], v[192:195]\n\t"
            "s_waitcnt lgkmcnt(2)\n\t"
            "v_smfmac_f32_16x16x64_bf16 v[188:191], v[172:175], v[160:167], v255\n\t"
            "s_nop 3\n\t"
            "v_cvt_pk_bf16_f32 v184, v184, v185\n\t"
            "v_cvt_pk_bf16_f32 v185, v186, v187\n\t"
            "s_nop 1\n\t"
            "v_mfma_f32_4x4x4_16b_bf16 v[196:199], v[184:185], v[184:185], v[196:199]\n\t"
            "v_cvt_pk_bf16_f32 v186, v188, v189\n\t"
            "v_cvt_pk_bf16_f32 v187, v190, v191\n\t"
            "s_nop 1\n\t"
            "v_mfma_f32_4x4x4_16b_bf16 v[196:199], v[186:187], v[186:187], v[196:199]\n\t"
            "ds_write_b128 v237, v[184:187] offset:256\n\t"
            "v_mfma_f32_16x16x32_bf16 v[192:195], v[238:241], v[184:187], v[192:195]\n\t"
            "s_nop 7\n\t"
            "v_cndmask_b32_e64 v193, v196, v197, s[60:61]\n\t"
            "v_cndmask_b32_e64 v193, v193, v198, s[62:63]\n\t"
            "v_cndmask_b32_e64 v193, v193, v199, s[64:65]\n\t"
            "ds_write_b64 v248, v[192:193] offset:18816\n\t"
            "s_waitcnt lgkmcnt(0)\n\t"
            "s_nop 7\n\t"
            "v_mov_b32 v144, s44\n\t"
            "v_mov_b32 v145, 0xbfffffff\n\t"
            "ds_write_b32 v144, v145\n\t"
            "s_setprio 1\n\t"
            "v_mov_b32 v144, s23\n\t"
            ".Lgo%=:\n\t"
            "ds_read_b32 v145, v144 offset:124\n\t"
            "s_waitcnt lgkmcnt(0)\n\t"
            "v_cmp_ne_u32 vcc, -1, v145\n\t"
            "s_nop 4\n\t"
            "s_cbranch_vccnz .Lgo%=\n\t"
            "s_nop 7\n\t"
            "ds_read_b32 v249, v252 offset:1024\n\t"
            "s_add_u32 s49, s70, 0\n\t"
            "buffer_load_dwordx4 v[0:3], v247, s[28:31], s49 offen sc1\n\t"
            "s_add_u32 s49, s70, 1024\n\t"
            "buffer_load_dwordx4 v[4:7], v247, s[28:31], s49 offen sc1\n\t"
            "s_add_u32 s49, s70, 2048\n\t"
            "buffer_load_dwordx4 v[8:11], v247, s[28:31], s49 offen sc1\n\t"
            "s_add_u32 s49, s70, 3072\n\t"
            "buffer_load_dwordx4 v[12:15], v247, s[28:31], s49 offen sc1\n\t"
            "s_add_u32 s49, s70, 4096\n\t"
            "buffer_load_dwordx4 v[16:19], v247, s[28:31], s49 offen sc1\n\t"
            "s_add_u32 s49, s70, 5120\n\t"
            "buffer_load_dwordx4 v[20:23], v247, s[28:31], s49 offen sc1\n\t"
            "s_add_u32 s49, s70, 6144\n\t"
            "buffer_load_dwordx4 v[24:27], v247, s[28:31], s49 offen sc1\n\t"
            "s_add_u32 s49, s70, 7168\n\t"
            "buffer_load_dwordx4 v[28:31], v247, s[28:31], s49 offen sc1\n\t"
            "s_add_u32 s49, s70, 8192\n\t"
            "buffer_load_dwordx4 v[32:35], v247, s[28:31], s49 offen sc1\n\t"
            "s_add_u32 s49, s70, 9216\n\t"
            "buffer_load_dwordx4 v[36:39], v247, s[28:31], s49 offen sc1\n\t"
            "s_add_u32 s49, s70, 10240\n\t"
            "buffer_load_dwordx4 v[40:43], v247, s[28:31], s49 offen sc1\n\t"
            "s_add_u32 s49, s70, 11264\n\t"
            "buffer_load_dwordx4 v[44:47], v247, s[28:31], s49 offen sc1\n\t"
            "s_add_u32 s49, s70, 12288\n\t"
            "buffer_load_dwordx4 v[48:51], v247, s[28:31], s49 offen sc1\n\t"
            "s_add_u32 s49, s70, 13312\n\t"
            "buffer_load_dwordx4 v[52:55], v247, s[28:31], s49 offen sc1\n\t"
            "s_add_u32 s49, s70, 14336\n\t"
            "buffer_load_dwordx4 v[56:59], v247, s[28:31], s49 offen sc1\n\t"
            "s_add_u32 s49, s70, 15360\n\t"
            "buffer_load_dwordx4 v[60:63], v247, s[28:31], s49 offen sc1\n\t"
            "s_add_u32 s49, s70, 16384\n\t"
            "buffer_load_dwordx4 v[64:67], v247, s[28:31], s49 offen sc1\n\t"
            "s_add_u32 s49, s70, 17408\n\t"
            "buffer_load_dwordx4 v[68:71], v247, s[28:31], s49 offen sc1\n\t"
            "s_waitcnt lgkmcnt(0)\n\t"
            "v_and_b32 v249, s69, v249\n\t"
            "s_mov_b32 exec_hi, 0x3fffff\n\t"
            "s_add_u32 m0, s66, 0\n\t"
            "s_add_u32 s49, s79, 0\n\t"
            "buffer_load_dwordx4 v249, s[72:75], s49 offen lds\n\t"
            "s_mov_b32 exec_hi, -1\n\t"
            "s_mov_b32 exec_hi, 0x3fffff\n\t"
            "s_add_u32 m0, s66, 872\n\t"
            "s_add_u32 s49, s68, 196224\n\t"
            "buffer_load_dwordx4 v249, s[72:75], s49 offen lds\n\t"
            "s_mov_b32 exec_hi, -1\n\t"
            "s_mov_b32 exec_hi, 0x3fffff\n\t"
            "s_add_u32 m0, s66, 1744\n\t"
            "s_add_u32 s49, s68, 392448\n\t"
            "buffer_load_dwordx4 v249, s[72:75], s49 offen lds\n\t"
            "s_mov_b32 exec_hi, -1\n\t"
            "s_mov_b32 exec_hi, 0x3fffff\n\t"
            "s_add_u32 m0, s66, 2616\n\t"
            "s_add_u32 s49, s68, 588672\n\t"
            "buffer_load_dwordx4 v249, s[72:75], s49 offen lds\n\t"
            "s_mov_b32 exec_hi, -1\n\t"
            "s_mov_b32 exec_hi, 0x3fffff\n\t"
            "s_add_u32 m0, s66, 3488\n\t"
            "s_add_u32 s49, s68, 784896\n\t"
            "buffer_load_dwordx4 v249, s[72:75], s49 offen lds\n\t"
            "s_mov_b32 exec_hi, -1\n\t"
            "s_mov_b32 exec_hi, 0x3fffff\n\t"
            "s_add_u32 m0, s66, 4360\n\t"
            "s_add_u32 s49, s68, 981120\n\t"
            "buffer_load_dwordx4 v249, s[72:75], s49 offen lds\n\t"
            "s_mov_b32 exec_hi, -1\n\t"
            "s_mov_b32 exec_hi, 0x3fffff\n\t"
            "s_add_u32 m0, s66, 5232\n\t"
            "s_add_u32 s49, s68, 1177344\n\t"
            "buffer_load_dwordx4 v249, s[72:75], s49 offen lds\n\t"
            "s_mov_b32 exec_hi, -1\n\t"
            "s_mov_b32 exec_hi, 0x3fffff\n\t"
            "s_add_u32 m0, s66, 6104\n\t"
            "s_add_u32 s49, s68, 1373568\n\t"
            "buffer_load_dwordx4 v249, s[72:75], s49 offen lds\n\t"
            "s_mov_b32 exec_hi, -1\n\t"
            "s_mov_b32 exec_hi, 0x3fffff\n\t"
            "s_add_u32 m0, s66, 6976\n\t"
            "s_add_u32 s49, s68, 1569792\n\t"
            "buffer_load_dwordx4 v249, s[72:75], s49 offen lds\n\t"
            "s_mov_b32 exec_hi, -1\n\t"
            "s_add_u32 s45, s46, s27\n\t"
            "s_mov_b32 s46, s47\n\t"
            "s_mov_b32 s47, s68\n\t"
            "s_xor_b32 s48, s48, s71\n\t"
            "s_xor_b32 s67, s67, s71\n\t"
            "s_add_u32 s76, s76, 4\n\t"
            "s_lshl_b32 s56, s56, 2\n\t"
            "s_lshr_b32 s49, s26, 30\n\t"
            "s_or_b32 s56, s56, s49\n\t"
            "s_cmp_ge_u32 s76, s24\n\t"
            "s_cselect_b64 vcc, -1, 0\n\t"
            "s_nop 7\n\t"
            "v_mov_b32 v144, s23\n\t"
            ".Lpg%=:\n\t"
            "ds_read_b128 v[148:151], v144 offset:96\n\t"
            "s_waitcnt lgkmcnt(0)\n\t"
            "v_and_b32 v148, v148, v149\n\t"
            "v_and_b32 v150, v150, v151\n\t"
            "v_and_b32 v148, v148, v150\n\t"
            "v_cmp_le_i32 vcc, 0, v148\n\t"
            "s_nop 4\n\t"
            "s_cbranch_vccnz .Lpg%=\n\t"
            "s_setprio 3\n\t"
            "s_nop 7\n\t"
            "ds_read_b128 v[168:171], v254\n\t"
            "v_add_u32 v112, s22, v133\n\t"
            "v_xor_b32 v113, 64, v112\n\t"
            "v_add_u32 v114, s22, v247\n\t"
            "v_mov_b64 v[176:177], 0\n\t"
            "v_mov_b64 v[178:179], 0\n\t"
            "v_mov_b64 v[180:181], 0\n\t"
            "v_mov_b64 v[182:183], 0\n\t"
            "v_mov_b32 v249, s76\n\t"
            "v_mov_b64 v[184:185], 0\n\t"
            "v_mov_b64 v[186:187], 0\n\t"
            "v_mov_b64 v[188:189], 0\n\t"
            "v_mov_b64 v[190:191], 0\n\t"
            "v_add_u32 v236, s67, v250\n\t"
            "v_add_u32 v248, s67, v251\n\t"
            "ds_read_b128 v[120:123], v112\n\t"
            "ds_read_b128 v[124:127], v113\n\t"
            "ds_read_b128 v[224:227], v112 offset:128\n\t"
            "ds_read_b128 v[228:231], v114 offset:12288\n\t"
            "ds_read_b128 v[232:235], v114 offset:13312\n\t"
            "s_waitcnt vmcnt(9)\n\t"
            "ds_read_b128 v[172:175], v254 offset:64\n\t"
            "v_add_u32 v114, s48, v247\n\t"
            "ds_read_b32 v249, v249\n\t"
            "s_waitcnt lgkmcnt(6)\n\t"
            "v_mfma_f32_16x16x32_bf16 v[200:203], v[0:3], v[120:123], 0\n\t"
            "v_mfma_f32_16x16x32_bf16 v[204:207], v[24:27], v[120:123], 0\n\t"
            "s_waitcnt lgkmcnt(3)\n\t"
            "v_add_f32 v228, v228, v230\n\t"
            "v_add_f32 v229, v229, v231\n\t"
            "v_mfma_f32_16x16x32_bf16 v[208:211], v[48:51], v[120:123], 0\n\t"
            "ds_read_b128 v[120:123], v113 offset:128\n\t"
            "s_waitcnt lgkmcnt(3)\n\t"
            "v_add_f32 v232, v232, v234\n\t"
            "v_mfma_f32_16x16x32_bf16 v[200:203], v[4:7], v[124:127], v[200:203]\n\t"
            "s_waitcnt lgkmcnt(1)\n\t"
            "v_readfirstlane_b32 s26, v249\n\t"
            "v_add_f32 v228, v228, v232\n\t"
            "v_mfma_f32_16x16x32_bf16 v[204:207], v[28:31], v[124:127], v[204:207]\n\t"
            "s_and_b32 s68, s26, 0x7ffffff\n\t"
            "s_add_u32 s68, s68, s78\n\t"
            "s_and_b32 s69, s26, 0x38000000\n\t"
            "s_or_b32 s69, s69, s39\n\t"
            "s_bitcmp1_b32 s26, 27\n\t"
            "s_cselect_b32 s79, 2.0, s68\n\t"
            "v_mfma_f32_16x16x32_bf16 v[208:211], v[52:55], v[124:127], v[208:211]\n\t"
            "s_and_b32 s57, s56, 3\n\t"
            "s_lshl_b32 s58, s57, 3\n\t"
            "s_lshr_b32 m0, s54, s58\n\t"
            "v_mfma_f32_16x16x32_bf16 v[200:203], v[8:11], v[224:227], v[200:203]\n\t"
            "s_lshl_b32 s58, s57, 4\n\t"
            "s_lshr_b64 s[36:37], s[50:51], s58\n\t"
            "s_and_b32 s57, s36, 0xffff\n\t"
            "s_add_u32 s57, s57, s67\n\t"
            "v_mfma_f32_16x16x32_bf16 v[204:207], v[32:35], v[224:227], v[204:207]\n\t"
            "s_lshr_b64 s[36:37], s[52:53], s58\n\t"
            "s_and_b32 s58, s36, 0xffff\n\t"
            "s_add_u32 s58, s58, s67\n\t"
            "v_mfma_f32_16x16x32_bf16 v[208:211], v[56:59], v[224:227], v[208:211]\n\t"
            "s_and_b32 s59, s50, 0xffff\n\t"
            "s_add_u32 s59, s59, s67\n\t"
            "s_and_b32 s38, s52, 0xffff\n\t"
            "s_add_u32 s38, s38, s67\n\t"
            "s_waitcnt lgkmcnt(0)\n\t"
            "v_mfma_f32_16x16x32_bf16 v[200:203], v[12:15], v[120:123], v[200:203]\n\t"
            "ds_read_b128 v[124:127], v112 offset:256\n\t"
            "v_writelane_b32 v236, s59, m0\n\t"
            "v_mfma_f32_16x16x32_bf16 v[204:207], v[36:39], v[120:123], v[204:207]\n\t"
            "ds_read_b128 v[224:227], v113 offset:256\n\t"
            "v_writelane_b32 v236, s57, 63\n\t"
            "v_mfma_f32_16x16x32_bf16 v[208:211], v[60:63], v[120:123], v[208:211]\n\t"
            "ds_read_b128 v[120:123], v112 offset:6144\n\t"
            "v_xor_b32 v237, 16, v236\n\t"
            "v_add_f32 v233, v233, v235\n\t"
            "v_mul_f32 v228, 0x3baaaaab, v228\n\t"
            "v_add_f32 v229, v229, v233\n\t"
            "v_mul_f32_e64 v228, v228, -v228\n\t"
            "v_fmac_f32 v228, 0x3baaaaab, v229\n\t"
            "v_max_f32 v228, 0, v228\n\t"
            "v_add_f32 v228, 0x3727c5ac, v228\n\t"
            "v_rsq_f32 v228, v228\n\t"
            "v_writelane_b32 v248, s38, m0\n\t"
            "v_writelane_b32 v248, s58, 63\n\t"
            "ds_read_b32 v249, v252 offset:1024\n\t"
            "ds_read_b128 v[232:235], v114 offset:19840\n\t"
            "ds_write_b32 v136, v228\n\t"
            "ds_read_b32 v116, v138\n\t"
            "s_waitcnt lgkmcnt(6)\n\t"
            "v_mfma_f32_16x16x32_bf16 v[200:203], v[16:19], v[124:127], v[200:203]\n\t"
            "ds_read_b128 v[228:231], v114 offset:18816\n\t"
            "v_mfma_f32_16x16x32_bf16 v[204:207], v[40:43], v[124:127], v[204:207]\n\t"
            "ds_read_b32 v117, v138 offset:64\n\t"
            "v_mfma_f32_16x16x32_bf16 v[208:211], v[64:67], v[124:127], v[208:211]\n\t"
            "ds_read_b128 v[124:127], v113 offset:6144\n\t"
            "s_waitcnt lgkmcnt(7)\n\t"
            "v_mfma_f32_16x16x32_bf16 v[220:223], v[48:51], v[120:123], 0\n\t"
            "v_mfma_f32_16x16x32_bf16 v[200:203], v[20:23], v[224:227], v[200:203]\n\t"
            "v_mfma_f32_16x16x32_bf16 v[204:207], v[44:47], v[224:227], v[204:207]\n\t"
            "s_waitcnt lgkmcnt(6)\n\t"
            "v_and_b32 v249, s69, v249\n\t"
            "v_mfma_f32_16x16x32_bf16 v[208:211], v[68:71], v[224:227], v[208:211]\n\t"
            "ds_read_b128 v[224:227], v112 offset:6272\n\t"
            "s_waitcnt lgkmcnt(6)\n\t"
            "v_add_f32 v232, v232, v234\n\t"
            "v_mfma_f32_16x16x32_bf16 v[212:215], v[0:3], v[120:123], 0\n\t"
            "v_add_f32 v233, v233, v235\n\t"
            "v_mfma_f32_16x16x32_bf16 v[216:219], v[24:27], v[120:123], 0\n\t"
            "ds_read_b128 v[120:123], v113 offset:6272\n\t"
            "s_waitcnt lgkmcnt(5)\n\t"
            "v_mul_f32 v200, v116, v200\n\t"
            "v_mul_f32 v201, v116, v201\n\t"
            "v_mul_f32 v204, v116, v204\n\t"
            "v_mul_f32 v205, v116, v205\n\t"
            "v_mul_f32 v208, v116, v208\n\t"
            "v_mul_f32 v209, v116, v209\n\t"
            "s_waitcnt lgkmcnt(2)\n\t"
            "v_mfma_f32_16x16x32_bf16 v[220:223], v[52:55], v[124:127], v[220:223]\n\t"
            "s_waitcnt vmcnt(0)\n\t"
            "ds_read_b64 v[144:145], v253\n\t"
            "v_mfma_f32_16x16x32_bf16 v[212:215], v[4:7], v[124:127], v[212:215]\n\t"
            "ds_read_b64 v[146:147], v253 offset:96\n\t"
            "v_mul_f32 v202, v116, v202\n\t"
            "v_mfma_f32_16x16x32_bf16 v[216:219], v[28:31], v[124:127], v[216:219]\n\t"
            "ds_read_b64 v[148:149], v253 offset:1744\n\t"
            "v_mul_f32 v203, v116, v203\n\t"
            "v_cvt_pk_bf16_f32 v200, v200, v201\n\t"
            "v_mul_f32 v206, v116, v206\n\t"
            "v_mul_f32 v207, v116, v207\n\t"
            "v_cvt_pk_bf16_f32 v204, v204, v205\n\t"
            "s_waitcnt lgkmcnt(4)\n\t"
            "v_mfma_f32_16x16x32_bf16 v[212:215], v[8:11], v[224:227], v[212:215]\n\t"
            "ds_read_b64 v[150:151], v253 offset:1840\n\t"
            "v_mul_f32 v210, v116, v210\n\t"
            "v_mfma_f32_16x16x32_bf16 v[216:219], v[32:35], v[224:227], v[216:219]\n\t"
            "ds_read_b64 v[152:153], v253 offset:8\n\t"
            "v_mul_f32 v211, v116, v211\n\t"
            "v_mfma_f32_16x16x32_bf16 v[220:223], v[56:59], v[224:227], v[220:223]\n\t"
            "ds_read_b64 v[154:155], v253 offset:104\n\t"
            "v_cvt_pk_bf16_f32 v208, v208, v209\n\t"
            "s_waitcnt lgkmcnt(6)\n\t"
            "v_mfma_f32_16x16x32_bf16 v[212:215], v[12:15], v[120:123], v[212:215]\n\t"
            "ds_read_b64 v[156:157], v253 offset:1752\n\t"
            "v_cvt_pk_bf16_f32 v201, v202, v203\n\t"
            "v_mfma_f32_16x16x32_bf16 v[216:219], v[36:39], v[120:123], v[216:219]\n\t"
            "ds_read_b64 v[158:159], v253 offset:1848\n\t"
            "v_cvt_pk_bf16_f32 v205, v206, v207\n\t"
            "v_mfma_f32_16x16x32_bf16 v[220:223], v[60:63], v[120:123], v[220:223]\n\t"
            "ds_read_b64 v[160:161], v253 offset:16\n\t"
            "v_cvt_pk_bf16_f32 v209, v210, v211\n\t"
            "v_add_f32 v228, v228, v230\n\t"
            "v_add_f32 v228, v228, v232\n\t"
            "v_add_f32 v229, v229, v231\n\t"
            "v_mul_f32 v228, 0x3baaaaab, v228\n\t"
            "v_add_f32 v229, v229, v233\n\t"
            "v_mul_f32_e64 v228, v228, -v228\n\t"
            "v_fmac_f32 v228, 0x3baaaaab, v229\n\t"
            "v_max_f32 v228, 0, v228\n\t"
            "v_add_f32 v228, 0x3727c5ac, v228\n\t"
            "s_waitcnt lgkmcnt(5)\n\t"
            "v_smfmac_f32_16x16x64_bf16 v[176:179], v[168:171], v[144:151], v255\n\t"
            "ds_read_b64 v[162:163], v253 offset:112\n\t"
            "v_rsq_f32 v228, v228\n\t"
            "ds_read_b64 v[164:165], v253 offset:1760\n\t"
            "ds_read_b64 v[166:167], v253 offset:1856\n\t"
            "ds_read_b128 v[168:171], v254 offset:128\n\t"
            "ds_read_b64 v[144:145], v253 offset:24\n\t"
            "s_waitcnt lgkmcnt(6)\n\t"
            "v_smfmac_f32_16x16x64_bf16 v[180:183], v[172:175], v[152:159], v255\n\t"
            "ds_read_b64 v[146:147], v253 offset:120\n\t"
            "v_cvt_pk_bf16_f32 v176, v176, v177\n\t"
            "v_cvt_pk_bf16_f32 v177, v178, v179\n\t"
            "s_nop 1\n\t"
            "v_mfma_f32_4x4x4_16b_bf16 v[196:199], v[176:177], v[176:177], 0\n\t"
            "ds_read_b64 v[148:149], v253 offset:1768\n\t"
            "ds_read_b64 v[150:151], v253 offset:1864\n\t"
            "ds_read_b128 v[172:175], v254 offset:192\n\t"
            "ds_read_b64 v[152:153], v253 offset:32\n\t"
            "v_cvt_pk_bf16_f32 v178, v180, v181\n\t"
            "v_cvt_pk_bf16_f32 v179, v182, v183\n\t"
            "v_mov_b64 v[180:181], 0\n\t"
            "v_mov_b64 v[182:183], 0\n\t"
            "v_mfma_f32_16x16x32_bf16 v[192:195], v[238:241], v[176:179], 0\n\t"
            "ds_read_b64 v[154:155], v253 offset:128\n\t"
            "v_mfma_f32_4x4x4_16b_bf16 v[196:199], v[178:179], v[178:179], v[196:199]\n\t"
            "ds_read_b64 v[156:157], v253 offset:1776\n\t"
            "s_waitcnt lgkmcnt(8)\n\t"
            "v_smfmac_f32_16x16x64_bf16 v[184:187], v[168:171], v[160:167], v255\n\t"
            "ds_read_b64 v[158:159], v253 offset:1872\n\t"
            "ds_read_b128 v[168:171], v254 offset:256\n\t"
            "ds_read_b64 v[160:161], v253 offset:40\n\t"
            "ds_read_b64 v[162:163], v253 offset:136\n\t"
            "ds_read_b64 v[164:165], v253 offset:1784\n\t"
            "ds_read_b64 v[166:167], v253 offset:1880\n\t"
            "ds_read_b128 v[124:127], v112 offset:6400\n\t"
            "v_add_u32 v112, s48, v133\n\t"
            "s_waitcnt lgkmcnt(10)\n\t"
            "v_smfmac_f32_16x16x64_bf16 v[188:191], v[172:175], v[144:151], v255\n\t"
            "ds_read_b128 v[172:175], v254 offset:320\n\t"
            "v_cvt_pk_bf16_f32 v184, v184, v185\n\t"
            "v_cvt_pk_bf16_f32 v185, v186, v187\n\t"
            "s_nop 1\n\t"
            "v_mfma_f32_4x4x4_16b_bf16 v[196:199], v[184:185], v[184:185], v[196:199]\n\t"
            "ds_read_b64 v[144:145], v253 offset:48\n\t"
            "ds_read_b64 v[146:147], v253 offset:144\n\t"
            "ds_read_b64 v[148:149], v253 offset:1792\n\t"
            "ds_read_b64 v[150:151], v253 offset:1888\n\t"
            "v_cvt_pk_bf16_f32 v186, v188, v189\n\t"
            "v_cvt_pk_bf16_f32 v187, v190, v191\n\t"
            "v_mov_b64 v[188:189], 0\n\t"
            "v_mov_b64 v[190:191], 0\n\t"
            "v_mfma_f32_16x16x32_bf16 v[192:195], v[238:241], v[184:187], v[192:195]\n\t"
            "s_waitcnt lgkmcnt(14)\n\t"
            "ds_read_b128 v[224:227], v113 offset:6400\n\t"
            "v_xor_b32 v113, 64, v112\n\t"
            "v_mfma_f32_4x4x4_16b_bf16 v[196:199], v[186:187], v[186:187], v[196:199]\n\t"
            "s_waitcnt lgkmcnt(14)\n\t"
            "ds_read_b128 v[120:123], v112\n\t"
            "s_waitcnt lgkmcnt(14)\n\t"
            "ds_write_b128 v236, v[176:179]\n\t"
            "s_waitcnt lgkmcnt(8)\n\t"
            "v_mfma_f32_16x16x32_bf16 v[212:215], v[16:19], v[124:127], v[212:215]\n\t"
            "v_mov_b64 v[176:177], 0\n\t"
            "v_mov_b64 v[178:179], 0\n\t"
            "s_waitcnt lgkmcnt(7)\n\t"
            "v_smfmac_f32_16x16x64_bf16 v[180:183], v[172:175], v[160:167], v255\n\t"
            "ds_read_b128 v[172:175], v254 offset:448\n\t"
            "v_smfmac_f32_16x16x64_bf16 v[176:179], v[168:171], v[152:159], v255\n\t"
            "ds_read_b128 v[168:171], v254 offset:384\n\t"
            "v_mfma_f32_16x16x32_bf16 v[216:219], v[40:43], v[124:127], v[216:219]\n\t"
            "ds_read_b64 v[152:153], v253 offset:56\n\t"
            "v_mfma_f32_16x16x32_bf16 v[220:223], v[64:67], v[124:127], v[220:223]\n\t"
            "ds_read_b64 v[154:155], v253 offset:152\n\t"
            "ds_read_b64 v[156:157], v253 offset:1800\n\t"
            "s_waitcnt lgkmcnt(7)\n\t"
            "v_mfma_f32_16x16x32_bf16 v[212:215], v[20:23], v[224:227], v[212:215]\n\t"
            "ds_read_b64 v[158:159], v253 offset:1896\n\t"
            "v_cvt_pk_bf16_f32 v176, v176, v177\n\t"
            "v_mfma_f32_16x16x32_bf16 v[216:219], v[44:47], v[224:227], v[216:219]\n\t"
            "ds_read_b64 v[160:161], v253 offset:64\n\t"
            "v_cvt_pk_bf16_f32 v177, v178, v179\n\t"
            "v_mfma_f32_16x16x32_bf16 v[220:223], v[68:71], v[224:227], v[220:223]\n\t"
            "ds_read_b64 v[162:163], v253 offset:160\n\t"
            "v_cvt_pk_bf16_f32 v178, v180, v181\n\t"
            "v_mfma_f32_4x4x4_16b_bf16 v[196:199], v[176:177], v[176:177], v[196:199]\n\t"
            "ds_read_b64 v[164:165], v253 offset:1808\n\t"
            "v_cvt_pk_bf16_f32 v179, v182, v183\n\t"
            "v_mov_b64 v[180:181], 0\n\t"
            "v_mov_b64 v[182:183], 0\n\t"
            "v_mfma_f32_16x16x32_bf16 v[192:195], v[238:241], v[176:179], v[192:195]\n\t"
            "ds_read_b64 v[166:167], v253 offset:1904\n\t"
            "v_mul_f32 v212, v117, v212\n\t"
            "v_mfma_f32_4x4x4_16b_bf16 v[196:199], v[178:179], v[178:179], v[196:199]\n\t"
            "ds_read_b128 v[124:127], v113\n\t"
            "v_mul_f32 v213, v117, v213\n\t"
            "v_mul_f32 v216, v117, v216\n\t"
            "v_mul_f32 v217, v117, v217\n\t"
            "v_mul_f32 v220, v117, v220\n\t"
            "v_mul_f32 v221, v117, v221\n\t"
            "v_mul_f32 v214, v117, v214\n\t"
            "v_mul_f32 v215, v117, v215\n\t"
            "v_cvt_pk_bf16_f32 v212, v212, v213\n\t"
            "v_mul_f32 v218, v117, v218\n\t"
            "v_mul_f32 v219, v117, v219\n\t"
            "s_waitcnt lgkmcnt(5)\n\t"
            "v_smfmac_f32_16x16x64_bf16 v[188:191], v[172:175], v[152:159], v255\n\t"
            "ds_read_b128 v[172:175], v254 offset:576\n\t"
            "v_cvt_pk_bf16_f32 v216, v216, v217\n\t"
            "v_mul_f32 v222, v117, v222\n\t"
            "v_mul_f32 v223, v117, v223\n\t"
            "v_cvt_pk_bf16_f32 v220, v220, v221\n\t"
            "v_cvt_pk_bf16_f32 v213, v214, v215\n\t"
            "v_cvt_pk_bf16_f32 v217, v218, v219\n\t"
            "v_cvt_pk_bf16_f32 v221, v222, v223\n\t"
            "ds_read_b64 v[152:153], v253 offset:80\n\t"
            "ds_read_b64 v[154:155], v253 offset:176\n\t"
            "ds_read_b64 v[156:157], v253 offset:1824\n\t"
            "ds_read_b64 v[158:159], v253 offset:1920\n\t"
            "ds_read_b128 v[224:227], v112 offset:128\n\t"
            "ds_write_b128 v237, v[184:187]\n\t"
            "v_mov_b64 v[184:185], 0\n\t"
            "v_mov_b64 v[186:187], 0\n\t"
            "s_nop 1\n\t"
            "v_smfmac_f32_16x16x64_bf16 v[184:187], v[168:171], v[144:151], v255\n\t"
            "ds_read_b128 v[168:171], v254 offset:512\n\t"
            "ds_read_b64 v[144:145], v253 offset:72\n\t"
            "ds_read_b64 v[146:147], v253 offset:168\n\t"
            "s_waitcnt lgkmcnt(14)\n\t"
            "ds_read_b64 v[148:149], v253 offset:1816\n\t"
            "s_waitcnt lgkmcnt(14)\n\t"
            "ds_read_b64 v[150:151], v253 offset:1912\n\t"
            "s_waitcnt lgkmcnt(14)\n\t"
            "ds_write_b128 v236, v[176:179] offset:128\n\t"
            "v_mov_b64 v[176:177], 0\n\t"
            "v_mov_b64 v[178:179], 0\n\t"
            "v_cvt_pk_bf16_f32 v184, v184, v185\n\t"
            "v_cvt_pk_bf16_f32 v185, v186, v187\n\t"
            "v_cvt_pk_bf16_f32 v186, v188, v189\n\t"
            "v_cvt_pk_bf16_f32 v187, v190, v191\n\t"
            "v_mfma_f32_4x4x4_16b_bf16 v[196:199], v[184:185], v[184:185], v[196:199]\n\t"
            "s_waitcnt lgkmcnt(14)\n\t"
            "ds_write_b128 v237, v[184:187] offset:128\n\t"
            "v_mfma_f32_16x16x32_bf16 v[192:195], v[238:241], v[184:187], v[192:195]\n\t"
            "v_mov_b64 v[184:185], 0\n\t"
            "v_mov_b64 v[188:189], 0\n\t"
            "s_waitcnt lgkmcnt(6)\n\t"
            "v_smfmac_f32_16x16x64_bf16 v[176:179], v[168:171], v[160:167], v255\n\t"
            "ds_read_b128 v[168:171], v254 offset:640\n\t"
            "v_mfma_f32_4x4x4_16b_bf16 v[196:199], v[186:187], v[186:187], v[196:199]\n\t"
            "ds_read_b64 v[160:161], v253 offset:88\n\t"
            "v_mov_b64 v[186:187], 0\n\t"
            "v_mov_b64 v[190:191], 0\n\t"
            "s_waitcnt lgkmcnt(4)\n\t"
            "v_smfmac_f32_16x16x64_bf16 v[180:183], v[172:175], v[144:151], v255\n\t"
            "ds_read_b64 v[162:163], v253 offset:184\n\t"
            "ds_read_b64 v[164:165], v253 offset:1832\n\t"
            "v_cvt_pk_bf16_f32 v176, v176, v177\n\t"
            "v_cvt_pk_bf16_f32 v177, v178, v179\n\t"
            "s_nop 1\n\t"
            "v_mfma_f32_4x4x4_16b_bf16 v[196:199], v[176:177], v[176:177], v[196:199]\n\t"
            "ds_read_b64 v[166:167], v253 offset:1928\n\t"
            "ds_read_b128 v[172:175], v254 offset:704\n\t"
            "ds_write_b64 v131, v[200:201]\n\t"
            "v_mfma_f32_16x16x32_bf16 v[200:203], v[120:123], v[0:3], 0\n\t"
            "ds_write_b64 v131, v[204:205] offset:128\n\t"
            "v_mfma_f32_16x16x32_bf16 v[204:207], v[120:123], v[24:27], 0\n\t"
            "ds_write_b64 v131, v[208:209] offset:256\n\t"
            "s_waitcnt lgkmcnt(8)\n\t"
            "v_smfmac_f32_16x16x64_bf16 v[184:187], v[168:171], v[152:159], v255\n\t"
            "ds_write_b64 v132, v[212:213]\n\t"
            "v_mfma_f32_16x16x32_bf16 v[208:211], v[120:123], v[48:51], 0\n\t"
            "ds_read_b128 v[120:123], v113 offset:128\n\t"
            "v_cvt_pk_bf16_f32 v178, v180, v181\n\t"
            "v_mfma_f32_16x16x32_bf16 v[200:203], v[124:127], v[4:7], v[200:203]\n\t"
            "ds_write_b64 v132, v[216:217] offset:128\n\t"
            "v_mfma_f32_16x16x32_bf16 v[204:207], v[124:127], v[28:31], v[204:207]\n\t"
            "ds_write_b64 v132, v[220:221] offset:256\n\t"
            "s_waitcnt lgkmcnt(7)\n\t"
            "v_smfmac_f32_16x16x64_bf16 v[188:191], v[172:175], v[160:167], v255\n\t"
            "s_mov_b32 exec_hi, 0x3fffff\n\t"
            "s_add_u32 m0, s66, 0\n\t"
            "s_add_u32 s49, s79, 0\n\t"
            "buffer_load_dwordx4 v249, s[72:75], s49 offen lds\n\t"
            "s_mov_b32 exec_hi, -1\n\t"
            "v_mfma_f32_16x16x32_bf16 v[208:211], v[124:127], v[52:55], v[208:211]\n\t"
            "ds_read_b128 v[124:127], v112 offset:256\n\t"
            "v_cvt_pk_bf16_f32 v179, v182, v183\n\t"
            "v_mfma_f32_16x16x32_bf16 v[200:203], v[224:227], v[8:11], v[200:203]\n\t"
            "s_mov_b32 exec_hi, 0x3fffff\n\t"
            "s_add_u32 m0, s66, 872\n\t"
            "s_add_u32 s49, s68, 196224\n\t"
            "buffer_load_dwordx4 v249, s[72:75], s49 offen lds\n\t"
            "s_mov_b32 exec_hi, -1\n\t"
            "v_mfma_f32_16x16x32_bf16 v[204:207], v[224:227], v[32:35], v[204:207]\n\t"
            "s_mov_b32 exec_hi, 0x3fffff\n\t"
            "s_add_u32 m0, s66, 1744\n\t"
            "s_add_u32 s49, s68, 392448\n\t"
            "buffer_load_dwordx4 v249, s[72:75], s49 offen lds\n\t"
            "s_mov_b32 exec_hi, -1\n\t"
            "v_mfma_f32_16x16x32_bf16 v[208:211], v[224:227], v[56:59], v[208:211]\n\t"
            "ds_read_b128 v[224:227], v113 offset:256\n\t"
            "v_cvt_pk_bf16_f32 v184, v184, v185\n\t"
            "s_waitcnt lgkmcnt(4)\n\t"
            "v_mfma_f32_16x16x32_bf16 v[200:203], v[120:123], v[12:15], v[200:203]\n\t"
            "s_mov_b32 exec_hi, 0x3fffff\n\t"
            "s_add_u32 m0, s66, 2616\n\t"
            "s_add_u32 s49, s68, 588672\n\t"
            "buffer_load_dwordx4 v249, s[72:75], s49 offen lds\n\t"
            "s_mov_b32 exec_hi, -1\n\t"
            "v_mfma_f32_16x16x32_bf16 v[204:207], v[120:123], v[36:39], v[204:207]\n\t"
            "s_mov_b32 exec_hi, 0x3fffff\n\t"
            "s_add_u32 m0, s66, 3488\n\t"
            "s_add_u32 s49, s68, 784896\n\t"
            "buffer_load_dwordx4 v249, s[72:75], s49 offen lds\n\t"
            "s_mov_b32 exec_hi, -1\n\t"
            "v_mfma_f32_16x16x32_bf16 v[208:211], v[120:123], v[60:63], v[208:211]\n\t"
            "ds_read_b128 v[120:123], v112 offset:6144\n\t"
            "v_cvt_pk_bf16_f32 v185, v186, v187\n\t"
            "s_waitcnt lgkmcnt(2)\n\t"
            "v_mfma_f32_16x16x32_bf16 v[200:203], v[124:127], v[16:19], v[200:203]\n\t"
            "s_mov_b32 exec_hi, 0x3fffff\n\t"
            "s_add_u32 m0, s66, 4360\n\t"
            "s_add_u32 s49, s68, 981120\n\t"
            "buffer_load_dwordx4 v249, s[72:75], s49 offen lds\n\t"
            "s_mov_b32 exec_hi, -1\n\t"
            "v_mfma_f32_16x16x32_bf16 v[204:207], v[124:127], v[40:43], v[204:207]\n\t"
            "ds_write_b32 v136, v228\n\t"
            "v_mfma_f32_16x16x32_bf16 v[208:211], v[124:127], v[64:67], v[208:211]\n\t"
            "ds_read_b128 v[124:127], v113 offset:6144\n\t"
            "v_cvt_pk_bf16_f32 v186, v188, v189\n\t"
            "s_waitcnt lgkmcnt(3)\n\t"
            "v_mfma_f32_16x16x32_bf16 v[200:203], v[224:227], v[20:23], v[200:203]\n\t"
            "ds_read_b128 v[116:119], v137\n\t"
            "v_cvt_pk_bf16_f32 v187, v190, v191\n\t"
            "v_mfma_f32_16x16x32_bf16 v[204:207], v[224:227], v[44:47], v[204:207]\n\t"
            "s_mov_b32 exec_hi, 0x3fffff\n\t"
            "s_add_u32 m0, s66, 5232\n\t"
            "s_add_u32 s49, s68, 1177344\n\t"
            "buffer_load_dwordx4 v249, s[72:75], s49 offen lds\n\t"
            "s_mov_b32 exec_hi, -1\n\t"
            "v_mfma_f32_16x16x32_bf16 v[208:211], v[224:227], v[68:71], v[208:211]\n\t"
            "ds_read_b128 v[224:227], v112 offset:6272\n\t"
            "s_waitcnt lgkmcnt(4)\n\t"
            "v_mfma_f32_16x16x32_bf16 v[212:215], v[120:123], v[0:3], 0\n\t"
            "s_mov_b32 exec_hi, 0x3fffff\n\t"
            "s_add_u32 m0, s66, 6104\n\t"
            "s_add_u32 s49, s68, 1373568\n\t"
            "buffer_load_dwordx4 v249, s[72:75], s49 offen lds\n\t"
            "s_mov_b32 exec_hi, -1\n\t"
            "v_mfma_f32_16x16x32_bf16 v[216:219], v[120:123], v[24:27], 0\n\t"
            "s_mov_b32 exec_hi, 0x3fffff\n\t"
            "s_add_u32 m0, s66, 6976\n\t"
            "s_add_u32 s49, s68, 1569792\n\t"
            "buffer_load_dwordx4 v249, s[72:75], s49 offen lds\n\t"
            "s_mov_b32 exec_hi, -1\n\t"
            "v_mfma_f32_16x16x32_bf16 v[220:223], v[120:123], v[48:51], 0\n\t"
            "ds_read_b128 v[120:123], v113 offset:6272\n\t"
            "s_waitcnt lgkmcnt(2)\n\t"
            "v_mul_f32 v200, v116, v200\n\t"
            "v_mfma_f32_16x16x32_bf16 v[212:215], v[124:127], v[4:7], v[212:215]\n\t"
            "v_mul_f32 v201, v117, v201\n\t"
            "v_mul_f32 v202, v118, v202\n\t"
            "v_mfma_f32_16x16x32_bf16 v[216:219], v[124:127], v[28:31], v[216:219]\n\t"
            "v_mul_f32 v203, v119, v203\n\t"
            "v_mul_f32 v204, v116, v204\n\t"
            "v_mfma_f32_16x16x32_bf16 v[220:223], v[124:127], v[52:55], v[220:223]\n\t"
            "ds_read_b128 v[124:127], v112 offset:6400\n\t"
            "v_mul_f32 v205, v117, v205\n\t"
            "s_waitcnt lgkmcnt(2)\n\t"
            "v_mfma_f32_16x16x32_bf16 v[212:215], v[224:227], v[8:11], v[212:215]\n\t"
            "v_mul_f32 v206, v118, v206\n\t"
            "v_mul_f32 v207, v119, v207\n\t"
            "v_mfma_f32_16x16x32_bf16 v[216:219], v[224:227], v[32:35], v[216:219]\n\t"
            "v_mul_f32 v208, v116, v208\n\t"
            "v_mul_f32 v209, v117, v209\n\t"
            "v_mfma_f32_16x16x32_bf16 v[220:223], v[224:227], v[56:59], v[220:223]\n\t"
            "ds_read_b128 v[224:227], v113 offset:6400\n\t"
            "v_mul_f32 v210, v118, v210\n\t"
            "v_mfma_f32_4x4x4_16b_bf16 v[196:199], v[178:179], v[178:179], v[196:199]\n\t"
            "v_mul_f32 v211, v119, v211\n\t"
            "v_mfma_f32_16x16x32_bf16 v[192:195], v[238:241], v[176:179], v[192:195]\n\t"
            "ds_read_b128 v[116:119], v137 offset:64\n\t"
            "v_cvt_pk_bf16_f32 v200, v200, v201\n\t"
            "s_waitcnt lgkmcnt(3)\n\t"
            "v_mfma_f32_16x16x32_bf16 v[212:215], v[120:123], v[12:15], v[212:215]\n\t"
            "s_add_u32 s49, s33, 18432\n\t"
            "buffer_load_dwordx4 v[72:75], v247, s[28:31], s49 offen sc1\n\t"
            "v_mfma_f32_16x16x32_bf16 v[216:219], v[120:123], v[36:39], v[216:219]\n\t"
            "s_add_u32 s49, s33, 19456\n\t"
            "buffer_load_dwordx4 v[76:79], v247, s[28:31], s49 offen sc1\n\t"
            "v_mfma_f32_16x16x32_bf16 v[220:223], v[120:123], v[60:63], v[220:223]\n\t"
            "ds_read_b128 v[120:123], v112 offset:12288\n\t"
            "v_cvt_pk_bf16_f32 v201, v202, v203\n\t"
            "s_waitcnt lgkmcnt(3)\n\t"
            "v_mfma_f32_16x16x32_bf16 v[212:215], v[124:127], v[16:19], v[212:215]\n\t"
            "s_add_u32 s49, s33, 20480\n\t"
            "buffer_load_dwordx4 v[80:83], v247, s[28:31], s49 offen sc1\n\t"
            "v_mfma_f32_16x16x32_bf16 v[216:219], v[124:127], v[40:43], v[216:219]\n\t"
            "s_add_u32 s49, s33, 21504\n\t"
            "buffer_load_dwordx4 v[84:87], v247, s[28:31], s49 offen sc1\n\t"
            "v_mfma_f32_16x16x32_bf16 v[220:223], v[124:127], v[64:67], v[220:223]\n\t"
            "ds_read_b128 v[124:127], v113 offset:12288\n\t"
            "v_cvt_pk_bf16_f32 v204, v204, v205\n\t"
            "s_waitcnt lgkmcnt(3)\n\t"
            "v_mfma_f32_16x16x32_bf16 v[212:215], v[224:227], v[20:23], v[212:215]\n\t"
            "s_add_u32 s49, s33, 22528\n\t"
            "buffer_load_dwordx4 v[88:91], v247, s[28:31], s49 offen sc1\n\t"
            "v_mfma_f32_16x16x32_bf16 v[216:219], v[224:227], v[44:47], v[216:219]\n\t"
            "s_add_u32 s49, s33, 23552\n\t"
            "buffer_load_dwordx4 v[92:95], v247, s[28:31], s49 offen sc1\n\t"
            "v_mfma_f32_16x16x32_bf16 v[220:223], v[224:227], v[68:71], v[220:223]\n\t"
            "ds_read_b128 v[224:227], v112 offset:12416\n\t"
            "v_cvt_pk_bf16_f32 v208, v208, v209\n\t"
            "s_waitcnt lgkmcnt(2)\n\t"
            "v_mfma_f32_16x16x32_bf16 v[228:231], v[120:123], v[0:3], 0\n\t"
            "v_mul_f32 v212, v116, v212\n\t"
            "v_mul_f32 v213, v117, v213\n\t"
            "v_mfma_f32_16x16x32_bf16 v[232:235], v[120:123], v[24:27], 0\n\t"
            "v_mul_f32 v214, v118, v214\n\t"
            "v_mul_f32 v215, v119, v215\n\t"
            "s_waitcnt lgkmcnt(1)\n\t"
            "v_mfma_f32_16x16x32_bf16 v[228:231], v[124:127], v[4:7], v[228:231]\n\t"
            "v_cvt_pk_bf16_f32 v202, v212, v213\n\t"
            "v_cvt_pk_bf16_f32 v203, v214, v215\n\t"
            "v_mfma_f32_16x16x32_bf16 v[212:215], v[120:123], v[48:51], 0\n\t"
            "ds_read_b128 v[120:123], v113 offset:12416\n\t"
            "v_mul_f32 v216, v116, v216\n\t"
            "v_mfma_f32_16x16x32_bf16 v[232:235], v[124:127], v[28:31], v[232:235]\n\t"
            "v_mul_f32 v217, v117, v217\n\t"
            "v_mul_f32 v218, v118, v218\n\t"
            "v_mfma_f32_16x16x32_bf16 v[212:215], v[124:127], v[52:55], v[212:215]\n\t"
            "ds_read_b128 v[124:127], v112 offset:12544\n\t"
            "v_mul_f32 v219, v119, v219\n\t"
            "v_mfma_f32_4x4x4_16b_bf16 v[196:199], v[184:185], v[184:185], v[196:199]\n\t"
            "v_mul_f32 v220, v116, v220\n\t"
            "v_mfma_f32_16x16x32_bf16 v[192:195], v[238:241], v[184:187], v[192:195]\n\t"
            "v_mul_f32 v221, v117, v221\n\t"
            "v_mul_f32 v222, v118, v222\n\t"
            "s_waitcnt lgkmcnt(2)\n\t"
            "v_mfma_f32_16x16x32_bf16 v[228:231], v[224:227], v[8:11], v[228:231]\n\t"
            "v_mul_f32 v223, v119, v223\n\t"
            "v_cvt_pk_bf16_f32 v205, v206, v207\n\t"
            "v_mfma_f32_16x16x32_bf16 v[232:235], v[224:227], v[32:35], v[232:235]\n\t"
            "ds_read_b128 v[116:119], v137 offset:128\n\t"
            "v_cvt_pk_bf16_f32 v209, v210, v211\n\t"
            "v_mfma_f32_16x16x32_bf16 v[212:215], v[224:227], v[56:59], v[212:215]\n\t"
            "ds_read_b128 v[224:227], v113 offset:12544\n\t"
            "v_cvt_pk_bf16_f32 v206, v216, v217\n\t"
            "v_mfma_f32_4x4x4_16b_bf16 v[196:199], v[186:187], v[186:187], v[196:199]\n\t"
            "s_add_u32 s49, s33, 24576\n\t"
            "buffer_load_dwordx4 v[96:99], v247, s[28:31], s49 offen sc1\n\t"
            "s_waitcnt lgkmcnt(3)\n\t"
            "v_mfma_f32_16x16x32_bf16 v[228:231], v[120:123], v[12:15], v[228:231]\n\t"
            "s_add_u32 s49, s33, 25600\n\t"
            "buffer_load_dwordx4 v[100:103], v247, s[28:31], s49 offen sc1\n\t"
            "v_mfma_f32_16x16x32_bf16 v[232:235], v[120:123], v[36:39], v[232:235]\n\t"
            "v_cvt_pk_bf16_f32 v207, v218, v219\n\t"
            "v_cvt_pk_bf16_f32 v210, v220, v221\n\t"
            "v_mfma_f32_16x16x32_bf16 v[212:215], v[120:123], v[60:63], v[212:215]\n\t"
            "v_cndmask_b32_e64 v193, v196, v197, s[60:61]\n\t"
            "v_cvt_pk_bf16_f32 v211, v222, v223\n\t"
            "s_waitcnt lgkmcnt(2)\n\t"
            "v_mfma_f32_16x16x32_bf16 v[228:231], v[124:127], v[16:19], v[228:231]\n\t"
            "ds_write_b128 v236, v[176:179] offset:256\n\t"
            "v_mfma_f32_16x16x32_bf16 v[232:235], v[124:127], v[40:43], v[232:235]\n\t"
            "ds_write_b128 v237, v[184:187] offset:256\n\t"
            "v_mfma_f32_16x16x32_bf16 v[212:215], v[124:127], v[64:67], v[212:215]\n\t"
            "v_cndmask_b32_e64 v193, v193, v198, s[62:63]\n\t"
            "v_cndmask_b32_e64 v193, v193, v199, s[64:65]\n\t"
            "s_waitcnt lgkmcnt(2)\n\t"
            "v_mfma_f32_16x16x32_bf16 v[228:231], v[224:227], v[20:23], v[228:231]\n\t"
            "s_add_u32 s49, s33, 26624\n\t"
            "buffer_load_dwordx4 v[104:107], v247, s[28:31], s49 offen sc1\n\t"
            "v_mfma_f32_16x16x32_bf16 v[232:235], v[224:227], v[44:47], v[232:235]\n\t"
            "ds_write_b128 v134, v[200:203]\n\t"
            "v_mfma_f32_16x16x32_bf16 v[212:215], v[224:227], v[68:71], v[212:215]\n\t"
            "ds_write_b128 v134, v[204:207] offset:1536\n\t"
            "ds_write_b128 v134, v[208:211] offset:3072\n\t"
            "ds_write_b64 v248, v[192:193] offset:18816\n\t"
            "v_mul_f32 v228, v116, v228\n\t"
            "v_mul_f32 v229, v117, v229\n\t"
            "v_mul_f32 v230, v118, v230\n\t"
            "v_mul_f32 v232, v116, v232\n\t"
            "v_mul_f32 v233, v117, v233\n\t"
            "v_mul_f32 v212, v116, v212\n\t"
            "v_mul_f32 v213, v117, v213\n\t"
            "v_mul_f32 v231, v119, v231\n\t"
            "v_cvt_pk_bf16_f32 v228, v228, v229\n\t"
            "v_mul_f32 v234, v118, v234\n\t"
            "v_mul_f32 v235, v119, v235\n\t"
            "v_cvt_pk_bf16_f32 v232, v232, v233\n\t"
            "v_mul_f32 v214, v118, v214\n\t"
            "v_mul_f32 v215, v119, v215\n\t"
            "v_cvt_pk_bf16_f32 v212, v212, v213\n\t"
            "v_cvt_pk_bf16_f32 v229, v230, v231\n\t"
            "v_cvt_pk_bf16_f32 v233, v234, v235\n\t"
            "v_cvt_pk_bf16_f32 v213, v214, v215\n\t"
            "ds_write_b64 v135, v[228:229]\n\t"
            "ds_write_b64 v135, v[232:233] offset:1536\n\t"
            "ds_write_b64 v135, v[212:213] offset:3072\n\t"
            "s_add_u32 s49, s33, 27648\n\t"
            "buffer_load_dwordx4 v[108:111], v247, s[28:31], s49 offen sc1\n\t"
            "s_waitcnt lgkmcnt(0)\n\t"
            "s_nop 7\n\t"
            "v_mov_b32 v144, s44\n\t"
            "v_mov_b32 v145, -1\n\t"
            "ds_write_b32 v144, v145\n\t"
            "s_cmp_ge_u32 s33, 221184\n\t"
            "s_cbranch_scc1 .Lrp%=\n\t"
            "s_add_u32 s49, s33, 28672\n\t"
            "buffer_load_dwordx4 v[112:115], v247, s[28:31], s49 offen sc1\n\t"
            "s_add_u32 s49, s33, 29696\n\t"
            "buffer_load_dwordx4 v[116:119], v247, s[28:31], s49 offen sc1\n\t"
            "s_add_u32 s49, s33, 30720\n\t"
            "buffer_load_dwordx4 v[120:123], v247, s[28:31], s49 offen sc1\n\t"
            "s_add_u32 s49, s33, 31744\n\t"
            "buffer_load_dwordx4 v[124:127], v247, s[28:31], s49 offen sc1\n\t"
            "s_add_u32 s49, s33, 32768\n\t"
            "buffer_load_dwordx4 v[128:131], v247, s[28:31], s49 offen sc1\n\t"
            "s_add_u32 s49, s33, 33792\n\t"
            "buffer_load_dwordx4 v[132:135], v247, s[28:31], s49 offen sc1\n\t"
            "s_add_u32 s49, s33, 34816\n\t"
            "buffer_load_dwordx4 v[136:139], v247, s[28:31], s49 offen sc1\n\t"
            "s_add_u32 s49, s33, 35840\n\t"
            "buffer_load_dwordx4 v[140:143], v247, s[28:31], s49 offen sc1\n\t"
            "s_barrier\n\t"
            "s_waitcnt vmcnt(0)\n\t"
            "s_nop 7\n\t"
            "v_add_u32 v232, s67, v242\n\t"
            "v_xor_b32 v233, 64, v232\n\t"
            "v_add_u32 v234, s67, v247\n\t"
            "ds_read_b128 v[144:147], v232\n\t"
            "ds_read_b128 v[148:151], v233\n\t"
            "ds_read_b128 v[152:155], v232 offset:128\n\t"
            "ds_read_b128 v[156:159], v233 offset:128\n\t"
            "ds_read_b128 v[160:163], v232 offset:256\n\t"
            "ds_read_b128 v[164:167], v233 offset:256\n\t"
            "ds_read_b128 v[176:179], v234 offset:18816\n\t"
            "ds_read_b128 v[180:183], v234 offset:19840\n\t"
            "s_waitcnt lgkmcnt(7)\n\t"
            "v_mfma_f32_16x16x32_bf16 v[200:203], v[144:147], v[0:3], 0\n\t"
            "s_waitcnt lgkmcnt(6)\n\t"
            "v_mfma_f32_16x16x32_bf16 v[200:203], v[148:151], v[4:7], v[200:203]\n\t"
            "s_waitcnt lgkmcnt(5)\n\t"
            "v_mfma_f32_16x16x32_bf16 v[200:203], v[152:155], v[8:11], v[200:203]\n\t"
            "s_waitcnt lgkmcnt(4)\n\t"
            "v_mfma_f32_16x16x32_bf16 v[200:203], v[156:159], v[12:15], v[200:203]\n\t"
            "s_waitcnt lgkmcnt(1)\n\t"
            "v_add_f32 v176, v176, v178\n\t"
            "s_waitcnt lgkmcnt(0)\n\t"
            "v_add_f32 v180, v180, v182\n\t"
            "v_mfma_f32_16x16x32_bf16 v[200:203], v[160:163], v[16:19], v[200:203]\n\t"
            "v_add_f32 v176, v176, v180\n\t"
            "v_add_f32 v177, v177, v179\n\t"
            "v_mfma_f32_16x16x32_bf16 v[200:203], v[164:167], v[20:23], v[200:203]\n\t"
            "v_add_f32 v181, v181, v183\n\t"
            "v_mul_f32 v176, 0x3baaaaab, v176\n\t"
            "v_mfma_f32_16x16x32_bf16 v[204:207], v[144:147], v[24:27], 0\n\t"
            "v_add_f32 v177, v177, v181\n\t"
            "v_mul_f32_e64 v176, v176, -v176\n\t"
            "v_mfma_f32_16x16x32_bf16 v[204:207], v[148:151], v[28:31], v[204:207]\n\t"
            "v_fmac_f32 v176, 0x3baaaaab, v177\n\t"
            "v_max_f32 v176, 0, v176\n\t"
            "v_mfma_f32_16x16x32_bf16 v[204:207], v[152:155], v[32:35], v[204:207]\n\t"
            "v_add_f32 v176, 0x3727c5ac, v176\n\t"
            "v_rsq_f32 v176, v176\n\t"
            "v_mfma_f32_16x16x32_bf16 v[204:207], v[156:159], v[36:39], v[204:207]\n\t"
            "ds_write_b32 v246, v176\n\t"
            "v_mfma_f32_16x16x32_bf16 v[204:207], v[160:163], v[40:43], v[204:207]\n\t"
            "ds_read_b128 v[228:231], v245\n\t"
            "v_mfma_f32_16x16x32_bf16 v[204:207], v[164:167], v[44:47], v[204:207]\n\t"
            "v_mfma_f32_16x16x32_bf16 v[208:211], v[144:147], v[48:51], 0\n\t"
            "v_mfma_f32_16x16x32_bf16 v[208:211], v[148:151], v[52:55], v[208:211]\n\t"
            "v_mfma_f32_16x16x32_bf16 v[208:211], v[152:155], v[56:59], v[208:211]\n\t"
            "v_mfma_f32_16x16x32_bf16 v[208:211], v[156:159], v[60:63], v[208:211]\n\t"
            "v_mfma_f32_16x16x32_bf16 v[208:211], v[160:163], v[64:67], v[208:211]\n\t"
            "v_mfma_f32_16x16x32_bf16 v[208:211], v[164:167], v[68:71], v[208:211]\n\t"
            "v_mfma_f32_16x16x32_bf16 v[212:215], v[144:147], v[72:75], 0\n\t"
            "s_waitcnt lgkmcnt(0)\n\t"
            "v_mul_f32 v200, v228, v200\n\t"
            "v_mul_f32 v201, v229, v201\n\t"
            "v_mfma_f32_16x16x32_bf16 v[212:215], v[148:151], v[76:79], v[212:215]\n\t"
            "v_mul_f32 v204, v228, v204\n\t"
            "v_mul_f32 v205, v229, v205\n\t"
            "v_mfma_f32_16x16x32_bf16 v[212:215], v[152:155], v[80:83], v[212:215]\n\t"
            "v_mul_f32 v208, v228, v208\n\t"
            "v_mul_f32 v209, v229, v209\n\t"
            "v_mfma_f32_16x16x32_bf16 v[212:215], v[156:159], v[84:87], v[212:215]\n\t"
            "v_mul_f32 v202, v230, v202\n\t"
            "v_mul_f32 v203, v231, v203\n\t"
            "v_mfma_f32_16x16x32_bf16 v[212:215], v[160:163], v[88:91], v[212:215]\n\t"
            "v_cvt_pk_bf16_f32 v200, v200, v201\n\t"
            "v_mul_f32 v206, v230, v206\n\t"
            "v_mfma_f32_16x16x32_bf16 v[212:215], v[164:167], v[92:95], v[212:215]\n\t"
            "v_mul_f32 v207, v231, v207\n\t"
            "v_cvt_pk_bf16_f32 v204, v204, v205\n\t"
            "v_mfma_f32_16x16x32_bf16 v[216:219], v[144:147], v[96:99], 0\n\t"
            "v_mul_f32 v210, v230, v210\n\t"
            "v_mul_f32 v211, v231, v211\n\t"
            "v_mfma_f32_16x16x32_bf16 v[216:219], v[148:151], v[100:103], v[216:219]\n\t"
            "v_cvt_pk_bf16_f32 v208, v208, v209\n\t"
            "v_cvt_pk_bf16_f32 v201, v202, v203\n\t"
            "v_mfma_f32_16x16x32_bf16 v[216:219], v[152:155], v[104:107], v[216:219]\n\t"
            "v_mul_f32 v212, v228, v212\n\t"
            "v_mul_f32 v213, v229, v213\n\t"
            "v_mfma_f32_16x16x32_bf16 v[216:219], v[156:159], v[108:111], v[216:219]\n\t"
            "v_mul_f32 v214, v230, v214\n\t"
            "v_mul_f32 v215, v231, v215\n\t"
            "v_mfma_f32_16x16x32_bf16 v[216:219], v[160:163], v[112:115], v[216:219]\n\t"
            "v_cvt_pk_bf16_f32 v212, v212, v213\n\t"
            "v_cvt_pk_bf16_f32 v205, v206, v207\n\t"
            "v_mfma_f32_16x16x32_bf16 v[216:219], v[164:167], v[116:119], v[216:219]\n\t"
            "v_cvt_pk_bf16_f32 v209, v210, v211\n\t"
            "v_cvt_pk_bf16_f32 v213, v214, v215\n\t"
            "v_mfma_f32_16x16x32_bf16 v[220:223], v[144:147], v[120:123], 0\n\t"
            "v_mfma_f32_16x16x32_bf16 v[220:223], v[148:151], v[124:127], v[220:223]\n\t"
            "v_mfma_f32_16x16x32_bf16 v[220:223], v[152:155], v[128:131], v[220:223]\n\t"
            "v_mfma_f32_16x16x32_bf16 v[220:223], v[156:159], v[132:135], v[220:223]\n\t"
            "s_nop 1\n\t"
            "v_mul_f32 v216, v228, v216\n\t"
            "v_mfma_f32_16x16x32_bf16 v[220:223], v[160:163], v[136:139], v[220:223]\n\t"
            "v_mul_f32 v217, v229, v217\n\t"
            "v_mul_f32 v218, v230, v218\n\t"
            "v_mfma_f32_16x16x32_bf16 v[220:223], v[164:167], v[140:143], v[220:223]\n\t"
            "v_mul_f32 v219, v231, v219\n\t"
            "v_cvt_pk_bf16_f32 v216, v216, v217\n\t"
            "v_cvt_pk_bf16_f32 v217, v218, v219\n\t"
            "s_nop 4\n\t"
            "v_mul_f32 v220, v228, v220\n\t"
            "v_mul_f32 v221, v229, v221\n\t"
            "v_mul_f32 v222, v230, v222\n\t"
            "v_mul_f32 v223, v231, v223\n\t"
            "v_cvt_pk_bf16_f32 v220, v220, v221\n\t"
            "v_cvt_pk_bf16_f32 v221, v222, v223\n\t"
            "s_nop 7\n\t"
            "s_add_u32 s45, s46, s27\n\t"
            "s_mov_b32 s46, s47\n\t"
            "s_mov_b32 s47, s68\n\t"
            "s_xor_b32 s48, s48, s71\n\t"
            "s_xor_b32 s67, s67, s71\n\t"
            "s_add_u32 s76, s76, 4\n\t"
            "s_lshl_b32 s56, s56, 2\n\t"
            "s_lshr_b32 s49, s26, 30\n\t"
            "s_or_b32 s56, s56, s49\n\t"
            "ds_read_b128 v[168:171], v254\n\t"
            "ds_read_b128 v[172:175], v254 offset:64\n\t"
            "s_waitcnt vmcnt(0)\n\t"
            "ds_read_b64 v[144:145], v253\n\t"
            "ds_read_b64 v[146:147], v253 offset:96\n\t"
            "ds_read_b64 v[148:149], v253 offset:1744\n\t"
            "ds_read_b64 v[150:151], v253 offset:1840\n\t"
            "ds_read_b64 v[152:153], v253 offset:8\n\t"
            "ds_read_b64 v[154:155], v253 offset:104\n\t"
            "ds_read_b64 v[156:157], v253 offset:1752\n\t"
            "ds_read_b64 v[158:159], v253 offset:1848\n\t"
            "ds_read_b64 v[160:161], v253 offset:16\n\t"
            "ds_read_b64 v[162:163], v253 offset:112\n\t"
            "ds_read_b64 v[164:165], v253 offset:1760\n\t"
            "ds_read_b64 v[166:167], v253 offset:1856\n\t"
            "s_waitcnt lgkmcnt(0)\n\t"
            "s_nop 7\n\t"
            ".p2align 6\n\t"
            ".Lwc_loop%=:\n\t"
            "s_setprio 3\n\t"
            "ds_read_b128 v[228:231], v245 offset:64\n\t"
            "v_mov_b64 v[176:177], 0\n\t"
            "v_mov_b64 v[178:179], 0\n\t"
            "v_mov_b32 v249, s76\n\t"
            "v_mov_b64 v[180:181], 0\n\t"
            "s_waitcnt lgkmcnt(9)\n\t"
            "v_smfmac_f32_16x16x64_bf16 v[176:179], v[168:171], v[144:151], v255\n\t"
            "ds_read_b128 v[168:171], v254 offset:128\n\t"
            "v_mov_b64 v[182:183], 0\n\t"
            "v_mov_b64 v[184:185], 0\n\t"
            "v_mov_b64 v[186:187], 0\n\t"
            "s_waitcnt lgkmcnt(6)\n\t"
            "v_smfmac_f32_16x16x64_bf16 v[180:183], v[172:175], v[152:159], v255\n\t"
            "ds_read_b32 v249, v249\n\t"
            "v_mov_b64 v[188:189], 0\n\t"
            "v_mov_b64 v[190:191], 0\n\t"
            "v_cvt_pk_bf16_f32 v176, v176, v177\n\t"
            "v_cvt_pk_bf16_f32 v177, v178, v179\n\t"
            "v_add_u32 v236, s67, v250\n\t"
            "v_add_u32 v232, s48, v242\n\t"
            "v_mfma_f32_4x4x4_16b_bf16 v[196:199], v[176:177], v[176:177], 0\n\t"
            "ds_read_b64 v[144:145], v253 offset:24\n\t"
            "v_cvt_pk_bf16_f32 v178, v180, v181\n\t"
            "v_cvt_pk_bf16_f32 v179, v182, v183\n\t"
            "s_waitcnt lgkmcnt(3)\n\t"
            "s_barrier\n\t"
            "v_mov_b64 v[180:181], 0\n\t"
            "v_mov_b64 v[182:183], 0\n\t"
            "v_xor_b32 v233, 64, v232\n\t"
            "v_add_u32 v248, s67, v251\n\t"
            "v_add_u32 v234, s67, v247\n\t"
            "ds_read_b64 v[146:147], v253 offset:120\n\t"
            "ds_read_b64 v[148:149], v253 offset:1768\n\t"
            "ds_read_b64 v[150:151], v253 offset:1864\n\t"
            "s_waitcnt lgkmcnt(5)\n\t"
            "v_smfmac_f32_16x16x64_bf16 v[184:187], v[168:171], v[160:167], v255\n\t"
            "ds_read_b128 v[172:175], v254 offset:192\n\t"
            "v_mfma_f32_4x4x4_16b_bf16 v[196:199], v[178:179], v[178:179], v[196:199]\n\t"
            "ds_read_b64 v[152:153], v253 offset:32\n\t"
            "v_mfma_f32_16x16x32_bf16 v[192:195], v[238:241], v[176:179], 0\n\t"
            "ds_read_b64 v[154:155], v253 offset:128\n\t"
            "s_waitcnt lgkmcnt(7)\n\t"
            "v_readfirstlane_b32 s26, v249\n\t"
            "ds_read_b64 v[156:157], v253 offset:1776\n\t"
            "s_and_b32 s68, s26, 0x7ffffff\n\t"
            "s_add_u32 s68, s68, s78\n\t"
            "s_and_b32 s69, s26, 0x38000000\n\t"
            "s_or_b32 s69, s69, s39\n\t"
            "s_bitcmp1_b32 s26, 27\n\t"
            "s_cselect_b32 s79, 2.0, s68\n\t"
            "ds_read_b64 v[158:159], v253 offset:1872\n\t"
            "s_and_b32 s57, s56, 3\n\t"
            "s_lshl_b32 s58, s57, 3\n\t"
            "s_lshr_b32 m0, s54, s58\n\t"
            "ds_read_b128 v[168:171], v254 offset:256\n\t"
            "s_lshl_b32 s58, s57, 4\n\t"
            "s_lshr_b64 s[36:37], s[50:51], s58\n\t"
            "s_and_b32 s57, s36, 0xffff\n\t"
            "s_add_u32 s57, s57, s67\n\t"
            "ds_read_b64 v[160:161], v253 offset:40\n\t"
            "s_lshr_b64 s[36:37], s[52:53], s58\n\t"
            "s_and_b32 s58, s36, 0xffff\n\t"
            "s_add_u32 s58, s58, s67\n\t"
            "ds_read_b64 v[162:163], v253 offset:136\n\t"
            "s_and_b32 s59, s50, 0xffff\n\t"
            "s_add_u32 s59, s59, s67\n\t"
            "s_and_b32 s38, s52, 0xffff\n\t"
            "s_add_u32 s38, s38, s67\n\t"
            "s_waitcnt lgkmcnt(7)\n\t"
            "v_smfmac_f32_16x16x64_bf16 v[188:191], v[172:175], v[144:151], v255\n\t"
            "ds_read_b64 v[164:165], v253 offset:1784\n\t"
            "v_writelane_b32 v236, s59, m0\n\t"
            "v_writelane_b32 v236, s57, 63\n\t"
            "v_cvt_pk_bf16_f32 v184, v184, v185\n\t"
            "v_cvt_pk_bf16_f32 v185, v186, v187\n\t"
            "v_xor_b32 v237, 16, v236\n\t"
            "v_writelane_b32 v248, s38, m0\n\t"
            "v_mfma_f32_4x4x4_16b_bf16 v[196:199], v[184:185], v[184:185], v[196:199]\n\t"
            "ds_read_b64 v[166:167], v253 offset:1880\n\t"
            "v_cvt_pk_bf16_f32 v186, v188, v189\n\t"
            "v_cvt_pk_bf16_f32 v187, v190, v191\n\t"
            "v_mov_b64 v[188:189], 0\n\t"
            "v_mov_b64 v[190:191], 0\n\t"
            "v_writelane_b32 v248, s58, 63\n\t"
            "ds_read_b128 v[172:175], v254 offset:320\n\t"
            "ds_read_b64 v[144:145], v253 offset:48\n\t"
            "ds_read_b64 v[146:147], v253 offset:144\n\t"
            "ds_read_b64 v[148:149], v253 offset:1792\n\t"
            "ds_read_b64 v[150:151], v253 offset:1888\n\t"
            "ds_read_b32 v249, v252 offset:1024\n\t"
            "s_waitcnt lgkmcnt(14)\n\t"
            "ds_write_b128 v236, v[176:179]\n\t"
            "v_mov_b64 v[176:177], 0\n\t"
            "v_mov_b64 v[178:179], 0\n\t"
            "s_waitcnt lgkmcnt(11)\n\t"
            "s_nop 0\n\t"
            "v_smfmac_f32_16x16x64_bf16 v[176:179], v[168:171], v[152:159], v255\n\t"
            "ds_read_b128 v[168:171], v254 offset:384\n\t"
            "v_mfma_f32_4x4x4_16b_bf16 v[196:199], v[186:187], v[186:187], v[196:199]\n\t"
            "ds_read_b64 v[152:153], v253 offset:56\n\t"
            "v_mfma_f32_16x16x32_bf16 v[192:195], v[238:241], v[184:187], v[192:195]\n\t"
            "ds_read_b64 v[154:155], v253 offset:152\n\t"
            "s_waitcnt lgkmcnt(9)\n\t"
            "v_smfmac_f32_16x16x64_bf16 v[180:183], v[172:175], v[160:167], v255\n\t"
            "ds_read_b64 v[156:157], v253 offset:1800\n\t"
            "ds_read_b64 v[158:159], v253 offset:1896\n\t"
            "v_cvt_pk_bf16_f32 v176, v176, v177\n\t"
            "v_cvt_pk_bf16_f32 v177, v178, v179\n\t"
            "s_nop 1\n\t"
            "v_mfma_f32_4x4x4_16b_bf16 v[196:199], v[176:177], v[176:177], v[196:199]\n\t"
            "ds_read_b128 v[172:175], v254 offset:448\n\t"
            "s_waitcnt lgkmcnt(7)\n\t"
            "v_and_b32 v249, s69, v249\n\t"
            "v_cvt_pk_bf16_f32 v178, v180, v181\n\t"
            "v_cvt_pk_bf16_f32 v179, v182, v183\n\t"
            "v_mov_b64 v[180:181], 0\n\t"
            "v_mov_b64 v[182:183], 0\n\t"
            "ds_read_b64 v[160:161], v253 offset:64\n\t"
            "ds_read_b64 v[162:163], v253 offset:160\n\t"
            "ds_read_b64 v[164:165], v253 offset:1808\n\t"
            "ds_read_b64 v[166:167], v253 offset:1904\n\t"
            "ds_write_b128 v237, v[184:187]\n\t"
            "v_mov_b64 v[184:185], 0\n\t"
            "v_mov_b64 v[186:187], 0\n\t"
            "s_waitcnt lgkmcnt(10)\n\t"
            "s_nop 0\n\t"
            "v_smfmac_f32_16x16x64_bf16 v[184:187], v[168:171], v[144:151], v255\n\t"
            "ds_read_b128 v[168:171], v254 offset:512\n\t"
            "v_mfma_f32_4x4x4_16b_bf16 v[196:199], v[178:179], v[178:179], v[196:199]\n\t"
            "ds_read_b64 v[144:145], v253 offset:72\n\t"
            "v_mfma_f32_16x16x32_bf16 v[192:195], v[238:241], v[176:179], v[192:195]\n\t"
            "ds_read_b64 v[146:147], v253 offset:168\n\t"
            "s_waitcnt lgkmcnt(8)\n\t"
            "v_smfmac_f32_16x16x64_bf16 v[188:191], v[172:175], v[152:159], v255\n\t"
            "ds_read_b64 v[148:149], v253 offset:1816\n\t"
            "ds_read_b64 v[150:151], v253 offset:1912\n\t"
            "v_cvt_pk_bf16_f32 v184, v184, v185\n\t"
            "v_cvt_pk_bf16_f32 v185, v186, v187\n\t"
            "s_nop 1\n\t"
            "v_mfma_f32_4x4x4_16b_bf16 v[196:199], v[184:185], v[184:185], v[196:199]\n\t"
            "ds_read_b128 v[172:175], v254 offset:576\n\t"
            "ds_read_b64 v[152:153], v253 offset:80\n\t"
            "ds_read_b64 v[154:155], v253 offset:176\n\t"
            "v_cvt_pk_bf16_f32 v186, v188, v189\n\t"
            "v_cvt_pk_bf16_f32 v187, v190, v191\n\t"
            "v_mov_b64 v[188:189], 0\n\t"
            "v_mov_b64 v[190:191], 0\n\t"
            "ds_read_b64 v[156:157], v253 offset:1824\n\t"
            "ds_read_b64 v[158:159], v253 offset:1920\n\t"
            "s_waitcnt lgkmcnt(14)\n\t"
            "ds_write_b128 v236, v[176:179] offset:128\n\t"
            "v_mov_b64 v[176:177], 0\n\t"
            "v_mov_b64 v[178:179], 0\n\t"
            "s_waitcnt lgkmcnt(10)\n\t"
            "s_nop 0\n\t"
            "v_smfmac_f32_16x16x64_bf16 v[176:179], v[168:171], v[160:167], v255\n\t"
            "ds_read_b128 v[168:171], v254 offset:640\n\t"
            "v_mfma_f32_4x4x4_16b_bf16 v[196:199], v[186:187], v[186:187], v[196:199]\n\t"
            "ds_read_b64 v[160:161], v253 offset:88\n\t"
            "v_mfma_f32_16x16x32_bf16 v[192:195], v[238:241], v[184:187], v[192:195]\n\t"
            "ds_read_b64 v[162:163], v253 offset:184\n\t"
            "s_waitcnt lgkmcnt(8)\n\t"
            "v_smfmac_f32_16x16x64_bf16 v[180:183], v[172:175], v[144:151], v255\n\t"
            "ds_read_b64 v[164:165], v253 offset:1832\n\t"
            "ds_read_b64 v[166:167], v253 offset:1928\n\t"
            "v_cvt_pk_bf16_f32 v176, v176, v177\n\t"
            "v_cvt_pk_bf16_f32 v177, v178, v179\n\t"
            "s_nop 1\n\t"
            "v_mfma_f32_4x4x4_16b_bf16 v[196:199], v[176:177], v[176:177], v[196:199]\n\t"
            "ds_read_b128 v[172:175], v254 offset:704\n\t"
            "ds_read_b128 v[144:147], v232 offset:6144\n\t"
            "ds_read_b128 v[148:151], v233 offset:6144\n\t"
            "v_cvt_pk_bf16_f32 v178, v180, v181\n\t"
            "v_cvt_pk_bf16_f32 v179, v182, v183\n\t"
            "ds_read_b128 v[180:183], v233 offset:12288\n\t"
            "ds_write_b128 v237, v[184:187] offset:128\n\t"
            "v_mov_b64 v[184:185], 0\n\t"
            "v_mov_b64 v[186:187], 0\n\t"
            "s_waitcnt lgkmcnt(9)\n\t"
            "s_nop 0\n\t"
            "v_smfmac_f32_16x16x64_bf16 v[184:187], v[168:171], v[152:159], v255\n\t"
            "ds_read_b128 v[152:155], v232 offset:6272\n\t"
            "v_mfma_f32_4x4x4_16b_bf16 v[196:199], v[178:179], v[178:179], v[196:199]\n\t"
            "ds_read_b128 v[156:159], v233 offset:6272\n\t"
            "v_mfma_f32_16x16x32_bf16 v[192:195], v[238:241], v[176:179], v[192:195]\n\t"
            "ds_write_b128 v236, v[176:179] offset:256\n\t"
            "s_waitcnt lgkmcnt(7)\n\t"
            "v_smfmac_f32_16x16x64_bf16 v[188:191], v[172:175], v[160:167], v255\n\t"
            "ds_read_b128 v[160:163], v232 offset:6400\n\t"
            "ds_read_b128 v[164:167], v233 offset:6400\n\t"
            "v_cvt_pk_bf16_f32 v184, v184, v185\n\t"
            "v_cvt_pk_bf16_f32 v185, v186, v187\n\t"
            "s_nop 1\n\t"
            "v_mfma_f32_4x4x4_16b_bf16 v[196:199], v[184:185], v[184:185], v[196:199]\n\t"
            "ds_read_b128 v[176:179], v232 offset:12288\n\t"
            "s_mov_b32 exec_hi, 0x3fffff\n\t"
            "s_add_u32 m0, s66, 0\n\t"
            "s_add_u32 s49, s79, 0\n\t"
            "buffer_load_dwordx4 v249, s[72:75], s49 offen lds\n\t"
            "s_mov_b32 exec_hi, -1\n\t"
            "v_cvt_pk_bf16_f32 v186, v188, v189\n\t"
            "v_cvt_pk_bf16_f32 v187, v190, v191\n\t"
            "s_nop 1\n\t"
            "v_mfma_f32_4x4x4_16b_bf16 v[196:199], v[186:187], v[186:187], v[196:199]\n\t"
            "ds_read_b128 v[188:191], v233 offset:12416\n\t"
            "v_mfma_f32_16x16x32_bf16 v[192:195], v[238:241], v[184:187], v[192:195]\n\t"
            "ds_write_b128 v237, v[184:187] offset:256\n\t"
            "s_waitcnt lgkmcnt(11)\n\t"
            "v_mfma_f32_16x16x32_bf16 v[224:227], v[144:147], v[0:3], 0\n\t"
            "s_setprio 3\n\t"
            "ds_read_b128 v[184:187], v232 offset:12416\n\t"
            "s_waitcnt lgkmcnt(11)\n\t"
            "v_mfma_f32_16x16x32_bf16 v[224:227], v[148:151], v[4:7], v[224:227]\n\t"
            "s_mov_b32 exec_hi, 0x3fffff\n\t"
            "s_add_u32 m0, s66, 872\n\t"
            "s_add_u32 s49, s68, 196224\n\t"
            "buffer_load_dwordx4 v249, s[72:75], s49 offen lds\n\t"
            "s_mov_b32 exec_hi, -1\n\t"
            "s_waitcnt lgkmcnt(8)\n\t"
            "v_mfma_f32_16x16x32_bf16 v[224:227], v[152:155], v[8:11], v[224:227]\n\t"
            "v_cndmask_b32_e64 v193, v196, v197, s[60:61]\n\t"
            "v_cndmask_b32_e64 v193, v193, v198, s[62:63]\n\t"
            "s_waitcnt lgkmcnt(7)\n\t"
            "v_mfma_f32_16x16x32_bf16 v[224:227], v[156:159], v[12:15], v[224:227]\n\t"
            "v_cndmask_b32_e64 v193, v193, v199, s[64:65]\n\t"
            "s_waitcnt lgkmcnt(5)\n\t"
            "v_mfma_f32_16x16x32_bf16 v[224:227], v[160:163], v[16:19], v[224:227]\n\t"
            "ds_read_b128 v[196:199], v233 offset:12544\n\t"
            "s_waitcnt lgkmcnt(5)\n\t"
            "v_mfma_f32_16x16x32_bf16 v[224:227], v[164:167], v[20:23], v[224:227]\n\t"
            "ds_write_b64 v248, v[192:193] offset:18816\n\t"
            "v_mfma_f32_16x16x32_bf16 v[168:171], v[144:147], v[24:27], 0\n\t"
            "ds_read_b128 v[192:195], v232 offset:12544\n\t"
            "v_add_u32 v232, s67, v242\n\t"
            "v_mfma_f32_16x16x32_bf16 v[168:171], v[148:151], v[28:31], v[168:171]\n\t"
            "s_mov_b32 exec_hi, 0x3fffff\n\t"
            "s_add_u32 m0, s66, 1744\n\t"
            "s_add_u32 s49, s68, 392448\n\t"
            "buffer_load_dwordx4 v249, s[72:75], s49 offen lds\n\t"
            "s_mov_b32 exec_hi, -1\n\t"
            "v_mfma_f32_16x16x32_bf16 v[168:171], v[152:155], v[32:35], v[168:171]\n\t"
            "v_mul_f32 v224, v228, v224\n\t"
            "v_mul_f32 v225, v229, v225\n\t"
            "v_mfma_f32_16x16x32_bf16 v[168:171], v[156:159], v[36:39], v[168:171]\n\t"
            "v_mul_f32 v226, v230, v226\n\t"
            "v_mul_f32 v227, v231, v227\n\t"
            "v_mfma_f32_16x16x32_bf16 v[168:171], v[160:163], v[40:43], v[168:171]\n\t"
            "v_cvt_pk_bf16_f32 v202, v224, v225\n\t"
            "v_cvt_pk_bf16_f32 v203, v226, v227\n\t"
            "v_mfma_f32_16x16x32_bf16 v[168:171], v[164:167], v[44:47], v[168:171]\n\t"
            "s_mov_b32 exec_hi, 0x3fffff\n\t"
            "s_add_u32 m0, s66, 2616\n\t"
            "s_add_u32 s49, s68, 588672\n\t"
            "buffer_load_dwordx4 v249, s[72:75], s49 offen lds\n\t"
            "s_mov_b32 exec_hi, -1\n\t"
            "v_mfma_f32_16x16x32_bf16 v[172:175], v[144:147], v[48:51], 0\n\t"
            "s_mov_b32 exec_hi, 0x3fffff\n\t"
            "s_add_u32 m0, s66, 3488\n\t"
            "s_add_u32 s49, s68, 784896\n\t"
            "buffer_load_dwordx4 v249, s[72:75], s49 offen lds\n\t"
            "s_mov_b32 exec_hi, -1\n\t"
            "v_mfma_f32_16x16x32_bf16 v[172:175], v[148:151], v[52:55], v[172:175]\n\t"
            "v_mul_f32 v168, v228, v168\n\t"
            "v_mul_f32 v169, v229, v169\n\t"
            "v_mfma_f32_16x16x32_bf16 v[172:175], v[152:155], v[56:59], v[172:175]\n\t"
            "v_mul_f32 v170, v230, v170\n\t"
            "v_mul_f32 v171, v231, v171\n\t"
            "v_mfma_f32_16x16x32_bf16 v[172:175], v[156:159], v[60:63], v[172:175]\n\t"
            "v_cvt_pk_bf16_f32 v206, v168, v169\n\t"
            "v_cvt_pk_bf16_f32 v207, v170, v171\n\t"
            "v_mfma_f32_16x16x32_bf16 v[172:175], v[160:163], v[64:67], v[172:175]\n\t"
            "s_mov_b32 exec_hi, 0x3fffff\n\t"
            "s_add_u32 m0, s66, 4360\n\t"
            "s_add_u32 s49, s68, 981120\n\t"
            "buffer_load_dwordx4 v249, s[72:75], s49 offen lds\n\t"
            "s_mov_b32 exec_hi, -1\n\t"
            "v_mfma_f32_16x16x32_bf16 v[172:175], v[164:167], v[68:71], v[172:175]\n\t"
            "s_mov_b32 exec_hi, 0x3fffff\n\t"
            "s_add_u32 m0, s66, 5232\n\t"
            "s_add_u32 s49, s68, 1177344\n\t"
            "buffer_load_dwordx4 v249, s[72:75], s49 offen lds\n\t"
            "s_mov_b32 exec_hi, -1\n\t"
            "v_mfma_f32_16x16x32_bf16 v[224:227], v[144:147], v[72:75], 0\n\t"
            "s_mov_b32 exec_hi, 0x3fffff\n\t"
            "s_add_u32 m0, s66, 6104\n\t"
            "s_add_u32 s49, s68, 1373568\n\t"
            "buffer_load_dwordx4 v249, s[72:75], s49 offen lds\n\t"
            "s_mov_b32 exec_hi, -1\n\t"
            "v_mfma_f32_16x16x32_bf16 v[224:227], v[148:151], v[76:79], v[224:227]\n\t"
            "v_mul_f32 v172, v228, v172\n\t"
            "v_mul_f32 v173, v229, v173\n\t"
            "v_mfma_f32_16x16x32_bf16 v[224:227], v[152:155], v[80:83], v[224:227]\n\t"
            "v_mul_f32 v174, v230, v174\n\t"
            "v_mul_f32 v175, v231, v175\n\t"
            "v_mfma_f32_16x16x32_bf16 v[224:227], v[156:159], v[84:87], v[224:227]\n\t"
            "v_cvt_pk_bf16_f32 v210, v172, v173\n\t"
            "v_cvt_pk_bf16_f32 v211, v174, v175\n\t"
            "v_mfma_f32_16x16x32_bf16 v[224:227], v[160:163], v[88:91], v[224:227]\n\t"
            "s_mov_b32 exec_hi, 0x3fffff\n\t"
            "s_add_u32 m0, s66, 6976\n\t"
            "s_add_u32 s49, s68, 1569792\n\t"
            "buffer_load_dwordx4 v249, s[72:75], s49 offen lds\n\t"
            "s_mov_b32 exec_hi, -1\n\t"
            "v_mfma_f32_16x16x32_bf16 v[224:227], v[164:167], v[92:95], v[224:227]\n\t"
            "s_add_u32 s45, s46, s27\n\t"
            "s_mov_b32 s46, s47\n\t"
            "s_mov_b32 s47, s68\n\t"
            "v_mfma_f32_16x16x32_bf16 v[168:171], v[144:147], v[96:99], 0\n\t"
            "s_xor_b32 s48, s48, s71\n\t"
            "s_xor_b32 s67, s67, s71\n\t"
            "s_add_u32 s76, s76, 4\n\t"
            "v_mfma_f32_16x16x32_bf16 v[168:171], v[148:151], v[100:103], v[168:171]\n\t"
            "s_lshl_b32 s56, s56, 2\n\t"
            "s_lshr_b32 s49, s26, 30\n\t"
            "s_or_b32 s56, s56, s49\n\t"
            "v_mfma_f32_16x16x32_bf16 v[168:171], v[152:155], v[104:107], v[168:171]\n\t"
            "v_mul_f32 v224, v228, v224\n\t"
            "v_mul_f32 v225, v229, v225\n\t"
            "v_mfma_f32_16x16x32_bf16 v[168:171], v[156:159], v[108:111], v[168:171]\n\t"
            "v_mul_f32 v226, v230, v226\n\t"
            "v_mul_f32 v227, v231, v227\n\t"
            "v_mfma_f32_16x16x32_bf16 v[168:171], v[160:163], v[112:115], v[168:171]\n\t"
            "v_cvt_pk_bf16_f32 v214, v224, v225\n\t"
            "v_cvt_pk_bf16_f32 v215, v226, v227\n\t"
            "v_mfma_f32_16x16x32_bf16 v[168:171], v[164:167], v[116:119], v[168:171]\n\t"
            "ds_write_b128 v243, v[200:203]\n\t"
            "v_mfma_f32_16x16x32_bf16 v[172:175], v[144:147], v[120:123], 0\n\t"
            "ds_write_b128 v243, v[204:207] offset:1536\n\t"
            "v_mfma_f32_16x16x32_bf16 v[172:175], v[148:151], v[124:127], v[172:175]\n\t"
            "ds_write_b128 v243, v[208:211] offset:3072\n\t"
            "v_mfma_f32_16x16x32_bf16 v[172:175], v[152:155], v[128:131], v[172:175]\n\t"
            "ds_write_b128 v243, v[212:215] offset:4608\n\t"
            "v_mfma_f32_16x16x32_bf16 v[172:175], v[156:159], v[132:135], v[172:175]\n\t"
            "v_mul_f32 v168, v228, v168\n\t"
            "v_mul_f32 v169, v229, v169\n\t"
            "v_mfma_f32_16x16x32_bf16 v[172:175], v[160:163], v[136:139], v[172:175]\n\t"
            "v_mul_f32 v170, v230, v170\n\t"
            "v_mul_f32 v171, v231, v171\n\t"
            "v_mfma_f32_16x16x32_bf16 v[172:175], v[164:167], v[140:143], v[172:175]\n\t"
            "v_cvt_pk_bf16_f32 v218, v168, v169\n\t"
            "v_cvt_pk_bf16_f32 v219, v170, v171\n\t"
            "s_waitcnt lgkmcnt(10)\n\t"
            "v_mfma_f32_16x16x32_bf16 v[224:227], v[176:179], v[0:3], 0\n\t"
            "ds_write_b128 v243, v[216:219] offset:6144\n\t"
            "v_mfma_f32_16x16x32_bf16 v[224:227], v[180:183], v[4:7], v[224:227]\n\t"
            "v_xor_b32 v233, 64, v232\n\t"
            "s_waitcnt lgkmcnt(8)\n\t"
            "v_mfma_f32_16x16x32_bf16 v[224:227], v[184:187], v[8:11], v[224:227]\n\t"
            "v_mul_f32 v172, v228, v172\n\t"
            "v_mul_f32 v173, v229, v173\n\t"
            "v_mfma_f32_16x16x32_bf16 v[224:227], v[188:191], v[12:15], v[224:227]\n\t"
            "v_mul_f32 v174, v230, v174\n\t"
            "v_mul_f32 v175, v231, v175\n\t"
            "s_waitcnt lgkmcnt(5)\n\t"
            "v_mfma_f32_16x16x32_bf16 v[224:227], v[192:195], v[16:19], v[224:227]\n\t"
            "ds_read_b128 v[228:231], v245 offset:128\n\t"
            "v_cvt_pk_bf16_f32 v222, v172, v173\n\t"
            "v_mfma_f32_16x16x32_bf16 v[224:227], v[196:199], v[20:23], v[224:227]\n\t"
            "v_cvt_pk_bf16_f32 v223, v174, v175\n\t"
            "v_mfma_f32_16x16x32_bf16 v[168:171], v[176:179], v[24:27], 0\n\t"
            "ds_write_b128 v243, v[220:223] offset:7680\n\t"
            "v_mfma_f32_16x16x32_bf16 v[168:171], v[180:183], v[28:31], v[168:171]\n\t"
            "v_mfma_f32_16x16x32_bf16 v[168:171], v[184:187], v[32:35], v[168:171]\n\t"
            "v_mfma_f32_16x16x32_bf16 v[168:171], v[188:191], v[36:39], v[168:171]\n\t"
            "v_mfma_f32_16x16x32_bf16 v[168:171], v[192:195], v[40:43], v[168:171]\n\t"
            "v_mfma_f32_16x16x32_bf16 v[168:171], v[196:199], v[44:47], v[168:171]\n\t"
            "s_waitcnt lgkmcnt(1)\n\t"
            "v_mul_f32 v224, v228, v224\n\t"
            "v_mfma_f32_16x16x32_bf16 v[172:175], v[176:179], v[48:51], 0\n\t"
            "v_mul_f32 v225, v229, v225\n\t"
            "v_mul_f32 v226, v230, v226\n\t"
            "v_mfma_f32_16x16x32_bf16 v[172:175], v[180:183], v[52:55], v[172:175]\n\t"
            "v_mul_f32 v227, v231, v227\n\t"
            "v_cvt_pk_bf16_f32 v224, v224, v225\n\t"
            "v_mfma_f32_16x16x32_bf16 v[172:175], v[184:187], v[56:59], v[172:175]\n\t"
            "v_cvt_pk_bf16_f32 v225, v226, v227\n\t"
            "v_mul_f32 v168, v228, v168\n\t"
            "v_mfma_f32_16x16x32_bf16 v[172:175], v[188:191], v[60:63], v[172:175]\n\t"
            "ds_write_b64 v244, v[224:225]\n\t"
            "v_mfma_f32_16x16x32_bf16 v[172:175], v[192:195], v[64:67], v[172:175]\n\t"
            "v_mul_f32 v169, v229, v169\n\t"
            "v_mul_f32 v170, v230, v170\n\t"
            "v_mfma_f32_16x16x32_bf16 v[172:175], v[196:199], v[68:71], v[172:175]\n\t"
            "v_mul_f32 v171, v231, v171\n\t"
            "v_cvt_pk_bf16_f32 v168, v168, v169\n\t"
            "v_mfma_f32_16x16x32_bf16 v[224:227], v[176:179], v[72:75], 0\n\t"
            "v_cvt_pk_bf16_f32 v169, v170, v171\n\t"
            "v_mfma_f32_16x16x32_bf16 v[224:227], v[180:183], v[76:79], v[224:227]\n\t"
            "ds_write_b64 v244, v[168:169] offset:1536\n\t"
            "v_mfma_f32_16x16x32_bf16 v[224:227], v[184:187], v[80:83], v[224:227]\n\t"
            "s_nop 0\n\t"
            "v_mul_f32 v172, v228, v172\n\t"
            "v_mul_f32 v173, v229, v173\n\t"
            "v_mfma_f32_16x16x32_bf16 v[224:227], v[188:191], v[84:87], v[224:227]\n\t"
            "v_mul_f32 v174, v230, v174\n\t"
            "v_mul_f32 v175, v231, v175\n\t"
            "v_mfma_f32_16x16x32_bf16 v[224:227], v[192:195], v[88:91], v[224:227]\n\t"
            "v_cvt_pk_bf16_f32 v172, v172, v173\n\t"
            "v_cvt_pk_bf16_f32 v173, v174, v175\n\t"
            "v_mfma_f32_16x16x32_bf16 v[224:227], v[196:199], v[92:95], v[224:227]\n\t"
            "ds_write_b64 v244, v[172:173] offset:3072\n\t"
            "v_mfma_f32_16x16x32_bf16 v[168:171], v[176:179], v[96:99], 0\n\t"
            "v_mfma_f32_16x16x32_bf16 v[168:171], v[180:183], v[100:103], v[168:171]\n\t"
            "v_mfma_f32_16x16x32_bf16 v[168:171], v[184:187], v[104:107], v[168:171]\n\t"
            "v_mfma_f32_16x16x32_bf16 v[168:171], v[188:191], v[108:111], v[168:171]\n\t"
            "v_mfma_f32_16x16x32_bf16 v[168:171], v[192:195], v[112:115], v[168:171]\n\t"
            "s_nop 1\n\t"
            "v_mul_f32 v224, v228, v224\n\t"
            "v_mfma_f32_16x16x32_bf16 v[168:171], v[196:199], v[116:119], v[168:171]\n\t"
            "v_mul_f32 v225, v229, v225\n\t"
            "v_mul_f32 v226, v230, v226\n\t"
            "v_mfma_f32_16x16x32_bf16 v[172:175], v[176:179], v[120:123], 0\n\t"
            "v_mul_f32 v227, v231, v227\n\t"
            "v_cvt_pk_bf16_f32 v224, v224, v225\n\t"
            "v_mfma_f32_16x16x32_bf16 v[172:175], v[180:183], v[124:127], v[172:175]\n\t"
            "v_cvt_pk_bf16_f32 v225, v226, v227\n\t"
            "v_mfma_f32_16x16x32_bf16 v[172:175], v[184:187], v[128:131], v[172:175]\n\t"
            "v_mul_f32 v168, v228, v168\n\t"
            "v_mul_f32 v169, v229, v169\n\t"
            "v_mfma_f32_16x16x32_bf16 v[172:175], v[188:191], v[132:135], v[172:175]\n\t"
            "v_mul_f32 v170, v230, v170\n\t"
            "v_mul_f32 v171, v231, v171\n\t"
            "v_mfma_f32_16x16x32_bf16 v[172:175], v[192:195], v[136:139], v[172:175]\n\t"
            "v_cvt_pk_bf16_f32 v168, v168, v169\n\t"
            "v_cvt_pk_bf16_f32 v169, v170, v171\n\t"
            "v_mfma_f32_16x16x32_bf16 v[172:175], v[196:199], v[140:143], v[172:175]\n\t"
            "ds_write_b64 v244, v[224:225] offset:4608\n\t"
            "ds_write_b64 v244, v[168:169] offset:6144\n\t"
            "ds_read_b128 v[168:171], v254\n\t"
            "s_nop 4\n\t"
            "v_mul_f32 v172, v228, v172\n\t"
            "v_mul_f32 v173, v229, v173\n\t"
            "v_mul_f32 v174, v230, v174\n\t"
            "v_mul_f32 v175, v231, v175\n\t"
            "v_cvt_pk_bf16_f32 v172, v172, v173\n\t"
            "v_cvt_pk_bf16_f32 v173, v174, v175\n\t"
            "ds_write_b64 v244, v[172:173] offset:7680\n\t"
            "ds_read_b128 v[172:175], v254 offset:64\n\t"
            "s_waitcnt lgkmcnt(1)\n\t"
            "s_cmp_lt_u32 s46, 0x7000000\n\t"
            "s_barrier\n\t"
            "s_cbranch_scc0 .Lvx%=\n\t"
            "ds_read_b128 v[144:147], v232\n\t"
            "ds_read_b128 v[148:151], v233\n\t"
            "ds_read_b128 v[152:155], v232 offset:128\n\t"
            "ds_read_b128 v[156:159], v233 offset:128\n\t"
            "ds_read_b128 v[160:163], v232 offset:256\n\t"
            "ds_read_b128 v[164:167], v233 offset:256\n\t"
            "ds_read_b128 v[176:179], v234 offset:18816\n\t"
            "ds_read_b128 v[180:183], v234 offset:19840\n\t"
            "s_waitcnt lgkmcnt(7)\n\t"
            "v_mfma_f32_16x16x32_bf16 v[200:203], v[144:147], v[0:3], 0\n\t"
            "s_waitcnt lgkmcnt(6)\n\t"
            "v_mfma_f32_16x16x32_bf16 v[200:203], v[148:151], v[4:7], v[200:203]\n\t"
            "s_waitcnt lgkmcnt(5)\n\t"
            "v_mfma_f32_16x16x32_bf16 v[200:203], v[152:155], v[8:11], v[200:203]\n\t"
            "s_waitcnt lgkmcnt(4)\n\t"
            "v_mfma_f32_16x16x32_bf16 v[200:203], v[156:159], v[12:15], v[200:203]\n\t"
            "s_waitcnt lgkmcnt(1)\n\t"
            "v_add_f32 v176, v176, v178\n\t"
            "s_waitcnt lgkmcnt(0)\n\t"
            "v_add_f32 v180, v180, v182\n\t"
            "v_mfma_f32_16x16x32_bf16 v[200:203], v[160:163], v[16:19], v[200:203]\n\t"
            "v_add_f32 v176, v176, v180\n\t"
            "v_add_f32 v177, v177, v179\n\t"
            "v_mfma_f32_16x16x32_bf16 v[200:203], v[164:167], v[20:23], v[200:203]\n\t"
            "v_add_f32 v181, v181, v183\n\t"
            "v_mul_f32 v176, 0x3baaaaab, v176\n\t"
            "v_mfma_f32_16x16x32_bf16 v[204:207], v[144:147], v[24:27], 0\n\t"
            "v_add_f32 v177, v177, v181\n\t"
            "v_mul_f32_e64 v176, v176, -v176\n\t"
            "v_mfma_f32_16x16x32_bf16 v[204:207], v[148:151], v[28:31], v[204:207]\n\t"
            "v_fmac_f32 v176, 0x3baaaaab, v177\n\t"
            "v_max_f32 v176, 0, v176\n\t"
            "v_mfma_f32_16x16x32_bf16 v[204:207], v[152:155], v[32:35], v[204:207]\n\t"
            "v_add_f32 v176, 0x3727c5ac, v176\n\t"
            "v_rsq_f32 v176, v176\n\t"
            "v_mfma_f32_16x16x32_bf16 v[204:207], v[156:159], v[36:39], v[204:207]\n\t"
            "ds_write_b32 v246, v176\n\t"
            "v_mfma_f32_16x16x32_bf16 v[204:207], v[160:163], v[40:43], v[204:207]\n\t"
            "ds_read_b128 v[228:231], v245\n\t"
            "v_mfma_f32_16x16x32_bf16 v[204:207], v[164:167], v[44:47], v[204:207]\n\t"
            "v_mfma_f32_16x16x32_bf16 v[208:211], v[144:147], v[48:51], 0\n\t"
            "v_mfma_f32_16x16x32_bf16 v[208:211], v[148:151], v[52:55], v[208:211]\n\t"
            "v_mfma_f32_16x16x32_bf16 v[208:211], v[152:155], v[56:59], v[208:211]\n\t"
            "v_mfma_f32_16x16x32_bf16 v[208:211], v[156:159], v[60:63], v[208:211]\n\t"
            "v_mfma_f32_16x16x32_bf16 v[208:211], v[160:163], v[64:67], v[208:211]\n\t"
            "v_mfma_f32_16x16x32_bf16 v[208:211], v[164:167], v[68:71], v[208:211]\n\t"
            "v_mfma_f32_16x16x32_bf16 v[212:215], v[144:147], v[72:75], 0\n\t"
            "s_waitcnt lgkmcnt(0)\n\t"
            "v_mul_f32 v200, v228, v200\n\t"
            "v_mul_f32 v201, v229, v201\n\t"
            "v_mfma_f32_16x16x32_bf16 v[212:215], v[148:151], v[76:79], v[212:215]\n\t"
            "v_mul_f32 v204, v228, v204\n\t"
            "v_mul_f32 v205, v229, v205\n\t"
            "v_mfma_f32_16x16x32_bf16 v[212:215], v[152:155], v[80:83], v[212:215]\n\t"
            "v_mul_f32 v208, v228, v208\n\t"
            "v_mul_f32 v209, v229, v209\n\t"
            "v_mfma_f32_16x16x32_bf16 v[212:215], v[156:159], v[84:87], v[212:215]\n\t"
            "v_mul_f32 v202, v230, v202\n\t"
            "v_mul_f32 v203, v231, v203\n\t"
            "v_mfma_f32_16x16x32_bf16 v[212:215], v[160:163], v[88:91], v[212:215]\n\t"
            "v_cvt_pk_bf16_f32 v200, v200, v201\n\t"
            "v_mul_f32 v206, v230, v206\n\t"
            "v_mfma_f32_16x16x32_bf16 v[212:215], v[164:167], v[92:95], v[212:215]\n\t"
            "v_mul_f32 v207, v231, v207\n\t"
            "v_cvt_pk_bf16_f32 v204, v204, v205\n\t"
            "v_mfma_f32_16x16x32_bf16 v[216:219], v[144:147], v[96:99], 0\n\t"
            "s_waitcnt vmcnt(0)\n\t"
            "v_mul_f32 v210, v230, v210\n\t"
            "v_mul_f32 v211, v231, v211\n\t"
            "v_mfma_f32_16x16x32_bf16 v[216:219], v[148:151], v[100:103], v[216:219]\n\t"
            "s_nop 0\n\t"
            "v_mul_f32 v212, v228, v212\n\t"
            "v_mul_f32 v213, v229, v213\n\t"
            "v_mfma_f32_16x16x32_bf16 v[216:219], v[152:155], v[104:107], v[216:219]\n\t"
            "v_cvt_pk_bf16_f32 v208, v208, v209\n\t"
            "v_mul_f32 v214, v230, v214\n\t"
            "v_mfma_f32_16x16x32_bf16 v[216:219], v[156:159], v[108:111], v[216:219]\n\t"
            "v_mul_f32 v215, v231, v215\n\t"
            "v_cvt_pk_bf16_f32 v212, v212, v213\n\t"
            "v_mfma_f32_16x16x32_bf16 v[216:219], v[160:163], v[112:115], v[216:219]\n\t"
            "v_cvt_pk_bf16_f32 v201, v202, v203\n\t"
            "v_cvt_pk_bf16_f32 v205, v206, v207\n\t"
            "v_mfma_f32_16x16x32_bf16 v[216:219], v[164:167], v[116:119], v[216:219]\n\t"
            "v_cvt_pk_bf16_f32 v209, v210, v211\n\t"
            "v_cvt_pk_bf16_f32 v213, v214, v215\n\t"
            "v_mfma_f32_16x16x32_bf16 v[220:223], v[144:147], v[120:123], 0\n\t"
            "ds_read_b64 v[144:145], v253\n\t"
            "v_mfma_f32_16x16x32_bf16 v[220:223], v[148:151], v[124:127], v[220:223]\n\t"
            "ds_read_b64 v[146:147], v253 offset:96\n\t"
            "v_mfma_f32_16x16x32_bf16 v[220:223], v[152:155], v[128:131], v[220:223]\n\t"
            "ds_read_b64 v[148:149], v253 offset:1744\n\t"
            "v_mul_f32 v216, v228, v216\n\t"
            "v_mfma_f32_16x16x32_bf16 v[220:223], v[156:159], v[132:135], v[220:223]\n\t"
            "ds_read_b64 v[150:151], v253 offset:1840\n\t"
            "v_mul_f32 v217, v229, v217\n\t"
            "v_mfma_f32_16x16x32_bf16 v[220:223], v[160:163], v[136:139], v[220:223]\n\t"
            "ds_read_b64 v[152:153], v253 offset:8\n\t"
            "v_mul_f32 v218, v230, v218\n\t"
            "v_mfma_f32_16x16x32_bf16 v[220:223], v[164:167], v[140:143], v[220:223]\n\t"
            "ds_read_b64 v[154:155], v253 offset:104\n\t"
            "v_mul_f32 v219, v231, v219\n\t"
            "v_cvt_pk_bf16_f32 v216, v216, v217\n\t"
            "v_cvt_pk_bf16_f32 v217, v218, v219\n\t"
            "ds_read_b64 v[156:157], v253 offset:1752\n\t"
            "ds_read_b64 v[158:159], v253 offset:1848\n\t"
            "ds_read_b64 v[160:161], v253 offset:16\n\t"
            "ds_read_b64 v[162:163], v253 offset:112\n\t"
            "v_mul_f32 v220, v228, v220\n\t"
            "v_mul_f32 v221, v229, v221\n\t"
            "v_mul_f32 v222, v230, v222\n\t"
            "v_mul_f32 v223, v231, v223\n\t"
            "v_cvt_pk_bf16_f32 v220, v220, v221\n\t"
            "v_cvt_pk_bf16_f32 v221, v222, v223\n\t"
            "ds_read_b64 v[164:165], v253 offset:1760\n\t"
            "ds_read_b64 v[166:167], v253 offset:1856\n\t"
            "s_branch .Lwc_loop%=\n\t"
            ".Lvx%=:\n\t"
            "s_waitcnt lgkmcnt(0)\n\t"
            "s_nop 7\n\t"
            "s_setprio 3\n\t"
            "v_add_u32 v236, s19, v252\n\t"
            "ds_read_b128 v[244:247], v236\n\t"
            "s_add_u32 s70, s33, 73728\n\t"
            "s_waitcnt lgkmcnt(0)\n\t"
            "buffer_load_dwordx4 v[200:203], v244, s[28:31], s21 offen\n\t"
            "buffer_load_dwordx4 v[204:207], v245, s[28:31], s21 offen\n\t"
            "buffer_load_dwordx4 v[208:211], v246, s[28:31], s21 offen\n\t"
            "s_add_u32 s21, s21, 6144\n\t"
            "buffer_load_dwordx4 v[212:215], v244, s[28:31], s21 offen\n\t"
            "buffer_load_dwordx4 v[216:219], v245, s[28:31], s21 offen\n\t"
            "buffer_load_dwordx4 v[220:223], v246, s[28:31], s21 offen\n\t"
            "s_add_u32 s49, s70, 0\n\t"
            "buffer_load_dwordx4 v[0:3], v247, s[28:31], s49 offen sc1\n\t"
            "s_add_u32 s49, s70, 6144\n\t"
            "buffer_load_dwordx4 v[24:27], v247, s[28:31], s49 offen sc1\n\t"
            "s_add_u32 s49, s70, 12288\n\t"
            "buffer_load_dwordx4 v[48:51], v247, s[28:31], s49 offen sc1\n\t"
            "s_add_u32 s49, s70, 18432\n\t"
            "buffer_load_dwordx4 v[72:75], v247, s[28:31], s49 offen sc1\n\t"
            "s_add_u32 s49, s70, 24576\n\t"
            "buffer_load_dwordx4 v[96:99], v247, s[28:31], s49 offen sc1\n\t"
            "s_add_u32 s49, s70, 30720\n\t"
            "buffer_load_dwordx4 v[120:123], v247, s[28:31], s49 offen sc1\n\t"
            "s_add_u32 s49, s70, 1024\n\t"
            "buffer_load_dwordx4 v[4:7], v247, s[28:31], s49 offen sc1\n\t"
            "s_add_u32 s49, s70, 7168\n\t"
            "buffer_load_dwordx4 v[28:31], v247, s[28:31], s49 offen sc1\n\t"
            "s_add_u32 s49, s70, 13312\n\t"
            "buffer_load_dwordx4 v[52:55], v247, s[28:31], s49 offen sc1\n\t"
            "s_add_u32 s49, s70, 19456\n\t"
            "buffer_load_dwordx4 v[76:79], v247, s[28:31], s49 offen sc1\n\t"
            "s_add_u32 s49, s70, 25600\n\t"
            "buffer_load_dwordx4 v[100:103], v247, s[28:31], s49 offen sc1\n\t"
            "s_add_u32 s49, s70, 31744\n\t"
            "buffer_load_dwordx4 v[124:127], v247, s[28:31], s49 offen sc1\n\t"
            "s_add_u32 s49, s70, 2048\n\t"
            "buffer_load_dwordx4 v[8:11], v247, s[28:31], s49 offen sc1\n\t"
            "s_add_u32 s49, s70, 8192\n\t"
            "buffer_load_dwordx4 v[32:35], v247, s[28:31], s49 offen sc1\n\t"
            "s_add_u32 s49, s70, 14336\n\t"
            "buffer_load_dwordx4 v[56:59], v247, s[28:31], s49 offen sc1\n\t"
            "s_add_u32 s49, s70, 20480\n\t"
            "buffer_load_dwordx4 v[80:83], v247, s[28:31], s49 offen sc1\n\t"
            "s_add_u32 s49, s70, 26624\n\t"
            "buffer_load_dwordx4 v[104:107], v247, s[28:31], s49 offen sc1\n\t"
            "s_add_u32 s49, s70, 32768\n\t"
            "buffer_load_dwordx4 v[128:131], v247, s[28:31], s49 offen sc1\n\t"
            "s_add_u32 s49, s70, 3072\n\t"
            "buffer_load_dwordx4 v[12:15], v247, s[28:31], s49 offen sc1\n\t"
            "s_add_u32 s49, s70, 9216\n\t"
            "buffer_load_dwordx4 v[36:39], v247, s[28:31], s49 offen sc1\n\t"
            "s_add_u32 s49, s70, 15360\n\t"
            "buffer_load_dwordx4 v[60:63], v247, s[28:31], s49 offen sc1\n\t"
            "s_add_u32 s49, s70, 21504\n\t"
            "buffer_load_dwordx4 v[84:87], v247, s[28:31], s49 offen sc1\n\t"
            "s_add_u32 s49, s70, 27648\n\t"
            "buffer_load_dwordx4 v[108:111], v247, s[28:31], s49 offen sc1\n\t"
            "s_add_u32 s49, s70, 33792\n\t"
            "buffer_load_dwordx4 v[132:135], v247, s[28:31], s49 offen sc1\n\t"
            "s_add_u32 s49, s70, 4096\n\t"
            "buffer_load_dwordx4 v[16:19], v247, s[28:31], s49 offen sc1\n\t"
            "s_add_u32 s49, s70, 10240\n\t"
            "buffer_load_dwordx4 v[40:43], v247, s[28:31], s49 offen sc1\n\t"
            "s_add_u32 s49, s70, 16384\n\t"
            "buffer_load_dwordx4 v[64:67], v247, s[28:31], s49 offen sc1\n\t"
            "s_add_u32 s49, s70, 22528\n\t"
            "buffer_load_dwordx4 v[88:91], v247, s[28:31], s49 offen sc1\n\t"
            "s_add_u32 s49, s70, 28672\n\t"
            "buffer_load_dwordx4 v[112:115], v247, s[28:31], s49 offen sc1\n\t"
            "s_add_u32 s49, s70, 34816\n\t"
            "buffer_load_dwordx4 v[136:139], v247, s[28:31], s49 offen sc1\n\t"
            "s_add_u32 s49, s70, 5120\n\t"
            "buffer_load_dwordx4 v[20:23], v247, s[28:31], s49 offen sc1\n\t"
            "s_add_u32 s49, s70, 11264\n\t"
            "buffer_load_dwordx4 v[44:47], v247, s[28:31], s49 offen sc1\n\t"
            "s_add_u32 s49, s70, 17408\n\t"
            "buffer_load_dwordx4 v[68:71], v247, s[28:31], s49 offen sc1\n\t"
            "s_add_u32 s49, s70, 23552\n\t"
            "buffer_load_dwordx4 v[92:95], v247, s[28:31], s49 offen sc1\n\t"
            "s_add_u32 s49, s70, 29696\n\t"
            "buffer_load_dwordx4 v[116:119], v247, s[28:31], s49 offen sc1\n\t"
            "s_add_u32 s49, s70, 35840\n\t"
            "buffer_load_dwordx4 v[140:143], v247, s[28:31], s49 offen sc1\n\t"
            "s_nop 7\n\t"
            "v_bfe_u32 v248, v247, 2, 6\n\t"
            "v_add_u32 v248, s23, v248\n\t"
            "v_bfe_u32 v249, v247, 2, 6\n\t"
            "v_add_u32 v249, s23, v249\n\t"
            "v_lshrrev_b32 v236, 4, v247\n\t"
            "v_and_b32 v236, 48, v236\n\t"
            "v_add_u32 v236, s20, v236\n\t"
            "v_add_u32 v242, s22, v242\n\t"
            "v_xor_b32 v243, 64, v242\n\t"
            "ds_read_b32 v248, v248\n\t"
            "ds_read_b32 v249, v249 offset:64\n\t"
            "s_waitcnt lgkmcnt(1)\n\t"
            "v_ashrrev_i32 v237, 31, v248\n\t"
            "v_and_b32 v237, 1177344, v237\n\t"
            "v_sub_u32 v236, v236, v237\n\t"
            "v_bfe_i32 v237, v248, 30, 1\n\t"
            "v_and_b32 v237, 2304, v237\n\t"
            "v_sub_u32 v236, v236, v237\n\t"
            "v_and_b32 v248, 0x7ffffff, v248\n\t"
            "v_add_u32 v248, v248, v236\n\t"
            "v_lshrrev_b32 v236, 4, v247\n\t"
            "v_and_b32 v236, 48, v236\n\t"
            "s_waitcnt lgkmcnt(0)\n\t"
            "v_ashrrev_i32 v237, 31, v249\n\t"
            "v_add_u32 v236, s20, v236\n\t"
            "v_and_b32 v237, 1177344, v237\n\t"
            "v_sub_u32 v236, v236, v237\n\t"
            "v_bfe_i32 v237, v249, 30, 1\n\t"
            "v_and_b32 v237, 2304, v237\n\t"
            "v_sub_u32 v236, v236, v237\n\t"
            "v_and_b32 v249, 0x7ffffff, v249\n\t"
            "v_add_u32 v249, v249, v236\n\t"
            "s_nop 7\n\t"
            "v_mov_b32 v144, s23\n\t"
            ".Ldv%=:\n\t"
            "ds_read_b128 v[148:151], v144 offset:96\n\t"
            "s_waitcnt lgkmcnt(0)\n\t"
            "v_or_b32 v148, v148, v149\n\t"
            "v_or3_b32 v148, v148, v150, v151\n\t"
            "v_cmp_gt_i32 vcc, 0, v148\n\t"
            "s_nop 4\n\t"
            "s_cbranch_vccnz .Ldv%=\n\t"
            "s_waitcnt vmcnt(0)\n\t"
            "s_nop 7\n\t"
            "ds_read_b128 v[184:187], v242 offset:53760\n\t"
            "v_lshlrev_b32 v152, 16, v200\n\t"
            "v_and_b32 v153, 0xffff0000, v200\n\t"
            "v_lshlrev_b32 v154, 16, v201\n\t"
            "v_and_b32 v155, 0xffff0000, v201\n\t"
            "v_lshlrev_b32 v156, 16, v202\n\t"
            "v_and_b32 v157, 0xffff0000, v202\n\t"
            "v_lshlrev_b32 v158, 16, v203\n\t"
            "v_and_b32 v159, 0xffff0000, v203\n\t"
            "v_lshlrev_b32 v160, 16, v204\n\t"
            "v_and_b32 v161, 0xffff0000, v204\n\t"
            "v_lshlrev_b32 v162, 16, v205\n\t"
            "v_and_b32 v163, 0xffff0000, v205\n\t"
            "v_lshlrev_b32 v164, 16, v206\n\t"
            "v_and_b32 v165, 0xffff0000, v206\n\t"
            "v_lshlrev_b32 v166, 16, v207\n\t"
            "v_and_b32 v167, 0xffff0000, v207\n\t"
            "v_lshlrev_b32 v176, 16, v208\n\t"
            "v_and_b32 v177, 0xffff0000, v208\n\t"
            "v_lshlrev_b32 v178, 16, v209\n\t"
            "v_and_b32 v179, 0xffff0000, v209\n\t"
            "v_lshlrev_b32 v180, 16, v210\n\t"
            "v_and_b32 v181, 0xffff0000, v210\n\t"
            "v_lshlrev_b32 v182, 16, v211\n\t"
            "v_and_b32 v183, 0xffff0000, v211\n\t"
            "v_lshlrev_b32 v144, 16, v212\n\t"
            "v_and_b32 v145, 0xffff0000, v212\n\t"
            "v_lshlrev_b32 v146, 16, v213\n\t"
            "v_and_b32 v147, 0xffff0000, v213\n\t"
            "v_lshlrev_b32 v148, 16, v214\n\t"
            "v_and_b32 v149, 0xffff0000, v214\n\t"
            "s_waitcnt lgkmcnt(0)\n\t"
            "v_mfma_f32_16x16x32_bf16 v[152:155], v[0:3], v[184:187], v[152:155]\n\t"
            "ds_read_b128 v[188:191], v243 offset:53760\n\t"
            "v_lshlrev_b32 v150, 16, v215\n\t"
            "v_mfma_f32_16x16x32_bf16 v[156:159], v[24:27], v[184:187], v[156:159]\n\t"
            "ds_read_b128 v[192:195], v242 offset:53888\n\t"
            "v_and_b32 v151, 0xffff0000, v215\n\t"
            "v_mfma_f32_16x16x32_bf16 v[160:163], v[48:51], v[184:187], v[160:163]\n\t"
            "v_lshlrev_b32 v168, 16, v216\n\t"
            "v_and_b32 v169, 0xffff0000, v216\n\t"
            "v_mfma_f32_16x16x32_bf16 v[164:167], v[72:75], v[184:187], v[164:167]\n\t"
            "v_lshlrev_b32 v170, 16, v217\n\t"
            "v_and_b32 v171, 0xffff0000, v217\n\t"
            "v_mfma_f32_16x16x32_bf16 v[176:179], v[96:99], v[184:187], v[176:179]\n\t"
            "v_lshlrev_b32 v172, 16, v218\n\t"
            "v_and_b32 v173, 0xffff0000, v218\n\t"
            "v_mfma_f32_16x16x32_bf16 v[180:183], v[120:123], v[184:187], v[180:183]\n\t"
            "ds_read_b128 v[184:187], v243 offset:53888\n\t"
            "v_lshlrev_b32 v174, 16, v219\n\t"
            "v_and_b32 v175, 0xffff0000, v219\n\t"
            "s_waitcnt lgkmcnt(2)\n\t"
            "v_mfma_f32_16x16x32_bf16 v[152:155], v[4:7], v[188:191], v[152:155]\n\t"
            "v_mfma_f32_16x16x32_bf16 v[156:159], v[28:31], v[188:191], v[156:159]\n\t"
            "v_mfma_f32_16x16x32_bf16 v[160:163], v[52:55], v[188:191], v[160:163]\n\t"
            "v_mfma_f32_16x16x32_bf16 v[164:167], v[76:79], v[188:191], v[164:167]\n\t"
            "v_mfma_f32_16x16x32_bf16 v[176:179], v[100:103], v[188:191], v[176:179]\n\t"
            "v_mfma_f32_16x16x32_bf16 v[180:183], v[124:127], v[188:191], v[180:183]\n\t"
            "ds_read_b128 v[188:191], v242 offset:54016\n\t"
            "s_waitcnt lgkmcnt(2)\n\t"
            "v_mfma_f32_16x16x32_bf16 v[152:155], v[8:11], v[192:195], v[152:155]\n\t"
            "v_mfma_f32_16x16x32_bf16 v[156:159], v[32:35], v[192:195], v[156:159]\n\t"
            "v_mfma_f32_16x16x32_bf16 v[160:163], v[56:59], v[192:195], v[160:163]\n\t"
            "v_mfma_f32_16x16x32_bf16 v[164:167], v[80:83], v[192:195], v[164:167]\n\t"
            "v_mfma_f32_16x16x32_bf16 v[176:179], v[104:107], v[192:195], v[176:179]\n\t"
            "v_mfma_f32_16x16x32_bf16 v[180:183], v[128:131], v[192:195], v[180:183]\n\t"
            "ds_read_b128 v[192:195], v243 offset:54016\n\t"
            "s_waitcnt lgkmcnt(2)\n\t"
            "v_mfma_f32_16x16x32_bf16 v[152:155], v[12:15], v[184:187], v[152:155]\n\t"
            "v_mfma_f32_16x16x32_bf16 v[156:159], v[36:39], v[184:187], v[156:159]\n\t"
            "v_mfma_f32_16x16x32_bf16 v[160:163], v[60:63], v[184:187], v[160:163]\n\t"
            "v_mfma_f32_16x16x32_bf16 v[164:167], v[84:87], v[184:187], v[164:167]\n\t"
            "v_mfma_f32_16x16x32_bf16 v[176:179], v[108:111], v[184:187], v[176:179]\n\t"
            "v_mfma_f32_16x16x32_bf16 v[180:183], v[132:135], v[184:187], v[180:183]\n\t"
            "ds_read_b128 v[184:187], v242 offset:59904\n\t"
            "s_waitcnt lgkmcnt(2)\n\t"
            "v_mfma_f32_16x16x32_bf16 v[152:155], v[16:19], v[188:191], v[152:155]\n\t"
            "v_mfma_f32_16x16x32_bf16 v[156:159], v[40:43], v[188:191], v[156:159]\n\t"
            "v_mfma_f32_16x16x32_bf16 v[160:163], v[64:67], v[188:191], v[160:163]\n\t"
            "v_mfma_f32_16x16x32_bf16 v[164:167], v[88:91], v[188:191], v[164:167]\n\t"
            "v_mfma_f32_16x16x32_bf16 v[176:179], v[112:115], v[188:191], v[176:179]\n\t"
            "v_mfma_f32_16x16x32_bf16 v[180:183], v[136:139], v[188:191], v[180:183]\n\t"
            "ds_read_b128 v[188:191], v243 offset:59904\n\t"
            "s_waitcnt lgkmcnt(2)\n\t"
            "v_mfma_f32_16x16x32_bf16 v[152:155], v[20:23], v[192:195], v[152:155]\n\t"
            "v_mfma_f32_16x16x32_bf16 v[156:159], v[44:47], v[192:195], v[156:159]\n\t"
            "v_mfma_f32_16x16x32_bf16 v[160:163], v[68:71], v[192:195], v[160:163]\n\t"
            "v_mfma_f32_16x16x32_bf16 v[164:167], v[92:95], v[192:195], v[164:167]\n\t"
            "v_mfma_f32_16x16x32_bf16 v[176:179], v[116:119], v[192:195], v[176:179]\n\t"
            "v_mfma_f32_16x16x32_bf16 v[180:183], v[140:143], v[192:195], v[180:183]\n\t"
            "ds_read_b128 v[192:195], v242 offset:60032\n\t"
            "s_waitcnt lgkmcnt(2)\n\t"
            "v_mfma_f32_16x16x32_bf16 v[144:147], v[0:3], v[184:187], v[144:147]\n\t"
            "v_cvt_pk_bf16_f32 v152, v152, v153\n\t"
            "v_cvt_pk_bf16_f32 v153, v154, v155\n\t"
            "v_mfma_f32_16x16x32_bf16 v[148:151], v[24:27], v[184:187], v[148:151]\n\t"
            "v_cvt_pk_bf16_f32 v160, v160, v161\n\t"
            "v_cvt_pk_bf16_f32 v161, v162, v163\n\t"
            "v_mfma_f32_16x16x32_bf16 v[168:171], v[48:51], v[184:187], v[168:171]\n\t"
            "v_cvt_pk_bf16_f32 v154, v156, v157\n\t"
            "v_cvt_pk_bf16_f32 v155, v158, v159\n\t"
            "v_mfma_f32_16x16x32_bf16 v[172:175], v[72:75], v[184:187], v[172:175]\n\t"
            "v_cvt_pk_bf16_f32 v162, v164, v165\n\t"
            "v_cvt_pk_bf16_f32 v163, v166, v167\n\t"
            "s_waitcnt lgkmcnt(1)\n\t"
            "v_mfma_f32_16x16x32_bf16 v[144:147], v[4:7], v[188:191], v[144:147]\n\t"
            "v_lshlrev_b32 v156, 16, v220\n\t"
            "v_and_b32 v157, 0xffff0000, v220\n\t"
            "v_mfma_f32_16x16x32_bf16 v[148:151], v[28:31], v[188:191], v[148:151]\n\t"
            "v_lshlrev_b32 v158, 16, v221\n\t"
            "v_and_b32 v159, 0xffff0000, v221\n\t"
            "v_mfma_f32_16x16x32_bf16 v[168:171], v[52:55], v[188:191], v[168:171]\n\t"
            "v_lshlrev_b32 v164, 16, v222\n\t"
            "v_and_b32 v165, 0xffff0000, v222\n\t"
            "v_mfma_f32_16x16x32_bf16 v[156:159], v[96:99], v[184:187], v[156:159]\n\t"
            "v_lshlrev_b32 v166, 16, v223\n\t"
            "v_and_b32 v167, 0xffff0000, v223\n\t"
            "v_mfma_f32_16x16x32_bf16 v[172:175], v[76:79], v[188:191], v[172:175]\n\t"
            "buffer_store_dwordx4 v[152:155], v248, s[40:43], 0 offen sc1\n\t"
            "v_mfma_f32_16x16x32_bf16 v[164:167], v[120:123], v[184:187], v[164:167]\n\t"
            "ds_read_b128 v[184:187], v243 offset:60032\n\t"
            "v_cvt_pk_bf16_f32 v176, v176, v177\n\t"
            "v_mfma_f32_16x16x32_bf16 v[156:159], v[100:103], v[188:191], v[156:159]\n\t"
            "buffer_store_dwordx4 v[160:163], v248, s[40:43], 0 offen offset:64 sc1\n\t"
            "v_mfma_f32_16x16x32_bf16 v[164:167], v[124:127], v[188:191], v[164:167]\n\t"
            "ds_read_b128 v[188:191], v242 offset:60160\n\t"
            "v_cvt_pk_bf16_f32 v177, v178, v179\n\t"
            "s_waitcnt lgkmcnt(2)\n\t"
            "v_mfma_f32_16x16x32_bf16 v[144:147], v[8:11], v[192:195], v[144:147]\n\t"
            "v_cvt_pk_bf16_f32 v178, v180, v181\n\t"
            "v_cvt_pk_bf16_f32 v179, v182, v183\n\t"
            "v_mfma_f32_16x16x32_bf16 v[148:151], v[32:35], v[192:195], v[148:151]\n\t"
            "buffer_store_dwordx4 v[176:179], v248, s[40:43], 0 offen offset:128 sc1\n\t"
            "v_mfma_f32_16x16x32_bf16 v[168:171], v[56:59], v[192:195], v[168:171]\n\t"
            "v_mfma_f32_16x16x32_bf16 v[172:175], v[80:83], v[192:195], v[172:175]\n\t"
            "v_mfma_f32_16x16x32_bf16 v[156:159], v[104:107], v[192:195], v[156:159]\n\t"
            "v_mfma_f32_16x16x32_bf16 v[164:167], v[128:131], v[192:195], v[164:167]\n\t"
            "ds_read_b128 v[192:195], v243 offset:60160\n\t"
            "s_waitcnt lgkmcnt(2)\n\t"
            "v_mfma_f32_16x16x32_bf16 v[144:147], v[12:15], v[184:187], v[144:147]\n\t"
            "v_mfma_f32_16x16x32_bf16 v[148:151], v[36:39], v[184:187], v[148:151]\n\t"
            "s_waitcnt lgkmcnt(1)\n\t"
            "v_mfma_f32_16x16x32_bf16 v[144:147], v[16:19], v[188:191], v[144:147]\n\t"
            "v_mfma_f32_16x16x32_bf16 v[168:171], v[60:63], v[184:187], v[168:171]\n\t"
            "v_mfma_f32_16x16x32_bf16 v[148:151], v[40:43], v[188:191], v[148:151]\n\t"
            "v_mfma_f32_16x16x32_bf16 v[172:175], v[84:87], v[184:187], v[172:175]\n\t"
            "v_mfma_f32_16x16x32_bf16 v[168:171], v[64:67], v[188:191], v[168:171]\n\t"
            "v_mfma_f32_16x16x32_bf16 v[156:159], v[108:111], v[184:187], v[156:159]\n\t"
            "s_waitcnt lgkmcnt(0)\n\t"
            "v_mfma_f32_16x16x32_bf16 v[144:147], v[20:23], v[192:195], v[144:147]\n\t"
            "v_mfma_f32_16x16x32_bf16 v[148:151], v[44:47], v[192:195], v[148:151]\n\t"
            "v_mfma_f32_16x16x32_bf16 v[172:175], v[88:91], v[188:191], v[172:175]\n\t"
            "v_mfma_f32_16x16x32_bf16 v[164:167], v[132:135], v[184:187], v[164:167]\n\t"
            "v_mfma_f32_16x16x32_bf16 v[168:171], v[68:71], v[192:195], v[168:171]\n\t"
            "v_mfma_f32_16x16x32_bf16 v[156:159], v[112:115], v[188:191], v[156:159]\n\t"
            "v_mfma_f32_16x16x32_bf16 v[172:175], v[92:95], v[192:195], v[172:175]\n\t"
            "s_nop 1\n\t"
            "v_cvt_pk_bf16_f32 v144, v144, v145\n\t"
            "v_mfma_f32_16x16x32_bf16 v[164:167], v[136:139], v[188:191], v[164:167]\n\t"
            "v_cvt_pk_bf16_f32 v145, v146, v147\n\t"
            "v_cvt_pk_bf16_f32 v146, v148, v149\n\t"
            "v_mfma_f32_16x16x32_bf16 v[156:159], v[116:119], v[192:195], v[156:159]\n\t"
            "v_cvt_pk_bf16_f32 v147, v150, v151\n\t"
            "v_cvt_pk_bf16_f32 v168, v168, v169\n\t"
            "v_mfma_f32_16x16x32_bf16 v[164:167], v[140:143], v[192:195], v[164:167]\n\t"
            "buffer_store_dwordx4 v[144:147], v249, s[40:43], 0 offen sc1\n\t"
            "v_cvt_pk_bf16_f32 v169, v170, v171\n\t"
            "v_cvt_pk_bf16_f32 v170, v172, v173\n\t"
            "v_cvt_pk_bf16_f32 v171, v174, v175\n\t"
            "s_nop 0\n\t"
            "v_cvt_pk_bf16_f32 v156, v156, v157\n\t"
            "v_cvt_pk_bf16_f32 v157, v158, v159\n\t"
            "buffer_store_dwordx4 v[168:171], v249, s[40:43], 0 offen offset:64 sc1\n\t"
            "v_cvt_pk_bf16_f32 v158, v164, v165\n\t"
            "v_cvt_pk_bf16_f32 v159, v166, v167\n\t"
            "buffer_store_dwordx4 v[156:159], v249, s[40:43], 0 offen offset:128 sc1\n\t"
            "s_nop 7\n\t"
            "s_branch .Lre%=\n\t"
            ".Lrp%=:\n\t"
            "v_add_u32 v236, s19, v252\n\t"
            "ds_write_b128 v236, v[244:247]\n\t"
            "v_add_u32 v236, s48, v244\n\t"
            "v_add_u32 v237, s48, v245\n\t"
            "v_add_u32 v248, s48, v246\n\t"
            "ds_read_b128 v[200:203], v236\n\t"
            "ds_read_b128 v[204:207], v237\n\t"
            "ds_read_b128 v[208:211], v248\n\t"
            "ds_read_b128 v[212:215], v236 offset:6144\n\t"
            "ds_read_b128 v[216:219], v237 offset:6144\n\t"
            "ds_read_b128 v[220:223], v248 offset:6144\n\t"
            "ds_read_b128 v[224:227], v236 offset:12288\n\t"
            "ds_read_b128 v[228:231], v237 offset:12288\n\t"
            "ds_read_b128 v[232:235], v248 offset:12288\n\t"
            "s_add_u32 s49, s33, 0\n\t"
            "buffer_load_dwordx4 v[0:3], v247, s[28:31], s49 offen sc1\n\t"
            "s_add_u32 s49, s33, 1024\n\t"
            "buffer_load_dwordx4 v[4:7], v247, s[28:31], s49 offen sc1\n\t"
            "s_add_u32 s49, s33, 2048\n\t"
            "buffer_load_dwordx4 v[8:11], v247, s[28:31], s49 offen sc1\n\t"
            "s_add_u32 s49, s33, 3072\n\t"
            "buffer_load_dwordx4 v[12:15], v247, s[28:31], s49 offen sc1\n\t"
            "s_add_u32 s49, s33, 4096\n\t"
            "buffer_load_dwordx4 v[16:19], v247, s[28:31], s49 offen sc1\n\t"
            "s_add_u32 s49, s33, 5120\n\t"
            "buffer_load_dwordx4 v[20:23], v247, s[28:31], s49 offen sc1\n\t"
            "s_add_u32 s49, s33, 6144\n\t"
            "buffer_load_dwordx4 v[24:27], v247, s[28:31], s49 offen sc1\n\t"
            "s_add_u32 s49, s33, 7168\n\t"
            "buffer_load_dwordx4 v[28:31], v247, s[28:31], s49 offen sc1\n\t"
            "s_add_u32 s49, s33, 8192\n\t"
            "buffer_load_dwordx4 v[32:35], v247, s[28:31], s49 offen sc1\n\t"
            "s_add_u32 s49, s33, 9216\n\t"
            "buffer_load_dwordx4 v[36:39], v247, s[28:31], s49 offen sc1\n\t"
            "s_add_u32 s49, s33, 10240\n\t"
            "buffer_load_dwordx4 v[40:43], v247, s[28:31], s49 offen sc1\n\t"
            "s_add_u32 s49, s33, 11264\n\t"
            "buffer_load_dwordx4 v[44:47], v247, s[28:31], s49 offen sc1\n\t"
            "s_add_u32 s49, s33, 12288\n\t"
            "buffer_load_dwordx4 v[48:51], v247, s[28:31], s49 offen sc1\n\t"
            "s_add_u32 s49, s33, 13312\n\t"
            "buffer_load_dwordx4 v[52:55], v247, s[28:31], s49 offen sc1\n\t"
            "s_add_u32 s49, s33, 14336\n\t"
            "buffer_load_dwordx4 v[56:59], v247, s[28:31], s49 offen sc1\n\t"
            "s_add_u32 s49, s33, 15360\n\t"
            "buffer_load_dwordx4 v[60:63], v247, s[28:31], s49 offen sc1\n\t"
            "s_add_u32 s49, s33, 16384\n\t"
            "buffer_load_dwordx4 v[64:67], v247, s[28:31], s49 offen sc1\n\t"
            "s_add_u32 s49, s33, 17408\n\t"
            "buffer_load_dwordx4 v[68:71], v247, s[28:31], s49 offen sc1\n\t"
            "s_add_u32 s49, s33, 28672\n\t"
            "buffer_load_dwordx4 v[112:115], v247, s[28:31], s49 offen sc1\n\t"
            "s_add_u32 s49, s33, 29696\n\t"
            "buffer_load_dwordx4 v[116:119], v247, s[28:31], s49 offen sc1\n\t"
            "s_add_u32 s49, s33, 30720\n\t"
            "buffer_load_dwordx4 v[120:123], v247, s[28:31], s49 offen sc1\n\t"
            "s_add_u32 s49, s33, 31744\n\t"
            "buffer_load_dwordx4 v[124:127], v247, s[28:31], s49 offen sc1\n\t"
            "s_add_u32 s49, s33, 32768\n\t"
            "buffer_load_dwordx4 v[128:131], v247, s[28:31], s49 offen sc1\n\t"
            "s_add_u32 s49, s33, 33792\n\t"
            "buffer_load_dwordx4 v[132:135], v247, s[28:31], s49 offen sc1\n\t"
            "s_add_u32 s49, s33, 34816\n\t"
            "buffer_load_dwordx4 v[136:139], v247, s[28:31], s49 offen sc1\n\t"
            "s_add_u32 s49, s33, 35840\n\t"
            "buffer_load_dwordx4 v[140:143], v247, s[28:31], s49 offen sc1\n\t"
            "s_waitcnt lgkmcnt(0)\n\t"
            "s_barrier\n\t"
            "s_add_u32 s45, s46, s27\n\t"
            "s_mov_b32 s46, s47\n\t"
            "s_mov_b32 s47, s68\n\t"
            "s_xor_b32 s48, s48, s71\n\t"
            "s_xor_b32 s67, s67, s71\n\t"
            "s_add_u32 s76, s76, 4\n\t"
            "s_lshl_b32 s56, s56, 2\n\t"
            "s_lshr_b32 s49, s26, 30\n\t"
            "s_or_b32 s56, s56, s49\n\t"
            "s_cmp_ge_u32 s76, s24\n\t"
            "s_cselect_b64 vcc, -1, 0\n\t"
            "ds_read_b128 v[168:171], v254\n\t"
            "ds_read_b128 v[172:175], v254 offset:64\n\t"
            "s_waitcnt vmcnt(0)\n\t"
            "ds_read_b64 v[144:145], v253\n\t"
            "ds_read_b64 v[146:147], v253 offset:96\n\t"
            "ds_read_b64 v[148:149], v253 offset:1744\n\t"
            "ds_read_b64 v[150:151], v253 offset:1840\n\t"
            "ds_read_b64 v[152:153], v253 offset:8\n\t"
            "ds_read_b64 v[154:155], v253 offset:104\n\t"
            "ds_read_b64 v[156:157], v253 offset:1752\n\t"
            "ds_read_b64 v[158:159], v253 offset:1848\n\t"
            "ds_read_b64 v[160:161], v253 offset:16\n\t"
            "ds_read_b64 v[162:163], v253 offset:112\n\t"
            "ds_read_b64 v[164:165], v253 offset:1760\n\t"
            "ds_read_b64 v[166:167], v253 offset:1856\n\t"
            "s_waitcnt lgkmcnt(0)\n\t"
            "s_nop 7\n\t"
            ".p2align 6\n\t"
            ".Lwb_loop%=:\n\t"
            "s_setprio 3\n\t"
            "v_mov_b64 v[176:177], 0\n\t"
            "v_mov_b64 v[178:179], 0\n\t"
            "v_mov_b32 v249, s76\n\t"
            "v_mov_b64 v[180:181], 0\n\t"
            "s_waitcnt lgkmcnt(7)\n\t"
            "v_smfmac_f32_16x16x64_bf16 v[176:179], v[168:171], v[144:151], v255\n\t"
            "ds_read_b128 v[168:171], v254 offset:128\n\t"
            "v_mov_b64 v[182:183], 0\n\t"
            "v_mov_b64 v[184:185], 0\n\t"
            "v_mov_b64 v[186:187], 0\n\t"
            "s_waitcnt lgkmcnt(5)\n\t"
            "v_smfmac_f32_16x16x64_bf16 v[180:183], v[172:175], v[152:159], v255\n\t"
            "ds_read_b32 v249, v249\n\t"
            "v_mov_b64 v[188:189], 0\n\t"
            "v_mov_b64 v[190:191], 0\n\t"
            "v_cvt_pk_bf16_f32 v176, v176, v177\n\t"
            "v_cvt_pk_bf16_f32 v177, v178, v179\n\t"
            "v_add_u32 v236, s67, v250\n\t"
            "v_add_u32 v248, s67, v251\n\t"
            "v_mfma_f32_4x4x4_16b_bf16 v[196:199], v[176:177], v[176:177], 0\n\t"
            "ds_read_b64 v[144:145], v253 offset:24\n\t"
            "v_cvt_pk_bf16_f32 v178, v180, v181\n\t"
            "v_cvt_pk_bf16_f32 v179, v182, v183\n\t"
            "s_barrier\n\t"
            "v_mov_b64 v[180:181], 0\n\t"
            "v_mov_b64 v[182:183], 0\n\t"
            "ds_read_b64 v[146:147], v253 offset:120\n\t"
            "ds_read_b64 v[148:149], v253 offset:1768\n\t"
            "ds_read_b64 v[150:151], v253 offset:1864\n\t"
            "ds_read_b128 v[172:175], v254 offset:192\n\t"
            "s_waitcnt lgkmcnt(6)\n\t"
            "v_smfmac_f32_16x16x64_bf16 v[184:187], v[168:171], v[160:167], v255\n\t"
            "ds_read_b64 v[152:153], v253 offset:32\n\t"
            "v_mfma_f32_4x4x4_16b_bf16 v[196:199], v[178:179], v[178:179], v[196:199]\n\t"
            "ds_read_b64 v[154:155], v253 offset:128\n\t"
            "v_mfma_f32_16x16x32_bf16 v[192:195], v[238:241], v[176:179], 0\n\t"
            "ds_read_b64 v[156:157], v253 offset:1776\n\t"
            "s_waitcnt lgkmcnt(8)\n\t"
            "v_readfirstlane_b32 s26, v249\n\t"
            "ds_read_b64 v[158:159], v253 offset:1872\n\t"
            "s_and_b32 s68, s26, 0x7ffffff\n\t"
            "s_add_u32 s68, s68, s78\n\t"
            "s_and_b32 s69, s26, 0x38000000\n\t"
            "s_or_b32 s69, s69, s39\n\t"
            "s_bitcmp1_b32 s26, 27\n\t"
            "s_cselect_b32 s79, 2.0, s68\n\t"
            "ds_read_b128 v[168:171], v254 offset:256\n\t"
            "s_and_b32 s57, s56, 3\n\t"
            "s_lshl_b32 s58, s57, 3\n\t"
            "s_lshr_b32 m0, s54, s58\n\t"
            "ds_read_b64 v[160:161], v253 offset:40\n\t"
            "s_lshl_b32 s58, s57, 4\n\t"
            "s_lshr_b64 s[36:37], s[50:51], s58\n\t"
            "s_and_b32 s57, s36, 0xffff\n\t"
            "s_add_u32 s57, s57, s67\n\t"
            "ds_read_b64 v[162:163], v253 offset:136\n\t"
            "s_waitcnt lgkmcnt(7)\n\t"
            "v_smfmac_f32_16x16x64_bf16 v[188:191], v[172:175], v[144:151], v255\n\t"
            "s_lshr_b64 s[36:37], s[52:53], s58\n\t"
            "s_and_b32 s58, s36, 0xffff\n\t"
            "s_add_u32 s58, s58, s67\n\t"
            "ds_read_b64 v[164:165], v253 offset:1784\n\t"
            "s_and_b32 s59, s50, 0xffff\n\t"
            "s_add_u32 s59, s59, s67\n\t"
            "s_and_b32 s38, s52, 0xffff\n\t"
            "s_add_u32 s38, s38, s67\n\t"
            "ds_read_b64 v[166:167], v253 offset:1880\n\t"
            "v_writelane_b32 v236, s59, m0\n\t"
            "v_writelane_b32 v236, s57, 63\n\t"
            "v_cvt_pk_bf16_f32 v184, v184, v185\n\t"
            "v_cvt_pk_bf16_f32 v185, v186, v187\n\t"
            "v_cvt_pk_bf16_f32 v186, v188, v189\n\t"
            "v_cvt_pk_bf16_f32 v187, v190, v191\n\t"
            "v_mfma_f32_4x4x4_16b_bf16 v[196:199], v[184:185], v[184:185], v[196:199]\n\t"
            "ds_read_b128 v[172:175], v254 offset:320\n\t"
            "v_xor_b32 v237, 16, v236\n\t"
            "v_mov_b64 v[188:189], 0\n\t"
            "v_mov_b64 v[190:191], 0\n\t"
            "v_writelane_b32 v248, s38, m0\n\t"
            "v_writelane_b32 v248, s58, 63\n\t"
            "ds_read_b64 v[144:145], v253 offset:48\n\t"
            "ds_read_b64 v[146:147], v253 offset:144\n\t"
            "ds_read_b64 v[148:149], v253 offset:1792\n\t"
            "ds_read_b64 v[150:151], v253 offset:1888\n\t"
            "ds_read_b32 v249, v252 offset:1024\n\t"
            "s_waitcnt lgkmcnt(14)\n\t"
            "ds_write_b128 v236, v[176:179]\n\t"
            "v_mov_b64 v[176:177], 0\n\t"
            "v_mov_b64 v[178:179], 0\n\t"
            "s_waitcnt lgkmcnt(11)\n\t"
            "s_nop 0\n\t"
            "v_smfmac_f32_16x16x64_bf16 v[176:179], v[168:171], v[152:159], v255\n\t"
            "ds_read_b128 v[168:171], v254 offset:384\n\t"
            "v_mfma_f32_4x4x4_16b_bf16 v[196:199], v[186:187], v[186:187], v[196:199]\n\t"
            "ds_read_b64 v[152:153], v253 offset:56\n\t"
            "v_mfma_f32_16x16x32_bf16 v[192:195], v[238:241], v[184:187], v[192:195]\n\t"
            "ds_read_b64 v[154:155], v253 offset:152\n\t"
            "s_waitcnt lgkmcnt(9)\n\t"
            "v_smfmac_f32_16x16x64_bf16 v[180:183], v[172:175], v[160:167], v255\n\t"
            "ds_read_b64 v[156:157], v253 offset:1800\n\t"
            "ds_read_b64 v[158:159], v253 offset:1896\n\t"
            "v_cvt_pk_bf16_f32 v176, v176, v177\n\t"
            "v_cvt_pk_bf16_f32 v177, v178, v179\n\t"
            "s_nop 1\n\t"
            "v_mfma_f32_4x4x4_16b_bf16 v[196:199], v[176:177], v[176:177], v[196:199]\n\t"
            "ds_read_b128 v[172:175], v254 offset:448\n\t"
            "s_waitcnt lgkmcnt(7)\n\t"
            "v_and_b32 v249, s69, v249\n\t"
            "v_cvt_pk_bf16_f32 v178, v180, v181\n\t"
            "v_cvt_pk_bf16_f32 v179, v182, v183\n\t"
            "v_mov_b64 v[180:181], 0\n\t"
            "v_mov_b64 v[182:183], 0\n\t"
            "ds_read_b64 v[160:161], v253 offset:64\n\t"
            "ds_read_b64 v[162:163], v253 offset:160\n\t"
            "ds_read_b64 v[164:165], v253 offset:1808\n\t"
            "ds_read_b64 v[166:167], v253 offset:1904\n\t"
            "ds_write_b128 v237, v[184:187]\n\t"
            "v_mov_b64 v[184:185], 0\n\t"
            "v_mov_b64 v[186:187], 0\n\t"
            "s_waitcnt lgkmcnt(10)\n\t"
            "s_nop 0\n\t"
            "v_smfmac_f32_16x16x64_bf16 v[184:187], v[168:171], v[144:151], v255\n\t"
            "ds_read_b128 v[168:171], v254 offset:512\n\t"
            "v_mfma_f32_4x4x4_16b_bf16 v[196:199], v[178:179], v[178:179], v[196:199]\n\t"
            "ds_read_b64 v[144:145], v253 offset:72\n\t"
            "v_mfma_f32_16x16x32_bf16 v[192:195], v[238:241], v[176:179], v[192:195]\n\t"
            "ds_read_b64 v[146:147], v253 offset:168\n\t"
            "s_waitcnt lgkmcnt(8)\n\t"
            "v_smfmac_f32_16x16x64_bf16 v[188:191], v[172:175], v[152:159], v255\n\t"
            "ds_read_b64 v[148:149], v253 offset:1816\n\t"
            "ds_read_b64 v[150:151], v253 offset:1912\n\t"
            "v_cvt_pk_bf16_f32 v184, v184, v185\n\t"
            "v_cvt_pk_bf16_f32 v185, v186, v187\n\t"
            "s_nop 1\n\t"
            "v_mfma_f32_4x4x4_16b_bf16 v[196:199], v[184:185], v[184:185], v[196:199]\n\t"
            "ds_read_b128 v[172:175], v254 offset:576\n\t"
            "ds_read_b64 v[152:153], v253 offset:80\n\t"
            "ds_read_b64 v[154:155], v253 offset:176\n\t"
            "v_cvt_pk_bf16_f32 v186, v188, v189\n\t"
            "v_cvt_pk_bf16_f32 v187, v190, v191\n\t"
            "v_mov_b64 v[188:189], 0\n\t"
            "v_mov_b64 v[190:191], 0\n\t"
            "ds_read_b64 v[156:157], v253 offset:1824\n\t"
            "ds_read_b64 v[158:159], v253 offset:1920\n\t"
            "s_waitcnt lgkmcnt(14)\n\t"
            "ds_write_b128 v236, v[176:179] offset:128\n\t"
            "v_mov_b64 v[176:177], 0\n\t"
            "v_mov_b64 v[178:179], 0\n\t"
            "s_waitcnt lgkmcnt(10)\n\t"
            "s_nop 0\n\t"
            "v_smfmac_f32_16x16x64_bf16 v[176:179], v[168:171], v[160:167], v255\n\t"
            "ds_read_b128 v[168:171], v254 offset:640\n\t"
            "v_mfma_f32_4x4x4_16b_bf16 v[196:199], v[186:187], v[186:187], v[196:199]\n\t"
            "ds_read_b64 v[160:161], v253 offset:88\n\t"
            "v_mfma_f32_16x16x32_bf16 v[192:195], v[238:241], v[184:187], v[192:195]\n\t"
            "ds_read_b64 v[162:163], v253 offset:184\n\t"
            "s_waitcnt lgkmcnt(8)\n\t"
            "v_smfmac_f32_16x16x64_bf16 v[180:183], v[172:175], v[144:151], v255\n\t"
            "ds_read_b64 v[164:165], v253 offset:1832\n\t"
            "ds_read_b64 v[166:167], v253 offset:1928\n\t"
            "v_cvt_pk_bf16_f32 v176, v176, v177\n\t"
            "v_cvt_pk_bf16_f32 v177, v178, v179\n\t"
            "s_nop 1\n\t"
            "v_mfma_f32_4x4x4_16b_bf16 v[196:199], v[176:177], v[176:177], v[196:199]\n\t"
            "ds_read_b128 v[172:175], v254 offset:704\n\t"
            "ds_read_b128 v[144:147], v242\n\t"
            "ds_read_b128 v[148:151], v243\n\t"
            "v_cvt_pk_bf16_f32 v178, v180, v181\n\t"
            "v_cvt_pk_bf16_f32 v179, v182, v183\n\t"
            "v_lshlrev_b32 v180, 16, v202\n\t"
            "v_and_b32 v181, 0xffff0000, v202\n\t"
            "v_lshlrev_b32 v182, 16, v203\n\t"
            "v_and_b32 v183, 0xffff0000, v203\n\t"
            "ds_write_b128 v237, v[184:187] offset:128\n\t"
            "v_mov_b64 v[184:185], 0\n\t"
            "v_mov_b64 v[186:187], 0\n\t"
            "s_waitcnt lgkmcnt(8)\n\t"
            "s_nop 0\n\t"
            "v_smfmac_f32_16x16x64_bf16 v[184:187], v[168:171], v[152:159], v255\n\t"
            "ds_read_b128 v[152:155], v242 offset:128\n\t"
            "v_mfma_f32_4x4x4_16b_bf16 v[196:199], v[178:179], v[178:179], v[196:199]\n\t"
            "ds_read_b128 v[156:159], v243 offset:128\n\t"
            "v_mfma_f32_16x16x32_bf16 v[192:195], v[238:241], v[176:179], v[192:195]\n\t"
            "ds_read_b128 v[168:171], v242 offset:6144\n\t"
            "s_waitcnt lgkmcnt(6)\n\t"
            "v_smfmac_f32_16x16x64_bf16 v[188:191], v[172:175], v[160:167], v255\n\t"
            "ds_read_b128 v[160:163], v242 offset:256\n\t"
            "ds_read_b128 v[164:167], v243 offset:256\n\t"
            "v_cvt_pk_bf16_f32 v184, v184, v185\n\t"
            "v_cvt_pk_bf16_f32 v185, v186, v187\n\t"
            "s_nop 1\n\t"
            "v_mfma_f32_4x4x4_16b_bf16 v[196:199], v[184:185], v[184:185], v[196:199]\n\t"
            "ds_read_b128 v[172:175], v243 offset:6144\n\t"
            "s_mov_b32 exec_hi, 0x3fffff\n\t"
            "s_add_u32 m0, s66, 0\n\t"
            "s_add_u32 s49, s79, 0\n\t"
            "buffer_load_dwordx4 v249, s[72:75], s49 offen lds\n\t"
            "s_mov_b32 exec_hi, -1\n\t"
            "v_cvt_pk_bf16_f32 v186, v188, v189\n\t"
            "v_cvt_pk_bf16_f32 v187, v190, v191\n\t"
            "v_lshlrev_b32 v188, 16, v206\n\t"
            "v_and_b32 v189, 0xffff0000, v206\n\t"
            "v_mfma_f32_4x4x4_16b_bf16 v[196:199], v[186:187], v[186:187], v[196:199]\n\t"
            "ds_write_b128 v236, v[176:179] offset:256\n\t"
            "v_mfma_f32_16x16x32_bf16 v[192:195], v[238:241], v[184:187], v[192:195]\n\t"
            "v_lshlrev_b32 v176, 16, v200\n\t"
            "v_and_b32 v177, 0xffff0000, v200\n\t"
            "v_lshlrev_b32 v178, 16, v201\n\t"
            "v_and_b32 v179, 0xffff0000, v201\n\t"
            "v_lshlrev_b32 v190, 16, v207\n\t"
            "v_and_b32 v191, 0xffff0000, v207\n\t"
            "s_waitcnt lgkmcnt(9)\n\t"
            "v_mfma_f32_16x16x32_bf16 v[176:179], v[0:3], v[144:147], v[176:179]\n\t"
            "s_setprio 3\n\t"
            "s_mov_b32 exec_hi, 0x3fffff\n\t"
            "s_add_u32 m0, s66, 872\n\t"
            "s_add_u32 s49, s68, 196224\n\t"
            "buffer_load_dwordx4 v249, s[72:75], s49 offen lds\n\t"
            "s_mov_b32 exec_hi, -1\n\t"
            "s_waitcnt lgkmcnt(8)\n\t"
            "v_mfma_f32_16x16x32_bf16 v[176:179], v[4:7], v[148:151], v[176:179]\n\t"
            "s_mov_b32 exec_hi, 0x3fffff\n\t"
            "s_add_u32 m0, s66, 1744\n\t"
            "s_add_u32 s49, s68, 392448\n\t"
            "buffer_load_dwordx4 v249, s[72:75], s49 offen lds\n\t"
            "s_mov_b32 exec_hi, -1\n\t"
            "s_waitcnt lgkmcnt(6)\n\t"
            "v_mfma_f32_16x16x32_bf16 v[176:179], v[8:11], v[152:155], v[176:179]\n\t"
            "s_mov_b32 exec_hi, 0x3fffff\n\t"
            "s_add_u32 m0, s66, 2616\n\t"
            "s_add_u32 s49, s68, 588672\n\t"
            "buffer_load_dwordx4 v249, s[72:75], s49 offen lds\n\t"
            "s_mov_b32 exec_hi, -1\n\t"
            "s_waitcnt lgkmcnt(5)\n\t"
            "v_mfma_f32_16x16x32_bf16 v[176:179], v[12:15], v[156:159], v[176:179]\n\t"
            "s_mov_b32 exec_hi, 0x3fffff\n\t"
            "s_add_u32 m0, s66, 3488\n\t"
            "s_add_u32 s49, s68, 784896\n\t"
            "buffer_load_dwordx4 v249, s[72:75], s49 offen lds\n\t"
            "s_mov_b32 exec_hi, -1\n\t"
            "s_waitcnt lgkmcnt(3)\n\t"
            "v_mfma_f32_16x16x32_bf16 v[176:179], v[16:19], v[160:163], v[176:179]\n\t"
            "ds_write_b128 v237, v[184:187] offset:256\n\t"
            "s_waitcnt lgkmcnt(3)\n\t"
            "v_mfma_f32_16x16x32_bf16 v[176:179], v[20:23], v[164:167], v[176:179]\n\t"
            "v_lshlrev_b32 v184, 16, v204\n\t"
            "v_and_b32 v185, 0xffff0000, v204\n\t"
            "v_mfma_f32_16x16x32_bf16 v[180:183], v[24:27], v[144:147], v[180:183]\n\t"
            "v_lshlrev_b32 v186, 16, v205\n\t"
            "v_and_b32 v187, 0xffff0000, v205\n\t"
            "v_mfma_f32_16x16x32_bf16 v[180:183], v[28:31], v[148:151], v[180:183]\n\t"
            "s_mov_b32 exec_hi, 0x3fffff\n\t"
            "s_add_u32 m0, s66, 4360\n\t"
            "s_add_u32 s49, s68, 981120\n\t"
            "buffer_load_dwordx4 v249, s[72:75], s49 offen lds\n\t"
            "s_mov_b32 exec_hi, -1\n\t"
            "v_mfma_f32_16x16x32_bf16 v[180:183], v[32:35], v[152:155], v[180:183]\n\t"
            "s_mov_b32 exec_hi, 0x3fffff\n\t"
            "s_add_u32 m0, s66, 5232\n\t"
            "s_add_u32 s49, s68, 1177344\n\t"
            "buffer_load_dwordx4 v249, s[72:75], s49 offen lds\n\t"
            "s_mov_b32 exec_hi, -1\n\t"
            "v_mfma_f32_16x16x32_bf16 v[180:183], v[36:39], v[156:159], v[180:183]\n\t"
            "v_cndmask_b32_e64 v193, v196, v197, s[60:61]\n\t"
            "v_cndmask_b32_e64 v193, v193, v198, s[62:63]\n\t"
            "v_mfma_f32_16x16x32_bf16 v[180:183], v[40:43], v[160:163], v[180:183]\n\t"
            "s_mov_b32 exec_hi, 0x3fffff\n\t"
            "s_add_u32 m0, s66, 6104\n\t"
            "s_add_u32 s49, s68, 1373568\n\t"
            "buffer_load_dwordx4 v249, s[72:75], s49 offen lds\n\t"
            "s_mov_b32 exec_hi, -1\n\t"
            "v_mfma_f32_16x16x32_bf16 v[180:183], v[44:47], v[164:167], v[180:183]\n\t"
            "v_cndmask_b32_e64 v193, v193, v199, s[64:65]\n\t"
            "v_lshlrev_b32 v194, 16, v209\n\t"
            "v_mfma_f32_16x16x32_bf16 v[184:187], v[48:51], v[144:147], v[184:187]\n\t"
            "ds_read_b128 v[196:199], v252\n\t"
            "v_and_b32 v195, 0xffff0000, v209\n\t"
            "v_mfma_f32_16x16x32_bf16 v[184:187], v[52:55], v[148:151], v[184:187]\n\t"
            "s_mov_b32 exec_hi, 0x3fffff\n\t"
            "s_add_u32 m0, s66, 6976\n\t"
            "s_add_u32 s49, s68, 1569792\n\t"
            "buffer_load_dwordx4 v249, s[72:75], s49 offen lds\n\t"
            "s_mov_b32 exec_hi, -1\n\t"
            "v_mfma_f32_16x16x32_bf16 v[184:187], v[56:59], v[152:155], v[184:187]\n\t"
            "s_bfe_u32 s57, s56, 0x20004\n\t"
            "s_lshl_b32 s58, s57, 3\n\t"
            "s_lshr_b32 s58, s55, s58\n\t"
            "s_lshl_b64 s[36:37], s[34:35], s58\n\t"
            "v_mfma_f32_16x16x32_bf16 v[184:187], v[60:63], v[156:159], v[184:187]\n\t"
            "s_bitcmp1_b32 s57, 1\n\t"
            "s_cselect_b32 s58, 1177344, 0\n\t"
            "s_bitcmp1_b32 s57, 0\n\t"
            "s_cselect_b32 s57, 2304, 0\n\t"
            "s_add_u32 s57, s57, s58\n\t"
            "v_mfma_f32_16x16x32_bf16 v[184:187], v[64:67], v[160:163], v[184:187]\n\t"
            "ds_write_b64 v248, v[192:193] offset:18816\n\t"
            "v_mfma_f32_16x16x32_bf16 v[184:187], v[68:71], v[164:167], v[184:187]\n\t"
            "v_lshlrev_b32 v192, 16, v208\n\t"
            "v_and_b32 v193, 0xffff0000, v208\n\t"
            "v_mfma_f32_16x16x32_bf16 v[188:191], v[72:75], v[144:147], v[188:191]\n\t"
            "s_waitcnt lgkmcnt(1)\n\t"
            "s_mov_b64 exec, s[36:37]\n\t"
            "v_add_u32 v196, s57, v196\n\t"
            "s_mov_b64 exec, -1\n\t"
            "v_mfma_f32_16x16x32_bf16 v[188:191], v[76:79], v[148:151], v[188:191]\n\t"
            "v_cvt_pk_bf16_f32 v176, v176, v177\n\t"
            "v_cvt_pk_bf16_f32 v177, v178, v179\n\t"
            "v_mfma_f32_16x16x32_bf16 v[188:191], v[80:83], v[152:155], v[188:191]\n\t"
            "v_cvt_pk_bf16_f32 v178, v180, v181\n\t"
            "v_cvt_pk_bf16_f32 v179, v182, v183\n\t"
            "v_mfma_f32_16x16x32_bf16 v[188:191], v[84:87], v[156:159], v[188:191]\n\t"
            "s_cbranch_vccnz .Lst0_%=\n\t"
            "buffer_store_dwordx4 v[176:179], v196, s[40:43], s45 offen\n\t"
            ".Lsr0_%=:\n\t"
            "v_mfma_f32_16x16x32_bf16 v[188:191], v[88:91], v[160:163], v[188:191]\n\t"
            "s_nop 0\n\t"
            "v_lshlrev_b32 v176, 16, v210\n\t"
            "v_and_b32 v177, 0xffff0000, v210\n\t"
            "v_mfma_f32_16x16x32_bf16 v[188:191], v[92:95], v[164:167], v[188:191]\n\t"
            "v_lshlrev_b32 v178, 16, v211\n\t"
            "v_and_b32 v179, 0xffff0000, v211\n\t"
            "v_mfma_f32_16x16x32_bf16 v[192:195], v[96:99], v[144:147], v[192:195]\n\t"
            "v_lshlrev_b32 v180, 16, v212\n\t"
            "v_and_b32 v181, 0xffff0000, v212\n\t"
            "v_mfma_f32_16x16x32_bf16 v[192:195], v[100:103], v[148:151], v[192:195]\n\t"
            "v_lshlrev_b32 v182, 16, v213\n\t"
            "v_and_b32 v183, 0xffff0000, v213\n\t"
            "v_mfma_f32_16x16x32_bf16 v[192:195], v[104:107], v[152:155], v[192:195]\n\t"
            "v_cvt_pk_bf16_f32 v184, v184, v185\n\t"
            "v_cvt_pk_bf16_f32 v185, v186, v187\n\t"
            "v_mfma_f32_16x16x32_bf16 v[192:195], v[108:111], v[156:159], v[192:195]\n\t"
            "v_cvt_pk_bf16_f32 v186, v188, v189\n\t"
            "v_cvt_pk_bf16_f32 v187, v190, v191\n\t"
            "v_mfma_f32_16x16x32_bf16 v[192:195], v[112:115], v[160:163], v[192:195]\n\t"
            "s_cbranch_vccnz .Lst1_%=\n\t"
            "buffer_store_dwordx4 v[184:187], v196, s[40:43], s45 offen offset:64\n\t"
            ".Lsr1_%=:\n\t"
            "v_mfma_f32_16x16x32_bf16 v[192:195], v[116:119], v[164:167], v[192:195]\n\t"
            "s_nop 0\n\t"
            "v_lshlrev_b32 v184, 16, v214\n\t"
            "v_and_b32 v185, 0xffff0000, v214\n\t"
            "v_mfma_f32_16x16x32_bf16 v[176:179], v[120:123], v[144:147], v[176:179]\n\t"
            "ds_read_b128 v[144:147], v242 offset:6272\n\t"
            "v_lshlrev_b32 v186, 16, v215\n\t"
            "v_mfma_f32_16x16x32_bf16 v[176:179], v[124:127], v[148:151], v[176:179]\n\t"
            "ds_read_b128 v[148:151], v243 offset:6272\n\t"
            "v_and_b32 v187, 0xffff0000, v215\n\t"
            "v_mfma_f32_16x16x32_bf16 v[176:179], v[128:131], v[152:155], v[176:179]\n\t"
            "ds_read_b128 v[152:155], v242 offset:6400\n\t"
            "v_lshlrev_b32 v188, 16, v216\n\t"
            "v_mfma_f32_16x16x32_bf16 v[176:179], v[132:135], v[156:159], v[176:179]\n\t"
            "ds_read_b128 v[156:159], v243 offset:6400\n\t"
            "v_and_b32 v189, 0xffff0000, v216\n\t"
            "v_mfma_f32_16x16x32_bf16 v[176:179], v[136:139], v[160:163], v[176:179]\n\t"
            "ds_read_b128 v[160:163], v242 offset:12288\n\t"
            "v_lshlrev_b32 v190, 16, v217\n\t"
            "v_mfma_f32_16x16x32_bf16 v[176:179], v[140:143], v[164:167], v[176:179]\n\t"
            "ds_read_b128 v[164:167], v243 offset:12288\n\t"
            "v_and_b32 v191, 0xffff0000, v217\n\t"
            "v_mfma_f32_16x16x32_bf16 v[180:183], v[0:3], v[168:171], v[180:183]\n\t"
            "v_cvt_pk_bf16_f32 v192, v192, v193\n\t"
            "v_cvt_pk_bf16_f32 v193, v194, v195\n\t"
            "v_mfma_f32_16x16x32_bf16 v[180:183], v[4:7], v[172:175], v[180:183]\n\t"
            "v_add_u32 v236, s48, v244\n\t"
            "v_add_u32 v237, s48, v245\n\t"
            "v_cvt_pk_bf16_f32 v194, v176, v177\n\t"
            "s_waitcnt lgkmcnt(5)\n\t"
            "v_mfma_f32_16x16x32_bf16 v[180:183], v[8:11], v[144:147], v[180:183]\n\t"
            "ds_read_b128 v[200:203], v236\n\t"
            "v_cvt_pk_bf16_f32 v195, v178, v179\n\t"
            "s_waitcnt lgkmcnt(5)\n\t"
            "v_mfma_f32_16x16x32_bf16 v[180:183], v[12:15], v[148:151], v[180:183]\n\t"
            "ds_read_b128 v[204:207], v237\n\t"
            "v_lshlrev_b32 v176, 16, v220\n\t"
            "s_waitcnt lgkmcnt(5)\n\t"
            "v_mfma_f32_16x16x32_bf16 v[180:183], v[16:19], v[152:155], v[180:183]\n\t"
            "ds_read_b128 v[212:215], v236 offset:6144\n\t"
            "v_and_b32 v177, 0xffff0000, v220\n\t"
            "s_waitcnt lgkmcnt(5)\n\t"
            "v_mfma_f32_16x16x32_bf16 v[180:183], v[20:23], v[156:159], v[180:183]\n\t"
            "s_cbranch_vccnz .Lst2_%=\n\t"
            "buffer_store_dwordx4 v[192:195], v196, s[40:43], s45 offen offset:128\n\t"
            ".Lsr2_%=:\n\t"
            "v_mfma_f32_16x16x32_bf16 v[184:187], v[24:27], v[168:171], v[184:187]\n\t"
            "s_nop 0\n\t"
            "v_lshlrev_b32 v192, 16, v218\n\t"
            "v_and_b32 v193, 0xffff0000, v218\n\t"
            "v_mfma_f32_16x16x32_bf16 v[184:187], v[28:31], v[172:175], v[184:187]\n\t"
            "v_lshlrev_b32 v194, 16, v219\n\t"
            "v_and_b32 v195, 0xffff0000, v219\n\t"
            "v_mfma_f32_16x16x32_bf16 v[184:187], v[32:35], v[144:147], v[184:187]\n\t"
            "ds_read_b128 v[216:219], v237 offset:6144\n\t"
            "v_lshlrev_b32 v178, 16, v221\n\t"
            "v_mfma_f32_16x16x32_bf16 v[184:187], v[36:39], v[148:151], v[184:187]\n\t"
            "v_and_b32 v179, 0xffff0000, v221\n\t"
            "v_cvt_pk_bf16_f32 v180, v180, v181\n\t"
            "v_mfma_f32_16x16x32_bf16 v[184:187], v[40:43], v[152:155], v[184:187]\n\t"
            "v_cvt_pk_bf16_f32 v181, v182, v183\n\t"
            "v_add_u32 v248, s48, v246\n\t"
            "v_mfma_f32_16x16x32_bf16 v[184:187], v[44:47], v[156:159], v[184:187]\n\t"
            "ds_read_b128 v[208:211], v248\n\t"
            "v_mfma_f32_16x16x32_bf16 v[188:191], v[48:51], v[168:171], v[188:191]\n\t"
            "v_mfma_f32_16x16x32_bf16 v[188:191], v[52:55], v[172:175], v[188:191]\n\t"
            "v_mfma_f32_16x16x32_bf16 v[188:191], v[56:59], v[144:147], v[188:191]\n\t"
            "v_mfma_f32_16x16x32_bf16 v[188:191], v[60:63], v[148:151], v[188:191]\n\t"
            "v_mfma_f32_16x16x32_bf16 v[188:191], v[64:67], v[152:155], v[188:191]\n\t"
            "s_nop 1\n\t"
            "v_cvt_pk_bf16_f32 v182, v184, v185\n\t"
            "v_mfma_f32_16x16x32_bf16 v[188:191], v[68:71], v[156:159], v[188:191]\n\t"
            "v_cvt_pk_bf16_f32 v183, v186, v187\n\t"
            "v_lshlrev_b32 v184, 16, v224\n\t"
            "v_mfma_f32_16x16x32_bf16 v[192:195], v[72:75], v[168:171], v[192:195]\n\t"
            "s_cbranch_vccnz .Lst3_%=\n\t"
            "buffer_store_dwordx4 v[180:183], v197, s[40:43], s45 offen\n\t"
            ".Lsr3_%=:\n\t"
            "v_mfma_f32_16x16x32_bf16 v[192:195], v[76:79], v[172:175], v[192:195]\n\t"
            "s_nop 0\n\t"
            "v_lshlrev_b32 v180, 16, v222\n\t"
            "v_and_b32 v181, 0xffff0000, v222\n\t"
            "v_mfma_f32_16x16x32_bf16 v[192:195], v[80:83], v[144:147], v[192:195]\n\t"
            "v_lshlrev_b32 v182, 16, v223\n\t"
            "v_and_b32 v183, 0xffff0000, v223\n\t"
            "v_mfma_f32_16x16x32_bf16 v[192:195], v[84:87], v[148:151], v[192:195]\n\t"
            "ds_read_b128 v[220:223], v248 offset:6144\n\t"
            "v_and_b32 v185, 0xffff0000, v224\n\t"
            "v_mfma_f32_16x16x32_bf16 v[192:195], v[88:91], v[152:155], v[192:195]\n\t"
            "v_lshlrev_b32 v186, 16, v225\n\t"
            "v_and_b32 v187, 0xffff0000, v225\n\t"
            "v_mfma_f32_16x16x32_bf16 v[192:195], v[92:95], v[156:159], v[192:195]\n\t"
            "v_cvt_pk_bf16_f32 v188, v188, v189\n\t"
            "v_cvt_pk_bf16_f32 v189, v190, v191\n\t"
            "v_mfma_f32_16x16x32_bf16 v[176:179], v[96:99], v[168:171], v[176:179]\n\t"
            "v_mfma_f32_16x16x32_bf16 v[176:179], v[100:103], v[172:175], v[176:179]\n\t"
            "v_mfma_f32_16x16x32_bf16 v[176:179], v[104:107], v[144:147], v[176:179]\n\t"
            "v_mfma_f32_16x16x32_bf16 v[176:179], v[108:111], v[148:151], v[176:179]\n\t"
            "s_nop 1\n\t"
            "v_cvt_pk_bf16_f32 v190, v192, v193\n\t"
            "v_mfma_f32_16x16x32_bf16 v[176:179], v[112:115], v[152:155], v[176:179]\n\t"
            "v_cvt_pk_bf16_f32 v191, v194, v195\n\t"
            "v_lshlrev_b32 v192, 16, v228\n\t"
            "v_mfma_f32_16x16x32_bf16 v[176:179], v[116:119], v[156:159], v[176:179]\n\t"
            "s_cbranch_vccnz .Lst4_%=\n\t"
            "buffer_store_dwordx4 v[188:191], v197, s[40:43], s45 offen offset:64\n\t"
            ".Lsr4_%=:\n\t"
            "v_mfma_f32_16x16x32_bf16 v[180:183], v[120:123], v[168:171], v[180:183]\n\t"
            "ds_read_b128 v[168:171], v242 offset:12416\n\t"
            "v_lshlrev_b32 v188, 16, v226\n\t"
            "v_mfma_f32_16x16x32_bf16 v[180:183], v[124:127], v[172:175], v[180:183]\n\t"
            "ds_read_b128 v[172:175], v243 offset:12416\n\t"
            "v_and_b32 v189, 0xffff0000, v226\n\t"
            "v_mfma_f32_16x16x32_bf16 v[180:183], v[128:131], v[144:147], v[180:183]\n\t"
            "ds_read_b128 v[144:147], v242 offset:12544\n\t"
            "v_lshlrev_b32 v190, 16, v227\n\t"
            "v_mfma_f32_16x16x32_bf16 v[180:183], v[132:135], v[148:151], v[180:183]\n\t"
            "ds_read_b128 v[148:151], v243 offset:12544\n\t"
            "v_and_b32 v191, 0xffff0000, v227\n\t"
            "v_mfma_f32_16x16x32_bf16 v[180:183], v[136:139], v[152:155], v[180:183]\n\t"
            "s_waitcnt lgkmcnt(0)\n\t"
            "s_barrier\n\t"
            "ds_read_b128 v[224:227], v236 offset:12288\n\t"
            "v_and_b32 v193, 0xffff0000, v228\n\t"
            "v_mfma_f32_16x16x32_bf16 v[180:183], v[140:143], v[156:159], v[180:183]\n\t"
            "v_lshlrev_b32 v194, 16, v229\n\t"
            "v_and_b32 v195, 0xffff0000, v229\n\t"
            "v_mfma_f32_16x16x32_bf16 v[184:187], v[0:3], v[160:163], v[184:187]\n\t"
            "v_cvt_pk_bf16_f32 v176, v176, v177\n\t"
            "v_cvt_pk_bf16_f32 v177, v178, v179\n\t"
            "v_mfma_f32_16x16x32_bf16 v[184:187], v[4:7], v[164:167], v[184:187]\n\t"
            "s_nop 1\n\t"
            "v_cvt_pk_bf16_f32 v178, v180, v181\n\t"
            "v_mfma_f32_16x16x32_bf16 v[184:187], v[8:11], v[168:171], v[184:187]\n\t"
            "v_cvt_pk_bf16_f32 v179, v182, v183\n\t"
            "v_lshlrev_b32 v180, 16, v232\n\t"
            "v_mfma_f32_16x16x32_bf16 v[184:187], v[12:15], v[172:175], v[184:187]\n\t"
            "s_cbranch_vccnz .Lst5_%=\n\t"
            "buffer_store_dwordx4 v[176:179], v197, s[40:43], s45 offen offset:128\n\t"
            ".Lsr5_%=:\n\t"
            "v_mfma_f32_16x16x32_bf16 v[184:187], v[16:19], v[144:147], v[184:187]\n\t"
            "s_nop 0\n\t"
            "v_lshlrev_b32 v176, 16, v230\n\t"
            "v_and_b32 v177, 0xffff0000, v230\n\t"
            "v_mfma_f32_16x16x32_bf16 v[184:187], v[20:23], v[148:151], v[184:187]\n\t"
            "v_lshlrev_b32 v178, 16, v231\n\t"
            "v_and_b32 v179, 0xffff0000, v231\n\t"
            "v_mfma_f32_16x16x32_bf16 v[188:191], v[24:27], v[160:163], v[188:191]\n\t"
            "ds_read_b128 v[228:231], v237 offset:12288\n\t"
            "v_and_b32 v181, 0xffff0000, v232\n\t"
            "v_mfma_f32_16x16x32_bf16 v[188:191], v[28:31], v[164:167], v[188:191]\n\t"
            "v_lshlrev_b32 v182, 16, v233\n\t"
            "v_and_b32 v183, 0xffff0000, v233\n\t"
            "v_mfma_f32_16x16x32_bf16 v[188:191], v[32:35], v[168:171], v[188:191]\n\t"
            "v_cvt_pk_bf16_f32 v184, v184, v185\n\t"
            "v_cvt_pk_bf16_f32 v185, v186, v187\n\t"
            "v_mfma_f32_16x16x32_bf16 v[188:191], v[36:39], v[172:175], v[188:191]\n\t"
            "v_mfma_f32_16x16x32_bf16 v[188:191], v[40:43], v[144:147], v[188:191]\n\t"
            "v_mfma_f32_16x16x32_bf16 v[188:191], v[44:47], v[148:151], v[188:191]\n\t"
            "v_mfma_f32_16x16x32_bf16 v[192:195], v[48:51], v[160:163], v[192:195]\n\t"
            "v_mfma_f32_16x16x32_bf16 v[192:195], v[52:55], v[164:167], v[192:195]\n\t"
            "v_mfma_f32_16x16x32_bf16 v[192:195], v[56:59], v[168:171], v[192:195]\n\t"
            "v_mfma_f32_16x16x32_bf16 v[192:195], v[60:63], v[172:175], v[192:195]\n\t"
            "v_mfma_f32_16x16x32_bf16 v[192:195], v[64:67], v[144:147], v[192:195]\n\t"
            "v_mfma_f32_16x16x32_bf16 v[192:195], v[68:71], v[148:151], v[192:195]\n\t"
            "s_nop 1\n\t"
            "v_cvt_pk_bf16_f32 v186, v188, v189\n\t"
            "v_mfma_f32_16x16x32_bf16 v[176:179], v[72:75], v[160:163], v[176:179]\n\t"
            "v_cvt_pk_bf16_f32 v187, v190, v191\n\t"
            "v_mfma_f32_16x16x32_bf16 v[176:179], v[76:79], v[164:167], v[176:179]\n\t"
            "s_cbranch_vccnz .Lst6_%=\n\t"
            "buffer_store_dwordx4 v[184:187], v198, s[40:43], s45 offen\n\t"
            ".Lsr6_%=:\n\t"
            "v_mfma_f32_16x16x32_bf16 v[176:179], v[80:83], v[168:171], v[176:179]\n\t"
            "s_nop 0\n\t"
            "v_lshlrev_b32 v184, 16, v234\n\t"
            "v_and_b32 v185, 0xffff0000, v234\n\t"
            "v_mfma_f32_16x16x32_bf16 v[176:179], v[84:87], v[172:175], v[176:179]\n\t"
            "v_lshlrev_b32 v186, 16, v235\n\t"
            "v_and_b32 v187, 0xffff0000, v235\n\t"
            "v_mfma_f32_16x16x32_bf16 v[176:179], v[88:91], v[144:147], v[176:179]\n\t"
            "ds_read_b128 v[232:235], v248 offset:12288\n\t"
            "v_cvt_pk_bf16_f32 v192, v192, v193\n\t"
            "v_mfma_f32_16x16x32_bf16 v[176:179], v[92:95], v[148:151], v[176:179]\n\t"
            "v_cvt_pk_bf16_f32 v193, v194, v195\n\t"
            "v_mfma_f32_16x16x32_bf16 v[180:183], v[96:99], v[160:163], v[180:183]\n\t"
            "v_mfma_f32_16x16x32_bf16 v[180:183], v[100:103], v[164:167], v[180:183]\n\t"
            "v_mfma_f32_16x16x32_bf16 v[180:183], v[104:107], v[168:171], v[180:183]\n\t"
            "v_mfma_f32_16x16x32_bf16 v[180:183], v[108:111], v[172:175], v[180:183]\n\t"
            "v_mfma_f32_16x16x32_bf16 v[180:183], v[112:115], v[144:147], v[180:183]\n\t"
            "s_nop 1\n\t"
            "v_cvt_pk_bf16_f32 v194, v176, v177\n\t"
            "v_mfma_f32_16x16x32_bf16 v[180:183], v[116:119], v[148:151], v[180:183]\n\t"
            "v_cvt_pk_bf16_f32 v195, v178, v179\n\t"
            "v_mfma_f32_16x16x32_bf16 v[184:187], v[120:123], v[160:163], v[184:187]\n\t"
            "s_cbranch_vccnz .Lst7_%=\n\t"
            "buffer_store_dwordx4 v[192:195], v198, s[40:43], s45 offen offset:64\n\t"
            ".Lsr7_%=:\n\t"
            "v_mfma_f32_16x16x32_bf16 v[184:187], v[124:127], v[164:167], v[184:187]\n\t"
            "s_waitcnt vmcnt(8)\n\t"
            "ds_read_b64 v[152:153], v253 offset:8\n\t"
            "v_mfma_f32_16x16x32_bf16 v[184:187], v[128:131], v[168:171], v[184:187]\n\t"
            "ds_read_b128 v[168:171], v254\n\t"
            "v_cvt_pk_bf16_f32 v180, v180, v181\n\t"
            "v_mfma_f32_16x16x32_bf16 v[184:187], v[132:135], v[172:175], v[184:187]\n\t"
            "ds_read_b128 v[172:175], v254 offset:64\n\t"
            "v_cvt_pk_bf16_f32 v181, v182, v183\n\t"
            "v_mfma_f32_16x16x32_bf16 v[184:187], v[136:139], v[144:147], v[184:187]\n\t"
            "ds_read_b64 v[144:145], v253\n\t"
            "v_mfma_f32_16x16x32_bf16 v[184:187], v[140:143], v[148:151], v[184:187]\n\t"
            "ds_read_b64 v[146:147], v253 offset:96\n\t"
            "ds_read_b64 v[148:149], v253 offset:1744\n\t"
            "ds_read_b64 v[150:151], v253 offset:1840\n\t"
            "ds_read_b64 v[154:155], v253 offset:104\n\t"
            "ds_read_b64 v[156:157], v253 offset:1752\n\t"
            "ds_read_b64 v[158:159], v253 offset:1848\n\t"
            "ds_read_b64 v[160:161], v253 offset:16\n\t"
            "ds_read_b64 v[162:163], v253 offset:112\n\t"
            "v_cvt_pk_bf16_f32 v182, v184, v185\n\t"
            "v_cvt_pk_bf16_f32 v183, v186, v187\n\t"
            "s_waitcnt lgkmcnt(14)\n\t"
            "ds_read_b64 v[164:165], v253 offset:1760\n\t"
            "s_waitcnt lgkmcnt(14)\n\t"
            "ds_read_b64 v[166:167], v253 offset:1856\n\t"
            "s_cbranch_vccnz .Lst8_%=\n\t"
            "buffer_store_dwordx4 v[180:183], v198, s[40:43], s45 offen offset:128\n\t"
            ".Lsr8_%=:\n\t"
            "s_add_u32 s45, s46, s27\n\t"
            "s_mov_b32 s46, s47\n\t"
            "s_mov_b32 s47, s68\n\t"
            "s_xor_b32 s48, s48, s71\n\t"
            "s_xor_b32 s67, s67, s71\n\t"
            "s_add_u32 s76, s76, 4\n\t"
            "s_lshl_b32 s56, s56, 2\n\t"
            "s_lshr_b32 s49, s26, 30\n\t"
            "s_or_b32 s56, s56, s49\n\t"
            "s_cmp_ge_u32 s76, s24\n\t"
            "s_cselect_b64 vcc, -1, 0\n\t"
            "s_cmp_lt_u32 s46, 0x7000000\n\t"
            "s_cbranch_scc1 .Lwb_loop%=\n\t"
            "s_branch .Lsx%=\n\t"
            ".Lst0_%=:\n\t"
            "buffer_store_dwordx4 v[176:179], v196, s[40:43], s45 offen sc1\n\t"
            "s_branch .Lsr0_%=\n\t"
            ".Lst1_%=:\n\t"
            "buffer_store_dwordx4 v[184:187], v196, s[40:43], s45 offen offset:64 sc1\n\t"
            "s_branch .Lsr1_%=\n\t"
            ".Lst2_%=:\n\t"
            "buffer_store_dwordx4 v[192:195], v196, s[40:43], s45 offen offset:128 sc1\n\t"
            "s_branch .Lsr2_%=\n\t"
            ".Lst3_%=:\n\t"
            "buffer_store_dwordx4 v[180:183], v197, s[40:43], s45 offen sc1\n\t"
            "s_branch .Lsr3_%=\n\t"
            ".Lst4_%=:\n\t"
            "buffer_store_dwordx4 v[188:191], v197, s[40:43], s45 offen offset:64 sc1\n\t"
            "s_branch .Lsr4_%=\n\t"
            ".Lst5_%=:\n\t"
            "buffer_store_dwordx4 v[176:179], v197, s[40:43], s45 offen offset:128 sc1\n\t"
            "s_branch .Lsr5_%=\n\t"
            ".Lst6_%=:\n\t"
            "buffer_store_dwordx4 v[184:187], v198, s[40:43], s45 offen sc1\n\t"
            "s_branch .Lsr6_%=\n\t"
            ".Lst7_%=:\n\t"
            "buffer_store_dwordx4 v[192:195], v198, s[40:43], s45 offen offset:64 sc1\n\t"
            "s_branch .Lsr7_%=\n\t"
            ".Lst8_%=:\n\t"
            "buffer_store_dwordx4 v[180:183], v198, s[40:43], s45 offen offset:128 sc1\n\t"
            "s_branch .Lsr8_%=\n\t"
            ".Lsx%=:\n\t"
            "s_waitcnt lgkmcnt(0)\n\t"
            "s_nop 7\n\t"
            "s_setprio 3\n\t"
            "s_nop 7\n\t"
            "ds_read_b128 v[196:199], v252\n\t"
            "s_bfe_u32 s57, s56, 0x20004\n\t"
            "s_lshl_b32 s58, s57, 3\n\t"
            "s_lshr_b32 s58, s55, s58\n\t"
            "s_lshl_b64 s[36:37], s[34:35], s58\n\t"
            "s_bitcmp1_b32 s57, 1\n\t"
            "s_cselect_b32 s58, 1177344, 0\n\t"
            "s_bitcmp1_b32 s57, 0\n\t"
            "s_cselect_b32 s57, 2304, 0\n\t"
            "s_add_u32 s57, s57, s58\n\t"
            "s_waitcnt lgkmcnt(0)\n\t"
            "s_mov_b64 exec, s[36:37]\n\t"
            "v_add_u32 v196, s57, v196\n\t"
            "s_mov_b64 exec, -1\n\t"
            "s_nop 7\n\t"
            "v_mov_b32 v144, s23\n\t"
            ".Ldp%=:\n\t"
            "ds_read_b128 v[148:151], v144 offset:96\n\t"
            "s_waitcnt lgkmcnt(0)\n\t"
            "v_or_b32 v148, v148, v149\n\t"
            "v_or3_b32 v148, v148, v150, v151\n\t"
            "v_cmp_gt_i32 vcc, 0, v148\n\t"
            "s_nop 4\n\t"
            "s_cbranch_vccnz .Ldp%=\n\t"
            "s_nop 7\n\t"
            "ds_read_b128 v[184:187], v242\n\t"
            "v_lshlrev_b32 v152, 16, v200\n\t"
            "v_and_b32 v153, 0xffff0000, v200\n\t"
            "v_lshlrev_b32 v154, 16, v201\n\t"
            "v_and_b32 v155, 0xffff0000, v201\n\t"
            "v_lshlrev_b32 v156, 16, v202\n\t"
            "v_and_b32 v157, 0xffff0000, v202\n\t"
            "v_lshlrev_b32 v158, 16, v203\n\t"
            "v_and_b32 v159, 0xffff0000, v203\n\t"
            "v_lshlrev_b32 v160, 16, v204\n\t"
            "v_and_b32 v161, 0xffff0000, v204\n\t"
            "v_lshlrev_b32 v162, 16, v205\n\t"
            "v_and_b32 v163, 0xffff0000, v205\n\t"
            "v_lshlrev_b32 v164, 16, v206\n\t"
            "v_and_b32 v165, 0xffff0000, v206\n\t"
            "v_lshlrev_b32 v166, 16, v207\n\t"
            "v_and_b32 v167, 0xffff0000, v207\n\t"
            "v_lshlrev_b32 v176, 16, v208\n\t"
            "v_and_b32 v177, 0xffff0000, v208\n\t"
            "v_lshlrev_b32 v178, 16, v209\n\t"
            "v_and_b32 v179, 0xffff0000, v209\n\t"
            "v_lshlrev_b32 v180, 16, v210\n\t"
            "v_and_b32 v181, 0xffff0000, v210\n\t"
            "v_lshlrev_b32 v182, 16, v211\n\t"
            "v_and_b32 v183, 0xffff0000, v211\n\t"
            "v_lshlrev_b32 v144, 16, v212\n\t"
            "v_and_b32 v145, 0xffff0000, v212\n\t"
            "v_lshlrev_b32 v146, 16, v213\n\t"
            "v_and_b32 v147, 0xffff0000, v213\n\t"
            "v_lshlrev_b32 v148, 16, v214\n\t"
            "v_and_b32 v149, 0xffff0000, v214\n\t"
            "s_waitcnt lgkmcnt(0)\n\t"
            "v_mfma_f32_16x16x32_bf16 v[152:155], v[0:3], v[184:187], v[152:155]\n\t"
            "ds_read_b128 v[188:191], v243\n\t"
            "v_lshlrev_b32 v150, 16, v215\n\t"
            "v_mfma_f32_16x16x32_bf16 v[156:159], v[24:27], v[184:187], v[156:159]\n\t"
            "ds_read_b128 v[192:195], v242 offset:128\n\t"
            "v_and_b32 v151, 0xffff0000, v215\n\t"
            "v_mfma_f32_16x16x32_bf16 v[160:163], v[48:51], v[184:187], v[160:163]\n\t"
            "v_lshlrev_b32 v168, 16, v216\n\t"
            "v_and_b32 v169, 0xffff0000, v216\n\t"
            "v_mfma_f32_16x16x32_bf16 v[164:167], v[72:75], v[184:187], v[164:167]\n\t"
            "v_lshlrev_b32 v170, 16, v217\n\t"
            "v_and_b32 v171, 0xffff0000, v217\n\t"
            "v_mfma_f32_16x16x32_bf16 v[176:179], v[96:99], v[184:187], v[176:179]\n\t"
            "v_lshlrev_b32 v172, 16, v218\n\t"
            "v_and_b32 v173, 0xffff0000, v218\n\t"
            "v_mfma_f32_16x16x32_bf16 v[180:183], v[120:123], v[184:187], v[180:183]\n\t"
            "ds_read_b128 v[184:187], v243 offset:128\n\t"
            "v_lshlrev_b32 v174, 16, v219\n\t"
            "v_and_b32 v175, 0xffff0000, v219\n\t"
            "s_waitcnt lgkmcnt(2)\n\t"
            "v_mfma_f32_16x16x32_bf16 v[152:155], v[4:7], v[188:191], v[152:155]\n\t"
            "v_mfma_f32_16x16x32_bf16 v[156:159], v[28:31], v[188:191], v[156:159]\n\t"
            "v_mfma_f32_16x16x32_bf16 v[160:163], v[52:55], v[188:191], v[160:163]\n\t"
            "v_mfma_f32_16x16x32_bf16 v[164:167], v[76:79], v[188:191], v[164:167]\n\t"
            "v_mfma_f32_16x16x32_bf16 v[176:179], v[100:103], v[188:191], v[176:179]\n\t"
            "v_mfma_f32_16x16x32_bf16 v[180:183], v[124:127], v[188:191], v[180:183]\n\t"
            "ds_read_b128 v[188:191], v242 offset:256\n\t"
            "s_waitcnt lgkmcnt(2)\n\t"
            "v_mfma_f32_16x16x32_bf16 v[152:155], v[8:11], v[192:195], v[152:155]\n\t"
            "v_mfma_f32_16x16x32_bf16 v[156:159], v[32:35], v[192:195], v[156:159]\n\t"
            "v_mfma_f32_16x16x32_bf16 v[160:163], v[56:59], v[192:195], v[160:163]\n\t"
            "v_mfma_f32_16x16x32_bf16 v[164:167], v[80:83], v[192:195], v[164:167]\n\t"
            "v_mfma_f32_16x16x32_bf16 v[176:179], v[104:107], v[192:195], v[176:179]\n\t"
            "v_mfma_f32_16x16x32_bf16 v[180:183], v[128:131], v[192:195], v[180:183]\n\t"
            "ds_read_b128 v[192:195], v243 offset:256\n\t"
            "s_waitcnt lgkmcnt(2)\n\t"
            "v_mfma_f32_16x16x32_bf16 v[152:155], v[12:15], v[184:187], v[152:155]\n\t"
            "v_mfma_f32_16x16x32_bf16 v[156:159], v[36:39], v[184:187], v[156:159]\n\t"
            "v_mfma_f32_16x16x32_bf16 v[160:163], v[60:63], v[184:187], v[160:163]\n\t"
            "v_mfma_f32_16x16x32_bf16 v[164:167], v[84:87], v[184:187], v[164:167]\n\t"
            "v_mfma_f32_16x16x32_bf16 v[176:179], v[108:111], v[184:187], v[176:179]\n\t"
            "v_mfma_f32_16x16x32_bf16 v[180:183], v[132:135], v[184:187], v[180:183]\n\t"
            "ds_read_b128 v[184:187], v242 offset:6144\n\t"
            "s_waitcnt lgkmcnt(2)\n\t"
            "v_mfma_f32_16x16x32_bf16 v[152:155], v[16:19], v[188:191], v[152:155]\n\t"
            "v_mfma_f32_16x16x32_bf16 v[156:159], v[40:43], v[188:191], v[156:159]\n\t"
            "v_mfma_f32_16x16x32_bf16 v[160:163], v[64:67], v[188:191], v[160:163]\n\t"
            "v_mfma_f32_16x16x32_bf16 v[164:167], v[88:91], v[188:191], v[164:167]\n\t"
            "v_mfma_f32_16x16x32_bf16 v[176:179], v[112:115], v[188:191], v[176:179]\n\t"
            "v_mfma_f32_16x16x32_bf16 v[180:183], v[136:139], v[188:191], v[180:183]\n\t"
            "ds_read_b128 v[188:191], v243 offset:6144\n\t"
            "s_waitcnt lgkmcnt(2)\n\t"
            "v_mfma_f32_16x16x32_bf16 v[152:155], v[20:23], v[192:195], v[152:155]\n\t"
            "v_mfma_f32_16x16x32_bf16 v[156:159], v[44:47], v[192:195], v[156:159]\n\t"
            "v_mfma_f32_16x16x32_bf16 v[160:163], v[68:71], v[192:195], v[160:163]\n\t"
            "v_mfma_f32_16x16x32_bf16 v[164:167], v[92:95], v[192:195], v[164:167]\n\t"
            "v_mfma_f32_16x16x32_bf16 v[176:179], v[116:119], v[192:195], v[176:179]\n\t"
            "v_mfma_f32_16x16x32_bf16 v[180:183], v[140:143], v[192:195], v[180:183]\n\t"
            "ds_read_b128 v[192:195], v242 offset:6272\n\t"
            "s_waitcnt lgkmcnt(2)\n\t"
            "v_mfma_f32_16x16x32_bf16 v[144:147], v[0:3], v[184:187], v[144:147]\n\t"
            "v_cvt_pk_bf16_f32 v152, v152, v153\n\t"
            "v_cvt_pk_bf16_f32 v153, v154, v155\n\t"
            "v_mfma_f32_16x16x32_bf16 v[148:151], v[24:27], v[184:187], v[148:151]\n\t"
            "v_cvt_pk_bf16_f32 v160, v160, v161\n\t"
            "v_cvt_pk_bf16_f32 v161, v162, v163\n\t"
            "v_mfma_f32_16x16x32_bf16 v[168:171], v[48:51], v[184:187], v[168:171]\n\t"
            "v_cvt_pk_bf16_f32 v154, v156, v157\n\t"
            "v_cvt_pk_bf16_f32 v155, v158, v159\n\t"
            "v_mfma_f32_16x16x32_bf16 v[172:175], v[72:75], v[184:187], v[172:175]\n\t"
            "v_cvt_pk_bf16_f32 v162, v164, v165\n\t"
            "v_cvt_pk_bf16_f32 v163, v166, v167\n\t"
            "s_waitcnt lgkmcnt(1)\n\t"
            "v_mfma_f32_16x16x32_bf16 v[144:147], v[4:7], v[188:191], v[144:147]\n\t"
            "v_lshlrev_b32 v156, 16, v220\n\t"
            "v_and_b32 v157, 0xffff0000, v220\n\t"
            "v_mfma_f32_16x16x32_bf16 v[148:151], v[28:31], v[188:191], v[148:151]\n\t"
            "v_lshlrev_b32 v158, 16, v221\n\t"
            "v_and_b32 v159, 0xffff0000, v221\n\t"
            "v_mfma_f32_16x16x32_bf16 v[168:171], v[52:55], v[188:191], v[168:171]\n\t"
            "v_lshlrev_b32 v164, 16, v222\n\t"
            "v_and_b32 v165, 0xffff0000, v222\n\t"
            "v_mfma_f32_16x16x32_bf16 v[156:159], v[96:99], v[184:187], v[156:159]\n\t"
            "v_lshlrev_b32 v166, 16, v223\n\t"
            "v_and_b32 v167, 0xffff0000, v223\n\t"
            "v_mfma_f32_16x16x32_bf16 v[172:175], v[76:79], v[188:191], v[172:175]\n\t"
            "buffer_store_dwordx4 v[152:155], v196, s[40:43], s45 offen sc1\n\t"
            "v_mfma_f32_16x16x32_bf16 v[164:167], v[120:123], v[184:187], v[164:167]\n\t"
            "ds_read_b128 v[184:187], v243 offset:6272\n\t"
            "v_cvt_pk_bf16_f32 v176, v176, v177\n\t"
            "v_mfma_f32_16x16x32_bf16 v[156:159], v[100:103], v[188:191], v[156:159]\n\t"
            "buffer_store_dwordx4 v[160:163], v196, s[40:43], s45 offen offset:64 sc1\n\t"
            "v_mfma_f32_16x16x32_bf16 v[164:167], v[124:127], v[188:191], v[164:167]\n\t"
            "ds_read_b128 v[188:191], v242 offset:6400\n\t"
            "v_cvt_pk_bf16_f32 v177, v178, v179\n\t"
            "s_waitcnt lgkmcnt(2)\n\t"
            "v_mfma_f32_16x16x32_bf16 v[144:147], v[8:11], v[192:195], v[144:147]\n\t"
            "v_cvt_pk_bf16_f32 v178, v180, v181\n\t"
            "v_cvt_pk_bf16_f32 v179, v182, v183\n\t"
            "v_mfma_f32_16x16x32_bf16 v[148:151], v[32:35], v[192:195], v[148:151]\n\t"
            "buffer_store_dwordx4 v[176:179], v196, s[40:43], s45 offen offset:128 sc1\n\t"
            "v_mfma_f32_16x16x32_bf16 v[168:171], v[56:59], v[192:195], v[168:171]\n\t"
            "v_lshlrev_b32 v152, 16, v224\n\t"
            "v_and_b32 v153, 0xffff0000, v224\n\t"
            "v_mfma_f32_16x16x32_bf16 v[172:175], v[80:83], v[192:195], v[172:175]\n\t"
            "v_lshlrev_b32 v154, 16, v225\n\t"
            "v_and_b32 v155, 0xffff0000, v225\n\t"
            "v_mfma_f32_16x16x32_bf16 v[156:159], v[104:107], v[192:195], v[156:159]\n\t"
            "v_lshlrev_b32 v180, 16, v226\n\t"
            "v_and_b32 v181, 0xffff0000, v226\n\t"
            "v_mfma_f32_16x16x32_bf16 v[164:167], v[128:131], v[192:195], v[164:167]\n\t"
            "ds_read_b128 v[192:195], v243 offset:6400\n\t"
            "v_lshlrev_b32 v182, 16, v227\n\t"
            "s_waitcnt lgkmcnt(2)\n\t"
            "v_mfma_f32_16x16x32_bf16 v[144:147], v[12:15], v[184:187], v[144:147]\n\t"
            "v_and_b32 v183, 0xffff0000, v227\n\t"
            "v_lshlrev_b32 v160, 16, v228\n\t"
            "v_mfma_f32_16x16x32_bf16 v[148:151], v[36:39], v[184:187], v[148:151]\n\t"
            "v_and_b32 v161, 0xffff0000, v228\n\t"
            "v_lshlrev_b32 v162, 16, v229\n\t"
            "s_waitcnt lgkmcnt(1)\n\t"
            "v_mfma_f32_16x16x32_bf16 v[144:147], v[16:19], v[188:191], v[144:147]\n\t"
            "v_and_b32 v163, 0xffff0000, v229\n\t"
            "v_lshlrev_b32 v176, 16, v230\n\t"
            "v_mfma_f32_16x16x32_bf16 v[168:171], v[60:63], v[184:187], v[168:171]\n\t"
            "v_and_b32 v177, 0xffff0000, v230\n\t"
            "v_lshlrev_b32 v178, 16, v231\n\t"
            "v_mfma_f32_16x16x32_bf16 v[148:151], v[40:43], v[188:191], v[148:151]\n\t"
            "v_and_b32 v179, 0xffff0000, v231\n\t"
            "v_mfma_f32_16x16x32_bf16 v[172:175], v[84:87], v[184:187], v[172:175]\n\t"
            "v_mfma_f32_16x16x32_bf16 v[168:171], v[64:67], v[188:191], v[168:171]\n\t"
            "v_mfma_f32_16x16x32_bf16 v[156:159], v[108:111], v[184:187], v[156:159]\n\t"
            "s_waitcnt lgkmcnt(0)\n\t"
            "v_mfma_f32_16x16x32_bf16 v[144:147], v[20:23], v[192:195], v[144:147]\n\t"
            "v_mfma_f32_16x16x32_bf16 v[148:151], v[44:47], v[192:195], v[148:151]\n\t"
            "v_mfma_f32_16x16x32_bf16 v[164:167], v[132:135], v[184:187], v[164:167]\n\t"
            "ds_read_b128 v[184:187], v242 offset:12288\n\t"
            "v_mfma_f32_16x16x32_bf16 v[172:175], v[88:91], v[188:191], v[172:175]\n\t"
            "v_mfma_f32_16x16x32_bf16 v[168:171], v[68:71], v[192:195], v[168:171]\n\t"
            "v_mfma_f32_16x16x32_bf16 v[156:159], v[112:115], v[188:191], v[156:159]\n\t"
            "s_nop 1\n\t"
            "v_cvt_pk_bf16_f32 v144, v144, v145\n\t"
            "v_mfma_f32_16x16x32_bf16 v[164:167], v[136:139], v[188:191], v[164:167]\n\t"
            "ds_read_b128 v[188:191], v243 offset:12288\n\t"
            "v_cvt_pk_bf16_f32 v145, v146, v147\n\t"
            "v_mfma_f32_16x16x32_bf16 v[172:175], v[92:95], v[192:195], v[172:175]\n\t"
            "v_cvt_pk_bf16_f32 v146, v148, v149\n\t"
            "v_cvt_pk_bf16_f32 v147, v150, v151\n\t"
            "v_mfma_f32_16x16x32_bf16 v[156:159], v[116:119], v[192:195], v[156:159]\n\t"
            "buffer_store_dwordx4 v[144:147], v197, s[40:43], s45 offen sc1\n\t"
            "v_mfma_f32_16x16x32_bf16 v[164:167], v[140:143], v[192:195], v[164:167]\n\t"
            "ds_read_b128 v[192:195], v242 offset:12416\n\t"
            "v_cvt_pk_bf16_f32 v168, v168, v169\n\t"
            "s_waitcnt lgkmcnt(2)\n\t"
            "v_mfma_f32_16x16x32_bf16 v[152:155], v[0:3], v[184:187], v[152:155]\n\t"
            "v_cvt_pk_bf16_f32 v169, v170, v171\n\t"
            "v_cvt_pk_bf16_f32 v170, v172, v173\n\t"
            "v_mfma_f32_16x16x32_bf16 v[180:183], v[24:27], v[184:187], v[180:183]\n\t"
            "v_cvt_pk_bf16_f32 v171, v174, v175\n\t"
            "v_cvt_pk_bf16_f32 v156, v156, v157\n\t"
            "v_mfma_f32_16x16x32_bf16 v[160:163], v[48:51], v[184:187], v[160:163]\n\t"
            "buffer_store_dwordx4 v[168:171], v197, s[40:43], s45 offen offset:64 sc1\n\t"
            "v_mfma_f32_16x16x32_bf16 v[176:179], v[72:75], v[184:187], v[176:179]\n\t"
            "v_cvt_pk_bf16_f32 v157, v158, v159\n\t"
            "v_cvt_pk_bf16_f32 v158, v164, v165\n\t"
            "s_waitcnt lgkmcnt(1)\n\t"
            "v_mfma_f32_16x16x32_bf16 v[152:155], v[4:7], v[188:191], v[152:155]\n\t"
            "v_cvt_pk_bf16_f32 v159, v166, v167\n\t"
            "v_lshlrev_b32 v164, 16, v232\n\t"
            "v_mfma_f32_16x16x32_bf16 v[180:183], v[28:31], v[188:191], v[180:183]\n\t"
            "buffer_store_dwordx4 v[156:159], v197, s[40:43], s45 offen offset:128 sc1\n\t"
            "v_mfma_f32_16x16x32_bf16 v[160:163], v[52:55], v[188:191], v[160:163]\n\t"
            "v_and_b32 v165, 0xffff0000, v232\n\t"
            "v_lshlrev_b32 v166, 16, v233\n\t"
            "v_mfma_f32_16x16x32_bf16 v[176:179], v[76:79], v[188:191], v[176:179]\n\t"
            "v_and_b32 v167, 0xffff0000, v233\n\t"
            "v_lshlrev_b32 v156, 16, v234\n\t"
            "s_waitcnt lgkmcnt(0)\n\t"
            "v_mfma_f32_16x16x32_bf16 v[152:155], v[8:11], v[192:195], v[152:155]\n\t"
            "v_and_b32 v157, 0xffff0000, v234\n\t"
            "v_lshlrev_b32 v158, 16, v235\n\t"
            "v_mfma_f32_16x16x32_bf16 v[164:167], v[96:99], v[184:187], v[164:167]\n\t"
            "v_and_b32 v159, 0xffff0000, v235\n\t"
            "v_mfma_f32_16x16x32_bf16 v[164:167], v[100:103], v[188:191], v[164:167]\n\t"
            "s_nop 0\n\t"
            "v_mfma_f32_16x16x32_bf16 v[156:159], v[120:123], v[184:187], v[156:159]\n\t"
            "ds_read_b128 v[184:187], v243 offset:12416\n\t"
            "v_mfma_f32_16x16x32_bf16 v[156:159], v[124:127], v[188:191], v[156:159]\n\t"
            "ds_read_b128 v[188:191], v242 offset:12544\n\t"
            "v_mfma_f32_16x16x32_bf16 v[180:183], v[32:35], v[192:195], v[180:183]\n\t"
            "v_mfma_f32_16x16x32_bf16 v[160:163], v[56:59], v[192:195], v[160:163]\n\t"
            "v_mfma_f32_16x16x32_bf16 v[176:179], v[80:83], v[192:195], v[176:179]\n\t"
            "v_mfma_f32_16x16x32_bf16 v[164:167], v[104:107], v[192:195], v[164:167]\n\t"
            "v_mfma_f32_16x16x32_bf16 v[156:159], v[128:131], v[192:195], v[156:159]\n\t"
            "ds_read_b128 v[192:195], v243 offset:12544\n\t"
            "s_waitcnt lgkmcnt(2)\n\t"
            "v_mfma_f32_16x16x32_bf16 v[152:155], v[12:15], v[184:187], v[152:155]\n\t"
            "v_mfma_f32_16x16x32_bf16 v[180:183], v[36:39], v[184:187], v[180:183]\n\t"
            "s_waitcnt lgkmcnt(1)\n\t"
            "v_mfma_f32_16x16x32_bf16 v[152:155], v[16:19], v[188:191], v[152:155]\n\t"
            "v_mfma_f32_16x16x32_bf16 v[160:163], v[60:63], v[184:187], v[160:163]\n\t"
            "v_mfma_f32_16x16x32_bf16 v[180:183], v[40:43], v[188:191], v[180:183]\n\t"
            "v_mfma_f32_16x16x32_bf16 v[176:179], v[84:87], v[184:187], v[176:179]\n\t"
            "s_waitcnt lgkmcnt(0)\n\t"
            "v_mfma_f32_16x16x32_bf16 v[152:155], v[20:23], v[192:195], v[152:155]\n\t"
            "v_mfma_f32_16x16x32_bf16 v[160:163], v[64:67], v[188:191], v[160:163]\n\t"
            "v_mfma_f32_16x16x32_bf16 v[180:183], v[44:47], v[192:195], v[180:183]\n\t"
            "v_mfma_f32_16x16x32_bf16 v[164:167], v[108:111], v[184:187], v[164:167]\n\t"
            "v_mfma_f32_16x16x32_bf16 v[176:179], v[88:91], v[188:191], v[176:179]\n\t"
            "v_mfma_f32_16x16x32_bf16 v[156:159], v[132:135], v[184:187], v[156:159]\n\t"
            "v_mfma_f32_16x16x32_bf16 v[160:163], v[68:71], v[192:195], v[160:163]\n\t"
            "s_nop 1\n\t"
            "v_cvt_pk_bf16_f32 v152, v152, v153\n\t"
            "v_mfma_f32_16x16x32_bf16 v[164:167], v[112:115], v[188:191], v[164:167]\n\t"
            "v_cvt_pk_bf16_f32 v153, v154, v155\n\t"
            "v_mfma_f32_16x16x32_bf16 v[176:179], v[92:95], v[192:195], v[176:179]\n\t"
            "v_cvt_pk_bf16_f32 v154, v180, v181\n\t"
            "v_cvt_pk_bf16_f32 v155, v182, v183\n\t"
            "v_mfma_f32_16x16x32_bf16 v[156:159], v[136:139], v[188:191], v[156:159]\n\t"
            "buffer_store_dwordx4 v[152:155], v198, s[40:43], s45 offen sc1\n\t"
            "v_mfma_f32_16x16x32_bf16 v[164:167], v[116:119], v[192:195], v[164:167]\n\t"
            "v_cvt_pk_bf16_f32 v160, v160, v161\n\t"
            "v_cvt_pk_bf16_f32 v161, v162, v163\n\t"
            "v_mfma_f32_16x16x32_bf16 v[156:159], v[140:143], v[192:195], v[156:159]\n\t"
            "v_cvt_pk_bf16_f32 v162, v176, v177\n\t"
            "v_cvt_pk_bf16_f32 v163, v178, v179\n\t"
            "buffer_store_dwordx4 v[160:163], v198, s[40:43], s45 offen offset:64 sc1\n\t"
            "s_nop 1\n\t"
            "v_cvt_pk_bf16_f32 v164, v164, v165\n\t"
            "v_cvt_pk_bf16_f32 v165, v166, v167\n\t"
            "s_nop 0\n\t"
            "v_cvt_pk_bf16_f32 v166, v156, v157\n\t"
            "v_cvt_pk_bf16_f32 v167, v158, v159\n\t"
            "buffer_store_dwordx4 v[164:167], v198, s[40:43], s45 offen offset:128 sc1\n\t"
            "s_nop 7\n\t"
            ".Lre%=:\n\t"
            : "+{v[128:131]}"(f0), "+{v[132:135]}"(f1), "+{v[136:139]}"(f2), "+{v242}"(r242), "+{v243}"(r243), "+{v[244:247]}"(r244),
              "+{s45}"(s45), "+{s46}"(s46), "+{s47}"(s47), "+{s48}"(s48), "+{s67}"(s67), "+{s76}"(s76), "+{s56}"(s56), "+{s44}"(s44), "+{s70}"(s70), "+{s21}"(s21)
            : B_VIN, B_SIN
            : "v0", "v1", "v2", "v3", "v4", "v5", "v6", "v7", "v8", "v9", "v10", "v11", "v12", "v13", "v14", "v15", "v16", "v17", "v18", "v19", "v20", "v21", "v22", "v23", "v24", "v25", "v26", "v27", "v28", "v29", "v30", "v31", "v32", "v33", "v34", "v35", "v36", "v37", "v38", "v39", "v40", "v41", "v42", "v43", "v44", "v45", "v46", "v47", "v48", "v49", "v50", "v51", "v52", "v53", "v54", "v55", "v56", "v57", "v58", "v59", "v60", "v61", "v62", "v63", "v64", "v65", "v66", "v67", "v68", "v69", "v70", "v71", "v72", "v73", "v74", "v75", "v76", "v77", "v78", "v79", "v80", "v81", "v82", "v83", "v84", "v85", "v86", "v87", "v88", "v89", "v90", "v91", "v92", "v93", "v94", "v95", "v96", "v97", "v98", "v99", "v100", "v101", "v102", "v103", "v104", "v105", "v106", "v107", "v108", "v109", "v110", "v111", "v112", "v113", "v114", "v115", "v116", "v117", "v118", "v119", "v120", "v121", "v122", "v123", "v124", "v125", "v126", "v127", "v140", "v141", "v142", "v143", "v144", "v145", "v146", "v147", "v148", "v149", "v150", "v151", "v152", "v153", "v154", "v155", "v156", "v157", "v158", "v159", "v160", "v161", "v162", "v163", "v164", "v165", "v166", "v167", "v168", "v169", "v170", "v171", "v172", "v173", "v174", "v175", "v176", "v177", "v178", "v179", "v180", "v181", "v182", "v183", "v184", "v185", "v186", "v187", "v188", "v189", "v190", "v191", "v192", "v193", "v194", "v195", "v196", "v197", "v198", "v199", "v200", "v201", "v202", "v203", "v204", "v205", "v206", "v207", "v208", "v209", "v210", "v211", "v212", "v213", "v214", "v215", "v216", "v217", "v218", "v219", "v220", "v221", "v222", "v223", "v224", "v225", "v226", "v227", "v228", "v229", "v230", "v231", "v232", "v233", "v234", "v235", "v236", "v237", "v248", "v249", "s26", "s68", "s69", "s79", "s36", "s37", "s38", "s57", "s58", "s59", "s49", "vcc", "scc", "m0", "memory");
        }
        asm volatile("wk_code_end1:");
        asm volatile("wk_code_end2:");

    }
}

extern "C" void kernel_launch(void* const* d_in, const int* in_sizes, int n_in,
                              void* d_out, int out_size, void* d_ws, size_t ws_size,
                              hipStream_t stream) {
    const unsigned short* x = (const unsigned short*)d_in[0];
    const unsigned short* dw = (const unsigned short*)d_in[1];
    const unsigned short* wqkv = (const unsigned short*)d_in[2];
    const unsigned short* wo = (const unsigned short*)d_in[3];
    unsigned short* out = (unsigned short*)d_out;
    u32x4* wf = (u32x4*)d_ws;
    win_attn_kernel<<<GRID, 512, 0, stream>>>(x, dw, wqkv, wo, wf, out);
}
```

```cpp
#include <hip/hip_runtime.h>
#include <hip/hip_bf16.h>
#include <stdint.h>

typedef short bf16x8 __attribute__((ext_vector_type(8)));
typedef float f32x4 __attribute__((ext_vector_type(4)));
typedef float f32x2 __attribute__((ext_vector_type(2)));
typedef unsigned u32x2 __attribute__((ext_vector_type(2)));
typedef unsigned u32x4 __attribute__((ext_vector_type(4)));

constexpr int IMG = 511;
constexpr int CH = 192;
constexpr int NWX = 73;
constexpr int NWIN = NWX * NWX;
constexpr int GRID = 256;
constexpr int NSHORT = 21 * GRID - NWIN;
constexpr int ROWBYTES = IMG * CH * 2;
constexpr int XBIAS = (IMG + 1) * CH * 2;
constexpr unsigned OOB = 0x40000000u;

constexpr int X_OFF = 32768;
constexpr int X1_OFF = 53632;
constexpr int O_OFF = 74496;
constexpr int V_OFF = 92928;
constexpr int KQL_OFF = 111360;
constexpr int OL_OFF = 128256;
constexpr int AT_OFF = 137472;
constexpr int CT_OFF = AT_OFF + 19584;
constexpr int RS_OFF = CT_OFF + 4096;
constexpr int WL_OFF = RS_OFF + 2304 + 64;
constexpr int LDS_BYTES = WL_OFF + 128;
#define RS_BASE (RS_OFF + (wave < 4 ? wave * 384 : 768 + wave * 192))
constexpr int TP_OFF = X1_OFF + 8192;
constexpr int WA = 12, WB = 4;
constexpr int STL_OFF = O_OFF + 12288;
#define LB_OFF(b) ((b) < 7 ? V_OFF + (b) * 6160 : X1_OFF + ((b) - 7) * 6160)
#define LPOS(wc) (((wc) & 1) ? 0 : 6)
constexpr unsigned CROW = 0x060a0230u;
constexpr unsigned CLANE = 0x00132C3Fu;

constexpr unsigned long long TOKMAP[6] = {0x20001c190f2a2202ull, 0x052517121f061a13ull, 0x181e08151d0b260cull, 0x1b2b2903102d0901ull, 0x04162e0e2824142cull, 0x110a072127232f0dull};
constexpr unsigned long long ROWMAP[8] = {0x08321b240f1c0006ull, 0x23310e2a09170d14ull, 0x210c2f030a273418ull, 0x1d2b11221f043739ull, 0x3234363a103e192dull, 0x291a20022c3f1605ull, 0x303a3c2628122e15ull, 0x39253d1e01070b13ull};

constexpr int WF_BYTES = 8 * 36 * 1024;
constexpr int EP_OFF = WF_BYTES;
constexpr int CN_OFF = EP_OFF + 256, FL_OFF = EP_OFF + 512;
constexpr int SH_OFF = EP_OFF + 1024;
constexpr int WS_BYTES = SH_OFF + GRID * 12288;
constexpr int GO_OFF = WL_OFF + 124;
constexpr int PG_OFF = WL_OFF + 96;
constexpr bool PF1 = 0;
constexpr int SPIN_MAX = 1 << 20;

#define MFMA32(a, b, c) __builtin_amdgcn_mfma_f32_16x16x32_bf16((a), (b), (c), 0, 0, 0)
#define BC8(v) __builtin_bit_cast(bf16x8, (v))
#define BAR() asm volatile("s_waitcnt lgkmcnt(0)\n\ts_barrier" ::: "memory")
#define SB() __builtin_amdgcn_sched_barrier(0)
#define OPQ(x) asm volatile("" : "+v"(x))
#define ZERO4 ((f32x4){0.f, 0.f, 0.f, 0.f})

typedef __bf16 nbf16x2 __attribute__((ext_vector_type(2)));
__device__ __forceinline__ unsigned pk2(float a, float b) {
    f32x2 v = {a, b};
    nbf16x2 r = __builtin_convertvector(v, nbf16x2);
    return __builtin_bit_cast(unsigned, r);
}
__device__ __forceinline__ float bflo(unsigned u) { return __uint_as_float(u << 16); }
__device__ __forceinline__ float bfhi(unsigned u) { return __uint_as_float(u & 0xffff0000u); }

__device__ __forceinline__ float bf2f(unsigned short v) { return __uint_as_float((unsigned)v << 16); }
__host__ __device__ __forceinline__ constexpr unsigned byte_of(unsigned long long a, unsigned long long b, int i) {
    return (unsigned)(((i & 8) ? b : a) >> (8 * (i & 7))) & 0xFFu;
}
__host__ __device__ __forceinline__ constexpr void seq_to_win(int seq, int& wy, int& wx) {
    const bool last = seq >= 5110;
    const int s = last ? 35 : seq / 146, r = seq - 146 * s;
    const int kx = last ? r / 6 : r >> 2, pos = r - (last ? 6 : 4) * kx;
    const bool low = last && pos >= (kx == 36 ? 2 : 4);
    wy = low ? 72 : 2 * s + (pos & 1);
    wx = 2 * kx + (low ? (kx == 36 ? 0 : pos - 4) : pos >> 1);
}
__host__ __device__ __forceinline__ constexpr int win_to_seq(int wy, int wx) {
    const int kx = wx >> 1;
    return wy == 72 ? 5110 + 6 * kx + (kx == 36 ? 2 : 4 + (wx & 1)) : 146 * (wy >> 1) + (wy >= 70 ? 6 : 4) * kx + 2 * (wx & 1) + (wy & 1);
}
__host__ __device__ __forceinline__ constexpr int block_of(int wy, int wx) { return wy >= 70 ? 1295 + (wx & ~1) + (wy == 72) : 37 * (wy >> 1) + (wx >> 1); }
__host__ __device__ __forceinline__ constexpr void block_pos(int b, int& ky, int& kx) {
    if (b >= 1295) { kx = (b - 1295) >> 1; ky = 70 + 2 * ((b - 1295) & 1); }
    else { const int s = b / 37; kx = b - 37 * s; ky = 2 * s; }
}
__host__ __device__ __forceinline__ constexpr unsigned wl_entry(int seq) {
    int wy = 0, wx = 0;
    seq_to_win(seq, wy, wx);
    return (unsigned)((wy * 7 * IMG + wx * 7) * (CH * 2)) | (wy == 0 ? 0x08000000u : 0u) | (wx == 0 ? 0x10000000u : 0u) |
           (wx == NWX - 1 ? 0x20000000u : 0u) | ((unsigned)(wx & 1) << 30) | ((unsigned)(wy & 1) << 31);
}
__host__ __device__ __forceinline__ constexpr unsigned dma_off(int lane) {
    const int col = (lane * 43) >> 8;
    return (unsigned)(col * (CH * 2 - 96) + 16 * lane) | (col == 0 ? 0x10000000u : 0u) | (col == 8 ? 0x20000000u : 0u);
}

__host__ __device__ __forceinline__ constexpr unsigned pf_off(int lane) {
    const int l = lane < 63 ? lane : 62, r = l / 21;
    return (unsigned)(r * ROWBYTES + (l - 21 * r) * 128) | (r == 0 ? 0x08000000u : 0u);
}
constexpr int RECN = 80;
struct Tables { unsigned rec[GRID][RECN]; unsigned img[768]; };
constexpr Tables make_tables() {
    Tables t = {};
    for (int bx = 0; bx < GRID; ++bx) {
        unsigned* r = t.rec[bx];
        const int q0 = (bx & 7) * 32 + (bx >> 3), cnt = q0 < NSHORT ? 20 : 21, start = q0 < NSHORT ? 20 * q0 : 21 * q0 - NSHORT;
        for (int i = 0; i < 32; ++i) r[i] = i < cnt ? wl_entry(start + i) : 0x7ffffffu;
        int wy = 0, wx = 0;
        seq_to_win(start, wy, wx);
        const int key0 = block_of(wy, wx);
        seq_to_win(start + cnt - 1, wy, wx);
        const int nblk = block_of(wy, wx) - key0 + 1;
        for (int b = 0; b < 8; ++b) {
            int ky = 0, kx = 0;
            block_pos(key0 + b, ky, kx);
            r[64 + b] = b < nblk ? (unsigned)(((7 * ky + 5) * IMG + 14 * kx + 5) * (CH * 2) + XBIAS) | (kx == 36 ? 1u : 0u) : OOB;
            for (int c = 0; c < 4; ++c) {
                const int y = ky + (c >> 1), x = 2 * kx + (c & 1), i = win_to_seq(y, x) - start;
                const bool ok = b < nblk && y < NWX && x < NWX && i >= 0 && i < cnt;
                r[32 + 4 * b + c] = ok ? (unsigned)(((7 * y + LPOS(y)) * IMG + 7 * x + LPOS(x)) * (CH * 2) + i) : OOB + 47;
            }
        }
        r[72] = q0; r[73] = cnt;
    }
    for (int lane = 0; lane < 64; ++lane) {
        for (int e = 0; e < 3; ++e) {
            const int tk = byte_of(TOKMAP[2 * e], TOKMAP[2 * e + 1], lane & 15);
            t.img[192 + 4 * lane + e] = (unsigned)((tk / 7) * ROWBYTES + (tk % 7) * (CH * 2)) + 16 * (lane >> 4);
        }
        t.img[448 + 4 * lane] = dma_off(lane);
        t.img[449 + 4 * lane] = pf_off(lane);
    }
    return t;
}
static __constant__ const Tables TAB = make_tables();
typedef __attribute__((address_space(1))) unsigned gu32;
typedef __attribute__((address_space(1))) unsigned long long gu64;
#define RLX_AGENT __ATOMIC_RELAXED, __HIP_MEMORY_SCOPE_AGENT
#define TAG_OF(e) (((unsigned long long)~((e) + 1u) << 32) | ((e) + 1u))

__device__ __forceinline__ float rstd_of(const char* p) {
    f32x4 p0 = *(const f32x4*)(p);
    f32x4 p1 = *(const f32x4*)(p + 1024);
    float s = (p0[0] + p0[2]) + (p1[0] + p1[2]);
    float q = (p0[1] + p0[3]) + (p1[1] + p1[3]);
    float mean = s * (1.f / 192.f);
    float var = __builtin_fmaf(q, 1.f / 192.f, -mean * mean);
    var = __builtin_fmaxf(var, 0.f);
    return __builtin_amdgcn_rsqf(var + 1e-5f);
}

#if defined(__HIP_DEVICE_COMPILE__)
#define NOPK __attribute__((target("no-packed-fp32-ops")))
#else
#define NOPK
#endif
__global__ __launch_bounds__(512, 2) NOPK void win_attn_kernel(const unsigned short* __restrict__ x, const unsigned short* __restrict__ dw,
                                                          const unsigned short* __restrict__ wqkv, const unsigned short* __restrict__ wo,
                                                          u32x4* wf, unsigned short* __restrict__ out) {
    __shared__ __attribute__((aligned(16))) char smem[LDS_BYTES];

    asm volatile("wk_code_begin:");
    const int tid = threadIdx.x;
    const int wave = __builtin_amdgcn_readfirstlane(tid >> 6);
    const int w = wave & 3;
    const unsigned ldsb = (unsigned)(size_t)smem;
#define GET_LANE(l) int l; asm volatile("v_mbcnt_lo_u32_b32 %0, -1, 0\n\tv_mbcnt_hi_u32_b32 %0, -1, %0" : "=v"(l)); __builtin_assume(l >= 0 && l < 64);
#define ROLE_SETUP                                                                                 \
    int bx = blockIdx.x;                                                                           \
    asm volatile("" : "+s"(bx));                                                                   \
    bx = __builtin_amdgcn_readfirstlane(bx);                                                       \
    const unsigned* rec = TAB.rec[bx];                                                             \
    const unsigned en0 = rec[0], en1 = rec[1];                                                     \
    const int q0 = rec[72], cnt = rec[73];                                                         \
    const auto wrsrc = __builtin_amdgcn_make_buffer_rsrc((void*)wf, 0, WS_BYTES, 0x00020000);      \
    const auto xrsrc = __builtin_amdgcn_make_buffer_rsrc((void*)((const char*)x - XBIAS), 0, IMG * IMG * CH * 2 + XBIAS, 0x00020000);
#define DMA_SLICE(en)                                                                              \
        {                                                                                          \
            const unsigned vo_ = dma_off(lane) & (0x401fffffu | ((en) & 0x38000000u));             \
            unsigned so_ = ((en) & 0x7ffffffu) + 96 * w, n_ = 8;                                   \
            asm volatile("s_mov_b32 m0, %2\n\ts_mov_b32 exec_hi, 0x3fffff\n\tbuffer_load_dwordx4 %3, %4, %5 offen lds\n"     \
                         ".Lds%=:\n\ts_add_u32 m0, m0, 872\n\ts_add_u32 %0, %0, %6\n\ts_sub_u32 %1, %1, 1\n\t"            \
                         "buffer_load_dwordx4 %3, %4, %0 offen lds\n\ts_cmp_lg_u32 %1, 0\n\ts_cbranch_scc1 .Lds%=\n\ts_mov_b32 exec_hi, -1" \
                         : "+s"(so_), "+s"(n_) : "s"(ldsb + w * 8192), "v"(vo_), "s"(xrsrc), "s"(((en) & 0x08000000u) ? OOB : so_), "n"(ROWBYTES) \
                         : "m0", "scc", "memory");                                                 \
        }
    asm volatile(
        "s_load_dwordx8 s[24:31], %[ka], 0x0\n\t"
        "s_load_dwordx2 s[32:33], %[ka], 0x20\n\t"
        "s_mul_i32 s36, %[bx], %[recb]\n\t"
        "s_xor_b32 s37, %[wave], 4\n\t"
        "s_lshl2_add_u32 s38, s37, s36\n\t"
        "s_load_dword s38, %[tab], s38 offset:0x100\n\t"
        "s_load_dword s39, %[tab], s36\n\t"
        "v_mbcnt_lo_u32_b32 v1, -1, 0\n\t"
        "v_mbcnt_hi_u32_b32 v1, -1, v1\n\t"
        "s_and_b32 s44, %[bx], 7\n\t"
        "s_lshl_b32 s44, s44, 5\n\t"
        "s_lshr_b32 s45, %[bx], 3\n\t"
        "s_add_u32 s44, s44, s45\n\t"
        "v_lshlrev_b32 v2, 4, v1\n\t"
        "s_mov_b32 s43, 0x20000\n\t"
        "s_cmp_ge_u32 s44, 36\n\t"
        "s_cbranch_scc1 .Lnp%=\n\t"
        "s_mul_i32 s45, s44, 6\n\t"
        "s_lshl1_add_u32 s46, s44, 210\n\t"
        "s_cmp_ge_u32 %[wave], 6\n\t"
        "s_cselect_b64 vcc, -1, 0\n\t"
        "s_cselect_b32 s45, s46, s45\n\t"
        "s_movk_i32 s52, 1152\n\t"
        "s_cselect_b32 s52, 384, s52\n\t"
        "s_cselect_b32 s53, 0, 0xbbaaaaab\n\t"
        "s_add_u32 s45, s45, %[wave]\n\t"
        "s_mul_i32 s46, s45, 171\n\t"
        "s_lshr_b32 s46, s46, 10\n\t"
        "s_mul_i32 s47, s46, 6\n\t"
        "s_sub_u32 s47, s45, s47\n\t"
        "s_mul_i32 s48, s46, 43\n\t"
        "s_lshr_b32 s48, s48, 8\n\t"
        "s_mul_i32 s49, s48, 6\n\t"
        "s_sub_u32 s49, s46, s49\n\t"
        "s_mul_i32 s50, s48, 48\n\t"
        "s_cmp_lt_u32 s49, 3\n\t"
        "s_cselect_b32 s34, 1.0, 0x3e553b94\n\t"
        "s_movk_i32 s51, -48\n\t"
        "s_cselect_b32 s51, 192, s51\n\t"
        "s_cmp_lt_u32 s48, 4\n\t"
        "s_cselect_b32 s51, s51, s50\n\t"
        "s_cselect_b32 s34, s34, 1.0\n\t"
        "s_add_u32 s51, s51, s50\n\t"
        "s_lshl4_add_u32 s51, s49, s51\n\t"
        "s_and_b32 s49, s49, 1\n\t"
        "s_mul_i32 s49, s49, 12\n\t"
        "s_add_u32 s49, s49, 576\n\t"
        "s_cmp_ge_u32 %[wave], 6\n\t"
        "s_cselect_b32 s49, s49, 0\n\t"
        "s_sub_u32 s51, s51, s49\n\t"
        "s_and_b32 s49, s47, 1\n\t"
        "s_mul_i32 s49, s49, 88\n\t"
        "s_lshl3_add_u32 s47, s47, s49\n\t"
        "v_lshrrev_b32 v3, 5, v1\n\t"
        "v_mul_u32_u24 v3, 48, v3\n\t"
        "v_lshrrev_b32 v4, 1, v1\n\t"
        "v_and_or_b32 v3, v4, 8, v3\n\t"
        "v_add_u32 v3, s47, v3\n\t"
        "v_mul_u32_u24 v3, s52, v3\n\t"
        "v_and_b32 v4, 15, v1\n\t"
        "v_and_b32 v5, 12, v1\n\t"
        "v_cndmask_b32 v5, 0, v5, vcc\n\t"
        "v_add3_u32 v5, v4, v5, s51\n\t"
        "v_lshl_add_u32 v3, v5, 1, v3\n\t"
        "v_mov_b64 v[8:9], 0\n\t"
        "v_mov_b64 v[10:11], 0\n\t"
        "v_mov_b64 v[12:13], 0\n\t"
        "v_mov_b64 v[14:15], 0\n\t"
        "s_mul_i32 s42, s52, 192\n\t"
        "s_cmp_ge_u32 %[wave], 6\n\t"
        "s_waitcnt lgkmcnt(0)\n\t"
        "s_cselect_b64 s[40:41], s[30:31], s[28:29]\n\t"
        "s_and_b32 s41, s41, 0xffff\n\t"
        "buffer_load_short_d16_hi v8, v3, s[40:43], 0 offen\n\t"
        "buffer_load_short_d16_hi v9, v3, s[40:43], s52 offen\n\t"
        "s_lshl_b32 s46, s52, 1\n\t"
        "buffer_load_short_d16_hi v10, v3, s[40:43], s46 offen\n\t"
        "s_add_u32 s46, s46, s52\n\t"
        "buffer_load_short_d16_hi v11, v3, s[40:43], s46 offen\n\t"
        "s_add_u32 s46, s46, s52\n\t"
        "buffer_load_short_d16_hi v12, v3, s[40:43], s46 offen\n\t"
        "s_add_u32 s46, s46, s52\n\t"
        "buffer_load_short_d16_hi v13, v3, s[40:43], s46 offen\n\t"
        "s_add_u32 s46, s46, s52\n\t"
        "buffer_load_short_d16_hi v14, v3, s[40:43], s46 offen\n\t"
        "s_add_u32 s46, s46, s52\n\t"
        "buffer_load_short_d16_hi v15, v3, s[40:43], s46 offen\n\t"
        "v_mov_b32 v6, %[ep]\n\t"
        "global_load_dword v7, v6, s[32:33] sc1\n\t"
        "v_mov_b32 v5, %[red]\n\t"
        "v_mad_u32_u24 v4, v4, %[c144], v5\n\t"
        "v_lshrrev_b32 v5, 2, v1\n\t"
        "v_and_b32 v5, 12, v5\n\t"
        "s_lshl_b32 s46, %[wave], 4\n\t"
        "v_add3_u32 v5, v4, v5, s46\n\t"
        "s_mov_b32 s40, s32\n\t"
        "s_and_b32 s41, s33, 0xffff\n\t"
        "s_mov_b32 s42, %[wsb]\n\t"
        "s_lshl_b32 s45, s45, 10\n\t"
        "s_waitcnt vmcnt(1)\n\t"
        "v_add_f32 v16, v8, v10\n\t"
        "v_add_f32 v17, v9, v11\n\t"
        "v_add_f32 v18, v12, v14\n\t"
        "v_add_f32 v19, v13, v15\n\t"
        "v_add_f32 v16, v16, v18\n\t"
        "v_add_f32 v17, v17, v19\n\t"
        "v_add_f32 v16, v16, v17\n\t"
        "ds_write_b32 v5, v16\n\t"
        "s_waitcnt lgkmcnt(0)\n\t"
        "s_barrier\n\t"
        "ds_read_b128 v[16:19], v4\n\t"
        "ds_read_b128 v[20:23], v4 offset:16\n\t"
        "ds_read_b128 v[24:27], v4 offset:32\n\t"
        "ds_read_b128 v[28:31], v4 offset:48\n\t"
        "ds_read_b128 v[32:35], v4 offset:64\n\t"
        "ds_read_b128 v[36:39], v4 offset:80\n\t"
        "s_waitcnt lgkmcnt(4)\n\t"
        "v_add_f32 v16, v16, v18\n\t"
        "v_add_f32 v17, v17, v19\n\t"
        "v_add_f32 v20, v20, v22\n\t"
        "v_add_f32 v21, v21, v23\n\t"
        "s_waitcnt lgkmcnt(2)\n\t"
        "v_add_f32 v24, v24, v26\n\t"
        "v_add_f32 v25, v25, v27\n\t"
        "v_add_f32 v28, v28, v30\n\t"
        "v_add_f32 v29, v29, v31\n\t"
        "v_add_f32 v16, v16, v20\n\t"
        "v_add_f32 v17, v17, v21\n\t"
        "s_waitcnt lgkmcnt(0)\n\t"
        "v_add_f32 v32, v32, v34\n\t"
        "v_add_f32 v33, v33, v35\n\t"
        "v_add_f32 v36, v36, v38\n\t"
        "v_add_f32 v37, v37, v39\n\t"
        "v_add_f32 v24, v24, v28\n\t"
        "v_add_f32 v25, v25, v29\n\t"
        "v_add_f32 v32, v32, v36\n\t"
        "v_add_f32 v33, v33, v37\n\t"
        "v_add_f32 v16, v16, v24\n\t"
        "v_add_f32 v17, v17, v25\n\t"
        "v_add_f32 v16, v16, v32\n\t"
        "v_add_f32 v17, v17, v33\n\t"
        "v_add_f32 v16, v16, v17\n\t"
        "v_mul_f32 v16, s53, v16\n\t"
        "v_mul_f32 v16, s34, v16\n\t"
        "v_fma_f32 v8, v8, s34, v16\n\t"
        "v_fma_f32 v9, v9, s34, v16\n\t"
        "v_fma_f32 v10, v10, s34, v16\n\t"
        "v_fma_f32 v11, v11, s34, v16\n\t"
        "v_fma_f32 v12, v12, s34, v16\n\t"
        "v_fma_f32 v13, v13, s34, v16\n\t"
        "v_fma_f32 v14, v14, s34, v16\n\t"
        "v_fma_f32 v15, v15, s34, v16\n\t"
        "v_cvt_pk_bf16_f32 v8, v8, v9\n\t"
        "v_cvt_pk_bf16_f32 v9, v10, v11\n\t"
        "v_cvt_pk_bf16_f32 v10, v12, v13\n\t"
        "v_cvt_pk_bf16_f32 v11, v14, v15\n\t"
        "buffer_store_dwordx4 v[8:11], v2, s[40:43], s45 offen sc1\n\t"
        "s_or_b32 s46, s44, s37\n\t"
        "s_cmp_lg_u32 s46, 0\n\t"
        "s_cbranch_scc1 .Lnc%=\n\t"
        "v_mov_b32 v3, 0\n\t"
        "global_store_dword v6, v3, s[32:33] offset:256 sc1\n\t"
        ".Lnc%=:\n\t"
        "v_lshl_add_u32 v6, s44, 3, v6\n\t"
        "s_waitcnt vmcnt(0)\n\t"
        "s_barrier\n\t"
        "s_cmp_lg_u32 %[wave], 0\n\t"
        "s_cbranch_scc1 .Lnp%=\n\t"
        "v_add_u32 v4, 1, v7\n\t"
        "v_not_b32 v5, v4\n\t"
        "s_mov_b64 exec, 1\n\t"
        "global_store_dwordx2 v6, v[4:5], s[32:33] offset:512 sc1\n\t"
        "s_mov_b64 exec, -1\n\t"
        ".Lnp%=:\n\t"
        "s_getpc_b64 s[40:41]\n\t"
        ".Lwk_here%=:\n\t"
        "s_sub_u32 s40, s40, .Lwk_here%=-wk_code_begin\n\t"
        "s_subb_u32 s41, s41, 0\n\t"
        "s_and_b32 s41, s41, 0xffff\n\t"
        "s_mov_b32 s42, wk_code_end-wk_code_begin\n\t"
        "s_max_u32 s42, s42, wk_code_end1-wk_code_begin\n\t"
        "s_max_u32 s42, s42, wk_code_end2-wk_code_begin\n\t"
        "s_add_u32 m0, %[lds], %[sink]\n\t"
        "v_lshlrev_b32 v3, 7, v1\n\t"
        "s_lshl_b32 s45, %[wave], 13\n\t"
        "buffer_load_dword v3, s[40:43], s45 offen lds\n\t"
        "s_mov_b64 s[40:41], %[tab]\n\t"
        "s_and_b32 s41, s41, 0xffff\n\t"
        "s_mov_b32 s42, %[tabb]\n\t"
        "s_lshl_b32 s46, %[wave], 10\n\t"
        "s_add_u32 s47, s46, %[imgs]\n\t"
        "s_add_u32 s48, s46, %[imgd]\n\t"
        "s_lshl_b32 s49, %[wave], 8\n\t"
        "s_add_u32 s49, s49, %[mapd]\n\t"
        "s_add_u32 s50, s36, 128\n\t"
        "s_mov_b64 vcc, -1\n\t"
        "s_cmp_eq_u32 %[wave], 0\n\t"
        "s_cselect_b32 s47, s36, s47\n\t"
        "s_cselect_b32 s48, %[wld], s48\n\t"
        "s_cselect_b64 vcc, 0xff, vcc\n\t"
        "s_cmp_ge_u32 %[wave], 4\n\t"
        "s_cselect_b32 s47, s50, s47\n\t"
        "s_cselect_b32 s48, s49, s48\n\t"
        "s_cselect_b64 exec, 0xff, vcc\n\t"
        "s_add_u32 m0, %[lds], s48\n\t"
        "s_and_b32 s49, %[wave], 3\n\t"
        "buffer_load_dwordx4 v2, s[40:43], s47 offen lds\n\t"
        "s_mov_b64 exec, -1\n\t"
        "v_mul_u32_u24 v3, 43, v1\n\t"
        "v_lshrrev_b32 v3, 8, v3\n\t"
        "v_mad_u32_u24 v4, v3, %[c288], v2\n\t"
        "s_mul_i32 s49, s49, 96\n\t"
        "s_mul_i32 s50, s37, 6160\n\t"
        "s_add_u32 s50, s50, %[lb0]\n\t"
        "s_cmp_eq_u32 s37, 7\n\t"
        "s_cselect_b32 s50, %[lb7], s50\n\t"
        "s_add_u32 s46, s46, %[tp]\n\t"
        "s_waitcnt lgkmcnt(0)\n\t"
        "s_cmp_ge_u32 %[wave], 4\n\t"
        "s_cbranch_scc1 .Lib%=\n\t"
        "s_mov_b32 s40, s26\n\t"
        "s_and_b32 s41, s27, 0xffff\n\t"
        "s_movk_i32 s42, 3456\n\t"
        "s_add_u32 m0, %[lds], s46\n\t"
        "s_nop 0\n\t"
        "buffer_load_dwordx4 v4, s[40:43], s49 offen lds\n\t"
        ".Lib%=:\n\t"
        "s_sub_u32 s40, s24, %[xb]\n\t"
        "s_subb_u32 s41, s25, 0\n\t"
        "s_and_b32 s41, s41, 0xffff\n\t"
        "s_mov_b32 s42, %[xsz]\n\t"
        "s_cmp_eq_u32 s38, 2.0\n\t"
        "s_cbranch_scc1 .Lic%=\n\t"
        "s_mov_b32 exec_hi, 0xffff\n\t"
        "s_add_u32 m0, %[lds], s50\n\t"
        "s_mov_b32 s46, 4\n\t"
        "s_bitcmp1_b32 s38, 0\n\t"
        "s_cselect_b32 s47, 2.0, s38\n\t"
        "s_bitset0_b32 s38, 0\n\t"
        ".Lid%=:\n\t"
        "buffer_load_dwordx4 v2, s[40:43], s38 offen lds\n\t"
        "buffer_load_dwordx4 v2, s[40:43], s47 offen offset:768 lds\n\t"
        "s_add_u32 m0, m0, 1536\n\t"
        "s_add_u32 s38, s38, %[rb]\n\t"
        "s_add_u32 s47, s47, %[rb]\n\t"
        "s_sub_u32 s46, s46, 1\n\t"
        "s_cmp_lg_u32 s46, 0\n\t"
        "s_cbranch_scc1 .Lid%=\n\t"
        ".Lic%=:\n\t"
        "s_cmp_lt_u32 %[wave], 4\n\t"
        "s_cbranch_scc1 .Lie%=\n\t"
        "s_mov_b32 exec_hi, 0x3fffff\n\t"
        "s_and_b32 s46, s39, 0x10000000\n\t"
        "s_and_b32 s47, s39, 0x20000000\n\t"
        "v_cmp_eq_u32 vcc, 0, v3\n\t"
        "v_mov_b32 v5, s46\n\t"
        "v_mov_b32 v6, s47\n\t"
        "v_cndmask_b32 v5, 0, v5, vcc\n\t"
        "v_cmp_eq_u32 vcc, 8, v3\n\t"
        "s_and_b32 s46, s39, 0x7ffffff\n\t"
        "s_add_u32 s46, s46, s49\n\t"
        "v_cndmask_b32 v6, 0, v6, vcc\n\t"
        "v_or3_b32 v4, v4, v5, v6\n\t"
        "s_bitcmp1_b32 s39, 27\n\t"
        "s_cselect_b32 s47, 2.0, s46\n\t"
        "s_lshl_b32 s45, s37, 13\n\t"
        "s_add_u32 m0, %[lds], s45\n\t"
        "s_mov_b32 s48, 8\n\t"
        "buffer_load_dwordx4 v4, s[40:43], s47 offen lds\n\t"
        ".Lia%=:\n\t"
        "s_add_u32 m0, m0, 872\n\t"
        "s_add_u32 s46, s46, %[rb]\n\t"
        "s_sub_u32 s48, s48, 1\n\t"
        "buffer_load_dwordx4 v4, s[40:43], s46 offen lds\n\t"
        "s_cmp_lg_u32 s48, 0\n\t"
        "s_cbranch_scc1 .Lia%=\n\t"
        ".Lie%=:\n\t"
        "s_mov_b64 exec, -1\n\t"
        :: [ka] "s"(__builtin_amdgcn_kernarg_segment_ptr()), [bx] "s"(blockIdx.x), [wave] "s"(wave), [tab] "s"(&TAB), [lds] "s"(ldsb),
           [red] "s"(ldsb + O_OFF), [c144] "s"(144), [c288] "s"(288), [recb] "n"(RECN * 4), [ep] "n"(EP_OFF), [wsb] "n"(WS_BYTES), [sink] "n"(X_OFF + 18432),
           [tabb] "n"(sizeof(Tables)), [imgs] "n"(GRID * RECN * 4 - 1024), [imgd] "n"(AT_OFF + 18816 - 1024), [mapd] "n"(TP_OFF + 3072), [wld] "n"(WL_OFF),
           [lb0] "n"(LB_OFF(0)), [lb7] "n"(LB_OFF(7)), [tp] "n"(TP_OFF), [xb] "n"(XBIAS), [xsz] "n"(IMG * IMG * CH * 2 + XBIAS), [rb] "n"(ROWBYTES)
        : "v1", "v2", "v3", "v4", "v5", "v6", "v7", "v8", "v9", "v10", "v11", "v12", "v13", "v14", "v15", "v16", "v17", "v18", "v19", "v20", "v21", "v22", "v23", "v24", "v25", "v26", "v27", "v28", "v29", "v30", "v31", "v32", "v33", "v34", "v35", "v36", "v37", "v38", "v39", "s24", "s25", "s26", "s27", "s28", "s29", "s30", "s31", "s32", "s33", "s34", "s36", "s37", "s38", "s39", "s40", "s41", "s42", "s43", "s44", "s45", "s46", "s47", "s48", "s49", "s50", "s51", "s52", "s53", "m0", "vcc", "scc", "memory");
#define WAIT_GO                                                                                    \
        {                                                                                          \
            unsigned go_;                                                                          \
            asm volatile(".Lgo%=:\n\tds_read_b32 %0, %1\n\ts_waitcnt lgkmcnt(0)\n\tv_cmp_ne_u32 vcc, -1, %0\n\ts_nop 4\n\ts_cbranch_vccnz .Lgo%=" \
                         : "=&v"(go_) : "v"(ldsb + GO_OFF) : "vcc", "memory");                     \
        }
    bf16x8 W[36];
#define LOAD_W(j0, j1, k0, k1)                                                                     \
    _Pragma("unroll") for (int ks_ = (k0); ks_ < (k1); ++ks_)                                      \
        _Pragma("unroll") for (int j_ = (j0); j_ < (j1); ++j_)                                     \
            W[j_ * 6 + ks_] = BC8(__builtin_amdgcn_raw_buffer_load_b128(wrsrc, lane * 16, wave * 36864 + (j_ * 6 + ks_) * 1024, 16));

#define LOAD_WI(i0, i1)                                                                            \
    _Pragma("unroll") for (int i_ = (i0); i_ < (i1); ++i_)                                         \
        W[i_] = BC8(__builtin_amdgcn_raw_buffer_load_b128(wrsrc, lane * 16, wave * 36864 + i_ * 1024, 16));

#define LOAD_WL(i0, i1)                                                                            \
    _Pragma("unroll") for (int i_ = (i0); i_ < (i1); ++i_)                                         \
        W[i_] = BC8(__builtin_amdgcn_raw_buffer_load_b128(wrsrc, lane * 16, wl_ + i_ * 1024, 16));

#define LANE_SETUP                                                                                 \
    int lane;                                                                                      \
    asm volatile("v_mbcnt_lo_u32_b32 %0, -1, 0\n\tv_mbcnt_hi_u32_b32 %0, -1, %0" : "=v"(lane));    \
    __builtin_assume(lane >= 0 && lane < 64);                                                      \
    const int g = lane >> 4, l15 = lane & 15, sw = l15 >> 1;                                       \
    int fE = X_OFF + l15 * 384 + ((g ^ sw) << 4);                                                  \
    int fO = fE ^ 64;                                                                              \
    OPQ(fE); OPQ(fO);
#define FRAG(base, t, ks) (*(const bf16x8*)(smem + (((ks) & 1) ? fO : fE) + ((base) - X_OFF) + (t) * 6144 + ((ks) >> 1) * 128))
#define RSTD_PUBLISH(ln_, stoff)                                                                   \
    *(float*)(smem + ((ln_) < 48 ? RS_BASE : RS_OFF + 2304 - 192) + (ln_) * 4) = rstd_of(smem + (stoff) + (ln_) * 16);

#define XPAT(r) (((r) < 48 ? (r) : 48) * 384 + (((2 * w) ^ (((r) >> 1) & 7)) << 4))
#define SPAT(r) ((w >> 1) * 1024 + (w & 1) * 8 + (r) * 16)
#define CONV_CONSTS(var) \
            const unsigned long long rlo = g == 0 ? ROWMAP[0] : (g == 1 ? ROWMAP[2] : (g == 2 ? ROWMAP[4] : ROWMAP[6])); \
            const unsigned long long rhi = g == 0 ? ROWMAP[1] : (g == 1 ? ROWMAP[3] : (g == 2 ? ROWMAP[5] : ROWMAP[7])); \
            int xrow = byte_of(rlo, rhi, l15); \
            if (lane == (int)((CLANE >> (8 * (var))) & 0xFF)) xrow = 48; \
            if (lane == 63) xrow = (CROW >> (8 * (var))) & 0xFF; \
            const unsigned pXw_ = XPAT(xrow); \
            const unsigned pSTw_ = (w >> 1) * 1024 + (w & 1) * 8 + xrow * 16; \
            const int oy = (0x5320 >> (4 * (l15 >> 2))) & 15, ox = (0x5320 >> (4 * (l15 & 3))) & 15; \
            const unsigned aB_ = ldsb + w * 8192 + (oy + (g >> 1)) * 872 + (ox + 2 * (g & 1)) * 96; \
            const int dyi = g - (l15 >> 3); \
            const unsigned aA_ = ldsb + AT_OFF + (l15 & 3) * 16 + \
                                 ((dyi < 0 || dyi > 2) ? 18816 : (dyi == 0 ? 0 : (dyi == 1 ? 6208 : 12544)) + ((l15 >> 2) & 1) * 3072 + w * 768); \
            const unsigned idx_ = (l15 & 3) * 0x5555u; \
            const unsigned se = (0 || l15 == 4 * g) ? 0x3F803F80u : 0u; \
            const u32x4 sel4 = {se, se, se, se};
#define PIN_CONSTS asm volatile("" :: "v"(pXw_), "v"(pSTw_), "v"(aB_), "v"(aA_), "v"(idx_), "v"(se));

    if (wave < 4) {
        ROLE_SETUP
        LANE_SETUP
        asm volatile("s_waitcnt vmcnt(0)" ::: "memory");
        if (lane < 48) {
            unsigned tp[9];
#pragma unroll
            for (int k = 0; k < 9; ++k) tp[k] = *(const unsigned short*)(smem + TP_OFF + w * 1024 + k * 96 + lane * 2);
            tp[4] |= 0x3F800000u;
#pragma unroll
            for (int dy = 0; dy < 3; ++dy) {
                char* p = smem + AT_OFF + (dy == 0 ? 0 : (dy == 1 ? 6208 : 12544)) + w * 768 + lane * 16;
                *(u32x4*)p = (u32x4){tp[3 * dy], tp[3 * dy + 1], tp[3 * dy + 2], 0u};
                *(u32x4*)(p + 3072) = (u32x4){0u, tp[3 * dy], tp[3 * dy + 1], tp[3 * dy + 2]};
            }
        }
        BAR();
        if (PF1) {
            const int li = w * 64 + lane, r = (li * 2428) >> 16;
            const unsigned vo = (r > 8 || (r == 0 && (en1 & 0x08000000u))) ? OOB : r * ROWBYTES + (li - 27 * r) * 128;
            asm volatile("s_mov_b32 m0, %0\n\ts_nop 0\n\tbuffer_load_dword %1, %2, %3 offen lds"
                         :: "s"(ldsb + X_OFF + 18432), "v"(vo), "s"(xrsrc), "s"(en1 & 0x7ffffffu) : "m0", "memory");
        }
        {
            const int dW = X_OFF + l15 * 384 + (((2 * w + (g >> 1)) ^ sw) << 4) + 8 * (g & 1);
            const unsigned rsb = ldsb + RS_BASE;
            const unsigned pE_ = fE - X_OFF, aO_ = ldsb + dW + (O_OFF - X_OFF), aZ_ = ldsb + AT_OFF + 18816, aVB_ = ldsb + V_OFF + w * 4608 + 1024 + lane * 8;
            unsigned aRSw_ = rsb + lane * 4;
            const unsigned pST_ = lane * 16, aPLw_ = rsb + lane * 2, aRSr_ = rsb + l15 * 4;
            const unsigned g16_ = g * 16, aPLr_ = (l15 & 3) ? aZ_ : rsb + g * 8;
            const unsigned loVL_ = ((2 * w + (l15 >> 3)) << 4) + (l15 & 7) * 2, loOL_ = ((2 * w + (g >> 1)) << 4) + 8 * (g & 1);
            const unsigned on = (l15 & 3) ? 0u : 0x3F803F80u;
            const u32x4 ones4 = {on, on, on, on};
            const unsigned snk = ldsb + O_OFF + 15360 + w * 192, kr = ldsb + KQL_OFF + l15 * 800;
            const bool ok1 = 16 + l15 < cnt, dg = g == (l15 >> 2);
            u32x4 pl = {kr + w * 192 + g * 16, (ok1 ? kr + 12800 + w * 192 : snk) + g * 16, dg ? kr + 768 + w * 4 : snk, dg && ok1 ? kr + 12800 + 768 + w * 4 : snk};
            unsigned pcl, pch, len;
            asm volatile("s_getpc_b64 s[36:37]\n.Lwk_h2%=:\n\ts_sub_u32 s36, s36, .Lwk_h2%=-wk_code_begin\n\ts_subb_u32 s37, s37, 0\n\t"
                         "s_mov_b32 %2, wk_code_end-wk_code_begin\n\ts_max_u32 %2, %2, wk_code_end1-wk_code_begin" : "={s36}"(pcl), "={s37}"(pch), "=s"(len) :: "scc");
            pcl = __builtin_amdgcn_readfirstlane(pcl); pch = __builtin_amdgcn_readfirstlane(pch); len = __builtin_amdgcn_readfirstlane(len);
            const unsigned long long tb = w == 0 ? ((unsigned long long)pch << 32) | pcl : (unsigned long long)wf;
            const u32x4 tr = {(unsigned)tb, (unsigned)(tb >> 32) & 0xffffu, w == 0 ? len : (w == 1 ? (unsigned)WF_BYTES : SH_OFF + bx * 12288 + 12288), 0x00020000u};
            const unsigned to = w == 0 ? ((bx >> 3) % 6) * 8192 : (w == 1 ? 6 * 36864 + ((bx >> 3) % 9) * 8192 : SH_OFF + bx * 12288 + (w - 2) * 8192);
            asm volatile("" :: "{v244}"(pE_), "{v245}"(aO_), "{v246}"(aZ_), "{v247}"(aVB_), "{v248}"(pST_), "{v249}"(aRSw_), "{v250}"(aPLw_), "{v251}"(aRSr_), "{v252}"(g16_), "{v253}"(aPLr_), "{v254}"(loVL_), "{v255}"(loOL_), "{v[240:243]}"(ones4), "{v[232:235]}"(pl), "{s[36:39]}"(tr), "{s41}"(to));
            if (w == 0) {
                gu64* fp = (gu64*)((char*)wf + (lane < 36 ? FL_OFF + lane * 8 : EP_OFF));
#pragma unroll 1
                for (int it = 0; it < SPIN_MAX; ++it) {
                    const unsigned long long f = __hip_atomic_load(fp, RLX_AGENT);
                    const unsigned ep = __builtin_amdgcn_readlane((unsigned)f, 63);
                    if ((__builtin_amdgcn_ballot_w64(f != TAG_OF(ep)) & 0xfffffffffull) == 0ull) break;
                    __builtin_amdgcn_s_sleep(1);
                }
                __builtin_amdgcn_fence(__ATOMIC_ACQUIRE, "wavefront");
                asm volatile("ds_write_b32 %0, %1" :: "v"(ldsb + GO_OFF), "v"(~0u) : "memory");
            } else WAIT_GO
            __builtin_amdgcn_s_setprio(2);
            unsigned s44 = cnt, s48 = ldsb + X_OFF, s70 = 0;
            unsigned s72 = ldsb + KQL_OFF + w * 192, s73 = ldsb + OL_OFF, s77 = ldsb + OL_OFF, s76 = ldsb + KQL_OFF + 768 + w * 4;
            const unsigned long long wb = (unsigned long long)wf;
            const u32x4 wr = {(unsigned)wb, (unsigned)(wb >> 32) & 0xffffu, (unsigned)WS_BYTES, 0x00020000u};
            asm volatile(
            "s_setprio 2\n\t"
            "s_add_u32 s49, s33, 0\n\t"
            "buffer_load_dwordx4 v[0:3], v248, s[28:31], s49 offen sc1\n\t"
            "s_add_u32 s49, s33, 1024\n\t"
            "buffer_load_dwordx4 v[4:7], v248, s[28:31], s49 offen sc1\n\t"
            "s_add_u32 s49, s33, 2048\n\t"
            "buffer_load_dwordx4 v[8:11], v248, s[28:31], s49 offen sc1\n\t"
            "s_add_u32 s49, s33, 3072\n\t"
            "buffer_load_dwordx4 v[12:15], v248, s[28:31], s49 offen sc1\n\t"
            "s_add_u32 s49, s33, 4096\n\t"
            "buffer_load_dwordx4 v[16:19], v248, s[28:31], s49 offen sc1\n\t"
            "s_add_u32 s49, s33, 5120\n\t"
            "buffer_load_dwordx4 v[20:23], v248, s[28:31], s49 offen sc1\n\t"
            "s_add_u32 s49, s33, 6144\n\t"
            "buffer_load_dwordx4 v[24:27], v248, s[28:31], s49 offen sc1\n\t"
            "s_add_u32 s49, s33, 7168\n\t"
            "buffer_load_dwordx4 v[28:31], v248, s[28:31], s49 offen sc1\n\t"
            "s_add_u32 s49, s33, 8192\n\t"
            "buffer_load_dwordx4 v[32:35], v248, s[28:31], s49 offen sc1\n\t"
            "s_add_u32 s49, s33, 9216\n\t"
            "buffer_load_dwordx4 v[36:39], v248, s[28:31], s49 offen sc1\n\t"
            "s_add_u32 s49, s33, 10240\n\t"
            "buffer_load_dwordx4 v[40:43], v248, s[28:31], s49 offen sc1\n\t"
            "s_add_u32 s49, s33, 11264\n\t"
            "buffer_load_dwordx4 v[44:47], v248, s[28:31], s49 offen sc1\n\t"
            "s_add_u32 s49, s33, 12288\n\t"
            "buffer_load_dwordx4 v[48:51], v248, s[28:31], s49 offen sc1\n\t"
            "s_add_u32 s49, s33, 13312\n\t"
            "buffer_load_dwordx4 v[52:55], v248, s[28:31], s49 offen sc1\n\t"
            "s_add_u32 s49, s33, 14336\n\t"
            "buffer_load_dwordx4 v[56:59], v248, s[28:31], s49 offen sc1\n\t"
            "s_add_u32 s49, s33, 15360\n\t"
            "buffer_load_dwordx4 v[60:63], v248, s[28:31], s49 offen sc1\n\t"
            "v_mov_b32 v224, s46\n\t"
            ".Ls1%=:\n\t"
            "ds_read_b128 v[220:223], v224\n\t"
            "s_waitcnt lgkmcnt(0)\n\t"
            "v_and_b32 v220, v220, v221\n\t"
            "v_and_b32 v222, v222, v223\n\t"
            "v_and_b32 v220, v220, v222\n\t"
            "s_nop 0\n\t"
            "v_readfirstlane_b32 s49, v220\n\t"
            "s_nop 3\n\t"
            "s_bitcmp1_b32 s49, 27\n\t"
            "s_cbranch_scc0 .Ls1%=\n\t"
            "s_nop 7\n\t"
            "v_add_u32 v237, s35, v248\n\t"
            "v_add_u32 v238, s56, v244\n\t"
            "v_xor_b32 v239, 64, v238\n\t"
            "v_add_u32 v236, s56, v248\n\t"
            "ds_read_b128 v[200:203], v237\n\t"
            "ds_read_b128 v[204:207], v237 offset:1024\n\t"
            "ds_read_b128 v[144:147], v238\n\t"
            "ds_read_b128 v[148:151], v239\n\t"
            "ds_read_b128 v[152:155], v238 offset:128\n\t"
            "ds_read_b128 v[156:159], v239 offset:128\n\t"
            "ds_read_b128 v[160:163], v238 offset:256\n\t"
            "ds_read_b128 v[164:167], v239 offset:256\n\t"
            "ds_read_b128 v[168:171], v238 offset:6144\n\t"
            "ds_read_b128 v[172:175], v239 offset:6144\n\t"
            "ds_read_b128 v[176:179], v238 offset:6272\n\t"
            "ds_read_b128 v[180:183], v239 offset:6272\n\t"
            "ds_read_b128 v[184:187], v238 offset:6400\n\t"
            "ds_read_b128 v[188:191], v239 offset:6400\n\t"
            "ds_read_b128 v[208:211], v237 offset:2048\n\t"
            "s_waitcnt lgkmcnt(14)\n\t"
            "ds_read_b128 v[224:227], v236 offset:12288\n\t"
            "s_waitcnt lgkmcnt(14)\n\t"
            "ds_read_b128 v[228:231], v236 offset:13312\n\t"
            "s_add_u32 s49, s33, 16384\n\t"
            "buffer_load_dwordx4 v[64:67], v248, s[28:31], s49 offen sc1\n\t"
            "s_add_u32 s49, s33, 17408\n\t"
            "buffer_load_dwordx4 v[68:71], v248, s[28:31], s49 offen sc1\n\t"
            "s_waitcnt vmcnt(12)\n\t"
            "s_waitcnt lgkmcnt(14)\n\t"
            "v_mfma_f32_16x16x32_bf16 v[192:195], v[0:3], v[144:147], 0\n\t"
            "buffer_store_dwordx4 v[200:203], v248, s[28:31], s34 offen\n\t"
            "s_waitcnt lgkmcnt(8)\n\t"
            "v_mfma_f32_16x16x32_bf16 v[212:215], v[0:3], v[168:171], 0\n\t"
            "buffer_store_dwordx4 v[204:207], v248, s[28:31], s34 offen offset:1024\n\t"
            "v_mfma_f32_16x16x32_bf16 v[192:195], v[4:7], v[148:151], v[192:195]\n\t"
            "s_waitcnt lgkmcnt(2)\n\t"
            "buffer_store_dwordx4 v[208:211], v248, s[28:31], s34 offen offset:2048\n\t"
            "v_mfma_f32_16x16x32_bf16 v[212:215], v[4:7], v[172:175], v[212:215]\n\t"
            "s_add_u32 s49, s33, 18432\n\t"
            "buffer_load_dwordx4 v[72:75], v248, s[28:31], s49 offen sc1\n\t"
            "v_mfma_f32_16x16x32_bf16 v[192:195], v[8:11], v[152:155], v[192:195]\n\t"
            "s_add_u32 s49, s33, 19456\n\t"
            "buffer_load_dwordx4 v[76:79], v248, s[28:31], s49 offen sc1\n\t"
            "v_mfma_f32_16x16x32_bf16 v[212:215], v[8:11], v[176:179], v[212:215]\n\t"
            "s_add_u32 s49, s33, 20480\n\t"
            "buffer_load_dwordx4 v[80:83], v248, s[28:31], s49 offen sc1\n\t"
            "v_mfma_f32_16x16x32_bf16 v[192:195], v[12:15], v[156:159], v[192:195]\n\t"
            "s_add_u32 s49, s33, 21504\n\t"
            "buffer_load_dwordx4 v[84:87], v248, s[28:31], s49 offen sc1\n\t"
            "v_mfma_f32_16x16x32_bf16 v[212:215], v[12:15], v[180:183], v[212:215]\n\t"
            "s_add_u32 s49, s33, 22528\n\t"
            "buffer_load_dwordx4 v[88:91], v248, s[28:31], s49 offen sc1\n\t"
            "v_mfma_f32_16x16x32_bf16 v[192:195], v[16:19], v[160:163], v[192:195]\n\t"
            "s_add_u32 s49, s33, 23552\n\t"
            "buffer_load_dwordx4 v[92:95], v248, s[28:31], s49 offen sc1\n\t"
            "v_mfma_f32_16x16x32_bf16 v[212:215], v[16:19], v[184:187], v[212:215]\n\t"
            "s_waitcnt lgkmcnt(1)\n\t"
            "v_add_f32 v224, v224, v226\n\t"
            "s_waitcnt lgkmcnt(0)\n\t"
            "v_add_f32 v228, v228, v230\n\t"
            "v_mfma_f32_16x16x32_bf16 v[192:195], v[20:23], v[164:167], v[192:195]\n\t"
            "v_add_f32 v224, v224, v228\n\t"
            "v_add_f32 v225, v225, v227\n\t"
            "v_mfma_f32_16x16x32_bf16 v[212:215], v[20:23], v[188:191], v[212:215]\n\t"
            "s_waitcnt vmcnt(15)\n\t"
            "s_add_u32 s49, s33, 24576\n\t"
            "buffer_load_dwordx4 v[96:99], v248, s[28:31], s49 offen sc1\n\t"
            "v_mfma_f32_16x16x32_bf16 v[196:199], v[24:27], v[144:147], 0\n\t"
            "s_add_u32 s49, s33, 25600\n\t"
            "buffer_load_dwordx4 v[100:103], v248, s[28:31], s49 offen sc1\n\t"
            "v_mfma_f32_16x16x32_bf16 v[216:219], v[24:27], v[168:171], 0\n\t"
            "s_add_u32 s49, s33, 26624\n\t"
            "buffer_load_dwordx4 v[104:107], v248, s[28:31], s49 offen sc1\n\t"
            "v_mfma_f32_16x16x32_bf16 v[196:199], v[28:31], v[148:151], v[196:199]\n\t"
            "s_add_u32 s49, s33, 27648\n\t"
            "buffer_load_dwordx4 v[108:111], v248, s[28:31], s49 offen sc1\n\t"
            "v_mfma_f32_16x16x32_bf16 v[216:219], v[28:31], v[172:175], v[216:219]\n\t"
            "s_add_u32 s49, s33, 28672\n\t"
            "buffer_load_dwordx4 v[112:115], v248, s[28:31], s49 offen sc1\n\t"
            "v_mfma_f32_16x16x32_bf16 v[196:199], v[32:35], v[152:155], v[196:199]\n\t"
            "s_add_u32 s49, s33, 29696\n\t"
            "buffer_load_dwordx4 v[116:119], v248, s[28:31], s49 offen sc1\n\t"
            "v_mfma_f32_16x16x32_bf16 v[216:219], v[32:35], v[176:179], v[216:219]\n\t"
            "v_add_f32 v229, v229, v231\n\t"
            "v_mul_f32 v224, 0x3baaaaab, v224\n\t"
            "v_mfma_f32_16x16x32_bf16 v[196:199], v[36:39], v[156:159], v[196:199]\n\t"
            "v_add_f32 v225, v225, v229\n\t"
            "v_mul_f32_e64 v224, v224, -v224\n\t"
            "v_mfma_f32_16x16x32_bf16 v[216:219], v[36:39], v[180:183], v[216:219]\n\t"
            "v_fmac_f32 v224, 0x3baaaaab, v225\n\t"
            "v_max_f32 v224, 0, v224\n\t"
            "v_mfma_f32_16x16x32_bf16 v[196:199], v[40:43], v[160:163], v[196:199]\n\t"
            "v_add_f32 v224, 0x3727c5ac, v224\n\t"
            "v_rsq_f32 v224, v224\n\t"
            "v_mfma_f32_16x16x32_bf16 v[216:219], v[40:43], v[184:187], v[216:219]\n\t"
            "ds_write_b32 v249, v224\n\t"
            "v_mfma_f32_16x16x32_bf16 v[196:199], v[44:47], v[164:167], v[196:199]\n\t"
            "ds_read_b32 v236, v251\n\t"
            "v_mfma_f32_16x16x32_bf16 v[216:219], v[44:47], v[188:191], v[216:219]\n\t"
            "ds_read_b32 v237, v251 offset:64\n\t"
            "s_waitcnt vmcnt(15)\n\t"
            "v_mfma_f32_16x16x32_bf16 v[200:203], v[48:51], v[144:147], 0\n\t"
            "s_add_u32 s49, s33, 30720\n\t"
            "buffer_load_dwordx4 v[120:123], v248, s[28:31], s49 offen sc1\n\t"
            "v_mfma_f32_16x16x32_bf16 v[220:223], v[48:51], v[168:171], 0\n\t"
            "s_add_u32 s49, s33, 31744\n\t"
            "buffer_load_dwordx4 v[124:127], v248, s[28:31], s49 offen sc1\n\t"
            "v_mfma_f32_16x16x32_bf16 v[200:203], v[52:55], v[148:151], v[200:203]\n\t"
            "s_add_u32 s49, s33, 32768\n\t"
            "buffer_load_dwordx4 v[128:131], v248, s[28:31], s49 offen sc1\n\t"
            "v_mfma_f32_16x16x32_bf16 v[220:223], v[52:55], v[172:175], v[220:223]\n\t"
            "s_add_u32 s49, s33, 33792\n\t"
            "buffer_load_dwordx4 v[132:135], v248, s[28:31], s49 offen sc1\n\t"
            "v_mfma_f32_16x16x32_bf16 v[200:203], v[56:59], v[152:155], v[200:203]\n\t"
            "s_add_u32 s49, s33, 34816\n\t"
            "buffer_load_dwordx4 v[136:139], v248, s[28:31], s49 offen sc1\n\t"
            "v_mfma_f32_16x16x32_bf16 v[220:223], v[56:59], v[176:179], v[220:223]\n\t"
            "s_add_u32 s49, s33, 35840\n\t"
            "buffer_load_dwordx4 v[140:143], v248, s[28:31], s49 offen sc1\n\t"
            "s_setprio 0\n\t"
            "v_mfma_f32_16x16x32_bf16 v[200:203], v[60:63], v[156:159], v[200:203]\n\t"
            "s_waitcnt lgkmcnt(1)\n\t"
            "v_mul_f32 v192, v236, v192\n\t"
            "v_mul_f32 v193, v236, v193\n\t"
            "v_mfma_f32_16x16x32_bf16 v[220:223], v[60:63], v[180:183], v[220:223]\n\t"
            "s_waitcnt lgkmcnt(0)\n\t"
            "v_mul_f32 v212, v237, v212\n\t"
            "v_mul_f32 v213, v237, v213\n\t"
            "v_mfma_f32_16x16x32_bf16 v[200:203], v[64:67], v[160:163], v[200:203]\n\t"
            "v_mul_f32 v194, v236, v194\n\t"
            "v_mul_f32 v195, v236, v195\n\t"
            "v_mfma_f32_16x16x32_bf16 v[220:223], v[64:67], v[184:187], v[220:223]\n\t"
            "v_cvt_pk_bf16_f32 v192, v192, v193\n\t"
            "v_mul_f32 v214, v237, v214\n\t"
            "v_mfma_f32_16x16x32_bf16 v[200:203], v[68:71], v[164:167], v[200:203]\n\t"
            "v_mul_f32 v215, v237, v215\n\t"
            "v_cvt_pk_bf16_f32 v212, v212, v213\n\t"
            "v_mfma_f32_16x16x32_bf16 v[220:223], v[68:71], v[188:191], v[220:223]\n\t"
            "s_waitcnt vmcnt(12)\n\t"
            "v_cvt_pk_bf16_f32 v193, v194, v195\n\t"
            "v_mfma_f32_16x16x32_bf16 v[204:207], v[72:75], v[144:147], 0\n\t"
            "v_cvt_pk_bf16_f32 v213, v214, v215\n\t"
            "v_mul_f32 v196, v236, v196\n\t"
            "v_mfma_f32_16x16x32_bf16 v[224:227], v[72:75], v[168:171], 0\n\t"
            "v_mul_f32 v197, v236, v197\n\t"
            "v_mul_f32 v198, v236, v198\n\t"
            "v_mfma_f32_16x16x32_bf16 v[204:207], v[76:79], v[148:151], v[204:207]\n\t"
            "v_mul_f32 v199, v236, v199\n\t"
            "v_mul_f32 v216, v237, v216\n\t"
            "v_mfma_f32_16x16x32_bf16 v[224:227], v[76:79], v[172:175], v[224:227]\n\t"
            "v_mul_f32 v217, v237, v217\n\t"
            "v_mul_f32 v218, v237, v218\n\t"
            "v_mfma_f32_16x16x32_bf16 v[204:207], v[80:83], v[152:155], v[204:207]\n\t"
            "v_mul_f32 v219, v237, v219\n\t"
            "v_cvt_pk_bf16_f32 v194, v196, v197\n\t"
            "v_mfma_f32_16x16x32_bf16 v[224:227], v[80:83], v[176:179], v[224:227]\n\t"
            "v_cvt_pk_bf16_f32 v195, v198, v199\n\t"
            "v_cvt_pk_bf16_f32 v214, v216, v217\n\t"
            "v_mfma_f32_16x16x32_bf16 v[204:207], v[84:87], v[156:159], v[204:207]\n\t"
            "v_cvt_pk_bf16_f32 v215, v218, v219\n\t"
            "v_mul_f32 v200, v236, v200\n\t"
            "v_mfma_f32_16x16x32_bf16 v[224:227], v[84:87], v[180:183], v[224:227]\n\t"
            "v_mul_f32 v201, v236, v201\n\t"
            "v_mul_f32 v220, v237, v220\n\t"
            "v_mfma_f32_16x16x32_bf16 v[204:207], v[88:91], v[160:163], v[204:207]\n\t"
            "v_mul_f32 v221, v237, v221\n\t"
            "v_mul_f32 v202, v236, v202\n\t"
            "v_mfma_f32_16x16x32_bf16 v[224:227], v[88:91], v[184:187], v[224:227]\n\t"
            "v_mul_f32 v203, v236, v203\n\t"
            "v_cvt_pk_bf16_f32 v200, v200, v201\n\t"
            "v_mfma_f32_16x16x32_bf16 v[204:207], v[92:95], v[164:167], v[204:207]\n\t"
            "v_mul_f32 v222, v237, v222\n\t"
            "v_mul_f32 v223, v237, v223\n\t"
            "v_mfma_f32_16x16x32_bf16 v[224:227], v[92:95], v[188:191], v[224:227]\n\t"
            "s_waitcnt vmcnt(6)\n\t"
            "v_cvt_pk_bf16_f32 v220, v220, v221\n\t"
            "v_mfma_f32_16x16x32_bf16 v[196:199], v[96:99], v[144:147], 0\n\t"
            "v_cvt_pk_bf16_f32 v201, v202, v203\n\t"
            "v_cvt_pk_bf16_f32 v221, v222, v223\n\t"
            "v_mfma_f32_16x16x32_bf16 v[216:219], v[96:99], v[168:171], 0\n\t"
            "v_mul_f32 v204, v236, v204\n\t"
            "v_mul_f32 v205, v236, v205\n\t"
            "v_mfma_f32_16x16x32_bf16 v[196:199], v[100:103], v[148:151], v[196:199]\n\t"
            "v_mul_f32 v224, v237, v224\n\t"
            "v_mul_f32 v225, v237, v225\n\t"
            "v_mfma_f32_16x16x32_bf16 v[216:219], v[100:103], v[172:175], v[216:219]\n\t"
            "v_mul_f32 v206, v236, v206\n\t"
            "v_mul_f32 v207, v236, v207\n\t"
            "v_mfma_f32_16x16x32_bf16 v[196:199], v[104:107], v[152:155], v[196:199]\n\t"
            "v_cvt_pk_bf16_f32 v204, v204, v205\n\t"
            "v_mul_f32 v226, v237, v226\n\t"
            "v_mfma_f32_16x16x32_bf16 v[216:219], v[104:107], v[176:179], v[216:219]\n\t"
            "v_mul_f32 v227, v237, v227\n\t"
            "v_cvt_pk_bf16_f32 v224, v224, v225\n\t"
            "v_mfma_f32_16x16x32_bf16 v[196:199], v[108:111], v[156:159], v[196:199]\n\t"
            "v_cvt_pk_bf16_f32 v205, v206, v207\n\t"
            "v_cvt_pk_bf16_f32 v225, v226, v227\n\t"
            "v_mfma_f32_16x16x32_bf16 v[216:219], v[108:111], v[180:183], v[216:219]\n\t"
            "ds_write_b128 v232, v[192:195]\n\t"
            "v_mfma_f32_16x16x32_bf16 v[196:199], v[112:115], v[160:163], v[196:199]\n\t"
            "ds_write_b128 v233, v[212:215]\n\t"
            "v_mfma_f32_16x16x32_bf16 v[216:219], v[112:115], v[184:187], v[216:219]\n\t"
            "v_mfma_f32_16x16x32_bf16 v[196:199], v[116:119], v[164:167], v[196:199]\n\t"
            "v_mfma_f32_16x16x32_bf16 v[216:219], v[116:119], v[188:191], v[216:219]\n\t"
            "s_waitcnt vmcnt(0)\n\t"
            "v_mfma_f32_16x16x32_bf16 v[208:211], v[120:123], v[144:147], 0\n\t"
            "v_mfma_f32_16x16x32_bf16 v[228:231], v[120:123], v[168:171], 0\n\t"
            "v_mfma_f32_16x16x32_bf16 v[208:211], v[124:127], v[148:151], v[208:211]\n\t"
            "v_mfma_f32_16x16x32_bf16 v[228:231], v[124:127], v[172:175], v[228:231]\n\t"
            "s_nop 1\n\t"
            "v_mul_f32 v196, v236, v196\n\t"
            "v_mfma_f32_16x16x32_bf16 v[208:211], v[128:131], v[152:155], v[208:211]\n\t"
            "v_mul_f32 v197, v236, v197\n\t"
            "v_mul_f32 v198, v236, v198\n\t"
            "v_mfma_f32_16x16x32_bf16 v[228:231], v[128:131], v[176:179], v[228:231]\n\t"
            "v_mul_f32 v199, v236, v199\n\t"
            "v_mul_f32 v216, v237, v216\n\t"
            "v_mfma_f32_16x16x32_bf16 v[208:211], v[132:135], v[156:159], v[208:211]\n\t"
            "v_mul_f32 v217, v237, v217\n\t"
            "v_mul_f32 v218, v237, v218\n\t"
            "v_mfma_f32_16x16x32_bf16 v[228:231], v[132:135], v[180:183], v[228:231]\n\t"
            "v_mul_f32 v219, v237, v219\n\t"
            "v_cvt_pk_bf16_f32 v206, v196, v197\n\t"
            "v_mfma_f32_16x16x32_bf16 v[208:211], v[136:139], v[160:163], v[208:211]\n\t"
            "v_cvt_pk_bf16_f32 v207, v198, v199\n\t"
            "v_cvt_pk_bf16_f32 v226, v216, v217\n\t"
            "v_mfma_f32_16x16x32_bf16 v[228:231], v[136:139], v[184:187], v[228:231]\n\t"
            "v_cvt_pk_bf16_f32 v227, v218, v219\n\t"
            "v_mfma_f32_16x16x32_bf16 v[208:211], v[140:143], v[164:167], v[208:211]\n\t"
            "ds_write_b128 v232, v[204:207] offset:64\n\t"
            "v_mfma_f32_16x16x32_bf16 v[228:231], v[140:143], v[188:191], v[228:231]\n\t"
            "ds_write_b128 v233, v[224:227] offset:64\n\t"
            "s_nop 4\n\t"
            "v_mul_f32 v208, v236, v208\n\t"
            "v_mul_f32 v209, v236, v209\n\t"
            "v_mul_f32 v210, v236, v210\n\t"
            "v_mul_f32 v211, v236, v211\n\t"
            "v_cvt_pk_bf16_f32 v202, v208, v209\n\t"
            "v_cvt_pk_bf16_f32 v203, v210, v211\n\t"
            "v_mul_f32 v228, v237, v228\n\t"
            "v_mul_f32 v229, v237, v229\n\t"
            "v_mfma_f32_16x16x16_bf16 v[196:199], v[200:201], v[202:203], 0\n\t"
            "v_mul_f32 v230, v237, v230\n\t"
            "v_mul_f32 v231, v237, v231\n\t"
            "v_cvt_pk_bf16_f32 v222, v228, v229\n\t"
            "v_cvt_pk_bf16_f32 v223, v230, v231\n\t"
            "s_nop 1\n\t"
            "v_mfma_f32_16x16x16_bf16 v[216:219], v[220:221], v[222:223], 0\n\t"
            "ds_write_b128 v232, v[200:203] offset:128\n\t"
            "v_mfma_f32_16x16x32_bf16 v[196:199], v[192:195], v[204:207], v[196:199]\n\t"
            "ds_write_b128 v233, v[220:223] offset:128\n\t"
            "s_nop 2\n\t"
            "v_mfma_f32_16x16x32_bf16 v[216:219], v[212:215], v[224:227], v[216:219]\n\t"
            "s_nop 2\n\t"
            "v_cndmask_b32_e64 v196, v196, v197, s[50:51]\n\t"
            "v_cndmask_b32_e64 v196, v196, v198, s[52:53]\n\t"
            "v_cndmask_b32_e64 v196, v196, v199, s[54:55]\n\t"
            "ds_write_b32 v234, v196\n\t"
            "s_nop 0\n\t"
            "v_cndmask_b32_e64 v216, v216, v217, s[50:51]\n\t"
            "v_cndmask_b32_e64 v216, v216, v218, s[52:53]\n\t"
            "v_cndmask_b32_e64 v216, v216, v219, s[54:55]\n\t"
            "ds_write_b32 v235, v216\n\t"
            "s_waitcnt lgkmcnt(0)\n\t"
            "s_nop 7\n\t"
            "s_cmp_lg_u32 s33, 0\n\t"
            "s_cbranch_scc1 .Lar%=\n\t"
            "s_mov_b64 exec, 1\n\t"
            "v_mov_b32 v216, 1\n\t"
            "s_mov_b32 s49, 295168\n\t"
            "buffer_atomic_add v216, off, s[28:31], s49\n\t"
            "s_mov_b64 exec, -1\n\t"
            ".Lar%=:\n\t"
            "v_mov_b32 v224, s46\n\t"
            ".Ls2%=:\n\t"
            "ds_read_b128 v[220:223], v224\n\t"
            "s_waitcnt lgkmcnt(0)\n\t"
            "v_and_b32 v220, v220, v221\n\t"
            "v_and_b32 v222, v222, v223\n\t"
            "v_and_b32 v220, v220, v222\n\t"
            "s_nop 0\n\t"
            "v_readfirstlane_b32 s49, v220\n\t"
            "s_nop 3\n\t"
            "s_bitcmp1_b32 s49, 31\n\t"
            "s_cbranch_scc0 .Ls2%=\n\t"
            "s_waitcnt lgkmcnt(0)\n\t"
            "s_nop 7\n\t"
            "s_setprio 2\n\t"
            "v_add_u32 v232, s48, v244\n\t"
            "v_add_u32 v208, s48, v248\n\t"
            "v_xor_b32 v233, 64, v232\n\t"
            "v_add_u32 v234, s72, v252\n\t"
            "v_cndmask_b32_e64 v235, v246, v234, s[50:51]\n\t"
            "v_cndmask_b32_e64 v234, v246, v234, s[26:27]\n\t"
            "v_mov_b32 v239, s76\n\t"
            "ds_read_b128 v[216:219], v232\n\t"
            "ds_read_b128 v[204:207], v208 offset:18816\n\t"
            "ds_read_b128 v[208:211], v208 offset:19840\n\t"
            "ds_read_b128 v[220:223], v233\n\t"
            "ds_read_b128 v[224:227], v232 offset:128\n\t"
            "ds_read_b128 v[188:191], v233 offset:6272\n\t"
            "ds_read_b128 v[192:195], v232 offset:6400\n\t"
            "ds_read_b128 v[200:203], v233 offset:6400\n\t"
            "ds_read_b32 v239, v239\n\t"
            "s_waitcnt lgkmcnt(8)\n\t"
            "v_mfma_f32_16x16x32_bf16 v[148:151], v[72:75], v[216:219], 0\n\t"
            "s_waitcnt lgkmcnt(7)\n\t"
            "v_add_f32 v204, v204, v206\n\t"
            "v_add_f32 v205, v205, v207\n\t"
            "s_waitcnt lgkmcnt(6)\n\t"
            "v_add_f32 v208, v208, v210\n\t"
            "v_add_f32 v209, v209, v211\n\t"
            "v_add_f32 v204, v204, v208\n\t"
            "v_add_f32 v205, v205, v209\n\t"
            "v_mfma_f32_16x16x32_bf16 v[208:211], v[96:99], v[216:219], 0\n\t"
            "v_mul_f32 v204, 0x3baaaaab, v204\n\t"
            "v_mul_f32_e64 v204, v204, -v204\n\t"
            "v_mfma_f32_16x16x32_bf16 v[212:215], v[120:123], v[216:219], 0\n\t"
            "v_fmac_f32 v204, 0x3baaaaab, v205\n\t"
            "v_max_f32 v204, 0, v204\n\t"
            "v_mfma_f32_16x16x32_bf16 v[144:147], v[0:3], v[216:219], 0\n\t"
            "v_add_f32 v204, 0x3727c5ac, v204\n\t"
            "v_rsq_f32 v204, v204\n\t"
            "s_nop 0\n\t"
            "ds_write_b32 v249, v204\n\t"
            "v_mfma_f32_16x16x32_bf16 v[204:207], v[24:27], v[216:219], 0\n\t"
            "ds_read_b32 v236, v251\n\t"
            "v_mfma_f32_16x16x32_bf16 v[152:155], v[48:51], v[216:219], 0\n\t"
            "ds_read_b128 v[216:219], v233 offset:128\n\t"
            "s_waitcnt lgkmcnt(8)\n\t"
            "v_mfma_f32_16x16x32_bf16 v[148:151], v[76:79], v[220:223], v[148:151]\n\t"
            "ds_read_b32 v237, v251 offset:64\n\t"
            "v_mfma_f32_16x16x32_bf16 v[208:211], v[100:103], v[220:223], v[208:211]\n\t"
            "ds_read_b32 v238, v251 offset:128\n\t"
            "v_mfma_f32_16x16x32_bf16 v[212:215], v[124:127], v[220:223], v[212:215]\n\t"
            "v_mfma_f32_16x16x32_bf16 v[144:147], v[4:7], v[220:223], v[144:147]\n\t"
            "v_mfma_f32_16x16x32_bf16 v[204:207], v[28:31], v[220:223], v[204:207]\n\t"
            "v_mfma_f32_16x16x32_bf16 v[152:155], v[52:55], v[220:223], v[152:155]\n\t"
            "ds_read_b128 v[220:223], v232 offset:256\n\t"
            "s_waitcnt lgkmcnt(10)\n\t"
            "v_mfma_f32_16x16x32_bf16 v[144:147], v[8:11], v[224:227], v[144:147]\n\t"
            "v_mfma_f32_16x16x32_bf16 v[204:207], v[32:35], v[224:227], v[204:207]\n\t"
            "v_mfma_f32_16x16x32_bf16 v[152:155], v[56:59], v[224:227], v[152:155]\n\t"
            "v_mfma_f32_16x16x32_bf16 v[148:151], v[80:83], v[224:227], v[148:151]\n\t"
            "v_mfma_f32_16x16x32_bf16 v[208:211], v[104:107], v[224:227], v[208:211]\n\t"
            "v_mfma_f32_16x16x32_bf16 v[212:215], v[128:131], v[224:227], v[212:215]\n\t"
            "ds_read_b128 v[224:227], v233 offset:256\n\t"
            "s_waitcnt lgkmcnt(4)\n\t"
            "v_mfma_f32_16x16x32_bf16 v[148:151], v[84:87], v[216:219], v[148:151]\n\t"
            "v_mfma_f32_16x16x32_bf16 v[208:211], v[108:111], v[216:219], v[208:211]\n\t"
            "v_mfma_f32_16x16x32_bf16 v[212:215], v[132:135], v[216:219], v[212:215]\n\t"
            "v_mfma_f32_16x16x32_bf16 v[144:147], v[12:15], v[216:219], v[144:147]\n\t"
            "v_mfma_f32_16x16x32_bf16 v[204:207], v[36:39], v[216:219], v[204:207]\n\t"
            "v_mfma_f32_16x16x32_bf16 v[152:155], v[60:63], v[216:219], v[152:155]\n\t"
            "ds_read_b128 v[216:219], v232 offset:6144\n\t"
            "s_waitcnt lgkmcnt(2)\n\t"
            "v_mfma_f32_16x16x32_bf16 v[148:151], v[88:91], v[220:223], v[148:151]\n\t"
            "v_mfma_f32_16x16x32_bf16 v[208:211], v[112:115], v[220:223], v[208:211]\n\t"
            "v_mfma_f32_16x16x32_bf16 v[212:215], v[136:139], v[220:223], v[212:215]\n\t"
            "v_mfma_f32_16x16x32_bf16 v[144:147], v[16:19], v[220:223], v[144:147]\n\t"
            "v_mfma_f32_16x16x32_bf16 v[204:207], v[40:43], v[220:223], v[204:207]\n\t"
            "v_mfma_f32_16x16x32_bf16 v[152:155], v[64:67], v[220:223], v[152:155]\n\t"
            "ds_read_b128 v[220:223], v233 offset:6144\n\t"
            "s_waitcnt lgkmcnt(2)\n\t"
            "v_mfma_f32_16x16x32_bf16 v[144:147], v[20:23], v[224:227], v[144:147]\n\t"
            "v_mfma_f32_16x16x32_bf16 v[204:207], v[44:47], v[224:227], v[204:207]\n\t"
            "v_mfma_f32_16x16x32_bf16 v[152:155], v[68:71], v[224:227], v[152:155]\n\t"
            "v_mfma_f32_16x16x32_bf16 v[148:151], v[92:95], v[224:227], v[148:151]\n\t"
            "v_mfma_f32_16x16x32_bf16 v[208:211], v[116:119], v[224:227], v[208:211]\n\t"
            "v_mfma_f32_16x16x32_bf16 v[212:215], v[140:143], v[224:227], v[212:215]\n\t"
            "ds_read_b128 v[224:227], v232 offset:6272\n\t"
            "s_waitcnt lgkmcnt(2)\n\t"
            "v_mfma_f32_16x16x32_bf16 v[160:163], v[72:75], v[216:219], 0\n\t"
            "v_mul_f32 v144, v236, v144\n\t"
            "v_mul_f32 v145, v236, v145\n\t"
            "v_mul_f32 v146, v236, v146\n\t"
            "s_waitcnt lgkmcnt(1)\n\t"
            "v_mfma_f32_16x16x32_bf16 v[160:163], v[76:79], v[220:223], v[160:163]\n\t"
            "v_mul_f32 v152, v236, v152\n\t"
            "v_mul_f32 v153, v236, v153\n\t"
            "v_mul_f32 v154, v236, v154\n\t"
            "v_mul_f32 v155, v236, v155\n\t"
            "v_cvt_pk_bf16_f32 v152, v152, v153\n\t"
            "v_cvt_pk_bf16_f32 v153, v154, v155\n\t"
            "v_mul_f32 v212, v236, v212\n\t"
            "v_mul_f32 v213, v236, v213\n\t"
            "v_mul_f32 v214, v236, v214\n\t"
            "v_mul_f32 v215, v236, v215\n\t"
            "v_cvt_pk_bf16_f32 v154, v212, v213\n\t"
            "v_cvt_pk_bf16_f32 v155, v214, v215\n\t"
            "v_mul_f32 v147, v236, v147\n\t"
            "v_cvt_pk_bf16_f32 v144, v144, v145\n\t"
            "v_mul_f32 v148, v236, v148\n\t"
            "v_mul_f32 v149, v236, v149\n\t"
            "v_cvt_pk_bf16_f32 v145, v146, v147\n\t"
            "v_mul_f32 v204, v236, v204\n\t"
            "v_mul_f32 v205, v236, v205\n\t"
            "v_mul_f32 v206, v236, v206\n\t"
            "v_mul_f32 v207, v236, v207\n\t"
            "v_mul_f32 v150, v236, v150\n\t"
            "s_waitcnt lgkmcnt(0)\n\t"
            "v_mfma_f32_16x16x32_bf16 v[160:163], v[80:83], v[224:227], v[160:163]\n\t"
            "ds_read_b128 v[212:215], v232 offset:12416\n\t"
            "v_mul_f32 v151, v236, v151\n\t"
            "v_mfma_f32_16x16x32_bf16 v[160:163], v[84:87], v[188:191], v[160:163]\n\t"
            "v_cvt_pk_bf16_f32 v148, v148, v149\n\t"
            "v_cvt_pk_bf16_f32 v146, v204, v205\n\t"
            "v_mfma_f32_16x16x32_bf16 v[160:163], v[88:91], v[192:195], v[160:163]\n\t"
            "v_cvt_pk_bf16_f32 v147, v206, v207\n\t"
            "v_cvt_pk_bf16_f32 v149, v150, v151\n\t"
            "v_mfma_f32_16x16x32_bf16 v[160:163], v[92:95], v[200:203], v[160:163]\n\t"
            "ds_read_b128 v[204:207], v232 offset:12288\n\t"
            "v_mul_f32 v208, v236, v208\n\t"
            "v_mfma_f32_16x16x16_bf16 v[180:183], v[152:153], v[154:155], 0\n\t"
            "v_mul_f32 v209, v236, v209\n\t"
            "v_mul_f32 v210, v236, v210\n\t"
            "v_mul_f32 v211, v236, v211\n\t"
            "v_mfma_f32_16x16x32_bf16 v[196:199], v[24:27], v[216:219], 0\n\t"
            "v_cvt_pk_bf16_f32 v150, v208, v209\n\t"
            "v_cvt_pk_bf16_f32 v151, v210, v211\n\t"
            "v_mfma_f32_16x16x32_bf16 v[196:199], v[28:31], v[220:223], v[196:199]\n\t"
            "ds_read_b128 v[208:211], v233 offset:12288\n\t"
            "v_mul_f32 v160, v237, v160\n\t"
            "v_mfma_f32_16x16x32_bf16 v[196:199], v[32:35], v[224:227], v[196:199]\n\t"
            "v_mul_f32 v161, v237, v161\n\t"
            "v_mul_f32 v162, v237, v162\n\t"
            "v_mfma_f32_16x16x32_bf16 v[196:199], v[36:39], v[188:191], v[196:199]\n\t"
            "v_mul_f32 v163, v237, v163\n\t"
            "v_cvt_pk_bf16_f32 v160, v160, v161\n\t"
            "v_mfma_f32_16x16x32_bf16 v[196:199], v[40:43], v[192:195], v[196:199]\n\t"
            "v_cvt_pk_bf16_f32 v161, v162, v163\n\t"
            "v_mfma_f32_16x16x32_bf16 v[196:199], v[44:47], v[200:203], v[196:199]\n\t"
            "v_mfma_f32_16x16x32_bf16 v[156:159], v[0:3], v[216:219], 0\n\t"
            "v_mfma_f32_16x16x32_bf16 v[156:159], v[4:7], v[220:223], v[156:159]\n\t"
            "v_mfma_f32_16x16x32_bf16 v[156:159], v[8:11], v[224:227], v[156:159]\n\t"
            "v_mfma_f32_16x16x32_bf16 v[156:159], v[12:15], v[188:191], v[156:159]\n\t"
            "v_mfma_f32_16x16x32_bf16 v[156:159], v[16:19], v[192:195], v[156:159]\n\t"
            "v_mfma_f32_16x16x32_bf16 v[156:159], v[20:23], v[200:203], v[156:159]\n\t"
            "s_nop 1\n\t"
            "v_mul_f32 v196, v237, v196\n\t"
            "v_mfma_f32_16x16x32_bf16 v[164:167], v[48:51], v[216:219], 0\n\t"
            "v_mul_f32 v197, v237, v197\n\t"
            "v_mul_f32 v198, v237, v198\n\t"
            "v_mfma_f32_16x16x32_bf16 v[164:167], v[52:55], v[220:223], v[164:167]\n\t"
            "v_mul_f32 v199, v237, v199\n\t"
            "v_mfma_f32_16x16x32_bf16 v[164:167], v[56:59], v[224:227], v[164:167]\n\t"
            "v_mul_f32 v156, v237, v156\n\t"
            "v_mul_f32 v157, v237, v157\n\t"
            "v_mfma_f32_16x16x32_bf16 v[164:167], v[60:63], v[188:191], v[164:167]\n\t"
            "v_mul_f32 v158, v237, v158\n\t"
            "v_mul_f32 v159, v237, v159\n\t"
            "v_mfma_f32_16x16x32_bf16 v[164:167], v[64:67], v[192:195], v[164:167]\n\t"
            "v_cvt_pk_bf16_f32 v156, v156, v157\n\t"
            "v_cvt_pk_bf16_f32 v157, v158, v159\n\t"
            "v_mfma_f32_16x16x32_bf16 v[164:167], v[68:71], v[200:203], v[164:167]\n\t"
            "v_cvt_pk_bf16_f32 v158, v196, v197\n\t"
            "v_cvt_pk_bf16_f32 v159, v198, v199\n\t"
            "v_mfma_f32_16x16x32_bf16 v[228:231], v[96:99], v[216:219], 0\n\t"
            "v_mfma_f32_16x16x32_bf16 v[228:231], v[100:103], v[220:223], v[228:231]\n\t"
            "v_mfma_f32_16x16x32_bf16 v[228:231], v[104:107], v[224:227], v[228:231]\n\t"
            "v_mfma_f32_16x16x32_bf16 v[228:231], v[108:111], v[188:191], v[228:231]\n\t"
            "s_nop 1\n\t"
            "v_mul_f32 v164, v237, v164\n\t"
            "v_mfma_f32_16x16x32_bf16 v[228:231], v[112:115], v[192:195], v[228:231]\n\t"
            "v_mul_f32 v165, v237, v165\n\t"
            "v_mul_f32 v166, v237, v166\n\t"
            "v_mfma_f32_16x16x32_bf16 v[228:231], v[116:119], v[200:203], v[228:231]\n\t"
            "v_mul_f32 v167, v237, v167\n\t"
            "v_cvt_pk_bf16_f32 v164, v164, v165\n\t"
            "v_mfma_f32_16x16x32_bf16 v[180:183], v[144:147], v[148:151], v[180:183]\n\t"
            "v_cvt_pk_bf16_f32 v165, v166, v167\n\t"
            "v_mfma_f32_16x16x32_bf16 v[196:199], v[120:123], v[216:219], 0\n\t"
            "ds_read_b128 v[216:219], v233 offset:12416\n\t"
            "v_mfma_f32_16x16x32_bf16 v[196:199], v[124:127], v[220:223], v[196:199]\n\t"
            "ds_read_b128 v[220:223], v232 offset:12544\n\t"
            "v_mul_f32 v228, v237, v228\n\t"
            "v_mfma_f32_16x16x32_bf16 v[196:199], v[128:131], v[224:227], v[196:199]\n\t"
            "ds_read_b128 v[224:227], v233 offset:12544\n\t"
            "v_mul_f32 v229, v237, v229\n\t"
            "v_mfma_f32_16x16x32_bf16 v[196:199], v[132:135], v[188:191], v[196:199]\n\t"
            "v_mul_f32 v230, v237, v230\n\t"
            "v_mul_f32 v231, v237, v231\n\t"
            "v_mfma_f32_16x16x32_bf16 v[196:199], v[136:139], v[192:195], v[196:199]\n\t"
            "v_cvt_pk_bf16_f32 v162, v228, v229\n\t"
            "v_cvt_pk_bf16_f32 v163, v230, v231\n\t"
            "v_mfma_f32_16x16x32_bf16 v[196:199], v[140:143], v[200:203], v[196:199]\n\t"
            "v_exp_f32 v180, v180\n\t"
            "s_waitcnt lgkmcnt(4)\n\t"
            "v_mfma_f32_16x16x32_bf16 v[176:179], v[48:51], v[204:207], 0\n\t"
            "v_exp_f32 v181, v181\n\t"
            "s_waitcnt lgkmcnt(3)\n\t"
            "v_mfma_f32_16x16x32_bf16 v[176:179], v[52:55], v[208:211], v[176:179]\n\t"
            "v_exp_f32 v182, v182\n\t"
            "v_mfma_f32_16x16x32_bf16 v[176:179], v[56:59], v[212:215], v[176:179]\n\t"
            "v_exp_f32 v183, v183\n\t"
            "s_waitcnt lgkmcnt(2)\n\t"
            "v_mfma_f32_16x16x32_bf16 v[176:179], v[60:63], v[216:219], v[176:179]\n\t"
            "v_mul_f32 v196, v237, v196\n\t"
            "v_mul_f32 v197, v237, v197\n\t"
            "s_waitcnt lgkmcnt(1)\n\t"
            "v_mfma_f32_16x16x32_bf16 v[176:179], v[64:67], v[220:223], v[176:179]\n\t"
            "v_mul_f32 v198, v237, v198\n\t"
            "v_mul_f32 v199, v237, v199\n\t"
            "s_waitcnt lgkmcnt(0)\n\t"
            "v_mfma_f32_16x16x32_bf16 v[176:179], v[68:71], v[224:227], v[176:179]\n\t"
            "v_cvt_pk_bf16_f32 v166, v196, v197\n\t"
            "v_cvt_pk_bf16_f32 v167, v198, v199\n\t"
            "v_cvt_pk_bf16_f32 v180, v180, v181\n\t"
            "v_cvt_pk_bf16_f32 v181, v182, v183\n\t"
            "v_mfma_f32_16x16x16_bf16 v[188:191], v[152:153], v[166:167], 0\n\t"
            "v_mfma_f32_16x16x32_bf16 v[168:171], v[0:3], v[204:207], 0\n\t"
            "s_nop 1\n\t"
            "v_mul_f32 v176, v238, v176\n\t"
            "v_mfma_f32_16x16x32_bf16 v[168:171], v[4:7], v[208:211], v[168:171]\n\t"
            "v_mul_f32 v177, v238, v177\n\t"
            "v_mul_f32 v178, v238, v178\n\t"
            "v_mfma_f32_16x16x32_bf16 v[168:171], v[8:11], v[212:215], v[168:171]\n\t"
            "v_mul_f32 v179, v238, v179\n\t"
            "v_cvt_pk_bf16_f32 v176, v176, v177\n\t"
            "v_mfma_f32_16x16x32_bf16 v[168:171], v[12:15], v[216:219], v[168:171]\n\t"
            "v_cvt_pk_bf16_f32 v177, v178, v179\n\t"
            "v_mfma_f32_16x16x32_bf16 v[168:171], v[16:19], v[220:223], v[168:171]\n\t"
            "v_mfma_f32_16x16x32_bf16 v[168:171], v[20:23], v[224:227], v[168:171]\n\t"
            "v_mfma_f32_16x16x16_bf16 v[192:195], v[164:165], v[166:167], 0\n\t"
            "v_mfma_f32_16x16x32_bf16 v[188:191], v[144:147], v[160:163], v[188:191]\n\t"
            "v_mfma_f32_16x16x16_bf16 v[184:187], v[164:165], v[154:155], 0\n\t"
            "s_nop 3\n\t"
            "v_mfma_f32_16x16x32_bf16 v[192:195], v[156:159], v[160:163], v[192:195]\n\t"
            "v_mfma_f32_16x16x32_bf16 v[172:175], v[72:75], v[204:207], 0\n\t"
            "v_mfma_f32_16x16x32_bf16 v[172:175], v[76:79], v[208:211], v[172:175]\n\t"
            "v_mul_f32 v168, v238, v168\n\t"
            "v_mfma_f32_16x16x32_bf16 v[172:175], v[80:83], v[212:215], v[172:175]\n\t"
            "v_mul_f32 v169, v238, v169\n\t"
            "v_mul_f32 v170, v238, v170\n\t"
            "v_mfma_f32_16x16x32_bf16 v[172:175], v[84:87], v[216:219], v[172:175]\n\t"
            "v_exp_f32 v188, v188\n\t"
            "v_mfma_f32_16x16x32_bf16 v[172:175], v[88:91], v[220:223], v[172:175]\n\t"
            "v_exp_f32 v189, v189\n\t"
            "v_mfma_f32_16x16x32_bf16 v[172:175], v[92:95], v[224:227], v[172:175]\n\t"
            "v_exp_f32 v190, v190\n\t"
            "v_mfma_f32_16x16x32_bf16 v[184:187], v[156:159], v[148:151], v[184:187]\n\t"
            "v_exp_f32 v191, v191\n\t"
            "v_mfma_f32_16x16x32_bf16 v[200:203], v[120:123], v[204:207], 0\n\t"
            "v_exp_f32 v192, v192\n\t"
            "v_mfma_f32_16x16x32_bf16 v[200:203], v[124:127], v[208:211], v[200:203]\n\t"
            "v_exp_f32 v193, v193\n\t"
            "v_mfma_f32_16x16x32_bf16 v[200:203], v[128:131], v[212:215], v[200:203]\n\t"
            "v_exp_f32 v194, v194\n\t"
            "v_mfma_f32_16x16x32_bf16 v[200:203], v[132:135], v[216:219], v[200:203]\n\t"
            "v_exp_f32 v184, v184\n\t"
            "v_mfma_f32_16x16x32_bf16 v[200:203], v[136:139], v[220:223], v[200:203]\n\t"
            "v_exp_f32 v185, v185\n\t"
            "v_mfma_f32_16x16x32_bf16 v[200:203], v[140:143], v[224:227], v[200:203]\n\t"
            "v_exp_f32 v186, v186\n\t"
            "v_exp_f32 v187, v187\n\t"
            "v_cvt_pk_bf16_f32 v182, v184, v185\n\t"
            "v_cvt_pk_bf16_f32 v183, v186, v187\n\t"
            "v_mfma_f32_16x16x16_bf16 v[184:187], v[176:177], v[154:155], 0\n\t"
            "v_exp_f32 v195, v195\n\t"
            "v_cvt_pk_bf16_f32 v188, v188, v189\n\t"
            "v_mfma_f32_16x16x32_bf16 v[196:199], v[24:27], v[204:207], 0\n\t"
            "v_cvt_pk_bf16_f32 v189, v190, v191\n\t"
            "v_cvt_pk_bf16_f32 v190, v192, v193\n\t"
            "v_mfma_f32_16x16x32_bf16 v[196:199], v[28:31], v[208:211], v[196:199]\n\t"
            "v_cvt_pk_bf16_f32 v191, v194, v195\n\t"
            "v_mul_f32 v171, v238, v171\n\t"
            "v_mfma_f32_16x16x32_bf16 v[196:199], v[32:35], v[212:215], v[196:199]\n\t"
            "v_cvt_pk_bf16_f32 v168, v168, v169\n\t"
            "v_cvt_pk_bf16_f32 v169, v170, v171\n\t"
            "v_mfma_f32_16x16x32_bf16 v[196:199], v[36:39], v[216:219], v[196:199]\n\t"
            "v_mul_f32 v172, v238, v172\n\t"
            "v_mul_f32 v173, v238, v173\n\t"
            "v_mfma_f32_16x16x32_bf16 v[196:199], v[40:43], v[220:223], v[196:199]\n\t"
            "v_mul_f32 v200, v238, v200\n\t"
            "v_mul_f32 v201, v238, v201\n\t"
            "v_mfma_f32_16x16x32_bf16 v[196:199], v[44:47], v[224:227], v[196:199]\n\t"
            "v_mul_f32 v202, v238, v202\n\t"
            "v_mul_f32 v203, v238, v203\n\t"
            "v_mfma_f32_16x16x32_bf16 v[228:231], v[96:99], v[204:207], 0\n\t"
            "ds_read_b64 v[204:205], v235 offset:136\n\t"
            "v_mul_f32 v174, v238, v174\n\t"
            "v_mfma_f32_16x16x32_bf16 v[228:231], v[100:103], v[208:211], v[228:231]\n\t"
            "ds_read_b64 v[206:207], v234 offset:128\n\t"
            "v_mul_f32 v175, v238, v175\n\t"
            "v_mfma_f32_16x16x32_bf16 v[228:231], v[104:107], v[212:215], v[228:231]\n\t"
            "ds_read_b128 v[208:211], v234\n\t"
            "v_mul_f32 v196, v238, v196\n\t"
            "v_mfma_f32_16x16x32_bf16 v[228:231], v[108:111], v[216:219], v[228:231]\n\t"
            "ds_read_b128 v[212:215], v235 offset:64\n\t"
            "v_mul_f32 v197, v238, v197\n\t"
            "v_mfma_f32_16x16x32_bf16 v[228:231], v[112:115], v[220:223], v[228:231]\n\t"
            "v_mul_f32 v198, v238, v198\n\t"
            "v_mul_f32 v199, v238, v199\n\t"
            "v_mfma_f32_16x16x32_bf16 v[228:231], v[116:119], v[224:227], v[228:231]\n\t"
            "v_cvt_pk_bf16_f32 v170, v196, v197\n\t"
            "v_cvt_pk_bf16_f32 v171, v198, v199\n\t"
            "v_mfma_f32_16x16x16_bf16 v[192:195], v[176:177], v[166:167], 0\n\t"
            "v_cvt_pk_bf16_f32 v172, v172, v173\n\t"
            "v_cvt_pk_bf16_f32 v178, v200, v201\n\t"
            "v_cvt_pk_bf16_f32 v179, v202, v203\n\t"
            "v_mfma_f32_16x16x32_bf16 v[184:187], v[168:171], v[148:151], v[184:187]\n\t"
            "v_cvt_pk_bf16_f32 v173, v174, v175\n\t"
            "v_mul_f32 v228, v238, v228\n\t"
            "v_mfma_f32_16x16x32_bf16 v[192:195], v[168:171], v[160:163], v[192:195]\n\t"
            "v_mul_f32 v229, v238, v229\n\t"
            "v_mul_f32 v230, v238, v230\n\t"
            "v_mul_f32 v231, v238, v231\n\t"
            "s_waitcnt lgkmcnt(2)\n\t"
            "v_mfma_f32_16x16x32_bf16 v[216:219], v[204:207], v[152:155], 0\n\t"
            "v_cvt_pk_bf16_f32 v174, v228, v229\n\t"
            "v_cvt_pk_bf16_f32 v175, v230, v231\n\t"
            "s_waitcnt lgkmcnt(1)\n\t"
            "v_mfma_f32_16x16x32_bf16 v[216:219], v[208:211], v[148:151], v[216:219]\n\t"
            "v_exp_f32 v184, v184\n\t"
            "s_waitcnt lgkmcnt(0)\n\t"
            "v_mfma_f32_16x16x32_bf16 v[216:219], v[212:215], v[144:147], v[216:219]\n\t"
            "v_exp_f32 v185, v185\n\t"
            "v_mfma_f32_16x16x32_bf16 v[220:223], v[204:207], v[164:167], 0\n\t"
            "v_exp_f32 v186, v186\n\t"
            "v_mfma_f32_16x16x32_bf16 v[220:223], v[208:211], v[160:163], v[220:223]\n\t"
            "v_exp_f32 v187, v187\n\t"
            "v_mfma_f32_16x16x32_bf16 v[220:223], v[212:215], v[156:159], v[220:223]\n\t"
            "v_cvt_pk_bf16_f32 v184, v184, v185\n\t"
            "v_cvt_pk_bf16_f32 v185, v186, v187\n\t"
            "v_mfma_f32_16x16x16_bf16 v[228:231], v[164:165], v[178:179], 0\n\t"
            "v_exp_f32 v216, v216\n\t"
            "v_mov_b32 v187, 0\n\t"
            "v_mfma_f32_16x16x16_bf16 v[200:203], v[176:177], v[178:179], 0\n\t"
            "v_cvt_pk_bf16_f32 v216, v216, 0\n\t"
            "v_cndmask_b32_e64 v186, 0, v216, s[58:59]\n\t"
            "v_exp_f32 v192, v192\n\t"
            "v_mfma_f32_16x16x32_bf16 v[224:227], v[204:207], v[176:179], 0\n\t"
            "v_exp_f32 v193, v193\n\t"
            "v_mfma_f32_16x16x32_bf16 v[224:227], v[208:211], v[172:175], v[224:227]\n\t"
            "v_exp_f32 v194, v194\n\t"
            "v_mfma_f32_16x16x32_bf16 v[224:227], v[212:215], v[168:171], v[224:227]\n\t"
            "v_exp_f32 v195, v195\n\t"
            "v_mfma_f32_16x16x16_bf16 v[196:199], v[152:153], v[178:179], 0\n\t"
            "v_exp_f32 v220, v220\n\t"
            "v_cvt_pk_bf16_f32 v192, v192, v193\n\t"
            "v_mfma_f32_16x16x32_bf16 v[204:207], v[240:243], v[180:183], 0\n\t"
            "v_cvt_pk_bf16_f32 v193, v194, v195\n\t"
            "v_cvt_pk_bf16_f32 v220, v220, 0\n\t"
            "v_mfma_f32_16x16x32_bf16 v[204:207], v[240:243], v[184:187], v[204:207]\n\t"
            "v_cndmask_b32_e64 v194, 0, v220, s[58:59]\n\t"
            "v_mov_b32 v195, 0\n\t"
            "v_mfma_f32_16x16x32_bf16 v[200:203], v[168:171], v[172:175], v[200:203]\n\t"
            "v_cndmask_b32_e64 v235, v217, v221, s[60:61]\n\t"
            "v_cndmask_b32_e64 v235, v235, v225, s[62:63]\n\t"
            "v_mfma_f32_16x16x32_bf16 v[208:211], v[240:243], v[188:191], 0\n\t"
            "v_cndmask_b32_e64 v235, v235, v239, s[64:65]\n\t"
            "v_cmp_nlt_f32_e64 s[24:25], |v235|, s23\n\t"
            "v_mfma_f32_16x16x32_bf16 v[208:211], v[240:243], v[192:195], v[208:211]\n\t"
            "v_exp_f32 v235, v235\n\t"
            "v_mfma_f32_16x16x32_bf16 v[196:199], v[144:147], v[172:175], v[196:199]\n\t"
            "v_exp_f32 v200, v200\n\t"
            "v_mfma_f32_16x16x32_bf16 v[228:231], v[156:159], v[172:175], v[228:231]\n\t"
            "v_exp_f32 v201, v201\n\t"
            "v_exp_f32 v202, v202\n\t"
            "v_exp_f32 v203, v203\n\t"
            "v_exp_f32 v224, v224\n\t"
            "v_cvt_pk_bf16_f32 v235, v235, 0\n\t"
            "v_cvt_pk_bf16_f32 v200, v200, v201\n\t"
            "v_exp_f32 v196, v196\n\t"
            "v_exp_f32 v197, v197\n\t"
            "v_exp_f32 v198, v198\n\t"
            "v_exp_f32 v199, v199\n\t"
            "v_exp_f32 v228, v228\n\t"
            "v_exp_f32 v229, v229\n\t"
            "v_exp_f32 v230, v230\n\t"
            "v_exp_f32 v231, v231\n\t"
            "v_cvt_pk_bf16_f32 v196, v196, v197\n\t"
            "v_cvt_pk_bf16_f32 v197, v198, v199\n\t"
            "v_cvt_pk_bf16_f32 v198, v228, v229\n\t"
            "v_cvt_pk_bf16_f32 v199, v230, v231\n\t"
            "v_cndmask_b32_e64 v235, 0, v235, s[66:67]\n\t"
            "v_cvt_pk_bf16_f32 v201, v202, v203\n\t"
            "v_mfma_f32_16x16x32_bf16 v[212:215], v[240:243], v[196:199], 0\n\t"
            "v_cvt_pk_bf16_f32 v224, v224, 0\n\t"
            "v_cndmask_b32_e64 v202, 0, v224, s[58:59]\n\t"
            "v_mov_b32 v203, 0\n\t"
            "v_add_u32 v205, 0xe0800000, v204\n\t"
            "v_add_u32 v209, 0xe0800000, v208\n\t"
            "v_mfma_f32_16x16x32_bf16 v[212:215], v[240:243], v[200:203], v[212:215]\n\t"
            "ds_write_b16 v250, v235 offset:256\n\t"
            "v_rcp_f32 v236, v204\n\t"
            "v_rcp_f32 v237, v208\n\t"
            "s_nop 4\n\t"
            "v_add_u32 v213, 0xe0800000, v212\n\t"
            "v_rcp_f32 v238, v212\n\t"
            "v_or3_b32 v205, v205, v209, v213\n\t"
            "v_cmp_le_u32 vcc, 2.0, v205\n\t"
            "s_waitcnt lgkmcnt(0)\n\t"
            "s_nop 7\n\t"
            "s_branch .Lwa_in%=\n\t"
            ".p2align 6\n\t"
            ".Lwa_loop%=:\n\t"
            "ds_read_b64 v[156:157], v247\n\t"
            "v_add_u32 v232, s48, v244\n\t"
            "v_add_u32 v208, s48, v248\n\t"
            "v_add_u32 v235, s45, v248\n\t"
            "v_xor_b32 v234, s73, v254\n\t"
            "v_xor_b32 v233, 64, v232\n\t"
            "v_mov_b32 v159, 0\n\t"
            "v_mov_b32 v167, 0\n\t"
            "v_mov_b32 v163, 0\n\t"
            "v_mov_b32 v239, s76\n\t"
            "ds_read_b128 v[216:219], v232\n\t"
            "ds_read_b128 v[204:207], v208 offset:18816\n\t"
            "ds_read_b128 v[208:211], v208 offset:19840\n\t"
            "ds_read_b128 v[168:171], v235\n\t"
            "ds_read_u16 v158, v234\n\t"
            "ds_read_b128 v[220:223], v233\n\t"
            "ds_read_b128 v[172:175], v235 offset:1536\n\t"
            "ds_read_b64 v[164:165], v247 offset:1536\n\t"
            "ds_read_u16 v166, v234 offset:128\n\t"
            "ds_read_b128 v[176:179], v235 offset:3072\n\t"
            "ds_read_b64 v[160:161], v247 offset:3072\n\t"
            "ds_read_u16 v162, v234 offset:256\n\t"
            "v_add_u32 v234, s72, v252\n\t"
            "v_cndmask_b32_e64 v235, v246, v234, s[50:51]\n\t"
            "v_cndmask_b32_e64 v234, v246, v234, s[26:27]\n\t"
            "ds_read_b128 v[224:227], v232 offset:128\n\t"
            "s_waitcnt lgkmcnt(14)\n\t"
            "ds_read_b32 v239, v239\n\t"
            "s_waitcnt lgkmcnt(13)\n\t"
            "v_mfma_f32_16x16x32_bf16 v[148:151], v[72:75], v[216:219], 0\n\t"
            "s_waitcnt lgkmcnt(12)\n\t"
            "v_add_f32 v204, v204, v206\n\t"
            "v_add_f32 v205, v205, v207\n\t"
            "s_waitcnt lgkmcnt(11)\n\t"
            "v_add_f32 v208, v208, v210\n\t"
            "v_add_f32 v209, v209, v211\n\t"
            "v_add_f32 v204, v204, v208\n\t"
            "v_add_f32 v205, v205, v209\n\t"
            "v_mfma_f32_16x16x32_bf16 v[208:211], v[96:99], v[216:219], 0\n\t"
            "v_mul_f32 v204, 0x3baaaaab, v204\n\t"
            "v_mul_f32_e64 v204, v204, -v204\n\t"
            "v_mfma_f32_16x16x32_bf16 v[212:215], v[120:123], v[216:219], 0\n\t"
            "v_fmac_f32 v204, 0x3baaaaab, v205\n\t"
            "v_max_f32 v204, 0, v204\n\t"
            "s_waitcnt lgkmcnt(10)\n\t"
            "v_mfma_f32_16x16x32_bf16 v[228:231], v[168:171], v[180:183], 0\n\t"
            "v_add_f32 v204, 0x3727c5ac, v204\n\t"
            "v_rsq_f32 v204, v204\n\t"
            "s_waitcnt lgkmcnt(9)\n\t"
            "v_mfma_f32_16x16x32_bf16 v[228:231], v[156:159], v[184:187], v[228:231]\n\t"
            "ds_write_b32 v249, v204\n\t"
            "v_mfma_f32_16x16x32_bf16 v[144:147], v[0:3], v[216:219], 0\n\t"
            "v_mfma_f32_16x16x32_bf16 v[204:207], v[24:27], v[216:219], 0\n\t"
            "v_mfma_f32_16x16x32_bf16 v[152:155], v[48:51], v[216:219], 0\n\t"
            "ds_read_b128 v[216:219], v233 offset:128\n\t"
            "s_waitcnt lgkmcnt(10)\n\t"
            "v_mfma_f32_16x16x32_bf16 v[148:151], v[76:79], v[220:223], v[148:151]\n\t"
            "s_nop 0\n\t"
            "v_mul_f32 v228, v236, v228\n\t"
            "v_mfma_f32_16x16x32_bf16 v[208:211], v[100:103], v[220:223], v[208:211]\n\t"
            "v_mul_f32 v229, v236, v229\n\t"
            "v_mul_f32 v230, v236, v230\n\t"
            "v_mfma_f32_16x16x32_bf16 v[212:215], v[124:127], v[220:223], v[212:215]\n\t"
            "v_mul_f32 v231, v236, v231\n\t"
            "v_cvt_pk_bf16_f32 v228, v228, v229\n\t"
            "v_mfma_f32_16x16x32_bf16 v[144:147], v[4:7], v[220:223], v[144:147]\n\t"
            "v_cvt_pk_bf16_f32 v229, v230, v231\n\t"
            "v_mfma_f32_16x16x32_bf16 v[204:207], v[28:31], v[220:223], v[204:207]\n\t"
            "ds_write_b64 v245, v[228:229]\n\t"
            "v_mfma_f32_16x16x32_bf16 v[152:155], v[52:55], v[220:223], v[152:155]\n\t"
            "ds_read_b128 v[220:223], v232 offset:256\n\t"
            "s_waitcnt lgkmcnt(11)\n\t"
            "v_mfma_f32_16x16x32_bf16 v[228:231], v[172:175], v[180:183], 0\n\t"
            "s_waitcnt lgkmcnt(9)\n\t"
            "v_mfma_f32_16x16x32_bf16 v[228:231], v[164:167], v[184:187], v[228:231]\n\t"
            "s_waitcnt lgkmcnt(8)\n\t"
            "v_mfma_f32_16x16x32_bf16 v[180:183], v[176:179], v[180:183], 0\n\t"
            "s_waitcnt lgkmcnt(6)\n\t"
            "v_mfma_f32_16x16x32_bf16 v[180:183], v[160:163], v[184:187], v[180:183]\n\t"
            "s_waitcnt lgkmcnt(5)\n\t"
            "v_mfma_f32_16x16x32_bf16 v[144:147], v[8:11], v[224:227], v[144:147]\n\t"
            "v_mfma_f32_16x16x32_bf16 v[204:207], v[32:35], v[224:227], v[204:207]\n\t"
            "v_mfma_f32_16x16x32_bf16 v[152:155], v[56:59], v[224:227], v[152:155]\n\t"
            "v_mfma_f32_16x16x32_bf16 v[148:151], v[80:83], v[224:227], v[148:151]\n\t"
            "v_mul_f32 v228, v236, v228\n\t"
            "v_mfma_f32_16x16x32_bf16 v[208:211], v[104:107], v[224:227], v[208:211]\n\t"
            "v_mul_f32 v229, v236, v229\n\t"
            "v_mul_f32 v230, v236, v230\n\t"
            "v_mfma_f32_16x16x32_bf16 v[212:215], v[128:131], v[224:227], v[212:215]\n\t"
            "ds_read_b128 v[224:227], v233 offset:256\n\t"
            "v_mul_f32 v231, v236, v231\n\t"
            "v_mfma_f32_16x16x32_bf16 v[184:187], v[168:171], v[188:191], 0\n\t"
            "v_cvt_pk_bf16_f32 v228, v228, v229\n\t"
            "v_cvt_pk_bf16_f32 v229, v230, v231\n\t"
            "v_mfma_f32_16x16x32_bf16 v[184:187], v[156:159], v[192:195], v[184:187]\n\t"
            "ds_write_b64 v245, v[228:229] offset:128\n\t"
            "v_mfma_f32_16x16x32_bf16 v[228:231], v[172:175], v[188:191], 0\n\t"
            "v_mul_f32 v180, v236, v180\n\t"
            "v_mul_f32 v181, v236, v181\n\t"
            "v_mfma_f32_16x16x32_bf16 v[228:231], v[164:167], v[192:195], v[228:231]\n\t"
            "v_mul_f32 v182, v236, v182\n\t"
            "v_mul_f32 v183, v236, v183\n\t"
            "s_waitcnt lgkmcnt(4)\n\t"
            "v_mfma_f32_16x16x32_bf16 v[148:151], v[84:87], v[216:219], v[148:151]\n\t"
            "ds_read_b32 v236, v251\n\t"
            "v_cvt_pk_bf16_f32 v180, v180, v181\n\t"
            "v_mfma_f32_16x16x32_bf16 v[208:211], v[108:111], v[216:219], v[208:211]\n\t"
            "v_cvt_pk_bf16_f32 v181, v182, v183\n\t"
            "v_mul_f32 v184, v237, v184\n\t"
            "v_mfma_f32_16x16x32_bf16 v[212:215], v[132:135], v[216:219], v[212:215]\n\t"
            "ds_write_b64 v245, v[180:181] offset:256\n\t"
            "v_mfma_f32_16x16x32_bf16 v[144:147], v[12:15], v[216:219], v[144:147]\n\t"
            "v_mul_f32 v185, v237, v185\n\t"
            "v_mul_f32 v186, v237, v186\n\t"
            "v_mfma_f32_16x16x32_bf16 v[204:207], v[36:39], v[216:219], v[204:207]\n\t"
            "v_mul_f32 v187, v237, v187\n\t"
            "v_cvt_pk_bf16_f32 v184, v184, v185\n\t"
            "v_mfma_f32_16x16x32_bf16 v[152:155], v[60:63], v[216:219], v[152:155]\n\t"
            "ds_read_b128 v[216:219], v232 offset:6144\n\t"
            "v_cvt_pk_bf16_f32 v185, v186, v187\n\t"
            "v_mfma_f32_16x16x32_bf16 v[180:183], v[168:171], v[196:199], 0\n\t"
            "ds_read_b64 v[186:187], v253 offset:352\n\t"
            "v_mul_f32 v228, v237, v228\n\t"
            "v_mfma_f32_16x16x32_bf16 v[180:183], v[156:159], v[200:203], v[180:183]\n\t"
            "ds_write_b64 v245, v[184:185] offset:6144\n\t"
            "s_waitcnt lgkmcnt(7)\n\t"
            "v_mfma_f32_16x16x32_bf16 v[148:151], v[88:91], v[220:223], v[148:151]\n\t"
            "ds_read_b64 v[184:185], v253 offset:320\n\t"
            "v_mul_f32 v229, v237, v229\n\t"
            "v_mfma_f32_16x16x32_bf16 v[208:211], v[112:115], v[220:223], v[208:211]\n\t"
            "v_mul_f32 v230, v237, v230\n\t"
            "v_mul_f32 v231, v237, v231\n\t"
            "v_mfma_f32_16x16x32_bf16 v[212:215], v[136:139], v[220:223], v[212:215]\n\t"
            "v_mul_f32 v180, v238, v180\n\t"
            "v_mul_f32 v181, v238, v181\n\t"
            "v_mfma_f32_16x16x32_bf16 v[188:191], v[176:179], v[188:191], 0\n\t"
            "v_mul_f32 v182, v238, v182\n\t"
            "v_mul_f32 v183, v238, v183\n\t"
            "v_mfma_f32_16x16x32_bf16 v[188:191], v[160:163], v[192:195], v[188:191]\n\t"
            "ds_read_b128 v[192:195], v232 offset:6400\n\t"
            "v_cvt_pk_bf16_f32 v180, v180, v181\n\t"
            "v_mfma_f32_16x16x32_bf16 v[144:147], v[16:19], v[220:223], v[144:147]\n\t"
            "v_cvt_pk_bf16_f32 v181, v182, v183\n\t"
            "v_cvt_pk_bf16_f32 v228, v228, v229\n\t"
            "v_mfma_f32_16x16x32_bf16 v[204:207], v[40:43], v[220:223], v[204:207]\n\t"
            "ds_read_b64 v[182:183], v253 offset:288\n\t"
            "v_cvt_pk_bf16_f32 v229, v230, v231\n\t"
            "v_mfma_f32_16x16x32_bf16 v[152:155], v[64:67], v[220:223], v[152:155]\n\t"
            "ds_read_b128 v[220:223], v233 offset:6144\n\t"
            "v_mul_f32 v188, v237, v188\n\t"
            "s_waitcnt lgkmcnt(10)\n\t"
            "v_mfma_f32_16x16x32_bf16 v[144:147], v[20:23], v[224:227], v[144:147]\n\t"
            "ds_write_b64 v245, v[180:181] offset:12288\n\t"
            "v_mfma_f32_16x16x32_bf16 v[204:207], v[44:47], v[224:227], v[204:207]\n\t"
            "ds_read_b64 v[180:181], v253 offset:256\n\t"
            "v_mul_f32 v189, v237, v189\n\t"
            "v_mfma_f32_16x16x32_bf16 v[152:155], v[68:71], v[224:227], v[152:155]\n\t"
            "ds_write_b64 v245, v[228:229] offset:6272\n\t"
            "v_mfma_f32_16x16x32_bf16 v[148:151], v[92:95], v[224:227], v[148:151]\n\t"
            "v_mul_f32 v190, v237, v190\n\t"
            "v_mul_f32 v191, v237, v191\n\t"
            "v_mfma_f32_16x16x32_bf16 v[208:211], v[116:119], v[224:227], v[208:211]\n\t"
            "ds_read_b32 v237, v251 offset:64\n\t"
            "v_cvt_pk_bf16_f32 v188, v188, v189\n\t"
            "v_mfma_f32_16x16x32_bf16 v[212:215], v[140:143], v[224:227], v[212:215]\n\t"
            "ds_read_b128 v[224:227], v232 offset:6272\n\t"
            "v_cvt_pk_bf16_f32 v189, v190, v191\n\t"
            "v_mfma_f32_16x16x32_bf16 v[228:231], v[172:175], v[196:199], 0\n\t"
            "s_waitcnt lgkmcnt(14)\n\t"
            "ds_write_b64 v245, v[188:189] offset:6400\n\t"
            "v_mfma_f32_16x16x32_bf16 v[228:231], v[164:167], v[200:203], v[228:231]\n\t"
            "s_waitcnt lgkmcnt(14)\n\t"
            "ds_read_b128 v[188:191], v233 offset:6272\n\t"
            "v_mul_f32 v152, v236, v152\n\t"
            "v_mfma_f32_16x16x32_bf16 v[196:199], v[176:179], v[196:199], 0\n\t"
            "v_mul_f32 v153, v236, v153\n\t"
            "v_mul_f32 v154, v236, v154\n\t"
            "v_mfma_f32_16x16x32_bf16 v[196:199], v[160:163], v[200:203], v[196:199]\n\t"
            "s_waitcnt lgkmcnt(14)\n\t"
            "ds_read_b128 v[200:203], v233 offset:6400\n\t"
            "v_mul_f32 v155, v236, v155\n\t"
            "s_waitcnt lgkmcnt(6)\n\t"
            "v_mfma_f32_16x16x32_bf16 v[168:171], v[168:171], v[180:183], 0\n\t"
            "v_mul_f32 v228, v238, v228\n\t"
            "v_mul_f32 v229, v238, v229\n\t"
            "v_mfma_f32_16x16x32_bf16 v[168:171], v[156:159], v[184:187], v[168:171]\n\t"
            "v_mul_f32 v230, v238, v230\n\t"
            "v_mul_f32 v231, v238, v231\n\t"
            "v_mfma_f32_16x16x32_bf16 v[176:179], v[176:179], v[180:183], 0\n\t"
            "v_cvt_pk_bf16_f32 v228, v228, v229\n\t"
            "v_cvt_pk_bf16_f32 v229, v230, v231\n\t"
            "v_mfma_f32_16x16x32_bf16 v[176:179], v[160:163], v[184:187], v[176:179]\n\t"
            "ds_write_b64 v245, v[228:229] offset:12416\n\t"
            "v_mfma_f32_16x16x32_bf16 v[228:231], v[240:243], v[180:183], 0\n\t"
            "v_cvt_pk_bf16_f32 v152, v152, v153\n\t"
            "v_cvt_pk_bf16_f32 v153, v154, v155\n\t"
            "v_mfma_f32_16x16x32_bf16 v[228:231], v[240:243], v[184:187], v[228:231]\n\t"
            "v_mul_f32 v212, v236, v212\n\t"
            "v_mul_f32 v213, v236, v213\n\t"
            "v_mfma_f32_16x16x32_bf16 v[160:163], v[72:75], v[216:219], 0\n\t"
            "v_mul_f32 v214, v236, v214\n\t"
            "v_mul_f32 v215, v236, v215\n\t"
            "v_mfma_f32_16x16x32_bf16 v[160:163], v[76:79], v[220:223], v[160:163]\n\t"
            "v_cvt_pk_bf16_f32 v154, v212, v213\n\t"
            "v_cvt_pk_bf16_f32 v155, v214, v215\n\t"
            "s_waitcnt lgkmcnt(4)\n\t"
            "v_mfma_f32_16x16x32_bf16 v[160:163], v[80:83], v[224:227], v[160:163]\n\t"
            "ds_read_b128 v[212:215], v232 offset:12416\n\t"
            "v_mul_f32 v196, v238, v196\n\t"
            "s_waitcnt lgkmcnt(3)\n\t"
            "v_mfma_f32_16x16x32_bf16 v[160:163], v[84:87], v[188:191], v[160:163]\n\t"
            "v_mul_f32 v197, v238, v197\n\t"
            "v_mul_f32 v198, v238, v198\n\t"
            "v_mfma_f32_16x16x32_bf16 v[160:163], v[88:91], v[192:195], v[160:163]\n\t"
            "v_mul_f32 v199, v238, v199\n\t"
            "v_cvt_pk_bf16_f32 v196, v196, v197\n\t"
            "s_waitcnt lgkmcnt(2)\n\t"
            "v_mfma_f32_16x16x32_bf16 v[160:163], v[92:95], v[200:203], v[160:163]\n\t"
            "ds_read_b32 v238, v251 offset:128\n\t"
            "v_cvt_pk_bf16_f32 v197, v198, v199\n\t"
            "v_mfma_f32_16x16x32_bf16 v[172:175], v[172:175], v[180:183], 0\n\t"
            "ds_write_b64 v245, v[196:197] offset:12544\n\t"
            "v_mfma_f32_16x16x32_bf16 v[172:175], v[164:167], v[184:187], v[172:175]\n\t"
            "v_rcp_f32 v229, v228\n\t"
            "v_mfma_f32_16x16x16_bf16 v[180:183], v[152:153], v[154:155], 0\n\t"
            "v_mul_f32 v168, v229, v168\n\t"
            "v_mul_f32 v169, v229, v169\n\t"
            "v_mul_f32 v176, v229, v176\n\t"
            "v_mfma_f32_16x16x32_bf16 v[196:199], v[24:27], v[216:219], 0\n\t"
            "v_mul_f32 v177, v229, v177\n\t"
            "v_mul_f32 v170, v229, v170\n\t"
            "v_mfma_f32_16x16x32_bf16 v[196:199], v[28:31], v[220:223], v[196:199]\n\t"
            "v_mul_f32 v172, v229, v172\n\t"
            "v_mul_f32 v173, v229, v173\n\t"
            "v_mfma_f32_16x16x32_bf16 v[196:199], v[32:35], v[224:227], v[196:199]\n\t"
            "v_mul_f32 v171, v229, v171\n\t"
            "v_cvt_pk_bf16_f32 v168, v168, v169\n\t"
            "v_mfma_f32_16x16x32_bf16 v[196:199], v[36:39], v[188:191], v[196:199]\n\t"
            "v_mul_f32 v174, v229, v174\n\t"
            "v_mul_f32 v175, v229, v175\n\t"
            "v_mfma_f32_16x16x32_bf16 v[196:199], v[40:43], v[192:195], v[196:199]\n\t"
            "v_cvt_pk_bf16_f32 v172, v172, v173\n\t"
            "v_mul_f32 v178, v229, v178\n\t"
            "v_mfma_f32_16x16x32_bf16 v[196:199], v[44:47], v[200:203], v[196:199]\n\t"
            "v_mul_f32 v179, v229, v179\n\t"
            "v_cvt_pk_bf16_f32 v176, v176, v177\n\t"
            "v_mfma_f32_16x16x32_bf16 v[156:159], v[0:3], v[216:219], 0\n\t"
            "v_xor_b32 v230, s73, v255\n\t"
            "v_cvt_pk_bf16_f32 v169, v170, v171\n\t"
            "v_mfma_f32_16x16x32_bf16 v[156:159], v[4:7], v[220:223], v[156:159]\n\t"
            "v_cvt_pk_bf16_f32 v173, v174, v175\n\t"
            "v_cvt_pk_bf16_f32 v177, v178, v179\n\t"
            "v_mfma_f32_16x16x32_bf16 v[156:159], v[8:11], v[224:227], v[156:159]\n\t"
            "s_mov_b64 exec, s[68:69]\n\t"
            "ds_write_b64 v230, v[168:169]\n\t"
            "ds_write_b64 v230, v[172:173] offset:128\n\t"
            "ds_write_b64 v230, v[176:177] offset:256\n\t"
            "s_mov_b64 exec, -1\n\t"
            "v_mfma_f32_16x16x32_bf16 v[156:159], v[12:15], v[188:191], v[156:159]\n\t"
            "v_mul_f32 v144, v236, v144\n\t"
            "v_mul_f32 v145, v236, v145\n\t"
            "v_mfma_f32_16x16x32_bf16 v[156:159], v[16:19], v[192:195], v[156:159]\n\t"
            "v_mul_f32 v146, v236, v146\n\t"
            "v_mul_f32 v147, v236, v147\n\t"
            "v_mfma_f32_16x16x32_bf16 v[156:159], v[20:23], v[200:203], v[156:159]\n\t"
            "v_cvt_pk_bf16_f32 v144, v144, v145\n\t"
            "v_mul_f32 v148, v236, v148\n\t"
            "v_mfma_f32_16x16x32_bf16 v[164:167], v[48:51], v[216:219], 0\n\t"
            "v_mul_f32 v149, v236, v149\n\t"
            "v_cvt_pk_bf16_f32 v145, v146, v147\n\t"
            "v_mfma_f32_16x16x32_bf16 v[164:167], v[52:55], v[220:223], v[164:167]\n\t"
            "v_mul_f32 v204, v236, v204\n\t"
            "v_mul_f32 v205, v236, v205\n\t"
            "v_mfma_f32_16x16x32_bf16 v[164:167], v[56:59], v[224:227], v[164:167]\n\t"
            "v_mul_f32 v206, v236, v206\n\t"
            "v_mul_f32 v207, v236, v207\n\t"
            "v_mfma_f32_16x16x32_bf16 v[164:167], v[60:63], v[188:191], v[164:167]\n\t"
            "s_waitcnt lgkmcnt(0)\n\t"
            "s_barrier\n\t"
            "s_setprio 2\n\t"
            "v_mul_f32 v150, v236, v150\n\t"
            "v_mul_f32 v151, v236, v151\n\t"
            "v_mfma_f32_16x16x32_bf16 v[164:167], v[64:67], v[192:195], v[164:167]\n\t"
            "v_cvt_pk_bf16_f32 v148, v148, v149\n\t"
            "v_cvt_pk_bf16_f32 v146, v204, v205\n\t"
            "v_mfma_f32_16x16x32_bf16 v[164:167], v[68:71], v[200:203], v[164:167]\n\t"
            "v_cvt_pk_bf16_f32 v147, v206, v207\n\t"
            "v_cvt_pk_bf16_f32 v149, v150, v151\n\t"
            "v_mfma_f32_16x16x32_bf16 v[228:231], v[96:99], v[216:219], 0\n\t"
            "ds_read_b128 v[204:207], v232 offset:12288\n\t"
            "v_mul_f32 v208, v236, v208\n\t"
            "v_mfma_f32_16x16x32_bf16 v[228:231], v[100:103], v[220:223], v[228:231]\n\t"
            "v_mul_f32 v209, v236, v209\n\t"
            "v_mul_f32 v210, v236, v210\n\t"
            "v_mfma_f32_16x16x32_bf16 v[228:231], v[104:107], v[224:227], v[228:231]\n\t"
            "v_mul_f32 v211, v236, v211\n\t"
            "v_cvt_pk_bf16_f32 v150, v208, v209\n\t"
            "v_mfma_f32_16x16x32_bf16 v[228:231], v[108:111], v[188:191], v[228:231]\n\t"
            "v_cvt_pk_bf16_f32 v151, v210, v211\n\t"
            "v_mul_f32 v156, v237, v156\n\t"
            "v_mfma_f32_16x16x32_bf16 v[228:231], v[112:115], v[192:195], v[228:231]\n\t"
            "ds_read_b128 v[208:211], v233 offset:12288\n\t"
            "v_mul_f32 v157, v237, v157\n\t"
            "v_mfma_f32_16x16x32_bf16 v[228:231], v[116:119], v[200:203], v[228:231]\n\t"
            "v_mul_f32 v158, v237, v158\n\t"
            "v_mul_f32 v159, v237, v159\n\t"
            "v_mfma_f32_16x16x32_bf16 v[180:183], v[144:147], v[148:151], v[180:183]\n\t"
            "v_cvt_pk_bf16_f32 v156, v156, v157\n\t"
            "v_cvt_pk_bf16_f32 v157, v158, v159\n\t"
            "v_mul_f32 v196, v237, v196\n\t"
            "v_mul_f32 v197, v237, v197\n\t"
            "v_mul_f32 v198, v237, v198\n\t"
            "v_mul_f32 v199, v237, v199\n\t"
            "v_cvt_pk_bf16_f32 v158, v196, v197\n\t"
            "v_cvt_pk_bf16_f32 v159, v198, v199\n\t"
            "v_mfma_f32_16x16x32_bf16 v[196:199], v[120:123], v[216:219], 0\n\t"
            "ds_read_b128 v[216:219], v233 offset:12416\n\t"
            "v_mul_f32 v164, v237, v164\n\t"
            "v_mfma_f32_16x16x32_bf16 v[196:199], v[124:127], v[220:223], v[196:199]\n\t"
            "ds_read_b128 v[220:223], v232 offset:12544\n\t"
            "v_mul_f32 v165, v237, v165\n\t"
            "v_mfma_f32_16x16x32_bf16 v[196:199], v[128:131], v[224:227], v[196:199]\n\t"
            "ds_read_b128 v[224:227], v233 offset:12544\n\t"
            "v_mul_f32 v166, v237, v166\n\t"
            "v_mfma_f32_16x16x32_bf16 v[196:199], v[132:135], v[188:191], v[196:199]\n\t"
            "v_mul_f32 v167, v237, v167\n\t"
            "v_cvt_pk_bf16_f32 v164, v164, v165\n\t"
            "v_mfma_f32_16x16x32_bf16 v[196:199], v[136:139], v[192:195], v[196:199]\n\t"
            "v_cvt_pk_bf16_f32 v165, v166, v167\n\t"
            "v_mul_f32 v160, v237, v160\n\t"
            "v_mfma_f32_16x16x32_bf16 v[196:199], v[140:143], v[200:203], v[196:199]\n\t"
            "v_mul_f32 v161, v237, v161\n\t"
            "v_mul_f32 v162, v237, v162\n\t"
            "s_waitcnt lgkmcnt(4)\n\t"
            "v_mfma_f32_16x16x32_bf16 v[176:179], v[48:51], v[204:207], 0\n\t"
            "v_mul_f32 v163, v237, v163\n\t"
            "v_cvt_pk_bf16_f32 v160, v160, v161\n\t"
            "s_waitcnt lgkmcnt(3)\n\t"
            "v_mfma_f32_16x16x32_bf16 v[176:179], v[52:55], v[208:211], v[176:179]\n\t"
            "v_cvt_pk_bf16_f32 v161, v162, v163\n\t"
            "v_mul_f32 v228, v237, v228\n\t"
            "v_mfma_f32_16x16x32_bf16 v[176:179], v[56:59], v[212:215], v[176:179]\n\t"
            "v_mul_f32 v196, v237, v196\n\t"
            "v_mul_f32 v197, v237, v197\n\t"
            "s_waitcnt lgkmcnt(2)\n\t"
            "v_mfma_f32_16x16x32_bf16 v[176:179], v[60:63], v[216:219], v[176:179]\n\t"
            "v_mul_f32 v198, v237, v198\n\t"
            "v_mul_f32 v199, v237, v199\n\t"
            "s_waitcnt lgkmcnt(1)\n\t"
            "v_mfma_f32_16x16x32_bf16 v[176:179], v[64:67], v[220:223], v[176:179]\n\t"
            "v_cvt_pk_bf16_f32 v166, v196, v197\n\t"
            "v_cvt_pk_bf16_f32 v167, v198, v199\n\t"
            "s_waitcnt lgkmcnt(0)\n\t"
            "v_mfma_f32_16x16x32_bf16 v[176:179], v[68:71], v[224:227], v[176:179]\n\t"
            "v_mul_f32 v229, v237, v229\n\t"
            "v_mul_f32 v230, v237, v230\n\t"
            "v_mfma_f32_16x16x16_bf16 v[188:191], v[152:153], v[166:167], 0\n\t"
            "v_mul_f32 v231, v237, v231\n\t"
            "v_cvt_pk_bf16_f32 v162, v228, v229\n\t"
            "v_cvt_pk_bf16_f32 v163, v230, v231\n\t"
            "v_mfma_f32_16x16x32_bf16 v[168:171], v[0:3], v[204:207], 0\n\t"
            "v_exp_f32 v180, v180\n\t"
            "v_mfma_f32_16x16x32_bf16 v[168:171], v[4:7], v[208:211], v[168:171]\n\t"
            "v_exp_f32 v181, v181\n\t"
            "v_mfma_f32_16x16x32_bf16 v[168:171], v[8:11], v[212:215], v[168:171]\n\t"
            "v_exp_f32 v182, v182\n\t"
            "v_mfma_f32_16x16x32_bf16 v[168:171], v[12:15], v[216:219], v[168:171]\n\t"
            "v_exp_f32 v183, v183\n\t"
            "v_mfma_f32_16x16x32_bf16 v[168:171], v[16:19], v[220:223], v[168:171]\n\t"
            "v_mul_f32 v176, v238, v176\n\t"
            "v_mul_f32 v177, v238, v177\n\t"
            "v_mfma_f32_16x16x32_bf16 v[168:171], v[20:23], v[224:227], v[168:171]\n\t"
            "v_mul_f32 v178, v238, v178\n\t"
            "v_mul_f32 v179, v238, v179\n\t"
            "v_mfma_f32_16x16x16_bf16 v[192:195], v[164:165], v[166:167], 0\n\t"
            "v_cvt_pk_bf16_f32 v176, v176, v177\n\t"
            "v_cvt_pk_bf16_f32 v180, v180, v181\n\t"
            "v_cvt_pk_bf16_f32 v177, v178, v179\n\t"
            "v_mfma_f32_16x16x32_bf16 v[188:191], v[144:147], v[160:163], v[188:191]\n\t"
            "v_cvt_pk_bf16_f32 v181, v182, v183\n\t"
            "v_mul_f32 v168, v238, v168\n\t"
            "v_mfma_f32_16x16x16_bf16 v[184:187], v[164:165], v[154:155], 0\n\t"
            "v_mul_f32 v169, v238, v169\n\t"
            "v_mul_f32 v170, v238, v170\n\t"
            "v_mul_f32 v171, v238, v171\n\t"
            "v_mfma_f32_16x16x32_bf16 v[192:195], v[156:159], v[160:163], v[192:195]\n\t"
            "s_nop 0\n\t"
            "v_exp_f32 v188, v188\n\t"
            "v_mfma_f32_16x16x32_bf16 v[172:175], v[72:75], v[204:207], 0\n\t"
            "v_exp_f32 v189, v189\n\t"
            "v_mfma_f32_16x16x32_bf16 v[172:175], v[76:79], v[208:211], v[172:175]\n\t"
            "v_exp_f32 v190, v190\n\t"
            "v_mfma_f32_16x16x32_bf16 v[172:175], v[80:83], v[212:215], v[172:175]\n\t"
            "v_exp_f32 v191, v191\n\t"
            "v_mfma_f32_16x16x32_bf16 v[172:175], v[84:87], v[216:219], v[172:175]\n\t"
            "v_exp_f32 v192, v192\n\t"
            "v_mfma_f32_16x16x32_bf16 v[172:175], v[88:91], v[220:223], v[172:175]\n\t"
            "v_exp_f32 v193, v193\n\t"
            "v_mfma_f32_16x16x32_bf16 v[172:175], v[92:95], v[224:227], v[172:175]\n\t"
            "v_exp_f32 v194, v194\n\t"
            "v_mfma_f32_16x16x32_bf16 v[184:187], v[156:159], v[148:151], v[184:187]\n\t"
            "v_exp_f32 v195, v195\n\t"
            "v_mfma_f32_16x16x32_bf16 v[200:203], v[120:123], v[204:207], 0\n\t"
            "v_cvt_pk_bf16_f32 v188, v188, v189\n\t"
            "v_cvt_pk_bf16_f32 v189, v190, v191\n\t"
            "v_mfma_f32_16x16x32_bf16 v[200:203], v[124:127], v[208:211], v[200:203]\n\t"
            "v_cvt_pk_bf16_f32 v190, v192, v193\n\t"
            "v_cvt_pk_bf16_f32 v191, v194, v195\n\t"
            "v_mfma_f32_16x16x32_bf16 v[200:203], v[128:131], v[212:215], v[200:203]\n\t"
            "v_exp_f32 v184, v184\n\t"
            "v_mfma_f32_16x16x32_bf16 v[200:203], v[132:135], v[216:219], v[200:203]\n\t"
            "v_exp_f32 v185, v185\n\t"
            "v_mfma_f32_16x16x32_bf16 v[200:203], v[136:139], v[220:223], v[200:203]\n\t"
            "v_exp_f32 v186, v186\n\t"
            "v_mfma_f32_16x16x32_bf16 v[200:203], v[140:143], v[224:227], v[200:203]\n\t"
            "v_exp_f32 v187, v187\n\t"
            "v_cvt_pk_bf16_f32 v182, v184, v185\n\t"
            "v_cvt_pk_bf16_f32 v183, v186, v187\n\t"
            "v_mfma_f32_16x16x16_bf16 v[184:187], v[176:177], v[154:155], 0\n\t"
            "v_cvt_pk_bf16_f32 v168, v168, v169\n\t"
            "v_cvt_pk_bf16_f32 v169, v170, v171\n\t"
            "v_mul_f32 v172, v238, v172\n\t"
            "v_mfma_f32_16x16x32_bf16 v[196:199], v[24:27], v[204:207], 0\n\t"
            "s_setprio 0\n\t"
            "v_mul_f32 v173, v238, v173\n\t"
            "v_mul_f32 v200, v238, v200\n\t"
            "v_mfma_f32_16x16x32_bf16 v[196:199], v[28:31], v[208:211], v[196:199]\n\t"
            "v_mul_f32 v201, v238, v201\n\t"
            "v_mul_f32 v202, v238, v202\n\t"
            "v_mfma_f32_16x16x32_bf16 v[196:199], v[32:35], v[212:215], v[196:199]\n\t"
            "v_mul_f32 v203, v238, v203\n\t"
            "v_mul_f32 v174, v238, v174\n\t"
            "v_mfma_f32_16x16x32_bf16 v[196:199], v[36:39], v[216:219], v[196:199]\n\t"
            "v_mul_f32 v175, v238, v175\n\t"
            "v_cvt_pk_bf16_f32 v172, v172, v173\n\t"
            "v_mfma_f32_16x16x32_bf16 v[196:199], v[40:43], v[220:223], v[196:199]\n\t"
            "v_cvt_pk_bf16_f32 v178, v200, v201\n\t"
            "v_cvt_pk_bf16_f32 v179, v202, v203\n\t"
            "v_mfma_f32_16x16x32_bf16 v[196:199], v[44:47], v[224:227], v[196:199]\n\t"
            "v_cvt_pk_bf16_f32 v173, v174, v175\n\t"
            "v_mfma_f32_16x16x32_bf16 v[228:231], v[96:99], v[204:207], 0\n\t"
            "ds_read_b64 v[204:205], v235 offset:136\n\t"
            "v_mfma_f32_16x16x32_bf16 v[228:231], v[100:103], v[208:211], v[228:231]\n\t"
            "ds_read_b64 v[206:207], v234 offset:128\n\t"
            "v_mfma_f32_16x16x32_bf16 v[228:231], v[104:107], v[212:215], v[228:231]\n\t"
            "ds_read_b128 v[208:211], v234\n\t"
            "v_mfma_f32_16x16x32_bf16 v[228:231], v[108:111], v[216:219], v[228:231]\n\t"
            "ds_read_b128 v[212:215], v235 offset:64\n\t"
            "v_mul_f32 v196, v238, v196\n\t"
            "v_mfma_f32_16x16x32_bf16 v[228:231], v[112:115], v[220:223], v[228:231]\n\t"
            "v_mul_f32 v197, v238, v197\n\t"
            "v_mul_f32 v198, v238, v198\n\t"
            "v_mfma_f32_16x16x32_bf16 v[228:231], v[116:119], v[224:227], v[228:231]\n\t"
            "v_mul_f32 v199, v238, v199\n\t"
            "v_cvt_pk_bf16_f32 v170, v196, v197\n\t"
            "v_mfma_f32_16x16x16_bf16 v[192:195], v[176:177], v[166:167], 0\n\t"
            "v_cvt_pk_bf16_f32 v171, v198, v199\n\t"
            "s_nop 1\n\t"
            "v_mfma_f32_16x16x32_bf16 v[184:187], v[168:171], v[148:151], v[184:187]\n\t"
            "s_nop 0\n\t"
            "v_mul_f32 v228, v238, v228\n\t"
            "v_mul_f32 v229, v238, v229\n\t"
            "v_mul_f32 v230, v238, v230\n\t"
            "v_mul_f32 v231, v238, v231\n\t"
            "v_mfma_f32_16x16x32_bf16 v[192:195], v[168:171], v[160:163], v[192:195]\n\t"
            "v_cvt_pk_bf16_f32 v174, v228, v229\n\t"
            "v_cvt_pk_bf16_f32 v175, v230, v231\n\t"
            "s_waitcnt lgkmcnt(2)\n\t"
            "v_mfma_f32_16x16x32_bf16 v[216:219], v[204:207], v[152:155], 0\n\t"
            "v_exp_f32 v184, v184\n\t"
            "s_waitcnt lgkmcnt(1)\n\t"
            "v_mfma_f32_16x16x32_bf16 v[216:219], v[208:211], v[148:151], v[216:219]\n\t"
            "v_exp_f32 v185, v185\n\t"
            "s_waitcnt lgkmcnt(0)\n\t"
            "v_mfma_f32_16x16x32_bf16 v[216:219], v[212:215], v[144:147], v[216:219]\n\t"
            "v_exp_f32 v186, v186\n\t"
            "v_mfma_f32_16x16x32_bf16 v[220:223], v[204:207], v[164:167], 0\n\t"
            "v_exp_f32 v187, v187\n\t"
            "v_mfma_f32_16x16x32_bf16 v[220:223], v[208:211], v[160:163], v[220:223]\n\t"
            "v_cvt_pk_bf16_f32 v184, v184, v185\n\t"
            "v_cvt_pk_bf16_f32 v185, v186, v187\n\t"
            "v_mfma_f32_16x16x32_bf16 v[220:223], v[212:215], v[156:159], v[220:223]\n\t"
            "s_nop 0\n\t"
            "v_exp_f32 v216, v216\n\t"
            "v_mfma_f32_16x16x16_bf16 v[228:231], v[164:165], v[178:179], 0\n\t"
            "v_cvt_pk_bf16_f32 v216, v216, 0\n\t"
            "v_cndmask_b32_e64 v186, 0, v216, s[58:59]\n\t"
            "v_mov_b32 v187, 0\n\t"
            "v_mfma_f32_16x16x16_bf16 v[200:203], v[176:177], v[178:179], 0\n\t"
            "v_exp_f32 v192, v192\n\t"
            "v_exp_f32 v193, v193\n\t"
            "v_mfma_f32_16x16x32_bf16 v[224:227], v[204:207], v[176:179], 0\n\t"
            "v_exp_f32 v194, v194\n\t"
            "v_mfma_f32_16x16x32_bf16 v[224:227], v[208:211], v[172:175], v[224:227]\n\t"
            "v_exp_f32 v195, v195\n\t"
            "v_mfma_f32_16x16x32_bf16 v[224:227], v[212:215], v[168:171], v[224:227]\n\t"
            "v_exp_f32 v220, v220\n\t"
            "v_mfma_f32_16x16x16_bf16 v[196:199], v[152:153], v[178:179], 0\n\t"
            "v_cvt_pk_bf16_f32 v192, v192, v193\n\t"
            "v_cvt_pk_bf16_f32 v193, v194, v195\n\t"
            "v_cvt_pk_bf16_f32 v220, v220, 0\n\t"
            "v_mfma_f32_16x16x32_bf16 v[204:207], v[240:243], v[180:183], 0\n\t"
            "v_cndmask_b32_e64 v194, 0, v220, s[58:59]\n\t"
            "v_mov_b32 v195, 0\n\t"
            "v_mfma_f32_16x16x32_bf16 v[204:207], v[240:243], v[184:187], v[204:207]\n\t"
            "v_cndmask_b32_e64 v235, v217, v221, s[60:61]\n\t"
            "v_cndmask_b32_e64 v235, v235, v225, s[62:63]\n\t"
            "v_mfma_f32_16x16x32_bf16 v[200:203], v[168:171], v[172:175], v[200:203]\n\t"
            "v_cndmask_b32_e64 v235, v235, v239, s[64:65]\n\t"
            "v_cmp_nlt_f32_e64 s[24:25], |v235|, s23\n\t"
            "v_mfma_f32_16x16x32_bf16 v[208:211], v[240:243], v[188:191], 0\n\t"
            "v_exp_f32 v235, v235\n\t"
            "v_mfma_f32_16x16x32_bf16 v[208:211], v[240:243], v[192:195], v[208:211]\n\t"
            "v_exp_f32 v224, v224\n\t"
            "v_mfma_f32_16x16x32_bf16 v[196:199], v[144:147], v[172:175], v[196:199]\n\t"
            "s_nop 0\n\t"
            "v_exp_f32 v200, v200\n\t"
            "v_mfma_f32_16x16x32_bf16 v[228:231], v[156:159], v[172:175], v[228:231]\n\t"
            "v_exp_f32 v201, v201\n\t"
            "v_exp_f32 v202, v202\n\t"
            "v_exp_f32 v203, v203\n\t"
            "v_cvt_pk_bf16_f32 v235, v235, 0\n\t"
            "v_cvt_pk_bf16_f32 v200, v200, v201\n\t"
            "v_cndmask_b32_e64 v235, 0, v235, s[66:67]\n\t"
            "v_exp_f32 v196, v196\n\t"
            "v_exp_f32 v197, v197\n\t"
            "v_exp_f32 v198, v198\n\t"
            "v_exp_f32 v199, v199\n\t"
            "v_exp_f32 v228, v228\n\t"
            "v_exp_f32 v229, v229\n\t"
            "v_exp_f32 v230, v230\n\t"
            "v_exp_f32 v231, v231\n\t"
            "v_cvt_pk_bf16_f32 v196, v196, v197\n\t"
            "v_cvt_pk_bf16_f32 v197, v198, v199\n\t"
            "v_cvt_pk_bf16_f32 v198, v228, v229\n\t"
            "v_cvt_pk_bf16_f32 v199, v230, v231\n\t"
            "v_cvt_pk_bf16_f32 v201, v202, v203\n\t"
            "v_cvt_pk_bf16_f32 v224, v224, 0\n\t"
            "v_mfma_f32_16x16x32_bf16 v[212:215], v[240:243], v[196:199], 0\n\t"
            "ds_write_b16 v250, v235 offset:256\n\t"
            "v_cndmask_b32_e64 v202, 0, v224, s[58:59]\n\t"
            "v_mov_b32 v203, 0\n\t"
            "v_add_u32 v205, 0xe0800000, v204\n\t"
            "v_add_u32 v209, 0xe0800000, v208\n\t"
            "v_mfma_f32_16x16x32_bf16 v[212:215], v[240:243], v[200:203], v[212:215]\n\t"
            "v_rcp_f32 v236, v204\n\t"
            "v_rcp_f32 v237, v208\n\t"
            "s_nop 5\n\t"
            "v_add_u32 v213, 0xe0800000, v212\n\t"
            "v_rcp_f32 v238, v212\n\t"
            "v_or3_b32 v205, v205, v209, v213\n\t"
            "v_cmp_le_u32 vcc, 2.0, v205\n\t"
            ".Lwa_in%=:\n\t"
            "s_or_b64 vcc, vcc, s[24:25]\n\t"
            "s_cbranch_scc1 .Lwa_slow%=\n\t"
            ".Lwa_ok%=:\n\t"
            "s_bfe_u32 s49, s70, 0x30001\n\t"
            "s_lshl_b32 s49, s49, 4\n\t"
            "s_or_b32 s73, s77, s49\n\t"
            "s_add_u32 s70, s70, 1\n\t"
            "s_add_u32 s72, s72, 800\n\t"
            "s_add_u32 s76, s76, 800\n\t"
            "s_add_u32 s77, s77, 384\n\t"
            "s_xor_b32 s48, s48, s47\n\t"
            "s_sub_u32 s44, s44, 1\n\t"
            "s_cmp_gt_u32 s44, 2\n\t"
            "s_barrier\n\t"
            "s_setprio 0\n\t"
            "s_cbranch_scc1 .Lwa_loop%=\n\t"
            "s_cmp_eq_u32 s44, 0\n\t"
            "s_cbranch_scc1 .Lwa_done%=\n\t"
            "s_cmp_lg_u32 s44, 2\n\t"
            "s_cbranch_scc1 .Lwa_loop%=\n\t"
            "s_mov_b32 m0, s40\n\t"
            "v_lshlrev_b32 v234, 3, v248\n\t"
            "buffer_load_dword v234, s[36:39], s41 offen lds\n\t"
            "s_branch .Lwa_loop%=\n\t"
            ".Lwa_slow%=:\n\t"
            "s_waitcnt lgkmcnt(0)\n\t"
            "s_nop 7\n\t"
            "v_mfma_f32_16x16x16_bf16 v[180:183], v[152:153], v[154:155], 0\n\t"
            "v_add_u32 v234, s72, v252\n\t"
            "v_cndmask_b32_e64 v235, v246, v234, s[50:51]\n\t"
            "v_cndmask_b32_e64 v234, v246, v234, s[26:27]\n\t"
            "v_mfma_f32_16x16x16_bf16 v[228:231], v[164:165], v[154:155], 0\n\t"
            "ds_read_b64 v[216:217], v235 offset:136\n\t"
            "ds_read_b64 v[218:219], v234 offset:128\n\t"
            "v_mfma_f32_16x16x16_bf16 v[184:187], v[176:177], v[154:155], 0\n\t"
            "ds_read_b128 v[220:223], v234\n\t"
            "ds_read_b128 v[224:227], v235 offset:64\n\t"
            "v_mfma_f32_16x16x32_bf16 v[180:183], v[144:147], v[148:151], v[180:183]\n\t"
            "v_mfma_f32_16x16x32_bf16 v[228:231], v[156:159], v[148:151], v[228:231]\n\t"
            "s_nop 1\n\t"
            "v_mfma_f32_16x16x32_bf16 v[184:187], v[168:171], v[148:151], v[184:187]\n\t"
            "v_mfma_f32_16x16x16_bf16 v[188:191], v[152:153], v[166:167], 0\n\t"
            "v_mfma_f32_16x16x16_bf16 v[192:195], v[176:177], v[166:167], 0\n\t"
            "v_mfma_f32_16x16x16_bf16 v[196:199], v[152:153], v[178:179], 0\n\t"
            "v_mfma_f32_16x16x16_bf16 v[200:203], v[176:177], v[178:179], 0\n\t"
            "s_waitcnt lgkmcnt(2)\n\t"
            "v_mfma_f32_16x16x32_bf16 v[204:207], v[216:219], v[152:155], 0\n\t"
            "s_waitcnt lgkmcnt(1)\n\t"
            "v_mfma_f32_16x16x32_bf16 v[204:207], v[220:223], v[148:151], v[204:207]\n\t"
            "s_waitcnt lgkmcnt(0)\n\t"
            "v_mfma_f32_16x16x32_bf16 v[204:207], v[224:227], v[144:147], v[204:207]\n\t"
            "v_mfma_f32_16x16x32_bf16 v[208:211], v[216:219], v[164:167], 0\n\t"
            "v_mfma_f32_16x16x32_bf16 v[208:211], v[220:223], v[160:163], v[208:211]\n\t"
            "v_mfma_f32_16x16x32_bf16 v[208:211], v[224:227], v[156:159], v[208:211]\n\t"
            "v_mfma_f32_16x16x32_bf16 v[188:191], v[144:147], v[160:163], v[188:191]\n\t"
            "v_mfma_f32_16x16x32_bf16 v[192:195], v[168:171], v[160:163], v[192:195]\n\t"
            "v_mfma_f32_16x16x32_bf16 v[212:215], v[216:219], v[176:179], 0\n\t"
            "s_nop 1\n\t"
            "v_max3_f32 v232, v204, v180, v181\n\t"
            "v_mfma_f32_16x16x32_bf16 v[212:215], v[220:223], v[172:175], v[212:215]\n\t"
            "v_max3_f32 v232, v232, v182, v183\n\t"
            "v_max3_f32 v232, v232, v228, v229\n\t"
            "v_mfma_f32_16x16x32_bf16 v[212:215], v[224:227], v[168:171], v[212:215]\n\t"
            "v_max3_f32 v232, v232, v230, v231\n\t"
            "v_max3_f32 v232, v232, v184, v185\n\t"
            "v_mfma_f32_16x16x32_bf16 v[196:199], v[144:147], v[172:175], v[196:199]\n\t"
            "v_max3_f32 v232, v232, v186, v187\n\t"
            "v_mov_b32 v233, v232\n\t"
            "v_mfma_f32_16x16x32_bf16 v[200:203], v[168:171], v[172:175], v[200:203]\n\t"
            "s_nop 0\n\t"
            "v_permlane32_swap_b32 v232, v233\n\t"
            "v_max_f32 v232, v232, v233\n\t"
            "v_mov_b32 v233, v232\n\t"
            "v_max3_f32 v234, v205, v209, v213\n\t"
            "v_max_f32 v234, v234, v239\n\t"
            "v_permlane16_swap_b32 v232, v233\n\t"
            "v_max_f32 v232, v232, v233\n\t"
            "v_sub_f32 v180, v180, v232\n\t"
            "v_sub_f32 v181, v181, v232\n\t"
            "v_exp_f32 v180, v180\n\t"
            "v_exp_f32 v181, v181\n\t"
            "v_sub_f32 v182, v182, v232\n\t"
            "v_sub_f32 v183, v183, v232\n\t"
            "v_exp_f32 v182, v182\n\t"
            "v_exp_f32 v183, v183\n\t"
            "v_sub_f32 v228, v228, v232\n\t"
            "v_sub_f32 v229, v229, v232\n\t"
            "v_sub_f32 v230, v230, v232\n\t"
            "v_sub_f32 v231, v231, v232\n\t"
            "v_exp_f32 v228, v228\n\t"
            "v_exp_f32 v229, v229\n\t"
            "v_exp_f32 v230, v230\n\t"
            "v_exp_f32 v231, v231\n\t"
            "v_cvt_pk_bf16_f32 v180, v180, v181\n\t"
            "v_cvt_pk_bf16_f32 v181, v182, v183\n\t"
            "v_cvt_pk_bf16_f32 v182, v228, v229\n\t"
            "v_cvt_pk_bf16_f32 v183, v230, v231\n\t"
            "v_mfma_f32_16x16x16_bf16 v[228:231], v[164:165], v[166:167], 0\n\t"
            "v_sub_f32 v184, v184, v232\n\t"
            "v_sub_f32 v185, v185, v232\n\t"
            "v_sub_f32 v186, v186, v232\n\t"
            "v_sub_f32 v187, v187, v232\n\t"
            "v_sub_f32 v204, v204, v232\n\t"
            "v_max3_f32 v232, v208, v188, v189\n\t"
            "v_max3_f32 v232, v232, v190, v191\n\t"
            "v_mfma_f32_16x16x32_bf16 v[228:231], v[156:159], v[160:163], v[228:231]\n\t"
            "v_max_f32_dpp v234, v234, v234 row_ror:8 row_mask:0xf bank_mask:0xf\n\t"
            "v_cndmask_b32_e64 v235, v205, v209, s[60:61]\n\t"
            "v_cndmask_b32_e64 v235, v235, v213, s[62:63]\n\t"
            "v_max_f32_dpp v234, v234, v234 row_ror:4 row_mask:0xf bank_mask:0xf\n\t"
            "v_cndmask_b32_e64 v235, v235, v239, s[64:65]\n\t"
            "v_exp_f32 v184, v184\n\t"
            "v_max_f32_dpp v234, v234, v234 row_ror:2 row_mask:0xf bank_mask:0xf\n\t"
            "v_exp_f32 v185, v185\n\t"
            "v_max3_f32 v232, v232, v228, v229\n\t"
            "v_max3_f32 v232, v232, v230, v231\n\t"
            "v_max3_f32 v232, v232, v192, v193\n\t"
            "v_max3_f32 v232, v232, v194, v195\n\t"
            "v_mov_b32 v233, v232\n\t"
            "v_max_f32_dpp v234, v234, v234 row_ror:1 row_mask:0xf bank_mask:0xf\n\t"
            "v_sub_f32 v235, v235, v234\n\t"
            "v_permlane32_swap_b32 v232, v233\n\t"
            "v_max_f32 v232, v232, v233\n\t"
            "v_mov_b32 v233, v232\n\t"
            "v_exp_f32 v235, v235\n\t"
            "v_exp_f32 v186, v186\n\t"
            "v_permlane16_swap_b32 v232, v233\n\t"
            "v_max_f32 v232, v232, v233\n\t"
            "v_sub_f32 v188, v188, v232\n\t"
            "v_sub_f32 v189, v189, v232\n\t"
            "v_exp_f32 v188, v188\n\t"
            "v_exp_f32 v189, v189\n\t"
            "v_sub_f32 v190, v190, v232\n\t"
            "v_sub_f32 v191, v191, v232\n\t"
            "v_exp_f32 v190, v190\n\t"
            "v_exp_f32 v191, v191\n\t"
            "v_sub_f32 v228, v228, v232\n\t"
            "v_sub_f32 v229, v229, v232\n\t"
            "v_sub_f32 v230, v230, v232\n\t"
            "v_sub_f32 v231, v231, v232\n\t"
            "v_exp_f32 v228, v228\n\t"
            "v_exp_f32 v229, v229\n\t"
            "v_exp_f32 v230, v230\n\t"
            "v_exp_f32 v231, v231\n\t"
            "v_cvt_pk_bf16_f32 v188, v188, v189\n\t"
            "v_cvt_pk_bf16_f32 v189, v190, v191\n\t"
            "v_cvt_pk_bf16_f32 v190, v228, v229\n\t"
            "v_cvt_pk_bf16_f32 v191, v230, v231\n\t"
            "v_mfma_f32_16x16x16_bf16 v[228:231], v[164:165], v[178:179], 0\n\t"
            "v_sub_f32 v192, v192, v232\n\t"
            "v_sub_f32 v193, v193, v232\n\t"
            "v_sub_f32 v194, v194, v232\n\t"
            "v_sub_f32 v195, v195, v232\n\t"
            "v_sub_f32 v208, v208, v232\n\t"
            "v_max3_f32 v232, v212, v196, v197\n\t"
            "v_max3_f32 v232, v232, v198, v199\n\t"
            "v_mfma_f32_16x16x32_bf16 v[228:231], v[156:159], v[172:175], v[228:231]\n\t"
            "v_exp_f32 v192, v192\n\t"
            "v_exp_f32 v193, v193\n\t"
            "v_exp_f32 v187, v187\n\t"
            "v_exp_f32 v204, v204\n\t"
            "v_exp_f32 v194, v194\n\t"
            "v_exp_f32 v195, v195\n\t"
            "v_exp_f32 v208, v208\n\t"
            "v_cvt_pk_bf16_f32 v184, v184, v185\n\t"
            "v_max3_f32 v232, v232, v228, v229\n\t"
            "v_max3_f32 v232, v232, v230, v231\n\t"
            "v_max3_f32 v232, v232, v200, v201\n\t"
            "v_max3_f32 v232, v232, v202, v203\n\t"
            "v_mov_b32 v233, v232\n\t"
            "v_cvt_pk_bf16_f32 v192, v192, v193\n\t"
            "v_cvt_pk_bf16_f32 v235, v235, 0\n\t"
            "v_permlane32_swap_b32 v232, v233\n\t"
            "v_max_f32 v232, v232, v233\n\t"
            "v_mov_b32 v233, v232\n\t"
            "v_cvt_pk_bf16_f32 v185, v186, v187\n\t"
            "v_cvt_pk_bf16_f32 v204, v204, 0\n\t"
            "v_permlane16_swap_b32 v232, v233\n\t"
            "v_max_f32 v232, v232, v233\n\t"
            "v_sub_f32 v196, v196, v232\n\t"
            "v_sub_f32 v197, v197, v232\n\t"
            "v_exp_f32 v196, v196\n\t"
            "v_exp_f32 v197, v197\n\t"
            "v_sub_f32 v198, v198, v232\n\t"
            "v_sub_f32 v199, v199, v232\n\t"
            "v_exp_f32 v198, v198\n\t"
            "v_exp_f32 v199, v199\n\t"
            "v_sub_f32 v228, v228, v232\n\t"
            "v_sub_f32 v229, v229, v232\n\t"
            "v_sub_f32 v230, v230, v232\n\t"
            "v_sub_f32 v231, v231, v232\n\t"
            "v_sub_f32 v200, v200, v232\n\t"
            "v_sub_f32 v201, v201, v232\n\t"
            "v_exp_f32 v228, v228\n\t"
            "v_exp_f32 v229, v229\n\t"
            "v_exp_f32 v230, v230\n\t"
            "v_exp_f32 v231, v231\n\t"
            "v_exp_f32 v200, v200\n\t"
            "v_exp_f32 v201, v201\n\t"
            "v_sub_f32 v202, v202, v232\n\t"
            "v_sub_f32 v203, v203, v232\n\t"
            "v_sub_f32 v212, v212, v232\n\t"
            "v_cvt_pk_bf16_f32 v196, v196, v197\n\t"
            "v_exp_f32 v202, v202\n\t"
            "v_exp_f32 v203, v203\n\t"
            "v_exp_f32 v212, v212\n\t"
            "v_cvt_pk_bf16_f32 v197, v198, v199\n\t"
            "v_cvt_pk_bf16_f32 v198, v228, v229\n\t"
            "v_cvt_pk_bf16_f32 v199, v230, v231\n\t"
            "v_cvt_pk_bf16_f32 v200, v200, v201\n\t"
            "v_cvt_pk_bf16_f32 v193, v194, v195\n\t"
            "v_cvt_pk_bf16_f32 v208, v208, 0\n\t"
            "v_cvt_pk_bf16_f32 v201, v202, v203\n\t"
            "v_cvt_pk_bf16_f32 v212, v212, 0\n\t"
            "v_cndmask_b32_e64 v235, 0, v235, s[66:67]\n\t"
            "v_cndmask_b32_e64 v186, 0, v204, s[58:59]\n\t"
            "v_mfma_f32_16x16x32_bf16 v[204:207], v[240:243], v[180:183], 0\n\t"
            "v_cndmask_b32_e64 v194, 0, v208, s[58:59]\n\t"
            "v_cndmask_b32_e64 v202, 0, v212, s[58:59]\n\t"
            "v_mfma_f32_16x16x32_bf16 v[208:211], v[240:243], v[188:191], 0\n\t"
            "ds_write_b16 v250, v235 offset:256\n\t"
            "v_mfma_f32_16x16x32_bf16 v[212:215], v[240:243], v[196:199], 0\n\t"
            "v_mov_b32 v187, 0\n\t"
            "v_mov_b32 v195, 0\n\t"
            "v_mov_b32 v203, 0\n\t"
            "v_mfma_f32_16x16x32_bf16 v[204:207], v[240:243], v[184:187], v[204:207]\n\t"
            "v_mfma_f32_16x16x32_bf16 v[208:211], v[240:243], v[192:195], v[208:211]\n\t"
            "v_mfma_f32_16x16x32_bf16 v[212:215], v[240:243], v[200:203], v[212:215]\n\t"
            "s_nop 5\n\t"
            "v_rcp_f32 v236, v204\n\t"
            "v_rcp_f32 v237, v208\n\t"
            "v_rcp_f32 v238, v212\n\t"
            "s_waitcnt lgkmcnt(0)\n\t"
            "s_nop 7\n\t"
            "s_branch .Lwa_ok%=\n\t"
            ".Lwa_done%=:\n\t"
            "s_setprio 2\n\t"
            "s_waitcnt lgkmcnt(0)\n\t"
            "s_nop 7\n\t"
            "ds_read_b64 v[156:157], v247\n\t"
            "v_add_u32 v235, s45, v248\n\t"
            "v_xor_b32 v234, s73, v254\n\t"
            "v_mov_b32 v159, 0\n\t"
            "v_mov_b32 v163, 0\n\t"
            "v_mov_b32 v167, 0\n\t"
            "ds_read_b128 v[168:171], v235\n\t"
            "ds_read_u16 v158, v234\n\t"
            "ds_read_b128 v[172:175], v235 offset:1536\n\t"
            "ds_read_b128 v[176:179], v235 offset:3072\n\t"
            "ds_read_b64 v[160:161], v247 offset:3072\n\t"
            "ds_read_u16 v162, v234 offset:256\n\t"
            "ds_read_b64 v[164:165], v247 offset:1536\n\t"
            "ds_read_u16 v166, v234 offset:128\n\t"
            "s_waitcnt lgkmcnt(7)\n\t"
            "v_mfma_f32_16x16x32_bf16 v[228:231], v[168:171], v[180:183], 0\n\t"
            "s_waitcnt lgkmcnt(6)\n\t"
            "v_mfma_f32_16x16x32_bf16 v[228:231], v[156:159], v[184:187], v[228:231]\n\t"
            "s_nop 7\n\t"
            "v_mul_f32 v228, v236, v228\n\t"
            "v_mul_f32 v229, v236, v229\n\t"
            "v_mul_f32 v230, v236, v230\n\t"
            "v_mul_f32 v231, v236, v231\n\t"
            "v_cvt_pk_bf16_f32 v228, v228, v229\n\t"
            "v_cvt_pk_bf16_f32 v229, v230, v231\n\t"
            "ds_write_b64 v245, v[228:229]\n\t"
            "s_waitcnt lgkmcnt(6)\n\t"
            "v_mfma_f32_16x16x32_bf16 v[228:231], v[172:175], v[180:183], 0\n\t"
            "s_waitcnt lgkmcnt(5)\n\t"
            "v_mfma_f32_16x16x32_bf16 v[180:183], v[176:179], v[180:183], 0\n\t"
            "s_waitcnt lgkmcnt(3)\n\t"
            "v_mfma_f32_16x16x32_bf16 v[180:183], v[160:163], v[184:187], v[180:183]\n\t"
            "s_waitcnt lgkmcnt(1)\n\t"
            "v_mfma_f32_16x16x32_bf16 v[228:231], v[164:167], v[184:187], v[228:231]\n\t"
            "v_mfma_f32_16x16x32_bf16 v[184:187], v[168:171], v[188:191], 0\n\t"
            "v_mfma_f32_16x16x32_bf16 v[184:187], v[156:159], v[192:195], v[184:187]\n\t"
            "s_nop 3\n\t"
            "v_mul_f32 v180, v236, v180\n\t"
            "v_mul_f32 v181, v236, v181\n\t"
            "v_mul_f32 v182, v236, v182\n\t"
            "v_mul_f32 v183, v236, v183\n\t"
            "v_cvt_pk_bf16_f32 v180, v180, v181\n\t"
            "v_cvt_pk_bf16_f32 v181, v182, v183\n\t"
            "v_mul_f32 v228, v236, v228\n\t"
            "v_mul_f32 v229, v236, v229\n\t"
            "v_mul_f32 v230, v236, v230\n\t"
            "v_mul_f32 v231, v236, v231\n\t"
            "v_cvt_pk_bf16_f32 v228, v228, v229\n\t"
            "v_cvt_pk_bf16_f32 v229, v230, v231\n\t"
            "v_mul_f32 v184, v237, v184\n\t"
            "v_mul_f32 v185, v237, v185\n\t"
            "v_mul_f32 v186, v237, v186\n\t"
            "v_mul_f32 v187, v237, v187\n\t"
            "v_cvt_pk_bf16_f32 v184, v184, v185\n\t"
            "v_cvt_pk_bf16_f32 v185, v186, v187\n\t"
            "ds_read_b64 v[186:187], v253 offset:352\n\t"
            "ds_write_b64 v245, v[180:181] offset:256\n\t"
            "v_mfma_f32_16x16x32_bf16 v[180:183], v[168:171], v[196:199], 0\n\t"
            "ds_write_b64 v245, v[228:229] offset:128\n\t"
            "v_mfma_f32_16x16x32_bf16 v[180:183], v[156:159], v[200:203], v[180:183]\n\t"
            "ds_write_b64 v245, v[184:185] offset:6144\n\t"
            "v_mfma_f32_16x16x32_bf16 v[228:231], v[172:175], v[188:191], 0\n\t"
            "ds_read_b64 v[184:185], v253 offset:320\n\t"
            "v_mfma_f32_16x16x32_bf16 v[228:231], v[164:167], v[192:195], v[228:231]\n\t"
            "v_mfma_f32_16x16x32_bf16 v[188:191], v[176:179], v[188:191], 0\n\t"
            "v_mfma_f32_16x16x32_bf16 v[188:191], v[160:163], v[192:195], v[188:191]\n\t"
            "s_nop 1\n\t"
            "v_mul_f32 v180, v238, v180\n\t"
            "v_mul_f32 v181, v238, v181\n\t"
            "v_mul_f32 v182, v238, v182\n\t"
            "v_mul_f32 v183, v238, v183\n\t"
            "v_cvt_pk_bf16_f32 v180, v180, v181\n\t"
            "v_cvt_pk_bf16_f32 v181, v182, v183\n\t"
            "v_mul_f32 v228, v237, v228\n\t"
            "v_mul_f32 v229, v237, v229\n\t"
            "v_mul_f32 v230, v237, v230\n\t"
            "v_mul_f32 v231, v237, v231\n\t"
            "v_cvt_pk_bf16_f32 v228, v228, v229\n\t"
            "v_cvt_pk_bf16_f32 v229, v230, v231\n\t"
            "v_mul_f32 v188, v237, v188\n\t"
            "v_mul_f32 v189, v237, v189\n\t"
            "v_mul_f32 v190, v237, v190\n\t"
            "v_mul_f32 v191, v237, v191\n\t"
            "v_cvt_pk_bf16_f32 v188, v188, v189\n\t"
            "v_cvt_pk_bf16_f32 v189, v190, v191\n\t"
            "ds_read_b64 v[182:183], v253 offset:288\n\t"
            "ds_write_b64 v245, v[180:181] offset:12288\n\t"
            "ds_read_b64 v[180:181], v253 offset:256\n\t"
            "ds_write_b64 v245, v[228:229] offset:6272\n\t"
            "v_mfma_f32_16x16x32_bf16 v[228:231], v[172:175], v[196:199], 0\n\t"
            "ds_write_b64 v245, v[188:189] offset:6400\n\t"
            "v_mfma_f32_16x16x32_bf16 v[228:231], v[164:167], v[200:203], v[228:231]\n\t"
            "v_mfma_f32_16x16x32_bf16 v[196:199], v[176:179], v[196:199], 0\n\t"
            "v_mfma_f32_16x16x32_bf16 v[196:199], v[160:163], v[200:203], v[196:199]\n\t"
            "s_nop 5\n\t"
            "v_mul_f32 v228, v238, v228\n\t"
            "s_waitcnt lgkmcnt(2)\n\t"
            "v_mfma_f32_16x16x32_bf16 v[168:171], v[168:171], v[180:183], 0\n\t"
            "v_mfma_f32_16x16x32_bf16 v[172:175], v[172:175], v[180:183], 0\n\t"
            "v_mfma_f32_16x16x32_bf16 v[176:179], v[176:179], v[180:183], 0\n\t"
            "v_mul_f32 v229, v238, v229\n\t"
            "v_mfma_f32_16x16x32_bf16 v[168:171], v[156:159], v[184:187], v[168:171]\n\t"
            "v_mul_f32 v230, v238, v230\n\t"
            "v_mul_f32 v231, v238, v231\n\t"
            "v_mfma_f32_16x16x32_bf16 v[172:175], v[164:167], v[184:187], v[172:175]\n\t"
            "v_cvt_pk_bf16_f32 v228, v228, v229\n\t"
            "v_cvt_pk_bf16_f32 v229, v230, v231\n\t"
            "v_mfma_f32_16x16x32_bf16 v[176:179], v[160:163], v[184:187], v[176:179]\n\t"
            "ds_write_b64 v245, v[228:229] offset:12416\n\t"
            "v_mfma_f32_16x16x32_bf16 v[228:231], v[240:243], v[180:183], 0\n\t"
            "v_mul_f32 v196, v238, v196\n\t"
            "v_mul_f32 v197, v238, v197\n\t"
            "v_mfma_f32_16x16x32_bf16 v[228:231], v[240:243], v[184:187], v[228:231]\n\t"
            "v_mul_f32 v198, v238, v198\n\t"
            "v_mul_f32 v199, v238, v199\n\t"
            "v_cvt_pk_bf16_f32 v196, v196, v197\n\t"
            "v_cvt_pk_bf16_f32 v197, v198, v199\n\t"
            "ds_write_b64 v245, v[196:197] offset:12544\n\t"
            "s_nop 2\n\t"
            "v_rcp_f32 v229, v228\n\t"
            "s_nop 0\n\t"
            "v_mul_f32 v168, v229, v168\n\t"
            "v_mul_f32 v169, v229, v169\n\t"
            "v_mul_f32 v172, v229, v172\n\t"
            "v_mul_f32 v173, v229, v173\n\t"
            "v_mul_f32 v176, v229, v176\n\t"
            "v_mul_f32 v177, v229, v177\n\t"
            "v_mul_f32 v170, v229, v170\n\t"
            "v_mul_f32 v171, v229, v171\n\t"
            "v_cvt_pk_bf16_f32 v168, v168, v169\n\t"
            "v_mul_f32 v174, v229, v174\n\t"
            "v_mul_f32 v175, v229, v175\n\t"
            "v_cvt_pk_bf16_f32 v172, v172, v173\n\t"
            "v_mul_f32 v178, v229, v178\n\t"
            "v_mul_f32 v179, v229, v179\n\t"
            "v_cvt_pk_bf16_f32 v176, v176, v177\n\t"
            "v_xor_b32 v230, s73, v255\n\t"
            "v_cvt_pk_bf16_f32 v169, v170, v171\n\t"
            "v_cvt_pk_bf16_f32 v173, v174, v175\n\t"
            "v_cvt_pk_bf16_f32 v177, v178, v179\n\t"
            "s_mov_b64 exec, s[68:69]\n\t"
            "ds_write_b64 v230, v[168:169]\n\t"
            "ds_write_b64 v230, v[172:173] offset:128\n\t"
            "ds_write_b64 v230, v[176:177] offset:256\n\t"
            "s_mov_b64 exec, -1\n\t"
            "s_waitcnt lgkmcnt(0)\n\t"
            "v_mov_b32 v144, s42\n\t"
            "v_bfrev_b32 v145, -2\n\t"
            "ds_write_b32 v144, v145\n\t"
            "s_waitcnt lgkmcnt(0)\n\t"
            "s_nop 7\n\t"
            : "+{v[232:235]}"(pl), "+{v249}"(aRSw_),
              "+{s44}"(s44), "+{s48}"(s48), "+{s70}"(s70), "+{s72}"(s72), "+{s73}"(s73), "+{s76}"(s76), "+{s77}"(s77)
            : "{v244}"(pE_), "{v245}"(aO_), "{v246}"(aZ_), "{v247}"(aVB_), "{v248}"(pST_), "{v250}"(aPLw_), "{v251}"(aRSr_), "{v252}"(g16_), "{v253}"(aPLr_), "{v254}"(loVL_), "{v255}"(loOL_), "{v[240:243]}"(ones4), "{s47}"((ldsb + X_OFF) ^ (ldsb + X1_OFF)),
              "{s[58:59]}"(0xFFFFull), "{s[60:61]}"(0xFFFF0000ull), "{s[62:63]}"(0xFFFF00000000ull), "{s[64:65]}"(0xFFFF000000000000ull),
              "{s[66:67]}"(0x1FFFFFFFFFFFFull), "{s[68:69]}"(0x0001000100010001ull), "{s[26:27]}"(0x1111111111111111ull), "{s23}"(64.f), "{s57}"(RS_OFF + 768 + (4 + (w >> 1)) * 192 - RS_BASE), "{s45}"(ldsb + V_OFF + w * 4608),
              "{s[28:31]}"(wr), "{s33}"(wave * 36864), "{s46}"(ldsb + PG_OFF), "{s42}"(ldsb + PG_OFF + w * 4), "{s56}"(ldsb + O_OFF), "{s[36:39]}"(tr), "{s41}"(to), "{s40}"(ldsb + X_OFF + 18432), "{s34}"(SH_OFF + bx * 12288 + w * 3072), "{s35}"(ldsb + O_OFF + w * 3072),
              "{s[50:51]}"(0x2222222222222222ull), "{s[52:53]}"(0x4444444444444444ull), "{s[54:55]}"(0x8888888888888888ull)
            : "s24", "s25", "v0", "v1", "v2", "v3", "v4", "v5", "v6", "v7", "v8", "v9", "v10", "v11", "v12", "v13", "v14", "v15", "v16", "v17", "v18", "v19", "v20", "v21", "v22", "v23", "v24", "v25", "v26", "v27", "v28", "v29", "v30", "v31", "v32", "v33", "v34", "v35", "v36", "v37", "v38", "v39", "v40", "v41", "v42", "v43", "v44", "v45", "v46", "v47", "v48", "v49", "v50", "v51", "v52", "v53", "v54", "v55", "v56", "v57", "v58", "v59", "v60", "v61", "v62", "v63", "v64", "v65", "v66", "v67", "v68", "v69", "v70", "v71", "v72", "v73", "v74", "v75", "v76", "v77", "v78", "v79", "v80", "v81", "v82", "v83", "v84", "v85", "v86", "v87", "v88", "v89", "v90", "v91", "v92", "v93", "v94", "v95", "v96", "v97", "v98", "v99", "v100", "v101", "v102", "v103", "v104", "v105", "v106", "v107", "v108", "v109", "v110", "v111", "v112", "v113", "v114", "v115", "v116", "v117", "v118", "v119", "v120", "v121", "v122", "v123", "v124", "v125", "v126", "v127", "v128", "v129", "v130", "v131", "v132", "v133", "v134", "v135", "v136", "v137", "v138", "v139", "v140", "v141", "v142", "v143", "v144", "v145", "v146", "v147", "v148", "v149", "v150", "v151", "v152", "v153", "v154", "v155", "v156", "v157", "v158", "v159", "v160", "v161", "v162", "v163", "v164", "v165", "v166", "v167", "v168", "v169", "v170", "v171", "v172", "v173", "v174", "v175", "v176", "v177", "v178", "v179", "v180", "v181", "v182", "v183", "v184", "v185", "v186", "v187", "v188", "v189", "v190", "v191", "v192", "v193", "v194", "v195", "v196", "v197", "v198", "v199", "v200", "v201", "v202", "v203", "v204", "v205", "v206", "v207", "v208", "v209", "v210", "v211", "v212", "v213", "v214", "v215", "v216", "v217", "v218", "v219", "v220", "v221", "v222", "v223", "v224", "v225", "v226", "v227", "v228", "v229", "v230", "v231", "v236", "v237", "v238", "v239", "s49", "vcc", "scc", "m0", "memory");
        }
        asm volatile("wk_code_end:");

        if (q0 == 40 && wave == 0) {
#pragma unroll 1
            for (int it = 0; it < SPIN_MAX && __hip_atomic_load((gu32*)((char*)wf + CN_OFF), RLX_AGENT) != (unsigned)GRID; ++it) __builtin_amdgcn_s_sleep(8);
            __hip_atomic_store((gu32*)((char*)wf + EP_OFF), __hip_atomic_load((gu32*)((char*)wf + EP_OFF), RLX_AGENT) + 1u, RLX_AGENT);
        }
    } else {
        ROLE_SETUP
        {
            LANE_SETUP
            const bool isV = wave < 6;
            CONV_CONSTS(0)
            const unsigned aCA_ = ldsb + CT_OFF + lane * 16, rsb = ldsb + RS_BASE, pE = fE - X_OFF;
            const unsigned aRSw = (lane < 48 ? rsb : ldsb + RS_OFF + 2304 - 192) + lane * 4, aRSr = rsb + (lane & 48);
            unsigned pr[3];
#pragma unroll
            for (int k = 0; k < 3; ++k) {
                const int c8 = 12 * (w - 2) + 4 * k + g;
                const int ws = c8 / 6, r6 = c8 - 6 * ws;
                pr[k] = l15 * 384 + (r6 >> 1) * 128 + (((2 * ws + (r6 & 1)) ^ sw) << 4);
            }
            unsigned r242 = isV ? pE : ldsb + O_OFF + pE, r243 = isV ? ldsb + V_OFF + w * 9216 + lane * 16 : (ldsb + O_OFF + pE) ^ 64;
            u32x4 r244 = {isV ? ldsb + V_OFF + w * 9216 + 1024 + lane * 8 : pr[0], isV ? aRSr : pr[1], isV ? aRSw : pr[2], (unsigned)lane * 16};
            const int hd = isV ? 2 * w : 2 * w - 3;
            const unsigned vq = ldsb + OL_OFF + l15 * 384 + (((2 * hd + (g >> 1)) ^ sw) << 4) + 8 * (g & 1);
            u32x4 f1 = {16 + l15 < cnt ? vq + 6144 : ldsb + OL_OFF + 22 * 384 + g * 8, pE, ldsb + V_OFF + hd * 4608 + lane * 16, ldsb + V_OFF + hd * 4608 + 1024 + lane * 8};
            u32x4 f2 = {aRSw, aRSr, rsb + l15 * 4, 0u};
            const unsigned lB = ldsb + LB_OFF(l15 & 7) + (g >> 1) * 1536 + (g & 1) * 768 + 96 * w;
            const unsigned long long ob = (unsigned long long)out, xb = (unsigned long long)((const char*)x - XBIAS), wb = (unsigned long long)wf;
            const u32x4 orr = {(unsigned)ob, (unsigned)(ob >> 32) & 0xffffu, (unsigned)(IMG * IMG * CH * 2), 0x00020000u};
            const u32x4 xr = {(unsigned)xb, (unsigned)(xb >> 32) & 0xffffu, (unsigned)(IMG * IMG * CH * 2 + XBIAS), 0x00020000u};
            const u32x4 wr = {(unsigned)wb, (unsigned)(wb >> 32) & 0xffffu, (unsigned)WS_BYTES, 0x00020000u};
            unsigned s45 = OOB, s46 = OOB, s47 = (en0 & 0x7ffffffu) + 96 * w;
            unsigned s48 = ldsb + X1_OFF, s67 = ldsb + X_OFF, s76 = ldsb + WL_OFF + 4, s56 = en0 >> 30;
            unsigned s21 = SH_OFF + bx * 12288, s44 = ldsb + PG_OFF + w * 4, s70 = isV ? wave * 36864 : (wave - 2) * 36864 + 18432;
            unsigned long long xp = 0, sp = 0;
#pragma unroll
            for (int v = 0; v < 4; ++v) {
                const unsigned r = (CROW >> (8 * v)) & 0xFF;
                xp |= (unsigned long long)XPAT(r) << (16 * v);
                sp |= (unsigned long long)SPAT(r) << (16 * v);
            }
#define B_VIN "{v250}"(pXw_), "{v251}"(pSTw_), "{v252}"(aCA_), "{v253}"(aB_), "{v254}"(aA_), "{v255}"(idx_), "{v[238:241]}"(sel4)
#define B_SIN "{s[40:43]}"(orr), "{s[72:75]}"(xr), "{s39}"(0x401fffffu), "{s[28:31]}"(wr), "{s33}"(wave * 36864), "{s78}"(96 * w), \
              "{s27}"(96 * w - 384), "{s[50:51]}"(xp), "{s[52:53]}"(sp), "{s54}"(CLANE), "{s55}"(CROW & 0x0F0F0F0Fu), "{s[34:35]}"(0x0001000100010001ull), \
              "{s[60:61]}"(0x2222222222222222ull), "{s[62:63]}"(0x4444444444444444ull), "{s[64:65]}"(0x8888888888888888ull), \
              "{s77}"(w == 2 ? 768 : 6 * ROWBYTES + 768), "{s25}"(w == 2 ? 3 * ROWBYTES : OOB), "{s32}"(ldsb + X_OFF + 18432), \
              "{s24}"(ldsb + WL_OFF + 12 + 4 * (cnt - 5)), "{s66}"(ldsb + w * 8192), "{s71}"((ldsb + X_OFF) ^ (ldsb + X1_OFF)), \
              "{s23}"(ldsb + WL_OFF), "{s22}"(ldsb + O_OFF), \
              "{s20}"(6 * ROWBYTES + 2304 + 192 * (w & 1)), "{s19}"(2048 + (w & 1) * 1024)
            asm volatile("" :: B_VIN, "{v242}"(r242), "{v243}"(r243), "{v[244:247]}"(r244), "{v[228:231]}"(f1), "{v[232:235]}"(f2), "v"(lB), "v"(vq));
            asm volatile("" :: B_SIN);
            asm volatile("s_waitcnt vmcnt(9)" ::: "memory");
            unsigned lm = *(const unsigned*)(smem + TP_OFF + 4096 + w * 256 + ((l15 & 7) * 4 + g) * 4);
            lm = l15 < 8 ? lm : OOB + 47;
            const int row = lm & 63;
            u32x4 f0 = {XPAT(row) + (unsigned)(O_OFF - X_OFF), SPAT(row) + (unsigned)(STL_OFF - X_OFF - 18816), lB, vq};
            asm volatile(
            "s_setprio 3\n\t"
            "s_waitcnt lgkmcnt(0)\n\t"
            "s_barrier\n\t"
            "s_nop 7\n\t"
            "ds_read_b128 v[168:171], v254\n\t"
            "v_mov_b64 v[176:177], 0\n\t"
            "v_mov_b64 v[178:179], 0\n\t"
            "v_mov_b64 v[180:181], 0\n\t"
            "v_mov_b64 v[182:183], 0\n\t"
            "v_mov_b64 v[184:185], 0\n\t"
            "v_mov_b64 v[186:187], 0\n\t"
            "v_mov_b64 v[188:189], 0\n\t"
            "v_mov_b64 v[190:191], 0\n\t"
            "v_add_u32 v236, s67, v224\n\t"
            "v_xor_b32 v237, 16, v236\n\t"
            "v_add_u32 v248, s67, v225\n\t"
            "ds_read_b64 v[144:145], v226\n\t"
            "ds_read_b64 v[146:147], v226 offset:384\n\t"
            "ds_read_b64 v[148:149], v226 offset:3072\n\t"
            "ds_read_b64 v[150:151], v226 offset:3456\n\t"
            "ds_read_b128 v[172:175], v254 offset:64\n\t"
            "ds_read_b64 v[152:153], v226 offset:8\n\t"
            "ds_read_b64 v[154:155], v226 offset:392\n\t"
            "ds_read_b64 v[156:157], v226 offset:3080\n\t"
            "ds_read_b64 v[158:159], v226 offset:3464\n\t"
            "ds_read_b64 v[160:161], v226 offset:16\n\t"
            "ds_read_b64 v[162:163], v226 offset:400\n\t"
            "ds_read_b64 v[164:165], v226 offset:3088\n\t"
            "ds_read_b64 v[166:167], v226 offset:3472\n\t"
            "s_waitcnt lgkmcnt(9)\n\t"
            "v_smfmac_f32_16x16x64_bf16 v[176:179], v[168:171], v[144:151], v255\n\t"
            "ds_read_b128 v[168:171], v254 offset:128\n\t"
            "ds_read_b64 v[144:145], v226 offset:24\n\t"
            "ds_read_b64 v[146:147], v226 offset:408\n\t"
            "ds_read_b64 v[148:149], v226 offset:3096\n\t"
            "s_waitcnt lgkmcnt(8)\n\t"
            "v_smfmac_f32_16x16x64_bf16 v[180:183], v[172:175], v[152:159], v255\n\t"
            "ds_read_b64 v[150:151], v226 offset:3480\n\t"
            "ds_read_b128 v[172:175], v254 offset:192\n\t"
            "ds_read_b64 v[152:153], v226 offset:32\n\t"
            "v_cvt_pk_bf16_f32 v176, v176, v177\n\t"
            "v_cvt_pk_bf16_f32 v177, v178, v179\n\t"
            "s_nop 1\n\t"
            "v_mfma_f32_4x4x4_16b_bf16 v[196:199], v[176:177], v[176:177], 0\n\t"
            "ds_read_b64 v[154:155], v226 offset:416\n\t"
            "ds_read_b64 v[156:157], v226 offset:3104\n\t"
            "v_cvt_pk_bf16_f32 v178, v180, v181\n\t"
            "v_cvt_pk_bf16_f32 v179, v182, v183\n\t"
            "v_mov_b64 v[180:181], 0\n\t"
            "v_mov_b64 v[182:183], 0\n\t"
            "v_mfma_f32_16x16x32_bf16 v[192:195], v[238:241], v[176:179], 0\n\t"
            "ds_read_b64 v[158:159], v226 offset:3488\n\t"
            "s_waitcnt lgkmcnt(9)\n\t"
            "v_smfmac_f32_16x16x64_bf16 v[184:187], v[168:171], v[160:167], v255\n\t"
            "ds_read_b128 v[168:171], v254 offset:256\n\t"
            "v_mfma_f32_4x4x4_16b_bf16 v[196:199], v[178:179], v[178:179], v[196:199]\n\t"
            "ds_read_b64 v[160:161], v226 offset:40\n\t"
            "ds_read_b64 v[162:163], v226 offset:424\n\t"
            "ds_read_b64 v[164:165], v226 offset:3112\n\t"
            "ds_read_b64 v[166:167], v226 offset:3496\n\t"
            "s_waitcnt lgkmcnt(9)\n\t"
            "v_smfmac_f32_16x16x64_bf16 v[188:191], v[172:175], v[144:151], v255\n\t"
            "ds_read_b128 v[172:175], v254 offset:320\n\t"
            "v_cvt_pk_bf16_f32 v184, v184, v185\n\t"
            "v_cvt_pk_bf16_f32 v185, v186, v187\n\t"
            "s_nop 1\n\t"
            "v_mfma_f32_4x4x4_16b_bf16 v[196:199], v[184:185], v[184:185], v[196:199]\n\t"
            "ds_read_b64 v[144:145], v226 offset:48\n\t"
            "ds_read_b64 v[146:147], v226 offset:432\n\t"
            "ds_read_b64 v[148:149], v226 offset:3120\n\t"
            "ds_read_b64 v[150:151], v226 offset:3504\n\t"
            "v_cvt_pk_bf16_f32 v186, v188, v189\n\t"
            "v_cvt_pk_bf16_f32 v187, v190, v191\n\t"
            "v_mov_b64 v[188:189], 0\n\t"
            "v_mov_b64 v[190:191], 0\n\t"
            "v_mfma_f32_16x16x32_bf16 v[192:195], v[238:241], v[184:187], v[192:195]\n\t"
            "ds_write_b128 v236, v[176:179]\n\t"
            "v_mfma_f32_4x4x4_16b_bf16 v[196:199], v[186:187], v[186:187], v[196:199]\n\t"
            "v_mov_b64 v[176:177], 0\n\t"
            "v_mov_b64 v[178:179], 0\n\t"
            "s_waitcnt lgkmcnt(10)\n\t"
            "s_nop 0\n\t"
            "v_smfmac_f32_16x16x64_bf16 v[176:179], v[168:171], v[152:159], v255\n\t"
            "ds_read_b128 v[168:171], v254 offset:384\n\t"
            "s_waitcnt lgkmcnt(6)\n\t"
            "v_smfmac_f32_16x16x64_bf16 v[180:183], v[172:175], v[160:167], v255\n\t"
            "ds_read_b64 v[152:153], v226 offset:56\n\t"
            "ds_read_b64 v[154:155], v226 offset:440\n\t"
            "ds_read_b64 v[156:157], v226 offset:3128\n\t"
            "ds_read_b64 v[158:159], v226 offset:3512\n\t"
            "ds_read_b128 v[172:175], v254 offset:448\n\t"
            "ds_read_b64 v[160:161], v226 offset:64\n\t"
            "v_cvt_pk_bf16_f32 v176, v176, v177\n\t"
            "v_cvt_pk_bf16_f32 v177, v178, v179\n\t"
            "v_cvt_pk_bf16_f32 v178, v180, v181\n\t"
            "v_cvt_pk_bf16_f32 v179, v182, v183\n\t"
            "v_mfma_f32_4x4x4_16b_bf16 v[196:199], v[176:177], v[176:177], v[196:199]\n\t"
            "ds_read_b64 v[162:163], v226 offset:448\n\t"
            "v_mfma_f32_16x16x32_bf16 v[192:195], v[238:241], v[176:179], v[192:195]\n\t"
            "ds_read_b64 v[164:165], v226 offset:3136\n\t"
            "v_mov_b64 v[180:181], 0\n\t"
            "v_mfma_f32_4x4x4_16b_bf16 v[196:199], v[178:179], v[178:179], v[196:199]\n\t"
            "ds_read_b64 v[166:167], v226 offset:3520\n\t"
            "v_mov_b64 v[182:183], 0\n\t"
            "s_waitcnt lgkmcnt(14)\n\t"
            "ds_write_b128 v237, v[184:187]\n\t"
            "v_mov_b64 v[184:185], 0\n\t"
            "v_mov_b64 v[186:187], 0\n\t"
            "s_waitcnt lgkmcnt(10)\n\t"
            "s_nop 0\n\t"
            "v_smfmac_f32_16x16x64_bf16 v[184:187], v[168:171], v[144:151], v255\n\t"
            "ds_read_b128 v[168:171], v254 offset:512\n\t"
            "s_waitcnt lgkmcnt(6)\n\t"
            "v_smfmac_f32_16x16x64_bf16 v[188:191], v[172:175], v[152:159], v255\n\t"
            "ds_read_b64 v[144:145], v226 offset:72\n\t"
            "ds_read_b64 v[146:147], v226 offset:456\n\t"
            "ds_read_b64 v[148:149], v226 offset:3144\n\t"
            "ds_read_b64 v[150:151], v226 offset:3528\n\t"
            "ds_read_b128 v[172:175], v254 offset:576\n\t"
            "ds_read_b64 v[152:153], v226 offset:80\n\t"
            "v_cvt_pk_bf16_f32 v184, v184, v185\n\t"
            "v_cvt_pk_bf16_f32 v185, v186, v187\n\t"
            "v_cvt_pk_bf16_f32 v186, v188, v189\n\t"
            "v_cvt_pk_bf16_f32 v187, v190, v191\n\t"
            "v_mfma_f32_4x4x4_16b_bf16 v[196:199], v[184:185], v[184:185], v[196:199]\n\t"
            "ds_read_b64 v[154:155], v226 offset:464\n\t"
            "v_mfma_f32_16x16x32_bf16 v[192:195], v[238:241], v[184:187], v[192:195]\n\t"
            "ds_read_b64 v[156:157], v226 offset:3152\n\t"
            "v_mov_b64 v[188:189], 0\n\t"
            "v_mfma_f32_4x4x4_16b_bf16 v[196:199], v[186:187], v[186:187], v[196:199]\n\t"
            "ds_read_b64 v[158:159], v226 offset:3536\n\t"
            "v_mov_b64 v[190:191], 0\n\t"
            "s_waitcnt lgkmcnt(14)\n\t"
            "ds_write_b128 v236, v[176:179] offset:128\n\t"
            "v_mov_b64 v[176:177], 0\n\t"
            "v_mov_b64 v[178:179], 0\n\t"
            "s_waitcnt lgkmcnt(10)\n\t"
            "s_nop 0\n\t"
            "v_smfmac_f32_16x16x64_bf16 v[176:179], v[168:171], v[160:167], v255\n\t"
            "ds_read_b128 v[168:171], v254 offset:640\n\t"
            "s_waitcnt lgkmcnt(6)\n\t"
            "v_smfmac_f32_16x16x64_bf16 v[180:183], v[172:175], v[144:151], v255\n\t"
            "ds_read_b64 v[160:161], v226 offset:88\n\t"
            "ds_read_b64 v[162:163], v226 offset:472\n\t"
            "ds_read_b64 v[164:165], v226 offset:3160\n\t"
            "ds_read_b64 v[166:167], v226 offset:3544\n\t"
            "ds_read_b128 v[172:175], v254 offset:704\n\t"
            "ds_write_b128 v237, v[184:187] offset:128\n\t"
            "v_mov_b64 v[184:185], 0\n\t"
            "v_mov_b64 v[186:187], 0\n\t"
            "v_cvt_pk_bf16_f32 v176, v176, v177\n\t"
            "v_cvt_pk_bf16_f32 v177, v178, v179\n\t"
            "v_cvt_pk_bf16_f32 v178, v180, v181\n\t"
            "v_cvt_pk_bf16_f32 v179, v182, v183\n\t"
            "v_mfma_f32_4x4x4_16b_bf16 v[196:199], v[176:177], v[176:177], v[196:199]\n\t"
            "ds_write_b128 v236, v[176:179] offset:256\n\t"
            "s_waitcnt lgkmcnt(7)\n\t"
            "v_smfmac_f32_16x16x64_bf16 v[184:187], v[168:171], v[152:159], v255\n\t"
            "v_mfma_f32_4x4x4_16b_bf16 v[196:199], v[178:179], v[178:179], v[196:199]\n\t"
            "v_mfma_f32_16x16x32_bf16 v[192:195], v[238:241], v[176:179], v[192:195]\n\t"
            "s_waitcnt lgkmcnt(2)\n\t"
            "v_smfmac_f32_16x16x64_bf16 v[188:191], v[172:175], v[160:167], v255\n\t"
            "s_nop 3\n\t"
            "v_cvt_pk_bf16_f32 v184, v184, v185\n\t"
            "v_cvt_pk_bf16_f32 v185, v186, v187\n\t"
            "s_nop 1\n\t"
            "v_mfma_f32_4x4x4_16b_bf16 v[196:199], v[184:185], v[184:185], v[196:199]\n\t"
            "v_cvt_pk_bf16_f32 v186, v188, v189\n\t"
            "v_cvt_pk_bf16_f32 v187, v190, v191\n\t"
            "s_nop 1\n\t"
            "v_mfma_f32_4x4x4_16b_bf16 v[196:199], v[186:187], v[186:187], v[196:199]\n\t"
            "ds_write_b128 v237, v[184:187] offset:256\n\t"
            "v_mfma_f32_16x16x32_bf16 v[192:195], v[238:241], v[184:187], v[192:195]\n\t"
            "s_nop 7\n\t"
            "v_cndmask_b32_e64 v193, v196, v197, s[60:61]\n\t"
            "v_cndmask_b32_e64 v193, v193, v198, s[62:63]\n\t"
            "v_cndmask_b32_e64 v193, v193, v199, s[64:65]\n\t"
            "ds_write_b64 v248, v[192:193] offset:18816\n\t"
            "s_waitcnt lgkmcnt(0)\n\t"
            "s_nop 7\n\t"
            "v_mov_b32 v144, s44\n\t"
            "v_mov_b32 v145, 0x0fffffff\n\t"
            "ds_write_b32 v144, v145\n\t"
            "s_nop 7\n\t"
            "ds_read_b128 v[168:171], v254\n\t"
            "s_waitcnt vmcnt(0)\n\t"
            "ds_read_b64 v[144:145], v253\n\t"
            "v_mov_b64 v[176:177], 0\n\t"
            "v_mov_b64 v[178:179], 0\n\t"
            "v_mov_b64 v[180:181], 0\n\t"
            "v_mov_b64 v[182:183], 0\n\t"
            "v_mov_b32 v249, s76\n\t"
            "v_mov_b64 v[184:185], 0\n\t"
            "v_mov_b64 v[186:187], 0\n\t"
            "v_mov_b64 v[188:189], 0\n\t"
            "v_mov_b64 v[190:191], 0\n\t"
            "v_add_u32 v236, s67, v250\n\t"
            "v_add_u32 v248, s67, v251\n\t"
            "ds_read_b64 v[146:147], v253 offset:96\n\t"
            "ds_read_b64 v[148:149], v253 offset:1744\n\t"
            "ds_read_b64 v[150:151], v253 offset:1840\n\t"
            "ds_read_b128 v[172:175], v254 offset:64\n\t"
            "ds_read_b64 v[152:153], v253 offset:8\n\t"
            "ds_read_b64 v[154:155], v253 offset:104\n\t"
            "ds_read_b64 v[156:157], v253 offset:1752\n\t"
            "ds_read_b64 v[158:159], v253 offset:1848\n\t"
            "ds_read_b64 v[160:161], v253 offset:16\n\t"
            "ds_read_b64 v[162:163], v253 offset:112\n\t"
            "ds_read_b64 v[164:165], v253 offset:1760\n\t"
            "ds_read_b64 v[166:167], v253 offset:1856\n\t"
            "ds_read_b32 v249, v249\n\t"
            "s_waitcnt lgkmcnt(10)\n\t"
            "v_smfmac_f32_16x16x64_bf16 v[176:179], v[168:171], v[144:151], v255\n\t"
            "ds_read_b128 v[168:171], v254 offset:128\n\t"
            "ds_read_b64 v[144:145], v253 offset:24\n\t"
            "ds_read_b64 v[146:147], v253 offset:120\n\t"
            "ds_read_b64 v[148:149], v253 offset:1768\n\t"
            "s_waitcnt lgkmcnt(9)\n\t"
            "v_smfmac_f32_16x16x64_bf16 v[180:183], v[172:175], v[152:159], v255\n\t"
            "ds_read_b64 v[150:151], v253 offset:1864\n\t"
            "ds_read_b128 v[172:175], v254 offset:192\n\t"
            "ds_read_b64 v[152:153], v253 offset:32\n\t"
            "v_cvt_pk_bf16_f32 v176, v176, v177\n\t"
            "v_cvt_pk_bf16_f32 v177, v178, v179\n\t"
            "s_waitcnt lgkmcnt(7)\n\t"
            "v_readfirstlane_b32 s26, v249\n\t"
            "v_mfma_f32_4x4x4_16b_bf16 v[196:199], v[176:177], v[176:177], 0\n\t"
            "ds_read_b64 v[154:155], v253 offset:128\n\t"
            "s_and_b32 s68, s26, 0x7ffffff\n\t"
            "s_add_u32 s68, s68, s78\n\t"
            "s_and_b32 s69, s26, 0x38000000\n\t"
            "s_or_b32 s69, s69, s39\n\t"
            "s_bitcmp1_b32 s26, 27\n\t"
            "s_cselect_b32 s79, 2.0, s68\n\t"
            "ds_read_b64 v[156:157], v253 offset:1776\n\t"
            "s_and_b32 s57, s56, 3\n\t"
            "s_lshl_b32 s58, s57, 3\n\t"
            "s_lshr_b32 m0, s54, s58\n\t"
            "s_waitcnt lgkmcnt(8)\n\t"
            "v_smfmac_f32_16x16x64_bf16 v[184:187], v[168:171], v[160:167], v255\n\t"
            "s_lshl_b32 s58, s57, 4\n\t"
            "s_lshr_b64 s[36:37], s[50:51], s58\n\t"
            "s_and_b32 s57, s36, 0xffff\n\t"
            "s_add_u32 s57, s57, s67\n\t"
            "ds_read_b64 v[158:159], v253 offset:1872\n\t"
            "s_lshr_b64 s[36:37], s[52:53], s58\n\t"
            "s_and_b32 s58, s36, 0xffff\n\t"
            "s_add_u32 s58, s58, s67\n\t"
            "ds_read_b128 v[168:171], v254 offset:256\n\t"
            "s_and_b32 s59, s50, 0xffff\n\t"
            "s_add_u32 s59, s59, s67\n\t"
            "s_and_b32 s38, s52, 0xffff\n\t"
            "s_add_u32 s38, s38, s67\n\t"
            "s_waitcnt lgkmcnt(5)\n\t"
            "v_smfmac_f32_16x16x64_bf16 v[188:191], v[172:175], v[144:151], v255\n\t"
            "ds_read_b64 v[160:161], v253 offset:40\n\t"
            "v_writelane_b32 v236, s59, m0\n\t"
            "v_writelane_b32 v236, s57, 63\n\t"
            "v_cvt_pk_bf16_f32 v178, v180, v181\n\t"
            "v_cvt_pk_bf16_f32 v179, v182, v183\n\t"
            "v_mov_b64 v[180:181], 0\n\t"
            "v_mov_b64 v[182:183], 0\n\t"
            "v_mfma_f32_16x16x32_bf16 v[192:195], v[238:241], v[176:179], 0\n\t"
            "ds_read_b64 v[162:163], v253 offset:136\n\t"
            "v_cvt_pk_bf16_f32 v184, v184, v185\n\t"
            "v_mfma_f32_4x4x4_16b_bf16 v[196:199], v[178:179], v[178:179], v[196:199]\n\t"
            "ds_read_b64 v[164:165], v253 offset:1784\n\t"
            "v_cvt_pk_bf16_f32 v185, v186, v187\n\t"
            "v_xor_b32 v237, 16, v236\n\t"
            "v_cvt_pk_bf16_f32 v186, v188, v189\n\t"
            "v_mfma_f32_4x4x4_16b_bf16 v[196:199], v[184:185], v[184:185], v[196:199]\n\t"
            "ds_read_b64 v[166:167], v253 offset:1880\n\t"
            "v_cvt_pk_bf16_f32 v187, v190, v191\n\t"
            "v_mov_b64 v[188:189], 0\n\t"
            "v_mov_b64 v[190:191], 0\n\t"
            "v_mfma_f32_16x16x32_bf16 v[192:195], v[238:241], v[184:187], v[192:195]\n\t"
            "ds_read_b128 v[172:175], v254 offset:320\n\t"
            "v_writelane_b32 v248, s38, m0\n\t"
            "v_mfma_f32_4x4x4_16b_bf16 v[196:199], v[186:187], v[186:187], v[196:199]\n\t"
            "ds_read_b64 v[144:145], v253 offset:48\n\t"
            "v_writelane_b32 v248, s58, 63\n\t"
            "ds_read_b64 v[146:147], v253 offset:144\n\t"
            "ds_read_b64 v[148:149], v253 offset:1792\n\t"
            "ds_read_b64 v[150:151], v253 offset:1888\n\t"
            "ds_write_b128 v236, v[176:179]\n\t"
            "v_mov_b64 v[176:177], 0\n\t"
            "v_mov_b64 v[178:179], 0\n\t"
            "s_waitcnt lgkmcnt(10)\n\t"
            "s_nop 0\n\t"
            "v_smfmac_f32_16x16x64_bf16 v[176:179], v[168:171], v[152:159], v255\n\t"
            "ds_read_b128 v[168:171], v254 offset:384\n\t"
            "ds_read_b64 v[152:153], v253 offset:56\n\t"
            "ds_read_b64 v[154:155], v253 offset:152\n\t"
            "s_waitcnt lgkmcnt(8)\n\t"
            "v_smfmac_f32_16x16x64_bf16 v[180:183], v[172:175], v[160:167], v255\n\t"
            "ds_read_b64 v[156:157], v253 offset:1800\n\t"
            "ds_read_b64 v[158:159], v253 offset:1896\n\t"
            "ds_read_b128 v[172:175], v254 offset:448\n\t"
            "ds_read_b64 v[160:161], v253 offset:64\n\t"
            "v_cvt_pk_bf16_f32 v176, v176, v177\n\t"
            "v_cvt_pk_bf16_f32 v177, v178, v179\n\t"
            "s_nop 1\n\t"
            "v_mfma_f32_4x4x4_16b_bf16 v[196:199], v[176:177], v[176:177], v[196:199]\n\t"
            "ds_read_b64 v[162:163], v253 offset:160\n\t"
            "v_cvt_pk_bf16_f32 v178, v180, v181\n\t"
            "v_cvt_pk_bf16_f32 v179, v182, v183\n\t"
            "v_mov_b64 v[180:181], 0\n\t"
            "v_mov_b64 v[182:183], 0\n\t"
            "v_mfma_f32_16x16x32_bf16 v[192:195], v[238:241], v[176:179], v[192:195]\n\t"
            "ds_read_b64 v[164:165], v253 offset:1808\n\t"
            "v_mfma_f32_4x4x4_16b_bf16 v[196:199], v[178:179], v[178:179], v[196:199]\n\t"
            "ds_read_b64 v[166:167], v253 offset:1904\n\t"
            "s_waitcnt lgkmcnt(14)\n\t"
            "ds_write_b128 v237, v[184:187]\n\t"
            "v_mov_b64 v[184:185], 0\n\t"
            "v_mov_b64 v[186:187], 0\n\t"
            "s_waitcnt lgkmcnt(10)\n\t"
            "s_nop 0\n\t"
            "v_smfmac_f32_16x16x64_bf16 v[184:187], v[168:171], v[144:151], v255\n\t"
            "ds_read_b128 v[168:171], v254 offset:512\n\t"
            "s_waitcnt lgkmcnt(6)\n\t"
            "v_smfmac_f32_16x16x64_bf16 v[188:191], v[172:175], v[152:159], v255\n\t"
            "ds_read_b64 v[144:145], v253 offset:72\n\t"
            "ds_read_b64 v[146:147], v253 offset:168\n\t"
            "ds_read_b64 v[148:149], v253 offset:1816\n\t"
            "ds_read_b64 v[150:151], v253 offset:1912\n\t"
            "ds_read_b128 v[172:175], v254 offset:576\n\t"
            "ds_read_b64 v[152:153], v253 offset:80\n\t"
            "v_cvt_pk_bf16_f32 v184, v184, v185\n\t"
            "v_cvt_pk_bf16_f32 v185, v186, v187\n\t"
            "v_cvt_pk_bf16_f32 v186, v188, v189\n\t"
            "v_cvt_pk_bf16_f32 v187, v190, v191\n\t"
            "v_mfma_f32_4x4x4_16b_bf16 v[196:199], v[184:185], v[184:185], v[196:199]\n\t"
            "ds_read_b64 v[154:155], v253 offset:176\n\t"
            "v_mfma_f32_16x16x32_bf16 v[192:195], v[238:241], v[184:187], v[192:195]\n\t"
            "ds_read_b64 v[156:157], v253 offset:1824\n\t"
            "v_mov_b64 v[188:189], 0\n\t"
            "v_mfma_f32_4x4x4_16b_bf16 v[196:199], v[186:187], v[186:187], v[196:199]\n\t"
            "ds_read_b64 v[158:159], v253 offset:1920\n\t"
            "v_mov_b64 v[190:191], 0\n\t"
            "s_waitcnt lgkmcnt(14)\n\t"
            "ds_write_b128 v236, v[176:179] offset:128\n\t"
            "v_mov_b64 v[176:177], 0\n\t"
            "v_mov_b64 v[178:179], 0\n\t"
            "s_waitcnt lgkmcnt(10)\n\t"
            "s_nop 0\n\t"
            "v_smfmac_f32_16x16x64_bf16 v[176:179], v[168:171], v[160:167], v255\n\t"
            "ds_read_b128 v[168:171], v254 offset:640\n\t"
            "s_waitcnt lgkmcnt(6)\n\t"
            "v_smfmac_f32_16x16x64_bf16 v[180:183], v[172:175], v[144:151], v255\n\t"
            "ds_read_b64 v[160:161], v253 offset:88\n\t"
            "ds_read_b64 v[162:163], v253 offset:184\n\t"
            "ds_read_b64 v[164:165], v253 offset:1832\n\t"
            "ds_read_b64 v[166:167], v253 offset:1928\n\t"
            "ds_read_b128 v[172:175], v254 offset:704\n\t"
            "ds_write_b128 v237, v[184:187] offset:128\n\t"
            "v_mov_b64 v[184:185], 0\n\t"
            "v_mov_b64 v[186:187], 0\n\t"
            "v_cvt_pk_bf16_f32 v176, v176, v177\n\t"
            "v_cvt_pk_bf16_f32 v177, v178, v179\n\t"
            "v_cvt_pk_bf16_f32 v178, v180, v181\n\t"
            "v_cvt_pk_bf16_f32 v179, v182, v183\n\t"
            "v_mfma_f32_4x4x4_16b_bf16 v[196:199], v[176:177], v[176:177], v[196:199]\n\t"
            "ds_write_b128 v236, v[176:179] offset:256\n\t"
            "s_waitcnt lgkmcnt(7)\n\t"
            "v_smfmac_f32_16x16x64_bf16 v[184:187], v[168:171], v[152:159], v255\n\t"
            "v_mfma_f32_4x4x4_16b_bf16 v[196:199], v[178:179], v[178:179], v[196:199]\n\t"
            "v_mfma_f32_16x16x32_bf16 v[192:195], v[238:241], v[176:179], v[192:195]\n\t"
            "s_waitcnt lgkmcnt(2)\n\t"
            "v_smfmac_f32_16x16x64_bf16 v[188:191], v[172:175], v[160:167], v255\n\t"
            "s_nop 3\n\t"
            "v_cvt_pk_bf16_f32 v184, v184, v185\n\t"
            "v_cvt_pk_bf16_f32 v185, v186, v187\n\t"
            "s_nop 1\n\t"
            "v_mfma_f32_4x4x4_16b_bf16 v[196:199], v[184:185], v[184:185], v[196:199]\n\t"
            "v_cvt_pk_bf16_f32 v186, v188, v189\n\t"
            "v_cvt_pk_bf16_f32 v187, v190, v191\n\t"
            "s_nop 1\n\t"
            "v_mfma_f32_4x4x4_16b_bf16 v[196:199], v[186:187], v[186:187], v[196:199]\n\t"
            "ds_write_b128 v237, v[184:187] offset:256\n\t"
            "v_mfma_f32_16x16x32_bf16 v[192:195], v[238:241], v[184:187], v[192:195]\n\t"
            "s_nop 7\n\t"
            "v_cndmask_b32_e64 v193, v196, v197, s[60:61]\n\t"
            "v_cndmask_b32_e64 v193, v193, v198, s[62:63]\n\t"
            "v_cndmask_b32_e64 v193, v193, v199, s[64:65]\n\t"
            "ds_write_b64 v248, v[192:193] offset:18816\n\t"
            "s_waitcnt lgkmcnt(0)\n\t"
            "s_nop 7\n\t"
            "v_mov_b32 v144, s44\n\t"
            "v_mov_b32 v145, 0xbfffffff\n\t"
            "ds_write_b32 v144, v145\n\t"
            "s_setprio 1\n\t"
            "v_mov_b32 v144, s23\n\t"
            ".Lgo%=:\n\t"
            "ds_read_b32 v145, v144 offset:124\n\t"
            "s_waitcnt lgkmcnt(0)\n\t"
            "v_cmp_ne_u32 vcc, -1, v145\n\t"
            "s_nop 4\n\t"
            "s_cbranch_vccnz .Lgo%=\n\t"
            "s_nop 7\n\t"
            "ds_read_b32 v249, v252 offset:1024\n\t"
            "s_add_u32 s49, s70, 0\n\t"
            "buffer_load_dwordx4 v[0:3], v247, s[28:31], s49 offen sc1\n\t"
            "s_add_u32 s49, s70, 1024\n\t"
            "buffer_load_dwordx4 v[4:7], v247, s[28:31], s49 offen sc1\n\t"
            "s_add_u32 s49, s70, 2048\n\t"
            "buffer_load_dwordx4 v[8:11], v247, s[28:31], s49 offen sc1\n\t"
            "s_add_u32 s49, s70, 3072\n\t"
            "buffer_load_dwordx4 v[12:15], v247, s[28:31], s49 offen sc1\n\t"
            "s_add_u32 s49, s70, 4096\n\t"
            "buffer_load_dwordx4 v[16:19], v247, s[28:31], s49 offen sc1\n\t"
            "s_add_u32 s49, s70, 5120\n\t"
            "buffer_load_dwordx4 v[20:23], v247, s[28:31], s49 offen sc1\n\t"
            "s_add_u32 s49, s70, 6144\n\t"
            "buffer_load_dwordx4 v[24:27], v247, s[28:31], s49 offen sc1\n\t"
            "s_add_u32 s49, s70, 7168\n\t"
            "buffer_load_dwordx4 v[28:31], v247, s[28:31], s49 offen sc1\n\t"
            "s_add_u32 s49, s70, 8192\n\t"
            "buffer_load_dwordx4 v[32:35], v247, s[28:31], s49 offen sc1\n\t"
            "s_add_u32 s49, s70, 9216\n\t"
            "buffer_load_dwordx4 v[36:39], v247, s[28:31], s49 offen sc1\n\t"
            "s_add_u32 s49, s70, 10240\n\t"
            "buffer_load_dwordx4 v[40:43], v247, s[28:31], s49 offen sc1\n\t"
            "s_add_u32 s49, s70, 11264\n\t"
            "buffer_load_dwordx4 v[44:47], v247, s[28:31], s49 offen sc1\n\t"
            "s_add_u32 s49, s70, 12288\n\t"
            "buffer_load_dwordx4 v[48:51], v247, s[28:31], s49 offen sc1\n\t"
            "s_add_u32 s49, s70, 13312\n\t"
            "buffer_load_dwordx4 v[52:55], v247, s[28:31], s49 offen sc1\n\t"
            "s_add_u32 s49, s70, 14336\n\t"
            "buffer_load_dwordx4 v[56:59], v247, s[28:31], s49 offen sc1\n\t"
            "s_add_u32 s49, s70, 15360\n\t"
            "buffer_load_dwordx4 v[60:63], v247, s[28:31], s49 offen sc1\n\t"
            "s_add_u32 s49, s70, 16384\n\t"
            "buffer_load_dwordx4 v[64:67], v247, s[28:31], s49 offen sc1\n\t"
            "s_add_u32 s49, s70, 17408\n\t"
            "buffer_load_dwordx4 v[68:71], v247, s[28:31], s49 offen sc1\n\t"
            "s_waitcnt lgkmcnt(0)\n\t"
            "v_and_b32 v249, s69, v249\n\t"
            "s_mov_b32 exec_hi, 0x3fffff\n\t"
            "s_add_u32 m0, s66, 0\n\t"
            "s_add_u32 s49, s79, 0\n\t"
            "buffer_load_dwordx4 v249, s[72:75], s49 offen lds\n\t"
            "s_mov_b32 exec_hi, -1\n\t"
            "s_mov_b32 exec_hi, 0x3fffff\n\t"
            "s_add_u32 m0, s66, 872\n\t"
            "s_add_u32 s49, s68, 196224\n\t"
            "buffer_load_dwordx4 v249, s[72:75], s49 offen lds\n\t"
            "s_mov_b32 exec_hi, -1\n\t"
            "s_mov_b32 exec_hi, 0x3fffff\n\t"
            "s_add_u32 m0, s66, 1744\n\t"
            "s_add_u32 s49, s68, 392448\n\t"
            "buffer_load_dwordx4 v249, s[72:75], s49 offen lds\n\t"
            "s_mov_b32 exec_hi, -1\n\t"
            "s_mov_b32 exec_hi, 0x3fffff\n\t"
            "s_add_u32 m0, s66, 2616\n\t"
            "s_add_u32 s49, s68, 588672\n\t"
            "buffer_load_dwordx4 v249, s[72:75], s49 offen lds\n\t"
            "s_mov_b32 exec_hi, -1\n\t"
            "s_mov_b32 exec_hi, 0x3fffff\n\t"
            "s_add_u32 m0, s66, 3488\n\t"
            "s_add_u32 s49, s68, 784896\n\t"
            "buffer_load_dwordx4 v249, s[72:75], s49 offen lds\n\t"
            "s_mov_b32 exec_hi, -1\n\t"
            "s_mov_b32 exec_hi, 0x3fffff\n\t"
            "s_add_u32 m0, s66, 4360\n\t"
            "s_add_u32 s49, s68, 981120\n\t"
            "buffer_load_dwordx4 v249, s[72:75], s49 offen lds\n\t"
            "s_mov_b32 exec_hi, -1\n\t"
            "s_mov_b32 exec_hi, 0x3fffff\n\t"
            "s_add_u32 m0, s66, 5232\n\t"
            "s_add_u32 s49, s68, 1177344\n\t"
            "buffer_load_dwordx4 v249, s[72:75], s49 offen lds\n\t"
            "s_mov_b32 exec_hi, -1\n\t"
            "s_mov_b32 exec_hi, 0x3fffff\n\t"
            "s_add_u32 m0, s66, 6104\n\t"
            "s_add_u32 s49, s68, 1373568\n\t"
            "buffer_load_dwordx4 v249, s[72:75], s49 offen lds\n\t"
            "s_mov_b32 exec_hi, -1\n\t"
            "s_mov_b32 exec_hi, 0x3fffff\n\t"
            "s_add_u32 m0, s66, 6976\n\t"
            "s_add_u32 s49, s68, 1569792\n\t"
            "buffer_load_dwordx4 v249, s[72:75], s49 offen lds\n\t"
            "s_mov_b32 exec_hi, -1\n\t"
            "s_add_u32 s45, s46, s27\n\t"
            "s_mov_b32 s46, s47\n\t"
            "s_mov_b32 s47, s68\n\t"
            "s_xor_b32 s48, s48, s71\n\t"
            "s_xor_b32 s67, s67, s71\n\t"
            "s_add_u32 s76, s76, 4\n\t"
            "s_lshl_b32 s56, s56, 2\n\t"
            "s_lshr_b32 s49, s26, 30\n\t"
            "s_or_b32 s56, s56, s49\n\t"
            "s_cmp_ge_u32 s76, s24\n\t"
            "s_cselect_b64 vcc, -1, 0\n\t"
            "s_nop 7\n\t"
            "v_mov_b32 v144, s23\n\t"
            ".Lpg%=:\n\t"
            "ds_read_b128 v[148:151], v144 offset:96\n\t"
            "s_waitcnt lgkmcnt(0)\n\t"
            "v_and_b32 v148, v148, v149\n\t"
            "v_and_b32 v150, v150, v151\n\t"
            "v_and_b32 v148, v148, v150\n\t"
            "v_cmp_le_i32 vcc, 0, v148\n\t"
            "s_nop 4\n\t"
            "s_cbranch_vccnz .Lpg%=\n\t"
            "s_setprio 3\n\t"
            "s_nop 7\n\t"
            "v_add_u32 v196, s22, v229\n\t"
            "v_xor_b32 v197, 64, v196\n\t"
            "v_add_u32 v198, s22, v247\n\t"
            "v_mov_b32 v249, s76\n\t"
            "v_add_u32 v236, s67, v250\n\t"
            "v_add_u32 v248, s67, v251\n\t"
            "ds_read_b128 v[144:147], v196\n\t"
            "ds_read_b128 v[148:151], v197\n\t"
            "ds_read_b128 v[152:155], v196 offset:128\n\t"
            "ds_read_b128 v[156:159], v197 offset:128\n\t"
            "ds_read_b128 v[160:163], v196 offset:256\n\t"
            "ds_read_b128 v[164:167], v197 offset:256\n\t"
            "ds_read_b128 v[216:219], v198 offset:12288\n\t"
            "ds_read_b128 v[220:223], v198 offset:13312\n\t"
            "v_add_u32 v198, s48, v247\n\t"
            "ds_read_b128 v[168:171], v196 offset:6144\n\t"
            "ds_read_b128 v[172:175], v197 offset:6144\n\t"
            "ds_read_b128 v[176:179], v196 offset:6272\n\t"
            "ds_read_b128 v[180:183], v197 offset:6272\n\t"
            "ds_read_b128 v[184:187], v196 offset:6400\n\t"
            "v_add_u32 v196, s48, v229\n\t"
            "ds_read_b128 v[188:191], v197 offset:6400\n\t"
            "s_waitcnt vmcnt(9)\n\t"
            "s_waitcnt lgkmcnt(13)\n\t"
            "v_mfma_f32_16x16x32_bf16 v[200:203], v[0:3], v[144:147], 0\n\t"
            "ds_read_b32 v249, v249\n\t"
            "v_xor_b32 v197, 64, v196\n\t"
            "s_waitcnt lgkmcnt(13)\n\t"
            "v_mfma_f32_16x16x32_bf16 v[200:203], v[4:7], v[148:151], v[200:203]\n\t"
            "s_add_u32 s49, s33, 18432\n\t"
            "buffer_load_dwordx4 v[72:75], v247, s[28:31], s49 offen sc1\n\t"
            "s_waitcnt lgkmcnt(12)\n\t"
            "v_mfma_f32_16x16x32_bf16 v[200:203], v[8:11], v[152:155], v[200:203]\n\t"
            "s_waitcnt lgkmcnt(8)\n\t"
            "v_add_f32 v216, v216, v218\n\t"
            "s_waitcnt lgkmcnt(7)\n\t"
            "v_add_f32 v220, v220, v222\n\t"
            "v_mfma_f32_16x16x32_bf16 v[200:203], v[12:15], v[156:159], v[200:203]\n\t"
            "v_add_f32 v216, v216, v220\n\t"
            "v_add_f32 v217, v217, v219\n\t"
            "v_mfma_f32_16x16x32_bf16 v[200:203], v[16:19], v[160:163], v[200:203]\n\t"
            "v_add_f32 v221, v221, v223\n\t"
            "v_mul_f32 v216, 0x3baaaaab, v216\n\t"
            "v_mfma_f32_16x16x32_bf16 v[200:203], v[20:23], v[164:167], v[200:203]\n\t"
            "v_add_f32 v217, v217, v221\n\t"
            "v_mul_f32_e64 v216, v216, -v216\n\t"
            "v_mfma_f32_16x16x32_bf16 v[204:207], v[24:27], v[144:147], 0\n\t"
            "v_fmac_f32 v216, 0x3baaaaab, v217\n\t"
            "v_max_f32 v216, 0, v216\n\t"
            "v_mfma_f32_16x16x32_bf16 v[204:207], v[28:31], v[148:151], v[204:207]\n\t"
            "v_add_f32 v216, 0x3727c5ac, v216\n\t"
            "v_rsq_f32 v216, v216\n\t"
            "v_mfma_f32_16x16x32_bf16 v[204:207], v[32:35], v[152:155], v[204:207]\n\t"
            "ds_write_b32 v232, v216\n\t"
            "v_mfma_f32_16x16x32_bf16 v[204:207], v[36:39], v[156:159], v[204:207]\n\t"
            "ds_read_b32 v192, v234\n\t"
            "s_waitcnt lgkmcnt(2)\n\t"
            "v_readfirstlane_b32 s26, v249\n\t"
            "v_mfma_f32_16x16x32_bf16 v[204:207], v[40:43], v[160:163], v[204:207]\n\t"
            "ds_read_b32 v193, v234 offset:64\n\t"
            "v_mfma_f32_16x16x32_bf16 v[204:207], v[44:47], v[164:167], v[204:207]\n\t"
            "ds_read_b32 v249, v252 offset:1024\n\t"
            "v_mfma_f32_16x16x32_bf16 v[208:211], v[48:51], v[144:147], 0\n\t"
            "ds_read_b128 v[144:147], v196\n\t"
            "v_mfma_f32_16x16x32_bf16 v[208:211], v[52:55], v[148:151], v[208:211]\n\t"
            "ds_read_b128 v[148:151], v197\n\t"
            "v_mfma_f32_16x16x32_bf16 v[208:211], v[56:59], v[152:155], v[208:211]\n\t"
            "ds_read_b128 v[152:155], v196 offset:128\n\t"
            "v_mfma_f32_16x16x32_bf16 v[208:211], v[60:63], v[156:159], v[208:211]\n\t"
            "ds_read_b128 v[156:159], v197 offset:128\n\t"
            "v_mfma_f32_16x16x32_bf16 v[208:211], v[64:67], v[160:163], v[208:211]\n\t"
            "ds_read_b128 v[160:163], v196 offset:256\n\t"
            "v_mfma_f32_16x16x32_bf16 v[208:211], v[68:71], v[164:167], v[208:211]\n\t"
            "ds_read_b128 v[164:167], v197 offset:256\n\t"
            "s_waitcnt lgkmcnt(8)\n\t"
            "v_mul_f32 v200, v192, v200\n\t"
            "v_mfma_f32_16x16x32_bf16 v[212:215], v[0:3], v[168:171], 0\n\t"
            "v_mul_f32 v201, v192, v201\n\t"
            "v_mul_f32 v204, v192, v204\n\t"
            "v_mfma_f32_16x16x32_bf16 v[212:215], v[4:7], v[172:175], v[212:215]\n\t"
            "v_mul_f32 v205, v192, v205\n\t"
            "v_mul_f32 v202, v192, v202\n\t"
            "v_mfma_f32_16x16x32_bf16 v[212:215], v[8:11], v[176:179], v[212:215]\n\t"
            "v_mul_f32 v203, v192, v203\n\t"
            "v_cvt_pk_bf16_f32 v200, v200, v201\n\t"
            "v_mfma_f32_16x16x32_bf16 v[212:215], v[12:15], v[180:183], v[212:215]\n\t"
            "v_mul_f32 v206, v192, v206\n\t"
            "v_mul_f32 v207, v192, v207\n\t"
            "v_mfma_f32_16x16x32_bf16 v[212:215], v[16:19], v[184:187], v[212:215]\n\t"
            "v_cvt_pk_bf16_f32 v204, v204, v205\n\t"
            "v_cvt_pk_bf16_f32 v201, v202, v203\n\t"
            "v_mfma_f32_16x16x32_bf16 v[212:215], v[20:23], v[188:191], v[212:215]\n\t"
            "v_cvt_pk_bf16_f32 v205, v206, v207\n\t"
            "v_mul_f32 v208, v192, v208\n\t"
            "v_mfma_f32_16x16x32_bf16 v[216:219], v[24:27], v[168:171], 0\n\t"
            "ds_write_b64 v227, v[200:201]\n\t"
            "v_mfma_f32_16x16x32_bf16 v[216:219], v[28:31], v[172:175], v[216:219]\n\t"
            "ds_read_b128 v[200:203], v198 offset:18816\n\t"
            "v_mul_f32 v209, v192, v209\n\t"
            "v_mfma_f32_16x16x32_bf16 v[216:219], v[32:35], v[176:179], v[216:219]\n\t"
            "ds_write_b64 v227, v[204:205] offset:128\n\t"
            "v_mfma_f32_16x16x32_bf16 v[216:219], v[36:39], v[180:183], v[216:219]\n\t"
            "ds_read_b128 v[204:207], v198 offset:19840\n\t"
            "v_mul_f32 v210, v192, v210\n\t"
            "v_mfma_f32_16x16x32_bf16 v[216:219], v[40:43], v[184:187], v[216:219]\n\t"
            "s_add_u32 s49, s33, 19456\n\t"
            "buffer_load_dwordx4 v[76:79], v247, s[28:31], s49 offen sc1\n\t"
            "v_mfma_f32_16x16x32_bf16 v[216:219], v[44:47], v[188:191], v[216:219]\n\t"
            "s_add_u32 s49, s33, 20480\n\t"
            "buffer_load_dwordx4 v[80:83], v247, s[28:31], s49 offen sc1\n\t"
            "v_mfma_f32_16x16x32_bf16 v[220:223], v[48:51], v[168:171], 0\n\t"
            "ds_read_b128 v[168:171], v196 offset:6144\n\t"
            "s_waitcnt lgkmcnt(3)\n\t"
            "v_add_f32 v200, v200, v202\n\t"
            "v_mfma_f32_16x16x32_bf16 v[220:223], v[52:55], v[172:175], v[220:223]\n\t"
            "ds_read_b128 v[172:175], v197 offset:6144\n\t"
            "s_waitcnt lgkmcnt(2)\n\t"
            "v_add_f32 v204, v204, v206\n\t"
            "v_mfma_f32_16x16x32_bf16 v[220:223], v[56:59], v[176:179], v[220:223]\n\t"
            "ds_read_b128 v[176:179], v196 offset:6272\n\t"
            "v_add_f32 v200, v200, v204\n\t"
            "v_mfma_f32_16x16x32_bf16 v[220:223], v[60:63], v[180:183], v[220:223]\n\t"
            "ds_read_b128 v[180:183], v197 offset:6272\n\t"
            "v_add_f32 v201, v201, v203\n\t"
            "v_mfma_f32_16x16x32_bf16 v[220:223], v[64:67], v[184:187], v[220:223]\n\t"
            "ds_read_b128 v[184:187], v196 offset:6400\n\t"
            "v_add_f32 v205, v205, v207\n\t"
            "v_mfma_f32_16x16x32_bf16 v[220:223], v[68:71], v[188:191], v[220:223]\n\t"
            "ds_read_b128 v[188:191], v197 offset:6400\n\t"
            "v_mul_f32 v200, 0x3baaaaab, v200\n\t"
            "v_add_f32 v201, v201, v205\n\t"
            "v_mul_f32_e64 v200, v200, -v200\n\t"
            "v_fmac_f32 v200, 0x3baaaaab, v201\n\t"
            "v_max_f32 v200, 0, v200\n\t"
            "v_add_f32 v200, 0x3727c5ac, v200\n\t"
            "v_rsq_f32 v200, v200\n\t"
            "v_mul_f32 v211, v192, v211\n\t"
            "v_cvt_pk_bf16_f32 v208, v208, v209\n\t"
            "v_cvt_pk_bf16_f32 v209, v210, v211\n\t"
            "v_mul_f32 v212, v193, v212\n\t"
            "v_mul_f32 v213, v193, v213\n\t"
            "v_mul_f32 v214, v193, v214\n\t"
            "v_mul_f32 v215, v193, v215\n\t"
            "v_cvt_pk_bf16_f32 v212, v212, v213\n\t"
            "v_cvt_pk_bf16_f32 v213, v214, v215\n\t"
            "v_mul_f32 v216, v193, v216\n\t"
            "v_mul_f32 v217, v193, v217\n\t"
            "v_mul_f32 v218, v193, v218\n\t"
            "v_mul_f32 v219, v193, v219\n\t"
            "v_mul_f32 v220, v193, v220\n\t"
            "v_mul_f32 v221, v193, v221\n\t"
            "v_mul_f32 v222, v193, v222\n\t"
            "v_mul_f32 v223, v193, v223\n\t"
            "v_cvt_pk_bf16_f32 v216, v216, v217\n\t"
            "v_cvt_pk_bf16_f32 v217, v218, v219\n\t"
            "v_cvt_pk_bf16_f32 v220, v220, v221\n\t"
            "v_cvt_pk_bf16_f32 v221, v222, v223\n\t"
            "ds_write_b32 v232, v200\n\t"
            "v_mfma_f32_16x16x32_bf16 v[200:203], v[144:147], v[0:3], 0\n\t"
            "ds_read_b128 v[192:195], v233\n\t"
            "v_mfma_f32_16x16x32_bf16 v[200:203], v[148:151], v[4:7], v[200:203]\n\t"
            "ds_write_b64 v227, v[208:209] offset:256\n\t"
            "v_mfma_f32_16x16x32_bf16 v[200:203], v[152:155], v[8:11], v[200:203]\n\t"
            "s_add_u32 s49, s33, 21504\n\t"
            "buffer_load_dwordx4 v[84:87], v247, s[28:31], s49 offen sc1\n\t"
            "v_mfma_f32_16x16x32_bf16 v[200:203], v[156:159], v[12:15], v[200:203]\n\t"
            "s_add_u32 s49, s33, 22528\n\t"
            "buffer_load_dwordx4 v[88:91], v247, s[28:31], s49 offen sc1\n\t"
            "v_mfma_f32_16x16x32_bf16 v[200:203], v[160:163], v[16:19], v[200:203]\n\t"
            "ds_write_b64 v228, v[212:213]\n\t"
            "v_mfma_f32_16x16x32_bf16 v[200:203], v[164:167], v[20:23], v[200:203]\n\t"
            "s_add_u32 s49, s33, 23552\n\t"
            "buffer_load_dwordx4 v[92:95], v247, s[28:31], s49 offen sc1\n\t"
            "v_mfma_f32_16x16x32_bf16 v[204:207], v[144:147], v[24:27], 0\n\t"
            "s_add_u32 s49, s33, 24576\n\t"
            "buffer_load_dwordx4 v[96:99], v247, s[28:31], s49 offen sc1\n\t"
            "v_mfma_f32_16x16x32_bf16 v[204:207], v[148:151], v[28:31], v[204:207]\n\t"
            "s_add_u32 s49, s33, 25600\n\t"
            "buffer_load_dwordx4 v[100:103], v247, s[28:31], s49 offen sc1\n\t"
            "v_mfma_f32_16x16x32_bf16 v[204:207], v[152:155], v[32:35], v[204:207]\n\t"
            "ds_write_b64 v228, v[216:217] offset:128\n\t"
            "v_mfma_f32_16x16x32_bf16 v[204:207], v[156:159], v[36:39], v[204:207]\n\t"
            "s_add_u32 s49, s33, 26624\n\t"
            "buffer_load_dwordx4 v[104:107], v247, s[28:31], s49 offen sc1\n\t"
            "v_mfma_f32_16x16x32_bf16 v[204:207], v[160:163], v[40:43], v[204:207]\n\t"
            "s_add_u32 s49, s33, 27648\n\t"
            "buffer_load_dwordx4 v[108:111], v247, s[28:31], s49 offen sc1\n\t"
            "v_mfma_f32_16x16x32_bf16 v[204:207], v[164:167], v[44:47], v[204:207]\n\t"
            "s_waitcnt lgkmcnt(3)\n\t"
            "v_mul_f32 v200, v192, v200\n\t"
            "v_mul_f32 v201, v193, v201\n\t"
            "v_mfma_f32_16x16x32_bf16 v[208:211], v[144:147], v[48:51], 0\n\t"
            "ds_read_b128 v[144:147], v196 offset:12288\n\t"
            "v_mul_f32 v202, v194, v202\n\t"
            "v_mfma_f32_16x16x32_bf16 v[208:211], v[148:151], v[52:55], v[208:211]\n\t"
            "ds_read_b128 v[148:151], v197 offset:12288\n\t"
            "v_mul_f32 v203, v195, v203\n\t"
            "v_mfma_f32_16x16x32_bf16 v[208:211], v[152:155], v[56:59], v[208:211]\n\t"
            "ds_read_b128 v[152:155], v196 offset:12416\n\t"
            "v_mul_f32 v204, v192, v204\n\t"
            "v_mfma_f32_16x16x32_bf16 v[208:211], v[156:159], v[60:63], v[208:211]\n\t"
            "ds_read_b128 v[156:159], v197 offset:12416\n\t"
            "v_mul_f32 v205, v193, v205\n\t"
            "v_mfma_f32_16x16x32_bf16 v[208:211], v[160:163], v[64:67], v[208:211]\n\t"
            "ds_read_b128 v[160:163], v196 offset:12544\n\t"
            "v_mul_f32 v206, v194, v206\n\t"
            "v_mfma_f32_16x16x32_bf16 v[208:211], v[164:167], v[68:71], v[208:211]\n\t"
            "ds_read_b128 v[164:167], v197 offset:12544\n\t"
            "v_mul_f32 v207, v195, v207\n\t"
            "v_mfma_f32_16x16x32_bf16 v[212:215], v[168:171], v[0:3], 0\n\t"
            "ds_write_b64 v228, v[220:221] offset:256\n\t"
            "v_mfma_f32_16x16x32_bf16 v[212:215], v[172:175], v[4:7], v[212:215]\n\t"
            "s_add_u32 s49, s33, 28672\n\t"
            "buffer_load_dwordx4 v[112:115], v247, s[28:31], s49 offen sc1\n\t"
            "v_mfma_f32_16x16x32_bf16 v[212:215], v[176:179], v[8:11], v[212:215]\n\t"
            "v_mul_f32 v208, v192, v208\n\t"
            "v_mul_f32 v209, v193, v209\n\t"
            "v_mfma_f32_16x16x32_bf16 v[212:215], v[180:183], v[12:15], v[212:215]\n\t"
            "v_mul_f32 v210, v194, v210\n\t"
            "v_mul_f32 v211, v195, v211\n\t"
            "v_mfma_f32_16x16x32_bf16 v[212:215], v[184:187], v[16:19], v[212:215]\n\t"
            "ds_read_b128 v[192:195], v233 offset:64\n\t"
            "v_cvt_pk_bf16_f32 v200, v200, v201\n\t"
            "v_mfma_f32_16x16x32_bf16 v[212:215], v[188:191], v[20:23], v[212:215]\n\t"
            "s_add_u32 s49, s33, 29696\n\t"
            "buffer_load_dwordx4 v[116:119], v247, s[28:31], s49 offen sc1\n\t"
            "v_mfma_f32_16x16x32_bf16 v[216:219], v[168:171], v[24:27], 0\n\t"
            "s_add_u32 s49, s33, 30720\n\t"
            "buffer_load_dwordx4 v[120:123], v247, s[28:31], s49 offen sc1\n\t"
            "v_mfma_f32_16x16x32_bf16 v[216:219], v[172:175], v[28:31], v[216:219]\n\t"
            "v_cvt_pk_bf16_f32 v201, v202, v203\n\t"
            "v_cvt_pk_bf16_f32 v204, v204, v205\n\t"
            "v_mfma_f32_16x16x32_bf16 v[216:219], v[176:179], v[32:35], v[216:219]\n\t"
            "s_waitcnt lgkmcnt(0)\n\t"
            "v_mul_f32 v212, v192, v212\n\t"
            "v_mul_f32 v213, v193, v213\n\t"
            "v_mfma_f32_16x16x32_bf16 v[216:219], v[180:183], v[36:39], v[216:219]\n\t"
            "v_mul_f32 v214, v194, v214\n\t"
            "v_mul_f32 v215, v195, v215\n\t"
            "v_mfma_f32_16x16x32_bf16 v[216:219], v[184:187], v[40:43], v[216:219]\n\t"
            "v_cvt_pk_bf16_f32 v202, v212, v213\n\t"
            "v_cvt_pk_bf16_f32 v203, v214, v215\n\t"
            "v_mfma_f32_16x16x32_bf16 v[216:219], v[188:191], v[44:47], v[216:219]\n\t"
            "s_add_u32 s49, s33, 31744\n\t"
            "buffer_load_dwordx4 v[124:127], v247, s[28:31], s49 offen sc1\n\t"
            "v_mfma_f32_16x16x32_bf16 v[220:223], v[168:171], v[48:51], 0\n\t"
            "ds_read_b128 v[168:171], v254\n\t"
            "v_cvt_pk_bf16_f32 v205, v206, v207\n\t"
            "v_mfma_f32_16x16x32_bf16 v[220:223], v[172:175], v[52:55], v[220:223]\n\t"
            "ds_read_b128 v[172:175], v254 offset:64\n\t"
            "v_cvt_pk_bf16_f32 v208, v208, v209\n\t"
            "v_mfma_f32_16x16x32_bf16 v[220:223], v[176:179], v[56:59], v[220:223]\n\t"
            "s_add_u32 s49, s33, 32768\n\t"
            "buffer_load_dwordx4 v[128:131], v247, s[28:31], s49 offen sc1\n\t"
            "v_mfma_f32_16x16x32_bf16 v[220:223], v[180:183], v[60:63], v[220:223]\n\t"
            "v_mul_f32 v216, v192, v216\n\t"
            "v_mul_f32 v217, v193, v217\n\t"
            "v_mfma_f32_16x16x32_bf16 v[220:223], v[184:187], v[64:67], v[220:223]\n\t"
            "v_mul_f32 v218, v194, v218\n\t"
            "v_mul_f32 v219, v195, v219\n\t"
            "v_mfma_f32_16x16x32_bf16 v[220:223], v[188:191], v[68:71], v[220:223]\n\t"
            "v_cvt_pk_bf16_f32 v206, v216, v217\n\t"
            "v_cvt_pk_bf16_f32 v207, v218, v219\n\t"
            "v_mfma_f32_16x16x32_bf16 v[212:215], v[144:147], v[0:3], 0\n\t"
            "s_add_u32 s49, s33, 33792\n\t"
            "buffer_load_dwordx4 v[132:135], v247, s[28:31], s49 offen sc1\n\t"
            "v_mfma_f32_16x16x32_bf16 v[212:215], v[148:151], v[4:7], v[212:215]\n\t"
            "s_add_u32 s49, s33, 34816\n\t"
            "buffer_load_dwordx4 v[136:139], v247, s[28:31], s49 offen sc1\n\t"
            "v_mfma_f32_16x16x32_bf16 v[212:215], v[152:155], v[8:11], v[212:215]\n\t"
            "s_add_u32 s49, s33, 35840\n\t"
            "buffer_load_dwordx4 v[140:143], v247, s[28:31], s49 offen sc1\n\t"
            "v_mfma_f32_16x16x32_bf16 v[212:215], v[156:159], v[12:15], v[212:215]\n\t"
            "s_and_b32 s68, s26, 0x7ffffff\n\t"
            "s_add_u32 s68, s68, s78\n\t"
            "s_and_b32 s69, s26, 0x38000000\n\t"
            "s_or_b32 s69, s69, s39\n\t"
            "s_bitcmp1_b32 s26, 27\n\t"
            "s_cselect_b32 s79, 2.0, s68\n\t"
            "v_mfma_f32_16x16x32_bf16 v[212:215], v[160:163], v[16:19], v[212:215]\n\t"
            "s_and_b32 s57, s56, 3\n\t"
            "s_lshl_b32 s58, s57, 3\n\t"
            "s_lshr_b32 m0, s54, s58\n\t"
            "v_mfma_f32_16x16x32_bf16 v[212:215], v[164:167], v[20:23], v[212:215]\n\t"
            "s_lshl_b32 s58, s57, 4\n\t"
            "s_lshr_b64 s[36:37], s[50:51], s58\n\t"
            "s_and_b32 s57, s36, 0xffff\n\t"
            "s_add_u32 s57, s57, s67\n\t"
            "v_mfma_f32_16x16x32_bf16 v[216:219], v[144:147], v[24:27], 0\n\t"
            "s_lshr_b64 s[36:37], s[52:53], s58\n\t"
            "s_and_b32 s58, s36, 0xffff\n\t"
            "s_add_u32 s58, s58, s67\n\t"
            "v_mfma_f32_16x16x32_bf16 v[216:219], v[148:151], v[28:31], v[216:219]\n\t"
            "s_and_b32 s59, s50, 0xffff\n\t"
            "s_add_u32 s59, s59, s67\n\t"
            "s_and_b32 s38, s52, 0xffff\n\t"
            "s_add_u32 s38, s38, s67\n\t"
            "v_mfma_f32_16x16x32_bf16 v[216:219], v[152:155], v[32:35], v[216:219]\n\t"
            "v_cvt_pk_bf16_f32 v209, v210, v211\n\t"
            "v_mul_f32 v220, v192, v220\n\t"
            "v_mfma_f32_16x16x32_bf16 v[216:219], v[156:159], v[36:39], v[216:219]\n\t"
            "v_mul_f32 v221, v193, v221\n\t"
            "v_mul_f32 v222, v194, v222\n\t"
            "v_mfma_f32_16x16x32_bf16 v[216:219], v[160:163], v[40:43], v[216:219]\n\t"
            "v_mul_f32 v223, v195, v223\n\t"
            "v_cvt_pk_bf16_f32 v210, v220, v221\n\t"
            "v_mfma_f32_16x16x32_bf16 v[216:219], v[164:167], v[44:47], v[216:219]\n\t"
            "ds_read_b128 v[192:195], v233 offset:128\n\t"
            "v_cvt_pk_bf16_f32 v211, v222, v223\n\t"
            "v_mfma_f32_16x16x32_bf16 v[220:223], v[144:147], v[48:51], 0\n\t"
            "s_waitcnt vmcnt(18)\n\t"
            "ds_read_b64 v[144:145], v253\n\t"
            "v_mov_b64 v[176:177], 0\n\t"
            "v_mfma_f32_16x16x32_bf16 v[220:223], v[148:151], v[52:55], v[220:223]\n\t"
            "ds_read_b64 v[146:147], v253 offset:96\n\t"
            "v_mov_b64 v[178:179], 0\n\t"
            "v_mfma_f32_16x16x32_bf16 v[220:223], v[152:155], v[56:59], v[220:223]\n\t"
            "ds_read_b64 v[148:149], v253 offset:1744\n\t"
            "v_mov_b64 v[180:181], 0\n\t"
            "v_mfma_f32_16x16x32_bf16 v[220:223], v[156:159], v[60:63], v[220:223]\n\t"
            "ds_read_b64 v[150:151], v253 offset:1840\n\t"
            "v_mov_b64 v[182:183], 0\n\t"
            "v_mfma_f32_16x16x32_bf16 v[220:223], v[160:163], v[64:67], v[220:223]\n\t"
            "ds_read_b64 v[152:153], v253 offset:8\n\t"
            "v_mov_b64 v[184:185], 0\n\t"
            "v_mfma_f32_16x16x32_bf16 v[220:223], v[164:167], v[68:71], v[220:223]\n\t"
            "ds_read_b64 v[154:155], v253 offset:104\n\t"
            "v_mov_b64 v[186:187], 0\n\t"
            "v_mov_b64 v[188:189], 0\n\t"
            "v_mov_b64 v[190:191], 0\n\t"
            "v_writelane_b32 v236, s59, m0\n\t"
            "v_writelane_b32 v236, s57, 63\n\t"
            "v_xor_b32 v237, 16, v236\n\t"
            "s_waitcnt lgkmcnt(6)\n\t"
            "v_mul_f32 v212, v192, v212\n\t"
            "v_mul_f32 v213, v193, v213\n\t"
            "v_mul_f32 v214, v194, v214\n\t"
            "v_mul_f32 v215, v195, v215\n\t"
            "v_mul_f32 v216, v192, v216\n\t"
            "v_mul_f32 v217, v193, v217\n\t"
            "v_mul_f32 v218, v194, v218\n\t"
            "v_mul_f32 v219, v195, v219\n\t"
            "v_mul_f32 v220, v192, v220\n\t"
            "v_mul_f32 v221, v193, v221\n\t"
            "v_mul_f32 v222, v194, v222\n\t"
            "v_mul_f32 v223, v195, v223\n\t"
            "v_writelane_b32 v248, s38, m0\n\t"
            "v_and_b32 v249, s69, v249\n\t"
            "v_cvt_pk_bf16_f32 v212, v212, v213\n\t"
            "v_cvt_pk_bf16_f32 v216, v216, v217\n\t"
            "s_waitcnt lgkmcnt(2)\n\t"
            "v_smfmac_f32_16x16x64_bf16 v[176:179], v[168:171], v[144:151], v255\n\t"
            "ds_read_b64 v[156:157], v253 offset:1752\n\t"
            "v_cvt_pk_bf16_f32 v220, v220, v221\n\t"
            "v_cvt_pk_bf16_f32 v213, v214, v215\n\t"
            "v_cvt_pk_bf16_f32 v217, v218, v219\n\t"
            "v_cvt_pk_bf16_f32 v221, v222, v223\n\t"
            "v_writelane_b32 v248, s58, 63\n\t"
            "ds_read_b64 v[158:159], v253 offset:1848\n\t"
            "ds_read_b64 v[160:161], v253 offset:16\n\t"
            "v_cvt_pk_bf16_f32 v176, v176, v177\n\t"
            "v_cvt_pk_bf16_f32 v177, v178, v179\n\t"
            "ds_read_b64 v[162:163], v253 offset:112\n\t"
            "ds_read_b64 v[164:165], v253 offset:1760\n\t"
            "ds_read_b64 v[166:167], v253 offset:1856\n\t"
            "ds_read_b128 v[168:171], v254 offset:128\n\t"
            "ds_read_b64 v[144:145], v253 offset:24\n\t"
            "ds_read_b64 v[146:147], v253 offset:120\n\t"
            "ds_read_b64 v[148:149], v253 offset:1768\n\t"
            "ds_read_b64 v[150:151], v253 offset:1864\n\t"
            "ds_write_b128 v230, v[200:203]\n\t"
            "ds_write_b128 v230, v[204:207] offset:1536\n\t"
            "s_waitcnt lgkmcnt(11)\n\t"
            "v_smfmac_f32_16x16x64_bf16 v[180:183], v[172:175], v[152:159], v255\n\t"
            "ds_read_b128 v[172:175], v254 offset:192\n\t"
            "ds_read_b64 v[152:153], v253 offset:32\n\t"
            "ds_read_b64 v[154:155], v253 offset:128\n\t"
            "ds_read_b64 v[156:157], v253 offset:1776\n\t"
            "s_waitcnt lgkmcnt(10)\n\t"
            "v_smfmac_f32_16x16x64_bf16 v[184:187], v[168:171], v[160:167], v255\n\t"
            "ds_read_b64 v[158:159], v253 offset:1872\n\t"
            "v_mfma_f32_4x4x4_16b_bf16 v[196:199], v[176:177], v[176:177], 0\n\t"
            "ds_read_b128 v[168:171], v254 offset:256\n\t"
            "v_cvt_pk_bf16_f32 v178, v180, v181\n\t"
            "v_cvt_pk_bf16_f32 v179, v182, v183\n\t"
            "v_mov_b64 v[180:181], 0\n\t"
            "v_mov_b64 v[182:183], 0\n\t"
            "s_nop 0\n\t"
            "v_cvt_pk_bf16_f32 v184, v184, v185\n\t"
            "v_cvt_pk_bf16_f32 v185, v186, v187\n\t"
            "ds_read_b64 v[160:161], v253 offset:40\n\t"
            "ds_read_b64 v[162:163], v253 offset:136\n\t"
            "ds_read_b64 v[164:165], v253 offset:1784\n\t"
            "s_waitcnt lgkmcnt(14)\n\t"
            "ds_read_b64 v[166:167], v253 offset:1880\n\t"
            "s_waitcnt lgkmcnt(9)\n\t"
            "v_smfmac_f32_16x16x64_bf16 v[188:191], v[172:175], v[144:151], v255\n\t"
            "ds_read_b128 v[172:175], v254 offset:320\n\t"
            "v_mfma_f32_4x4x4_16b_bf16 v[196:199], v[178:179], v[178:179], v[196:199]\n\t"
            "ds_read_b64 v[144:145], v253 offset:48\n\t"
            "v_mfma_f32_16x16x32_bf16 v[192:195], v[238:241], v[176:179], 0\n\t"
            "ds_read_b64 v[146:147], v253 offset:144\n\t"
            "ds_read_b64 v[148:149], v253 offset:1792\n\t"
            "ds_read_b64 v[150:151], v253 offset:1888\n\t"
            "ds_write_b128 v236, v[176:179]\n\t"
            "v_mov_b64 v[176:177], 0\n\t"
            "v_mov_b64 v[178:179], 0\n\t"
            "v_cvt_pk_bf16_f32 v186, v188, v189\n\t"
            "v_cvt_pk_bf16_f32 v187, v190, v191\n\t"
            "s_waitcnt lgkmcnt(10)\n\t"
            "v_smfmac_f32_16x16x64_bf16 v[176:179], v[168:171], v[152:159], v255\n\t"
            "ds_read_b128 v[168:171], v254 offset:384\n\t"
            "v_mfma_f32_4x4x4_16b_bf16 v[196:199], v[184:185], v[184:185], v[196:199]\n\t"
            "ds_read_b64 v[152:153], v253 offset:56\n\t"
            "v_mov_b64 v[188:189], 0\n\t"
            "v_mov_b64 v[190:191], 0\n\t"
            "ds_read_b64 v[154:155], v253 offset:152\n\t"
            "s_waitcnt lgkmcnt(8)\n\t"
            "v_smfmac_f32_16x16x64_bf16 v[180:183], v[172:175], v[160:167], v255\n\t"
            "ds_read_b64 v[156:157], v253 offset:1800\n\t"
            "v_mfma_f32_4x4x4_16b_bf16 v[196:199], v[186:187], v[186:187], v[196:199]\n\t"
            "ds_read_b64 v[158:159], v253 offset:1896\n\t"
            "v_mfma_f32_16x16x32_bf16 v[192:195], v[238:241], v[184:187], v[192:195]\n\t"
            "ds_read_b128 v[172:175], v254 offset:448\n\t"
            "v_cvt_pk_bf16_f32 v176, v176, v177\n\t"
            "v_cvt_pk_bf16_f32 v177, v178, v179\n\t"
            "s_nop 0\n\t"
            "v_cvt_pk_bf16_f32 v178, v180, v181\n\t"
            "v_cvt_pk_bf16_f32 v179, v182, v183\n\t"
            "v_mov_b64 v[180:181], 0\n\t"
            "v_mov_b64 v[182:183], 0\n\t"
            "ds_read_b64 v[160:161], v253 offset:64\n\t"
            "ds_read_b64 v[162:163], v253 offset:160\n\t"
            "ds_read_b64 v[164:165], v253 offset:1808\n\t"
            "ds_read_b64 v[166:167], v253 offset:1904\n\t"
            "s_waitcnt lgkmcnt(14)\n\t"
            "ds_write_b128 v237, v[184:187]\n\t"
            "v_mov_b64 v[184:185], 0\n\t"
            "v_mov_b64 v[186:187], 0\n\t"
            "s_waitcnt lgkmcnt(10)\n\t"
            "s_nop 0\n\t"
            "v_smfmac_f32_16x16x64_bf16 v[184:187], v[168:171], v[144:151], v255\n\t"
            "ds_read_b128 v[168:171], v254 offset:512\n\t"
            "v_mfma_f32_4x4x4_16b_bf16 v[196:199], v[176:177], v[176:177], v[196:199]\n\t"
            "ds_read_b64 v[144:145], v253 offset:72\n\t"
            "s_waitcnt lgkmcnt(7)\n\t"
            "v_smfmac_f32_16x16x64_bf16 v[188:191], v[172:175], v[152:159], v255\n\t"
            "ds_read_b64 v[146:147], v253 offset:168\n\t"
            "v_mfma_f32_4x4x4_16b_bf16 v[196:199], v[178:179], v[178:179], v[196:199]\n\t"
            "ds_read_b64 v[148:149], v253 offset:1816\n\t"
            "v_mfma_f32_16x16x32_bf16 v[192:195], v[238:241], v[176:179], v[192:195]\n\t"
            "ds_read_b64 v[150:151], v253 offset:1912\n\t"
            "v_cvt_pk_bf16_f32 v184, v184, v185\n\t"
            "v_cvt_pk_bf16_f32 v185, v186, v187\n\t"
            "s_nop 0\n\t"
            "v_cvt_pk_bf16_f32 v186, v188, v189\n\t"
            "v_cvt_pk_bf16_f32 v187, v190, v191\n\t"
            "v_mov_b64 v[188:189], 0\n\t"
            "v_mov_b64 v[190:191], 0\n\t"
            "ds_read_b128 v[172:175], v254 offset:576\n\t"
            "ds_read_b64 v[152:153], v253 offset:80\n\t"
            "ds_read_b64 v[154:155], v253 offset:176\n\t"
            "ds_read_b64 v[156:157], v253 offset:1824\n\t"
            "ds_read_b64 v[158:159], v253 offset:1920\n\t"
            "s_waitcnt lgkmcnt(14)\n\t"
            "ds_write_b128 v236, v[176:179] offset:128\n\t"
            "v_mov_b64 v[176:177], 0\n\t"
            "v_mov_b64 v[178:179], 0\n\t"
            "s_waitcnt lgkmcnt(10)\n\t"
            "s_nop 0\n\t"
            "v_smfmac_f32_16x16x64_bf16 v[176:179], v[168:171], v[160:167], v255\n\t"
            "ds_read_b128 v[168:171], v254 offset:640\n\t"
            "v_mfma_f32_4x4x4_16b_bf16 v[196:199], v[184:185], v[184:185], v[196:199]\n\t"
            "ds_read_b64 v[160:161], v253 offset:88\n\t"
            "ds_read_b64 v[162:163], v253 offset:184\n\t"
            "ds_read_b64 v[164:165], v253 offset:1832\n\t"
            "ds_read_b64 v[166:167], v253 offset:1928\n\t"
            "s_waitcnt lgkmcnt(10)\n\t"
            "v_smfmac_f32_16x16x64_bf16 v[180:183], v[172:175], v[144:151], v255\n\t"
            "ds_read_b128 v[172:175], v254 offset:704\n\t"
            "v_mfma_f32_4x4x4_16b_bf16 v[196:199], v[186:187], v[186:187], v[196:199]\n\t"
            "ds_write_b128 v237, v[184:187] offset:128\n\t"
            "v_mfma_f32_16x16x32_bf16 v[192:195], v[238:241], v[184:187], v[192:195]\n\t"
            "v_mov_b64 v[184:185], 0\n\t"
            "v_mov_b64 v[186:187], 0\n\t"
            "v_cvt_pk_bf16_f32 v176, v176, v177\n\t"
            "v_cvt_pk_bf16_f32 v177, v178, v179\n\t"
            "v_cvt_pk_bf16_f32 v178, v180, v181\n\t"
            "v_cvt_pk_bf16_f32 v179, v182, v183\n\t"
            "ds_write_b128 v230, v[208:211] offset:3072\n\t"
            "s_waitcnt lgkmcnt(7)\n\t"
            "v_smfmac_f32_16x16x64_bf16 v[184:187], v[168:171], v[152:159], v255\n\t"
            "ds_write_b128 v236, v[176:179] offset:256\n\t"
            "v_mfma_f32_4x4x4_16b_bf16 v[196:199], v[176:177], v[176:177], v[196:199]\n\t"
            "ds_write_b64 v231, v[212:213]\n\t"
            "s_waitcnt lgkmcnt(4)\n\t"
            "v_smfmac_f32_16x16x64_bf16 v[188:191], v[172:175], v[160:167], v255\n\t"
            "s_mov_b32 exec_hi, 0x3fffff\n\t"
            "s_add_u32 m0, s66, 0\n\t"
            "s_add_u32 s49, s79, 0\n\t"
            "buffer_load_dwordx4 v249, s[72:75], s49 offen lds\n\t"
            "s_mov_b32 exec_hi, -1\n\t"
            "v_mfma_f32_4x4x4_16b_bf16 v[196:199], v[178:179], v[178:179], v[196:199]\n\t"
            "s_mov_b32 exec_hi, 0x3fffff\n\t"
            "s_add_u32 m0, s66, 872\n\t"
            "s_add_u32 s49, s68, 196224\n\t"
            "buffer_load_dwordx4 v249, s[72:75], s49 offen lds\n\t"
            "s_mov_b32 exec_hi, -1\n\t"
            "v_mfma_f32_16x16x32_bf16 v[192:195], v[238:241], v[176:179], v[192:195]\n\t"
            "s_mov_b32 exec_hi, 0x3fffff\n\t"
            "s_add_u32 m0, s66, 1744\n\t"
            "s_add_u32 s49, s68, 392448\n\t"
            "buffer_load_dwordx4 v249, s[72:75], s49 offen lds\n\t"
            "s_mov_b32 exec_hi, -1\n\t"
            "s_mov_b32 exec_hi, 0x3fffff\n\t"
            "s_add_u32 m0, s66, 2616\n\t"
            "s_add_u32 s49, s68, 588672\n\t"
            "buffer_load_dwordx4 v249, s[72:75], s49 offen lds\n\t"
            "s_mov_b32 exec_hi, -1\n\t"
            "s_mov_b32 exec_hi, 0x3fffff\n\t"
            "s_add_u32 m0, s66, 3488\n\t"
            "s_add_u32 s49, s68, 784896\n\t"
            "buffer_load_dwordx4 v249, s[72:75], s49 offen lds\n\t"
            "s_mov_b32 exec_hi, -1\n\t"
            "s_mov_b32 exec_hi, 0x3fffff\n\t"
            "s_add_u32 m0, s66, 4360\n\t"
            "s_add_u32 s49, s68, 981120\n\t"
            "buffer_load_dwordx4 v249, s[72:75], s49 offen lds\n\t"
            "s_mov_b32 exec_hi, -1\n\t"
            "v_cvt_pk_bf16_f32 v184, v184, v185\n\t"
            "v_cvt_pk_bf16_f32 v185, v186, v187\n\t"
            "v_cvt_pk_bf16_f32 v186, v188, v189\n\t"
            "v_cvt_pk_bf16_f32 v187, v190, v191\n\t"
            "v_mfma_f32_4x4x4_16b_bf16 v[196:199], v[184:185], v[184:185], v[196:199]\n\t"
            "s_mov_b32 exec_hi, 0x3fffff\n\t"
            "s_add_u32 m0, s66, 5232\n\t"
            "s_add_u32 s49, s68, 1177344\n\t"
            "buffer_load_dwordx4 v249, s[72:75], s49 offen lds\n\t"
            "s_mov_b32 exec_hi, -1\n\t"
            "v_mfma_f32_4x4x4_16b_bf16 v[196:199], v[186:187], v[186:187], v[196:199]\n\t"
            "s_mov_b32 exec_hi, 0x3fffff\n\t"
            "s_add_u32 m0, s66, 6104\n\t"
            "s_add_u32 s49, s68, 1373568\n\t"
            "buffer_load_dwordx4 v249, s[72:75], s49 offen lds\n\t"
            "s_mov_b32 exec_hi, -1\n\t"
            "v_mfma_f32_16x16x32_bf16 v[192:195], v[238:241], v[184:187], v[192:195]\n\t"
            "ds_write_b128 v237, v[184:187] offset:256\n\t"
            "ds_write_b64 v231, v[216:217] offset:1536\n\t"
            "ds_write_b64 v231, v[220:221] offset:3072\n\t"
            "s_mov_b32 exec_hi, 0x3fffff\n\t"
            "s_add_u32 m0, s66, 6976\n\t"
            "s_add_u32 s49, s68, 1569792\n\t"
            "buffer_load_dwordx4 v249, s[72:75], s49 offen lds\n\t"
            "s_mov_b32 exec_hi, -1\n\t"
            "v_cndmask_b32_e64 v193, v196, v197, s[60:61]\n\t"
            "v_cndmask_b32_e64 v193, v193, v198, s[62:63]\n\t"
            "v_cndmask_b32_e64 v193, v193, v199, s[64:65]\n\t"
            "ds_write_b64 v248, v[192:193] offset:18816\n\t"
            "s_waitcnt lgkmcnt(0)\n\t"
            "s_nop 7\n\t"
            "v_mov_b32 v144, s44\n\t"
            "v_mov_b32 v145, -1\n\t"
            "ds_write_b32 v144, v145\n\t"
            "s_cmp_ge_u32 s33, 221184\n\t"
            "s_cbranch_scc1 .Lrp%=\n\t"
            "s_barrier\n\t"
            "s_waitcnt vmcnt(9)\n\t"
            "s_nop 7\n\t"
            "v_add_u32 v232, s67, v242\n\t"
            "v_xor_b32 v233, 64, v232\n\t"
            "v_add_u32 v234, s67, v247\n\t"
            "ds_read_b128 v[144:147], v232\n\t"
            "ds_read_b128 v[148:151], v233\n\t"
            "ds_read_b128 v[152:155], v232 offset:128\n\t"
            "ds_read_b128 v[156:159], v233 offset:128\n\t"
            "ds_read_b128 v[160:163], v232 offset:256\n\t"
            "ds_read_b128 v[164:167], v233 offset:256\n\t"
            "ds_read_b128 v[176:179], v234 offset:18816\n\t"
            "ds_read_b128 v[180:183], v234 offset:19840\n\t"
            "s_waitcnt lgkmcnt(7)\n\t"
            "v_mfma_f32_16x16x32_bf16 v[200:203], v[144:147], v[0:3], 0\n\t"
            "s_waitcnt lgkmcnt(6)\n\t"
            "v_mfma_f32_16x16x32_bf16 v[200:203], v[148:151], v[4:7], v[200:203]\n\t"
            "s_waitcnt lgkmcnt(5)\n\t"
            "v_mfma_f32_16x16x32_bf16 v[200:203], v[152:155], v[8:11], v[200:203]\n\t"
            "s_waitcnt lgkmcnt(4)\n\t"
            "v_mfma_f32_16x16x32_bf16 v[200:203], v[156:159], v[12:15], v[200:203]\n\t"
            "s_waitcnt lgkmcnt(1)\n\t"
            "v_add_f32 v176, v176, v178\n\t"
            "s_waitcnt lgkmcnt(0)\n\t"
            "v_add_f32 v180, v180, v182\n\t"
            "v_mfma_f32_16x16x32_bf16 v[200:203], v[160:163], v[16:19], v[200:203]\n\t"
            "v_add_f32 v176, v176, v180\n\t"
            "v_add_f32 v177, v177, v179\n\t"
            "v_mfma_f32_16x16x32_bf16 v[200:203], v[164:167], v[20:23], v[200:203]\n\t"
            "v_add_f32 v181, v181, v183\n\t"
            "v_mul_f32 v176, 0x3baaaaab, v176\n\t"
            "v_mfma_f32_16x16x32_bf16 v[204:207], v[144:147], v[24:27], 0\n\t"
            "v_add_f32 v177, v177, v181\n\t"
            "v_mul_f32_e64 v176, v176, -v176\n\t"
            "v_mfma_f32_16x16x32_bf16 v[204:207], v[148:151], v[28:31], v[204:207]\n\t"
            "v_fmac_f32 v176, 0x3baaaaab, v177\n\t"
            "v_max_f32 v176, 0, v176\n\t"
            "v_mfma_f32_16x16x32_bf16 v[204:207], v[152:155], v[32:35], v[204:207]\n\t"
            "v_add_f32 v176, 0x3727c5ac, v176\n\t"
            "v_rsq_f32 v176, v176\n\t"
            "v_mfma_f32_16x16x32_bf16 v[204:207], v[156:159], v[36:39], v[204:207]\n\t"
            "ds_write_b32 v246, v176\n\t"
            "v_mfma_f32_16x16x32_bf16 v[204:207], v[160:163], v[40:43], v[204:207]\n\t"
            "ds_read_b128 v[228:231], v245\n\t"
            "v_mfma_f32_16x16x32_bf16 v[204:207], v[164:167], v[44:47], v[204:207]\n\t"
            "v_mfma_f32_16x16x32_bf16 v[208:211], v[144:147], v[48:51], 0\n\t"
            "v_mfma_f32_16x16x32_bf16 v[208:211], v[148:151], v[52:55], v[208:211]\n\t"
            "v_mfma_f32_16x16x32_bf16 v[208:211], v[152:155], v[56:59], v[208:211]\n\t"
            "v_mfma_f32_16x16x32_bf16 v[208:211], v[156:159], v[60:63], v[208:211]\n\t"
            "v_mfma_f32_16x16x32_bf16 v[208:211], v[160:163], v[64:67], v[208:211]\n\t"
            "v_mfma_f32_16x16x32_bf16 v[208:211], v[164:167], v[68:71], v[208:211]\n\t"
            "v_mfma_f32_16x16x32_bf16 v[212:215], v[144:147], v[72:75], 0\n\t"
            "s_waitcnt lgkmcnt(0)\n\t"
            "v_mul_f32 v200, v228, v200\n\t"
            "v_mul_f32 v201, v229, v201\n\t"
            "v_mfma_f32_16x16x32_bf16 v[212:215], v[148:151], v[76:79], v[212:215]\n\t"
            "v_mul_f32 v204, v228, v204\n\t"
            "v_mul_f32 v205, v229, v205\n\t"
            "v_mfma_f32_16x16x32_bf16 v[212:215], v[152:155], v[80:83], v[212:215]\n\t"
            "v_mul_f32 v208, v228, v208\n\t"
            "v_mul_f32 v209, v229, v209\n\t"
            "v_mfma_f32_16x16x32_bf16 v[212:215], v[156:159], v[84:87], v[212:215]\n\t"
            "v_mul_f32 v202, v230, v202\n\t"
            "v_mul_f32 v203, v231, v203\n\t"
            "v_mfma_f32_16x16x32_bf16 v[212:215], v[160:163], v[88:91], v[212:215]\n\t"
            "v_cvt_pk_bf16_f32 v200, v200, v201\n\t"
            "v_mul_f32 v206, v230, v206\n\t"
            "v_mfma_f32_16x16x32_bf16 v[212:215], v[164:167], v[92:95], v[212:215]\n\t"
            "v_mul_f32 v207, v231, v207\n\t"
            "v_cvt_pk_bf16_f32 v204, v204, v205\n\t"
            "v_mfma_f32_16x16x32_bf16 v[216:219], v[144:147], v[96:99], 0\n\t"
            "v_mul_f32 v210, v230, v210\n\t"
            "v_mul_f32 v211, v231, v211\n\t"
            "v_mfma_f32_16x16x32_bf16 v[216:219], v[148:151], v[100:103], v[216:219]\n\t"
            "v_cvt_pk_bf16_f32 v208, v208, v209\n\t"
            "v_cvt_pk_bf16_f32 v201, v202, v203\n\t"
            "v_mfma_f32_16x16x32_bf16 v[216:219], v[152:155], v[104:107], v[216:219]\n\t"
            "v_mul_f32 v212, v228, v212\n\t"
            "v_mul_f32 v213, v229, v213\n\t"
            "v_mfma_f32_16x16x32_bf16 v[216:219], v[156:159], v[108:111], v[216:219]\n\t"
            "v_mul_f32 v214, v230, v214\n\t"
            "v_mul_f32 v215, v231, v215\n\t"
            "v_mfma_f32_16x16x32_bf16 v[216:219], v[160:163], v[112:115], v[216:219]\n\t"
            "v_cvt_pk_bf16_f32 v212, v212, v213\n\t"
            "v_cvt_pk_bf16_f32 v205, v206, v207\n\t"
            "v_mfma_f32_16x16x32_bf16 v[216:219], v[164:167], v[116:119], v[216:219]\n\t"
            "v_cvt_pk_bf16_f32 v209, v210, v211\n\t"
            "v_cvt_pk_bf16_f32 v213, v214, v215\n\t"
            "v_mfma_f32_16x16x32_bf16 v[220:223], v[144:147], v[120:123], 0\n\t"
            "v_mfma_f32_16x16x32_bf16 v[220:223], v[148:151], v[124:127], v[220:223]\n\t"
            "v_mfma_f32_16x16x32_bf16 v[220:223], v[152:155], v[128:131], v[220:223]\n\t"
            "v_mfma_f32_16x16x32_bf16 v[220:223], v[156:159], v[132:135], v[220:223]\n\t"
            "s_nop 1\n\t"
            "v_mul_f32 v216, v228, v216\n\t"
            "v_mfma_f32_16x16x32_bf16 v[220:223], v[160:163], v[136:139], v[220:223]\n\t"
            "v_mul_f32 v217, v229, v217\n\t"
            "v_mul_f32 v218, v230, v218\n\t"
            "v_mfma_f32_16x16x32_bf16 v[220:223], v[164:167], v[140:143], v[220:223]\n\t"
            "v_mul_f32 v219, v231, v219\n\t"
            "v_cvt_pk_bf16_f32 v216, v216, v217\n\t"
            "v_cvt_pk_bf16_f32 v217, v218, v219\n\t"
            "s_nop 4\n\t"
            "v_mul_f32 v220, v228, v220\n\t"
            "v_mul_f32 v221, v229, v221\n\t"
            "v_mul_f32 v222, v230, v222\n\t"
            "v_mul_f32 v223, v231, v223\n\t"
            "v_cvt_pk_bf16_f32 v220, v220, v221\n\t"
            "v_cvt_pk_bf16_f32 v221, v222, v223\n\t"
            "s_nop 7\n\t"
            "s_add_u32 s45, s46, s27\n\t"
            "s_mov_b32 s46, s47\n\t"
            "s_mov_b32 s47, s68\n\t"
            "s_xor_b32 s48, s48, s71\n\t"
            "s_xor_b32 s67, s67, s71\n\t"
            "s_add_u32 s76, s76, 4\n\t"
            "s_lshl_b32 s56, s56, 2\n\t"
            "s_lshr_b32 s49, s26, 30\n\t"
            "s_or_b32 s56, s56, s49\n\t"
            "ds_read_b128 v[168:171], v254\n\t"
            "ds_read_b128 v[172:175], v254 offset:64\n\t"
            "s_waitcnt vmcnt(0)\n\t"
            "ds_read_b64 v[144:145], v253\n\t"
            "ds_read_b64 v[146:147], v253 offset:96\n\t"
            "ds_read_b64 v[148:149], v253 offset:1744\n\t"
            "ds_read_b64 v[150:151], v253 offset:1840\n\t"
            "ds_read_b64 v[152:153], v253 offset:8\n\t"
            "ds_read_b64 v[154:155], v253 offset:104\n\t"
            "ds_read_b64 v[156:157], v253 offset:1752\n\t"
            "ds_read_b64 v[158:159], v253 offset:1848\n\t"
            "ds_read_b64 v[160:161], v253 offset:16\n\t"
            "ds_read_b64 v[162:163], v253 offset:112\n\t"
            "ds_read_b64 v[164:165], v253 offset:1760\n\t"
            "ds_read_b64 v[166:167], v253 offset:1856\n\t"
            "s_waitcnt lgkmcnt(0)\n\t"
            "s_nop 7\n\t"
            ".p2align 6\n\t"
            ".Lwc_loop%=:\n\t"
            "s_setprio 3\n\t"
            "ds_read_b128 v[228:231], v245 offset:64\n\t"
            "v_mov_b64 v[176:177], 0\n\t"
            "v_mov_b64 v[178:179], 0\n\t"
            "v_mov_b32 v249, s76\n\t"
            "v_mov_b64 v[180:181], 0\n\t"
            "s_waitcnt lgkmcnt(9)\n\t"
            "v_smfmac_f32_16x16x64_bf16 v[176:179], v[168:171], v[144:151], v255\n\t"
            "ds_read_b128 v[168:171], v254 offset:128\n\t"
            "v_mov_b64 v[182:183], 0\n\t"
            "v_mov_b64 v[184:185], 0\n\t"
            "v_mov_b64 v[186:187], 0\n\t"
            "s_waitcnt lgkmcnt(6)\n\t"
            "v_smfmac_f32_16x16x64_bf16 v[180:183], v[172:175], v[152:159], v255\n\t"
            "ds_read_b32 v249, v249\n\t"
            "v_mov_b64 v[188:189], 0\n\t"
            "v_mov_b64 v[190:191], 0\n\t"
            "v_cvt_pk_bf16_f32 v176, v176, v177\n\t"
            "v_cvt_pk_bf16_f32 v177, v178, v179\n\t"
            "v_add_u32 v236, s67, v250\n\t"
            "v_add_u32 v232, s48, v242\n\t"
            "v_mfma_f32_4x4x4_16b_bf16 v[196:199], v[176:177], v[176:177], 0\n\t"
            "ds_read_b64 v[144:145], v253 offset:24\n\t"
            "v_cvt_pk_bf16_f32 v178, v180, v181\n\t"
            "v_cvt_pk_bf16_f32 v179, v182, v183\n\t"
            "s_waitcnt lgkmcnt(3)\n\t"
            "s_barrier\n\t"
            "v_mov_b64 v[180:181], 0\n\t"
            "v_mov_b64 v[182:183], 0\n\t"
            "v_xor_b32 v233, 64, v232\n\t"
            "v_add_u32 v248, s67, v251\n\t"
            "v_add_u32 v234, s67, v247\n\t"
            "ds_read_b64 v[146:147], v253 offset:120\n\t"
            "ds_read_b64 v[148:149], v253 offset:1768\n\t"
            "ds_read_b64 v[150:151], v253 offset:1864\n\t"
            "s_waitcnt lgkmcnt(5)\n\t"
            "v_smfmac_f32_16x16x64_bf16 v[184:187], v[168:171], v[160:167], v255\n\t"
            "ds_read_b128 v[172:175], v254 offset:192\n\t"
            "v_mfma_f32_4x4x4_16b_bf16 v[196:199], v[178:179], v[178:179], v[196:199]\n\t"
            "ds_read_b64 v[152:153], v253 offset:32\n\t"
            "v_mfma_f32_16x16x32_bf16 v[192:195], v[238:241], v[176:179], 0\n\t"
            "ds_read_b64 v[154:155], v253 offset:128\n\t"
            "s_waitcnt lgkmcnt(7)\n\t"
            "v_readfirstlane_b32 s26, v249\n\t"
            "ds_read_b64 v[156:157], v253 offset:1776\n\t"
            "s_and_b32 s68, s26, 0x7ffffff\n\t"
            "s_add_u32 s68, s68, s78\n\t"
            "s_and_b32 s69, s26, 0x38000000\n\t"
            "s_or_b32 s69, s69, s39\n\t"
            "s_bitcmp1_b32 s26, 27\n\t"
            "s_cselect_b32 s79, 2.0, s68\n\t"
            "ds_read_b64 v[158:159], v253 offset:1872\n\t"
            "s_and_b32 s57, s56, 3\n\t"
            "s_lshl_b32 s58, s57, 3\n\t"
            "s_lshr_b32 m0, s54, s58\n\t"
            "ds_read_b128 v[168:171], v254 offset:256\n\t"
            "s_lshl_b32 s58, s57, 4\n\t"
            "s_lshr_b64 s[36:37], s[50:51], s58\n\t"
            "s_and_b32 s57, s36, 0xffff\n\t"
            "s_add_u32 s57, s57, s67\n\t"
            "ds_read_b64 v[160:161], v253 offset:40\n\t"
            "s_lshr_b64 s[36:37], s[52:53], s58\n\t"
            "s_and_b32 s58, s36, 0xffff\n\t"
            "s_add_u32 s58, s58, s67\n\t"
            "ds_read_b64 v[162:163], v253 offset:136\n\t"
            "s_and_b32 s59, s50, 0xffff\n\t"
            "s_add_u32 s59, s59, s67\n\t"
            "s_and_b32 s38, s52, 0xffff\n\t"
            "s_add_u32 s38, s38, s67\n\t"
            "s_waitcnt lgkmcnt(7)\n\t"
            "v_smfmac_f32_16x16x64_bf16 v[188:191], v[172:175], v[144:151], v255\n\t"
            "ds_read_b64 v[164:165], v253 offset:1784\n\t"
            "v_writelane_b32 v236, s59, m0\n\t"
            "v_writelane_b32 v236, s57, 63\n\t"
            "v_cvt_pk_bf16_f32 v184, v184, v185\n\t"
            "v_cvt_pk_bf16_f32 v185, v186, v187\n\t"
            "v_xor_b32 v237, 16, v236\n\t"
            "v_writelane_b32 v248, s38, m0\n\t"
            "v_mfma_f32_4x4x4_16b_bf16 v[196:199], v[184:185], v[184:185], v[196:199]\n\t"
            "ds_read_b64 v[166:167], v253 offset:1880\n\t"
            "v_cvt_pk_bf16_f32 v186, v188, v189\n\t"
            "v_cvt_pk_bf16_f32 v187, v190, v191\n\t"
            "v_mov_b64 v[188:189], 0\n\t"
            "v_mov_b64 v[190:191], 0\n\t"
            "v_writelane_b32 v248, s58, 63\n\t"
            "ds_read_b128 v[172:175], v254 offset:320\n\t"
            "ds_read_b64 v[144:145], v253 offset:48\n\t"
            "ds_read_b64 v[146:147], v253 offset:144\n\t"
            "ds_read_b64 v[148:149], v253 offset:1792\n\t"
            "ds_read_b64 v[150:151], v253 offset:1888\n\t"
            "ds_read_b32 v249, v252 offset:1024\n\t"
            "s_waitcnt lgkmcnt(14)\n\t"
            "ds_write_b128 v236, v[176:179]\n\t"
            "v_mov_b64 v[176:177], 0\n\t"
            "v_mov_b64 v[178:179], 0\n\t"
            "s_waitcnt lgkmcnt(11)\n\t"
            "s_nop 0\n\t"
            "v_smfmac_f32_16x16x64_bf16 v[176:179], v[168:171], v[152:159], v255\n\t"
            "ds_read_b128 v[168:171], v254 offset:384\n\t"
            "v_mfma_f32_4x4x4_16b_bf16 v[196:199], v[186:187], v[186:187], v[196:199]\n\t"
            "ds_read_b64 v[152:153], v253 offset:56\n\t"
            "v_mfma_f32_16x16x32_bf16 v[192:195], v[238:241], v[184:187], v[192:195]\n\t"
            "ds_read_b64 v[154:155], v253 offset:152\n\t"
            "s_waitcnt lgkmcnt(9)\n\t"
            "v_smfmac_f32_16x16x64_bf16 v[180:183], v[172:175], v[160:167], v255\n\t"
            "ds_read_b64 v[156:157], v253 offset:1800\n\t"
            "ds_read_b64 v[158:159], v253 offset:1896\n\t"
            "v_cvt_pk_bf16_f32 v176, v176, v177\n\t"
            "v_cvt_pk_bf16_f32 v177, v178, v179\n\t"
            "s_nop 1\n\t"
            "v_mfma_f32_4x4x4_16b_bf16 v[196:199], v[176:177], v[176:177], v[196:199]\n\t"
            "ds_read_b128 v[172:175], v254 offset:448\n\t"
            "s_waitcnt lgkmcnt(7)\n\t"
            "v_and_b32 v249, s69, v249\n\t"
            "v_cvt_pk_bf16_f32 v178, v180, v181\n\t"
            "v_cvt_pk_bf16_f32 v179, v182, v183\n\t"
            "v_mov_b64 v[180:181], 0\n\t"
            "v_mov_b64 v[182:183], 0\n\t"
            "ds_read_b64 v[160:161], v253 offset:64\n\t"
            "ds_read_b64 v[162:163], v253 offset:160\n\t"
            "ds_read_b64 v[164:165], v253 offset:1808\n\t"
            "ds_read_b64 v[166:167], v253 offset:1904\n\t"
            "ds_write_b128 v237, v[184:187]\n\t"
            "v_mov_b64 v[184:185], 0\n\t"
            "v_mov_b64 v[186:187], 0\n\t"
            "s_waitcnt lgkmcnt(10)\n\t"
            "s_nop 0\n\t"
            "v_smfmac_f32_16x16x64_bf16 v[184:187], v[168:171], v[144:151], v255\n\t"
            "ds_read_b128 v[168:171], v254 offset:512\n\t"
            "v_mfma_f32_4x4x4_16b_bf16 v[196:199], v[178:179], v[178:179], v[196:199]\n\t"
            "ds_read_b64 v[144:145], v253 offset:72\n\t"
            "v_mfma_f32_16x16x32_bf16 v[192:195], v[238:241], v[176:179], v[192:195]\n\t"
            "ds_read_b64 v[146:147], v253 offset:168\n\t"
            "s_waitcnt lgkmcnt(8)\n\t"
            "v_smfmac_f32_16x16x64_bf16 v[188:191], v[172:175], v[152:159], v255\n\t"
            "ds_read_b64 v[148:149], v253 offset:1816\n\t"
            "ds_read_b64 v[150:151], v253 offset:1912\n\t"
            "v_cvt_pk_bf16_f32 v184, v184, v185\n\t"
            "v_cvt_pk_bf16_f32 v185, v186, v187\n\t"
            "s_nop 1\n\t"
            "v_mfma_f32_4x4x4_16b_bf16 v[196:199], v[184:185], v[184:185], v[196:199]\n\t"
            "ds_read_b128 v[172:175], v254 offset:576\n\t"
            "ds_read_b64 v[152:153], v253 offset:80\n\t"
            "ds_read_b64 v[154:155], v253 offset:176\n\t"
            "v_cvt_pk_bf16_f32 v186, v188, v189\n\t"
            "v_cvt_pk_bf16_f32 v187, v190, v191\n\t"
            "v_mov_b64 v[188:189], 0\n\t"
            "v_mov_b64 v[190:191], 0\n\t"
            "ds_read_b64 v[156:157], v253 offset:1824\n\t"
            "ds_read_b64 v[158:159], v253 offset:1920\n\t"
            "s_waitcnt lgkmcnt(14)\n\t"
            "ds_write_b128 v236, v[176:179] offset:128\n\t"
            "v_mov_b64 v[176:177], 0\n\t"
            "v_mov_b64 v[178:179], 0\n\t"
            "s_waitcnt lgkmcnt(10)\n\t"
            "s_nop 0\n\t"
            "v_smfmac_f32_16x16x64_bf16 v[176:179], v[168:171], v[160:167], v255\n\t"
            "ds_read_b128 v[168:171], v254 offset:640\n\t"
            "v_mfma_f32_4x4x4_16b_bf16 v[196:199], v[186:187], v[186:187], v[196:199]\n\t"
            "ds_read_b64 v[160:161], v253 offset:88\n\t"
            "v_mfma_f32_16x16x32_bf16 v[192:195], v[238:241], v[184:187], v[192:195]\n\t"
            "ds_read_b64 v[162:163], v253 offset:184\n\t"
            "s_waitcnt lgkmcnt(8)\n\t"
            "v_smfmac_f32_16x16x64_bf16 v[180:183], v[172:175], v[144:151], v255\n\t"
            "ds_read_b64 v[164:165], v253 offset:1832\n\t"
            "ds_read_b64 v[166:167], v253 offset:1928\n\t"
            "v_cvt_pk_bf16_f32 v176, v176, v177\n\t"
            "v_cvt_pk_bf16_f32 v177, v178, v179\n\t"
            "s_nop 1\n\t"
            "v_mfma_f32_4x4x4_16b_bf16 v[196:199], v[176:177], v[176:177], v[196:199]\n\t"
            "ds_read_b128 v[172:175], v254 offset:704\n\t"
            "ds_read_b128 v[144:147], v232 offset:6144\n\t"
            "ds_read_b128 v[148:151], v233 offset:6144\n\t"
            "v_cvt_pk_bf16_f32 v178, v180, v181\n\t"
            "v_cvt_pk_bf16_f32 v179, v182, v183\n\t"
            "ds_read_b128 v[180:183], v233 offset:12288\n\t"
            "ds_write_b128 v237, v[184:187] offset:128\n\t"
            "v_mov_b64 v[184:185], 0\n\t"
            "v_mov_b64 v[186:187], 0\n\t"
            "s_waitcnt lgkmcnt(9)\n\t"
            "s_nop 0\n\t"
            "v_smfmac_f32_16x16x64_bf16 v[184:187], v[168:171], v[152:159], v255\n\t"
            "ds_read_b128 v[152:155], v232 offset:6272\n\t"
            "v_mfma_f32_4x4x4_16b_bf16 v[196:199], v[178:179], v[178:179], v[196:199]\n\t"
            "ds_read_b128 v[156:159], v233 offset:6272\n\t"
            "v_mfma_f32_16x16x32_bf16 v[192:195], v[238:241], v[176:179], v[192:195]\n\t"
            "ds_write_b128 v236, v[176:179] offset:256\n\t"
            "s_waitcnt lgkmcnt(7)\n\t"
            "v_smfmac_f32_16x16x64_bf16 v[188:191], v[172:175], v[160:167], v255\n\t"
            "ds_read_b128 v[160:163], v232 offset:6400\n\t"
            "ds_read_b128 v[164:167], v233 offset:6400\n\t"
            "v_cvt_pk_bf16_f32 v184, v184, v185\n\t"
            "v_cvt_pk_bf16_f32 v185, v186, v187\n\t"
            "s_nop 1\n\t"
            "v_mfma_f32_4x4x4_16b_bf16 v[196:199], v[184:185], v[184:185], v[196:199]\n\t"
            "ds_read_b128 v[176:179], v232 offset:12288\n\t"
            "s_mov_b32 exec_hi, 0x3fffff\n\t"
            "s_add_u32 m0, s66, 0\n\t"
            "s_add_u32 s49, s79, 0\n\t"
            "buffer_load_dwordx4 v249, s[72:75], s49 offen lds\n\t"
            "s_mov_b32 exec_hi, -1\n\t"
            "v_cvt_pk_bf16_f32 v186, v188, v189\n\t"
            "v_cvt_pk_bf16_f32 v187, v190, v191\n\t"
            "s_nop 1\n\t"
            "v_mfma_f32_4x4x4_16b_bf16 v[196:199], v[186:187], v[186:187], v[196:199]\n\t"
            "ds_read_b128 v[188:191], v233 offset:12416\n\t"
            "v_mfma_f32_16x16x32_bf16 v[192:195], v[238:241], v[184:187], v[192:195]\n\t"
            "ds_write_b128 v237, v[184:187] offset:256\n\t"
            "s_waitcnt lgkmcnt(11)\n\t"
            "v_mfma_f32_16x16x32_bf16 v[224:227], v[144:147], v[0:3], 0\n\t"
            "s_setprio 3\n\t"
            "ds_read_b128 v[184:187], v232 offset:12416\n\t"
            "s_waitcnt lgkmcnt(11)\n\t"
            "v_mfma_f32_16x16x32_bf16 v[224:227], v[148:151], v[4:7], v[224:227]\n\t"
            "s_mov_b32 exec_hi, 0x3fffff\n\t"
            "s_add_u32 m0, s66, 872\n\t"
            "s_add_u32 s49, s68, 196224\n\t"
            "buffer_load_dwordx4 v249, s[72:75], s49 offen lds\n\t"
            "s_mov_b32 exec_hi, -1\n\t"
            "s_waitcnt lgkmcnt(8)\n\t"
            "v_mfma_f32_16x16x32_bf16 v[224:227], v[152:155], v[8:11], v[224:227]\n\t"
            "v_cndmask_b32_e64 v193, v196, v197, s[60:61]\n\t"
            "v_cndmask_b32_e64 v193, v193, v198, s[62:63]\n\t"
            "s_waitcnt lgkmcnt(7)\n\t"
            "v_mfma_f32_16x16x32_bf16 v[224:227], v[156:159], v[12:15], v[224:227]\n\t"
            "v_cndmask_b32_e64 v193, v193, v199, s[64:65]\n\t"
            "s_waitcnt lgkmcnt(5)\n\t"
            "v_mfma_f32_16x16x32_bf16 v[224:227], v[160:163], v[16:19], v[224:227]\n\t"
            "ds_read_b128 v[196:199], v233 offset:12544\n\t"
            "s_waitcnt lgkmcnt(5)\n\t"
            "v_mfma_f32_16x16x32_bf16 v[224:227], v[164:167], v[20:23], v[224:227]\n\t"
            "ds_write_b64 v248, v[192:193] offset:18816\n\t"
            "v_mfma_f32_16x16x32_bf16 v[168:171], v[144:147], v[24:27], 0\n\t"
            "ds_read_b128 v[192:195], v232 offset:12544\n\t"
            "v_add_u32 v232, s67, v242\n\t"
            "v_mfma_f32_16x16x32_bf16 v[168:171], v[148:151], v[28:31], v[168:171]\n\t"
            "s_mov_b32 exec_hi, 0x3fffff\n\t"
            "s_add_u32 m0, s66, 1744\n\t"
            "s_add_u32 s49, s68, 392448\n\t"
            "buffer_load_dwordx4 v249, s[72:75], s49 offen lds\n\t"
            "s_mov_b32 exec_hi, -1\n\t"
            "v_mfma_f32_16x16x32_bf16 v[168:171], v[152:155], v[32:35], v[168:171]\n\t"
            "v_mul_f32 v224, v228, v224\n\t"
            "v_mul_f32 v225, v229, v225\n\t"
            "v_mfma_f32_16x16x32_bf16 v[168:171], v[156:159], v[36:39], v[168:171]\n\t"
            "v_mul_f32 v226, v230, v226\n\t"
            "v_mul_f32 v227, v231, v227\n\t"
            "v_mfma_f32_16x16x32_bf16 v[168:171], v[160:163], v[40:43], v[168:171]\n\t"
            "v_cvt_pk_bf16_f32 v202, v224, v225\n\t"
            "v_cvt_pk_bf16_f32 v203, v226, v227\n\t"
            "v_mfma_f32_16x16x32_bf16 v[168:171], v[164:167], v[44:47], v[168:171]\n\t"
            "s_mov_b32 exec_hi, 0x3fffff\n\t"
            "s_add_u32 m0, s66, 2616\n\t"
            "s_add_u32 s49, s68, 588672\n\t"
            "buffer_load_dwordx4 v249, s[72:75], s49 offen lds\n\t"
            "s_mov_b32 exec_hi, -1\n\t"
            "v_mfma_f32_16x16x32_bf16 v[172:175], v[144:147], v[48:51], 0\n\t"
            "s_mov_b32 exec_hi, 0x3fffff\n\t"
            "s_add_u32 m0, s66, 3488\n\t"
            "s_add_u32 s49, s68, 784896\n\t"
            "buffer_load_dwordx4 v249, s[72:75], s49 offen lds\n\t"
            "s_mov_b32 exec_hi, -1\n\t"
            "v_mfma_f32_16x16x32_bf16 v[172:175], v[148:151], v[52:55], v[172:175]\n\t"
            "v_mul_f32 v168, v228, v168\n\t"
            "v_mul_f32 v169, v229, v169\n\t"
            "v_mfma_f32_16x16x32_bf16 v[172:175], v[152:155], v[56:59], v[172:175]\n\t"
            "v_mul_f32 v170, v230, v170\n\t"
            "v_mul_f32 v171, v231, v171\n\t"
            "v_mfma_f32_16x16x32_bf16 v[172:175], v[156:159], v[60:63], v[172:175]\n\t"
            "v_cvt_pk_bf16_f32 v206, v168, v169\n\t"
            "v_cvt_pk_bf16_f32 v207, v170, v171\n\t"
            "v_mfma_f32_16x16x32_bf16 v[172:175], v[160:163], v[64:67], v[172:175]\n\t"
            "s_mov_b32 exec_hi, 0x3fffff\n\t"
            "s_add_u32 m0, s66, 4360\n\t"
            "s_add_u32 s49, s68, 981120\n\t"
            "buffer_load_dwordx4 v249, s[72:75], s49 offen lds\n\t"
            "s_mov_b32 exec_hi, -1\n\t"
            "v_mfma_f32_16x16x32_bf16 v[172:175], v[164:167], v[68:71], v[172:175]\n\t"
            "s_mov_b32 exec_hi, 0x3fffff\n\t"
            "s_add_u32 m0, s66, 5232\n\t"
            "s_add_u32 s49, s68, 1177344\n\t"
            "buffer_load_dwordx4 v249, s[72:75], s49 offen lds\n\t"
            "s_mov_b32 exec_hi, -1\n\t"
            "v_mfma_f32_16x16x32_bf16 v[224:227], v[144:147], v[72:75], 0\n\t"
            "s_mov_b32 exec_hi, 0x3fffff\n\t"
            "s_add_u32 m0, s66, 6104\n\t"
            "s_add_u32 s49, s68, 1373568\n\t"
            "buffer_load_dwordx4 v249, s[72:75], s49 offen lds\n\t"
            "s_mov_b32 exec_hi, -1\n\t"
            "v_mfma_f32_16x16x32_bf16 v[224:227], v[148:151], v[76:79], v[224:227]\n\t"
            "v_mul_f32 v172, v228, v172\n\t"
            "v_mul_f32 v173, v229, v173\n\t"
            "v_mfma_f32_16x16x32_bf16 v[224:227], v[152:155], v[80:83], v[224:227]\n\t"
            "v_mul_f32 v174, v230, v174\n\t"
            "v_mul_f32 v175, v231, v175\n\t"
            "v_mfma_f32_16x16x32_bf16 v[224:227], v[156:159], v[84:87], v[224:227]\n\t"
            "v_cvt_pk_bf16_f32 v210, v172, v173\n\t"
            "v_cvt_pk_bf16_f32 v211, v174, v175\n\t"
            "v_mfma_f32_16x16x32_bf16 v[224:227], v[160:163], v[88:91], v[224:227]\n\t"
            "s_mov_b32 exec_hi, 0x3fffff\n\t"
            "s_add_u32 m0, s66, 6976\n\t"
            "s_add_u32 s49, s68, 1569792\n\t"
            "buffer_load_dwordx4 v249, s[72:75], s49 offen lds\n\t"
            "s_mov_b32 exec_hi, -1\n\t"
            "v_mfma_f32_16x16x32_bf16 v[224:227], v[164:167], v[92:95], v[224:227]\n\t"
            "s_add_u32 s45, s46, s27\n\t"
            "s_mov_b32 s46, s47\n\t"
            "s_mov_b32 s47, s68\n\t"
            "v_mfma_f32_16x16x32_bf16 v[168:171], v[144:147], v[96:99], 0\n\t"
            "s_xor_b32 s48, s48, s71\n\t"
            "s_xor_b32 s67, s67, s71\n\t"
            "s_add_u32 s76, s76, 4\n\t"
            "v_mfma_f32_16x16x32_bf16 v[168:171], v[148:151], v[100:103], v[168:171]\n\t"
            "s_lshl_b32 s56, s56, 2\n\t"
            "s_lshr_b32 s49, s26, 30\n\t"
            "s_or_b32 s56, s56, s49\n\t"
            "v_mfma_f32_16x16x32_bf16 v[168:171], v[152:155], v[104:107], v[168:171]\n\t"
            "v_mul_f32 v224, v228, v224\n\t"
            "v_mul_f32 v225, v229, v225\n\t"
            "v_mfma_f32_16x16x32_bf16 v[168:171], v[156:159], v[108:111], v[168:171]\n\t"
            "v_mul_f32 v226, v230, v226\n\t"
            "v_mul_f32 v227, v231, v227\n\t"
            "v_mfma_f32_16x16x32_bf16 v[168:171], v[160:163], v[112:115], v[168:171]\n\t"
            "v_cvt_pk_bf16_f32 v214, v224, v225\n\t"
            "v_cvt_pk_bf16_f32 v215, v226, v227\n\t"
            "v_mfma_f32_16x16x32_bf16 v[168:171], v[164:167], v[116:119], v[168:171]\n\t"
            "ds_write_b128 v243, v[200:203]\n\t"
            "v_mfma_f32_16x16x32_bf16 v[172:175], v[144:147], v[120:123], 0\n\t"
            "ds_write_b128 v243, v[204:207] offset:1536\n\t"
            "v_mfma_f32_16x16x32_bf16 v[172:175], v[148:151], v[124:127], v[172:175]\n\t"
            "ds_write_b128 v243, v[208:211] offset:3072\n\t"
            "v_mfma_f32_16x16x32_bf16 v[172:175], v[152:155], v[128:131], v[172:175]\n\t"
            "ds_write_b128 v243, v[212:215] offset:4608\n\t"
            "v_mfma_f32_16x16x32_bf16 v[172:175], v[156:159], v[132:135], v[172:175]\n\t"
            "v_mul_f32 v168, v228, v168\n\t"
            "v_mul_f32 v169, v229, v169\n\t"
            "v_mfma_f32_16x16x32_bf16 v[172:175], v[160:163], v[136:139], v[172:175]\n\t"
            "v_mul_f32 v170, v230, v170\n\t"
            "v_mul_f32 v171, v231, v171\n\t"
            "v_mfma_f32_16x16x32_bf16 v[172:175], v[164:167], v[140:143], v[172:175]\n\t"
            "v_cvt_pk_bf16_f32 v218, v168, v169\n\t"
            "v_cvt_pk_bf16_f32 v219, v170, v171\n\t"
            "s_waitcnt lgkmcnt(10)\n\t"
            "v_mfma_f32_16x16x32_bf16 v[224:227], v[176:179], v[0:3], 0\n\t"
            "ds_write_b128 v243, v[216:219] offset:6144\n\t"
            "v_mfma_f32_16x16x32_bf16 v[224:227], v[180:183], v[4:7], v[224:227]\n\t"
            "v_xor_b32 v233, 64, v232\n\t"
            "s_waitcnt lgkmcnt(8)\n\t"
            "v_mfma_f32_16x16x32_bf16 v[224:227], v[184:187], v[8:11], v[224:227]\n\t"
            "v_mul_f32 v172, v228, v172\n\t"
            "v_mul_f32 v173, v229, v173\n\t"
            "v_mfma_f32_16x16x32_bf16 v[224:227], v[188:191], v[12:15], v[224:227]\n\t"
            "v_mul_f32 v174, v230, v174\n\t"
            "v_mul_f32 v175, v231, v175\n\t"
            "s_waitcnt lgkmcnt(5)\n\t"
            "v_mfma_f32_16x16x32_bf16 v[224:227], v[192:195], v[16:19], v[224:227]\n\t"
            "ds_read_b128 v[228:231], v245 offset:128\n\t"
            "v_cvt_pk_bf16_f32 v222, v172, v173\n\t"
            "v_mfma_f32_16x16x32_bf16 v[224:227], v[196:199], v[20:23], v[224:227]\n\t"
            "v_cvt_pk_bf16_f32 v223, v174, v175\n\t"
            "v_mfma_f32_16x16x32_bf16 v[168:171], v[176:179], v[24:27], 0\n\t"
            "ds_write_b128 v243, v[220:223] offset:7680\n\t"
            "v_mfma_f32_16x16x32_bf16 v[168:171], v[180:183], v[28:31], v[168:171]\n\t"
            "v_mfma_f32_16x16x32_bf16 v[168:171], v[184:187], v[32:35], v[168:171]\n\t"
            "v_mfma_f32_16x16x32_bf16 v[168:171], v[188:191], v[36:39], v[168:171]\n\t"
            "v_mfma_f32_16x16x32_bf16 v[168:171], v[192:195], v[40:43], v[168:171]\n\t"
            "v_mfma_f32_16x16x32_bf16 v[168:171], v[196:199], v[44:47], v[168:171]\n\t"
            "s_waitcnt lgkmcnt(1)\n\t"
            "v_mul_f32 v224, v228, v224\n\t"
            "v_mfma_f32_16x16x32_bf16 v[172:175], v[176:179], v[48:51], 0\n\t"
            "v_mul_f32 v225, v229, v225\n\t"
            "v_mul_f32 v226, v230, v226\n\t"
            "v_mfma_f32_16x16x32_bf16 v[172:175], v[180:183], v[52:55], v[172:175]\n\t"
            "v_mul_f32 v227, v231, v227\n\t"
            "v_cvt_pk_bf16_f32 v224, v224, v225\n\t"
            "v_mfma_f32_16x16x32_bf16 v[172:175], v[184:187], v[56:59], v[172:175]\n\t"
            "v_cvt_pk_bf16_f32 v225, v226, v227\n\t"
            "v_mul_f32 v168, v228, v168\n\t"
            "v_mfma_f32_16x16x32_bf16 v[172:175], v[188:191], v[60:63], v[172:175]\n\t"
            "ds_write_b64 v244, v[224:225]\n\t"
            "v_mfma_f32_16x16x32_bf16 v[172:175], v[192:195], v[64:67], v[172:175]\n\t"
            "v_mul_f32 v169, v229, v169\n\t"
            "v_mul_f32 v170, v230, v170\n\t"
            "v_mfma_f32_16x16x32_bf16 v[172:175], v[196:199], v[68:71], v[172:175]\n\t"
            "v_mul_f32 v171, v231, v171\n\t"
            "v_cvt_pk_bf16_f32 v168, v168, v169\n\t"
            "v_mfma_f32_16x16x32_bf16 v[224:227], v[176:179], v[72:75], 0\n\t"
            "v_cvt_pk_bf16_f32 v169, v170, v171\n\t"
            "v_mfma_f32_16x16x32_bf16 v[224:227], v[180:183], v[76:79], v[224:227]\n\t"
            "ds_write_b64 v244, v[168:169] offset:1536\n\t"
            "v_mfma_f32_16x16x32_bf16 v[224:227], v[184:187], v[80:83], v[224:227]\n\t"
            "s_nop 0\n\t"
            "v_mul_f32 v172, v228, v172\n\t"
            "v_mul_f32 v173, v229, v173\n\t"
            "v_mfma_f32_16x16x32_bf16 v[224:227], v[188:191], v[84:87], v[224:227]\n\t"
            "v_mul_f32 v174, v230, v174\n\t"
            "v_mul_f32 v175, v231, v175\n\t"
            "v_mfma_f32_16x16x32_bf16 v[224:227], v[192:195], v[88:91], v[224:227]\n\t"
            "v_cvt_pk_bf16_f32 v172, v172, v173\n\t"
            "v_cvt_pk_bf16_f32 v173, v174, v175\n\t"
            "v_mfma_f32_16x16x32_bf16 v[224:227], v[196:199], v[92:95], v[224:227]\n\t"
            "ds_write_b64 v244, v[172:173] offset:3072\n\t"
            "v_mfma_f32_16x16x32_bf16 v[168:171], v[176:179], v[96:99], 0\n\t"
            "v_mfma_f32_16x16x32_bf16 v[168:171], v[180:183], v[100:103], v[168:171]\n\t"
            "v_mfma_f32_16x16x32_bf16 v[168:171], v[184:187], v[104:107], v[168:171]\n\t"
            "v_mfma_f32_16x16x32_bf16 v[168:171], v[188:191], v[108:111], v[168:171]\n\t"
            "v_mfma_f32_16x16x32_bf16 v[168:171], v[192:195], v[112:115], v[168:171]\n\t"
            "s_nop 1\n\t"
            "v_mul_f32 v224, v228, v224\n\t"
            "v_mfma_f32_16x16x32_bf16 v[168:171], v[196:199], v[116:119], v[168:171]\n\t"
            "v_mul_f32 v225, v229, v225\n\t"
            "v_mul_f32 v226, v230, v226\n\t"
            "v_mfma_f32_16x16x32_bf16 v[172:175], v[176:179], v[120:123], 0\n\t"
            "v_mul_f32 v227, v231, v227\n\t"
            "v_cvt_pk_bf16_f32 v224, v224, v225\n\t"
            "v_mfma_f32_16x16x32_bf16 v[172:175], v[180:183], v[124:127], v[172:175]\n\t"
            "v_cvt_pk_bf16_f32 v225, v226, v227\n\t"
            "v_mfma_f32_16x16x32_bf16 v[172:175], v[184:187], v[128:131], v[172:175]\n\t"
            "v_mul_f32 v168, v228, v168\n\t"
            "v_mul_f32 v169, v229, v169\n\t"
            "v_mfma_f32_16x16x32_bf16 v[172:175], v[188:191], v[132:135], v[172:175]\n\t"
            "v_mul_f32 v170, v230, v170\n\t"
            "v_mul_f32 v171, v231, v171\n\t"
            "v_mfma_f32_16x16x32_bf16 v[172:175], v[192:195], v[136:139], v[172:175]\n\t"
            "v_cvt_pk_bf16_f32 v168, v168, v169\n\t"
            "v_cvt_pk_bf16_f32 v169, v170, v171\n\t"
            "v_mfma_f32_16x16x32_bf16 v[172:175], v[196:199], v[140:143], v[172:175]\n\t"
            "ds_write_b64 v244, v[224:225] offset:4608\n\t"
            "ds_write_b64 v244, v[168:169] offset:6144\n\t"
            "ds_read_b128 v[168:171], v254\n\t"
            "s_nop 4\n\t"
            "v_mul_f32 v172, v228, v172\n\t"
            "v_mul_f32 v173, v229, v173\n\t"
            "v_mul_f32 v174, v230, v174\n\t"
            "v_mul_f32 v175, v231, v175\n\t"
            "v_cvt_pk_bf16_f32 v172, v172, v173\n\t"
            "v_cvt_pk_bf16_f32 v173, v174, v175\n\t"
            "ds_write_b64 v244, v[172:173] offset:7680\n\t"
            "ds_read_b128 v[172:175], v254 offset:64\n\t"
            "s_waitcnt lgkmcnt(1)\n\t"
            "s_cmp_lt_u32 s46, 0x7000000\n\t"
            "s_barrier\n\t"
            "s_cbranch_scc0 .Lvx%=\n\t"
            "ds_read_b128 v[144:147], v232\n\t"
            "ds_read_b128 v[148:151], v233\n\t"
            "ds_read_b128 v[152:155], v232 offset:128\n\t"
            "ds_read_b128 v[156:159], v233 offset:128\n\t"
            "ds_read_b128 v[160:163], v232 offset:256\n\t"
            "ds_read_b128 v[164:167], v233 offset:256\n\t"
            "ds_read_b128 v[176:179], v234 offset:18816\n\t"
            "ds_read_b128 v[180:183], v234 offset:19840\n\t"
            "s_waitcnt lgkmcnt(7)\n\t"
            "v_mfma_f32_16x16x32_bf16 v[200:203], v[144:147], v[0:3], 0\n\t"
            "s_waitcnt lgkmcnt(6)\n\t"
            "v_mfma_f32_16x16x32_bf16 v[200:203], v[148:151], v[4:7], v[200:203]\n\t"
            "s_waitcnt lgkmcnt(5)\n\t"
            "v_mfma_f32_16x16x32_bf16 v[200:203], v[152:155], v[8:11], v[200:203]\n\t"
            "s_waitcnt lgkmcnt(4)\n\t"
            "v_mfma_f32_16x16x32_bf16 v[200:203], v[156:159], v[12:15], v[200:203]\n\t"
            "s_waitcnt lgkmcnt(1)\n\t"
            "v_add_f32 v176, v176, v178\n\t"
            "s_waitcnt lgkmcnt(0)\n\t"
            "v_add_f32 v180, v180, v182\n\t"
            "v_mfma_f32_16x16x32_bf16 v[200:203], v[160:163], v[16:19], v[200:203]\n\t"
            "v_add_f32 v176, v176, v180\n\t"
            "v_add_f32 v177, v177, v179\n\t"
            "v_mfma_f32_16x16x32_bf16 v[200:203], v[164:167], v[20:23], v[200:203]\n\t"
            "v_add_f32 v181, v181, v183\n\t"
            "v_mul_f32 v176, 0x3baaaaab, v176\n\t"
            "v_mfma_f32_16x16x32_bf16 v[204:207], v[144:147], v[24:27], 0\n\t"
            "v_add_f32 v177, v177, v181\n\t"
            "v_mul_f32_e64 v176, v176, -v176\n\t"
            "v_mfma_f32_16x16x32_bf16 v[204:207], v[148:151], v[28:31], v[204:207]\n\t"
            "v_fmac_f32 v176, 0x3baaaaab, v177\n\t"
            "v_max_f32 v176, 0, v176\n\t"
            "v_mfma_f32_16x16x32_bf16 v[204:207], v[152:155], v[32:35], v[204:207]\n\t"
            "v_add_f32 v176, 0x3727c5ac, v176\n\t"
            "v_rsq_f32 v176, v176\n\t"
            "v_mfma_f32_16x16x32_bf16 v[204:207], v[156:159], v[36:39], v[204:207]\n\t"
            "ds_write_b32 v246, v176\n\t"
            "v_mfma_f32_16x16x32_bf16 v[204:207], v[160:163], v[40:43], v[204:207]\n\t"
            "ds_read_b128 v[228:231], v245\n\t"
            "v_mfma_f32_16x16x32_bf16 v[204:207], v[164:167], v[44:47], v[204:207]\n\t"
            "v_mfma_f32_16x16x32_bf16 v[208:211], v[144:147], v[48:51], 0\n\t"
            "v_mfma_f32_16x16x32_bf16 v[208:211], v[148:151], v[52:55], v[208:211]\n\t"
            "v_mfma_f32_16x16x32_bf16 v[208:211], v[152:155], v[56:59], v[208:211]\n\t"
            "v_mfma_f32_16x16x32_bf16 v[208:211], v[156:159], v[60:63], v[208:211]\n\t"
            "v_mfma_f32_16x16x32_bf16 v[208:211], v[160:163], v[64:67], v[208:211]\n\t"
            "v_mfma_f32_16x16x32_bf16 v[208:211], v[164:167], v[68:71], v[208:211]\n\t"
            "v_mfma_f32_16x16x32_bf16 v[212:215], v[144:147], v[72:75], 0\n\t"
            "s_waitcnt lgkmcnt(0)\n\t"
            "v_mul_f32 v200, v228, v200\n\t"
            "v_mul_f32 v201, v229, v201\n\t"
            "v_mfma_f32_16x16x32_bf16 v[212:215], v[148:151], v[76:79], v[212:215]\n\t"
            "v_mul_f32 v204, v228, v204\n\t"
            "v_mul_f32 v205, v229, v205\n\t"
            "v_mfma_f32_16x16x32_bf16 v[212:215], v[152:155], v[80:83], v[212:215]\n\t"
            "v_mul_f32 v208, v228, v208\n\t"
            "v_mul_f32 v209, v229, v209\n\t"
            "v_mfma_f32_16x16x32_bf16 v[212:215], v[156:159], v[84:87], v[212:215]\n\t"
            "v_mul_f32 v202, v230, v202\n\t"
            "v_mul_f32 v203, v231, v203\n\t"
            "v_mfma_f32_16x16x32_bf16 v[212:215], v[160:163], v[88:91], v[212:215]\n\t"
            "v_cvt_pk_bf16_f32 v200, v200, v201\n\t"
            "v_mul_f32 v206, v230, v206\n\t"
            "v_mfma_f32_16x16x32_bf16 v[212:215], v[164:167], v[92:95], v[212:215]\n\t"
            "v_mul_f32 v207, v231, v207\n\t"
            "v_cvt_pk_bf16_f32 v204, v204, v205\n\t"
            "v_mfma_f32_16x16x32_bf16 v[216:219], v[144:147], v[96:99], 0\n\t"
            "s_waitcnt vmcnt(0)\n\t"
            "v_mul_f32 v210, v230, v210\n\t"
            "v_mul_f32 v211, v231, v211\n\t"
            "v_mfma_f32_16x16x32_bf16 v[216:219], v[148:151], v[100:103], v[216:219]\n\t"
            "s_nop 0\n\t"
            "v_mul_f32 v212, v228, v212\n\t"
            "v_mul_f32 v213, v229, v213\n\t"
            "v_mfma_f32_16x16x32_bf16 v[216:219], v[152:155], v[104:107], v[216:219]\n\t"
            "v_cvt_pk_bf16_f32 v208, v208, v209\n\t"
            "v_mul_f32 v214, v230, v214\n\t"
            "v_mfma_f32_16x16x32_bf16 v[216:219], v[156:159], v[108:111], v[216:219]\n\t"
            "v_mul_f32 v215, v231, v215\n\t"
            "v_cvt_pk_bf16_f32 v212, v212, v213\n\t"
            "v_mfma_f32_16x16x32_bf16 v[216:219], v[160:163], v[112:115], v[216:219]\n\t"
            "v_cvt_pk_bf16_f32 v201, v202, v203\n\t"
            "v_cvt_pk_bf16_f32 v205, v206, v207\n\t"
            "v_mfma_f32_16x16x32_bf16 v[216:219], v[164:167], v[116:119], v[216:219]\n\t"
            "v_cvt_pk_bf16_f32 v209, v210, v211\n\t"
            "v_cvt_pk_bf16_f32 v213, v214, v215\n\t"
            "v_mfma_f32_16x16x32_bf16 v[220:223], v[144:147], v[120:123], 0\n\t"
            "ds_read_b64 v[144:145], v253\n\t"
            "v_mfma_f32_16x16x32_bf16 v[220:223], v[148:151], v[124:127], v[220:223]\n\t"
            "ds_read_b64 v[146:147], v253 offset:96\n\t"
            "v_mfma_f32_16x16x32_bf16 v[220:223], v[152:155], v[128:131], v[220:223]\n\t"
            "ds_read_b64 v[148:149], v253 offset:1744\n\t"
            "v_mul_f32 v216, v228, v216\n\t"
            "v_mfma_f32_16x16x32_bf16 v[220:223], v[156:159], v[132:135], v[220:223]\n\t"
            "ds_read_b64 v[150:151], v253 offset:1840\n\t"
            "v_mul_f32 v217, v229, v217\n\t"
            "v_mfma_f32_16x16x32_bf16 v[220:223], v[160:163], v[136:139], v[220:223]\n\t"
            "ds_read_b64 v[152:153], v253 offset:8\n\t"
            "v_mul_f32 v218, v230, v218\n\t"
            "v_mfma_f32_16x16x32_bf16 v[220:223], v[164:167], v[140:143], v[220:223]\n\t"
            "ds_read_b64 v[154:155], v253 offset:104\n\t"
            "v_mul_f32 v219, v231, v219\n\t"
            "v_cvt_pk_bf16_f32 v216, v216, v217\n\t"
            "v_cvt_pk_bf16_f32 v217, v218, v219\n\t"
            "ds_read_b64 v[156:157], v253 offset:1752\n\t"
            "ds_read_b64 v[158:159], v253 offset:1848\n\t"
            "ds_read_b64 v[160:161], v253 offset:16\n\t"
            "ds_read_b64 v[162:163], v253 offset:112\n\t"
            "v_mul_f32 v220, v228, v220\n\t"
            "v_mul_f32 v221, v229, v221\n\t"
            "v_mul_f32 v222, v230, v222\n\t"
            "v_mul_f32 v223, v231, v223\n\t"
            "v_cvt_pk_bf16_f32 v220, v220, v221\n\t"
            "v_cvt_pk_bf16_f32 v221, v222, v223\n\t"
            "ds_read_b64 v[164:165], v253 offset:1760\n\t"
            "ds_read_b64 v[166:167], v253 offset:1856\n\t"
            "s_branch .Lwc_loop%=\n\t"
            ".Lvx%=:\n\t"
            "s_waitcnt lgkmcnt(0)\n\t"
            "s_nop 7\n\t"
            "s_setprio 3\n\t"
            "v_add_u32 v236, s19, v252\n\t"
            "ds_read_b128 v[244:247], v236\n\t"
            "s_add_u32 s70, s33, 73728\n\t"
            "s_waitcnt lgkmcnt(0)\n\t"
            "buffer_load_dwordx4 v[200:203], v244, s[28:31], s21 offen\n\t"
            "buffer_load_dwordx4 v[204:207], v245, s[28:31], s21 offen\n\t"
            "buffer_load_dwordx4 v[208:211], v246, s[28:31], s21 offen\n\t"
            "s_add_u32 s21, s21, 6144\n\t"
            "buffer_load_dwordx4 v[212:215], v244, s[28:31], s21 offen\n\t"
            "buffer_load_dwordx4 v[216:219], v245, s[28:31], s21 offen\n\t"
            "buffer_load_dwordx4 v[220:223], v246, s[28:31], s21 offen\n\t"
            "s_add_u32 s49, s70, 0\n\t"
            "buffer_load_dwordx4 v[0:3], v247, s[28:31], s49 offen sc1\n\t"
            "s_add_u32 s49, s70, 6144\n\t"
            "buffer_load_dwordx4 v[24:27], v247, s[28:31], s49 offen sc1\n\t"
            "s_add_u32 s49, s70, 12288\n\t"
            "buffer_load_dwordx4 v[48:51], v247, s[28:31], s49 offen sc1\n\t"
            "s_add_u32 s49, s70, 18432\n\t"
            "buffer_load_dwordx4 v[72:75], v247, s[28:31], s49 offen sc1\n\t"
            "s_add_u32 s49, s70, 24576\n\t"
            "buffer_load_dwordx4 v[96:99], v247, s[28:31], s49 offen sc1\n\t"
            "s_add_u32 s49, s70, 30720\n\t"
            "buffer_load_dwordx4 v[120:123], v247, s[28:31], s49 offen sc1\n\t"
            "s_add_u32 s49, s70, 1024\n\t"
            "buffer_load_dwordx4 v[4:7], v247, s[28:31], s49 offen sc1\n\t"
            "s_add_u32 s49, s70, 7168\n\t"
            "buffer_load_dwordx4 v[28:31], v247, s[28:31], s49 offen sc1\n\t"
            "s_add_u32 s49, s70, 13312\n\t"
            "buffer_load_dwordx4 v[52:55], v247, s[28:31], s49 offen sc1\n\t"
            "s_add_u32 s49, s70, 19456\n\t"
            "buffer_load_dwordx4 v[76:79], v247, s[28:31], s49 offen sc1\n\t"
            "s_add_u32 s49, s70, 25600\n\t"
            "buffer_load_dwordx4 v[100:103], v247, s[28:31], s49 offen sc1\n\t"
            "s_add_u32 s49, s70, 31744\n\t"
            "buffer_load_dwordx4 v[124:127], v247, s[28:31], s49 offen sc1\n\t"
            "s_add_u32 s49, s70, 2048\n\t"
            "buffer_load_dwordx4 v[8:11], v247, s[28:31], s49 offen sc1\n\t"
            "s_add_u32 s49, s70, 8192\n\t"
            "buffer_load_dwordx4 v[32:35], v247, s[28:31], s49 offen sc1\n\t"
            "s_add_u32 s49, s70, 14336\n\t"
            "buffer_load_dwordx4 v[56:59], v247, s[28:31], s49 offen sc1\n\t"
            "s_add_u32 s49, s70, 20480\n\t"
            "buffer_load_dwordx4 v[80:83], v247, s[28:31], s49 offen sc1\n\t"
            "s_add_u32 s49, s70, 26624\n\t"
            "buffer_load_dwordx4 v[104:107], v247, s[28:31], s49 offen sc1\n\t"
            "s_add_u32 s49, s70, 32768\n\t"
            "buffer_load_dwordx4 v[128:131], v247, s[28:31], s49 offen sc1\n\t"
            "s_add_u32 s49, s70, 3072\n\t"
            "buffer_load_dwordx4 v[12:15], v247, s[28:31], s49 offen sc1\n\t"
            "s_add_u32 s49, s70, 9216\n\t"
            "buffer_load_dwordx4 v[36:39], v247, s[28:31], s49 offen sc1\n\t"
            "s_add_u32 s49, s70, 15360\n\t"
            "buffer_load_dwordx4 v[60:63], v247, s[28:31], s49 offen sc1\n\t"
            "s_add_u32 s49, s70, 21504\n\t"
            "buffer_load_dwordx4 v[84:87], v247, s[28:31], s49 offen sc1\n\t"
            "s_add_u32 s49, s70, 27648\n\t"
            "buffer_load_dwordx4 v[108:111], v247, s[28:31], s49 offen sc1\n\t"
            "s_add_u32 s49, s70, 33792\n\t"
            "buffer_load_dwordx4 v[132:135], v247, s[28:31], s49 offen sc1\n\t"
            "s_add_u32 s49, s70, 4096\n\t"
            "buffer_load_dwordx4 v[16:19], v247, s[28:31], s49 offen sc1\n\t"
            "s_add_u32 s49, s70, 10240\n\t"
            "buffer_load_dwordx4 v[40:43], v247, s[28:31], s49 offen sc1\n\t"
            "s_add_u32 s49, s70, 16384\n\t"
            "buffer_load_dwordx4 v[64:67], v247, s[28:31], s49 offen sc1\n\t"
            "s_add_u32 s49, s70, 22528\n\t"
            "buffer_load_dwordx4 v[88:91], v247, s[28:31], s49 offen sc1\n\t"
            "s_add_u32 s49, s70, 28672\n\t"
            "buffer_load_dwordx4 v[112:115], v247, s[28:31], s49 offen sc1\n\t"
            "s_add_u32 s49, s70, 34816\n\t"
            "buffer_load_dwordx4 v[136:139], v247, s[28:31], s49 offen sc1\n\t"
            "s_add_u32 s49, s70, 5120\n\t"
            "buffer_load_dwordx4 v[20:23], v247, s[28:31], s49 offen sc1\n\t"
            "s_add_u32 s49, s70, 11264\n\t"
            "buffer_load_dwordx4 v[44:47], v247, s[28:31], s49 offen sc1\n\t"
            "s_add_u32 s49, s70, 17408\n\t"
            "buffer_load_dwordx4 v[68:71], v247, s[28:31], s49 offen sc1\n\t"
            "s_add_u32 s49, s70, 23552\n\t"
            "buffer_load_dwordx4 v[92:95], v247, s[28:31], s49 offen sc1\n\t"
            "s_add_u32 s49, s70, 29696\n\t"
            "buffer_load_dwordx4 v[116:119], v247, s[28:31], s49 offen sc1\n\t"
            "s_add_u32 s49, s70, 35840\n\t"
            "buffer_load_dwordx4 v[140:143], v247, s[28:31], s49 offen sc1\n\t"
            "s_nop 7\n\t"
            "v_bfe_u32 v248, v247, 2, 6\n\t"
            "v_add_u32 v248, s23, v248\n\t"
            "v_bfe_u32 v249, v247, 2, 6\n\t"
            "v_add_u32 v249, s23, v249\n\t"
            "v_lshrrev_b32 v236, 4, v247\n\t"
            "v_and_b32 v236, 48, v236\n\t"
            "v_add_u32 v236, s20, v236\n\t"
            "v_add_u32 v242, s22, v242\n\t"
            "v_xor_b32 v243, 64, v242\n\t"
            "ds_read_b32 v248, v248\n\t"
            "ds_read_b32 v249, v249 offset:64\n\t"
            "s_waitcnt lgkmcnt(1)\n\t"
            "v_ashrrev_i32 v237, 31, v248\n\t"
            "v_and_b32 v237, 1177344, v237\n\t"
            "v_sub_u32 v236, v236, v237\n\t"
            "v_bfe_i32 v237, v248, 30, 1\n\t"
            "v_and_b32 v237, 2304, v237\n\t"
            "v_sub_u32 v236, v236, v237\n\t"
            "v_and_b32 v248, 0x7ffffff, v248\n\t"
            "v_add_u32 v248, v248, v236\n\t"
            "v_lshrrev_b32 v236, 4, v247\n\t"
            "v_and_b32 v236, 48, v236\n\t"
            "s_waitcnt lgkmcnt(0)\n\t"
            "v_ashrrev_i32 v237, 31, v249\n\t"
            "v_add_u32 v236, s20, v236\n\t"
            "v_and_b32 v237, 1177344, v237\n\t"
            "v_sub_u32 v236, v236, v237\n\t"
            "v_bfe_i32 v237, v249, 30, 1\n\t"
            "v_and_b32 v237, 2304, v237\n\t"
            "v_sub_u32 v236, v236, v237\n\t"
            "v_and_b32 v249, 0x7ffffff, v249\n\t"
            "v_add_u32 v249, v249, v236\n\t"
            "s_nop 7\n\t"
            "v_mov_b32 v144, s23\n\t"
            ".Ldv%=:\n\t"
            "ds_read_b128 v[148:151], v144 offset:96\n\t"
            "s_waitcnt lgkmcnt(0)\n\t"
            "v_or_b32 v148, v148, v149\n\t"
            "v_or3_b32 v148, v148, v150, v151\n\t"
            "v_cmp_gt_i32 vcc, 0, v148\n\t"
            "s_nop 4\n\t"
            "s_cbranch_vccnz .Ldv%=\n\t"
            "s_waitcnt vmcnt(0)\n\t"
            "s_nop 7\n\t"
            "ds_read_b128 v[184:187], v242 offset:53760\n\t"
            "v_lshlrev_b32 v152, 16, v200\n\t"
            "v_and_b32 v153, 0xffff0000, v200\n\t"
            "v_lshlrev_b32 v154, 16, v201\n\t"
            "v_and_b32 v155, 0xffff0000, v201\n\t"
            "v_lshlrev_b32 v156, 16, v202\n\t"
            "v_and_b32 v157, 0xffff0000, v202\n\t"
            "v_lshlrev_b32 v158, 16, v203\n\t"
            "v_and_b32 v159, 0xffff0000, v203\n\t"
            "v_lshlrev_b32 v160, 16, v204\n\t"
            "v_and_b32 v161, 0xffff0000, v204\n\t"
            "v_lshlrev_b32 v162, 16, v205\n\t"
            "v_and_b32 v163, 0xffff0000, v205\n\t"
            "v_lshlrev_b32 v164, 16, v206\n\t"
            "v_and_b32 v165, 0xffff0000, v206\n\t"
            "v_lshlrev_b32 v166, 16, v207\n\t"
            "v_and_b32 v167, 0xffff0000, v207\n\t"
            "v_lshlrev_b32 v176, 16, v208\n\t"
            "v_and_b32 v177, 0xffff0000, v208\n\t"
            "v_lshlrev_b32 v178, 16, v209\n\t"
            "v_and_b32 v179, 0xffff0000, v209\n\t"
            "v_lshlrev_b32 v180, 16, v210\n\t"
            "v_and_b32 v181, 0xffff0000, v210\n\t"
            "v_lshlrev_b32 v182, 16, v211\n\t"
            "v_and_b32 v183, 0xffff0000, v211\n\t"
            "v_lshlrev_b32 v144, 16, v212\n\t"
            "v_and_b32 v145, 0xffff0000, v212\n\t"
            "v_lshlrev_b32 v146, 16, v213\n\t"
            "v_and_b32 v147, 0xffff0000, v213\n\t"
            "v_lshlrev_b32 v148, 16, v214\n\t"
            "v_and_b32 v149, 0xffff0000, v214\n\t"
            "s_waitcnt lgkmcnt(0)\n\t"
            "v_mfma_f32_16x16x32_bf16 v[152:155], v[0:3], v[184:187], v[152:155]\n\t"
            "ds_read_b128 v[188:191], v243 offset:53760\n\t"
            "v_lshlrev_b32 v150, 16, v215\n\t"
            "v_mfma_f32_16x16x32_bf16 v[156:159], v[24:27], v[184:187], v[156:159]\n\t"
            "ds_read_b128 v[192:195], v242 offset:53888\n\t"
            "v_and_b32 v151, 0xffff0000, v215\n\t"
            "v_mfma_f32_16x16x32_bf16 v[160:163], v[48:51], v[184:187], v[160:163]\n\t"
            "v_lshlrev_b32 v168, 16, v216\n\t"
            "v_and_b32 v169, 0xffff0000, v216\n\t"
            "v_mfma_f32_16x16x32_bf16 v[164:167], v[72:75], v[184:187], v[164:167]\n\t"
            "v_lshlrev_b32 v170, 16, v217\n\t"
            "v_and_b32 v171, 0xffff0000, v217\n\t"
            "v_mfma_f32_16x16x32_bf16 v[176:179], v[96:99], v[184:187], v[176:179]\n\t"
            "v_lshlrev_b32 v172, 16, v218\n\t"
            "v_and_b32 v173, 0xffff0000, v218\n\t"
            "v_mfma_f32_16x16x32_bf16 v[180:183], v[120:123], v[184:187], v[180:183]\n\t"
            "ds_read_b128 v[184:187], v243 offset:53888\n\t"
            "v_lshlrev_b32 v174, 16, v219\n\t"
            "v_and_b32 v175, 0xffff0000, v219\n\t"
            "s_waitcnt lgkmcnt(2)\n\t"
            "v_mfma_f32_16x16x32_bf16 v[152:155], v[4:7], v[188:191], v[152:155]\n\t"
            "v_mfma_f32_16x16x32_bf16 v[156:159], v[28:31], v[188:191], v[156:159]\n\t"
            "v_mfma_f32_16x16x32_bf16 v[160:163], v[52:55], v[188:191], v[160:163]\n\t"
            "v_mfma_f32_16x16x32_bf16 v[164:167], v[76:79], v[188:191], v[164:167]\n\t"
            "v_mfma_f32_16x16x32_bf16 v[176:179], v[100:103], v[188:191], v[176:179]\n\t"
            "v_mfma_f32_16x16x32_bf16 v[180:183], v[124:127], v[188:191], v[180:183]\n\t"
            "ds_read_b128 v[188:191], v242 offset:54016\n\t"
            "s_waitcnt lgkmcnt(2)\n\t"
            "v_mfma_f32_16x16x32_bf16 v[152:155], v[8:11], v[192:195], v[152:155]\n\t"
            "v_mfma_f32_16x16x32_bf16 v[156:159], v[32:35], v[192:195], v[156:159]\n\t"
            "v_mfma_f32_16x16x32_bf16 v[160:163], v[56:59], v[192:195], v[160:163]\n\t"
            "v_mfma_f32_16x16x32_bf16 v[164:167], v[80:83], v[192:195], v[164:167]\n\t"
            "v_mfma_f32_16x16x32_bf16 v[176:179], v[104:107], v[192:195], v[176:179]\n\t"
            "v_mfma_f32_16x16x32_bf16 v[180:183], v[128:131], v[192:195], v[180:183]\n\t"
            "ds_read_b128 v[192:195], v243 offset:54016\n\t"
            "s_waitcnt lgkmcnt(2)\n\t"
            "v_mfma_f32_16x16x32_bf16 v[152:155], v[12:15], v[184:187], v[152:155]\n\t"
            "v_mfma_f32_16x16x32_bf16 v[156:159], v[36:39], v[184:187], v[156:159]\n\t"
            "v_mfma_f32_16x16x32_bf16 v[160:163], v[60:63], v[184:187], v[160:163]\n\t"
            "v_mfma_f32_16x16x32_bf16 v[164:167], v[84:87], v[184:187], v[164:167]\n\t"
            "v_mfma_f32_16x16x32_bf16 v[176:179], v[108:111], v[184:187], v[176:179]\n\t"
            "v_mfma_f32_16x16x32_bf16 v[180:183], v[132:135], v[184:187], v[180:183]\n\t"
            "ds_read_b128 v[184:187], v242 offset:59904\n\t"
            "s_waitcnt lgkmcnt(2)\n\t"
            "v_mfma_f32_16x16x32_bf16 v[152:155], v[16:19], v[188:191], v[152:155]\n\t"
            "v_mfma_f32_16x16x32_bf16 v[156:159], v[40:43], v[188:191], v[156:159]\n\t"
            "v_mfma_f32_16x16x32_bf16 v[160:163], v[64:67], v[188:191], v[160:163]\n\t"
            "v_mfma_f32_16x16x32_bf16 v[164:167], v[88:91], v[188:191], v[164:167]\n\t"
            "v_mfma_f32_16x16x32_bf16 v[176:179], v[112:115], v[188:191], v[176:179]\n\t"
            "v_mfma_f32_16x16x32_bf16 v[180:183], v[136:139], v[188:191], v[180:183]\n\t"
            "ds_read_b128 v[188:191], v243 offset:59904\n\t"
            "s_waitcnt lgkmcnt(2)\n\t"
            "v_mfma_f32_16x16x32_bf16 v[152:155], v[20:23], v[192:195], v[152:155]\n\t"
            "v_mfma_f32_16x16x32_bf16 v[156:159], v[44:47], v[192:195], v[156:159]\n\t"
            "v_mfma_f32_16x16x32_bf16 v[160:163], v[68:71], v[192:195], v[160:163]\n\t"
            "v_mfma_f32_16x16x32_bf16 v[164:167], v[92:95], v[192:195], v[164:167]\n\t"
            "v_mfma_f32_16x16x32_bf16 v[176:179], v[116:119], v[192:195], v[176:179]\n\t"
            "v_mfma_f32_16x16x32_bf16 v[180:183], v[140:143], v[192:195], v[180:183]\n\t"
            "ds_read_b128 v[192:195], v242 offset:60032\n\t"
            "s_waitcnt lgkmcnt(2)\n\t"
            "v_mfma_f32_16x16x32_bf16 v[144:147], v[0:3], v[184:187], v[144:147]\n\t"
            "v_cvt_pk_bf16_f32 v152, v152, v153\n\t"
            "v_cvt_pk_bf16_f32 v153, v154, v155\n\t"
            "v_mfma_f32_16x16x32_bf16 v[148:151], v[24:27], v[184:187], v[148:151]\n\t"
            "v_cvt_pk_bf16_f32 v160, v160, v161\n\t"
            "v_cvt_pk_bf16_f32 v161, v162, v163\n\t"
            "v_mfma_f32_16x16x32_bf16 v[168:171], v[48:51], v[184:187], v[168:171]\n\t"
            "v_cvt_pk_bf16_f32 v154, v156, v157\n\t"
            "v_cvt_pk_bf16_f32 v155, v158, v159\n\t"
            "v_mfma_f32_16x16x32_bf16 v[172:175], v[72:75], v[184:187], v[172:175]\n\t"
            "v_cvt_pk_bf16_f32 v162, v164, v165\n\t"
            "v_cvt_pk_bf16_f32 v163, v166, v167\n\t"
            "s_waitcnt lgkmcnt(1)\n\t"
            "v_mfma_f32_16x16x32_bf16 v[144:147], v[4:7], v[188:191], v[144:147]\n\t"
            "v_lshlrev_b32 v156, 16, v220\n\t"
            "v_and_b32 v157, 0xffff0000, v220\n\t"
            "v_mfma_f32_16x16x32_bf16 v[148:151], v[28:31], v[188:191], v[148:151]\n\t"
            "v_lshlrev_b32 v158, 16, v221\n\t"
            "v_and_b32 v159, 0xffff0000, v221\n\t"
            "v_mfma_f32_16x16x32_bf16 v[168:171], v[52:55], v[188:191], v[168:171]\n\t"
            "v_lshlrev_b32 v164, 16, v222\n\t"
            "v_and_b32 v165, 0xffff0000, v222\n\t"
            "v_mfma_f32_16x16x32_bf16 v[156:159], v[96:99], v[184:187], v[156:159]\n\t"
            "v_lshlrev_b32 v166, 16, v223\n\t"
            "v_and_b32 v167, 0xffff0000, v223\n\t"
            "v_mfma_f32_16x16x32_bf16 v[172:175], v[76:79], v[188:191], v[172:175]\n\t"
            "buffer_store_dwordx4 v[152:155], v248, s[40:43], 0 offen sc1\n\t"
            "v_mfma_f32_16x16x32_bf16 v[164:167], v[120:123], v[184:187], v[164:167]\n\t"
            "ds_read_b128 v[184:187], v243 offset:60032\n\t"
            "v_cvt_pk_bf16_f32 v176, v176, v177\n\t"
            "v_mfma_f32_16x16x32_bf16 v[156:159], v[100:103], v[188:191], v[156:159]\n\t"
            "buffer_store_dwordx4 v[160:163], v248, s[40:43], 0 offen offset:64 sc1\n\t"
            "v_mfma_f32_16x16x32_bf16 v[164:167], v[124:127], v[188:191], v[164:167]\n\t"
            "ds_read_b128 v[188:191], v242 offset:60160\n\t"
            "v_cvt_pk_bf16_f32 v177, v178, v179\n\t"
            "s_waitcnt lgkmcnt(2)\n\t"
            "v_mfma_f32_16x16x32_bf16 v[144:147], v[8:11], v[192:195], v[144:147]\n\t"
            "v_cvt_pk_bf16_f32 v178, v180, v181\n\t"
            "v_cvt_pk_bf16_f32 v179, v182, v183\n\t"
            "v_mfma_f32_16x16x32_bf16 v[148:151], v[32:35], v[192:195], v[148:151]\n\t"
            "buffer_store_dwordx4 v[176:179], v248, s[40:43], 0 offen offset:128 sc1\n\t"
            "v_mfma_f32_16x16x32_bf16 v[168:171], v[56:59], v[192:195], v[168:171]\n\t"
            "v_mfma_f32_16x16x32_bf16 v[172:175], v[80:83], v[192:195], v[172:175]\n\t"
            "v_mfma_f32_16x16x32_bf16 v[156:159], v[104:107], v[192:195], v[156:159]\n\t"
            "v_mfma_f32_16x16x32_bf16 v[164:167], v[128:131], v[192:195], v[164:167]\n\t"
            "ds_read_b128 v[192:195], v243 offset:60160\n\t"
            "s_waitcnt lgkmcnt(2)\n\t"
            "v_mfma_f32_16x16x32_bf16 v[144:147], v[12:15], v[184:187], v[144:147]\n\t"
            "v_mfma_f32_16x16x32_bf16 v[148:151], v[36:39], v[184:187], v[148:151]\n\t"
            "s_waitcnt lgkmcnt(1)\n\t"
            "v_mfma_f32_16x16x32_bf16 v[144:147], v[16:19], v[188:191], v[144:147]\n\t"
            "v_mfma_f32_16x16x32_bf16 v[168:171], v[60:63], v[184:187], v[168:171]\n\t"
            "v_mfma_f32_16x16x32_bf16 v[148:151], v[40:43], v[188:191], v[148:151]\n\t"
            "v_mfma_f32_16x16x32_bf16 v[172:175], v[84:87], v[184:187], v[172:175]\n\t"
            "v_mfma_f32_16x16x32_bf16 v[168:171], v[64:67], v[188:191], v[168:171]\n\t"
            "v_mfma_f32_16x16x32_bf16 v[156:159], v[108:111], v[184:187], v[156:159]\n\t"
            "s_waitcnt lgkmcnt(0)\n\t"
            "v_mfma_f32_16x16x32_bf16 v[144:147], v[20:23], v[192:195], v[144:147]\n\t"
            "v_mfma_f32_16x16x32_bf16 v[148:151], v[44:47], v[192:195], v[148:151]\n\t"
            "v_mfma_f32_16x16x32_bf16 v[172:175], v[88:91], v[188:191], v[172:175]\n\t"
            "v_mfma_f32_16x16x32_bf16 v[164:167], v[132:135], v[184:187], v[164:167]\n\t"
            "v_mfma_f32_16x16x32_bf16 v[168:171], v[68:71], v[192:195], v[168:171]\n\t"
            "v_mfma_f32_16x16x32_bf16 v[156:159], v[112:115], v[188:191], v[156:159]\n\t"
            "v_mfma_f32_16x16x32_bf16 v[172:175], v[92:95], v[192:195], v[172:175]\n\t"
            "s_nop 1\n\t"
            "v_cvt_pk_bf16_f32 v144, v144, v145\n\t"
            "v_mfma_f32_16x16x32_bf16 v[164:167], v[136:139], v[188:191], v[164:167]\n\t"
            "v_cvt_pk_bf16_f32 v145, v146, v147\n\t"
            "v_cvt_pk_bf16_f32 v146, v148, v149\n\t"
            "v_mfma_f32_16x16x32_bf16 v[156:159], v[116:119], v[192:195], v[156:159]\n\t"
            "v_cvt_pk_bf16_f32 v147, v150, v151\n\t"
            "v_cvt_pk_bf16_f32 v168, v168, v169\n\t"
            "v_mfma_f32_16x16x32_bf16 v[164:167], v[140:143], v[192:195], v[164:167]\n\t"
            "buffer_store_dwordx4 v[144:147], v249, s[40:43], 0 offen sc1\n\t"
            "v_cvt_pk_bf16_f32 v169, v170, v171\n\t"
            "v_cvt_pk_bf16_f32 v170, v172, v173\n\t"
            "v_cvt_pk_bf16_f32 v171, v174, v175\n\t"
            "s_nop 0\n\t"
            "v_cvt_pk_bf16_f32 v156, v156, v157\n\t"
            "v_cvt_pk_bf16_f32 v157, v158, v159\n\t"
            "buffer_store_dwordx4 v[168:171], v249, s[40:43], 0 offen offset:64 sc1\n\t"
            "v_cvt_pk_bf16_f32 v158, v164, v165\n\t"
            "v_cvt_pk_bf16_f32 v159, v166, v167\n\t"
            "buffer_store_dwordx4 v[156:159], v249, s[40:43], 0 offen offset:128 sc1\n\t"
            "s_nop 7\n\t"
            "s_branch .Lre%=\n\t"
            ".Lrp%=:\n\t"
            "v_add_u32 v236, s19, v252\n\t"
            "ds_write_b128 v236, v[244:247]\n\t"
            "v_add_u32 v236, s48, v244\n\t"
            "v_add_u32 v237, s48, v245\n\t"
            "v_add_u32 v248, s48, v246\n\t"
            "ds_read_b128 v[200:203], v236\n\t"
            "ds_read_b128 v[204:207], v237\n\t"
            "ds_read_b128 v[208:211], v248\n\t"
            "ds_read_b128 v[212:215], v236 offset:6144\n\t"
            "ds_read_b128 v[216:219], v237 offset:6144\n\t"
            "ds_read_b128 v[220:223], v248 offset:6144\n\t"
            "ds_read_b128 v[224:227], v236 offset:12288\n\t"
            "ds_read_b128 v[228:231], v237 offset:12288\n\t"
            "ds_read_b128 v[232:235], v248 offset:12288\n\t"
            "s_waitcnt lgkmcnt(0)\n\t"
            "s_barrier\n\t"
            "s_add_u32 s45, s46, s27\n\t"
            "s_mov_b32 s46, s47\n\t"
            "s_mov_b32 s47, s68\n\t"
            "s_xor_b32 s48, s48, s71\n\t"
            "s_xor_b32 s67, s67, s71\n\t"
            "s_add_u32 s76, s76, 4\n\t"
            "s_lshl_b32 s56, s56, 2\n\t"
            "s_lshr_b32 s49, s26, 30\n\t"
            "s_or_b32 s56, s56, s49\n\t"
            "s_cmp_ge_u32 s76, s24\n\t"
            "s_cselect_b64 vcc, -1, 0\n\t"
            "s_add_u32 s49, s33, 0\n\t"
            "buffer_load_dwordx4 v[0:3], v247, s[28:31], s49 offen sc1\n\t"
            "s_add_u32 s49, s33, 1024\n\t"
            "buffer_load_dwordx4 v[4:7], v247, s[28:31], s49 offen sc1\n\t"
            "s_add_u32 s49, s33, 2048\n\t"
            "buffer_load_dwordx4 v[8:11], v247, s[28:31], s49 offen sc1\n\t"
            "s_add_u32 s49, s33, 3072\n\t"
            "buffer_load_dwordx4 v[12:15], v247, s[28:31], s49 offen sc1\n\t"
            "s_add_u32 s49, s33, 4096\n\t"
            "buffer_load_dwordx4 v[16:19], v247, s[28:31], s49 offen sc1\n\t"
            "s_add_u32 s49, s33, 5120\n\t"
            "buffer_load_dwordx4 v[20:23], v247, s[28:31], s49 offen sc1\n\t"
            "s_add_u32 s49, s33, 6144\n\t"
            "buffer_load_dwordx4 v[24:27], v247, s[28:31], s49 offen sc1\n\t"
            "s_add_u32 s49, s33, 7168\n\t"
            "buffer_load_dwordx4 v[28:31], v247, s[28:31], s49 offen sc1\n\t"
            "s_add_u32 s49, s33, 8192\n\t"
            "buffer_load_dwordx4 v[32:35], v247, s[28:31], s49 offen sc1\n\t"
            "s_add_u32 s49, s33, 9216\n\t"
            "buffer_load_dwordx4 v[36:39], v247, s[28:31], s49 offen sc1\n\t"
            "s_add_u32 s49, s33, 10240\n\t"
            "buffer_load_dwordx4 v[40:43], v247, s[28:31], s49 offen sc1\n\t"
            "s_add_u32 s49, s33, 11264\n\t"
            "buffer_load_dwordx4 v[44:47], v247, s[28:31], s49 offen sc1\n\t"
            "s_add_u32 s49, s33, 12288\n\t"
            "buffer_load_dwordx4 v[48:51], v247, s[28:31], s49 offen sc1\n\t"
            "s_add_u32 s49, s33, 13312\n\t"
            "buffer_load_dwordx4 v[52:55], v247, s[28:31], s49 offen sc1\n\t"
            "s_add_u32 s49, s33, 14336\n\t"
            "buffer_load_dwordx4 v[56:59], v247, s[28:31], s49 offen sc1\n\t"
            "s_add_u32 s49, s33, 15360\n\t"
            "buffer_load_dwordx4 v[60:63], v247, s[28:31], s49 offen sc1\n\t"
            "s_add_u32 s49, s33, 16384\n\t"
            "buffer_load_dwordx4 v[64:67], v247, s[28:31], s49 offen sc1\n\t"
            "s_add_u32 s49, s33, 17408\n\t"
            "buffer_load_dwordx4 v[68:71], v247, s[28:31], s49 offen sc1\n\t"
            "ds_read_b128 v[168:171], v254\n\t"
            "ds_read_b128 v[172:175], v254 offset:64\n\t"
            "s_waitcnt vmcnt(0)\n\t"
            "ds_read_b64 v[144:145], v253\n\t"
            "ds_read_b64 v[146:147], v253 offset:96\n\t"
            "ds_read_b64 v[148:149], v253 offset:1744\n\t"
            "ds_read_b64 v[150:151], v253 offset:1840\n\t"
            "ds_read_b64 v[152:153], v253 offset:8\n\t"
            "ds_read_b64 v[154:155], v253 offset:104\n\t"
            "ds_read_b64 v[156:157], v253 offset:1752\n\t"
            "ds_read_b64 v[158:159], v253 offset:1848\n\t"
            "ds_read_b64 v[160:161], v253 offset:16\n\t"
            "ds_read_b64 v[162:163], v253 offset:112\n\t"
            "ds_read_b64 v[164:165], v253 offset:1760\n\t"
            "ds_read_b64 v[166:167], v253 offset:1856\n\t"
            "s_waitcnt lgkmcnt(0)\n\t"
            "s_nop 7\n\t"
            ".p2align 6\n\t"
            ".Lwb_loop%=:\n\t"
            "s_setprio 3\n\t"
            "v_mov_b64 v[176:177], 0\n\t"
            "v_mov_b64 v[178:179], 0\n\t"
            "v_mov_b32 v249, s76\n\t"
            "v_mov_b64 v[180:181], 0\n\t"
            "s_waitcnt lgkmcnt(7)\n\t"
            "v_smfmac_f32_16x16x64_bf16 v[176:179], v[168:171], v[144:151], v255\n\t"
            "ds_read_b128 v[168:171], v254 offset:128\n\t"
            "v_mov_b64 v[182:183], 0\n\t"
            "v_mov_b64 v[184:185], 0\n\t"
            "v_mov_b64 v[186:187], 0\n\t"
            "s_waitcnt lgkmcnt(5)\n\t"
            "v_smfmac_f32_16x16x64_bf16 v[180:183], v[172:175], v[152:159], v255\n\t"
            "ds_read_b32 v249, v249\n\t"
            "v_mov_b64 v[188:189], 0\n\t"
            "v_mov_b64 v[190:191], 0\n\t"
            "v_cvt_pk_bf16_f32 v176, v176, v177\n\t"
            "v_cvt_pk_bf16_f32 v177, v178, v179\n\t"
            "v_add_u32 v236, s67, v250\n\t"
            "v_add_u32 v248, s67, v251\n\t"
            "v_mfma_f32_4x4x4_16b_bf16 v[196:199], v[176:177], v[176:177], 0\n\t"
            "ds_read_b64 v[144:145], v253 offset:24\n\t"
            "v_cvt_pk_bf16_f32 v178, v180, v181\n\t"
            "v_cvt_pk_bf16_f32 v179, v182, v183\n\t"
            "s_barrier\n\t"
            "v_mov_b64 v[180:181], 0\n\t"
            "v_mov_b64 v[182:183], 0\n\t"
            "ds_read_b64 v[146:147], v253 offset:120\n\t"
            "ds_read_b64 v[148:149], v253 offset:1768\n\t"
            "ds_read_b64 v[150:151], v253 offset:1864\n\t"
            "ds_read_b128 v[172:175], v254 offset:192\n\t"
            "s_waitcnt lgkmcnt(6)\n\t"
            "v_smfmac_f32_16x16x64_bf16 v[184:187], v[168:171], v[160:167], v255\n\t"
            "ds_read_b64 v[152:153], v253 offset:32\n\t"
            "v_mfma_f32_4x4x4_16b_bf16 v[196:199], v[178:179], v[178:179], v[196:199]\n\t"
            "ds_read_b64 v[154:155], v253 offset:128\n\t"
            "v_mfma_f32_16x16x32_bf16 v[192:195], v[238:241], v[176:179], 0\n\t"
            "ds_read_b64 v[156:157], v253 offset:1776\n\t"
            "s_waitcnt lgkmcnt(8)\n\t"
            "v_readfirstlane_b32 s26, v249\n\t"
            "ds_read_b64 v[158:159], v253 offset:1872\n\t"
            "s_and_b32 s68, s26, 0x7ffffff\n\t"
            "s_add_u32 s68, s68, s78\n\t"
            "s_and_b32 s69, s26, 0x38000000\n\t"
            "s_or_b32 s69, s69, s39\n\t"
            "s_bitcmp1_b32 s26, 27\n\t"
            "s_cselect_b32 s79, 2.0, s68\n\t"
            "ds_read_b128 v[168:171], v254 offset:256\n\t"
            "s_and_b32 s57, s56, 3\n\t"
            "s_lshl_b32 s58, s57, 3\n\t"
            "s_lshr_b32 m0, s54, s58\n\t"
            "ds_read_b64 v[160:161], v253 offset:40\n\t"
            "s_lshl_b32 s58, s57, 4\n\t"
            "s_lshr_b64 s[36:37], s[50:51], s58\n\t"
            "s_and_b32 s57, s36, 0xffff\n\t"
            "s_add_u32 s57, s57, s67\n\t"
            "ds_read_b64 v[162:163], v253 offset:136\n\t"
            "s_waitcnt lgkmcnt(7)\n\t"
            "v_smfmac_f32_16x16x64_bf16 v[188:191], v[172:175], v[144:151], v255\n\t"
            "s_lshr_b64 s[36:37], s[52:53], s58\n\t"
            "s_and_b32 s58, s36, 0xffff\n\t"
            "s_add_u32 s58, s58, s67\n\t"
            "ds_read_b64 v[164:165], v253 offset:1784\n\t"
            "s_and_b32 s59, s50, 0xffff\n\t"
            "s_add_u32 s59, s59, s67\n\t"
            "s_and_b32 s38, s52, 0xffff\n\t"
            "s_add_u32 s38, s38, s67\n\t"
            "ds_read_b64 v[166:167], v253 offset:1880\n\t"
            "v_writelane_b32 v236, s59, m0\n\t"
            "v_writelane_b32 v236, s57, 63\n\t"
            "v_cvt_pk_bf16_f32 v184, v184, v185\n\t"
            "v_cvt_pk_bf16_f32 v185, v186, v187\n\t"
            "v_cvt_pk_bf16_f32 v186, v188, v189\n\t"
            "v_cvt_pk_bf16_f32 v187, v190, v191\n\t"
            "v_mfma_f32_4x4x4_16b_bf16 v[196:199], v[184:185], v[184:185], v[196:199]\n\t"
            "ds_read_b128 v[172:175], v254 offset:320\n\t"
            "v_xor_b32 v237, 16, v236\n\t"
            "v_mov_b64 v[188:189], 0\n\t"
            "v_mov_b64 v[190:191], 0\n\t"
            "v_writelane_b32 v248, s38, m0\n\t"
            "v_writelane_b32 v248, s58, 63\n\t"
            "ds_read_b64 v[144:145], v253 offset:48\n\t"
            "ds_read_b64 v[146:147], v253 offset:144\n\t"
            "ds_read_b64 v[148:149], v253 offset:1792\n\t"
            "ds_read_b64 v[150:151], v253 offset:1888\n\t"
            "ds_read_b32 v249, v252 offset:1024\n\t"
            "s_waitcnt lgkmcnt(14)\n\t"
            "ds_write_b128 v236, v[176:179]\n\t"
            "v_mov_b64 v[176:177], 0\n\t"
            "v_mov_b64 v[178:179], 0\n\t"
            "s_waitcnt lgkmcnt(11)\n\t"
            "s_nop 0\n\t"
            "v_smfmac_f32_16x16x64_bf16 v[176:179], v[168:171], v[152:159], v255\n\t"
            "ds_read_b128 v[168:171], v254 offset:384\n\t"
            "v_mfma_f32_4x4x4_16b_bf16 v[196:199], v[186:187], v[186:187], v[196:199]\n\t"
            "ds_read_b64 v[152:153], v253 offset:56\n\t"
            "v_mfma_f32_16x16x32_bf16 v[192:195], v[238:241], v[184:187], v[192:195]\n\t"
            "ds_read_b64 v[154:155], v253 offset:152\n\t"
            "s_waitcnt lgkmcnt(9)\n\t"
            "v_smfmac_f32_16x16x64_bf16 v[180:183], v[172:175], v[160:167], v255\n\t"
            "ds_read_b64 v[156:157], v253 offset:1800\n\t"
            "ds_read_b64 v[158:159], v253 offset:1896\n\t"
            "v_cvt_pk_bf16_f32 v176, v176, v177\n\t"
            "v_cvt_pk_bf16_f32 v177, v178, v179\n\t"
            "s_nop 1\n\t"
            "v_mfma_f32_4x4x4_16b_bf16 v[196:199], v[176:177], v[176:177], v[196:199]\n\t"
            "ds_read_b128 v[172:175], v254 offset:448\n\t"
            "s_waitcnt lgkmcnt(7)\n\t"
            "v_and_b32 v249, s69, v249\n\t"
            "v_cvt_pk_bf16_f32 v178, v180, v181\n\t"
            "v_cvt_pk_bf16_f32 v179, v182, v183\n\t"
            "v_mov_b64 v[180:181], 0\n\t"
            "v_mov_b64 v[182:183], 0\n\t"
            "ds_read_b64 v[160:161], v253 offset:64\n\t"
            "ds_read_b64 v[162:163], v253 offset:160\n\t"
            "ds_read_b64 v[164:165], v253 offset:1808\n\t"
            "ds_read_b64 v[166:167], v253 offset:1904\n\t"
            "ds_write_b128 v237, v[184:187]\n\t"
            "v_mov_b64 v[184:185], 0\n\t"
            "v_mov_b64 v[186:187], 0\n\t"
            "s_waitcnt lgkmcnt(10)\n\t"
            "s_nop 0\n\t"
            "v_smfmac_f32_16x16x64_bf16 v[184:187], v[168:171], v[144:151], v255\n\t"
            "ds_read_b128 v[168:171], v254 offset:512\n\t"
            "v_mfma_f32_4x4x4_16b_bf16 v[196:199], v[178:179], v[178:179], v[196:199]\n\t"
            "ds_read_b64 v[144:145], v253 offset:72\n\t"
            "v_mfma_f32_16x16x32_bf16 v[192:195], v[238:241], v[176:179], v[192:195]\n\t"
            "ds_read_b64 v[146:147], v253 offset:168\n\t"
            "s_waitcnt lgkmcnt(8)\n\t"
            "v_smfmac_f32_16x16x64_bf16 v[188:191], v[172:175], v[152:159], v255\n\t"
            "ds_read_b64 v[148:149], v253 offset:1816\n\t"
            "ds_read_b64 v[150:151], v253 offset:1912\n\t"
            "v_cvt_pk_bf16_f32 v184, v184, v185\n\t"
            "v_cvt_pk_bf16_f32 v185, v186, v187\n\t"
            "s_nop 1\n\t"
            "v_mfma_f32_4x4x4_16b_bf16 v[196:199], v[184:185], v[184:185], v[196:199]\n\t"
            "ds_read_b128 v[172:175], v254 offset:576\n\t"
            "ds_read_b64 v[152:153], v253 offset:80\n\t"
            "ds_read_b64 v[154:155], v253 offset:176\n\t"
            "v_cvt_pk_bf16_f32 v186, v188, v189\n\t"
            "v_cvt_pk_bf16_f32 v187, v190, v191\n\t"
            "v_mov_b64 v[188:189], 0\n\t"
            "v_mov_b64 v[190:191], 0\n\t"
            "ds_read_b64 v[156:157], v253 offset:1824\n\t"
            "ds_read_b64 v[158:159], v253 offset:1920\n\t"
            "s_waitcnt lgkmcnt(14)\n\t"
            "ds_write_b128 v236, v[176:179] offset:128\n\t"
            "v_mov_b64 v[176:177], 0\n\t"
            "v_mov_b64 v[178:179], 0\n\t"
            "s_waitcnt lgkmcnt(10)\n\t"
            "s_nop 0\n\t"
            "v_smfmac_f32_16x16x64_bf16 v[176:179], v[168:171], v[160:167], v255\n\t"
            "ds_read_b128 v[168:171], v254 offset:640\n\t"
            "v_mfma_f32_4x4x4_16b_bf16 v[196:199], v[186:187], v[186:187], v[196:199]\n\t"
            "ds_read_b64 v[160:161], v253 offset:88\n\t"
            "v_mfma_f32_16x16x32_bf16 v[192:195], v[238:241], v[184:187], v[192:195]\n\t"
            "ds_read_b64 v[162:163], v253 offset:184\n\t"
            "s_waitcnt lgkmcnt(8)\n\t"
            "v_smfmac_f32_16x16x64_bf16 v[180:183], v[172:175], v[144:151], v255\n\t"
            "ds_read_b64 v[164:165], v253 offset:1832\n\t"
            "ds_read_b64 v[166:167], v253 offset:1928\n\t"
            "v_cvt_pk_bf16_f32 v176, v176, v177\n\t"
            "v_cvt_pk_bf16_f32 v177, v178, v179\n\t"
            "s_nop 1\n\t"
            "v_mfma_f32_4x4x4_16b_bf16 v[196:199], v[176:177], v[176:177], v[196:199]\n\t"
            "ds_read_b128 v[172:175], v254 offset:704\n\t"
            "ds_read_b128 v[144:147], v242\n\t"
            "ds_read_b128 v[148:151], v243\n\t"
            "v_cvt_pk_bf16_f32 v178, v180, v181\n\t"
            "v_cvt_pk_bf16_f32 v179, v182, v183\n\t"
            "v_lshlrev_b32 v180, 16, v202\n\t"
            "v_and_b32 v181, 0xffff0000, v202\n\t"
            "v_lshlrev_b32 v182, 16, v203\n\t"
            "v_and_b32 v183, 0xffff0000, v203\n\t"
            "ds_write_b128 v237, v[184:187] offset:128\n\t"
            "v_mov_b64 v[184:185], 0\n\t"
            "v_mov_b64 v[186:187], 0\n\t"
            "s_waitcnt lgkmcnt(8)\n\t"
            "s_nop 0\n\t"
            "v_smfmac_f32_16x16x64_bf16 v[184:187], v[168:171], v[152:159], v255\n\t"
            "ds_read_b128 v[152:155], v242 offset:128\n\t"
            "v_mfma_f32_4x4x4_16b_bf16 v[196:199], v[178:179], v[178:179], v[196:199]\n\t"
            "ds_read_b128 v[156:159], v243 offset:128\n\t"
            "v_mfma_f32_16x16x32_bf16 v[192:195], v[238:241], v[176:179], v[192:195]\n\t"
            "ds_read_b128 v[168:171], v242 offset:6144\n\t"
            "s_waitcnt lgkmcnt(6)\n\t"
            "v_smfmac_f32_16x16x64_bf16 v[188:191], v[172:175], v[160:167], v255\n\t"
            "ds_read_b128 v[160:163], v242 offset:256\n\t"
            "ds_read_b128 v[164:167], v243 offset:256\n\t"
            "v_cvt_pk_bf16_f32 v184, v184, v185\n\t"
            "v_cvt_pk_bf16_f32 v185, v186, v187\n\t"
            "s_nop 1\n\t"
            "v_mfma_f32_4x4x4_16b_bf16 v[196:199], v[184:185], v[184:185], v[196:199]\n\t"
            "ds_read_b128 v[172:175], v243 offset:6144\n\t"
            "s_mov_b32 exec_hi, 0x3fffff\n\t"
            "s_add_u32 m0, s66, 0\n\t"
            "s_add_u32 s49, s79, 0\n\t"
            "buffer_load_dwordx4 v249, s[72:75], s49 offen lds\n\t"
            "s_mov_b32 exec_hi, -1\n\t"
            "v_cvt_pk_bf16_f32 v186, v188, v189\n\t"
            "v_cvt_pk_bf16_f32 v187, v190, v191\n\t"
            "v_lshlrev_b32 v188, 16, v206\n\t"
            "v_and_b32 v189, 0xffff0000, v206\n\t"
            "v_mfma_f32_4x4x4_16b_bf16 v[196:199], v[186:187], v[186:187], v[196:199]\n\t"
            "ds_write_b128 v236, v[176:179] offset:256\n\t"
            "v_mfma_f32_16x16x32_bf16 v[192:195], v[238:241], v[184:187], v[192:195]\n\t"
            "v_lshlrev_b32 v176, 16, v200\n\t"
            "v_and_b32 v177, 0xffff0000, v200\n\t"
            "v_lshlrev_b32 v178, 16, v201\n\t"
            "v_and_b32 v179, 0xffff0000, v201\n\t"
            "v_lshlrev_b32 v190, 16, v207\n\t"
            "v_and_b32 v191, 0xffff0000, v207\n\t"
            "s_waitcnt lgkmcnt(9)\n\t"
            "v_mfma_f32_16x16x32_bf16 v[176:179], v[0:3], v[144:147], v[176:179]\n\t"
            "s_setprio 3\n\t"
            "s_mov_b32 exec_hi, 0x3fffff\n\t"
            "s_add_u32 m0, s66, 872\n\t"
            "s_add_u32 s49, s68, 196224\n\t"
            "buffer_load_dwordx4 v249, s[72:75], s49 offen lds\n\t"
            "s_mov_b32 exec_hi, -1\n\t"
            "s_waitcnt lgkmcnt(8)\n\t"
            "v_mfma_f32_16x16x32_bf16 v[176:179], v[4:7], v[148:151], v[176:179]\n\t"
            "s_mov_b32 exec_hi, 0x3fffff\n\t"
            "s_add_u32 m0, s66, 1744\n\t"
            "s_add_u32 s49, s68, 392448\n\t"
            "buffer_load_dwordx4 v249, s[72:75], s49 offen lds\n\t"
            "s_mov_b32 exec_hi, -1\n\t"
            "s_waitcnt lgkmcnt(6)\n\t"
            "v_mfma_f32_16x16x32_bf16 v[176:179], v[8:11], v[152:155], v[176:179]\n\t"
            "s_mov_b32 exec_hi, 0x3fffff\n\t"
            "s_add_u32 m0, s66, 2616\n\t"
            "s_add_u32 s49, s68, 588672\n\t"
            "buffer_load_dwordx4 v249, s[72:75], s49 offen lds\n\t"
            "s_mov_b32 exec_hi, -1\n\t"
            "s_waitcnt lgkmcnt(5)\n\t"
            "v_mfma_f32_16x16x32_bf16 v[176:179], v[12:15], v[156:159], v[176:179]\n\t"
            "s_mov_b32 exec_hi, 0x3fffff\n\t"
            "s_add_u32 m0, s66, 3488\n\t"
            "s_add_u32 s49, s68, 784896\n\t"
            "buffer_load_dwordx4 v249, s[72:75], s49 offen lds\n\t"
            "s_mov_b32 exec_hi, -1\n\t"
            "s_waitcnt lgkmcnt(3)\n\t"
            "v_mfma_f32_16x16x32_bf16 v[176:179], v[16:19], v[160:163], v[176:179]\n\t"
            "ds_write_b128 v237, v[184:187] offset:256\n\t"
            "s_waitcnt lgkmcnt(3)\n\t"
            "v_mfma_f32_16x16x32_bf16 v[176:179], v[20:23], v[164:167], v[176:179]\n\t"
            "v_lshlrev_b32 v184, 16, v204\n\t"
            "v_and_b32 v185, 0xffff0000, v204\n\t"
            "v_mfma_f32_16x16x32_bf16 v[180:183], v[24:27], v[144:147], v[180:183]\n\t"
            "v_lshlrev_b32 v186, 16, v205\n\t"
            "v_and_b32 v187, 0xffff0000, v205\n\t"
            "v_mfma_f32_16x16x32_bf16 v[180:183], v[28:31], v[148:151], v[180:183]\n\t"
            "s_mov_b32 exec_hi, 0x3fffff\n\t"
            "s_add_u32 m0, s66, 4360\n\t"
            "s_add_u32 s49, s68, 981120\n\t"
            "buffer_load_dwordx4 v249, s[72:75], s49 offen lds\n\t"
            "s_mov_b32 exec_hi, -1\n\t"
            "v_mfma_f32_16x16x32_bf16 v[180:183], v[32:35], v[152:155], v[180:183]\n\t"
            "s_mov_b32 exec_hi, 0x3fffff\n\t"
            "s_add_u32 m0, s66, 5232\n\t"
            "s_add_u32 s49, s68, 1177344\n\t"
            "buffer_load_dwordx4 v249, s[72:75], s49 offen lds\n\t"
            "s_mov_b32 exec_hi, -1\n\t"
            "v_mfma_f32_16x16x32_bf16 v[180:183], v[36:39], v[156:159], v[180:183]\n\t"
            "v_cndmask_b32_e64 v193, v196, v197, s[60:61]\n\t"
            "v_cndmask_b32_e64 v193, v193, v198, s[62:63]\n\t"
            "v_mfma_f32_16x16x32_bf16 v[180:183], v[40:43], v[160:163], v[180:183]\n\t"
            "s_mov_b32 exec_hi, 0x3fffff\n\t"
            "s_add_u32 m0, s66, 6104\n\t"
            "s_add_u32 s49, s68, 1373568\n\t"
            "buffer_load_dwordx4 v249, s[72:75], s49 offen lds\n\t"
            "s_mov_b32 exec_hi, -1\n\t"
            "v_mfma_f32_16x16x32_bf16 v[180:183], v[44:47], v[164:167], v[180:183]\n\t"
            "v_cndmask_b32_e64 v193, v193, v199, s[64:65]\n\t"
            "v_lshlrev_b32 v194, 16, v209\n\t"
            "v_mfma_f32_16x16x32_bf16 v[184:187], v[48:51], v[144:147], v[184:187]\n\t"
            "ds_read_b128 v[196:199], v252\n\t"
            "v_and_b32 v195, 0xffff0000, v209\n\t"
            "v_mfma_f32_16x16x32_bf16 v[184:187], v[52:55], v[148:151], v[184:187]\n\t"
            "s_mov_b32 exec_hi, 0x3fffff\n\t"
            "s_add_u32 m0, s66, 6976\n\t"
            "s_add_u32 s49, s68, 1569792\n\t"
            "buffer_load_dwordx4 v249, s[72:75], s49 offen lds\n\t"
            "s_mov_b32 exec_hi, -1\n\t"
            "v_mfma_f32_16x16x32_bf16 v[184:187], v[56:59], v[152:155], v[184:187]\n\t"
            "s_bfe_u32 s57, s56, 0x20004\n\t"
            "s_lshl_b32 s58, s57, 3\n\t"
            "s_lshr_b32 s58, s55, s58\n\t"
            "s_lshl_b64 s[36:37], s[34:35], s58\n\t"
            "v_mfma_f32_16x16x32_bf16 v[184:187], v[60:63], v[156:159], v[184:187]\n\t"
            "s_bitcmp1_b32 s57, 1\n\t"
            "s_cselect_b32 s58, 1177344, 0\n\t"
            "s_bitcmp1_b32 s57, 0\n\t"
            "s_cselect_b32 s57, 2304, 0\n\t"
            "s_add_u32 s57, s57, s58\n\t"
            "v_mfma_f32_16x16x32_bf16 v[184:187], v[64:67], v[160:163], v[184:187]\n\t"
            "ds_write_b64 v248, v[192:193] offset:18816\n\t"
            "v_mfma_f32_16x16x32_bf16 v[184:187], v[68:71], v[164:167], v[184:187]\n\t"
            "v_lshlrev_b32 v192, 16, v208\n\t"
            "v_and_b32 v193, 0xffff0000, v208\n\t"
            "v_mfma_f32_16x16x32_bf16 v[188:191], v[72:75], v[144:147], v[188:191]\n\t"
            "s_waitcnt lgkmcnt(1)\n\t"
            "s_mov_b64 exec, s[36:37]\n\t"
            "v_add_u32 v196, s57, v196\n\t"
            "s_mov_b64 exec, -1\n\t"
            "v_mfma_f32_16x16x32_bf16 v[188:191], v[76:79], v[148:151], v[188:191]\n\t"
            "v_cvt_pk_bf16_f32 v176, v176, v177\n\t"
            "v_cvt_pk_bf16_f32 v177, v178, v179\n\t"
            "v_mfma_f32_16x16x32_bf16 v[188:191], v[80:83], v[152:155], v[188:191]\n\t"
            "v_cvt_pk_bf16_f32 v178, v180, v181\n\t"
            "v_cvt_pk_bf16_f32 v179, v182, v183\n\t"
            "v_mfma_f32_16x16x32_bf16 v[188:191], v[84:87], v[156:159], v[188:191]\n\t"
            "s_cbranch_vccnz .Lst0_%=\n\t"
            "buffer_store_dwordx4 v[176:179], v196, s[40:43], s45 offen\n\t"
            ".Lsr0_%=:\n\t"
            "v_mfma_f32_16x16x32_bf16 v[188:191], v[88:91], v[160:163], v[188:191]\n\t"
            "s_nop 0\n\t"
            "v_lshlrev_b32 v176, 16, v210\n\t"
            "v_and_b32 v177, 0xffff0000, v210\n\t"
            "v_mfma_f32_16x16x32_bf16 v[188:191], v[92:95], v[164:167], v[188:191]\n\t"
            "v_lshlrev_b32 v178, 16, v211\n\t"
            "v_and_b32 v179, 0xffff0000, v211\n\t"
            "v_mfma_f32_16x16x32_bf16 v[192:195], v[96:99], v[144:147], v[192:195]\n\t"
            "v_lshlrev_b32 v180, 16, v212\n\t"
            "v_and_b32 v181, 0xffff0000, v212\n\t"
            "v_mfma_f32_16x16x32_bf16 v[192:195], v[100:103], v[148:151], v[192:195]\n\t"
            "v_lshlrev_b32 v182, 16, v213\n\t"
            "v_and_b32 v183, 0xffff0000, v213\n\t"
            "v_mfma_f32_16x16x32_bf16 v[192:195], v[104:107], v[152:155], v[192:195]\n\t"
            "v_cvt_pk_bf16_f32 v184, v184, v185\n\t"
            "v_cvt_pk_bf16_f32 v185, v186, v187\n\t"
            "v_mfma_f32_16x16x32_bf16 v[192:195], v[108:111], v[156:159], v[192:195]\n\t"
            "v_cvt_pk_bf16_f32 v186, v188, v189\n\t"
            "v_cvt_pk_bf16_f32 v187, v190, v191\n\t"
            "v_mfma_f32_16x16x32_bf16 v[192:195], v[112:115], v[160:163], v[192:195]\n\t"
            "s_cbranch_vccnz .Lst1_%=\n\t"
            "buffer_store_dwordx4 v[184:187], v196, s[40:43], s45 offen offset:64\n\t"
            ".Lsr1_%=:\n\t"
            "v_mfma_f32_16x16x32_bf16 v[192:195], v[116:119], v[164:167], v[192:195]\n\t"
            "s_nop 0\n\t"
            "v_lshlrev_b32 v184, 16, v214\n\t"
            "v_and_b32 v185, 0xffff0000, v214\n\t"
            "v_mfma_f32_16x16x32_bf16 v[176:179], v[120:123], v[144:147], v[176:179]\n\t"
            "ds_read_b128 v[144:147], v242 offset:6272\n\t"
            "v_lshlrev_b32 v186, 16, v215\n\t"
            "v_mfma_f32_16x16x32_bf16 v[176:179], v[124:127], v[148:151], v[176:179]\n\t"
            "ds_read_b128 v[148:151], v243 offset:6272\n\t"
            "v_and_b32 v187, 0xffff0000, v215\n\t"
            "v_mfma_f32_16x16x32_bf16 v[176:179], v[128:131], v[152:155], v[176:179]\n\t"
            "ds_read_b128 v[152:155], v242 offset:6400\n\t"
            "v_lshlrev_b32 v188, 16, v216\n\t"
            "v_mfma_f32_16x16x32_bf16 v[176:179], v[132:135], v[156:159], v[176:179]\n\t"
            "ds_read_b128 v[156:159], v243 offset:6400\n\t"
            "v_and_b32 v189, 0xffff0000, v216\n\t"
            "v_mfma_f32_16x16x32_bf16 v[176:179], v[136:139], v[160:163], v[176:179]\n\t"
            "ds_read_b128 v[160:163], v242 offset:12288\n\t"
            "v_lshlrev_b32 v190, 16, v217\n\t"
            "v_mfma_f32_16x16x32_bf16 v[176:179], v[140:143], v[164:167], v[176:179]\n\t"
            "ds_read_b128 v[164:167], v243 offset:12288\n\t"
            "v_and_b32 v191, 0xffff0000, v217\n\t"
            "v_mfma_f32_16x16x32_bf16 v[180:183], v[0:3], v[168:171], v[180:183]\n\t"
            "v_cvt_pk_bf16_f32 v192, v192, v193\n\t"
            "v_cvt_pk_bf16_f32 v193, v194, v195\n\t"
            "v_mfma_f32_16x16x32_bf16 v[180:183], v[4:7], v[172:175], v[180:183]\n\t"
            "v_mov_b32 v249, s76\n\t"
            "v_add_u32 v236, s48, v244\n\t"
            "v_cvt_pk_bf16_f32 v194, v176, v177\n\t"
            "s_waitcnt lgkmcnt(5)\n\t"
            "v_mfma_f32_16x16x32_bf16 v[180:183], v[8:11], v[144:147], v[180:183]\n\t"
            "ds_read_b32 v249, v249 offset:4\n\t"
            "v_cvt_pk_bf16_f32 v195, v178, v179\n\t"
            "s_waitcnt lgkmcnt(5)\n\t"
            "v_mfma_f32_16x16x32_bf16 v[180:183], v[12:15], v[148:151], v[180:183]\n\t"
            "ds_read_b128 v[200:203], v236\n\t"
            "v_lshlrev_b32 v176, 16, v220\n\t"
            "s_waitcnt lgkmcnt(5)\n\t"
            "v_mfma_f32_16x16x32_bf16 v[180:183], v[16:19], v[152:155], v[180:183]\n\t"
            "ds_read_b128 v[212:215], v236 offset:6144\n\t"
            "v_and_b32 v177, 0xffff0000, v220\n\t"
            "s_waitcnt lgkmcnt(5)\n\t"
            "v_mfma_f32_16x16x32_bf16 v[180:183], v[20:23], v[156:159], v[180:183]\n\t"
            "s_cbranch_vccnz .Lst2_%=\n\t"
            "buffer_store_dwordx4 v[192:195], v196, s[40:43], s45 offen offset:128\n\t"
            ".Lsr2_%=:\n\t"
            "v_mfma_f32_16x16x32_bf16 v[184:187], v[24:27], v[168:171], v[184:187]\n\t"
            "s_nop 0\n\t"
            "v_lshlrev_b32 v192, 16, v218\n\t"
            "v_and_b32 v193, 0xffff0000, v218\n\t"
            "v_mfma_f32_16x16x32_bf16 v[184:187], v[28:31], v[172:175], v[184:187]\n\t"
            "v_lshlrev_b32 v194, 16, v219\n\t"
            "v_and_b32 v195, 0xffff0000, v219\n\t"
            "v_mfma_f32_16x16x32_bf16 v[184:187], v[32:35], v[144:147], v[184:187]\n\t"
            "v_lshlrev_b32 v178, 16, v221\n\t"
            "v_and_b32 v179, 0xffff0000, v221\n\t"
            "v_mfma_f32_16x16x32_bf16 v[184:187], v[36:39], v[148:151], v[184:187]\n\t"
            "v_cvt_pk_bf16_f32 v180, v180, v181\n\t"
            "v_cvt_pk_bf16_f32 v181, v182, v183\n\t"
            "v_mfma_f32_16x16x32_bf16 v[184:187], v[40:43], v[152:155], v[184:187]\n\t"
            "s_waitcnt lgkmcnt(2)\n\t"
            "v_readfirstlane_b32 s44, v249\n\t"
            "v_add_u32 v237, s48, v245\n\t"
            "v_mfma_f32_16x16x32_bf16 v[184:187], v[44:47], v[156:159], v[184:187]\n\t"
            "s_and_b32 s70, s44, 0x7ffffff\n\t"
            "s_add_u32 s70, s70, s77\n\t"
            "s_and_b32 s44, s44, 0x08000000\n\t"
            "s_or_b32 s44, s44, s39\n\t"
            "v_mfma_f32_16x16x32_bf16 v[188:191], v[48:51], v[168:171], v[188:191]\n\t"
            "ds_read_b32 v249, v252 offset:1028\n\t"
            "v_add_u32 v248, s48, v246\n\t"
            "v_mfma_f32_16x16x32_bf16 v[188:191], v[52:55], v[172:175], v[188:191]\n\t"
            "ds_read_b128 v[204:207], v237\n\t"
            "v_cvt_pk_bf16_f32 v182, v184, v185\n\t"
            "v_mfma_f32_16x16x32_bf16 v[188:191], v[56:59], v[144:147], v[188:191]\n\t"
            "ds_read_b128 v[208:211], v248\n\t"
            "v_cvt_pk_bf16_f32 v183, v186, v187\n\t"
            "v_mfma_f32_16x16x32_bf16 v[188:191], v[60:63], v[148:151], v[188:191]\n\t"
            "ds_read_b128 v[216:219], v237 offset:6144\n\t"
            "v_lshlrev_b32 v184, 16, v224\n\t"
            "v_mfma_f32_16x16x32_bf16 v[188:191], v[64:67], v[152:155], v[188:191]\n\t"
            "s_cbranch_vccnz .Lst3_%=\n\t"
            "buffer_store_dwordx4 v[180:183], v197, s[40:43], s45 offen\n\t"
            ".Lsr3_%=:\n\t"
            "v_mfma_f32_16x16x32_bf16 v[188:191], v[68:71], v[156:159], v[188:191]\n\t"
            "s_nop 0\n\t"
            "v_lshlrev_b32 v180, 16, v222\n\t"
            "v_and_b32 v181, 0xffff0000, v222\n\t"
            "v_mfma_f32_16x16x32_bf16 v[192:195], v[72:75], v[168:171], v[192:195]\n\t"
            "v_lshlrev_b32 v182, 16, v223\n\t"
            "v_and_b32 v183, 0xffff0000, v223\n\t"
            "v_mfma_f32_16x16x32_bf16 v[192:195], v[76:79], v[172:175], v[192:195]\n\t"
            "ds_read_b128 v[220:223], v248 offset:6144\n\t"
            "v_and_b32 v185, 0xffff0000, v224\n\t"
            "v_mfma_f32_16x16x32_bf16 v[192:195], v[80:83], v[144:147], v[192:195]\n\t"
            "v_lshlrev_b32 v186, 16, v225\n\t"
            "v_and_b32 v187, 0xffff0000, v225\n\t"
            "v_mfma_f32_16x16x32_bf16 v[192:195], v[84:87], v[148:151], v[192:195]\n\t"
            "v_cvt_pk_bf16_f32 v188, v188, v189\n\t"
            "v_cvt_pk_bf16_f32 v189, v190, v191\n\t"
            "v_mfma_f32_16x16x32_bf16 v[192:195], v[88:91], v[152:155], v[192:195]\n\t"
            "s_waitcnt lgkmcnt(4)\n\t"
            "v_and_b32 v249, s44, v249\n\t"
            "v_mfma_f32_16x16x32_bf16 v[192:195], v[92:95], v[156:159], v[192:195]\n\t"
            "v_mfma_f32_16x16x32_bf16 v[176:179], v[96:99], v[168:171], v[176:179]\n\t"
            "v_mfma_f32_16x16x32_bf16 v[176:179], v[100:103], v[172:175], v[176:179]\n\t"
            "v_mfma_f32_16x16x32_bf16 v[176:179], v[104:107], v[144:147], v[176:179]\n\t"
            "v_mfma_f32_16x16x32_bf16 v[176:179], v[108:111], v[148:151], v[176:179]\n\t"
            "v_mfma_f32_16x16x32_bf16 v[176:179], v[112:115], v[152:155], v[176:179]\n\t"
            "v_mfma_f32_16x16x32_bf16 v[176:179], v[116:119], v[156:159], v[176:179]\n\t"
            "s_nop 1\n\t"
            "v_cvt_pk_bf16_f32 v190, v192, v193\n\t"
            "v_mfma_f32_16x16x32_bf16 v[180:183], v[120:123], v[168:171], v[180:183]\n\t"
            "ds_read_b128 v[168:171], v242 offset:12416\n\t"
            "v_cvt_pk_bf16_f32 v191, v194, v195\n\t"
            "v_mfma_f32_16x16x32_bf16 v[180:183], v[124:127], v[172:175], v[180:183]\n\t"
            "ds_read_b128 v[172:175], v243 offset:12416\n\t"
            "v_lshlrev_b32 v192, 16, v228\n\t"
            "v_mfma_f32_16x16x32_bf16 v[180:183], v[128:131], v[144:147], v[180:183]\n\t"
            "ds_read_b128 v[144:147], v242 offset:12544\n\t"
            "v_and_b32 v193, 0xffff0000, v228\n\t"
            "v_mfma_f32_16x16x32_bf16 v[180:183], v[132:135], v[148:151], v[180:183]\n\t"
            "ds_read_b128 v[148:151], v243 offset:12544\n\t"
            "v_lshlrev_b32 v194, 16, v229\n\t"
            "v_mfma_f32_16x16x32_bf16 v[180:183], v[136:139], v[152:155], v[180:183]\n\t"
            "s_waitcnt lgkmcnt(0)\n\t"
            "s_barrier\n\t"
            "s_cbranch_vccnz .Lst4_%=\n\t"
            "buffer_store_dwordx4 v[188:191], v197, s[40:43], s45 offen offset:64\n\t"
            ".Lsr4_%=:\n\t"
            "v_mfma_f32_16x16x32_bf16 v[180:183], v[140:143], v[156:159], v[180:183]\n\t"
            "s_nop 0\n\t"
            "v_lshlrev_b32 v188, 16, v226\n\t"
            "v_and_b32 v189, 0xffff0000, v226\n\t"
            "v_mfma_f32_16x16x32_bf16 v[184:187], v[0:3], v[160:163], v[184:187]\n\t"
            "v_lshlrev_b32 v190, 16, v227\n\t"
            "v_and_b32 v191, 0xffff0000, v227\n\t"
            "v_mfma_f32_16x16x32_bf16 v[184:187], v[4:7], v[164:167], v[184:187]\n\t"
            "ds_read_b128 v[224:227], v236 offset:12288\n\t"
            "v_and_b32 v195, 0xffff0000, v229\n\t"
            "v_mfma_f32_16x16x32_bf16 v[184:187], v[8:11], v[168:171], v[184:187]\n\t"
            "v_cvt_pk_bf16_f32 v176, v176, v177\n\t"
            "v_cvt_pk_bf16_f32 v177, v178, v179\n\t"
            "v_mfma_f32_16x16x32_bf16 v[184:187], v[12:15], v[172:175], v[184:187]\n\t"
            "v_cvt_pk_bf16_f32 v178, v180, v181\n\t"
            "v_cvt_pk_bf16_f32 v179, v182, v183\n\t"
            "v_mfma_f32_16x16x32_bf16 v[184:187], v[16:19], v[144:147], v[184:187]\n\t"
            "s_cbranch_vccnz .Lst5_%=\n\t"
            "buffer_store_dwordx4 v[176:179], v197, s[40:43], s45 offen offset:128\n\t"
            ".Lsr5_%=:\n\t"
            "v_mfma_f32_16x16x32_bf16 v[184:187], v[20:23], v[148:151], v[184:187]\n\t"
            "s_nop 0\n\t"
            "v_lshlrev_b32 v176, 16, v230\n\t"
            "v_and_b32 v177, 0xffff0000, v230\n\t"
            "v_mfma_f32_16x16x32_bf16 v[188:191], v[24:27], v[160:163], v[188:191]\n\t"
            "v_lshlrev_b32 v178, 16, v231\n\t"
            "v_and_b32 v179, 0xffff0000, v231\n\t"
            "v_mfma_f32_16x16x32_bf16 v[188:191], v[28:31], v[164:167], v[188:191]\n\t"
            "ds_read_b128 v[228:231], v237 offset:12288\n\t"
            "v_lshlrev_b32 v180, 16, v232\n\t"
            "v_mfma_f32_16x16x32_bf16 v[188:191], v[32:35], v[168:171], v[188:191]\n\t"
            "v_and_b32 v181, 0xffff0000, v232\n\t"
            "v_lshlrev_b32 v182, 16, v233\n\t"
            "v_mfma_f32_16x16x32_bf16 v[188:191], v[36:39], v[172:175], v[188:191]\n\t"
            "v_and_b32 v183, 0xffff0000, v233\n\t"
            "v_cvt_pk_bf16_f32 v184, v184, v185\n\t"
            "v_mfma_f32_16x16x32_bf16 v[188:191], v[40:43], v[144:147], v[188:191]\n\t"
            "v_cvt_pk_bf16_f32 v185, v186, v187\n\t"
            "v_mfma_f32_16x16x32_bf16 v[188:191], v[44:47], v[148:151], v[188:191]\n\t"
            "v_mfma_f32_16x16x32_bf16 v[192:195], v[48:51], v[160:163], v[192:195]\n\t"
            "v_mfma_f32_16x16x32_bf16 v[192:195], v[52:55], v[164:167], v[192:195]\n\t"
            "v_mfma_f32_16x16x32_bf16 v[192:195], v[56:59], v[168:171], v[192:195]\n\t"
            "v_mfma_f32_16x16x32_bf16 v[192:195], v[60:63], v[172:175], v[192:195]\n\t"
            "v_mfma_f32_16x16x32_bf16 v[192:195], v[64:67], v[144:147], v[192:195]\n\t"
            "v_mfma_f32_16x16x32_bf16 v[192:195], v[68:71], v[148:151], v[192:195]\n\t"
            "s_nop 1\n\t"
            "v_cvt_pk_bf16_f32 v186, v188, v189\n\t"
            "v_mfma_f32_16x16x32_bf16 v[176:179], v[72:75], v[160:163], v[176:179]\n\t"
            "v_cvt_pk_bf16_f32 v187, v190, v191\n\t"
            "v_mfma_f32_16x16x32_bf16 v[176:179], v[76:79], v[164:167], v[176:179]\n\t"
            "s_cbranch_vccnz .Lst6_%=\n\t"
            "buffer_store_dwordx4 v[184:187], v198, s[40:43], s45 offen\n\t"
            ".Lsr6_%=:\n\t"
            "v_mfma_f32_16x16x32_bf16 v[176:179], v[80:83], v[168:171], v[176:179]\n\t"
            "s_nop 0\n\t"
            "v_lshlrev_b32 v184, 16, v234\n\t"
            "v_and_b32 v185, 0xffff0000, v234\n\t"
            "v_mfma_f32_16x16x32_bf16 v[176:179], v[84:87], v[172:175], v[176:179]\n\t"
            "v_lshlrev_b32 v186, 16, v235\n\t"
            "v_and_b32 v187, 0xffff0000, v235\n\t"
            "v_mfma_f32_16x16x32_bf16 v[176:179], v[88:91], v[144:147], v[176:179]\n\t"
            "ds_read_b128 v[232:235], v248 offset:12288\n\t"
            "v_cvt_pk_bf16_f32 v192, v192, v193\n\t"
            "v_mfma_f32_16x16x32_bf16 v[176:179], v[92:95], v[148:151], v[176:179]\n\t"
            "v_cvt_pk_bf16_f32 v193, v194, v195\n\t"
            "v_mfma_f32_16x16x32_bf16 v[180:183], v[96:99], v[160:163], v[180:183]\n\t"
            "v_mfma_f32_16x16x32_bf16 v[180:183], v[100:103], v[164:167], v[180:183]\n\t"
            "v_mfma_f32_16x16x32_bf16 v[180:183], v[104:107], v[168:171], v[180:183]\n\t"
            "v_mfma_f32_16x16x32_bf16 v[180:183], v[108:111], v[172:175], v[180:183]\n\t"
            "v_mfma_f32_16x16x32_bf16 v[180:183], v[112:115], v[144:147], v[180:183]\n\t"
            "s_nop 1\n\t"
            "v_cvt_pk_bf16_f32 v194, v176, v177\n\t"
            "v_mfma_f32_16x16x32_bf16 v[180:183], v[116:119], v[148:151], v[180:183]\n\t"
            "v_cvt_pk_bf16_f32 v195, v178, v179\n\t"
            "v_mfma_f32_16x16x32_bf16 v[184:187], v[120:123], v[160:163], v[184:187]\n\t"
            "s_cbranch_vccnz .Lst7_%=\n\t"
            "buffer_store_dwordx4 v[192:195], v198, s[40:43], s45 offen offset:64\n\t"
            ".Lsr7_%=:\n\t"
            "v_mfma_f32_16x16x32_bf16 v[184:187], v[124:127], v[164:167], v[184:187]\n\t"
            "s_waitcnt vmcnt(8)\n\t"
            "ds_read_b64 v[152:153], v253 offset:8\n\t"
            "v_mfma_f32_16x16x32_bf16 v[184:187], v[128:131], v[168:171], v[184:187]\n\t"
            "ds_read_b128 v[168:171], v254\n\t"
            "v_cvt_pk_bf16_f32 v180, v180, v181\n\t"
            "v_mfma_f32_16x16x32_bf16 v[184:187], v[132:135], v[172:175], v[184:187]\n\t"
            "ds_read_b128 v[172:175], v254 offset:64\n\t"
            "v_cvt_pk_bf16_f32 v181, v182, v183\n\t"
            "v_mfma_f32_16x16x32_bf16 v[184:187], v[136:139], v[144:147], v[184:187]\n\t"
            "ds_read_b64 v[144:145], v253\n\t"
            "v_mfma_f32_16x16x32_bf16 v[184:187], v[140:143], v[148:151], v[184:187]\n\t"
            "ds_read_b64 v[146:147], v253 offset:96\n\t"
            "ds_read_b64 v[148:149], v253 offset:1744\n\t"
            "ds_read_b64 v[150:151], v253 offset:1840\n\t"
            "ds_read_b64 v[154:155], v253 offset:104\n\t"
            "ds_read_b64 v[156:157], v253 offset:1752\n\t"
            "ds_read_b64 v[158:159], v253 offset:1848\n\t"
            "ds_read_b64 v[160:161], v253 offset:16\n\t"
            "ds_read_b64 v[162:163], v253 offset:112\n\t"
            "v_cvt_pk_bf16_f32 v182, v184, v185\n\t"
            "v_cvt_pk_bf16_f32 v183, v186, v187\n\t"
            "s_waitcnt lgkmcnt(14)\n\t"
            "ds_read_b64 v[164:165], v253 offset:1760\n\t"
            "s_waitcnt lgkmcnt(14)\n\t"
            "ds_read_b64 v[166:167], v253 offset:1856\n\t"
            "s_cbranch_vccnz .Lst8_%=\n\t"
            "buffer_store_dwordx4 v[180:183], v198, s[40:43], s45 offen offset:128\n\t"
            ".Lsr8_%=:\n\t"
            "s_mov_b32 m0, s32\n\t"
            "s_add_u32 s49, s70, s25\n\t"
            "buffer_load_dword v249, s[72:75], s70 offen lds\n\t"
            "buffer_load_dword v249, s[72:75], s49 offen lds\n\t"
            "s_add_u32 s45, s46, s27\n\t"
            "s_mov_b32 s46, s47\n\t"
            "s_mov_b32 s47, s68\n\t"
            "s_xor_b32 s48, s48, s71\n\t"
            "s_xor_b32 s67, s67, s71\n\t"
            "s_add_u32 s76, s76, 4\n\t"
            "s_lshl_b32 s56, s56, 2\n\t"
            "s_lshr_b32 s49, s26, 30\n\t"
            "s_or_b32 s56, s56, s49\n\t"
            "s_cmp_ge_u32 s76, s24\n\t"
            "s_cselect_b64 vcc, -1, 0\n\t"
            "s_cmp_lt_u32 s46, 0x7000000\n\t"
            "s_cbranch_scc1 .Lwb_loop%=\n\t"
            "s_branch .Lsx%=\n\t"
            ".Lst0_%=:\n\t"
            "buffer_store_dwordx4 v[176:179], v196, s[40:43], s45 offen sc1\n\t"
            "s_branch .Lsr0_%=\n\t"
            ".Lst1_%=:\n\t"
            "buffer_store_dwordx4 v[184:187], v196, s[40:43], s45 offen offset:64 sc1\n\t"
            "s_branch .Lsr1_%=\n\t"
            ".Lst2_%=:\n\t"
            "buffer_store_dwordx4 v[192:195], v196, s[40:43], s45 offen offset:128 sc1\n\t"
            "s_branch .Lsr2_%=\n\t"
            ".Lst3_%=:\n\t"
            "buffer_store_dwordx4 v[180:183], v197, s[40:43], s45 offen sc1\n\t"
            "s_branch .Lsr3_%=\n\t"
            ".Lst4_%=:\n\t"
            "buffer_store_dwordx4 v[188:191], v197, s[40:43], s45 offen offset:64 sc1\n\t"
            "s_branch .Lsr4_%=\n\t"
            ".Lst5_%=:\n\t"
            "buffer_store_dwordx4 v[176:179], v197, s[40:43], s45 offen offset:128 sc1\n\t"
            "s_branch .Lsr5_%=\n\t"
            ".Lst6_%=:\n\t"
            "buffer_store_dwordx4 v[184:187], v198, s[40:43], s45 offen sc1\n\t"
            "s_branch .Lsr6_%=\n\t"
            ".Lst7_%=:\n\t"
            "buffer_store_dwordx4 v[192:195], v198, s[40:43], s45 offen offset:64 sc1\n\t"
            "s_branch .Lsr7_%=\n\t"
            ".Lst8_%=:\n\t"
            "buffer_store_dwordx4 v[180:183], v198, s[40:43], s45 offen offset:128 sc1\n\t"
            "s_branch .Lsr8_%=\n\t"
            ".Lsx%=:\n\t"
            "s_waitcnt lgkmcnt(0)\n\t"
            "s_nop 7\n\t"
            "s_setprio 3\n\t"
            "s_nop 7\n\t"
            "ds_read_b128 v[196:199], v252\n\t"
            "s_bfe_u32 s57, s56, 0x20004\n\t"
            "s_lshl_b32 s58, s57, 3\n\t"
            "s_lshr_b32 s58, s55, s58\n\t"
            "s_lshl_b64 s[36:37], s[34:35], s58\n\t"
            "s_bitcmp1_b32 s57, 1\n\t"
            "s_cselect_b32 s58, 1177344, 0\n\t"
            "s_bitcmp1_b32 s57, 0\n\t"
            "s_cselect_b32 s57, 2304, 0\n\t"
            "s_add_u32 s57, s57, s58\n\t"
            "s_waitcnt lgkmcnt(0)\n\t"
            "s_mov_b64 exec, s[36:37]\n\t"
            "v_add_u32 v196, s57, v196\n\t"
            "s_mov_b64 exec, -1\n\t"
            "s_nop 7\n\t"
            "v_mov_b32 v144, s23\n\t"
            ".Ldp%=:\n\t"
            "ds_read_b128 v[148:151], v144 offset:96\n\t"
            "s_waitcnt lgkmcnt(0)\n\t"
            "v_or_b32 v148, v148, v149\n\t"
            "v_or3_b32 v148, v148, v150, v151\n\t"
            "v_cmp_gt_i32 vcc, 0, v148\n\t"
            "s_nop 4\n\t"
            "s_cbranch_vccnz .Ldp%=\n\t"
            "s_nop 7\n\t"
            "ds_read_b128 v[184:187], v242\n\t"
            "v_lshlrev_b32 v152, 16, v200\n\t"
            "v_and_b32 v153, 0xffff0000, v200\n\t"
            "v_lshlrev_b32 v154, 16, v201\n\t"
            "v_and_b32 v155, 0xffff0000, v201\n\t"
            "v_lshlrev_b32 v156, 16, v202\n\t"
            "v_and_b32 v157, 0xffff0000, v202\n\t"
            "v_lshlrev_b32 v158, 16, v203\n\t"
            "v_and_b32 v159, 0xffff0000, v203\n\t"
            "v_lshlrev_b32 v160, 16, v204\n\t"
            "v_and_b32 v161, 0xffff0000, v204\n\t"
            "v_lshlrev_b32 v162, 16, v205\n\t"
            "v_and_b32 v163, 0xffff0000, v205\n\t"
            "v_lshlrev_b32 v164, 16, v206\n\t"
            "v_and_b32 v165, 0xffff0000, v206\n\t"
            "v_lshlrev_b32 v166, 16, v207\n\t"
            "v_and_b32 v167, 0xffff0000, v207\n\t"
            "v_lshlrev_b32 v176, 16, v208\n\t"
            "v_and_b32 v177, 0xffff0000, v208\n\t"
            "v_lshlrev_b32 v178, 16, v209\n\t"
            "v_and_b32 v179, 0xffff0000, v209\n\t"
            "v_lshlrev_b32 v180, 16, v210\n\t"
            "v_and_b32 v181, 0xffff0000, v210\n\t"
            "v_lshlrev_b32 v182, 16, v211\n\t"
            "v_and_b32 v183, 0xffff0000, v211\n\t"
            "v_lshlrev_b32 v144, 16, v212\n\t"
            "v_and_b32 v145, 0xffff0000, v212\n\t"
            "v_lshlrev_b32 v146, 16, v213\n\t"
            "v_and_b32 v147, 0xffff0000, v213\n\t"
            "v_lshlrev_b32 v148, 16, v214\n\t"
            "v_and_b32 v149, 0xffff0000, v214\n\t"
            "s_waitcnt lgkmcnt(0)\n\t"
            "v_mfma_f32_16x16x32_bf16 v[152:155], v[0:3], v[184:187], v[152:155]\n\t"
            "ds_read_b128 v[188:191], v243\n\t"
            "v_lshlrev_b32 v150, 16, v215\n\t"
            "v_mfma_f32_16x16x32_bf16 v[156:159], v[24:27], v[184:187], v[156:159]\n\t"
            "ds_read_b128 v[192:195], v242 offset:128\n\t"
            "v_and_b32 v151, 0xffff0000, v215\n\t"
            "v_mfma_f32_16x16x32_bf16 v[160:163], v[48:51], v[184:187], v[160:163]\n\t"
            "v_lshlrev_b32 v168, 16, v216\n\t"
            "v_and_b32 v169, 0xffff0000, v216\n\t"
            "v_mfma_f32_16x16x32_bf16 v[164:167], v[72:75], v[184:187], v[164:167]\n\t"
            "v_lshlrev_b32 v170, 16, v217\n\t"
            "v_and_b32 v171, 0xffff0000, v217\n\t"
            "v_mfma_f32_16x16x32_bf16 v[176:179], v[96:99], v[184:187], v[176:179]\n\t"
            "v_lshlrev_b32 v172, 16, v218\n\t"
            "v_and_b32 v173, 0xffff0000, v218\n\t"
            "v_mfma_f32_16x16x32_bf16 v[180:183], v[120:123], v[184:187], v[180:183]\n\t"
            "ds_read_b128 v[184:187], v243 offset:128\n\t"
            "v_lshlrev_b32 v174, 16, v219\n\t"
            "v_and_b32 v175, 0xffff0000, v219\n\t"
            "s_waitcnt lgkmcnt(2)\n\t"
            "v_mfma_f32_16x16x32_bf16 v[152:155], v[4:7], v[188:191], v[152:155]\n\t"
            "v_mfma_f32_16x16x32_bf16 v[156:159], v[28:31], v[188:191], v[156:159]\n\t"
            "v_mfma_f32_16x16x32_bf16 v[160:163], v[52:55], v[188:191], v[160:163]\n\t"
            "v_mfma_f32_16x16x32_bf16 v[164:167], v[76:79], v[188:191], v[164:167]\n\t"
            "v_mfma_f32_16x16x32_bf16 v[176:179], v[100:103], v[188:191], v[176:179]\n\t"
            "v_mfma_f32_16x16x32_bf16 v[180:183], v[124:127], v[188:191], v[180:183]\n\t"
            "ds_read_b128 v[188:191], v242 offset:256\n\t"
            "s_waitcnt lgkmcnt(2)\n\t"
            "v_mfma_f32_16x16x32_bf16 v[152:155], v[8:11], v[192:195], v[152:155]\n\t"
            "v_mfma_f32_16x16x32_bf16 v[156:159], v[32:35], v[192:195], v[156:159]\n\t"
            "v_mfma_f32_16x16x32_bf16 v[160:163], v[56:59], v[192:195], v[160:163]\n\t"
            "v_mfma_f32_16x16x32_bf16 v[164:167], v[80:83], v[192:195], v[164:167]\n\t"
            "v_mfma_f32_16x16x32_bf16 v[176:179], v[104:107], v[192:195], v[176:179]\n\t"
            "v_mfma_f32_16x16x32_bf16 v[180:183], v[128:131], v[192:195], v[180:183]\n\t"
            "ds_read_b128 v[192:195], v243 offset:256\n\t"
            "s_waitcnt lgkmcnt(2)\n\t"
            "v_mfma_f32_16x16x32_bf16 v[152:155], v[12:15], v[184:187], v[152:155]\n\t"
            "v_mfma_f32_16x16x32_bf16 v[156:159], v[36:39], v[184:187], v[156:159]\n\t"
            "v_mfma_f32_16x16x32_bf16 v[160:163], v[60:63], v[184:187], v[160:163]\n\t"
            "v_mfma_f32_16x16x32_bf16 v[164:167], v[84:87], v[184:187], v[164:167]\n\t"
            "v_mfma_f32_16x16x32_bf16 v[176:179], v[108:111], v[184:187], v[176:179]\n\t"
            "v_mfma_f32_16x16x32_bf16 v[180:183], v[132:135], v[184:187], v[180:183]\n\t"
            "ds_read_b128 v[184:187], v242 offset:6144\n\t"
            "s_waitcnt lgkmcnt(2)\n\t"
            "v_mfma_f32_16x16x32_bf16 v[152:155], v[16:19], v[188:191], v[152:155]\n\t"
            "v_mfma_f32_16x16x32_bf16 v[156:159], v[40:43], v[188:191], v[156:159]\n\t"
            "v_mfma_f32_16x16x32_bf16 v[160:163], v[64:67], v[188:191], v[160:163]\n\t"
            "v_mfma_f32_16x16x32_bf16 v[164:167], v[88:91], v[188:191], v[164:167]\n\t"
            "v_mfma_f32_16x16x32_bf16 v[176:179], v[112:115], v[188:191], v[176:179]\n\t"
            "v_mfma_f32_16x16x32_bf16 v[180:183], v[136:139], v[188:191], v[180:183]\n\t"
            "ds_read_b128 v[188:191], v243 offset:6144\n\t"
            "s_waitcnt lgkmcnt(2)\n\t"
            "v_mfma_f32_16x16x32_bf16 v[152:155], v[20:23], v[192:195], v[152:155]\n\t"
            "v_mfma_f32_16x16x32_bf16 v[156:159], v[44:47], v[192:195], v[156:159]\n\t"
            "v_mfma_f32_16x16x32_bf16 v[160:163], v[68:71], v[192:195], v[160:163]\n\t"
            "v_mfma_f32_16x16x32_bf16 v[164:167], v[92:95], v[192:195], v[164:167]\n\t"
            "v_mfma_f32_16x16x32_bf16 v[176:179], v[116:119], v[192:195], v[176:179]\n\t"
            "v_mfma_f32_16x16x32_bf16 v[180:183], v[140:143], v[192:195], v[180:183]\n\t"
            "ds_read_b128 v[192:195], v242 offset:6272\n\t"
            "s_waitcnt lgkmcnt(2)\n\t"
            "v_mfma_f32_16x16x32_bf16 v[144:147], v[0:3], v[184:187], v[144:147]\n\t"
            "v_cvt_pk_bf16_f32 v152, v152, v153\n\t"
            "v_cvt_pk_bf16_f32 v153, v154, v155\n\t"
            "v_mfma_f32_16x16x32_bf16 v[148:151], v[24:27], v[184:187], v[148:151]\n\t"
            "v_cvt_pk_bf16_f32 v160, v160, v161\n\t"
            "v_cvt_pk_bf16_f32 v161, v162, v163\n\t"
            "v_mfma_f32_16x16x32_bf16 v[168:171], v[48:51], v[184:187], v[168:171]\n\t"
            "v_cvt_pk_bf16_f32 v154, v156, v157\n\t"
            "v_cvt_pk_bf16_f32 v155, v158, v159\n\t"
            "v_mfma_f32_16x16x32_bf16 v[172:175], v[72:75], v[184:187], v[172:175]\n\t"
            "v_cvt_pk_bf16_f32 v162, v164, v165\n\t"
            "v_cvt_pk_bf16_f32 v163, v166, v167\n\t"
            "s_waitcnt lgkmcnt(1)\n\t"
            "v_mfma_f32_16x16x32_bf16 v[144:147], v[4:7], v[188:191], v[144:147]\n\t"
            "v_lshlrev_b32 v156, 16, v220\n\t"
            "v_and_b32 v157, 0xffff0000, v220\n\t"
            "v_mfma_f32_16x16x32_bf16 v[148:151], v[28:31], v[188:191], v[148:151]\n\t"
            "v_lshlrev_b32 v158, 16, v221\n\t"
            "v_and_b32 v159, 0xffff0000, v221\n\t"
            "v_mfma_f32_16x16x32_bf16 v[168:171], v[52:55], v[188:191], v[168:171]\n\t"
            "v_lshlrev_b32 v164, 16, v222\n\t"
            "v_and_b32 v165, 0xffff0000, v222\n\t"
            "v_mfma_f32_16x16x32_bf16 v[156:159], v[96:99], v[184:187], v[156:159]\n\t"
            "v_lshlrev_b32 v166, 16, v223\n\t"
            "v_and_b32 v167, 0xffff0000, v223\n\t"
            "v_mfma_f32_16x16x32_bf16 v[172:175], v[76:79], v[188:191], v[172:175]\n\t"
            "buffer_store_dwordx4 v[152:155], v196, s[40:43], s45 offen sc1\n\t"
            "v_mfma_f32_16x16x32_bf16 v[164:167], v[120:123], v[184:187], v[164:167]\n\t"
            "ds_read_b128 v[184:187], v243 offset:6272\n\t"
            "v_cvt_pk_bf16_f32 v176, v176, v177\n\t"
            "v_mfma_f32_16x16x32_bf16 v[156:159], v[100:103], v[188:191], v[156:159]\n\t"
            "buffer_store_dwordx4 v[160:163], v196, s[40:43], s45 offen offset:64 sc1\n\t"
            "v_mfma_f32_16x16x32_bf16 v[164:167], v[124:127], v[188:191], v[164:167]\n\t"
            "ds_read_b128 v[188:191], v242 offset:6400\n\t"
            "v_cvt_pk_bf16_f32 v177, v178, v179\n\t"
            "s_waitcnt lgkmcnt(2)\n\t"
            "v_mfma_f32_16x16x32_bf16 v[144:147], v[8:11], v[192:195], v[144:147]\n\t"
            "v_cvt_pk_bf16_f32 v178, v180, v181\n\t"
            "v_cvt_pk_bf16_f32 v179, v182, v183\n\t"
            "v_mfma_f32_16x16x32_bf16 v[148:151], v[32:35], v[192:195], v[148:151]\n\t"
            "buffer_store_dwordx4 v[176:179], v196, s[40:43], s45 offen offset:128 sc1\n\t"
            "v_mfma_f32_16x16x32_bf16 v[168:171], v[56:59], v[192:195], v[168:171]\n\t"
            "v_lshlrev_b32 v152, 16, v224\n\t"
            "v_and_b32 v153, 0xffff0000, v224\n\t"
            "v_mfma_f32_16x16x32_bf16 v[172:175], v[80:83], v[192:195], v[172:175]\n\t"
            "v_lshlrev_b32 v154, 16, v225\n\t"
            "v_and_b32 v155, 0xffff0000, v225\n\t"
            "v_mfma_f32_16x16x32_bf16 v[156:159], v[104:107], v[192:195], v[156:159]\n\t"
            "v_lshlrev_b32 v180, 16, v226\n\t"
            "v_and_b32 v181, 0xffff0000, v226\n\t"
            "v_mfma_f32_16x16x32_bf16 v[164:167], v[128:131], v[192:195], v[164:167]\n\t"
            "ds_read_b128 v[192:195], v243 offset:6400\n\t"
            "v_lshlrev_b32 v182, 16, v227\n\t"
            "s_waitcnt lgkmcnt(2)\n\t"
            "v_mfma_f32_16x16x32_bf16 v[144:147], v[12:15], v[184:187], v[144:147]\n\t"
            "v_and_b32 v183, 0xffff0000, v227\n\t"
            "v_lshlrev_b32 v160, 16, v228\n\t"
            "v_mfma_f32_16x16x32_bf16 v[148:151], v[36:39], v[184:187], v[148:151]\n\t"
            "v_and_b32 v161, 0xffff0000, v228\n\t"
            "v_lshlrev_b32 v162, 16, v229\n\t"
            "s_waitcnt lgkmcnt(1)\n\t"
            "v_mfma_f32_16x16x32_bf16 v[144:147], v[16:19], v[188:191], v[144:147]\n\t"
            "v_and_b32 v163, 0xffff0000, v229\n\t"
            "v_lshlrev_b32 v176, 16, v230\n\t"
            "v_mfma_f32_16x16x32_bf16 v[168:171], v[60:63], v[184:187], v[168:171]\n\t"
            "v_and_b32 v177, 0xffff0000, v230\n\t"
            "v_lshlrev_b32 v178, 16, v231\n\t"
            "v_mfma_f32_16x16x32_bf16 v[148:151], v[40:43], v[188:191], v[148:151]\n\t"
            "v_and_b32 v179, 0xffff0000, v231\n\t"
            "v_mfma_f32_16x16x32_bf16 v[172:175], v[84:87], v[184:187], v[172:175]\n\t"
            "v_mfma_f32_16x16x32_bf16 v[168:171], v[64:67], v[188:191], v[168:171]\n\t"
            "v_mfma_f32_16x16x32_bf16 v[156:159], v[108:111], v[184:187], v[156:159]\n\t"
            "s_waitcnt lgkmcnt(0)\n\t"
            "v_mfma_f32_16x16x32_bf16 v[144:147], v[20:23], v[192:195], v[144:147]\n\t"
            "v_mfma_f32_16x16x32_bf16 v[148:151], v[44:47], v[192:195], v[148:151]\n\t"
            "v_mfma_f32_16x16x32_bf16 v[164:167], v[132:135], v[184:187], v[164:167]\n\t"
            "ds_read_b128 v[184:187], v242 offset:12288\n\t"
            "v_mfma_f32_16x16x32_bf16 v[172:175], v[88:91], v[188:191], v[172:175]\n\t"
            "v_mfma_f32_16x16x32_bf16 v[168:171], v[68:71], v[192:195], v[168:171]\n\t"
            "v_mfma_f32_16x16x32_bf16 v[156:159], v[112:115], v[188:191], v[156:159]\n\t"
            "s_nop 1\n\t"
            "v_cvt_pk_bf16_f32 v144, v144, v145\n\t"
            "v_mfma_f32_16x16x32_bf16 v[164:167], v[136:139], v[188:191], v[164:167]\n\t"
            "ds_read_b128 v[188:191], v243 offset:12288\n\t"
            "v_cvt_pk_bf16_f32 v145, v146, v147\n\t"
            "v_mfma_f32_16x16x32_bf16 v[172:175], v[92:95], v[192:195], v[172:175]\n\t"
            "v_cvt_pk_bf16_f32 v146, v148, v149\n\t"
            "v_cvt_pk_bf16_f32 v147, v150, v151\n\t"
            "v_mfma_f32_16x16x32_bf16 v[156:159], v[116:119], v[192:195], v[156:159]\n\t"
            "buffer_store_dwordx4 v[144:147], v197, s[40:43], s45 offen sc1\n\t"
            "v_mfma_f32_16x16x32_bf16 v[164:167], v[140:143], v[192:195], v[164:167]\n\t"
            "ds_read_b128 v[192:195], v242 offset:12416\n\t"
            "v_cvt_pk_bf16_f32 v168, v168, v169\n\t"
            "s_waitcnt lgkmcnt(2)\n\t"
            "v_mfma_f32_16x16x32_bf16 v[152:155], v[0:3], v[184:187], v[152:155]\n\t"
            "v_cvt_pk_bf16_f32 v169, v170, v171\n\t"
            "v_cvt_pk_bf16_f32 v170, v172, v173\n\t"
            "v_mfma_f32_16x16x32_bf16 v[180:183], v[24:27], v[184:187], v[180:183]\n\t"
            "v_cvt_pk_bf16_f32 v171, v174, v175\n\t"
            "v_cvt_pk_bf16_f32 v156, v156, v157\n\t"
            "v_mfma_f32_16x16x32_bf16 v[160:163], v[48:51], v[184:187], v[160:163]\n\t"
            "buffer_store_dwordx4 v[168:171], v197, s[40:43], s45 offen offset:64 sc1\n\t"
            "v_mfma_f32_16x16x32_bf16 v[176:179], v[72:75], v[184:187], v[176:179]\n\t"
            "v_cvt_pk_bf16_f32 v157, v158, v159\n\t"
            "v_cvt_pk_bf16_f32 v158, v164, v165\n\t"
            "s_waitcnt lgkmcnt(1)\n\t"
            "v_mfma_f32_16x16x32_bf16 v[152:155], v[4:7], v[188:191], v[152:155]\n\t"
            "v_cvt_pk_bf16_f32 v159, v166, v167\n\t"
            "v_lshlrev_b32 v164, 16, v232\n\t"
            "v_mfma_f32_16x16x32_bf16 v[180:183], v[28:31], v[188:191], v[180:183]\n\t"
            "buffer_store_dwordx4 v[156:159], v197, s[40:43], s45 offen offset:128 sc1\n\t"
            "v_mfma_f32_16x16x32_bf16 v[160:163], v[52:55], v[188:191], v[160:163]\n\t"
            "v_and_b32 v165, 0xffff0000, v232\n\t"
            "v_lshlrev_b32 v166, 16, v233\n\t"
            "v_mfma_f32_16x16x32_bf16 v[176:179], v[76:79], v[188:191], v[176:179]\n\t"
            "v_and_b32 v167, 0xffff0000, v233\n\t"
            "v_lshlrev_b32 v156, 16, v234\n\t"
            "s_waitcnt lgkmcnt(0)\n\t"
            "v_mfma_f32_16x16x32_bf16 v[152:155], v[8:11], v[192:195], v[152:155]\n\t"
            "v_and_b32 v157, 0xffff0000, v234\n\t"
            "v_lshlrev_b32 v158, 16, v235\n\t"
            "v_mfma_f32_16x16x32_bf16 v[164:167], v[96:99], v[184:187], v[164:167]\n\t"
            "v_and_b32 v159, 0xffff0000, v235\n\t"
            "v_mfma_f32_16x16x32_bf16 v[164:167], v[100:103], v[188:191], v[164:167]\n\t"
            "s_nop 0\n\t"
            "v_mfma_f32_16x16x32_bf16 v[156:159], v[120:123], v[184:187], v[156:159]\n\t"
            "ds_read_b128 v[184:187], v243 offset:12416\n\t"
            "v_mfma_f32_16x16x32_bf16 v[156:159], v[124:127], v[188:191], v[156:159]\n\t"
            "ds_read_b128 v[188:191], v242 offset:12544\n\t"
            "v_mfma_f32_16x16x32_bf16 v[180:183], v[32:35], v[192:195], v[180:183]\n\t"
            "v_mfma_f32_16x16x32_bf16 v[160:163], v[56:59], v[192:195], v[160:163]\n\t"
            "v_mfma_f32_16x16x32_bf16 v[176:179], v[80:83], v[192:195], v[176:179]\n\t"
            "v_mfma_f32_16x16x32_bf16 v[164:167], v[104:107], v[192:195], v[164:167]\n\t"
            "v_mfma_f32_16x16x32_bf16 v[156:159], v[128:131], v[192:195], v[156:159]\n\t"
            "ds_read_b128 v[192:195], v243 offset:12544\n\t"
            "s_waitcnt lgkmcnt(2)\n\t"
            "v_mfma_f32_16x16x32_bf16 v[152:155], v[12:15], v[184:187], v[152:155]\n\t"
            "v_mfma_f32_16x16x32_bf16 v[180:183], v[36:39], v[184:187], v[180:183]\n\t"
            "s_waitcnt lgkmcnt(1)\n\t"
            "v_mfma_f32_16x16x32_bf16 v[152:155], v[16:19], v[188:191], v[152:155]\n\t"
            "v_mfma_f32_16x16x32_bf16 v[160:163], v[60:63], v[184:187], v[160:163]\n\t"
            "v_mfma_f32_16x16x32_bf16 v[180:183], v[40:43], v[188:191], v[180:183]\n\t"
            "v_mfma_f32_16x16x32_bf16 v[176:179], v[84:87], v[184:187], v[176:179]\n\t"
            "s_waitcnt lgkmcnt(0)\n\t"
            "v_mfma_f32_16x16x32_bf16 v[152:155], v[20:23], v[192:195], v[152:155]\n\t"
            "v_mfma_f32_16x16x32_bf16 v[160:163], v[64:67], v[188:191], v[160:163]\n\t"
            "v_mfma_f32_16x16x32_bf16 v[180:183], v[44:47], v[192:195], v[180:183]\n\t"
            "v_mfma_f32_16x16x32_bf16 v[164:167], v[108:111], v[184:187], v[164:167]\n\t"
            "v_mfma_f32_16x16x32_bf16 v[176:179], v[88:91], v[188:191], v[176:179]\n\t"
            "v_mfma_f32_16x16x32_bf16 v[156:159], v[132:135], v[184:187], v[156:159]\n\t"
            "v_mfma_f32_16x16x32_bf16 v[160:163], v[68:71], v[192:195], v[160:163]\n\t"
            "s_nop 1\n\t"
            "v_cvt_pk_bf16_f32 v152, v152, v153\n\t"
            "v_mfma_f32_16x16x32_bf16 v[164:167], v[112:115], v[188:191], v[164:167]\n\t"
            "v_cvt_pk_bf16_f32 v153, v154, v155\n\t"
            "v_mfma_f32_16x16x32_bf16 v[176:179], v[92:95], v[192:195], v[176:179]\n\t"
            "v_cvt_pk_bf16_f32 v154, v180, v181\n\t"
            "v_cvt_pk_bf16_f32 v155, v182, v183\n\t"
            "v_mfma_f32_16x16x32_bf16 v[156:159], v[136:139], v[188:191], v[156:159]\n\t"
            "buffer_store_dwordx4 v[152:155], v198, s[40:43], s45 offen sc1\n\t"
            "v_mfma_f32_16x16x32_bf16 v[164:167], v[116:119], v[192:195], v[164:167]\n\t"
            "v_cvt_pk_bf16_f32 v160, v160, v161\n\t"
            "v_cvt_pk_bf16_f32 v161, v162, v163\n\t"
            "v_mfma_f32_16x16x32_bf16 v[156:159], v[140:143], v[192:195], v[156:159]\n\t"
            "v_cvt_pk_bf16_f32 v162, v176, v177\n\t"
            "v_cvt_pk_bf16_f32 v163, v178, v179\n\t"
            "buffer_store_dwordx4 v[160:163], v198, s[40:43], s45 offen offset:64 sc1\n\t"
            "s_nop 1\n\t"
            "v_cvt_pk_bf16_f32 v164, v164, v165\n\t"
            "v_cvt_pk_bf16_f32 v165, v166, v167\n\t"
            "s_nop 0\n\t"
            "v_cvt_pk_bf16_f32 v166, v156, v157\n\t"
            "v_cvt_pk_bf16_f32 v167, v158, v159\n\t"
            "buffer_store_dwordx4 v[164:167], v198, s[40:43], s45 offen offset:128 sc1\n\t"
            "s_nop 7\n\t"
            ".Lre%=:\n\t"
            : "+{v[224:227]}"(f0), "+{v[228:231]}"(f1), "+{v[232:235]}"(f2), "+{v242}"(r242), "+{v243}"(r243), "+{v[244:247]}"(r244),
              "+{s45}"(s45), "+{s46}"(s46), "+{s47}"(s47), "+{s48}"(s48), "+{s67}"(s67), "+{s76}"(s76), "+{s56}"(s56), "+{s44}"(s44), "+{s70}"(s70), "+{s21}"(s21)
            : B_VIN, B_SIN
            : "v0", "v1", "v2", "v3", "v4", "v5", "v6", "v7", "v8", "v9", "v10", "v11", "v12", "v13", "v14", "v15", "v16", "v17", "v18", "v19", "v20", "v21", "v22", "v23", "v24", "v25", "v26", "v27", "v28", "v29", "v30", "v31", "v32", "v33", "v34", "v35", "v36", "v37", "v38", "v39", "v40", "v41", "v42", "v43", "v44", "v45", "v46", "v47", "v48", "v49", "v50", "v51", "v52", "v53", "v54", "v55", "v56", "v57", "v58", "v59", "v60", "v61", "v62", "v63", "v64", "v65", "v66", "v67", "v68", "v69", "v70", "v71", "v72", "v73", "v74", "v75", "v76", "v77", "v78", "v79", "v80", "v81", "v82", "v83", "v84", "v85", "v86", "v87", "v88", "v89", "v90", "v91", "v92", "v93", "v94", "v95", "v96", "v97", "v98", "v99", "v100", "v101", "v102", "v103", "v104", "v105", "v106", "v107", "v108", "v109", "v110", "v111", "v112", "v113", "v114", "v115", "v116", "v117", "v118", "v119", "v120", "v121", "v122", "v123", "v124", "v125", "v126", "v127", "v128", "v129", "v130", "v131", "v132", "v133", "v134", "v135", "v136", "v137", "v138", "v139", "v140", "v141", "v142", "v143", "v144", "v145", "v146", "v147", "v148", "v149", "v150", "v151", "v152", "v153", "v154", "v155", "v156", "v157", "v158", "v159", "v160", "v161", "v162", "v163", "v164", "v165", "v166", "v167", "v168", "v169", "v170", "v171", "v172", "v173", "v174", "v175", "v176", "v177", "v178", "v179", "v180", "v181", "v182", "v183", "v184", "v185", "v186", "v187", "v188", "v189", "v190", "v191", "v192", "v193", "v194", "v195", "v196", "v197", "v198", "v199", "v200", "v201", "v202", "v203", "v204", "v205", "v206", "v207", "v208", "v209", "v210", "v211", "v212", "v213", "v214", "v215", "v216", "v217", "v218", "v219", "v220", "v221", "v222", "v223", "v236", "v237", "v248", "v249", "s26", "s68", "s69", "s79", "s36", "s37", "s38", "s57", "s58", "s59", "s49", "vcc", "scc", "m0", "memory");
        }
        asm volatile("wk_code_end1:");
        asm volatile("wk_code_end2:");

    }
}

extern "C" void kernel_launch(void* const* d_in, const int* in_sizes, int n_in,
                              void* d_out, int out_size, void* d_ws, size_t ws_size,
                              hipStream_t stream) {
    const unsigned short* x = (const unsigned short*)d_in[0];
    const unsigned short* dw = (const unsigned short*)d_in[1];
    const unsigned short* wqkv = (const unsigned short*)d_in[2];
    const unsigned short* wo = (const unsigned short*)d_in[3];
    unsigned short* out = (unsigned short*)d_out;
    u32x4* wf = (u32x4*)d_ws;
    win_attn_kernel<<<GRID, 512, 0, stream>>>(x, dw, wqkv, wo, wf, out);
}
```

```cpp
#include <hip/hip_runtime.h>
#include <hip/hip_bf16.h>
#include <stdint.h>


typedef unsigned u32x4 __attribute__((ext_vector_type(4)));

constexpr int IMG = 511;
constexpr int CH = 192;
constexpr int NWX = 73;
constexpr int NWIN = NWX * NWX;
constexpr int GRID = 256;
constexpr int NSHORT = 21 * GRID - NWIN;
constexpr int ROWBYTES = IMG * CH * 2;
constexpr int XBIAS = (IMG + 1) * CH * 2;
constexpr unsigned OOB = 0x40000000u;

constexpr int X_OFF = 32768;
constexpr int X1_OFF = 53632;
constexpr int O_OFF = 74496;
constexpr int V_OFF = 92928;
constexpr int KQL_OFF = 111360;
constexpr int OL_OFF = 128256;
constexpr int AT_OFF = 137472;
constexpr int CT_OFF = AT_OFF + 19584;
constexpr int RS_OFF = CT_OFF + 4096;
constexpr int WL_OFF = RS_OFF + 2304 + 64;
constexpr int LDS_BYTES = WL_OFF + 128;
#define RS_BASE (RS_OFF + (wave < 4 ? wave * 384 : 768 + wave * 192))
constexpr int TP_OFF = X1_OFF + 8192;
constexpr int STL_OFF = O_OFF + 12288;
#define LB_OFF(b) ((b) < 7 ? V_OFF + (b) * 6160 : X1_OFF + ((b) - 7) * 6160)
#define LPOS(wc) (((wc) & 1) ? 0 : 6)
constexpr unsigned CROW = 0x060a0230u;
constexpr unsigned CLANE = 0x00132C3Fu;

constexpr unsigned long long TOKMAP[6] = {0x20001c190f2a2202ull, 0x052517121f061a13ull, 0x181e08151d0b260cull, 0x1b2b2903102d0901ull, 0x04162e0e2824142cull, 0x110a072127232f0dull};
constexpr unsigned long long ROWMAP[8] = {0x08321b240f1c0006ull, 0x23310e2a09170d14ull, 0x210c2f030a273418ull, 0x1d2b11221f043739ull, 0x3234363a103e192dull, 0x291a20022c3f1605ull, 0x303a3c2628122e15ull, 0x39253d1e01070b13ull};

constexpr int WF_BYTES = 8 * 36 * 1024;
constexpr int EP_OFF = WF_BYTES;
constexpr int CN_OFF = EP_OFF + 256, FL_OFF = EP_OFF + 512;
constexpr int SH_OFF = EP_OFF + 1024;
constexpr int WS_BYTES = SH_OFF + GRID * 12288;
constexpr int GO_OFF = WL_OFF + 124;
constexpr int PG_OFF = WL_OFF + 96;
constexpr int SPIN_MAX = 1 << 20;

#define BAR() asm volatile("s_waitcnt lgkmcnt(0)\n\ts_barrier" ::: "memory")
#define OPQ(x) asm volatile("" : "+v"(x))
__host__ __device__ __forceinline__ constexpr unsigned byte_of(unsigned long long a, unsigned long long b, int i) {
    return (unsigned)(((i & 8) ? b : a) >> (8 * (i & 7))) & 0xFFu;
}
__host__ __device__ __forceinline__ constexpr void seq_to_win(int seq, int& wy, int& wx) {
    const bool last = seq >= 5110;
    const int s = last ? 35 : seq / 146, r = seq - 146 * s;
    const int kx = last ? r / 6 : r >> 2, pos = r - (last ? 6 : 4) * kx;
    const bool low = last && pos >= (kx == 36 ? 2 : 4);
    wy = low ? 72 : 2 * s + (pos & 1);
    wx = 2 * kx + (low ? (kx == 36 ? 0 : pos - 4) : pos >> 1);
}
__host__ __device__ __forceinline__ constexpr int win_to_seq(int wy, int wx) {
    const int kx = wx >> 1;
    return wy == 72 ? 5110 + 6 * kx + (kx == 36 ? 2 : 4 + (wx & 1)) : 146 * (wy >> 1) + (wy >= 70 ? 6 : 4) * kx + 2 * (wx & 1) + (wy & 1);
}
__host__ __device__ __forceinline__ constexpr int block_of(int wy, int wx) { return wy >= 70 ? 1295 + (wx & ~1) + (wy == 72) : 37 * (wy >> 1) + (wx >> 1); }
__host__ __device__ __forceinline__ constexpr void block_pos(int b, int& ky, int& kx) {
    if (b >= 1295) { kx = (b - 1295) >> 1; ky = 70 + 2 * ((b - 1295) & 1); }
    else { const int s = b / 37; kx = b - 37 * s; ky = 2 * s; }
}
__host__ __device__ __forceinline__ constexpr unsigned wl_entry(int seq) {
    int wy = 0, wx = 0;
    seq_to_win(seq, wy, wx);
    return (unsigned)((wy * 7 * IMG + wx * 7) * (CH * 2)) | (wy == 0 ? 0x08000000u : 0u) | (wx == 0 ? 0x10000000u : 0u) |
           (wx == NWX - 1 ? 0x20000000u : 0u) | ((unsigned)(wx & 1) << 30) | ((unsigned)(wy & 1) << 31);
}
__host__ __device__ __forceinline__ constexpr unsigned dma_off(int lane) {
    const int col = (lane * 43) >> 8;
    return (unsigned)(col * (CH * 2 - 96) + 16 * lane) | (col == 0 ? 0x10000000u : 0u) | (col == 8 ? 0x20000000u : 0u);
}

__host__ __device__ __forceinline__ constexpr unsigned pf_off(int lane) {
    const int l = lane < 63 ? lane : 62, r = l / 21;
    return (unsigned)(r * ROWBYTES + (l - 21 * r) * 128) | (r == 0 ? 0x08000000u : 0u);
}
constexpr int RECN = 80;
struct Tables { unsigned rec[GRID][RECN]; unsigned img[768]; };
constexpr Tables make_tables() {
    Tables t = {};
    for (int bx = 0; bx < GRID; ++bx) {
        unsigned* r = t.rec[bx];
        const int q0 = (bx & 7) * 32 + (bx >> 3), cnt = q0 < NSHORT ? 20 : 21, start = q0 < NSHORT ? 20 * q0 : 21 * q0 - NSHORT;
        for (int i = 0; i < 32; ++i) r[i] = i < cnt ? wl_entry(start + i) : 0x7ffffffu;
        int wy = 0, wx = 0;
        seq_to_win(start, wy, wx);
        const int key0 = block_of(wy, wx);
        seq_to_win(start + cnt - 1, wy, wx);
        const int nblk = block_of(wy, wx) - key0 + 1;
        for (int b = 0; b < 8; ++b) {
            int ky = 0, kx = 0;
            block_pos(key0 + b, ky, kx);
            r[64 + b] = b < nblk ? (unsigned)(((7 * ky + 5) * IMG + 14 * kx + 5) * (CH * 2) + XBIAS) | (kx == 36 ? 1u : 0u) : OOB;
            for (int c = 0; c < 4; ++c) {
                const int y = ky + (c >> 1), x = 2 * kx + (c & 1), i = win_to_seq(y, x) - start;
                const bool ok = b < nblk && y < NWX && x < NWX && i >= 0 && i < cnt;
                r[32 + 4 * b + c] = ok ? (unsigned)(((7 * y + LPOS(y)) * IMG + 7 * x + LPOS(x)) * (CH * 2) + i) : OOB + 47;
            }
        }
        r[72] = q0; r[73] = cnt;
    }
    for (int lane = 0; lane < 64; ++lane) {
        for (int e = 0; e < 3; ++e) {
            const int tk = byte_of(TOKMAP[2 * e], TOKMAP[2 * e + 1], lane & 15);
            t.img[192 + 4 * lane + e] = (unsigned)((tk / 7) * ROWBYTES + (tk % 7) * (CH * 2)) + 16 * (lane >> 4);
        }
        t.img[448 + 4 * lane] = dma_off(lane);
        t.img[449 + 4 * lane] = pf_off(lane);
    }
    return t;
}
static __constant__ const Tables TAB = make_tables();
typedef __attribute__((address_space(1))) unsigned gu32;
typedef __attribute__((address_space(1))) unsigned long long gu64;
#define RLX_AGENT __ATOMIC_RELAXED, __HIP_MEMORY_SCOPE_AGENT
#define TAG_OF(e) (((unsigned long long)~((e) + 1u) << 32) | ((e) + 1u))

__global__ __launch_bounds__(512, 2) void win_attn_kernel(const unsigned short* __restrict__ x, const unsigned short* __restrict__ dw,
                                                          const unsigned short* __restrict__ wqkv, const unsigned short* __restrict__ wo,
                                                          u32x4* wf, unsigned short* __restrict__ out) {
    __shared__ __attribute__((aligned(16))) char smem[LDS_BYTES];

    asm volatile("wk_code_begin:");
    const int tid = threadIdx.x;
    const int wave = __builtin_amdgcn_readfirstlane(tid >> 6);
    const int w = wave & 3;
    const unsigned ldsb = (unsigned)(size_t)smem;
#define ROLE_SETUP                                                                                 \
    int bx = blockIdx.x;                                                                           \
    asm volatile("" : "+s"(bx));                                                                   \
    bx = __builtin_amdgcn_readfirstlane(bx);                                                       \
    const unsigned* rec = TAB.rec[bx];                                                             \
    const unsigned en0 = rec[0];                                                                   \
    const int q0 = rec[72], cnt = rec[73];
    asm volatile(
        "s_load_dwordx8 s[24:31], %[ka], 0x0\n\t"
        "s_load_dwordx2 s[32:33], %[ka], 0x20\n\t"
        "s_mul_i32 s36, %[bx], %[recb]\n\t"
        "s_xor_b32 s37, %[wave], 4\n\t"
        "s_lshl2_add_u32 s38, s37, s36\n\t"
        "s_load_dword s38, %[tab], s38 offset:0x100\n\t"
        "s_load_dword s39, %[tab], s36\n\t"
        "v_mbcnt_lo_u32_b32 v1, -1, 0\n\t"
        "v_mbcnt_hi_u32_b32 v1, -1, v1\n\t"
        "s_and_b32 s44, %[bx], 7\n\t"
        "s_lshl_b32 s44, s44, 5\n\t"
        "s_lshr_b32 s45, %[bx], 3\n\t"
        "s_add_u32 s44, s44, s45\n\t"
        "v_lshlrev_b32 v2, 4, v1\n\t"
        "s_mov_b32 s43, 0x20000\n\t"
        "s_cmp_ge_u32 s44, 36\n\t"
        "s_cbranch_scc1 .Lnp%=\n\t"
        "s_mul_i32 s45, s44, 6\n\t"
        "s_lshl1_add_u32 s46, s44, 210\n\t"
        "s_cmp_ge_u32 %[wave], 6\n\t"
        "s_cselect_b64 vcc, -1, 0\n\t"
        "s_cselect_b32 s45, s46, s45\n\t"
        "s_movk_i32 s52, 1152\n\t"
        "s_cselect_b32 s52, 384, s52\n\t"
        "s_cselect_b32 s53, 0, 0xbbaaaaab\n\t"
        "s_add_u32 s45, s45, %[wave]\n\t"
        "s_mul_i32 s46, s45, 171\n\t"
        "s_lshr_b32 s46, s46, 10\n\t"
        "s_mul_i32 s47, s46, 6\n\t"
        "s_sub_u32 s47, s45, s47\n\t"
        "s_mul_i32 s48, s46, 43\n\t"
        "s_lshr_b32 s48, s48, 8\n\t"
        "s_mul_i32 s49, s48, 6\n\t"
        "s_sub_u32 s49, s46, s49\n\t"
        "s_mul_i32 s50, s48, 48\n\t"
        "s_cmp_lt_u32 s49, 3\n\t"
        "s_cselect_b32 s34, 1.0, 0x3e553b94\n\t"
        "s_movk_i32 s51, -48\n\t"
        "s_cselect_b32 s51, 192, s51\n\t"
        "s_cmp_lt_u32 s48, 4\n\t"
        "s_cselect_b32 s51, s51, s50\n\t"
        "s_cselect_b32 s34, s34, 1.0\n\t"
        "s_add_u32 s51, s51, s50\n\t"
        "s_lshl4_add_u32 s51, s49, s51\n\t"
        "s_and_b32 s49, s49, 1\n\t"
        "s_mul_i32 s49, s49, 12\n\t"
        "s_add_u32 s49, s49, 576\n\t"
        "s_cmp_ge_u32 %[wave], 6\n\t"
        "s_cselect_b32 s49, s49, 0\n\t"
        "s_sub_u32 s51, s51, s49\n\t"
        "s_and_b32 s49, s47, 1\n\t"
        "s_mul_i32 s49, s49, 88\n\t"
        "s_lshl3_add_u32 s47, s47, s49\n\t"
        "v_lshrrev_b32 v3, 5, v1\n\t"
        "v_mul_u32_u24 v3, 48, v3\n\t"
        "v_lshrrev_b32 v4, 1, v1\n\t"
        "v_and_or_b32 v3, v4, 8, v3\n\t"
        "v_add_u32 v3, s47, v3\n\t"
        "v_mul_u32_u24 v3, s52, v3\n\t"
        "v_and_b32 v4, 15, v1\n\t"
        "v_and_b32 v5, 12, v1\n\t"
        "v_cndmask_b32 v5, 0, v5, vcc\n\t"
        "v_add3_u32 v5, v4, v5, s51\n\t"
        "v_lshl_add_u32 v3, v5, 1, v3\n\t"
        "v_mov_b64 v[8:9], 0\n\t"
        "v_mov_b64 v[10:11], 0\n\t"
        "v_mov_b64 v[12:13], 0\n\t"
        "v_mov_b64 v[14:15], 0\n\t"
        "s_mul_i32 s42, s52, 192\n\t"
        "s_cmp_ge_u32 %[wave], 6\n\t"
        "s_waitcnt lgkmcnt(0)\n\t"
        "s_cselect_b64 s[40:41], s[30:31], s[28:29]\n\t"
        "s_and_b32 s41, s41, 0xffff\n\t"
        "buffer_load_short_d16_hi v8, v3, s[40:43], 0 offen\n\t"
        "buffer_load_short_d16_hi v9, v3, s[40:43], s52 offen\n\t"
        "s_lshl_b32 s46, s52, 1\n\t"
        "buffer_load_short_d16_hi v10, v3, s[40:43], s46 offen\n\t"
        "s_add_u32 s46, s46, s52\n\t"
        "buffer_load_short_d16_hi v11, v3, s[40:43], s46 offen\n\t"
        "s_add_u32 s46, s46, s52\n\t"
        "buffer_load_short_d16_hi v12, v3, s[40:43], s46 offen\n\t"
        "s_add_u32 s46, s46, s52\n\t"
        "buffer_load_short_d16_hi v13, v3, s[40:43], s46 offen\n\t"
        "s_add_u32 s46, s46, s52\n\t"
        "buffer_load_short_d16_hi v14, v3, s[40:43], s46 offen\n\t"
        "s_add_u32 s46, s46, s52\n\t"
        "buffer_load_short_d16_hi v15, v3, s[40:43], s46 offen\n\t"
        "v_mov_b32 v6, %[ep]\n\t"
        "global_load_dword v7, v6, s[32:33] sc1\n\t"
        "v_mov_b32 v5, %[red]\n\t"
        "v_mad_u32_u24 v4, v4, %[c144], v5\n\t"
        "v_lshrrev_b32 v5, 2, v1\n\t"
        "v_and_b32 v5, 12, v5\n\t"
        "s_lshl_b32 s46, %[wave], 4\n\t"
        "v_add3_u32 v5, v4, v5, s46\n\t"
        "s_mov_b32 s40, s32\n\t"
        "s_and_b32 s41, s33, 0xffff\n\t"
        "s_mov_b32 s42, %[wsb]\n\t"
        "s_lshl_b32 s45, s45, 10\n\t"
        "s_waitcnt vmcnt(1)\n\t"
        "v_add_f32 v16, v8, v10\n\t"
        "v_add_f32 v17, v9, v11\n\t"
        "v_add_f32 v18, v12, v14\n\t"
        "v_add_f32 v19, v13, v15\n\t"
        "v_add_f32 v16, v16, v18\n\t"
        "v_add_f32 v17, v17, v19\n\t"
        "v_add_f32 v16, v16, v17\n\t"
        "ds_write_b32 v5, v16\n\t"
        "s_waitcnt lgkmcnt(0)\n\t"
        "s_barrier\n\t"
        "ds_read_b128 v[16:19], v4\n\t"
        "ds_read_b128 v[20:23], v4 offset:16\n\t"
        "ds_read_b128 v[24:27], v4 offset:32\n\t"
        "ds_read_b128 v[28:31], v4 offset:48\n\t"
        "ds_read_b128 v[32:35], v4 offset:64\n\t"
        "ds_read_b128 v[36:39], v4 offset:80\n\t"
        "s_waitcnt lgkmcnt(4)\n\t"
        "v_add_f32 v16, v16, v18\n\t"
        "v_add_f32 v17, v17, v19\n\t"
        "v_add_f32 v20, v20, v22\n\t"
        "v_add_f32 v21, v21, v23\n\t"
        "s_waitcnt lgkmcnt(2)\n\t"
        "v_add_f32 v24, v24, v26\n\t"
        "v_add_f32 v25, v25, v27\n\t"
        "v_add_f32 v28, v28, v30\n\t"
        "v_add_f32 v29, v29, v31\n\t"
        "v_add_f32 v16, v16, v20\n\t"
        "v_add_f32 v17, v17, v21\n\t"
        "s_waitcnt lgkmcnt(0)\n\t"
        "v_add_f32 v32, v32, v34\n\t"
        "v_add_f32 v33, v33, v35\n\t"
        "v_add_f32 v36, v36, v38\n\t"
        "v_add_f32 v37, v37, v39\n\t"
        "v_add_f32 v24, v24, v28\n\t"
        "v_add_f32 v25, v25, v29\n\t"
        "v_add_f32 v32, v32, v36\n\t"
        "v_add_f32 v33, v33, v37\n\t"
        "v_add_f32 v16, v16, v24\n\t"
        "v_add_f32 v17, v17, v25\n\t"
        "v_add_f32 v16, v16, v32\n\t"
        "v_add_f32 v17, v17, v33\n\t"
        "v_add_f32 v16, v16, v17\n\t"
        "v_mul_f32 v16, s53, v16\n\t"
        "v_mul_f32 v16, s34, v16\n\t"
        "v_fma_f32 v8, v8, s34, v16\n\t"
        "v_fma_f32 v9, v9, s34, v16\n\t"
        "v_fma_f32 v10, v10, s34, v16\n\t"
        "v_fma_f32 v11, v11, s34, v16\n\t"
        "v_fma_f32 v12, v12, s34, v16\n\t"
        "v_fma_f32 v13, v13, s34, v16\n\t"
        "v_fma_f32 v14, v14, s34, v16\n\t"
        "v_fma_f32 v15, v15, s34, v16\n\t"
        "v_cvt_pk_bf16_f32 v8, v8, v9\n\t"
        "v_cvt_pk_bf16_f32 v9, v10, v11\n\t"
        "v_cvt_pk_bf16_f32 v10, v12, v13\n\t"
        "v_cvt_pk_bf16_f32 v11, v14, v15\n\t"
        "buffer_store_dwordx4 v[8:11], v2, s[40:43], s45 offen sc1\n\t"
        "s_or_b32 s46, s44, s37\n\t"
        "s_cmp_lg_u32 s46, 0\n\t"
        "s_cbranch_scc1 .Lnc%=\n\t"
        "v_mov_b32 v3, 0\n\t"
        "global_store_dword v6, v3, s[32:33] offset:256 sc1\n\t"
        ".Lnc%=:\n\t"
        "v_lshl_add_u32 v6, s44, 3, v6\n\t"
        "s_waitcnt vmcnt(0)\n\t"
        "s_barrier\n\t"
        "s_cmp_lg_u32 %[wave], 0\n\t"
        "s_cbranch_scc1 .Lnp%=\n\t"
        "v_add_u32 v4, 1, v7\n\t"
        "v_not_b32 v5, v4\n\t"
        "s_mov_b64 exec, 1\n\t"
        "global_store_dwordx2 v6, v[4:5], s[32:33] offset:512 sc1\n\t"
        "s_mov_b64 exec, -1\n\t"
        ".Lnp%=:\n\t"
        "s_getpc_b64 s[40:41]\n\t"
        ".Lwk_here%=:\n\t"
        "s_sub_u32 s40, s40, .Lwk_here%=-wk_code_begin\n\t"
        "s_subb_u32 s41, s41, 0\n\t"
        "s_and_b32 s41, s41, 0xffff\n\t"
        "s_mov_b32 s42, wk_code_end-wk_code_begin\n\t"
        "s_max_u32 s42, s42, wk_code_end1-wk_code_begin\n\t"
        "s_max_u32 s42, s42, wk_code_end2-wk_code_begin\n\t"
        "s_add_u32 m0, %[lds], %[sink]\n\t"
        "v_lshlrev_b32 v3, 7, v1\n\t"
        "s_lshl_b32 s45, %[wave], 13\n\t"
        "buffer_load_dword v3, s[40:43], s45 offen lds\n\t"
        "s_mov_b64 s[40:41], %[tab]\n\t"
        "s_and_b32 s41, s41, 0xffff\n\t"
        "s_mov_b32 s42, %[tabb]\n\t"
        "s_lshl_b32 s46, %[wave], 10\n\t"
        "s_add_u32 s47, s46, %[imgs]\n\t"
        "s_add_u32 s48, s46, %[imgd]\n\t"
        "s_lshl_b32 s49, %[wave], 8\n\t"
        "s_add_u32 s49, s49, %[mapd]\n\t"
        "s_add_u32 s50, s36, 128\n\t"
        "s_mov_b64 vcc, -1\n\t"
        "s_cmp_eq_u32 %[wave], 0\n\t"
        "s_cselect_b32 s47, s36, s47\n\t"
        "s_cselect_b32 s48, %[wld], s48\n\t"
        "s_cselect_b64 vcc, 0xff, vcc\n\t"
        "s_cmp_ge_u32 %[wave], 4\n\t"
        "s_cselect_b32 s47, s50, s47\n\t"
        "s_cselect_b32 s48, s49, s48\n\t"
        "s_cselect_b64 exec, 0xff, vcc\n\t"
        "s_add_u32 m0, %[lds], s48\n\t"
        "s_and_b32 s49, %[wave], 3\n\t"
        "buffer_load_dwordx4 v2, s[40:43], s47 offen lds\n\t"
        "s_mov_b64 exec, -1\n\t"
        "v_mul_u32_u24 v3, 43, v1\n\t"
        "v_lshrrev_b32 v3, 8, v3\n\t"
        "v_mad_u32_u24 v4, v3, %[c288], v2\n\t"
        "s_mul_i32 s49, s49, 96\n\t"
        "s_mul_i32 s50, s37, 6160\n\t"
        "s_add_u32 s50, s50, %[lb0]\n\t"
        "s_cmp_eq_u32 s37, 7\n\t"
        "s_cselect_b32 s50, %[lb7], s50\n\t"
        "s_add_u32 s46, s46, %[tp]\n\t"
        "s_waitcnt lgkmcnt(0)\n\t"
        "s_cmp_ge_u32 %[wave], 4\n\t"
        "s_cbranch_scc1 .Lib%=\n\t"
        "s_mov_b32 s40, s26\n\t"
        "s_and_b32 s41, s27, 0xffff\n\t"
        "s_movk_i32 s42, 3456\n\t"
        "s_add_u32 m0, %[lds], s46\n\t"
        "s_nop 0\n\t"
        "buffer_load_dwordx4 v4, s[40:43], s49 offen lds\n\t"
        ".Lib%=:\n\t"
        "s_sub_u32 s40, s24, %[xb]\n\t"
        "s_subb_u32 s41, s25, 0\n\t"
        "s_and_b32 s41, s41, 0xffff\n\t"
        "s_mov_b32 s42, %[xsz]\n\t"
        "s_cmp_eq_u32 s38, 2.0\n\t"
        "s_cbranch_scc1 .Lic%=\n\t"
        "s_mov_b32 exec_hi, 0xffff\n\t"
        "s_add_u32 m0, %[lds], s50\n\t"
        "s_mov_b32 s46, 4\n\t"
        "s_bitcmp1_b32 s38, 0\n\t"
        "s_cselect_b32 s47, 2.0, s38\n\t"
        "s_bitset0_b32 s38, 0\n\t"
        ".Lid%=:\n\t"
        "buffer_load_dwordx4 v2, s[40:43], s38 offen lds\n\t"
        "buffer_load_dwordx4 v2, s[40:43], s47 offen offset:768 lds\n\t"
        "s_add_u32 m0, m0, 1536\n\t"
        "s_add_u32 s38, s38, %[rb]\n\t"
        "s_add_u32 s47, s47, %[rb]\n\t"
        "s_sub_u32 s46, s46, 1\n\t"
        "s_cmp_lg_u32 s46, 0\n\t"
        "s_cbranch_scc1 .Lid%=\n\t"
        ".Lic%=:\n\t"
        "s_cmp_lt_u32 %[wave], 4\n\t"
        "s_cbranch_scc1 .Lie%=\n\t"
        "s_mov_b32 exec_hi, 0x3fffff\n\t"
        "s_and_b32 s46, s39, 0x10000000\n\t"
        "s_and_b32 s47, s39, 0x20000000\n\t"
        "v_cmp_eq_u32 vcc, 0, v3\n\t"
        "v_mov_b32 v5, s46\n\t"
        "v_mov_b32 v6, s47\n\t"
        "v_cndmask_b32 v5, 0, v5, vcc\n\t"
        "v_cmp_eq_u32 vcc, 8, v3\n\t"
        "s_and_b32 s46, s39, 0x7ffffff\n\t"
        "s_add_u32 s46, s46, s49\n\t"
        "v_cndmask_b32 v6, 0, v6, vcc\n\t"
        "v_or3_b32 v4, v4, v5, v6\n\t"
        "s_bitcmp1_b32 s39, 27\n\t"
        "s_cselect_b32 s47, 2.0, s46\n\t"
        "s_lshl_b32 s45, s37, 13\n\t"
        "s_add_u32 m0, %[lds], s45\n\t"
        "s_mov_b32 s48, 8\n\t"
        "buffer_load_dwordx4 v4, s[40:43], s47 offen lds\n\t"
        ".Lia%=:\n\t"
        "s_add_u32 m0, m0, 872\n\t"
        "s_add_u32 s46, s46, %[rb]\n\t"
        "s_sub_u32 s48, s48, 1\n\t"
        "buffer_load_dwordx4 v4, s[40:43], s46 offen lds\n\t"
        "s_cmp_lg_u32 s48, 0\n\t"
        "s_cbranch_scc1 .Lia%=\n\t"
        ".Lie%=:\n\t"
        "s_mov_b64 exec, -1\n\t"
        :: [ka] "s"(__builtin_amdgcn_kernarg_segment_ptr()), [bx] "s"(blockIdx.x), [wave] "s"(wave), [tab] "s"(&TAB), [lds] "s"(ldsb),
           [red] "s"(ldsb + O_OFF), [c144] "s"(144), [c288] "s"(288), [recb] "n"(RECN * 4), [ep] "n"(EP_OFF), [wsb] "n"(WS_BYTES), [sink] "n"(X_OFF + 18432),
           [tabb] "n"(sizeof(Tables)), [imgs] "n"(GRID * RECN * 4 - 1024), [imgd] "n"(AT_OFF + 18816 - 1024), [mapd] "n"(TP_OFF + 3072), [wld] "n"(WL_OFF),
           [lb0] "n"(LB_OFF(0)), [lb7] "n"(LB_OFF(7)), [tp] "n"(TP_OFF), [xb] "n"(XBIAS), [xsz] "n"(IMG * IMG * CH * 2 + XBIAS), [rb] "n"(ROWBYTES)
        : "v1", "v2", "v3", "v4", "v5", "v6", "v7", "v8", "v9", "v10", "v11", "v12", "v13", "v14", "v15", "v16", "v17", "v18", "v19", "v20", "v21", "v22", "v23", "v24", "v25", "v26", "v27", "v28", "v29", "v30", "v31", "v32", "v33", "v34", "v35", "v36", "v37", "v38", "v39", "s24", "s25", "s26", "s27", "s28", "s29", "s30", "s31", "s32", "s33", "s34", "s36", "s37", "s38", "s39", "s40", "s41", "s42", "s43", "s44", "s45", "s46", "s47", "s48", "s49", "s50", "s51", "s52", "s53", "m0", "vcc", "scc", "memory");
#define WAIT_GO                                                                                    \
        {                                                                                          \
            unsigned go_;                                                                          \
            asm volatile(".Lgo%=:\n\tds_read_b32 %0, %1\n\ts_waitcnt lgkmcnt(0)\n\tv_cmp_ne_u32 vcc, -1, %0\n\ts_nop 4\n\ts_cbranch_vccnz .Lgo%=" \
                         : "=&v"(go_) : "v"(ldsb + GO_OFF) : "vcc", "memory");                     \
        }
#define LANE_SETUP                                                                                 \
    int lane;                                                                                      \
    asm volatile("v_mbcnt_lo_u32_b32 %0, -1, 0\n\tv_mbcnt_hi_u32_b32 %0, -1, %0" : "=v"(lane));    \
    __builtin_assume(lane >= 0 && lane < 64);                                                      \
    const int g = lane >> 4, l15 = lane & 15, sw = l15 >> 1;                                       \
    int fE = X_OFF + l15 * 384 + ((g ^ sw) << 4);                                                  \
    OPQ(fE);
#define XPAT(r) (((r) < 48 ? (r) : 48) * 384 + (((2 * w) ^ (((r) >> 1) & 7)) << 4))
#define SPAT(r) ((w >> 1) * 1024 + (w & 1) * 8 + (r) * 16)
#define CONV_CONSTS(var) \
            const unsigned long long rlo = g == 0 ? ROWMAP[0] : (g == 1 ? ROWMAP[2] : (g == 2 ? ROWMAP[4] : ROWMAP[6])); \
            const unsigned long long rhi = g == 0 ? ROWMAP[1] : (g == 1 ? ROWMAP[3] : (g == 2 ? ROWMAP[5] : ROWMAP[7])); \
            int xrow = byte_of(rlo, rhi, l15); \
            if (lane == (int)((CLANE >> (8 * (var))) & 0xFF)) xrow = 48; \
            if (lane == 63) xrow = (CROW >> (8 * (var))) & 0xFF; \
            const unsigned pXw_ = XPAT(xrow); \
            const unsigned pSTw_ = (w >> 1) * 1024 + (w & 1) * 8 + xrow * 16; \
            const int oy = (0x5320 >> (4 * (l15 >> 2))) & 15, ox = (0x5320 >> (4 * (l15 & 3))) & 15; \
            const unsigned aB_ = ldsb + w * 8192 + (oy + (g >> 1)) * 872 + (ox + 2 * (g & 1)) * 96; \
            const int dyi = g - (l15 >> 3); \
            const unsigned aA_ = ldsb + AT_OFF + (l15 & 3) * 16 + \
                                 ((dyi < 0 || dyi > 2) ? 18816 : (dyi == 0 ? 0 : (dyi == 1 ? 6208 : 12544)) + ((l15 >> 2) & 1) * 3072 + w * 768); \
            const unsigned idx_ = (l15 & 3) * 0x5555u; \
            const unsigned se = (0 || l15 == 4 * g) ? 0x3F803F80u : 0u; \
            const u32x4 sel4 = {se, se, se, se};

    if (wave < 4) {
        ROLE_SETUP
        LANE_SETUP
        asm volatile("s_waitcnt vmcnt(0)" ::: "memory");
        if (lane < 48) {
            unsigned tp[9];
#pragma unroll
            for (int k = 0; k < 9; ++k) tp[k] = *(const unsigned short*)(smem + TP_OFF + w * 1024 + k * 96 + lane * 2);
            tp[4] |= 0x3F800000u;
#pragma unroll
            for (int dy = 0; dy < 3; ++dy) {
                char* p = smem + AT_OFF + (dy == 0 ? 0 : (dy == 1 ? 6208 : 12544)) + w * 768 + lane * 16;
                *(u32x4*)p = (u32x4){tp[3 * dy], tp[3 * dy + 1], tp[3 * dy + 2], 0u};
                *(u32x4*)(p + 3072) = (u32x4){0u, tp[3 * dy], tp[3 * dy + 1], tp[3 * dy + 2]};
            }
        }
        BAR();
        {
            const int dW = X_OFF + l15 * 384 + (((2 * w + (g >> 1)) ^ sw) << 4) + 8 * (g & 1);
            const unsigned rsb = ldsb + RS_BASE;
            const unsigned pE_ = fE - X_OFF, aO_ = ldsb + dW + (O_OFF - X_OFF), aZ_ = ldsb + AT_OFF + 18816, aVB_ = ldsb + V_OFF + w * 4608 + 1024 + lane * 8;
            unsigned aRSw_ = rsb + lane * 4;
            const unsigned pST_ = lane * 16, aPLw_ = rsb + lane * 2, aRSr_ = rsb + l15 * 4;
            const unsigned g16_ = g * 16, aPLr_ = (l15 & 3) ? aZ_ : rsb + g * 8;
            const unsigned loVL_ = ((2 * w + (l15 >> 3)) << 4) + (l15 & 7) * 2, loOL_ = ((2 * w + (g >> 1)) << 4) + 8 * (g & 1);
            const unsigned on = (l15 & 3) ? 0u : 0x3F803F80u;
            const u32x4 ones4 = {on, on, on, on};
            const unsigned snk = ldsb + O_OFF + 15360 + w * 192, kr = ldsb + KQL_OFF + l15 * 800;
            const bool ok1 = 16 + l15 < cnt, dg = g == (l15 >> 2);
            u32x4 pl = {kr + w * 192 + g * 16, (ok1 ? kr + 12800 + w * 192 : snk) + g * 16, dg ? kr + 768 + w * 4 : snk, dg && ok1 ? kr + 12800 + 768 + w * 4 : snk};
            unsigned pcl, pch, len;
            asm volatile("s_getpc_b64 s[36:37]\n.Lwk_h2%=:\n\ts_sub_u32 s36, s36, .Lwk_h2%=-wk_code_begin\n\ts_subb_u32 s37, s37, 0\n\t"
                         "s_mov_b32 %2, wk_code_end-wk_code_begin\n\ts_max_u32 %2, %2, wk_code_end1-wk_code_begin" : "={s36}"(pcl), "={s37}"(pch), "=s"(len) :: "scc");
            pcl = __builtin_amdgcn_readfirstlane(pcl); pch = __builtin_amdgcn_readfirstlane(pch); len = __builtin_amdgcn_readfirstlane(len);
            const unsigned long long tb = w == 0 ? ((unsigned long long)pch << 32) | pcl : (unsigned long long)wf;
            const u32x4 tr = {(unsigned)tb, (unsigned)(tb >> 32) & 0xffffu, w == 0 ? len : (w == 1 ? (unsigned)WF_BYTES : SH_OFF + bx * 12288 + 12288), 0x00020000u};
            const unsigned to = w == 0 ? ((bx >> 3) % 6) * 8192 : (w == 1 ? 6 * 36864 + ((bx >> 3) % 9) * 8192 : SH_OFF + bx * 12288 + (w - 2) * 8192);
            asm volatile("" :: "{v244}"(pE_), "{v245}"(aO_), "{v246}"(aZ_), "{v247}"(aVB_), "{v248}"(pST_), "{v249}"(aRSw_), "{v250}"(aPLw_), "{v251}"(aRSr_), "{v252}"(g16_), "{v253}"(aPLr_), "{v254}"(loVL_), "{v255}"(loOL_), "{v[240:243]}"(ones4), "{v[232:235]}"(pl), "{s[36:39]}"(tr), "{s41}"(to));
            if (w == 0) {
                gu64* fp = (gu64*)((char*)wf + (lane < 36 ? FL_OFF + lane * 8 : EP_OFF));
#pragma unroll 1
                for (int it = 0; it < SPIN_MAX; ++it) {
                    const unsigned long long f = __hip_atomic_load(fp, RLX_AGENT);
                    const unsigned ep = __builtin_amdgcn_readlane((unsigned)f, 63);
                    if ((__builtin_amdgcn_ballot_w64(f != TAG_OF(ep)) & 0xfffffffffull) == 0ull) break;
                    __builtin_amdgcn_s_sleep(1);
                }
                __builtin_amdgcn_fence(__ATOMIC_ACQUIRE, "wavefront");
                asm volatile("ds_write_b32 %0, %1" :: "v"(ldsb + GO_OFF), "v"(~0u) : "memory");
            } else WAIT_GO
            __builtin_amdgcn_s_setprio(2);
            unsigned s44 = cnt, s48 = ldsb + X_OFF, s70 = 0;
            unsigned s72 = ldsb + KQL_OFF + w * 192, s73 = ldsb + OL_OFF, s77 = ldsb + OL_OFF, s76 = ldsb + KQL_OFF + 768 + w * 4;
            const unsigned long long wb = (unsigned long long)wf;
            const u32x4 wr = {(unsigned)wb, (unsigned)(wb >> 32) & 0xffffu, (unsigned)WS_BYTES, 0x00020000u};
            asm volatile(
            "s_setprio 2\n\t"
            "s_add_u32 s49, s33, 0\n\t"
            "buffer_load_dwordx4 v[0:3], v248, s[28:31], s49 offen sc1\n\t"
            "s_add_u32 s49, s33, 1024\n\t"
            "buffer_load_dwordx4 v[4:7], v248, s[28:31], s49 offen sc1\n\t"
            "s_add_u32 s49, s33, 2048\n\t"
            "buffer_load_dwordx4 v[8:11], v248, s[28:31], s49 offen sc1\n\t"
            "s_add_u32 s49, s33, 3072\n\t"
            "buffer_load_dwordx4 v[12:15], v248, s[28:31], s49 offen sc1\n\t"
            "s_add_u32 s49, s33, 4096\n\t"
            "buffer_load_dwordx4 v[16:19], v248, s[28:31], s49 offen sc1\n\t"
            "s_add_u32 s49, s33, 5120\n\t"
            "buffer_load_dwordx4 v[20:23], v248, s[28:31], s49 offen sc1\n\t"
            "s_add_u32 s49, s33, 6144\n\t"
            "buffer_load_dwordx4 v[24:27], v248, s[28:31], s49 offen sc1\n\t"
            "s_add_u32 s49, s33, 7168\n\t"
            "buffer_load_dwordx4 v[28:31], v248, s[28:31], s49 offen sc1\n\t"
            "s_add_u32 s49, s33, 8192\n\t"
            "buffer_load_dwordx4 v[32:35], v248, s[28:31], s49 offen sc1\n\t"
            "s_add_u32 s49, s33, 9216\n\t"
            "buffer_load_dwordx4 v[36:39], v248, s[28:31], s49 offen sc1\n\t"
            "s_add_u32 s49, s33, 10240\n\t"
            "buffer_load_dwordx4 v[40:43], v248, s[28:31], s49 offen sc1\n\t"
            "s_add_u32 s49, s33, 11264\n\t"
            "buffer_load_dwordx4 v[44:47], v248, s[28:31], s49 offen sc1\n\t"
            "s_add_u32 s49, s33, 12288\n\t"
            "buffer_load_dwordx4 v[48:51], v248, s[28:31], s49 offen sc1\n\t"
            "s_add_u32 s49, s33, 13312\n\t"
            "buffer_load_dwordx4 v[52:55], v248, s[28:31], s49 offen sc1\n\t"
            "s_add_u32 s49, s33, 14336\n\t"
            "buffer_load_dwordx4 v[56:59], v248, s[28:31], s49 offen sc1\n\t"
            "s_add_u32 s49, s33, 15360\n\t"
            "buffer_load_dwordx4 v[60:63], v248, s[28:31], s49 offen sc1\n\t"
            "v_mov_b32 v224, s46\n\t"
            ".Ls1%=:\n\t"
            "ds_read_b128 v[220:223], v224\n\t"
            "s_waitcnt lgkmcnt(0)\n\t"
            "v_and_b32 v220, v220, v221\n\t"
            "v_and_b32 v222, v222, v223\n\t"
            "v_and_b32 v220, v220, v222\n\t"
            "s_nop 0\n\t"
            "v_readfirstlane_b32 s49, v220\n\t"
            "s_nop 3\n\t"
            "s_bitcmp1_b32 s49, 27\n\t"
            "s_cbranch_scc0 .Ls1%=\n\t"
            "s_nop 7\n\t"
            "v_add_u32 v237, s35, v248\n\t"
            "v_add_u32 v238, s56, v244\n\t"
            "v_xor_b32 v239, 64, v238\n\t"
            "v_add_u32 v236, s56, v248\n\t"
            "ds_read_b128 v[200:203], v237\n\t"
            "ds_read_b128 v[204:207], v237 offset:1024\n\t"
            "ds_read_b128 v[144:147], v238\n\t"
            "ds_read_b128 v[148:151], v239\n\t"
            "ds_read_b128 v[152:155], v238 offset:128\n\t"
            "ds_read_b128 v[156:159], v239 offset:128\n\t"
            "ds_read_b128 v[160:163], v238 offset:256\n\t"
            "ds_read_b128 v[164:167], v239 offset:256\n\t"
            "ds_read_b128 v[168:171], v238 offset:6144\n\t"
            "ds_read_b128 v[172:175], v239 offset:6144\n\t"
            "ds_read_b128 v[176:179], v238 offset:6272\n\t"
            "ds_read_b128 v[180:183], v239 offset:6272\n\t"
            "ds_read_b128 v[184:187], v238 offset:6400\n\t"
            "ds_read_b128 v[188:191], v239 offset:6400\n\t"
            "ds_read_b128 v[208:211], v237 offset:2048\n\t"
            "s_waitcnt lgkmcnt(14)\n\t"
            "ds_read_b128 v[224:227], v236 offset:12288\n\t"
            "s_waitcnt lgkmcnt(14)\n\t"
            "ds_read_b128 v[228:231], v236 offset:13312\n\t"
            "s_add_u32 s49, s33, 16384\n\t"
            "buffer_load_dwordx4 v[64:67], v248, s[28:31], s49 offen sc1\n\t"
            "s_add_u32 s49, s33, 17408\n\t"
            "buffer_load_dwordx4 v[68:71], v248, s[28:31], s49 offen sc1\n\t"
            "s_waitcnt vmcnt(12)\n\t"
            "s_waitcnt lgkmcnt(14)\n\t"
            "v_mfma_f32_16x16x32_bf16 v[192:195], v[0:3], v[144:147], 0\n\t"
            "buffer_store_dwordx4 v[200:203], v248, s[28:31], s34 offen\n\t"
            "s_waitcnt lgkmcnt(8)\n\t"
            "v_mfma_f32_16x16x32_bf16 v[212:215], v[0:3], v[168:171], 0\n\t"
            "buffer_store_dwordx4 v[204:207], v248, s[28:31], s34 offen offset:1024\n\t"
            "v_mfma_f32_16x16x32_bf16 v[192:195], v[4:7], v[148:151], v[192:195]\n\t"
            "s_waitcnt lgkmcnt(2)\n\t"
            "buffer_store_dwordx4 v[208:211], v248, s[28:31], s34 offen offset:2048\n\t"
            "v_mfma_f32_16x16x32_bf16 v[212:215], v[4:7], v[172:175], v[212:215]\n\t"
            "s_add_u32 s49, s33, 18432\n\t"
            "buffer_load_dwordx4 v[72:75], v248, s[28:31], s49 offen sc1\n\t"
            "v_mfma_f32_16x16x32_bf16 v[192:195], v[8:11], v[152:155], v[192:195]\n\t"
            "s_add_u32 s49, s33, 19456\n\t"
            "buffer_load_dwordx4 v[76:79], v248, s[28:31], s49 offen sc1\n\t"
            "v_mfma_f32_16x16x32_bf16 v[212:215], v[8:11], v[176:179], v[212:215]\n\t"
            "s_add_u32 s49, s33, 20480\n\t"
            "buffer_load_dwordx4 v[80:83], v248, s[28:31], s49 offen sc1\n\t"
            "v_mfma_f32_16x16x32_bf16 v[192:195], v[12:15], v[156:159], v[192:195]\n\t"
            "s_add_u32 s49, s33, 21504\n\t"
            "buffer_load_dwordx4 v[84:87], v248, s[28:31], s49 offen sc1\n\t"
            "v_mfma_f32_16x16x32_bf16 v[212:215], v[12:15], v[180:183], v[212:215]\n\t"
            "s_add_u32 s49, s33, 22528\n\t"
            "buffer_load_dwordx4 v[88:91], v248, s[28:31], s49 offen sc1\n\t"
            "v_mfma_f32_16x16x32_bf16 v[192:195], v[16:19], v[160:163], v[192:195]\n\t"
            "s_add_u32 s49, s33, 23552\n\t"
            "buffer_load_dwordx4 v[92:95], v248, s[28:31], s49 offen sc1\n\t"
            "v_mfma_f32_16x16x32_bf16 v[212:215], v[16:19], v[184:187], v[212:215]\n\t"
            "s_waitcnt lgkmcnt(1)\n\t"
            "v_add_f32 v224, v224, v226\n\t"
            "s_waitcnt lgkmcnt(0)\n\t"
            "v_add_f32 v228, v228, v230\n\t"
            "v_mfma_f32_16x16x32_bf16 v[192:195], v[20:23], v[164:167], v[192:195]\n\t"
            "v_add_f32 v224, v224, v228\n\t"
            "v_add_f32 v225, v225, v227\n\t"
            "v_mfma_f32_16x16x32_bf16 v[212:215], v[20:23], v[188:191], v[212:215]\n\t"
            "s_waitcnt vmcnt(15)\n\t"
            "s_add_u32 s49, s33, 24576\n\t"
            "buffer_load_dwordx4 v[96:99], v248, s[28:31], s49 offen sc1\n\t"
            "v_mfma_f32_16x16x32_bf16 v[196:199], v[24:27], v[144:147], 0\n\t"
            "s_add_u32 s49, s33, 25600\n\t"
            "buffer_load_dwordx4 v[100:103], v248, s[28:31], s49 offen sc1\n\t"
            "v_mfma_f32_16x16x32_bf16 v[216:219], v[24:27], v[168:171], 0\n\t"
            "s_add_u32 s49, s33, 26624\n\t"
            "buffer_load_dwordx4 v[104:107], v248, s[28:31], s49 offen sc1\n\t"
            "v_mfma_f32_16x16x32_bf16 v[196:199], v[28:31], v[148:151], v[196:199]\n\t"
            "s_add_u32 s49, s33, 27648\n\t"
            "buffer_load_dwordx4 v[108:111], v248, s[28:31], s49 offen sc1\n\t"
            "v_mfma_f32_16x16x32_bf16 v[216:219], v[28:31], v[172:175], v[216:219]\n\t"
            "s_add_u32 s49, s33, 28672\n\t"
            "buffer_load_dwordx4 v[112:115], v248, s[28:31], s49 offen sc1\n\t"
            "v_mfma_f32_16x16x32_bf16 v[196:199], v[32:35], v[152:155], v[196:199]\n\t"
            "s_add_u32 s49, s33, 29696\n\t"
            "buffer_load_dwordx4 v[116:119], v248, s[28:31], s49 offen sc1\n\t"
            "v_mfma_f32_16x16x32_bf16 v[216:219], v[32:35], v[176:179], v[216:219]\n\t"
            "v_add_f32 v229, v229, v231\n\t"
            "v_mul_f32 v224, 0x3baaaaab, v224\n\t"
            "v_mfma_f32_16x16x32_bf16 v[196:199], v[36:39], v[156:159], v[196:199]\n\t"
            "v_add_f32 v225, v225, v229\n\t"
            "v_mul_f32_e64 v224, v224, -v224\n\t"
            "v_mfma_f32_16x16x32_bf16 v[216:219], v[36:39], v[180:183], v[216:219]\n\t"
            "v_fmac_f32 v224, 0x3baaaaab, v225\n\t"
            "v_max_f32 v224, 0, v224\n\t"
            "v_mfma_f32_16x16x32_bf16 v[196:199], v[40:43], v[160:163], v[196:199]\n\t"
            "v_add_f32 v224, 0x3727c5ac, v224\n\t"
            "v_rsq_f32 v224, v224\n\t"
            "v_mfma_f32_16x16x32_bf16 v[216:219], v[40:43], v[184:187], v[216:219]\n\t"
            "ds_write_b32 v249, v224\n\t"
            "v_mfma_f32_16x16x32_bf16 v[196:199], v[44:47], v[164:167], v[196:199]\n\t"
            "ds_read_b32 v236, v251\n\t"
            "v_mfma_f32_16x16x32_bf16 v[216:219], v[44:47], v[188:191], v[216:219]\n\t"
            "ds_read_b32 v237, v251 offset:64\n\t"
            "s_waitcnt vmcnt(15)\n\t"
            "v_mfma_f32_16x16x32_bf16 v[200:203], v[48:51], v[144:147], 0\n\t"
            "s_add_u32 s49, s33, 30720\n\t"
            "buffer_load_dwordx4 v[120:123], v248, s[28:31], s49 offen sc1\n\t"
            "v_mfma_f32_16x16x32_bf16 v[220:223], v[48:51], v[168:171], 0\n\t"
            "s_add_u32 s49, s33, 31744\n\t"
            "buffer_load_dwordx4 v[124:127], v248, s[28:31], s49 offen sc1\n\t"
            "v_mfma_f32_16x16x32_bf16 v[200:203], v[52:55], v[148:151], v[200:203]\n\t"
            "s_add_u32 s49, s33, 32768\n\t"
            "buffer_load_dwordx4 v[128:131], v248, s[28:31], s49 offen sc1\n\t"
            "v_mfma_f32_16x16x32_bf16 v[220:223], v[52:55], v[172:175], v[220:223]\n\t"
            "s_add_u32 s49, s33, 33792\n\t"
            "buffer_load_dwordx4 v[132:135], v248, s[28:31], s49 offen sc1\n\t"
            "v_mfma_f32_16x16x32_bf16 v[200:203], v[56:59], v[152:155], v[200:203]\n\t"
            "s_add_u32 s49, s33, 34816\n\t"
            "buffer_load_dwordx4 v[136:139], v248, s[28:31], s49 offen sc1\n\t"
            "v_mfma_f32_16x16x32_bf16 v[220:223], v[56:59], v[176:179], v[220:223]\n\t"
            "s_add_u32 s49, s33, 35840\n\t"
            "buffer_load_dwordx4 v[140:143], v248, s[28:31], s49 offen sc1\n\t"
            "s_setprio 0\n\t"
            "v_mfma_f32_16x16x32_bf16 v[200:203], v[60:63], v[156:159], v[200:203]\n\t"
            "s_waitcnt lgkmcnt(1)\n\t"
            "v_mul_f32 v192, v236, v192\n\t"
            "v_mul_f32 v193, v236, v193\n\t"
            "v_mfma_f32_16x16x32_bf16 v[220:223], v[60:63], v[180:183], v[220:223]\n\t"
            "s_waitcnt lgkmcnt(0)\n\t"
            "v_mul_f32 v212, v237, v212\n\t"
            "v_mul_f32 v213, v237, v213\n\t"
            "v_mfma_f32_16x16x32_bf16 v[200:203], v[64:67], v[160:163], v[200:203]\n\t"
            "v_mul_f32 v194, v236, v194\n\t"
            "v_mul_f32 v195, v236, v195\n\t"
            "v_mfma_f32_16x16x32_bf16 v[220:223], v[64:67], v[184:187], v[220:223]\n\t"
            "v_cvt_pk_bf16_f32 v192, v192, v193\n\t"
            "v_mul_f32 v214, v237, v214\n\t"
            "v_mfma_f32_16x16x32_bf16 v[200:203], v[68:71], v[164:167], v[200:203]\n\t"
            "v_mul_f32 v215, v237, v215\n\t"
            "v_cvt_pk_bf16_f32 v212, v212, v213\n\t"
            "v_mfma_f32_16x16x32_bf16 v[220:223], v[68:71], v[188:191], v[220:223]\n\t"
            "s_waitcnt vmcnt(12)\n\t"
            "v_cvt_pk_bf16_f32 v193, v194, v195\n\t"
            "v_mfma_f32_16x16x32_bf16 v[204:207], v[72:75], v[144:147], 0\n\t"
            "v_cvt_pk_bf16_f32 v213, v214, v215\n\t"
            "v_mul_f32 v196, v236, v196\n\t"
            "v_mfma_f32_16x16x32_bf16 v[224:227], v[72:75], v[168:171], 0\n\t"
            "v_mul_f32 v197, v236, v197\n\t"
            "v_mul_f32 v198, v236, v198\n\t"
            "v_mfma_f32_16x16x32_bf16 v[204:207], v[76:79], v[148:151], v[204:207]\n\t"
            "v_mul_f32 v199, v236, v199\n\t"
            "v_mul_f32 v216, v237, v216\n\t"
            "v_mfma_f32_16x16x32_bf16 v[224:227], v[76:79], v[172:175], v[224:227]\n\t"
            "v_mul_f32 v217, v237, v217\n\t"
            "v_mul_f32 v218, v237, v218\n\t"
            "v_mfma_f32_16x16x32_bf16 v[204:207], v[80:83], v[152:155], v[204:207]\n\t"
            "v_mul_f32 v219, v237, v219\n\t"
            "v_cvt_pk_bf16_f32 v194, v196, v197\n\t"
            "v_mfma_f32_16x16x32_bf16 v[224:227], v[80:83], v[176:179], v[224:227]\n\t"
            "v_cvt_pk_bf16_f32 v195, v198, v199\n\t"
            "v_cvt_pk_bf16_f32 v214, v216, v217\n\t"
            "v_mfma_f32_16x16x32_bf16 v[204:207], v[84:87], v[156:159], v[204:207]\n\t"
            "v_cvt_pk_bf16_f32 v215, v218, v219\n\t"
            "v_mul_f32 v200, v236, v200\n\t"
            "v_mfma_f32_16x16x32_bf16 v[224:227], v[84:87], v[180:183], v[224:227]\n\t"
            "v_mul_f32 v201, v236, v201\n\t"
            "v_mul_f32 v220, v237, v220\n\t"
            "v_mfma_f32_16x16x32_bf16 v[204:207], v[88:91], v[160:163], v[204:207]\n\t"
            "v_mul_f32 v221, v237, v221\n\t"
            "v_mul_f32 v202, v236, v202\n\t"
            "v_mfma_f32_16x16x32_bf16 v[224:227], v[88:91], v[184:187], v[224:227]\n\t"
            "v_mul_f32 v203, v236, v203\n\t"
            "v_cvt_pk_bf16_f32 v200, v200, v201\n\t"
            "v_mfma_f32_16x16x32_bf16 v[204:207], v[92:95], v[164:167], v[204:207]\n\t"
            "v_mul_f32 v222, v237, v222\n\t"
            "v_mul_f32 v223, v237, v223\n\t"
            "v_mfma_f32_16x16x32_bf16 v[224:227], v[92:95], v[188:191], v[224:227]\n\t"
            "s_waitcnt vmcnt(6)\n\t"
            "v_cvt_pk_bf16_f32 v220, v220, v221\n\t"
            "v_mfma_f32_16x16x32_bf16 v[196:199], v[96:99], v[144:147], 0\n\t"
            "v_cvt_pk_bf16_f32 v201, v202, v203\n\t"
            "v_cvt_pk_bf16_f32 v221, v222, v223\n\t"
            "v_mfma_f32_16x16x32_bf16 v[216:219], v[96:99], v[168:171], 0\n\t"
            "v_mul_f32 v204, v236, v204\n\t"
            "v_mul_f32 v205, v236, v205\n\t"
            "v_mfma_f32_16x16x32_bf16 v[196:199], v[100:103], v[148:151], v[196:199]\n\t"
            "v_mul_f32 v224, v237, v224\n\t"
            "v_mul_f32 v225, v237, v225\n\t"
            "v_mfma_f32_16x16x32_bf16 v[216:219], v[100:103], v[172:175], v[216:219]\n\t"
            "v_mul_f32 v206, v236, v206\n\t"
            "v_mul_f32 v207, v236, v207\n\t"
            "v_mfma_f32_16x16x32_bf16 v[196:199], v[104:107], v[152:155], v[196:199]\n\t"
            "v_cvt_pk_bf16_f32 v204, v204, v205\n\t"
            "v_mul_f32 v226, v237, v226\n\t"
            "v_mfma_f32_16x16x32_bf16 v[216:219], v[104:107], v[176:179], v[216:219]\n\t"
            "v_mul_f32 v227, v237, v227\n\t"
            "v_cvt_pk_bf16_f32 v224, v224, v225\n\t"
            "v_mfma_f32_16x16x32_bf16 v[196:199], v[108:111], v[156:159], v[196:199]\n\t"
            "v_cvt_pk_bf16_f32 v205, v206, v207\n\t"
            "v_cvt_pk_bf16_f32 v225, v226, v227\n\t"
            "v_mfma_f32_16x16x32_bf16 v[216:219], v[108:111], v[180:183], v[216:219]\n\t"
            "ds_write_b128 v232, v[192:195]\n\t"
            "v_mfma_f32_16x16x32_bf16 v[196:199], v[112:115], v[160:163], v[196:199]\n\t"
            "ds_write_b128 v233, v[212:215]\n\t"
            "v_mfma_f32_16x16x32_bf16 v[216:219], v[112:115], v[184:187], v[216:219]\n\t"
            "v_mfma_f32_16x16x32_bf16 v[196:199], v[116:119], v[164:167], v[196:199]\n\t"
            "v_mfma_f32_16x16x32_bf16 v[216:219], v[116:119], v[188:191], v[216:219]\n\t"
            "s_waitcnt vmcnt(0)\n\t"
            "v_mfma_f32_16x16x32_bf16 v[208:211], v[120:123], v[144:147], 0\n\t"
            "v_mfma_f32_16x16x32_bf16 v[228:231], v[120:123], v[168:171], 0\n\t"
            "v_mfma_f32_16x16x32_bf16 v[208:211], v[124:127], v[148:151], v[208:211]\n\t"
            "v_mfma_f32_16x16x32_bf16 v[228:231], v[124:127], v[172:175], v[228:231]\n\t"
            "s_nop 1\n\t"
            "v_mul_f32 v196, v236, v196\n\t"
            "v_mfma_f32_16x16x32_bf16 v[208:211], v[128:131], v[152:155], v[208:211]\n\t"
            "v_mul_f32 v197, v236, v197\n\t"
            "v_mul_f32 v198, v236, v198\n\t"
            "v_mfma_f32_16x16x32_bf16 v[228:231], v[128:131], v[176:179], v[228:231]\n\t"
            "v_mul_f32 v199, v236, v199\n\t"
            "v_mul_f32 v216, v237, v216\n\t"
            "v_mfma_f32_16x16x32_bf16 v[208:211], v[132:135], v[156:159], v[208:211]\n\t"
            "v_mul_f32 v217, v237, v217\n\t"
            "v_mul_f32 v218, v237, v218\n\t"
            "v_mfma_f32_16x16x32_bf16 v[228:231], v[132:135], v[180:183], v[228:231]\n\t"
            "v_mul_f32 v219, v237, v219\n\t"
            "v_cvt_pk_bf16_f32 v206, v196, v197\n\t"
            "v_mfma_f32_16x16x32_bf16 v[208:211], v[136:139], v[160:163], v[208:211]\n\t"
            "v_cvt_pk_bf16_f32 v207, v198, v199\n\t"
            "v_cvt_pk_bf16_f32 v226, v216, v217\n\t"
            "v_mfma_f32_16x16x32_bf16 v[228:231], v[136:139], v[184:187], v[228:231]\n\t"
            "v_cvt_pk_bf16_f32 v227, v218, v219\n\t"
            "v_mfma_f32_16x16x32_bf16 v[208:211], v[140:143], v[164:167], v[208:211]\n\t"
            "ds_write_b128 v232, v[204:207] offset:64\n\t"
            "v_mfma_f32_16x16x32_bf16 v[228:231], v[140:143], v[188:191], v[228:231]\n\t"
            "ds_write_b128 v233, v[224:227] offset:64\n\t"
            "s_nop 4\n\t"
            "v_mul_f32 v208, v236, v208\n\t"
            "v_mul_f32 v209, v236, v209\n\t"
            "v_mul_f32 v210, v236, v210\n\t"
            "v_mul_f32 v211, v236, v211\n\t"
            "v_cvt_pk_bf16_f32 v202, v208, v209\n\t"
            "v_cvt_pk_bf16_f32 v203, v210, v211\n\t"
            "v_mul_f32 v228, v237, v228\n\t"
            "v_mul_f32 v229, v237, v229\n\t"
            "v_mfma_f32_16x16x16_bf16 v[196:199], v[200:201], v[202:203], 0\n\t"
            "v_mul_f32 v230, v237, v230\n\t"
            "v_mul_f32 v231, v237, v231\n\t"
            "v_cvt_pk_bf16_f32 v222, v228, v229\n\t"
            "v_cvt_pk_bf16_f32 v223, v230, v231\n\t"
            "s_nop 1\n\t"
            "v_mfma_f32_16x16x16_bf16 v[216:219], v[220:221], v[222:223], 0\n\t"
            "ds_write_b128 v232, v[200:203] offset:128\n\t"
            "v_mfma_f32_16x16x32_bf16 v[196:199], v[192:195], v[204:207], v[196:199]\n\t"
            "ds_write_b128 v233, v[220:223] offset:128\n\t"
            "s_nop 2\n\t"
            "v_mfma_f32_16x16x32_bf16 v[216:219], v[212:215], v[224:227], v[216:219]\n\t"
            "s_nop 2\n\t"
            "v_cndmask_b32_e64 v196, v196, v197, s[50:51]\n\t"
            "v_cndmask_b32_e64 v196, v196, v198, s[52:53]\n\t"
            "v_cndmask_b32_e64 v196, v196, v199, s[54:55]\n\t"
            "ds_write_b32 v234, v196\n\t"
            "s_nop 0\n\t"
            "v_cndmask_b32_e64 v216, v216, v217, s[50:51]\n\t"
            "v_cndmask_b32_e64 v216, v216, v218, s[52:53]\n\t"
            "v_cndmask_b32_e64 v216, v216, v219, s[54:55]\n\t"
            "ds_write_b32 v235, v216\n\t"
            "s_waitcnt lgkmcnt(0)\n\t"
            "s_nop 7\n\t"
            "s_cmp_lg_u32 s33, 0\n\t"
            "s_cbranch_scc1 .Lar%=\n\t"
            "s_mov_b64 exec, 1\n\t"
            "v_mov_b32 v216, 1\n\t"
            "s_mov_b32 s49, 295168\n\t"
            "buffer_atomic_add v216, off, s[28:31], s49\n\t"
            "s_mov_b64 exec, -1\n\t"
            ".Lar%=:\n\t"
            "v_mov_b32 v224, s46\n\t"
            ".Ls2%=:\n\t"
            "ds_read_b128 v[220:223], v224\n\t"
            "s_waitcnt lgkmcnt(0)\n\t"
            "v_and_b32 v220, v220, v221\n\t"
            "v_and_b32 v222, v222, v223\n\t"
            "v_and_b32 v220, v220, v222\n\t"
            "s_nop 0\n\t"
            "v_readfirstlane_b32 s49, v220\n\t"
            "s_nop 3\n\t"
            "s_bitcmp1_b32 s49, 31\n\t"
            "s_cbranch_scc0 .Ls2%=\n\t"
            "s_waitcnt lgkmcnt(0)\n\t"
            "s_nop 7\n\t"
            "s_setprio 0\n\t"
            "v_add_u32 v232, s48, v244\n\t"
            "v_add_u32 v208, s48, v248\n\t"
            "v_xor_b32 v233, 64, v232\n\t"
            "v_add_u32 v234, s72, v252\n\t"
            "v_cndmask_b32_e64 v235, v246, v234, s[50:51]\n\t"
            "v_cndmask_b32_e64 v234, v246, v234, s[26:27]\n\t"
            "v_mov_b32 v239, s76\n\t"
            "ds_read_b128 v[216:219], v232\n\t"
            "ds_read_b128 v[204:207], v208 offset:18816\n\t"
            "ds_read_b128 v[208:211], v208 offset:19840\n\t"
            "ds_read_b128 v[220:223], v233\n\t"
            "ds_read_b128 v[224:227], v232 offset:128\n\t"
            "ds_read_b128 v[188:191], v233 offset:6272\n\t"
            "ds_read_b128 v[192:195], v232 offset:6400\n\t"
            "ds_read_b128 v[200:203], v233 offset:6400\n\t"
            "ds_read_b32 v239, v239\n\t"
            "s_waitcnt lgkmcnt(8)\n\t"
            "v_mfma_f32_16x16x32_bf16 v[148:151], v[72:75], v[216:219], 0\n\t"
            "s_waitcnt lgkmcnt(7)\n\t"
            "v_add_f32 v204, v204, v206\n\t"
            "v_add_f32 v205, v205, v207\n\t"
            "s_waitcnt lgkmcnt(6)\n\t"
            "v_add_f32 v208, v208, v210\n\t"
            "v_add_f32 v209, v209, v211\n\t"
            "v_add_f32 v204, v204, v208\n\t"
            "v_add_f32 v205, v205, v209\n\t"
            "v_mfma_f32_16x16x32_bf16 v[208:211], v[96:99], v[216:219], 0\n\t"
            "v_mul_f32 v204, 0x3baaaaab, v204\n\t"
            "v_mul_f32_e64 v204, v204, -v204\n\t"
            "v_mfma_f32_16x16x32_bf16 v[212:215], v[120:123], v[216:219], 0\n\t"
            "v_fmac_f32 v204, 0x3baaaaab, v205\n\t"
            "v_max_f32 v204, 0, v204\n\t"
            "v_mfma_f32_16x16x32_bf16 v[144:147], v[0:3], v[216:219], 0\n\t"
            "v_add_f32 v204, 0x3727c5ac, v204\n\t"
            "v_rsq_f32 v204, v204\n\t"
            "s_nop 0\n\t"
            "ds_write_b32 v249, v204\n\t"
            "v_mfma_f32_16x16x32_bf16 v[204:207], v[24:27], v[216:219], 0\n\t"
            "ds_read_b32 v236, v251\n\t"
            "v_mfma_f32_16x16x32_bf16 v[152:155], v[48:51], v[216:219], 0\n\t"
            "ds_read_b128 v[216:219], v233 offset:128\n\t"
            "s_waitcnt lgkmcnt(8)\n\t"
            "v_mfma_f32_16x16x32_bf16 v[148:151], v[76:79], v[220:223], v[148:151]\n\t"
            "ds_read_b32 v237, v251 offset:64\n\t"
            "v_mfma_f32_16x16x32_bf16 v[208:211], v[100:103], v[220:223], v[208:211]\n\t"
            "ds_read_b32 v238, v251 offset:128\n\t"
            "v_mfma_f32_16x16x32_bf16 v[212:215], v[124:127], v[220:223], v[212:215]\n\t"
            "v_mfma_f32_16x16x32_bf16 v[144:147], v[4:7], v[220:223], v[144:147]\n\t"
            "v_mfma_f32_16x16x32_bf16 v[204:207], v[28:31], v[220:223], v[204:207]\n\t"
            "v_mfma_f32_16x16x32_bf16 v[152:155], v[52:55], v[220:223], v[152:155]\n\t"
            "ds_read_b128 v[220:223], v232 offset:256\n\t"
            "s_waitcnt lgkmcnt(10)\n\t"
            "v_mfma_f32_16x16x32_bf16 v[144:147], v[8:11], v[224:227], v[144:147]\n\t"
            "v_mfma_f32_16x16x32_bf16 v[204:207], v[32:35], v[224:227], v[204:207]\n\t"
            "v_mfma_f32_16x16x32_bf16 v[152:155], v[56:59], v[224:227], v[152:155]\n\t"
            "v_mfma_f32_16x16x32_bf16 v[148:151], v[80:83], v[224:227], v[148:151]\n\t"
            "v_mfma_f32_16x16x32_bf16 v[208:211], v[104:107], v[224:227], v[208:211]\n\t"
            "v_mfma_f32_16x16x32_bf16 v[212:215], v[128:131], v[224:227], v[212:215]\n\t"
            "ds_read_b128 v[224:227], v233 offset:256\n\t"
            "s_waitcnt lgkmcnt(4)\n\t"
            "v_mfma_f32_16x16x32_bf16 v[148:151], v[84:87], v[216:219], v[148:151]\n\t"
            "v_mfma_f32_16x16x32_bf16 v[208:211], v[108:111], v[216:219], v[208:211]\n\t"
            "v_mfma_f32_16x16x32_bf16 v[212:215], v[132:135], v[216:219], v[212:215]\n\t"
            "v_mfma_f32_16x16x32_bf16 v[144:147], v[12:15], v[216:219], v[144:147]\n\t"
            "v_mfma_f32_16x16x32_bf16 v[204:207], v[36:39], v[216:219], v[204:207]\n\t"
            "v_mfma_f32_16x16x32_bf16 v[152:155], v[60:63], v[216:219], v[152:155]\n\t"
            "ds_read_b128 v[216:219], v232 offset:6144\n\t"
            "s_waitcnt lgkmcnt(2)\n\t"
            "v_mfma_f32_16x16x32_bf16 v[148:151], v[88:91], v[220:223], v[148:151]\n\t"
            "v_mfma_f32_16x16x32_bf16 v[208:211], v[112:115], v[220:223], v[208:211]\n\t"
            "v_mfma_f32_16x16x32_bf16 v[212:215], v[136:139], v[220:223], v[212:215]\n\t"
            "v_mfma_f32_16x16x32_bf16 v[144:147], v[16:19], v[220:223], v[144:147]\n\t"
            "v_mfma_f32_16x16x32_bf16 v[204:207], v[40:43], v[220:223], v[204:207]\n\t"
            "v_mfma_f32_16x16x32_bf16 v[152:155], v[64:67], v[220:223], v[152:155]\n\t"
            "ds_read_b128 v[220:223], v233 offset:6144\n\t"
            "s_waitcnt lgkmcnt(2)\n\t"
            "v_mfma_f32_16x16x32_bf16 v[144:147], v[20:23], v[224:227], v[144:147]\n\t"
            "v_mfma_f32_16x16x32_bf16 v[204:207], v[44:47], v[224:227], v[204:207]\n\t"
            "v_mfma_f32_16x16x32_bf16 v[152:155], v[68:71], v[224:227], v[152:155]\n\t"
            "v_mfma_f32_16x16x32_bf16 v[148:151], v[92:95], v[224:227], v[148:151]\n\t"
            "v_mfma_f32_16x16x32_bf16 v[208:211], v[116:119], v[224:227], v[208:211]\n\t"
            "v_mfma_f32_16x16x32_bf16 v[212:215], v[140:143], v[224:227], v[212:215]\n\t"
            "ds_read_b128 v[224:227], v232 offset:6272\n\t"
            "s_waitcnt lgkmcnt(2)\n\t"
            "v_mfma_f32_16x16x32_bf16 v[160:163], v[72:75], v[216:219], 0\n\t"
            "v_mul_f32 v144, v236, v144\n\t"
            "v_mul_f32 v145, v236, v145\n\t"
            "v_mul_f32 v146, v236, v146\n\t"
            "s_waitcnt lgkmcnt(1)\n\t"
            "v_mfma_f32_16x16x32_bf16 v[160:163], v[76:79], v[220:223], v[160:163]\n\t"
            "v_mul_f32 v152, v236, v152\n\t"
            "v_mul_f32 v153, v236, v153\n\t"
            "v_mul_f32 v154, v236, v154\n\t"
            "v_mul_f32 v155, v236, v155\n\t"
            "v_cvt_pk_bf16_f32 v152, v152, v153\n\t"
            "v_cvt_pk_bf16_f32 v153, v154, v155\n\t"
            "v_mul_f32 v212, v236, v212\n\t"
            "v_mul_f32 v213, v236, v213\n\t"
            "v_mul_f32 v214, v236, v214\n\t"
            "v_mul_f32 v215, v236, v215\n\t"
            "v_cvt_pk_bf16_f32 v154, v212, v213\n\t"
            "v_cvt_pk_bf16_f32 v155, v214, v215\n\t"
            "v_mul_f32 v147, v236, v147\n\t"
            "v_cvt_pk_bf16_f32 v144, v144, v145\n\t"
            "v_mul_f32 v148, v236, v148\n\t"
            "v_mul_f32 v149, v236, v149\n\t"
            "v_cvt_pk_bf16_f32 v145, v146, v147\n\t"
            "v_mul_f32 v204, v236, v204\n\t"
            "v_mul_f32 v205, v236, v205\n\t"
            "v_mul_f32 v206, v236, v206\n\t"
            "v_mul_f32 v207, v236, v207\n\t"
            "v_mul_f32 v150, v236, v150\n\t"
            "s_waitcnt lgkmcnt(0)\n\t"
            "v_mfma_f32_16x16x32_bf16 v[160:163], v[80:83], v[224:227], v[160:163]\n\t"
            "ds_read_b128 v[212:215], v232 offset:12416\n\t"
            "v_mul_f32 v151, v236, v151\n\t"
            "v_mfma_f32_16x16x32_bf16 v[160:163], v[84:87], v[188:191], v[160:163]\n\t"
            "v_cvt_pk_bf16_f32 v148, v148, v149\n\t"
            "v_cvt_pk_bf16_f32 v146, v204, v205\n\t"
            "v_mfma_f32_16x16x32_bf16 v[160:163], v[88:91], v[192:195], v[160:163]\n\t"
            "v_cvt_pk_bf16_f32 v147, v206, v207\n\t"
            "v_cvt_pk_bf16_f32 v149, v150, v151\n\t"
            "v_mfma_f32_16x16x32_bf16 v[160:163], v[92:95], v[200:203], v[160:163]\n\t"
            "ds_read_b128 v[204:207], v232 offset:12288\n\t"
            "v_mul_f32 v208, v236, v208\n\t"
            "v_mfma_f32_16x16x16_bf16 v[180:183], v[152:153], v[154:155], 0\n\t"
            "v_mul_f32 v209, v236, v209\n\t"
            "v_mul_f32 v210, v236, v210\n\t"
            "v_mul_f32 v211, v236, v211\n\t"
            "v_mfma_f32_16x16x32_bf16 v[196:199], v[24:27], v[216:219], 0\n\t"
            "v_cvt_pk_bf16_f32 v150, v208, v209\n\t"
            "v_cvt_pk_bf16_f32 v151, v210, v211\n\t"
            "v_mfma_f32_16x16x32_bf16 v[196:199], v[28:31], v[220:223], v[196:199]\n\t"
            "ds_read_b128 v[208:211], v233 offset:12288\n\t"
            "v_mul_f32 v160, v237, v160\n\t"
            "v_mfma_f32_16x16x32_bf16 v[196:199], v[32:35], v[224:227], v[196:199]\n\t"
            "v_mul_f32 v161, v237, v161\n\t"
            "v_mul_f32 v162, v237, v162\n\t"
            "v_mfma_f32_16x16x32_bf16 v[196:199], v[36:39], v[188:191], v[196:199]\n\t"
            "v_mul_f32 v163, v237, v163\n\t"
            "v_cvt_pk_bf16_f32 v160, v160, v161\n\t"
            "v_mfma_f32_16x16x32_bf16 v[196:199], v[40:43], v[192:195], v[196:199]\n\t"
            "v_cvt_pk_bf16_f32 v161, v162, v163\n\t"
            "v_mfma_f32_16x16x32_bf16 v[196:199], v[44:47], v[200:203], v[196:199]\n\t"
            "v_mfma_f32_16x16x32_bf16 v[156:159], v[0:3], v[216:219], 0\n\t"
            "v_mfma_f32_16x16x32_bf16 v[156:159], v[4:7], v[220:223], v[156:159]\n\t"
            "v_mfma_f32_16x16x32_bf16 v[156:159], v[8:11], v[224:227], v[156:159]\n\t"
            "v_mfma_f32_16x16x32_bf16 v[156:159], v[12:15], v[188:191], v[156:159]\n\t"
            "v_mfma_f32_16x16x32_bf16 v[156:159], v[16:19], v[192:195], v[156:159]\n\t"
            "v_mfma_f32_16x16x32_bf16 v[156:159], v[20:23], v[200:203], v[156:159]\n\t"
            "s_nop 1\n\t"
            "v_mul_f32 v196, v237, v196\n\t"
            "v_mfma_f32_16x16x32_bf16 v[164:167], v[48:51], v[216:219], 0\n\t"
            "v_mul_f32 v197, v237, v197\n\t"
            "v_mul_f32 v198, v237, v198\n\t"
            "v_mfma_f32_16x16x32_bf16 v[164:167], v[52:55], v[220:223], v[164:167]\n\t"
            "v_mul_f32 v199, v237, v199\n\t"
            "v_mfma_f32_16x16x32_bf16 v[164:167], v[56:59], v[224:227], v[164:167]\n\t"
            "v_mul_f32 v156, v237, v156\n\t"
            "v_mul_f32 v157, v237, v157\n\t"
            "v_mfma_f32_16x16x32_bf16 v[164:167], v[60:63], v[188:191], v[164:167]\n\t"
            "v_mul_f32 v158, v237, v158\n\t"
            "v_mul_f32 v159, v237, v159\n\t"
            "v_mfma_f32_16x16x32_bf16 v[164:167], v[64:67], v[192:195], v[164:167]\n\t"
            "v_cvt_pk_bf16_f32 v156, v156, v157\n\t"
            "v_cvt_pk_bf16_f32 v157, v158, v159\n\t"
            "v_mfma_f32_16x16x32_bf16 v[164:167], v[68:71], v[200:203], v[164:167]\n\t"
            "v_cvt_pk_bf16_f32 v158, v196, v197\n\t"
            "v_cvt_pk_bf16_f32 v159, v198, v199\n\t"
            "v_mfma_f32_16x16x32_bf16 v[228:231], v[96:99], v[216:219], 0\n\t"
            "v_mfma_f32_16x16x32_bf16 v[228:231], v[100:103], v[220:223], v[228:231]\n\t"
            "v_mfma_f32_16x16x32_bf16 v[228:231], v[104:107], v[224:227], v[228:231]\n\t"
            "v_mfma_f32_16x16x32_bf16 v[228:231], v[108:111], v[188:191], v[228:231]\n\t"
            "s_nop 1\n\t"
            "v_mul_f32 v164, v237, v164\n\t"
            "v_mfma_f32_16x16x32_bf16 v[228:231], v[112:115], v[192:195], v[228:231]\n\t"
            "v_mul_f32 v165, v237, v165\n\t"
            "v_mul_f32 v166, v237, v166\n\t"
            "v_mfma_f32_16x16x32_bf16 v[228:231], v[116:119], v[200:203], v[228:231]\n\t"
            "v_mul_f32 v167, v237, v167\n\t"
            "v_cvt_pk_bf16_f32 v164, v164, v165\n\t"
            "v_mfma_f32_16x16x32_bf16 v[180:183], v[144:147], v[148:151], v[180:183]\n\t"
            "v_cvt_pk_bf16_f32 v165, v166, v167\n\t"
            "v_mfma_f32_16x16x32_bf16 v[196:199], v[120:123], v[216:219], 0\n\t"
            "ds_read_b128 v[216:219], v233 offset:12416\n\t"
            "v_mfma_f32_16x16x32_bf16 v[196:199], v[124:127], v[220:223], v[196:199]\n\t"
            "ds_read_b128 v[220:223], v232 offset:12544\n\t"
            "v_mul_f32 v228, v237, v228\n\t"
            "v_mfma_f32_16x16x32_bf16 v[196:199], v[128:131], v[224:227], v[196:199]\n\t"
            "ds_read_b128 v[224:227], v233 offset:12544\n\t"
            "v_mul_f32 v229, v237, v229\n\t"
            "v_mfma_f32_16x16x32_bf16 v[196:199], v[132:135], v[188:191], v[196:199]\n\t"
            "v_mul_f32 v230, v237, v230\n\t"
            "v_mul_f32 v231, v237, v231\n\t"
            "v_mfma_f32_16x16x32_bf16 v[196:199], v[136:139], v[192:195], v[196:199]\n\t"
            "v_cvt_pk_bf16_f32 v162, v228, v229\n\t"
            "v_cvt_pk_bf16_f32 v163, v230, v231\n\t"
            "v_mfma_f32_16x16x32_bf16 v[196:199], v[140:143], v[200:203], v[196:199]\n\t"
            "v_exp_f32 v180, v180\n\t"
            "s_waitcnt lgkmcnt(4)\n\t"
            "v_mfma_f32_16x16x32_bf16 v[176:179], v[48:51], v[204:207], 0\n\t"
            "v_exp_f32 v181, v181\n\t"
            "s_waitcnt lgkmcnt(3)\n\t"
            "v_mfma_f32_16x16x32_bf16 v[176:179], v[52:55], v[208:211], v[176:179]\n\t"
            "v_exp_f32 v182, v182\n\t"
            "v_mfma_f32_16x16x32_bf16 v[176:179], v[56:59], v[212:215], v[176:179]\n\t"
            "v_exp_f32 v183, v183\n\t"
            "s_waitcnt lgkmcnt(2)\n\t"
            "v_mfma_f32_16x16x32_bf16 v[176:179], v[60:63], v[216:219], v[176:179]\n\t"
            "v_mul_f32 v196, v237, v196\n\t"
            "v_mul_f32 v197, v237, v197\n\t"
            "s_waitcnt lgkmcnt(1)\n\t"
            "v_mfma_f32_16x16x32_bf16 v[176:179], v[64:67], v[220:223], v[176:179]\n\t"
            "v_mul_f32 v198, v237, v198\n\t"
            "v_mul_f32 v199, v237, v199\n\t"
            "s_waitcnt lgkmcnt(0)\n\t"
            "v_mfma_f32_16x16x32_bf16 v[176:179], v[68:71], v[224:227], v[176:179]\n\t"
            "v_cvt_pk_bf16_f32 v166, v196, v197\n\t"
            "v_cvt_pk_bf16_f32 v167, v198, v199\n\t"
            "v_cvt_pk_bf16_f32 v180, v180, v181\n\t"
            "v_cvt_pk_bf16_f32 v181, v182, v183\n\t"
            "v_mfma_f32_16x16x16_bf16 v[188:191], v[152:153], v[166:167], 0\n\t"
            "v_mfma_f32_16x16x32_bf16 v[168:171], v[0:3], v[204:207], 0\n\t"
            "s_nop 1\n\t"
            "v_mul_f32 v176, v238, v176\n\t"
            "v_mfma_f32_16x16x32_bf16 v[168:171], v[4:7], v[208:211], v[168:171]\n\t"
            "v_mul_f32 v177, v238, v177\n\t"
            "v_mul_f32 v178, v238, v178\n\t"
            "v_mfma_f32_16x16x32_bf16 v[168:171], v[8:11], v[212:215], v[168:171]\n\t"
            "v_mul_f32 v179, v238, v179\n\t"
            "v_cvt_pk_bf16_f32 v176, v176, v177\n\t"
            "v_mfma_f32_16x16x32_bf16 v[168:171], v[12:15], v[216:219], v[168:171]\n\t"
            "v_cvt_pk_bf16_f32 v177, v178, v179\n\t"
            "v_mfma_f32_16x16x32_bf16 v[168:171], v[16:19], v[220:223], v[168:171]\n\t"
            "v_mfma_f32_16x16x32_bf16 v[168:171], v[20:23], v[224:227], v[168:171]\n\t"
            "v_mfma_f32_16x16x16_bf16 v[192:195], v[164:165], v[166:167], 0\n\t"
            "v_mfma_f32_16x16x32_bf16 v[188:191], v[144:147], v[160:163], v[188:191]\n\t"
            "v_mfma_f32_16x16x16_bf16 v[184:187], v[164:165], v[154:155], 0\n\t"
            "s_nop 3\n\t"
            "v_mfma_f32_16x16x32_bf16 v[192:195], v[156:159], v[160:163], v[192:195]\n\t"
            "v_mfma_f32_16x16x32_bf16 v[172:175], v[72:75], v[204:207], 0\n\t"
            "v_mfma_f32_16x16x32_bf16 v[172:175], v[76:79], v[208:211], v[172:175]\n\t"
            "v_mul_f32 v168, v238, v168\n\t"
            "v_mfma_f32_16x16x32_bf16 v[172:175], v[80:83], v[212:215], v[172:175]\n\t"
            "v_mul_f32 v169, v238, v169\n\t"
            "v_mul_f32 v170, v238, v170\n\t"
            "v_mfma_f32_16x16x32_bf16 v[172:175], v[84:87], v[216:219], v[172:175]\n\t"
            "v_exp_f32 v188, v188\n\t"
            "v_mfma_f32_16x16x32_bf16 v[172:175], v[88:91], v[220:223], v[172:175]\n\t"
            "v_exp_f32 v189, v189\n\t"
            "v_mfma_f32_16x16x32_bf16 v[172:175], v[92:95], v[224:227], v[172:175]\n\t"
            "v_exp_f32 v190, v190\n\t"
            "v_mfma_f32_16x16x32_bf16 v[184:187], v[156:159], v[148:151], v[184:187]\n\t"
            "v_exp_f32 v191, v191\n\t"
            "v_mfma_f32_16x16x32_bf16 v[200:203], v[120:123], v[204:207], 0\n\t"
            "v_exp_f32 v192, v192\n\t"
            "v_mfma_f32_16x16x32_bf16 v[200:203], v[124:127], v[208:211], v[200:203]\n\t"
            "v_exp_f32 v193, v193\n\t"
            "v_mfma_f32_16x16x32_bf16 v[200:203], v[128:131], v[212:215], v[200:203]\n\t"
            "v_exp_f32 v194, v194\n\t"
            "v_mfma_f32_16x16x32_bf16 v[200:203], v[132:135], v[216:219], v[200:203]\n\t"
            "v_exp_f32 v184, v184\n\t"
            "v_mfma_f32_16x16x32_bf16 v[200:203], v[136:139], v[220:223], v[200:203]\n\t"
            "v_exp_f32 v185, v185\n\t"
            "v_mfma_f32_16x16x32_bf16 v[200:203], v[140:143], v[224:227], v[200:203]\n\t"
            "v_exp_f32 v186, v186\n\t"
            "v_exp_f32 v187, v187\n\t"
            "v_cvt_pk_bf16_f32 v182, v184, v185\n\t"
            "v_cvt_pk_bf16_f32 v183, v186, v187\n\t"
            "v_mfma_f32_16x16x16_bf16 v[184:187], v[176:177], v[154:155], 0\n\t"
            "v_exp_f32 v195, v195\n\t"
            "v_cvt_pk_bf16_f32 v188, v188, v189\n\t"
            "v_mfma_f32_16x16x32_bf16 v[196:199], v[24:27], v[204:207], 0\n\t"
            "v_cvt_pk_bf16_f32 v189, v190, v191\n\t"
            "v_cvt_pk_bf16_f32 v190, v192, v193\n\t"
            "v_mfma_f32_16x16x32_bf16 v[196:199], v[28:31], v[208:211], v[196:199]\n\t"
            "v_cvt_pk_bf16_f32 v191, v194, v195\n\t"
            "v_mul_f32 v171, v238, v171\n\t"
            "v_mfma_f32_16x16x32_bf16 v[196:199], v[32:35], v[212:215], v[196:199]\n\t"
            "v_cvt_pk_bf16_f32 v168, v168, v169\n\t"
            "v_cvt_pk_bf16_f32 v169, v170, v171\n\t"
            "v_mfma_f32_16x16x32_bf16 v[196:199], v[36:39], v[216:219], v[196:199]\n\t"
            "v_mul_f32 v172, v238, v172\n\t"
            "v_mul_f32 v173, v238, v173\n\t"
            "v_mfma_f32_16x16x32_bf16 v[196:199], v[40:43], v[220:223], v[196:199]\n\t"
            "v_mul_f32 v200, v238, v200\n\t"
            "v_mul_f32 v201, v238, v201\n\t"
            "v_mfma_f32_16x16x32_bf16 v[196:199], v[44:47], v[224:227], v[196:199]\n\t"
            "v_mul_f32 v202, v238, v202\n\t"
            "v_mul_f32 v203, v238, v203\n\t"
            "v_mfma_f32_16x16x32_bf16 v[228:231], v[96:99], v[204:207], 0\n\t"
            "ds_read_b64 v[204:205], v235 offset:136\n\t"
            "v_mul_f32 v174, v238, v174\n\t"
            "v_mfma_f32_16x16x32_bf16 v[228:231], v[100:103], v[208:211], v[228:231]\n\t"
            "ds_read_b64 v[206:207], v234 offset:128\n\t"
            "v_mul_f32 v175, v238, v175\n\t"
            "v_mfma_f32_16x16x32_bf16 v[228:231], v[104:107], v[212:215], v[228:231]\n\t"
            "ds_read_b128 v[208:211], v234\n\t"
            "v_mul_f32 v196, v238, v196\n\t"
            "v_mfma_f32_16x16x32_bf16 v[228:231], v[108:111], v[216:219], v[228:231]\n\t"
            "ds_read_b128 v[212:215], v235 offset:64\n\t"
            "v_mul_f32 v197, v238, v197\n\t"
            "v_mfma_f32_16x16x32_bf16 v[228:231], v[112:115], v[220:223], v[228:231]\n\t"
            "v_mul_f32 v198, v238, v198\n\t"
            "v_mul_f32 v199, v238, v199\n\t"
            "v_mfma_f32_16x16x32_bf16 v[228:231], v[116:119], v[224:227], v[228:231]\n\t"
            "v_cvt_pk_bf16_f32 v170, v196, v197\n\t"
            "v_cvt_pk_bf16_f32 v171, v198, v199\n\t"
            "v_mfma_f32_16x16x16_bf16 v[192:195], v[176:177], v[166:167], 0\n\t"
            "v_cvt_pk_bf16_f32 v172, v172, v173\n\t"
            "v_cvt_pk_bf16_f32 v178, v200, v201\n\t"
            "v_cvt_pk_bf16_f32 v179, v202, v203\n\t"
            "v_mfma_f32_16x16x32_bf16 v[184:187], v[168:171], v[148:151], v[184:187]\n\t"
            "v_cvt_pk_bf16_f32 v173, v174, v175\n\t"
            "v_mul_f32 v228, v238, v228\n\t"
            "v_mfma_f32_16x16x32_bf16 v[192:195], v[168:171], v[160:163], v[192:195]\n\t"
            "v_mul_f32 v229, v238, v229\n\t"
            "v_mul_f32 v230, v238, v230\n\t"
            "v_mul_f32 v231, v238, v231\n\t"
            "s_waitcnt lgkmcnt(2)\n\t"
            "v_mfma_f32_16x16x32_bf16 v[216:219], v[204:207], v[152:155], 0\n\t"
            "v_cvt_pk_bf16_f32 v174, v228, v229\n\t"
            "v_cvt_pk_bf16_f32 v175, v230, v231\n\t"
            "s_waitcnt lgkmcnt(1)\n\t"
            "v_mfma_f32_16x16x32_bf16 v[216:219], v[208:211], v[148:151], v[216:219]\n\t"
            "v_exp_f32 v184, v184\n\t"
            "s_waitcnt lgkmcnt(0)\n\t"
            "v_mfma_f32_16x16x32_bf16 v[216:219], v[212:215], v[144:147], v[216:219]\n\t"
            "v_exp_f32 v185, v185\n\t"
            "v_mfma_f32_16x16x32_bf16 v[220:223], v[204:207], v[164:167], 0\n\t"
            "v_exp_f32 v186, v186\n\t"
            "v_mfma_f32_16x16x32_bf16 v[220:223], v[208:211], v[160:163], v[220:223]\n\t"
            "v_exp_f32 v187, v187\n\t"
            "v_mfma_f32_16x16x32_bf16 v[220:223], v[212:215], v[156:159], v[220:223]\n\t"
            "v_cvt_pk_bf16_f32 v184, v184, v185\n\t"
            "v_cvt_pk_bf16_f32 v185, v186, v187\n\t"
            "v_mfma_f32_16x16x16_bf16 v[228:231], v[164:165], v[178:179], 0\n\t"
            "v_exp_f32 v216, v216\n\t"
            "v_mov_b32 v187, 0\n\t"
            "v_mfma_f32_16x16x16_bf16 v[200:203], v[176:177], v[178:179], 0\n\t"
            "v_cvt_pk_bf16_f32 v216, v216, 0\n\t"
            "v_cndmask_b32_e64 v186, 0, v216, s[58:59]\n\t"
            "v_exp_f32 v192, v192\n\t"
            "v_mfma_f32_16x16x32_bf16 v[224:227], v[204:207], v[176:179], 0\n\t"
            "v_exp_f32 v193, v193\n\t"
            "v_mfma_f32_16x16x32_bf16 v[224:227], v[208:211], v[172:175], v[224:227]\n\t"
            "v_exp_f32 v194, v194\n\t"
            "v_mfma_f32_16x16x32_bf16 v[224:227], v[212:215], v[168:171], v[224:227]\n\t"
            "v_exp_f32 v195, v195\n\t"
            "v_mfma_f32_16x16x16_bf16 v[196:199], v[152:153], v[178:179], 0\n\t"
            "v_exp_f32 v220, v220\n\t"
            "v_cvt_pk_bf16_f32 v192, v192, v193\n\t"
            "v_mfma_f32_16x16x32_bf16 v[204:207], v[240:243], v[180:183], 0\n\t"
            "v_cvt_pk_bf16_f32 v193, v194, v195\n\t"
            "v_cvt_pk_bf16_f32 v220, v220, 0\n\t"
            "v_mfma_f32_16x16x32_bf16 v[204:207], v[240:243], v[184:187], v[204:207]\n\t"
            "v_cndmask_b32_e64 v194, 0, v220, s[58:59]\n\t"
            "v_mov_b32 v195, 0\n\t"
            "v_mfma_f32_16x16x32_bf16 v[200:203], v[168:171], v[172:175], v[200:203]\n\t"
            "v_cndmask_b32_e64 v235, v217, v221, s[60:61]\n\t"
            "v_cndmask_b32_e64 v235, v235, v225, s[62:63]\n\t"
            "v_mfma_f32_16x16x32_bf16 v[208:211], v[240:243], v[188:191], 0\n\t"
            "v_cndmask_b32_e64 v235, v235, v239, s[64:65]\n\t"
            "v_cmp_nlt_f32_e64 s[24:25], |v235|, s23\n\t"
            "v_mfma_f32_16x16x32_bf16 v[208:211], v[240:243], v[192:195], v[208:211]\n\t"
            "v_exp_f32 v235, v235\n\t"
            "v_mfma_f32_16x16x32_bf16 v[196:199], v[144:147], v[172:175], v[196:199]\n\t"
            "v_exp_f32 v200, v200\n\t"
            "v_mfma_f32_16x16x32_bf16 v[228:231], v[156:159], v[172:175], v[228:231]\n\t"
            "v_exp_f32 v201, v201\n\t"
            "v_exp_f32 v202, v202\n\t"
            "v_exp_f32 v203, v203\n\t"
            "v_exp_f32 v224, v224\n\t"
            "v_cvt_pk_bf16_f32 v235, v235, 0\n\t"
            "v_cvt_pk_bf16_f32 v200, v200, v201\n\t"
            "v_exp_f32 v196, v196\n\t"
            "v_exp_f32 v197, v197\n\t"
            "v_exp_f32 v198, v198\n\t"
            "v_exp_f32 v199, v199\n\t"
            "v_exp_f32 v228, v228\n\t"
            "v_exp_f32 v229, v229\n\t"
            "v_exp_f32 v230, v230\n\t"
            "v_exp_f32 v231, v231\n\t"
            "v_cvt_pk_bf16_f32 v196, v196, v197\n\t"
            "v_cvt_pk_bf16_f32 v197, v198, v199\n\t"
            "v_cvt_pk_bf16_f32 v198, v228, v229\n\t"
            "v_cvt_pk_bf16_f32 v199, v230, v231\n\t"
            "v_cndmask_b32_e64 v235, 0, v235, s[66:67]\n\t"
            "v_cvt_pk_bf16_f32 v201, v202, v203\n\t"
            "v_mfma_f32_16x16x32_bf16 v[212:215], v[240:243], v[196:199], 0\n\t"
            "v_cvt_pk_bf16_f32 v224, v224, 0\n\t"
            "v_cndmask_b32_e64 v202, 0, v224, s[58:59]\n\t"
            "v_mov_b32 v203, 0\n\t"
            "v_add_u32 v205, 0xe0800000, v204\n\t"
            "v_add_u32 v209, 0xe0800000, v208\n\t"
            "v_mfma_f32_16x16x32_bf16 v[212:215], v[240:243], v[200:203], v[212:215]\n\t"
            "ds_write_b16 v250, v235 offset:256\n\t"
            "v_rcp_f32 v236, v204\n\t"
            "v_rcp_f32 v237, v208\n\t"
            "s_nop 4\n\t"
            "v_add_u32 v213, 0xe0800000, v212\n\t"
            "v_rcp_f32 v238, v212\n\t"
            "v_or3_b32 v205, v205, v209, v213\n\t"
            "v_cmp_le_u32 vcc, 2.0, v205\n\t"
            "s_waitcnt lgkmcnt(0)\n\t"
            "s_nop 7\n\t"
            "s_branch .Lwa_in%=\n\t"
            ".p2align 6\n\t"
            ".Lwa_loop%=:\n\t"
            "ds_read_b64 v[156:157], v247\n\t"
            "v_add_u32 v232, s48, v244\n\t"
            "v_add_u32 v208, s48, v248\n\t"
            "v_add_u32 v235, s45, v248\n\t"
            "v_xor_b32 v234, s73, v254\n\t"
            "v_xor_b32 v233, 64, v232\n\t"
            "v_mov_b32 v159, 0\n\t"
            "v_mov_b32 v167, 0\n\t"
            "v_mov_b32 v163, 0\n\t"
            "v_mov_b32 v239, s76\n\t"
            "ds_read_b128 v[216:219], v232\n\t"
            "ds_read_b128 v[204:207], v208 offset:18816\n\t"
            "ds_read_b128 v[208:211], v208 offset:19840\n\t"
            "ds_read_b128 v[168:171], v235\n\t"
            "ds_read_u16 v158, v234\n\t"
            "ds_read_b128 v[220:223], v233\n\t"
            "ds_read_b128 v[172:175], v235 offset:1536\n\t"
            "ds_read_b64 v[164:165], v247 offset:1536\n\t"
            "ds_read_u16 v166, v234 offset:128\n\t"
            "ds_read_b128 v[176:179], v235 offset:3072\n\t"
            "ds_read_b64 v[160:161], v247 offset:3072\n\t"
            "ds_read_u16 v162, v234 offset:256\n\t"
            "v_add_u32 v234, s72, v252\n\t"
            "v_cndmask_b32_e64 v235, v246, v234, s[50:51]\n\t"
            "v_cndmask_b32_e64 v234, v246, v234, s[26:27]\n\t"
            "ds_read_b128 v[224:227], v232 offset:128\n\t"
            "s_waitcnt lgkmcnt(14)\n\t"
            "ds_read_b32 v239, v239\n\t"
            "s_waitcnt lgkmcnt(13)\n\t"
            "v_mfma_f32_16x16x32_bf16 v[148:151], v[72:75], v[216:219], 0\n\t"
            "s_waitcnt lgkmcnt(12)\n\t"
            "v_add_f32 v204, v204, v206\n\t"
            "v_add_f32 v205, v205, v207\n\t"
            "s_waitcnt lgkmcnt(11)\n\t"
            "v_add_f32 v208, v208, v210\n\t"
            "v_add_f32 v209, v209, v211\n\t"
            "v_add_f32 v204, v204, v208\n\t"
            "v_add_f32 v205, v205, v209\n\t"
            "v_mfma_f32_16x16x32_bf16 v[208:211], v[96:99], v[216:219], 0\n\t"
            "v_mul_f32 v204, 0x3baaaaab, v204\n\t"
            "v_mul_f32_e64 v204, v204, -v204\n\t"
            "v_mfma_f32_16x16x32_bf16 v[212:215], v[120:123], v[216:219], 0\n\t"
            "v_fmac_f32 v204, 0x3baaaaab, v205\n\t"
            "v_max_f32 v204, 0, v204\n\t"
            "s_waitcnt lgkmcnt(10)\n\t"
            "v_mfma_f32_16x16x32_bf16 v[228:231], v[168:171], v[180:183], 0\n\t"
            "v_add_f32 v204, 0x3727c5ac, v204\n\t"
            "v_rsq_f32 v204, v204\n\t"
            "s_waitcnt lgkmcnt(9)\n\t"
            "v_mfma_f32_16x16x32_bf16 v[228:231], v[156:159], v[184:187], v[228:231]\n\t"
            "ds_write_b32 v249, v204\n\t"
            "v_mfma_f32_16x16x32_bf16 v[144:147], v[0:3], v[216:219], 0\n\t"
            "v_mfma_f32_16x16x32_bf16 v[204:207], v[24:27], v[216:219], 0\n\t"
            "v_mfma_f32_16x16x32_bf16 v[152:155], v[48:51], v[216:219], 0\n\t"
            "ds_read_b128 v[216:219], v233 offset:128\n\t"
            "s_waitcnt lgkmcnt(10)\n\t"
            "v_mfma_f32_16x16x32_bf16 v[148:151], v[76:79], v[220:223], v[148:151]\n\t"
            "s_nop 0\n\t"
            "v_mul_f32 v228, v236, v228\n\t"
            "v_mfma_f32_16x16x32_bf16 v[208:211], v[100:103], v[220:223], v[208:211]\n\t"
            "v_mul_f32 v229, v236, v229\n\t"
            "v_mul_f32 v230, v236, v230\n\t"
            "v_mfma_f32_16x16x32_bf16 v[212:215], v[124:127], v[220:223], v[212:215]\n\t"
            "v_mul_f32 v231, v236, v231\n\t"
            "v_cvt_pk_bf16_f32 v228, v228, v229\n\t"
            "v_mfma_f32_16x16x32_bf16 v[144:147], v[4:7], v[220:223], v[144:147]\n\t"
            "v_cvt_pk_bf16_f32 v229, v230, v231\n\t"
            "v_mfma_f32_16x16x32_bf16 v[204:207], v[28:31], v[220:223], v[204:207]\n\t"
            "ds_write_b64 v245, v[228:229]\n\t"
            "v_mfma_f32_16x16x32_bf16 v[152:155], v[52:55], v[220:223], v[152:155]\n\t"
            "ds_read_b128 v[220:223], v232 offset:256\n\t"
            "s_waitcnt lgkmcnt(11)\n\t"
            "v_mfma_f32_16x16x32_bf16 v[228:231], v[172:175], v[180:183], 0\n\t"
            "s_waitcnt lgkmcnt(9)\n\t"
            "v_mfma_f32_16x16x32_bf16 v[228:231], v[164:167], v[184:187], v[228:231]\n\t"
            "s_waitcnt lgkmcnt(8)\n\t"
            "v_mfma_f32_16x16x32_bf16 v[180:183], v[176:179], v[180:183], 0\n\t"
            "s_waitcnt lgkmcnt(6)\n\t"
            "v_mfma_f32_16x16x32_bf16 v[180:183], v[160:163], v[184:187], v[180:183]\n\t"
            "s_waitcnt lgkmcnt(5)\n\t"
            "v_mfma_f32_16x16x32_bf16 v[144:147], v[8:11], v[224:227], v[144:147]\n\t"
            "v_mfma_f32_16x16x32_bf16 v[204:207], v[32:35], v[224:227], v[204:207]\n\t"
            "v_mfma_f32_16x16x32_bf16 v[152:155], v[56:59], v[224:227], v[152:155]\n\t"
            "v_mfma_f32_16x16x32_bf16 v[148:151], v[80:83], v[224:227], v[148:151]\n\t"
            "v_mul_f32 v228, v236, v228\n\t"
            "v_mfma_f32_16x16x32_bf16 v[208:211], v[104:107], v[224:227], v[208:211]\n\t"
            "v_mul_f32 v229, v236, v229\n\t"
            "v_mul_f32 v230, v236, v230\n\t"
            "v_mfma_f32_16x16x32_bf16 v[212:215], v[128:131], v[224:227], v[212:215]\n\t"
            "ds_read_b128 v[224:227], v233 offset:256\n\t"
            "v_mul_f32 v231, v236, v231\n\t"
            "v_mfma_f32_16x16x32_bf16 v[184:187], v[168:171], v[188:191], 0\n\t"
            "v_cvt_pk_bf16_f32 v228, v228, v229\n\t"
            "v_cvt_pk_bf16_f32 v229, v230, v231\n\t"
            "v_mfma_f32_16x16x32_bf16 v[184:187], v[156:159], v[192:195], v[184:187]\n\t"
            "ds_write_b64 v245, v[228:229] offset:128\n\t"
            "v_mfma_f32_16x16x32_bf16 v[228:231], v[172:175], v[188:191], 0\n\t"
            "v_mul_f32 v180, v236, v180\n\t"
            "v_mul_f32 v181, v236, v181\n\t"
            "v_mfma_f32_16x16x32_bf16 v[228:231], v[164:167], v[192:195], v[228:231]\n\t"
            "v_mul_f32 v182, v236, v182\n\t"
            "v_mul_f32 v183, v236, v183\n\t"
            "s_waitcnt lgkmcnt(4)\n\t"
            "v_mfma_f32_16x16x32_bf16 v[148:151], v[84:87], v[216:219], v[148:151]\n\t"
            "ds_read_b32 v236, v251\n\t"
            "v_cvt_pk_bf16_f32 v180, v180, v181\n\t"
            "v_mfma_f32_16x16x32_bf16 v[208:211], v[108:111], v[216:219], v[208:211]\n\t"
            "v_cvt_pk_bf16_f32 v181, v182, v183\n\t"
            "v_mul_f32 v184, v237, v184\n\t"
            "v_mfma_f32_16x16x32_bf16 v[212:215], v[132:135], v[216:219], v[212:215]\n\t"
            "ds_write_b64 v245, v[180:181] offset:256\n\t"
            "v_mfma_f32_16x16x32_bf16 v[144:147], v[12:15], v[216:219], v[144:147]\n\t"
            "v_mul_f32 v185, v237, v185\n\t"
            "v_mul_f32 v186, v237, v186\n\t"
            "v_mfma_f32_16x16x32_bf16 v[204:207], v[36:39], v[216:219], v[204:207]\n\t"
            "v_mul_f32 v187, v237, v187\n\t"
            "v_cvt_pk_bf16_f32 v184, v184, v185\n\t"
            "v_mfma_f32_16x16x32_bf16 v[152:155], v[60:63], v[216:219], v[152:155]\n\t"
            "ds_read_b128 v[216:219], v232 offset:6144\n\t"
            "v_cvt_pk_bf16_f32 v185, v186, v187\n\t"
            "v_mfma_f32_16x16x32_bf16 v[180:183], v[168:171], v[196:199], 0\n\t"
            "ds_read_b64 v[186:187], v253 offset:352\n\t"
            "v_mul_f32 v228, v237, v228\n\t"
            "v_mfma_f32_16x16x32_bf16 v[180:183], v[156:159], v[200:203], v[180:183]\n\t"
            "ds_write_b64 v245, v[184:185] offset:6144\n\t"
            "s_waitcnt lgkmcnt(7)\n\t"
            "v_mfma_f32_16x16x32_bf16 v[148:151], v[88:91], v[220:223], v[148:151]\n\t"
            "ds_read_b64 v[184:185], v253 offset:320\n\t"
            "v_mul_f32 v229, v237, v229\n\t"
            "v_mfma_f32_16x16x32_bf16 v[208:211], v[112:115], v[220:223], v[208:211]\n\t"
            "v_mul_f32 v230, v237, v230\n\t"
            "v_mul_f32 v231, v237, v231\n\t"
            "v_mfma_f32_16x16x32_bf16 v[212:215], v[136:139], v[220:223], v[212:215]\n\t"
            "v_mul_f32 v180, v238, v180\n\t"
            "v_mul_f32 v181, v238, v181\n\t"
            "v_mfma_f32_16x16x32_bf16 v[188:191], v[176:179], v[188:191], 0\n\t"
            "v_mul_f32 v182, v238, v182\n\t"
            "v_mul_f32 v183, v238, v183\n\t"
            "v_mfma_f32_16x16x32_bf16 v[188:191], v[160:163], v[192:195], v[188:191]\n\t"
            "ds_read_b128 v[192:195], v232 offset:6400\n\t"
            "v_cvt_pk_bf16_f32 v180, v180, v181\n\t"
            "v_mfma_f32_16x16x32_bf16 v[144:147], v[16:19], v[220:223], v[144:147]\n\t"
            "v_cvt_pk_bf16_f32 v181, v182, v183\n\t"
            "v_cvt_pk_bf16_f32 v228, v228, v229\n\t"
            "v_mfma_f32_16x16x32_bf16 v[204:207], v[40:43], v[220:223], v[204:207]\n\t"
            "ds_read_b64 v[182:183], v253 offset:288\n\t"
            "v_cvt_pk_bf16_f32 v229, v230, v231\n\t"
            "v_mfma_f32_16x16x32_bf16 v[152:155], v[64:67], v[220:223], v[152:155]\n\t"
            "ds_read_b128 v[220:223], v233 offset:6144\n\t"
            "v_mul_f32 v188, v237, v188\n\t"
            "s_waitcnt lgkmcnt(10)\n\t"
            "v_mfma_f32_16x16x32_bf16 v[144:147], v[20:23], v[224:227], v[144:147]\n\t"
            "ds_write_b64 v245, v[180:181] offset:12288\n\t"
            "v_mfma_f32_16x16x32_bf16 v[204:207], v[44:47], v[224:227], v[204:207]\n\t"
            "ds_read_b64 v[180:181], v253 offset:256\n\t"
            "v_mul_f32 v189, v237, v189\n\t"
            "v_mfma_f32_16x16x32_bf16 v[152:155], v[68:71], v[224:227], v[152:155]\n\t"
            "ds_write_b64 v245, v[228:229] offset:6272\n\t"
            "v_mfma_f32_16x16x32_bf16 v[148:151], v[92:95], v[224:227], v[148:151]\n\t"
            "v_mul_f32 v190, v237, v190\n\t"
            "v_mul_f32 v191, v237, v191\n\t"
            "v_mfma_f32_16x16x32_bf16 v[208:211], v[116:119], v[224:227], v[208:211]\n\t"
            "ds_read_b32 v237, v251 offset:64\n\t"
            "v_cvt_pk_bf16_f32 v188, v188, v189\n\t"
            "v_mfma_f32_16x16x32_bf16 v[212:215], v[140:143], v[224:227], v[212:215]\n\t"
            "ds_read_b128 v[224:227], v232 offset:6272\n\t"
            "v_cvt_pk_bf16_f32 v189, v190, v191\n\t"
            "v_mfma_f32_16x16x32_bf16 v[228:231], v[172:175], v[196:199], 0\n\t"
            "s_waitcnt lgkmcnt(14)\n\t"
            "ds_write_b64 v245, v[188:189] offset:6400\n\t"
            "v_mfma_f32_16x16x32_bf16 v[228:231], v[164:167], v[200:203], v[228:231]\n\t"
            "s_waitcnt lgkmcnt(14)\n\t"
            "ds_read_b128 v[188:191], v233 offset:6272\n\t"
            "v_mul_f32 v152, v236, v152\n\t"
            "v_mfma_f32_16x16x32_bf16 v[196:199], v[176:179], v[196:199], 0\n\t"
            "v_mul_f32 v153, v236, v153\n\t"
            "v_mul_f32 v154, v236, v154\n\t"
            "v_mfma_f32_16x16x32_bf16 v[196:199], v[160:163], v[200:203], v[196:199]\n\t"
            "s_waitcnt lgkmcnt(14)\n\t"
            "ds_read_b128 v[200:203], v233 offset:6400\n\t"
            "v_mul_f32 v155, v236, v155\n\t"
            "s_waitcnt lgkmcnt(6)\n\t"
            "v_mfma_f32_16x16x32_bf16 v[168:171], v[168:171], v[180:183], 0\n\t"
            "v_mul_f32 v228, v238, v228\n\t"
            "v_mul_f32 v229, v238, v229\n\t"
            "v_mfma_f32_16x16x32_bf16 v[168:171], v[156:159], v[184:187], v[168:171]\n\t"
            "v_mul_f32 v230, v238, v230\n\t"
            "v_mul_f32 v231, v238, v231\n\t"
            "v_mfma_f32_16x16x32_bf16 v[176:179], v[176:179], v[180:183], 0\n\t"
            "v_cvt_pk_bf16_f32 v228, v228, v229\n\t"
            "v_cvt_pk_bf16_f32 v229, v230, v231\n\t"
            "v_mfma_f32_16x16x32_bf16 v[176:179], v[160:163], v[184:187], v[176:179]\n\t"
            "ds_write_b64 v245, v[228:229] offset:12416\n\t"
            "v_mfma_f32_16x16x32_bf16 v[228:231], v[240:243], v[180:183], 0\n\t"
            "v_cvt_pk_bf16_f32 v152, v152, v153\n\t"
            "v_cvt_pk_bf16_f32 v153, v154, v155\n\t"
            "v_mfma_f32_16x16x32_bf16 v[228:231], v[240:243], v[184:187], v[228:231]\n\t"
            "v_mul_f32 v212, v236, v212\n\t"
            "v_mul_f32 v213, v236, v213\n\t"
            "v_mfma_f32_16x16x32_bf16 v[160:163], v[72:75], v[216:219], 0\n\t"
            "v_mul_f32 v214, v236, v214\n\t"
            "v_mul_f32 v215, v236, v215\n\t"
            "v_mfma_f32_16x16x32_bf16 v[160:163], v[76:79], v[220:223], v[160:163]\n\t"
            "v_cvt_pk_bf16_f32 v154, v212, v213\n\t"
            "v_cvt_pk_bf16_f32 v155, v214, v215\n\t"
            "s_waitcnt lgkmcnt(4)\n\t"
            "v_mfma_f32_16x16x32_bf16 v[160:163], v[80:83], v[224:227], v[160:163]\n\t"
            "ds_read_b128 v[212:215], v232 offset:12416\n\t"
            "v_mul_f32 v196, v238, v196\n\t"
            "s_waitcnt lgkmcnt(3)\n\t"
            "v_mfma_f32_16x16x32_bf16 v[160:163], v[84:87], v[188:191], v[160:163]\n\t"
            "v_mul_f32 v197, v238, v197\n\t"
            "v_mul_f32 v198, v238, v198\n\t"
            "v_mfma_f32_16x16x32_bf16 v[160:163], v[88:91], v[192:195], v[160:163]\n\t"
            "v_mul_f32 v199, v238, v199\n\t"
            "v_cvt_pk_bf16_f32 v196, v196, v197\n\t"
            "s_waitcnt lgkmcnt(2)\n\t"
            "v_mfma_f32_16x16x32_bf16 v[160:163], v[92:95], v[200:203], v[160:163]\n\t"
            "ds_read_b32 v238, v251 offset:128\n\t"
            "v_cvt_pk_bf16_f32 v197, v198, v199\n\t"
            "v_mfma_f32_16x16x32_bf16 v[172:175], v[172:175], v[180:183], 0\n\t"
            "ds_write_b64 v245, v[196:197] offset:12544\n\t"
            "v_mfma_f32_16x16x32_bf16 v[172:175], v[164:167], v[184:187], v[172:175]\n\t"
            "v_rcp_f32 v229, v228\n\t"
            "v_mfma_f32_16x16x16_bf16 v[180:183], v[152:153], v[154:155], 0\n\t"
            "v_mul_f32 v168, v229, v168\n\t"
            "v_mul_f32 v169, v229, v169\n\t"
            "v_mul_f32 v176, v229, v176\n\t"
            "v_mfma_f32_16x16x32_bf16 v[196:199], v[24:27], v[216:219], 0\n\t"
            "v_mul_f32 v177, v229, v177\n\t"
            "v_mul_f32 v170, v229, v170\n\t"
            "v_mfma_f32_16x16x32_bf16 v[196:199], v[28:31], v[220:223], v[196:199]\n\t"
            "v_mul_f32 v172, v229, v172\n\t"
            "v_mul_f32 v173, v229, v173\n\t"
            "v_mfma_f32_16x16x32_bf16 v[196:199], v[32:35], v[224:227], v[196:199]\n\t"
            "v_mul_f32 v171, v229, v171\n\t"
            "v_cvt_pk_bf16_f32 v168, v168, v169\n\t"
            "v_mfma_f32_16x16x32_bf16 v[196:199], v[36:39], v[188:191], v[196:199]\n\t"
            "v_mul_f32 v174, v229, v174\n\t"
            "v_mul_f32 v175, v229, v175\n\t"
            "v_mfma_f32_16x16x32_bf16 v[196:199], v[40:43], v[192:195], v[196:199]\n\t"
            "v_cvt_pk_bf16_f32 v172, v172, v173\n\t"
            "v_mul_f32 v178, v229, v178\n\t"
            "v_mfma_f32_16x16x32_bf16 v[196:199], v[44:47], v[200:203], v[196:199]\n\t"
            "v_mul_f32 v179, v229, v179\n\t"
            "v_cvt_pk_bf16_f32 v176, v176, v177\n\t"
            "v_mfma_f32_16x16x32_bf16 v[156:159], v[0:3], v[216:219], 0\n\t"
            "v_xor_b32 v230, s73, v255\n\t"
            "v_cvt_pk_bf16_f32 v169, v170, v171\n\t"
            "v_mfma_f32_16x16x32_bf16 v[156:159], v[4:7], v[220:223], v[156:159]\n\t"
            "v_cvt_pk_bf16_f32 v173, v174, v175\n\t"
            "v_cvt_pk_bf16_f32 v177, v178, v179\n\t"
            "v_mfma_f32_16x16x32_bf16 v[156:159], v[8:11], v[224:227], v[156:159]\n\t"
            "s_mov_b64 exec, s[68:69]\n\t"
            "ds_write_b64 v230, v[168:169]\n\t"
            "ds_write_b64 v230, v[172:173] offset:128\n\t"
            "ds_write_b64 v230, v[176:177] offset:256\n\t"
            "s_mov_b64 exec, -1\n\t"
            "v_mfma_f32_16x16x32_bf16 v[156:159], v[12:15], v[188:191], v[156:159]\n\t"
            "v_mul_f32 v144, v236, v144\n\t"
            "v_mul_f32 v145, v236, v145\n\t"
            "v_mfma_f32_16x16x32_bf16 v[156:159], v[16:19], v[192:195], v[156:159]\n\t"
            "v_mul_f32 v146, v236, v146\n\t"
            "v_mul_f32 v147, v236, v147\n\t"
            "v_mfma_f32_16x16x32_bf16 v[156:159], v[20:23], v[200:203], v[156:159]\n\t"
            "v_cvt_pk_bf16_f32 v144, v144, v145\n\t"
            "v_mul_f32 v148, v236, v148\n\t"
            "v_mfma_f32_16x16x32_bf16 v[164:167], v[48:51], v[216:219], 0\n\t"
            "v_mul_f32 v149, v236, v149\n\t"
            "v_cvt_pk_bf16_f32 v145, v146, v147\n\t"
            "v_mfma_f32_16x16x32_bf16 v[164:167], v[52:55], v[220:223], v[164:167]\n\t"
            "v_mul_f32 v204, v236, v204\n\t"
            "v_mul_f32 v205, v236, v205\n\t"
            "v_mfma_f32_16x16x32_bf16 v[164:167], v[56:59], v[224:227], v[164:167]\n\t"
            "v_mul_f32 v206, v236, v206\n\t"
            "v_mul_f32 v207, v236, v207\n\t"
            "v_mfma_f32_16x16x32_bf16 v[164:167], v[60:63], v[188:191], v[164:167]\n\t"
            "s_waitcnt lgkmcnt(0)\n\t"
            "s_barrier\n\t"
            "s_setprio 2\n\t"
            "v_mul_f32 v150, v236, v150\n\t"
            "v_mul_f32 v151, v236, v151\n\t"
            "v_mfma_f32_16x16x32_bf16 v[164:167], v[64:67], v[192:195], v[164:167]\n\t"
            "v_cvt_pk_bf16_f32 v148, v148, v149\n\t"
            "v_cvt_pk_bf16_f32 v146, v204, v205\n\t"
            "v_mfma_f32_16x16x32_bf16 v[164:167], v[68:71], v[200:203], v[164:167]\n\t"
            "v_cvt_pk_bf16_f32 v147, v206, v207\n\t"
            "v_cvt_pk_bf16_f32 v149, v150, v151\n\t"
            "v_mfma_f32_16x16x32_bf16 v[228:231], v[96:99], v[216:219], 0\n\t"
            "ds_read_b128 v[204:207], v232 offset:12288\n\t"
            "v_mul_f32 v208, v236, v208\n\t"
            "v_mfma_f32_16x16x32_bf16 v[228:231], v[100:103], v[220:223], v[228:231]\n\t"
            "v_mul_f32 v209, v236, v209\n\t"
            "v_mul_f32 v210, v236, v210\n\t"
            "v_mfma_f32_16x16x32_bf16 v[228:231], v[104:107], v[224:227], v[228:231]\n\t"
            "v_mul_f32 v211, v236, v211\n\t"
            "v_cvt_pk_bf16_f32 v150, v208, v209\n\t"
            "v_mfma_f32_16x16x32_bf16 v[228:231], v[108:111], v[188:191], v[228:231]\n\t"
            "v_cvt_pk_bf16_f32 v151, v210, v211\n\t"
            "v_mul_f32 v156, v237, v156\n\t"
            "v_mfma_f32_16x16x32_bf16 v[228:231], v[112:115], v[192:195], v[228:231]\n\t"
            "ds_read_b128 v[208:211], v233 offset:12288\n\t"
            "v_mul_f32 v157, v237, v157\n\t"
            "v_mfma_f32_16x16x32_bf16 v[228:231], v[116:119], v[200:203], v[228:231]\n\t"
            "v_mul_f32 v158, v237, v158\n\t"
            "v_mul_f32 v159, v237, v159\n\t"
            "v_mfma_f32_16x16x32_bf16 v[180:183], v[144:147], v[148:151], v[180:183]\n\t"
            "v_cvt_pk_bf16_f32 v156, v156, v157\n\t"
            "v_cvt_pk_bf16_f32 v157, v158, v159\n\t"
            "v_mul_f32 v196, v237, v196\n\t"
            "v_mul_f32 v197, v237, v197\n\t"
            "v_mul_f32 v198, v237, v198\n\t"
            "v_mul_f32 v199, v237, v199\n\t"
            "v_cvt_pk_bf16_f32 v158, v196, v197\n\t"
            "v_cvt_pk_bf16_f32 v159, v198, v199\n\t"
            "v_mfma_f32_16x16x32_bf16 v[196:199], v[120:123], v[216:219], 0\n\t"
            "ds_read_b128 v[216:219], v233 offset:12416\n\t"
            "v_mul_f32 v164, v237, v164\n\t"
            "v_mfma_f32_16x16x32_bf16 v[196:199], v[124:127], v[220:223], v[196:199]\n\t"
            "ds_read_b128 v[220:223], v232 offset:12544\n\t"
            "v_mul_f32 v165, v237, v165\n\t"
            "v_mfma_f32_16x16x32_bf16 v[196:199], v[128:131], v[224:227], v[196:199]\n\t"
            "ds_read_b128 v[224:227], v233 offset:12544\n\t"
            "v_mul_f32 v166, v237, v166\n\t"
            "v_mfma_f32_16x16x32_bf16 v[196:199], v[132:135], v[188:191], v[196:199]\n\t"
            "v_mul_f32 v167, v237, v167\n\t"
            "v_cvt_pk_bf16_f32 v164, v164, v165\n\t"
            "v_mfma_f32_16x16x32_bf16 v[196:199], v[136:139], v[192:195], v[196:199]\n\t"
            "v_cvt_pk_bf16_f32 v165, v166, v167\n\t"
            "v_mul_f32 v160, v237, v160\n\t"
            "v_mfma_f32_16x16x32_bf16 v[196:199], v[140:143], v[200:203], v[196:199]\n\t"
            "v_mul_f32 v161, v237, v161\n\t"
            "v_mul_f32 v162, v237, v162\n\t"
            "s_waitcnt lgkmcnt(4)\n\t"
            "v_mfma_f32_16x16x32_bf16 v[176:179], v[48:51], v[204:207], 0\n\t"
            "v_mul_f32 v163, v237, v163\n\t"
            "v_cvt_pk_bf16_f32 v160, v160, v161\n\t"
            "s_waitcnt lgkmcnt(3)\n\t"
            "v_mfma_f32_16x16x32_bf16 v[176:179], v[52:55], v[208:211], v[176:179]\n\t"
            "v_cvt_pk_bf16_f32 v161, v162, v163\n\t"
            "v_mul_f32 v228, v237, v228\n\t"
            "v_mfma_f32_16x16x32_bf16 v[176:179], v[56:59], v[212:215], v[176:179]\n\t"
            "v_mul_f32 v196, v237, v196\n\t"
            "v_mul_f32 v197, v237, v197\n\t"
            "s_waitcnt lgkmcnt(2)\n\t"
            "v_mfma_f32_16x16x32_bf16 v[176:179], v[60:63], v[216:219], v[176:179]\n\t"
            "v_mul_f32 v198, v237, v198\n\t"
            "v_mul_f32 v199, v237, v199\n\t"
            "s_waitcnt lgkmcnt(1)\n\t"
            "v_mfma_f32_16x16x32_bf16 v[176:179], v[64:67], v[220:223], v[176:179]\n\t"
            "v_cvt_pk_bf16_f32 v166, v196, v197\n\t"
            "v_cvt_pk_bf16_f32 v167, v198, v199\n\t"
            "s_waitcnt lgkmcnt(0)\n\t"
            "v_mfma_f32_16x16x32_bf16 v[176:179], v[68:71], v[224:227], v[176:179]\n\t"
            "v_mul_f32 v229, v237, v229\n\t"
            "v_mul_f32 v230, v237, v230\n\t"
            "v_mfma_f32_16x16x16_bf16 v[188:191], v[152:153], v[166:167], 0\n\t"
            "v_mul_f32 v231, v237, v231\n\t"
            "v_cvt_pk_bf16_f32 v162, v228, v229\n\t"
            "v_cvt_pk_bf16_f32 v163, v230, v231\n\t"
            "v_mfma_f32_16x16x32_bf16 v[168:171], v[0:3], v[204:207], 0\n\t"
            "v_exp_f32 v180, v180\n\t"
            "v_mfma_f32_16x16x32_bf16 v[168:171], v[4:7], v[208:211], v[168:171]\n\t"
            "v_exp_f32 v181, v181\n\t"
            "v_mfma_f32_16x16x32_bf16 v[168:171], v[8:11], v[212:215], v[168:171]\n\t"
            "v_exp_f32 v182, v182\n\t"
            "v_mfma_f32_16x16x32_bf16 v[168:171], v[12:15], v[216:219], v[168:171]\n\t"
            "v_exp_f32 v183, v183\n\t"
            "v_mfma_f32_16x16x32_bf16 v[168:171], v[16:19], v[220:223], v[168:171]\n\t"
            "v_mul_f32 v176, v238, v176\n\t"
            "v_mul_f32 v177, v238, v177\n\t"
            "v_mfma_f32_16x16x32_bf16 v[168:171], v[20:23], v[224:227], v[168:171]\n\t"
            "v_mul_f32 v178, v238, v178\n\t"
            "v_mul_f32 v179, v238, v179\n\t"
            "v_mfma_f32_16x16x16_bf16 v[192:195], v[164:165], v[166:167], 0\n\t"
            "v_cvt_pk_bf16_f32 v176, v176, v177\n\t"
            "v_cvt_pk_bf16_f32 v180, v180, v181\n\t"
            "v_cvt_pk_bf16_f32 v177, v178, v179\n\t"
            "v_mfma_f32_16x16x32_bf16 v[188:191], v[144:147], v[160:163], v[188:191]\n\t"
            "v_cvt_pk_bf16_f32 v181, v182, v183\n\t"
            "v_mul_f32 v168, v238, v168\n\t"
            "v_mfma_f32_16x16x16_bf16 v[184:187], v[164:165], v[154:155], 0\n\t"
            "v_mul_f32 v169, v238, v169\n\t"
            "v_mul_f32 v170, v238, v170\n\t"
            "v_mul_f32 v171, v238, v171\n\t"
            "v_mfma_f32_16x16x32_bf16 v[192:195], v[156:159], v[160:163], v[192:195]\n\t"
            "s_nop 0\n\t"
            "v_exp_f32 v188, v188\n\t"
            "v_mfma_f32_16x16x32_bf16 v[172:175], v[72:75], v[204:207], 0\n\t"
            "v_exp_f32 v189, v189\n\t"
            "v_mfma_f32_16x16x32_bf16 v[172:175], v[76:79], v[208:211], v[172:175]\n\t"
            "v_exp_f32 v190, v190\n\t"
            "v_mfma_f32_16x16x32_bf16 v[172:175], v[80:83], v[212:215], v[172:175]\n\t"
            "v_exp_f32 v191, v191\n\t"
            "v_mfma_f32_16x16x32_bf16 v[172:175], v[84:87], v[216:219], v[172:175]\n\t"
            "v_exp_f32 v192, v192\n\t"
            "v_mfma_f32_16x16x32_bf16 v[172:175], v[88:91], v[220:223], v[172:175]\n\t"
            "v_exp_f32 v193, v193\n\t"
            "v_mfma_f32_16x16x32_bf16 v[172:175], v[92:95], v[224:227], v[172:175]\n\t"
            "v_exp_f32 v194, v194\n\t"
            "v_mfma_f32_16x16x32_bf16 v[184:187], v[156:159], v[148:151], v[184:187]\n\t"
            "v_exp_f32 v195, v195\n\t"
            "v_mfma_f32_16x16x32_bf16 v[200:203], v[120:123], v[204:207], 0\n\t"
            "v_cvt_pk_bf16_f32 v188, v188, v189\n\t"
            "v_cvt_pk_bf16_f32 v189, v190, v191\n\t"
            "v_mfma_f32_16x16x32_bf16 v[200:203], v[124:127], v[208:211], v[200:203]\n\t"
            "v_cvt_pk_bf16_f32 v190, v192, v193\n\t"
            "v_cvt_pk_bf16_f32 v191, v194, v195\n\t"
            "v_mfma_f32_16x16x32_bf16 v[200:203], v[128:131], v[212:215], v[200:203]\n\t"
            "v_exp_f32 v184, v184\n\t"
            "v_mfma_f32_16x16x32_bf16 v[200:203], v[132:135], v[216:219], v[200:203]\n\t"
            "v_exp_f32 v185, v185\n\t"
            "v_mfma_f32_16x16x32_bf16 v[200:203], v[136:139], v[220:223], v[200:203]\n\t"
            "v_exp_f32 v186, v186\n\t"
            "v_mfma_f32_16x16x32_bf16 v[200:203], v[140:143], v[224:227], v[200:203]\n\t"
            "v_exp_f32 v187, v187\n\t"
            "v_cvt_pk_bf16_f32 v182, v184, v185\n\t"
            "v_cvt_pk_bf16_f32 v183, v186, v187\n\t"
            "v_mfma_f32_16x16x16_bf16 v[184:187], v[176:177], v[154:155], 0\n\t"
            "v_cvt_pk_bf16_f32 v168, v168, v169\n\t"
            "v_cvt_pk_bf16_f32 v169, v170, v171\n\t"
            "v_mul_f32 v172, v238, v172\n\t"
            "v_mfma_f32_16x16x32_bf16 v[196:199], v[24:27], v[204:207], 0\n\t"
            "s_setprio 0\n\t"
            "v_mul_f32 v173, v238, v173\n\t"
            "v_mul_f32 v200, v238, v200\n\t"
            "v_mfma_f32_16x16x32_bf16 v[196:199], v[28:31], v[208:211], v[196:199]\n\t"
            "v_mul_f32 v201, v238, v201\n\t"
            "v_mul_f32 v202, v238, v202\n\t"
            "v_mfma_f32_16x16x32_bf16 v[196:199], v[32:35], v[212:215], v[196:199]\n\t"
            "v_mul_f32 v203, v238, v203\n\t"
            "v_mul_f32 v174, v238, v174\n\t"
            "v_mfma_f32_16x16x32_bf16 v[196:199], v[36:39], v[216:219], v[196:199]\n\t"
            "v_mul_f32 v175, v238, v175\n\t"
            "v_cvt_pk_bf16_f32 v172, v172, v173\n\t"
            "v_mfma_f32_16x16x32_bf16 v[196:199], v[40:43], v[220:223], v[196:199]\n\t"
            "v_cvt_pk_bf16_f32 v178, v200, v201\n\t"
            "v_cvt_pk_bf16_f32 v179, v202, v203\n\t"
            "v_mfma_f32_16x16x32_bf16 v[196:199], v[44:47], v[224:227], v[196:199]\n\t"
            "v_cvt_pk_bf16_f32 v173, v174, v175\n\t"
            "v_mfma_f32_16x16x32_bf16 v[228:231], v[96:99], v[204:207], 0\n\t"
            "ds_read_b64 v[204:205], v235 offset:136\n\t"
            "v_mfma_f32_16x16x32_bf16 v[228:231], v[100:103], v[208:211], v[228:231]\n\t"
            "ds_read_b64 v[206:207], v234 offset:128\n\t"
            "v_mfma_f32_16x16x32_bf16 v[228:231], v[104:107], v[212:215], v[228:231]\n\t"
            "ds_read_b128 v[208:211], v234\n\t"
            "v_mfma_f32_16x16x32_bf16 v[228:231], v[108:111], v[216:219], v[228:231]\n\t"
            "ds_read_b128 v[212:215], v235 offset:64\n\t"
            "v_mul_f32 v196, v238, v196\n\t"
            "v_mfma_f32_16x16x32_bf16 v[228:231], v[112:115], v[220:223], v[228:231]\n\t"
            "v_mul_f32 v197, v238, v197\n\t"
            "v_mul_f32 v198, v238, v198\n\t"
            "v_mfma_f32_16x16x32_bf16 v[228:231], v[116:119], v[224:227], v[228:231]\n\t"
            "v_mul_f32 v199, v238, v199\n\t"
            "v_cvt_pk_bf16_f32 v170, v196, v197\n\t"
            "v_mfma_f32_16x16x16_bf16 v[192:195], v[176:177], v[166:167], 0\n\t"
            "v_cvt_pk_bf16_f32 v171, v198, v199\n\t"
            "s_nop 1\n\t"
            "v_mfma_f32_16x16x32_bf16 v[184:187], v[168:171], v[148:151], v[184:187]\n\t"
            "s_nop 0\n\t"
            "v_mul_f32 v228, v238, v228\n\t"
            "v_mul_f32 v229, v238, v229\n\t"
            "v_mul_f32 v230, v238, v230\n\t"
            "v_mul_f32 v231, v238, v231\n\t"
            "v_mfma_f32_16x16x32_bf16 v[192:195], v[168:171], v[160:163], v[192:195]\n\t"
            "v_cvt_pk_bf16_f32 v174, v228, v229\n\t"
            "v_cvt_pk_bf16_f32 v175, v230, v231\n\t"
            "s_waitcnt lgkmcnt(2)\n\t"
            "v_mfma_f32_16x16x32_bf16 v[216:219], v[204:207], v[152:155], 0\n\t"
            "v_exp_f32 v184, v184\n\t"
            "s_waitcnt lgkmcnt(1)\n\t"
            "v_mfma_f32_16x16x32_bf16 v[216:219], v[208:211], v[148:151], v[216:219]\n\t"
            "v_exp_f32 v185, v185\n\t"
            "s_waitcnt lgkmcnt(0)\n\t"
            "v_mfma_f32_16x16x32_bf16 v[216:219], v[212:215], v[144:147], v[216:219]\n\t"
            "v_exp_f32 v186, v186\n\t"
            "v_mfma_f32_16x16x32_bf16 v[220:223], v[204:207], v[164:167], 0\n\t"
            "v_exp_f32 v187, v187\n\t"
            "v_mfma_f32_16x16x32_bf16 v[220:223], v[208:211], v[160:163], v[220:223]\n\t"
            "v_cvt_pk_bf16_f32 v184, v184, v185\n\t"
            "v_cvt_pk_bf16_f32 v185, v186, v187\n\t"
            "v_mfma_f32_16x16x32_bf16 v[220:223], v[212:215], v[156:159], v[220:223]\n\t"
            "s_nop 0\n\t"
            "v_exp_f32 v216, v216\n\t"
            "v_mfma_f32_16x16x16_bf16 v[228:231], v[164:165], v[178:179], 0\n\t"
            "v_cvt_pk_bf16_f32 v216, v216, 0\n\t"
            "v_cndmask_b32_e64 v186, 0, v216, s[58:59]\n\t"
            "v_mov_b32 v187, 0\n\t"
            "v_mfma_f32_16x16x16_bf16 v[200:203], v[176:177], v[178:179], 0\n\t"
            "v_exp_f32 v192, v192\n\t"
            "v_exp_f32 v193, v193\n\t"
            "v_mfma_f32_16x16x32_bf16 v[224:227], v[204:207], v[176:179], 0\n\t"
            "v_exp_f32 v194, v194\n\t"
            "v_mfma_f32_16x16x32_bf16 v[224:227], v[208:211], v[172:175], v[224:227]\n\t"
            "v_exp_f32 v195, v195\n\t"
            "v_mfma_f32_16x16x32_bf16 v[224:227], v[212:215], v[168:171], v[224:227]\n\t"
            "v_exp_f32 v220, v220\n\t"
            "v_mfma_f32_16x16x16_bf16 v[196:199], v[152:153], v[178:179], 0\n\t"
            "v_cvt_pk_bf16_f32 v192, v192, v193\n\t"
            "v_cvt_pk_bf16_f32 v193, v194, v195\n\t"
            "v_cvt_pk_bf16_f32 v220, v220, 0\n\t"
            "v_mfma_f32_16x16x32_bf16 v[204:207], v[240:243], v[180:183], 0\n\t"
            "v_cndmask_b32_e64 v194, 0, v220, s[58:59]\n\t"
            "v_mov_b32 v195, 0\n\t"
            "v_mfma_f32_16x16x32_bf16 v[204:207], v[240:243], v[184:187], v[204:207]\n\t"
            "v_cndmask_b32_e64 v235, v217, v221, s[60:61]\n\t"
            "v_cndmask_b32_e64 v235, v235, v225, s[62:63]\n\t"
            "v_mfma_f32_16x16x32_bf16 v[200:203], v[168:171], v[172:175], v[200:203]\n\t"
            "v_cndmask_b32_e64 v235, v235, v239, s[64:65]\n\t"
            "v_cmp_nlt_f32_e64 s[24:25], |v235|, s23\n\t"
            "v_mfma_f32_16x16x32_bf16 v[208:211], v[240:243], v[188:191], 0\n\t"
            "v_exp_f32 v235, v235\n\t"
            "v_mfma_f32_16x16x32_bf16 v[208:211], v[240:243], v[192:195], v[208:211]\n\t"
            "v_exp_f32 v224, v224\n\t"
            "v_mfma_f32_16x16x32_bf16 v[196:199], v[144:147], v[172:175], v[196:199]\n\t"
            "s_nop 0\n\t"
            "v_exp_f32 v200, v200\n\t"
            "v_mfma_f32_16x16x32_bf16 v[228:231], v[156:159], v[172:175], v[228:231]\n\t"
            "v_exp_f32 v201, v201\n\t"
            "v_exp_f32 v202, v202\n\t"
            "v_exp_f32 v203, v203\n\t"
            "v_cvt_pk_bf16_f32 v235, v235, 0\n\t"
            "v_cvt_pk_bf16_f32 v200, v200, v201\n\t"
            "v_cndmask_b32_e64 v235, 0, v235, s[66:67]\n\t"
            "v_exp_f32 v196, v196\n\t"
            "v_exp_f32 v197, v197\n\t"
            "v_exp_f32 v198, v198\n\t"
            "v_exp_f32 v199, v199\n\t"
            "v_exp_f32 v228, v228\n\t"
            "v_exp_f32 v229, v229\n\t"
            "v_exp_f32 v230, v230\n\t"
            "v_exp_f32 v231, v231\n\t"
            "v_cvt_pk_bf16_f32 v196, v196, v197\n\t"
            "v_cvt_pk_bf16_f32 v197, v198, v199\n\t"
            "v_cvt_pk_bf16_f32 v198, v228, v229\n\t"
            "v_cvt_pk_bf16_f32 v199, v230, v231\n\t"
            "v_cvt_pk_bf16_f32 v201, v202, v203\n\t"
            "v_cvt_pk_bf16_f32 v224, v224, 0\n\t"
            "v_mfma_f32_16x16x32_bf16 v[212:215], v[240:243], v[196:199], 0\n\t"
            "ds_write_b16 v250, v235 offset:256\n\t"
            "v_cndmask_b32_e64 v202, 0, v224, s[58:59]\n\t"
            "v_mov_b32 v203, 0\n\t"
            "v_add_u32 v205, 0xe0800000, v204\n\t"
            "v_add_u32 v209, 0xe0800000, v208\n\t"
            "v_mfma_f32_16x16x32_bf16 v[212:215], v[240:243], v[200:203], v[212:215]\n\t"
            "v_rcp_f32 v236, v204\n\t"
            "v_rcp_f32 v237, v208\n\t"
            "s_nop 5\n\t"
            "v_add_u32 v213, 0xe0800000, v212\n\t"
            "v_rcp_f32 v238, v212\n\t"
            "v_or3_b32 v205, v205, v209, v213\n\t"
            "v_cmp_le_u32 vcc, 2.0, v205\n\t"
            ".Lwa_in%=:\n\t"
            "s_or_b64 vcc, vcc, s[24:25]\n\t"
            "s_cbranch_scc1 .Lwa_slow%=\n\t"
            ".Lwa_ok%=:\n\t"
            "s_bfe_u32 s49, s70, 0x30001\n\t"
            "s_lshl_b32 s49, s49, 4\n\t"
            "s_or_b32 s73, s77, s49\n\t"
            "s_add_u32 s70, s70, 1\n\t"
            "s_add_u32 s72, s72, 800\n\t"
            "s_add_u32 s76, s76, 800\n\t"
            "s_add_u32 s77, s77, 384\n\t"
            "s_xor_b32 s48, s48, s47\n\t"
            "s_sub_u32 s44, s44, 1\n\t"
            "s_cmp_gt_u32 s44, 2\n\t"
            "s_barrier\n\t"
            "s_setprio 0\n\t"
            "s_cbranch_scc1 .Lwa_loop%=\n\t"
            "s_cmp_eq_u32 s44, 0\n\t"
            "s_cbranch_scc1 .Lwa_done%=\n\t"
            "s_cmp_lg_u32 s44, 2\n\t"
            "s_cbranch_scc1 .Lwa_loop%=\n\t"
            "s_mov_b32 m0, s40\n\t"
            "v_lshlrev_b32 v234, 3, v248\n\t"
            "buffer_load_dword v234, s[36:39], s41 offen sc1 lds\n\t"
            "s_branch .Lwa_loop%=\n\t"
            ".Lwa_slow%=:\n\t"
            "s_waitcnt lgkmcnt(0)\n\t"
            "s_nop 7\n\t"
            "v_mfma_f32_16x16x16_bf16 v[180:183], v[152:153], v[154:155], 0\n\t"
            "v_add_u32 v234, s72, v252\n\t"
            "v_cndmask_b32_e64 v235, v246, v234, s[50:51]\n\t"
            "v_cndmask_b32_e64 v234, v246, v234, s[26:27]\n\t"
            "v_mfma_f32_16x16x16_bf16 v[228:231], v[164:165], v[154:155], 0\n\t"
            "ds_read_b64 v[216:217], v235 offset:136\n\t"
            "ds_read_b64 v[218:219], v234 offset:128\n\t"
            "v_mfma_f32_16x16x16_bf16 v[184:187], v[176:177], v[154:155], 0\n\t"
            "ds_read_b128 v[220:223], v234\n\t"
            "ds_read_b128 v[224:227], v235 offset:64\n\t"
            "v_mfma_f32_16x16x32_bf16 v[180:183], v[144:147], v[148:151], v[180:183]\n\t"
            "v_mfma_f32_16x16x32_bf16 v[228:231], v[156:159], v[148:151], v[228:231]\n\t"
            "s_nop 1\n\t"
            "v_mfma_f32_16x16x32_bf16 v[184:187], v[168:171], v[148:151], v[184:187]\n\t"
            "v_mfma_f32_16x16x16_bf16 v[188:191], v[152:153], v[166:167], 0\n\t"
            "v_mfma_f32_16x16x16_bf16 v[192:195], v[176:177], v[166:167], 0\n\t"
            "v_mfma_f32_16x16x16_bf16 v[196:199], v[152:153], v[178:179], 0\n\t"
            "v_mfma_f32_16x16x16_bf16 v[200:203], v[176:177], v[178:179], 0\n\t"
            "s_waitcnt lgkmcnt(2)\n\t"
            "v_mfma_f32_16x16x32_bf16 v[204:207], v[216:219], v[152:155], 0\n\t"
            "s_waitcnt lgkmcnt(1)\n\t"
            "v_mfma_f32_16x16x32_bf16 v[204:207], v[220:223], v[148:151], v[204:207]\n\t"
            "s_waitcnt lgkmcnt(0)\n\t"
            "v_mfma_f32_16x16x32_bf16 v[204:207], v[224:227], v[144:147], v[204:207]\n\t"
            "v_mfma_f32_16x16x32_bf16 v[208:211], v[216:219], v[164:167], 0\n\t"
            "v_mfma_f32_16x16x32_bf16 v[208:211], v[220:223], v[160:163], v[208:211]\n\t"
            "v_mfma_f32_16x16x32_bf16 v[208:211], v[224:227], v[156:159], v[208:211]\n\t"
            "v_mfma_f32_16x16x32_bf16 v[188:191], v[144:147], v[160:163], v[188:191]\n\t"
            "v_mfma_f32_16x16x32_bf16 v[192:195], v[168:171], v[160:163], v[192:195]\n\t"
            "v_mfma_f32_16x16x32_bf16 v[212:215], v[216:219], v[176:179], 0\n\t"
            "s_nop 1\n\t"
            "v_max3_f32 v232, v204, v180, v181\n\t"
            "v_mfma_f32_16x16x32_bf16 v[212:215], v[220:223], v[172:175], v[212:215]\n\t"
            "v_max3_f32 v232, v232, v182, v183\n\t"
            "v_max3_f32 v232, v232, v228, v229\n\t"
            "v_mfma_f32_16x16x32_bf16 v[212:215], v[224:227], v[168:171], v[212:215]\n\t"
            "v_max3_f32 v232, v232, v230, v231\n\t"
            "v_max3_f32 v232, v232, v184, v185\n\t"
            "v_mfma_f32_16x16x32_bf16 v[196:199], v[144:147], v[172:175], v[196:199]\n\t"
            "v_max3_f32 v232, v232, v186, v187\n\t"
            "v_mov_b32 v233, v232\n\t"
            "v_mfma_f32_16x16x32_bf16 v[200:203], v[168:171], v[172:175], v[200:203]\n\t"
            "s_nop 0\n\t"
            "v_permlane32_swap_b32 v232, v233\n\t"
            "v_max_f32 v232, v232, v233\n\t"
            "v_mov_b32 v233, v232\n\t"
            "v_max3_f32 v234, v205, v209, v213\n\t"
            "v_max_f32 v234, v234, v239\n\t"
            "v_permlane16_swap_b32 v232, v233\n\t"
            "v_max_f32 v232, v232, v233\n\t"
            "v_sub_f32 v180, v180, v232\n\t"
            "v_sub_f32 v181, v181, v232\n\t"
            "v_exp_f32 v180, v180\n\t"
            "v_exp_f32 v181, v181\n\t"
            "v_sub_f32 v182, v182, v232\n\t"
            "v_sub_f32 v183, v183, v232\n\t"
            "v_exp_f32 v182, v182\n\t"
            "v_exp_f32 v183, v183\n\t"
            "v_sub_f32 v228, v228, v232\n\t"
            "v_sub_f32 v229, v229, v232\n\t"
            "v_sub_f32 v230, v230, v232\n\t"
            "v_sub_f32 v231, v231, v232\n\t"
            "v_exp_f32 v228, v228\n\t"
            "v_exp_f32 v229, v229\n\t"
            "v_exp_f32 v230, v230\n\t"
            "v_exp_f32 v231, v231\n\t"
            "v_cvt_pk_bf16_f32 v180, v180, v181\n\t"
            "v_cvt_pk_bf16_f32 v181, v182, v183\n\t"
            "v_cvt_pk_bf16_f32 v182, v228, v229\n\t"
            "v_cvt_pk_bf16_f32 v183, v230, v231\n\t"
            "v_mfma_f32_16x16x16_bf16 v[228:231], v[164:165], v[166:167], 0\n\t"
            "v_sub_f32 v184, v184, v232\n\t"
            "v_sub_f32 v185, v185, v232\n\t"
            "v_sub_f32 v186, v186, v232\n\t"
            "v_sub_f32 v187, v187, v232\n\t"
            "v_sub_f32 v204, v204, v232\n\t"
            "v_max3_f32 v232, v208, v188, v189\n\t"
            "v_max3_f32 v232, v232, v190, v191\n\t"
            "v_mfma_f32_16x16x32_bf16 v[228:231], v[156:159], v[160:163], v[228:231]\n\t"
            "v_max_f32_dpp v234, v234, v234 row_ror:8 row_mask:0xf bank_mask:0xf\n\t"
            "v_cndmask_b32_e64 v235, v205, v209, s[60:61]\n\t"
            "v_cndmask_b32_e64 v235, v235, v213, s[62:63]\n\t"
            "v_max_f32_dpp v234, v234, v234 row_ror:4 row_mask:0xf bank_mask:0xf\n\t"
            "v_cndmask_b32_e64 v235, v235, v239, s[64:65]\n\t"
            "v_exp_f32 v184, v184\n\t"
            "v_max_f32_dpp v234, v234, v234 row_ror:2 row_mask:0xf bank_mask:0xf\n\t"
            "v_exp_f32 v185, v185\n\t"
            "v_max3_f32 v232, v232, v228, v229\n\t"
            "v_max3_f32 v232, v232, v230, v231\n\t"
            "v_max3_f32 v232, v232, v192, v193\n\t"
            "v_max3_f32 v232, v232, v194, v195\n\t"
            "v_mov_b32 v233, v232\n\t"
            "v_max_f32_dpp v234, v234, v234 row_ror:1 row_mask:0xf bank_mask:0xf\n\t"
            "v_sub_f32 v235, v235, v234\n\t"
            "v_permlane32_swap_b32 v232, v233\n\t"
            "v_max_f32 v232, v232, v233\n\t"
            "v_mov_b32 v233, v232\n\t"
            "v_exp_f32 v235, v235\n\t"
            "v_exp_f32 v186, v186\n\t"
            "v_permlane16_swap_b32 v232, v233\n\t"
            "v_max_f32 v232, v232, v233\n\t"
            "v_sub_f32 v188, v188, v232\n\t"
            "v_sub_f32 v189, v189, v232\n\t"
            "v_exp_f32 v188, v188\n\t"
            "v_exp_f32 v189, v189\n\t"
            "v_sub_f32 v190, v190, v232\n\t"
            "v_sub_f32 v191, v191, v232\n\t"
            "v_exp_f32 v190, v190\n\t"
            "v_exp_f32 v191, v191\n\t"
            "v_sub_f32 v228, v228, v232\n\t"
            "v_sub_f32 v229, v229, v232\n\t"
            "v_sub_f32 v230, v230, v232\n\t"
            "v_sub_f32 v231, v231, v232\n\t"
            "v_exp_f32 v228, v228\n\t"
            "v_exp_f32 v229, v229\n\t"
            "v_exp_f32 v230, v230\n\t"
            "v_exp_f32 v231, v231\n\t"
            "v_cvt_pk_bf16_f32 v188, v188, v189\n\t"
            "v_cvt_pk_bf16_f32 v189, v190, v191\n\t"
            "v_cvt_pk_bf16_f32 v190, v228, v229\n\t"
            "v_cvt_pk_bf16_f32 v191, v230, v231\n\t"
            "v_mfma_f32_16x16x16_bf16 v[228:231], v[164:165], v[178:179], 0\n\t"
            "v_sub_f32 v192, v192, v232\n\t"
            "v_sub_f32 v193, v193, v232\n\t"
            "v_sub_f32 v194, v194, v232\n\t"
            "v_sub_f32 v195, v195, v232\n\t"
            "v_sub_f32 v208, v208, v232\n\t"
            "v_max3_f32 v232, v212, v196, v197\n\t"
            "v_max3_f32 v232, v232, v198, v199\n\t"
            "v_mfma_f32_16x16x32_bf16 v[228:231], v[156:159], v[172:175], v[228:231]\n\t"
            "v_exp_f32 v192, v192\n\t"
            "v_exp_f32 v193, v193\n\t"
            "v_exp_f32 v187, v187\n\t"
            "v_exp_f32 v204, v204\n\t"
            "v_exp_f32 v194, v194\n\t"
            "v_exp_f32 v195, v195\n\t"
            "v_exp_f32 v208, v208\n\t"
            "v_cvt_pk_bf16_f32 v184, v184, v185\n\t"
            "v_max3_f32 v232, v232, v228, v229\n\t"
            "v_max3_f32 v232, v232, v230, v231\n\t"
            "v_max3_f32 v232, v232, v200, v201\n\t"
            "v_max3_f32 v232, v232, v202, v203\n\t"
            "v_mov_b32 v233, v232\n\t"
            "v_cvt_pk_bf16_f32 v192, v192, v193\n\t"
            "v_cvt_pk_bf16_f32 v235, v235, 0\n\t"
            "v_permlane32_swap_b32 v232, v233\n\t"
            "v_max_f32 v232, v232, v233\n\t"
            "v_mov_b32 v233, v232\n\t"
            "v_cvt_pk_bf16_f32 v185, v186, v187\n\t"
            "v_cvt_pk_bf16_f32 v204, v204, 0\n\t"
            "v_permlane16_swap_b32 v232, v233\n\t"
            "v_max_f32 v232, v232, v233\n\t"
            "v_sub_f32 v196, v196, v232\n\t"
            "v_sub_f32 v197, v197, v232\n\t"
            "v_exp_f32 v196, v196\n\t"
            "v_exp_f32 v197, v197\n\t"
            "v_sub_f32 v198, v198, v232\n\t"
            "v_sub_f32 v199, v199, v232\n\t"
            "v_exp_f32 v198, v198\n\t"
            "v_exp_f32 v199, v199\n\t"
            "v_sub_f32 v228, v228, v232\n\t"
            "v_sub_f32 v229, v229, v232\n\t"
            "v_sub_f32 v230, v230, v232\n\t"
            "v_sub_f32 v231, v231, v232\n\t"
            "v_sub_f32 v200, v200, v232\n\t"
            "v_sub_f32 v201, v201, v232\n\t"
            "v_exp_f32 v228, v228\n\t"
            "v_exp_f32 v229, v229\n\t"
            "v_exp_f32 v230, v230\n\t"
            "v_exp_f32 v231, v231\n\t"
            "v_exp_f32 v200, v200\n\t"
            "v_exp_f32 v201, v201\n\t"
            "v_sub_f32 v202, v202, v232\n\t"
            "v_sub_f32 v203, v203, v232\n\t"
            "v_sub_f32 v212, v212, v232\n\t"
            "v_cvt_pk_bf16_f32 v196, v196, v197\n\t"
            "v_exp_f32 v202, v202\n\t"
            "v_exp_f32 v203, v203\n\t"
            "v_exp_f32 v212, v212\n\t"
            "v_cvt_pk_bf16_f32 v197, v198, v199\n\t"
            "v_cvt_pk_bf16_f32 v198, v228, v229\n\t"
            "v_cvt_pk_bf16_f32 v199, v230, v231\n\t"
            "v_cvt_pk_bf16_f32 v200, v200, v201\n\t"
            "v_cvt_pk_bf16_f32 v193, v194, v195\n\t"
            "v_cvt_pk_bf16_f32 v208, v208, 0\n\t"
            "v_cvt_pk_bf16_f32 v201, v202, v203\n\t"
            "v_cvt_pk_bf16_f32 v212, v212, 0\n\t"
            "v_cndmask_b32_e64 v235, 0, v235, s[66:67]\n\t"
            "v_cndmask_b32_e64 v186, 0, v204, s[58:59]\n\t"
            "v_mfma_f32_16x16x32_bf16 v[204:207], v[240:243], v[180:183], 0\n\t"
            "v_cndmask_b32_e64 v194, 0, v208, s[58:59]\n\t"
            "v_cndmask_b32_e64 v202, 0, v212, s[58:59]\n\t"
            "v_mfma_f32_16x16x32_bf16 v[208:211], v[240:243], v[188:191], 0\n\t"
            "ds_write_b16 v250, v235 offset:256\n\t"
            "v_mfma_f32_16x16x32_bf16 v[212:215], v[240:243], v[196:199], 0\n\t"
            "v_mov_b32 v187, 0\n\t"
            "v_mov_b32 v195, 0\n\t"
            "v_mov_b32 v203, 0\n\t"
            "v_mfma_f32_16x16x32_bf16 v[204:207], v[240:243], v[184:187], v[204:207]\n\t"
            "v_mfma_f32_16x16x32_bf16 v[208:211], v[240:243], v[192:195], v[208:211]\n\t"
            "v_mfma_f32_16x16x32_bf16 v[212:215], v[240:243], v[200:203], v[212:215]\n\t"
            "s_nop 5\n\t"
            "v_rcp_f32 v236, v204\n\t"
            "v_rcp_f32 v237, v208\n\t"
            "v_rcp_f32 v238, v212\n\t"
            "s_waitcnt lgkmcnt(0)\n\t"
            "s_nop 7\n\t"
            "s_branch .Lwa_ok%=\n\t"
            ".Lwa_done%=:\n\t"
            "s_setprio 2\n\t"
            "s_waitcnt lgkmcnt(0)\n\t"
            "s_nop 7\n\t"
            "ds_read_b64 v[156:157], v247\n\t"
            "v_add_u32 v235, s45, v248\n\t"
            "v_xor_b32 v234, s73, v254\n\t"
            "v_mov_b32 v159, 0\n\t"
            "v_mov_b32 v163, 0\n\t"
            "v_mov_b32 v167, 0\n\t"
            "ds_read_b128 v[168:171], v235\n\t"
            "ds_read_u16 v158, v234\n\t"
            "ds_read_b128 v[172:175], v235 offset:1536\n\t"
            "ds_read_b128 v[176:179], v235 offset:3072\n\t"
            "ds_read_b64 v[160:161], v247 offset:3072\n\t"
            "ds_read_u16 v162, v234 offset:256\n\t"
            "ds_read_b64 v[164:165], v247 offset:1536\n\t"
            "ds_read_u16 v166, v234 offset:128\n\t"
            "s_waitcnt lgkmcnt(7)\n\t"
            "v_mfma_f32_16x16x32_bf16 v[228:231], v[168:171], v[180:183], 0\n\t"
            "s_waitcnt lgkmcnt(6)\n\t"
            "v_mfma_f32_16x16x32_bf16 v[228:231], v[156:159], v[184:187], v[228:231]\n\t"
            "s_nop 7\n\t"
            "v_mul_f32 v228, v236, v228\n\t"
            "v_mul_f32 v229, v236, v229\n\t"
            "v_mul_f32 v230, v236, v230\n\t"
            "v_mul_f32 v231, v236, v231\n\t"
            "v_cvt_pk_bf16_f32 v228, v228, v229\n\t"
            "v_cvt_pk_bf16_f32 v229, v230, v231\n\t"
            "ds_write_b64 v245, v[228:229]\n\t"
            "s_waitcnt lgkmcnt(6)\n\t"
            "v_mfma_f32_16x16x32_bf16 v[228:231], v[172:175], v[180:183], 0\n\t"
            "s_waitcnt lgkmcnt(5)\n\t"
            "v_mfma_f32_16x16x32_bf16 v[180:183], v[176:179], v[180:183], 0\n\t"
            "s_waitcnt lgkmcnt(3)\n\t"
            "v_mfma_f32_16x16x32_bf16 v[180:183], v[160:163], v[184:187], v[180:183]\n\t"
            "s_waitcnt lgkmcnt(1)\n\t"
            "v_mfma_f32_16x16x32_bf16 v[228:231], v[164:167], v[184:187], v[228:231]\n\t"
            "v_mfma_f32_16x16x32_bf16 v[184:187], v[168:171], v[188:191], 0\n\t"
            "v_mfma_f32_16x16x32_bf16 v[184:187], v[156:159], v[192:195], v[184:187]\n\t"
            "s_nop 3\n\t"
            "v_mul_f32 v180, v236, v180\n\t"
            "v_mul_f32 v181, v236, v181\n\t"
            "v_mul_f32 v182, v236, v182\n\t"
            "v_mul_f32 v183, v236, v183\n\t"
            "v_cvt_pk_bf16_f32 v180, v180, v181\n\t"
            "v_cvt_pk_bf16_f32 v181, v182, v183\n\t"
            "v_mul_f32 v228, v236, v228\n\t"
            "v_mul_f32 v229, v236, v229\n\t"
            "v_mul_f32 v230, v236, v230\n\t"
            "v_mul_f32 v231, v236, v231\n\t"
            "v_cvt_pk_bf16_f32 v228, v228, v229\n\t"
            "v_cvt_pk_bf16_f32 v229, v230, v231\n\t"
            "v_mul_f32 v184, v237, v184\n\t"
            "v_mul_f32 v185, v237, v185\n\t"
            "v_mul_f32 v186, v237, v186\n\t"
            "v_mul_f32 v187, v237, v187\n\t"
            "v_cvt_pk_bf16_f32 v184, v184, v185\n\t"
            "v_cvt_pk_bf16_f32 v185, v186, v187\n\t"
            "ds_read_b64 v[186:187], v253 offset:352\n\t"
            "ds_write_b64 v245, v[180:181] offset:256\n\t"
            "v_mfma_f32_16x16x32_bf16 v[180:183], v[168:171], v[196:199], 0\n\t"
            "ds_write_b64 v245, v[228:229] offset:128\n\t"
            "v_mfma_f32_16x16x32_bf16 v[180:183], v[156:159], v[200:203], v[180:183]\n\t"
            "ds_write_b64 v245, v[184:185] offset:6144\n\t"
            "v_mfma_f32_16x16x32_bf16 v[228:231], v[172:175], v[188:191], 0\n\t"
            "ds_read_b64 v[184:185], v253 offset:320\n\t"
            "v_mfma_f32_16x16x32_bf16 v[228:231], v[164:167], v[192:195], v[228:231]\n\t"
            "v_mfma_f32_16x16x32_bf16 v[188:191], v[176:179], v[188:191], 0\n\t"
            "v_mfma_f32_16x16x32_bf16 v[188:191], v[160:163], v[192:195], v[188:191]\n\t"
            "s_nop 1\n\t"
            "v_mul_f32 v180, v238, v180\n\t"
            "v_mul_f32 v181, v238, v181\n\t"
            "v_mul_f32 v182, v238, v182\n\t"
            "v_mul_f32 v183, v238, v183\n\t"
            "v_cvt_pk_bf16_f32 v180, v180, v181\n\t"
            "v_cvt_pk_bf16_f32 v181, v182, v183\n\t"
            "v_mul_f32 v228, v237, v228\n\t"
            "v_mul_f32 v229, v237, v229\n\t"
            "v_mul_f32 v230, v237, v230\n\t"
            "v_mul_f32 v231, v237, v231\n\t"
            "v_cvt_pk_bf16_f32 v228, v228, v229\n\t"
            "v_cvt_pk_bf16_f32 v229, v230, v231\n\t"
            "v_mul_f32 v188, v237, v188\n\t"
            "v_mul_f32 v189, v237, v189\n\t"
            "v_mul_f32 v190, v237, v190\n\t"
            "v_mul_f32 v191, v237, v191\n\t"
            "v_cvt_pk_bf16_f32 v188, v188, v189\n\t"
            "v_cvt_pk_bf16_f32 v189, v190, v191\n\t"
            "ds_read_b64 v[182:183], v253 offset:288\n\t"
            "ds_write_b64 v245, v[180:181] offset:12288\n\t"
            "ds_read_b64 v[180:181], v253 offset:256\n\t"
            "ds_write_b64 v245, v[228:229] offset:6272\n\t"
            "v_mfma_f32_16x16x32_bf16 v[228:231], v[172:175], v[196:199], 0\n\t"
            "ds_write_b64 v245, v[188:189] offset:6400\n\t"
            "v_mfma_f32_16x16x32_bf16 v[228:231], v[164:167], v[200:203], v[228:231]\n\t"
            "v_mfma_f32_16x16x32_bf16 v[196:199], v[176:179], v[196:199], 0\n\t"
            "v_mfma_f32_16x16x32_bf16 v[196:199], v[160:163], v[200:203], v[196:199]\n\t"
            "s_nop 5\n\t"
            "v_mul_f32 v228, v238, v228\n\t"
            "s_waitcnt lgkmcnt(2)\n\t"
            "v_mfma_f32_16x16x32_bf16 v[168:171], v[168:171], v[180:183], 0\n\t"
            "v_mfma_f32_16x16x32_bf16 v[172:175], v[172:175], v[180:183], 0\n\t"
            "v_mfma_f32_16x16x32_bf16 v[176:179], v[176:179], v[180:183], 0\n\t"
            "v_mul_f32 v229, v238, v229\n\t"
            "v_mfma_f32_16x16x32_bf16 v[168:171], v[156:159], v[184:187], v[168:171]\n\t"
            "v_mul_f32 v230, v238, v230\n\t"
            "v_mul_f32 v231, v238, v231\n\t"
            "v_mfma_f32_16x16x32_bf16 v[172:175], v[164:167], v[184:187], v[172:175]\n\t"
            "v_cvt_pk_bf16_f32 v228, v228, v229\n\t"
            "v_cvt_pk_bf16_f32 v229, v230, v231\n\t"
            "v_mfma_f32_16x16x32_bf16 v[176:179], v[160:163], v[184:187], v[176:179]\n\t"
            "ds_write_b64 v245, v[228:229] offset:12416\n\t"
            "v_mfma_f32_16x16x32_bf16 v[228:231], v[240:243], v[180:183], 0\n\t"
            "v_mul_f32 v196, v238, v196\n\t"
            "v_mul_f32 v197, v238, v197\n\t"
            "v_mfma_f32_16x16x32_bf16 v[228:231], v[240:243], v[184:187], v[228:231]\n\t"
            "v_mul_f32 v198, v238, v198\n\t"
            "v_mul_f32 v199, v238, v199\n\t"
            "v_cvt_pk_bf16_f32 v196, v196, v197\n\t"
            "v_cvt_pk_bf16_f32 v197, v198, v199\n\t"
            "ds_write_b64 v245, v[196:197] offset:12544\n\t"
            "s_nop 2\n\t"
            "v_rcp_f32 v229, v228\n\t"
            "s_nop 0\n\t"
            "v_mul_f32 v168, v229, v168\n\t"
            "v_mul_f32 v169, v229, v169\n\t"
            "v_mul_f32 v172, v229, v172\n\t"
            "v_mul_f32 v173, v229, v173\n\t"
            "v_mul_f32 v176, v229, v176\n\t"
            "v_mul_f32 v177, v229, v177\n\t"
            "v_mul_f32 v170, v229, v170\n\t"
            "v_mul_f32 v171, v229, v171\n\t"
            "v_cvt_pk_bf16_f32 v168, v168, v169\n\t"
            "v_mul_f32 v174, v229, v174\n\t"
            "v_mul_f32 v175, v229, v175\n\t"
            "v_cvt_pk_bf16_f32 v172, v172, v173\n\t"
            "v_mul_f32 v178, v229, v178\n\t"
            "v_mul_f32 v179, v229, v179\n\t"
            "v_cvt_pk_bf16_f32 v176, v176, v177\n\t"
            "v_xor_b32 v230, s73, v255\n\t"
            "v_cvt_pk_bf16_f32 v169, v170, v171\n\t"
            "v_cvt_pk_bf16_f32 v173, v174, v175\n\t"
            "v_cvt_pk_bf16_f32 v177, v178, v179\n\t"
            "s_mov_b64 exec, s[68:69]\n\t"
            "ds_write_b64 v230, v[168:169]\n\t"
            "ds_write_b64 v230, v[172:173] offset:128\n\t"
            "ds_write_b64 v230, v[176:177] offset:256\n\t"
            "s_mov_b64 exec, -1\n\t"
            "s_waitcnt lgkmcnt(0)\n\t"
            "v_mov_b32 v144, s42\n\t"
            "v_bfrev_b32 v145, -2\n\t"
            "ds_write_b32 v144, v145\n\t"
            "s_waitcnt lgkmcnt(0)\n\t"
            "s_nop 7\n\t"
            : "+{v[232:235]}"(pl), "+{v249}"(aRSw_),
              "+{s44}"(s44), "+{s48}"(s48), "+{s70}"(s70), "+{s72}"(s72), "+{s73}"(s73), "+{s76}"(s76), "+{s77}"(s77)
            : "{v244}"(pE_), "{v245}"(aO_), "{v246}"(aZ_), "{v247}"(aVB_), "{v248}"(pST_), "{v250}"(aPLw_), "{v251}"(aRSr_), "{v252}"(g16_), "{v253}"(aPLr_), "{v254}"(loVL_), "{v255}"(loOL_), "{v[240:243]}"(ones4), "{s47}"((ldsb + X_OFF) ^ (ldsb + X1_OFF)),
              "{s[58:59]}"(0xFFFFull), "{s[60:61]}"(0xFFFF0000ull), "{s[62:63]}"(0xFFFF00000000ull), "{s[64:65]}"(0xFFFF000000000000ull),
              "{s[66:67]}"(0x1FFFFFFFFFFFFull), "{s[68:69]}"(0x0001000100010001ull), "{s[26:27]}"(0x1111111111111111ull), "{s23}"(64.f), "{s57}"(RS_OFF + 768 + (4 + (w >> 1)) * 192 - RS_BASE), "{s45}"(ldsb + V_OFF + w * 4608),
              "{s[28:31]}"(wr), "{s33}"(wave * 36864), "{s46}"(ldsb + PG_OFF), "{s42}"(ldsb + PG_OFF + w * 4), "{s56}"(ldsb + O_OFF), "{s[36:39]}"(tr), "{s41}"(to), "{s40}"(ldsb + X_OFF + 18432), "{s34}"(SH_OFF + bx * 12288 + w * 3072), "{s35}"(ldsb + O_OFF + w * 3072),
              "{s[50:51]}"(0x2222222222222222ull), "{s[52:53]}"(0x4444444444444444ull), "{s[54:55]}"(0x8888888888888888ull)
            : "s24", "s25", "v0", "v1", "v2", "v3", "v4", "v5", "v6", "v7", "v8", "v9", "v10", "v11", "v12", "v13", "v14", "v15", "v16", "v17", "v18", "v19", "v20", "v21", "v22", "v23", "v24", "v25", "v26", "v27", "v28", "v29", "v30", "v31", "v32", "v33", "v34", "v35", "v36", "v37", "v38", "v39", "v40", "v41", "v42", "v43", "v44", "v45", "v46", "v47", "v48", "v49", "v50", "v51", "v52", "v53", "v54", "v55", "v56", "v57", "v58", "v59", "v60", "v61", "v62", "v63", "v64", "v65", "v66", "v67", "v68", "v69", "v70", "v71", "v72", "v73", "v74", "v75", "v76", "v77", "v78", "v79", "v80", "v81", "v82", "v83", "v84", "v85", "v86", "v87", "v88", "v89", "v90", "v91", "v92", "v93", "v94", "v95", "v96", "v97", "v98", "v99", "v100", "v101", "v102", "v103", "v104", "v105", "v106", "v107", "v108", "v109", "v110", "v111", "v112", "v113", "v114", "v115", "v116", "v117", "v118", "v119", "v120", "v121", "v122", "v123", "v124", "v125", "v126", "v127", "v128", "v129", "v130", "v131", "v132", "v133", "v134", "v135", "v136", "v137", "v138", "v139", "v140", "v141", "v142", "v143", "v144", "v145", "v146", "v147", "v148", "v149", "v150", "v151", "v152", "v153", "v154", "v155", "v156", "v157", "v158", "v159", "v160", "v161", "v162", "v163", "v164", "v165", "v166", "v167", "v168", "v169", "v170", "v171", "v172", "v173", "v174", "v175", "v176", "v177", "v178", "v179", "v180", "v181", "v182", "v183", "v184", "v185", "v186", "v187", "v188", "v189", "v190", "v191", "v192", "v193", "v194", "v195", "v196", "v197", "v198", "v199", "v200", "v201", "v202", "v203", "v204", "v205", "v206", "v207", "v208", "v209", "v210", "v211", "v212", "v213", "v214", "v215", "v216", "v217", "v218", "v219", "v220", "v221", "v222", "v223", "v224", "v225", "v226", "v227", "v228", "v229", "v230", "v231", "v236", "v237", "v238", "v239", "s49", "vcc", "scc", "m0", "memory");
        }
        asm volatile("wk_code_end:");

        if (q0 == 40 && wave == 0) {
#pragma unroll 1
            for (int it = 0; it < SPIN_MAX && __hip_atomic_load((gu32*)((char*)wf + CN_OFF), RLX_AGENT) != (unsigned)GRID; ++it) __builtin_amdgcn_s_sleep(8);
            __hip_atomic_store((gu32*)((char*)wf + EP_OFF), __hip_atomic_load((gu32*)((char*)wf + EP_OFF), RLX_AGENT) + 1u, RLX_AGENT);
        }
    } else {
        ROLE_SETUP
        {
            LANE_SETUP
            const bool isV = wave < 6;
            CONV_CONSTS(0)
            const unsigned aCA_ = ldsb + CT_OFF + lane * 16, rsb = ldsb + RS_BASE, pE = fE - X_OFF;
            const unsigned aRSw = (lane < 48 ? rsb : ldsb + RS_OFF + 2304 - 192) + lane * 4, aRSr = rsb + (lane & 48);
            unsigned pr[3];
#pragma unroll
            for (int k = 0; k < 3; ++k) {
                const int c8 = 12 * (w - 2) + 4 * k + g;
                const int ws = c8 / 6, r6 = c8 - 6 * ws;
                pr[k] = l15 * 384 + (r6 >> 1) * 128 + (((2 * ws + (r6 & 1)) ^ sw) << 4);
            }
            unsigned r242 = isV ? pE : ldsb + O_OFF + pE, r243 = isV ? ldsb + V_OFF + w * 9216 + lane * 16 : (ldsb + O_OFF + pE) ^ 64;
            u32x4 r244 = {isV ? ldsb + V_OFF + w * 9216 + 1024 + lane * 8 : pr[0], isV ? aRSr : pr[1], isV ? aRSw : pr[2], (unsigned)lane * 16};
            const int hd = isV ? 2 * w : 2 * w - 3;
            const unsigned vq = ldsb + OL_OFF + l15 * 384 + (((2 * hd + (g >> 1)) ^ sw) << 4) + 8 * (g & 1);
            u32x4 f1 = {16 + l15 < cnt ? vq + 6144 : ldsb + OL_OFF + 22 * 384 + g * 8, pE, ldsb + V_OFF + hd * 4608 + lane * 16, ldsb + V_OFF + hd * 4608 + 1024 + lane * 8};
            u32x4 f2 = {aRSw, aRSr, rsb + l15 * 4, 0u};
            const unsigned lB = ldsb + LB_OFF(l15 & 7) + (g >> 1) * 1536 + (g & 1) * 768 + 96 * w;
            const unsigned long long ob = (unsigned long long)out, xb = (unsigned long long)((const char*)x - XBIAS), wb = (unsigned long long)wf;
            const u32x4 orr = {(unsigned)ob, (unsigned)(ob >> 32) & 0xffffu, (unsigned)(IMG * IMG * CH * 2), 0x00020000u};
            const u32x4 xr = {(unsigned)xb, (unsigned)(xb >> 32) & 0xffffu, (unsigned)(IMG * IMG * CH * 2 + XBIAS), 0x00020000u};
            const u32x4 wr = {(unsigned)wb, (unsigned)(wb >> 32) & 0xffffu, (unsigned)WS_BYTES, 0x00020000u};
            unsigned s45 = OOB, s46 = OOB, s47 = (en0 & 0x7ffffffu) + 96 * w;
            unsigned s48 = ldsb + X1_OFF, s67 = ldsb + X_OFF, s76 = ldsb + WL_OFF + 4, s56 = en0 >> 30;
            unsigned s21 = SH_OFF + bx * 12288, s44 = ldsb + PG_OFF + w * 4, s70 = isV ? wave * 36864 : (wave - 2) * 36864 + 18432;
            unsigned long long xp = 0, sp = 0;
#pragma unroll
            for (int v = 0; v < 4; ++v) {
                const unsigned r = (CROW >> (8 * v)) & 0xFF;
                xp |= (unsigned long long)XPAT(r) << (16 * v);
                sp |= (unsigned long long)SPAT(r) << (16 * v);
            }
#define B_VIN "{v250}"(pXw_), "{v251}"(pSTw_), "{v252}"(aCA_), "{v253}"(aB_), "{v254}"(aA_), "{v255}"(idx_), "{v[238:241]}"(sel4)
#define B_SIN "{s[40:43]}"(orr), "{s[72:75]}"(xr), "{s39}"(0x401fffffu), "{s[28:31]}"(wr), "{s33}"(wave * 36864), "{s78}"(96 * w), \
              "{s27}"(96 * w - 384), "{s[50:51]}"(xp), "{s[52:53]}"(sp), "{s54}"(CLANE), "{s55}"(CROW & 0x0F0F0F0Fu), "{s[34:35]}"(0x0001000100010001ull), \
              "{s[60:61]}"(0x2222222222222222ull), "{s[62:63]}"(0x4444444444444444ull), "{s[64:65]}"(0x8888888888888888ull), \
              "{s77}"(w == 2 ? 768 : 6 * ROWBYTES + 768), "{s25}"(w == 2 ? 3 * ROWBYTES : OOB), "{s32}"(ldsb + X_OFF + 18432), \
              "{s24}"(ldsb + WL_OFF + 12 + 4 * (cnt - 5)), "{s66}"(ldsb + w * 8192), "{s71}"((ldsb + X_OFF) ^ (ldsb + X1_OFF)), \
              "{s23}"(ldsb + WL_OFF), "{s22}"(ldsb + O_OFF), \
              "{s20}"(6 * ROWBYTES + 2304 + 192 * (w & 1)), "{s19}"(2048 + (w & 1) * 1024)
            asm volatile("" :: B_VIN, "{v242}"(r242), "{v243}"(r243), "{v[244:247]}"(r244), "{v[228:231]}"(f1), "{v[232:235]}"(f2), "v"(lB), "v"(vq));
            asm volatile("" :: B_SIN);
            asm volatile("s_waitcnt vmcnt(9)" ::: "memory");
            unsigned lm = *(const unsigned*)(smem + TP_OFF + 4096 + w * 256 + ((l15 & 7) * 4 + g) * 4);
            lm = l15 < 8 ? lm : OOB + 47;
            const int row = lm & 63;
            u32x4 f0 = {XPAT(row) + (unsigned)(O_OFF - X_OFF), SPAT(row) + (unsigned)(STL_OFF - X_OFF - 18816), lB, vq};
            asm volatile(
            "s_setprio 3\n\t"
            "s_waitcnt lgkmcnt(0)\n\t"
            "s_barrier\n\t"
            "s_nop 7\n\t"
            "ds_read_b128 v[168:171], v254\n\t"
            "v_mov_b64 v[176:177], 0\n\t"
            "v_mov_b64 v[178:179], 0\n\t"
            "v_mov_b64 v[180:181], 0\n\t"
            "v_mov_b64 v[182:183], 0\n\t"
            "v_mov_b64 v[184:185], 0\n\t"
            "v_mov_b64 v[186:187], 0\n\t"
            "v_mov_b64 v[188:189], 0\n\t"
            "v_mov_b64 v[190:191], 0\n\t"
            "v_add_u32 v236, s67, v224\n\t"
            "v_xor_b32 v237, 16, v236\n\t"
            "v_add_u32 v248, s67, v225\n\t"
            "ds_read_b64 v[144:145], v226\n\t"
            "ds_read_b64 v[146:147], v226 offset:384\n\t"
            "ds_read_b64 v[148:149], v226 offset:3072\n\t"
            "ds_read_b64 v[150:151], v226 offset:3456\n\t"
            "ds_read_b128 v[172:175], v254 offset:64\n\t"
            "ds_read_b64 v[152:153], v226 offset:8\n\t"
            "ds_read_b64 v[154:155], v226 offset:392\n\t"
            "ds_read_b64 v[156:157], v226 offset:3080\n\t"
            "ds_read_b64 v[158:159], v226 offset:3464\n\t"
            "ds_read_b64 v[160:161], v226 offset:16\n\t"
            "ds_read_b64 v[162:163], v226 offset:400\n\t"
            "ds_read_b64 v[164:165], v226 offset:3088\n\t"
            "ds_read_b64 v[166:167], v226 offset:3472\n\t"
            "s_waitcnt lgkmcnt(9)\n\t"
            "v_smfmac_f32_16x16x64_bf16 v[176:179], v[168:171], v[144:151], v255\n\t"
            "ds_read_b128 v[168:171], v254 offset:128\n\t"
            "ds_read_b64 v[144:145], v226 offset:24\n\t"
            "ds_read_b64 v[146:147], v226 offset:408\n\t"
            "ds_read_b64 v[148:149], v226 offset:3096\n\t"
            "s_waitcnt lgkmcnt(8)\n\t"
            "v_smfmac_f32_16x16x64_bf16 v[180:183], v[172:175], v[152:159], v255\n\t"
            "ds_read_b64 v[150:151], v226 offset:3480\n\t"
            "ds_read_b128 v[172:175], v254 offset:192\n\t"
            "ds_read_b64 v[152:153], v226 offset:32\n\t"
            "v_cvt_pk_bf16_f32 v176, v176, v177\n\t"
            "v_cvt_pk_bf16_f32 v177, v178, v179\n\t"
            "s_nop 1\n\t"
            "v_mfma_f32_4x4x4_16b_bf16 v[196:199], v[176:177], v[176:177], 0\n\t"
            "ds_read_b64 v[154:155], v226 offset:416\n\t"
            "ds_read_b64 v[156:157], v226 offset:3104\n\t"
            "v_cvt_pk_bf16_f32 v178, v180, v181\n\t"
            "v_cvt_pk_bf16_f32 v179, v182, v183\n\t"
            "v_mov_b64 v[180:181], 0\n\t"
            "v_mov_b64 v[182:183], 0\n\t"
            "v_mfma_f32_16x16x32_bf16 v[192:195], v[238:241], v[176:179], 0\n\t"
            "ds_read_b64 v[158:159], v226 offset:3488\n\t"
            "s_waitcnt lgkmcnt(9)\n\t"
            "v_smfmac_f32_16x16x64_bf16 v[184:187], v[168:171], v[160:167], v255\n\t"
            "ds_read_b128 v[168:171], v254 offset:256\n\t"
            "v_mfma_f32_4x4x4_16b_bf16 v[196:199], v[178:179], v[178:179], v[196:199]\n\t"
            "ds_read_b64 v[160:161], v226 offset:40\n\t"
            "ds_read_b64 v[162:163], v226 offset:424\n\t"
            "ds_read_b64 v[164:165], v226 offset:3112\n\t"
            "ds_read_b64 v[166:167], v226 offset:3496\n\t"
            "s_waitcnt lgkmcnt(9)\n\t"
            "v_smfmac_f32_16x16x64_bf16 v[188:191], v[172:175], v[144:151], v255\n\t"
            "ds_read_b128 v[172:175], v254 offset:320\n\t"
            "v_cvt_pk_bf16_f32 v184, v184, v185\n\t"
            "v_cvt_pk_bf16_f32 v185, v186, v187\n\t"
            "s_nop 1\n\t"
            "v_mfma_f32_4x4x4_16b_bf16 v[196:199], v[184:185], v[184:185], v[196:199]\n\t"
            "ds_read_b64 v[144:145], v226 offset:48\n\t"
            "ds_read_b64 v[146:147], v226 offset:432\n\t"
            "ds_read_b64 v[148:149], v226 offset:3120\n\t"
            "ds_read_b64 v[150:151], v226 offset:3504\n\t"
            "v_cvt_pk_bf16_f32 v186, v188, v189\n\t"
            "v_cvt_pk_bf16_f32 v187, v190, v191\n\t"
            "v_mov_b64 v[188:189], 0\n\t"
            "v_mov_b64 v[190:191], 0\n\t"
            "v_mfma_f32_16x16x32_bf16 v[192:195], v[238:241], v[184:187], v[192:195]\n\t"
            "ds_write_b128 v236, v[176:179]\n\t"
            "v_mfma_f32_4x4x4_16b_bf16 v[196:199], v[186:187], v[186:187], v[196:199]\n\t"
            "v_mov_b64 v[176:177], 0\n\t"
            "v_mov_b64 v[178:179], 0\n\t"
            "s_waitcnt lgkmcnt(10)\n\t"
            "s_nop 0\n\t"
            "v_smfmac_f32_16x16x64_bf16 v[176:179], v[168:171], v[152:159], v255\n\t"
            "ds_read_b128 v[168:171], v254 offset:384\n\t"
            "s_waitcnt lgkmcnt(6)\n\t"
            "v_smfmac_f32_16x16x64_bf16 v[180:183], v[172:175], v[160:167], v255\n\t"
            "ds_read_b64 v[152:153], v226 offset:56\n\t"
            "ds_read_b64 v[154:155], v226 offset:440\n\t"
            "ds_read_b64 v[156:157], v226 offset:3128\n\t"
            "ds_read_b64 v[158:159], v226 offset:3512\n\t"
            "ds_read_b128 v[172:175], v254 offset:448\n\t"
            "ds_read_b64 v[160:161], v226 offset:64\n\t"
            "v_cvt_pk_bf16_f32 v176, v176, v177\n\t"
            "v_cvt_pk_bf16_f32 v177, v178, v179\n\t"
            "v_cvt_pk_bf16_f32 v178, v180, v181\n\t"
            "v_cvt_pk_bf16_f32 v179, v182, v183\n\t"
            "v_mfma_f32_4x4x4_16b_bf16 v[196:199], v[176:177], v[176:177], v[196:199]\n\t"
            "ds_read_b64 v[162:163], v226 offset:448\n\t"
            "v_mfma_f32_16x16x32_bf16 v[192:195], v[238:241], v[176:179], v[192:195]\n\t"
            "ds_read_b64 v[164:165], v226 offset:3136\n\t"
            "v_mov_b64 v[180:181], 0\n\t"
            "v_mfma_f32_4x4x4_16b_bf16 v[196:199], v[178:179], v[178:179], v[196:199]\n\t"
            "ds_read_b64 v[166:167], v226 offset:3520\n\t"
            "v_mov_b64 v[182:183], 0\n\t"
            "s_waitcnt lgkmcnt(14)\n\t"
            "ds_write_b128 v237, v[184:187]\n\t"
            "v_mov_b64 v[184:185], 0\n\t"
            "v_mov_b64 v[186:187], 0\n\t"
            "s_waitcnt lgkmcnt(10)\n\t"
            "s_nop 0\n\t"
            "v_smfmac_f32_16x16x64_bf16 v[184:187], v[168:171], v[144:151], v255\n\t"
            "ds_read_b128 v[168:171], v254 offset:512\n\t"
            "s_waitcnt lgkmcnt(6)\n\t"
            "v_smfmac_f32_16x16x64_bf16 v[188:191], v[172:175], v[152:159], v255\n\t"
            "ds_read_b64 v[144:145], v226 offset:72\n\t"
            "ds_read_b64 v[146:147], v226 offset:456\n\t"
            "ds_read_b64 v[148:149], v226 offset:3144\n\t"
            "ds_read_b64 v[150:151], v226 offset:3528\n\t"
            "ds_read_b128 v[172:175], v254 offset:576\n\t"
            "ds_read_b64 v[152:153], v226 offset:80\n\t"
            "v_cvt_pk_bf16_f32 v184, v184, v185\n\t"
            "v_cvt_pk_bf16_f32 v185, v186, v187\n\t"
            "v_cvt_pk_bf16_f32 v186, v188, v189\n\t"
            "v_cvt_pk_bf16_f32 v187, v190, v191\n\t"
            "v_mfma_f32_4x4x4_16b_bf16 v[196:199], v[184:185], v[184:185], v[196:199]\n\t"
            "ds_read_b64 v[154:155], v226 offset:464\n\t"
            "v_mfma_f32_16x16x32_bf16 v[192:195], v[238:241], v[184:187], v[192:195]\n\t"
            "ds_read_b64 v[156:157], v226 offset:3152\n\t"
            "v_mov_b64 v[188:189], 0\n\t"
            "v_mfma_f32_4x4x4_16b_bf16 v[196:199], v[186:187], v[186:187], v[196:199]\n\t"
            "ds_read_b64 v[158:159], v226 offset:3536\n\t"
            "v_mov_b64 v[190:191], 0\n\t"
            "s_waitcnt lgkmcnt(14)\n\t"
            "ds_write_b128 v236, v[176:179] offset:128\n\t"
            "v_mov_b64 v[176:177], 0\n\t"
            "v_mov_b64 v[178:179], 0\n\t"
            "s_waitcnt lgkmcnt(10)\n\t"
            "s_nop 0\n\t"
            "v_smfmac_f32_16x16x64_bf16 v[176:179], v[168:171], v[160:167], v255\n\t"
            "ds_read_b128 v[168:171], v254 offset:640\n\t"
            "s_waitcnt lgkmcnt(6)\n\t"
            "v_smfmac_f32_16x16x64_bf16 v[180:183], v[172:175], v[144:151], v255\n\t"
            "ds_read_b64 v[160:161], v226 offset:88\n\t"
            "ds_read_b64 v[162:163], v226 offset:472\n\t"
            "ds_read_b64 v[164:165], v226 offset:3160\n\t"
            "ds_read_b64 v[166:167], v226 offset:3544\n\t"
            "ds_read_b128 v[172:175], v254 offset:704\n\t"
            "ds_write_b128 v237, v[184:187] offset:128\n\t"
            "v_mov_b64 v[184:185], 0\n\t"
            "v_mov_b64 v[186:187], 0\n\t"
            "v_cvt_pk_bf16_f32 v176, v176, v177\n\t"
            "v_cvt_pk_bf16_f32 v177, v178, v179\n\t"
            "v_cvt_pk_bf16_f32 v178, v180, v181\n\t"
            "v_cvt_pk_bf16_f32 v179, v182, v183\n\t"
            "v_mfma_f32_4x4x4_16b_bf16 v[196:199], v[176:177], v[176:177], v[196:199]\n\t"
            "ds_write_b128 v236, v[176:179] offset:256\n\t"
            "s_waitcnt lgkmcnt(7)\n\t"
            "v_smfmac_f32_16x16x64_bf16 v[184:187], v[168:171], v[152:159], v255\n\t"
            "v_mfma_f32_4x4x4_16b_bf16 v[196:199], v[178:179], v[178:179], v[196:199]\n\t"
            "v_mfma_f32_16x16x32_bf16 v[192:195], v[238:241], v[176:179], v[192:195]\n\t"
            "s_waitcnt lgkmcnt(2)\n\t"
            "v_smfmac_f32_16x16x64_bf16 v[188:191], v[172:175], v[160:167], v255\n\t"
            "s_nop 3\n\t"
            "v_cvt_pk_bf16_f32 v184, v184, v185\n\t"
            "v_cvt_pk_bf16_f32 v185, v186, v187\n\t"
            "s_nop 1\n\t"
            "v_mfma_f32_4x4x4_16b_bf16 v[196:199], v[184:185], v[184:185], v[196:199]\n\t"
            "v_cvt_pk_bf16_f32 v186, v188, v189\n\t"
            "v_cvt_pk_bf16_f32 v187, v190, v191\n\t"
            "s_nop 1\n\t"
            "v_mfma_f32_4x4x4_16b_bf16 v[196:199], v[186:187], v[186:187], v[196:199]\n\t"
            "ds_write_b128 v237, v[184:187] offset:256\n\t"
            "v_mfma_f32_16x16x32_bf16 v[192:195], v[238:241], v[184:187], v[192:195]\n\t"
            "s_nop 7\n\t"
            "v_cndmask_b32_e64 v193, v196, v197, s[60:61]\n\t"
            "v_cndmask_b32_e64 v193, v193, v198, s[62:63]\n\t"
            "v_cndmask_b32_e64 v193, v193, v199, s[64:65]\n\t"
            "ds_write_b64 v248, v[192:193] offset:18816\n\t"
            "s_waitcnt lgkmcnt(0)\n\t"
            "s_nop 7\n\t"
            "v_mov_b32 v144, s44\n\t"
            "v_mov_b32 v145, 0x0fffffff\n\t"
            "ds_write_b32 v144, v145\n\t"
            "s_nop 7\n\t"
            "ds_read_b128 v[168:171], v254\n\t"
            "s_waitcnt vmcnt(0)\n\t"
            "ds_read_b64 v[144:145], v253\n\t"
            "v_mov_b64 v[176:177], 0\n\t"
            "v_mov_b64 v[178:179], 0\n\t"
            "v_mov_b64 v[180:181], 0\n\t"
            "v_mov_b64 v[182:183], 0\n\t"
            "v_mov_b32 v249, s76\n\t"
            "v_mov_b64 v[184:185], 0\n\t"
            "v_mov_b64 v[186:187], 0\n\t"
            "v_mov_b64 v[188:189], 0\n\t"
            "v_mov_b64 v[190:191], 0\n\t"
            "v_add_u32 v236, s67, v250\n\t"
            "v_add_u32 v248, s67, v251\n\t"
            "ds_read_b64 v[146:147], v253 offset:96\n\t"
            "ds_read_b64 v[148:149], v253 offset:1744\n\t"
            "ds_read_b64 v[150:151], v253 offset:1840\n\t"
            "ds_read_b128 v[172:175], v254 offset:64\n\t"
            "ds_read_b64 v[152:153], v253 offset:8\n\t"
            "ds_read_b64 v[154:155], v253 offset:104\n\t"
            "ds_read_b64 v[156:157], v253 offset:1752\n\t"
            "ds_read_b64 v[158:159], v253 offset:1848\n\t"
            "ds_read_b64 v[160:161], v253 offset:16\n\t"
            "ds_read_b64 v[162:163], v253 offset:112\n\t"
            "ds_read_b64 v[164:165], v253 offset:1760\n\t"
            "ds_read_b64 v[166:167], v253 offset:1856\n\t"
            "ds_read_b32 v249, v249\n\t"
            "s_waitcnt lgkmcnt(10)\n\t"
            "v_smfmac_f32_16x16x64_bf16 v[176:179], v[168:171], v[144:151], v255\n\t"
            "ds_read_b128 v[168:171], v254 offset:128\n\t"
            "ds_read_b64 v[144:145], v253 offset:24\n\t"
            "ds_read_b64 v[146:147], v253 offset:120\n\t"
            "ds_read_b64 v[148:149], v253 offset:1768\n\t"
            "s_waitcnt lgkmcnt(9)\n\t"
            "v_smfmac_f32_16x16x64_bf16 v[180:183], v[172:175], v[152:159], v255\n\t"
            "ds_read_b64 v[150:151], v253 offset:1864\n\t"
            "ds_read_b128 v[172:175], v254 offset:192\n\t"
            "ds_read_b64 v[152:153], v253 offset:32\n\t"
            "v_cvt_pk_bf16_f32 v176, v176, v177\n\t"
            "v_cvt_pk_bf16_f32 v177, v178, v179\n\t"
            "s_waitcnt lgkmcnt(7)\n\t"
            "v_readfirstlane_b32 s26, v249\n\t"
            "v_mfma_f32_4x4x4_16b_bf16 v[196:199], v[176:177], v[176:177], 0\n\t"
            "ds_read_b64 v[154:155], v253 offset:128\n\t"
            "s_and_b32 s68, s26, 0x7ffffff\n\t"
            "s_add_u32 s68, s68, s78\n\t"
            "s_and_b32 s69, s26, 0x38000000\n\t"
            "s_or_b32 s69, s69, s39\n\t"
            "s_bitcmp1_b32 s26, 27\n\t"
            "s_cselect_b32 s79, 2.0, s68\n\t"
            "ds_read_b64 v[156:157], v253 offset:1776\n\t"
            "s_and_b32 s57, s56, 3\n\t"
            "s_lshl_b32 s58, s57, 3\n\t"
            "s_lshr_b32 m0, s54, s58\n\t"
            "s_waitcnt lgkmcnt(8)\n\t"
            "v_smfmac_f32_16x16x64_bf16 v[184:187], v[168:171], v[160:167], v255\n\t"
            "s_lshl_b32 s58, s57, 4\n\t"
            "s_lshr_b64 s[36:37], s[50:51], s58\n\t"
            "s_and_b32 s57, s36, 0xffff\n\t"
            "s_add_u32 s57, s57, s67\n\t"
            "ds_read_b64 v[158:159], v253 offset:1872\n\t"
            "s_lshr_b64 s[36:37], s[52:53], s58\n\t"
            "s_and_b32 s58, s36, 0xffff\n\t"
            "s_add_u32 s58, s58, s67\n\t"
            "ds_read_b128 v[168:171], v254 offset:256\n\t"
            "s_and_b32 s59, s50, 0xffff\n\t"
            "s_add_u32 s59, s59, s67\n\t"
            "s_and_b32 s38, s52, 0xffff\n\t"
            "s_add_u32 s38, s38, s67\n\t"
            "s_waitcnt lgkmcnt(5)\n\t"
            "v_smfmac_f32_16x16x64_bf16 v[188:191], v[172:175], v[144:151], v255\n\t"
            "ds_read_b64 v[160:161], v253 offset:40\n\t"
            "v_writelane_b32 v236, s59, m0\n\t"
            "v_writelane_b32 v236, s57, 63\n\t"
            "v_cvt_pk_bf16_f32 v178, v180, v181\n\t"
            "v_cvt_pk_bf16_f32 v179, v182, v183\n\t"
            "v_mov_b64 v[180:181], 0\n\t"
            "v_mov_b64 v[182:183], 0\n\t"
            "v_mfma_f32_16x16x32_bf16 v[192:195], v[238:241], v[176:179], 0\n\t"
            "ds_read_b64 v[162:163], v253 offset:136\n\t"
            "v_cvt_pk_bf16_f32 v184, v184, v185\n\t"
            "v_mfma_f32_4x4x4_16b_bf16 v[196:199], v[178:179], v[178:179], v[196:199]\n\t"
            "ds_read_b64 v[164:165], v253 offset:1784\n\t"
            "v_cvt_pk_bf16_f32 v185, v186, v187\n\t"
            "v_xor_b32 v237, 16, v236\n\t"
            "v_cvt_pk_bf16_f32 v186, v188, v189\n\t"
            "v_mfma_f32_4x4x4_16b_bf16 v[196:199], v[184:185], v[184:185], v[196:199]\n\t"
            "ds_read_b64 v[166:167], v253 offset:1880\n\t"
            "v_cvt_pk_bf16_f32 v187, v190, v191\n\t"
            "v_mov_b64 v[188:189], 0\n\t"
            "v_mov_b64 v[190:191], 0\n\t"
            "v_mfma_f32_16x16x32_bf16 v[192:195], v[238:241], v[184:187], v[192:195]\n\t"
            "ds_read_b128 v[172:175], v254 offset:320\n\t"
            "v_writelane_b32 v248, s38, m0\n\t"
            "v_mfma_f32_4x4x4_16b_bf16 v[196:199], v[186:187], v[186:187], v[196:199]\n\t"
            "ds_read_b64 v[144:145], v253 offset:48\n\t"
            "v_writelane_b32 v248, s58, 63\n\t"
            "ds_read_b64 v[146:147], v253 offset:144\n\t"
            "ds_read_b64 v[148:149], v253 offset:1792\n\t"
            "ds_read_b64 v[150:151], v253 offset:1888\n\t"
            "ds_write_b128 v236, v[176:179]\n\t"
            "v_mov_b64 v[176:177], 0\n\t"
            "v_mov_b64 v[178:179], 0\n\t"
            "s_waitcnt lgkmcnt(10)\n\t"
            "s_nop 0\n\t"
            "v_smfmac_f32_16x16x64_bf16 v[176:179], v[168:171], v[152:159], v255\n\t"
            "ds_read_b128 v[168:171], v254 offset:384\n\t"
            "ds_read_b64 v[152:153], v253 offset:56\n\t"
            "ds_read_b64 v[154:155], v253 offset:152\n\t"
            "s_waitcnt lgkmcnt(8)\n\t"
            "v_smfmac_f32_16x16x64_bf16 v[180:183], v[172:175], v[160:167], v255\n\t"
            "ds_read_b64 v[156:157], v253 offset:1800\n\t"
            "ds_read_b64 v[158:159], v253 offset:1896\n\t"
            "ds_read_b128 v[172:175], v254 offset:448\n\t"
            "ds_read_b64 v[160:161], v253 offset:64\n\t"
            "v_cvt_pk_bf16_f32 v176, v176, v177\n\t"
            "v_cvt_pk_bf16_f32 v177, v178, v179\n\t"
            "s_nop 1\n\t"
            "v_mfma_f32_4x4x4_16b_bf16 v[196:199], v[176:177], v[176:177], v[196:199]\n\t"
            "ds_read_b64 v[162:163], v253 offset:160\n\t"
            "v_cvt_pk_bf16_f32 v178, v180, v181\n\t"
            "v_cvt_pk_bf16_f32 v179, v182, v183\n\t"
            "v_mov_b64 v[180:181], 0\n\t"
            "v_mov_b64 v[182:183], 0\n\t"
            "v_mfma_f32_16x16x32_bf16 v[192:195], v[238:241], v[176:179], v[192:195]\n\t"
            "ds_read_b64 v[164:165], v253 offset:1808\n\t"
            "v_mfma_f32_4x4x4_16b_bf16 v[196:199], v[178:179], v[178:179], v[196:199]\n\t"
            "ds_read_b64 v[166:167], v253 offset:1904\n\t"
            "s_waitcnt lgkmcnt(14)\n\t"
            "ds_write_b128 v237, v[184:187]\n\t"
            "v_mov_b64 v[184:185], 0\n\t"
            "v_mov_b64 v[186:187], 0\n\t"
            "s_waitcnt lgkmcnt(10)\n\t"
            "s_nop 0\n\t"
            "v_smfmac_f32_16x16x64_bf16 v[184:187], v[168:171], v[144:151], v255\n\t"
            "ds_read_b128 v[168:171], v254 offset:512\n\t"
            "s_waitcnt lgkmcnt(6)\n\t"
            "v_smfmac_f32_16x16x64_bf16 v[188:191], v[172:175], v[152:159], v255\n\t"
            "ds_read_b64 v[144:145], v253 offset:72\n\t"
            "ds_read_b64 v[146:147], v253 offset:168\n\t"
            "ds_read_b64 v[148:149], v253 offset:1816\n\t"
            "ds_read_b64 v[150:151], v253 offset:1912\n\t"
            "ds_read_b128 v[172:175], v254 offset:576\n\t"
            "ds_read_b64 v[152:153], v253 offset:80\n\t"
            "v_cvt_pk_bf16_f32 v184, v184, v185\n\t"
            "v_cvt_pk_bf16_f32 v185, v186, v187\n\t"
            "v_cvt_pk_bf16_f32 v186, v188, v189\n\t"
            "v_cvt_pk_bf16_f32 v187, v190, v191\n\t"
            "v_mfma_f32_4x4x4_16b_bf16 v[196:199], v[184:185], v[184:185], v[196:199]\n\t"
            "ds_read_b64 v[154:155], v253 offset:176\n\t"
            "v_mfma_f32_16x16x32_bf16 v[192:195], v[238:241], v[184:187], v[192:195]\n\t"
            "ds_read_b64 v[156:157], v253 offset:1824\n\t"
            "v_mov_b64 v[188:189], 0\n\t"
            "v_mfma_f32_4x4x4_16b_bf16 v[196:199], v[186:187], v[186:187], v[196:199]\n\t"
            "ds_read_b64 v[158:159], v253 offset:1920\n\t"
            "v_mov_b64 v[190:191], 0\n\t"
            "s_waitcnt lgkmcnt(14)\n\t"
            "ds_write_b128 v236, v[176:179] offset:128\n\t"
            "v_mov_b64 v[176:177], 0\n\t"
            "v_mov_b64 v[178:179], 0\n\t"
            "s_waitcnt lgkmcnt(10)\n\t"
            "s_nop 0\n\t"
            "v_smfmac_f32_16x16x64_bf16 v[176:179], v[168:171], v[160:167], v255\n\t"
            "ds_read_b128 v[168:171], v254 offset:640\n\t"
            "s_waitcnt lgkmcnt(6)\n\t"
            "v_smfmac_f32_16x16x64_bf16 v[180:183], v[172:175], v[144:151], v255\n\t"
            "ds_read_b64 v[160:161], v253 offset:88\n\t"
            "ds_read_b64 v[162:163], v253 offset:184\n\t"
            "ds_read_b64 v[164:165], v253 offset:1832\n\t"
            "ds_read_b64 v[166:167], v253 offset:1928\n\t"
            "ds_read_b128 v[172:175], v254 offset:704\n\t"
            "ds_write_b128 v237, v[184:187] offset:128\n\t"
            "v_mov_b64 v[184:185], 0\n\t"
            "v_mov_b64 v[186:187], 0\n\t"
            "v_cvt_pk_bf16_f32 v176, v176, v177\n\t"
            "v_cvt_pk_bf16_f32 v177, v178, v179\n\t"
            "v_cvt_pk_bf16_f32 v178, v180, v181\n\t"
            "v_cvt_pk_bf16_f32 v179, v182, v183\n\t"
            "v_mfma_f32_4x4x4_16b_bf16 v[196:199], v[176:177], v[176:177], v[196:199]\n\t"
            "ds_write_b128 v236, v[176:179] offset:256\n\t"
            "s_waitcnt lgkmcnt(7)\n\t"
            "v_smfmac_f32_16x16x64_bf16 v[184:187], v[168:171], v[152:159], v255\n\t"
            "v_mfma_f32_4x4x4_16b_bf16 v[196:199], v[178:179], v[178:179], v[196:199]\n\t"
            "v_mfma_f32_16x16x32_bf16 v[192:195], v[238:241], v[176:179], v[192:195]\n\t"
            "s_waitcnt lgkmcnt(2)\n\t"
            "v_smfmac_f32_16x16x64_bf16 v[188:191], v[172:175], v[160:167], v255\n\t"
            "s_nop 3\n\t"
            "v_cvt_pk_bf16_f32 v184, v184, v185\n\t"
            "v_cvt_pk_bf16_f32 v185, v186, v187\n\t"
            "s_nop 1\n\t"
            "v_mfma_f32_4x4x4_16b_bf16 v[196:199], v[184:185], v[184:185], v[196:199]\n\t"
            "v_cvt_pk_bf16_f32 v186, v188, v189\n\t"
            "v_cvt_pk_bf16_f32 v187, v190, v191\n\t"
            "s_nop 1\n\t"
            "v_mfma_f32_4x4x4_16b_bf16 v[196:199], v[186:187], v[186:187], v[196:199]\n\t"
            "ds_write_b128 v237, v[184:187] offset:256\n\t"
            "v_mfma_f32_16x16x32_bf16 v[192:195], v[238:241], v[184:187], v[192:195]\n\t"
            "s_nop 7\n\t"
            "v_cndmask_b32_e64 v193, v196, v197, s[60:61]\n\t"
            "v_cndmask_b32_e64 v193, v193, v198, s[62:63]\n\t"
            "v_cndmask_b32_e64 v193, v193, v199, s[64:65]\n\t"
            "ds_write_b64 v248, v[192:193] offset:18816\n\t"
            "s_waitcnt lgkmcnt(0)\n\t"
            "s_nop 7\n\t"
            "v_mov_b32 v144, s44\n\t"
            "v_mov_b32 v145, 0xbfffffff\n\t"
            "ds_write_b32 v144, v145\n\t"
            "s_setprio 1\n\t"
            "v_mov_b32 v144, s23\n\t"
            ".Lgo%=:\n\t"
            "ds_read_b32 v145, v144 offset:124\n\t"
            "s_waitcnt lgkmcnt(0)\n\t"
            "v_cmp_ne_u32 vcc, -1, v145\n\t"
            "s_nop 4\n\t"
            "s_cbranch_vccnz .Lgo%=\n\t"
            "s_nop 7\n\t"
            "ds_read_b32 v249, v252 offset:1024\n\t"
            "s_add_u32 s49, s70, 0\n\t"
            "buffer_load_dwordx4 v[0:3], v247, s[28:31], s49 offen sc1\n\t"
            "s_add_u32 s49, s70, 1024\n\t"
            "buffer_load_dwordx4 v[4:7], v247, s[28:31], s49 offen sc1\n\t"
            "s_add_u32 s49, s70, 2048\n\t"
            "buffer_load_dwordx4 v[8:11], v247, s[28:31], s49 offen sc1\n\t"
            "s_add_u32 s49, s70, 3072\n\t"
            "buffer_load_dwordx4 v[12:15], v247, s[28:31], s49 offen sc1\n\t"
            "s_add_u32 s49, s70, 4096\n\t"
            "buffer_load_dwordx4 v[16:19], v247, s[28:31], s49 offen sc1\n\t"
            "s_add_u32 s49, s70, 5120\n\t"
            "buffer_load_dwordx4 v[20:23], v247, s[28:31], s49 offen sc1\n\t"
            "s_add_u32 s49, s70, 6144\n\t"
            "buffer_load_dwordx4 v[24:27], v247, s[28:31], s49 offen sc1\n\t"
            "s_add_u32 s49, s70, 7168\n\t"
            "buffer_load_dwordx4 v[28:31], v247, s[28:31], s49 offen sc1\n\t"
            "s_add_u32 s49, s70, 8192\n\t"
            "buffer_load_dwordx4 v[32:35], v247, s[28:31], s49 offen sc1\n\t"
            "s_add_u32 s49, s70, 9216\n\t"
            "buffer_load_dwordx4 v[36:39], v247, s[28:31], s49 offen sc1\n\t"
            "s_add_u32 s49, s70, 10240\n\t"
            "buffer_load_dwordx4 v[40:43], v247, s[28:31], s49 offen sc1\n\t"
            "s_add_u32 s49, s70, 11264\n\t"
            "buffer_load_dwordx4 v[44:47], v247, s[28:31], s49 offen sc1\n\t"
            "s_add_u32 s49, s70, 12288\n\t"
            "buffer_load_dwordx4 v[48:51], v247, s[28:31], s49 offen sc1\n\t"
            "s_add_u32 s49, s70, 13312\n\t"
            "buffer_load_dwordx4 v[52:55], v247, s[28:31], s49 offen sc1\n\t"
            "s_add_u32 s49, s70, 14336\n\t"
            "buffer_load_dwordx4 v[56:59], v247, s[28:31], s49 offen sc1\n\t"
            "s_add_u32 s49, s70, 15360\n\t"
            "buffer_load_dwordx4 v[60:63], v247, s[28:31], s49 offen sc1\n\t"
            "s_add_u32 s49, s70, 16384\n\t"
            "buffer_load_dwordx4 v[64:67], v247, s[28:31], s49 offen sc1\n\t"
            "s_add_u32 s49, s70, 17408\n\t"
            "buffer_load_dwordx4 v[68:71], v247, s[28:31], s49 offen sc1\n\t"
            "s_waitcnt lgkmcnt(0)\n\t"
            "v_and_b32 v249, s69, v249\n\t"
            "s_mov_b32 exec_hi, 0x3fffff\n\t"
            "s_add_u32 m0, s66, 0\n\t"
            "s_add_u32 s49, s79, 0\n\t"
            "buffer_load_dwordx4 v249, s[72:75], s49 offen lds\n\t"
            "s_mov_b32 exec_hi, -1\n\t"
            "s_mov_b32 exec_hi, 0x3fffff\n\t"
            "s_add_u32 m0, s66, 872\n\t"
            "s_add_u32 s49, s68, 196224\n\t"
            "buffer_load_dwordx4 v249, s[72:75], s49 offen lds\n\t"
            "s_mov_b32 exec_hi, -1\n\t"
            "s_mov_b32 exec_hi, 0x3fffff\n\t"
            "s_add_u32 m0, s66, 1744\n\t"
            "s_add_u32 s49, s68, 392448\n\t"
            "buffer_load_dwordx4 v249, s[72:75], s49 offen lds\n\t"
            "s_mov_b32 exec_hi, -1\n\t"
            "s_mov_b32 exec_hi, 0x3fffff\n\t"
            "s_add_u32 m0, s66, 2616\n\t"
            "s_add_u32 s49, s68, 588672\n\t"
            "buffer_load_dwordx4 v249, s[72:75], s49 offen lds\n\t"
            "s_mov_b32 exec_hi, -1\n\t"
            "s_mov_b32 exec_hi, 0x3fffff\n\t"
            "s_add_u32 m0, s66, 3488\n\t"
            "s_add_u32 s49, s68, 784896\n\t"
            "buffer_load_dwordx4 v249, s[72:75], s49 offen lds\n\t"
            "s_mov_b32 exec_hi, -1\n\t"
            "s_mov_b32 exec_hi, 0x3fffff\n\t"
            "s_add_u32 m0, s66, 4360\n\t"
            "s_add_u32 s49, s68, 981120\n\t"
            "buffer_load_dwordx4 v249, s[72:75], s49 offen lds\n\t"
            "s_mov_b32 exec_hi, -1\n\t"
            "s_mov_b32 exec_hi, 0x3fffff\n\t"
            "s_add_u32 m0, s66, 5232\n\t"
            "s_add_u32 s49, s68, 1177344\n\t"
            "buffer_load_dwordx4 v249, s[72:75], s49 offen lds\n\t"
            "s_mov_b32 exec_hi, -1\n\t"
            "s_mov_b32 exec_hi, 0x3fffff\n\t"
            "s_add_u32 m0, s66, 6104\n\t"
            "s_add_u32 s49, s68, 1373568\n\t"
            "buffer_load_dwordx4 v249, s[72:75], s49 offen lds\n\t"
            "s_mov_b32 exec_hi, -1\n\t"
            "s_mov_b32 exec_hi, 0x3fffff\n\t"
            "s_add_u32 m0, s66, 6976\n\t"
            "s_add_u32 s49, s68, 1569792\n\t"
            "buffer_load_dwordx4 v249, s[72:75], s49 offen lds\n\t"
            "s_mov_b32 exec_hi, -1\n\t"
            "s_add_u32 s45, s46, s27\n\t"
            "s_mov_b32 s46, s47\n\t"
            "s_mov_b32 s47, s68\n\t"
            "s_xor_b32 s48, s48, s71\n\t"
            "s_xor_b32 s67, s67, s71\n\t"
            "s_add_u32 s76, s76, 4\n\t"
            "s_lshl_b32 s56, s56, 2\n\t"
            "s_lshr_b32 s49, s26, 30\n\t"
            "s_or_b32 s56, s56, s49\n\t"
            "s_cmp_ge_u32 s76, s24\n\t"
            "s_cselect_b64 vcc, -1, 0\n\t"
            "s_nop 7\n\t"
            "v_mov_b32 v144, s23\n\t"
            ".Lpg%=:\n\t"
            "ds_read_b128 v[148:151], v144 offset:96\n\t"
            "s_waitcnt lgkmcnt(0)\n\t"
            "v_and_b32 v148, v148, v149\n\t"
            "v_and_b32 v150, v150, v151\n\t"
            "v_and_b32 v148, v148, v150\n\t"
            "v_cmp_le_i32 vcc, 0, v148\n\t"
            "s_nop 4\n\t"
            "s_cbranch_vccnz .Lpg%=\n\t"
            "s_setprio 3\n\t"
            "s_nop 7\n\t"
            "v_add_u32 v196, s22, v229\n\t"
            "v_xor_b32 v197, 64, v196\n\t"
            "v_add_u32 v198, s22, v247\n\t"
            "v_mov_b32 v249, s76\n\t"
            "v_add_u32 v236, s67, v250\n\t"
            "v_add_u32 v248, s67, v251\n\t"
            "ds_read_b128 v[144:147], v196\n\t"
            "ds_read_b128 v[148:151], v197\n\t"
            "ds_read_b128 v[152:155], v196 offset:128\n\t"
            "ds_read_b128 v[156:159], v197 offset:128\n\t"
            "ds_read_b128 v[160:163], v196 offset:256\n\t"
            "ds_read_b128 v[164:167], v197 offset:256\n\t"
            "ds_read_b128 v[216:219], v198 offset:12288\n\t"
            "ds_read_b128 v[220:223], v198 offset:13312\n\t"
            "v_add_u32 v198, s48, v247\n\t"
            "ds_read_b128 v[168:171], v196 offset:6144\n\t"
            "ds_read_b128 v[172:175], v197 offset:6144\n\t"
            "ds_read_b128 v[176:179], v196 offset:6272\n\t"
            "ds_read_b128 v[180:183], v197 offset:6272\n\t"
            "ds_read_b128 v[184:187], v196 offset:6400\n\t"
            "v_add_u32 v196, s48, v229\n\t"
            "ds_read_b128 v[188:191], v197 offset:6400\n\t"
            "s_waitcnt vmcnt(9)\n\t"
            "s_waitcnt lgkmcnt(13)\n\t"
            "v_mfma_f32_16x16x32_bf16 v[200:203], v[0:3], v[144:147], 0\n\t"
            "ds_read_b32 v249, v249\n\t"
            "v_xor_b32 v197, 64, v196\n\t"
            "s_waitcnt lgkmcnt(13)\n\t"
            "v_mfma_f32_16x16x32_bf16 v[200:203], v[4:7], v[148:151], v[200:203]\n\t"
            "s_add_u32 s49, s33, 18432\n\t"
            "buffer_load_dwordx4 v[72:75], v247, s[28:31], s49 offen sc1\n\t"
            "s_waitcnt lgkmcnt(12)\n\t"
            "v_mfma_f32_16x16x32_bf16 v[200:203], v[8:11], v[152:155], v[200:203]\n\t"
            "s_waitcnt lgkmcnt(8)\n\t"
            "v_add_f32 v216, v216, v218\n\t"
            "s_waitcnt lgkmcnt(7)\n\t"
            "v_add_f32 v220, v220, v222\n\t"
            "v_mfma_f32_16x16x32_bf16 v[200:203], v[12:15], v[156:159], v[200:203]\n\t"
            "v_add_f32 v216, v216, v220\n\t"
            "v_add_f32 v217, v217, v219\n\t"
            "v_mfma_f32_16x16x32_bf16 v[200:203], v[16:19], v[160:163], v[200:203]\n\t"
            "v_add_f32 v221, v221, v223\n\t"
            "v_mul_f32 v216, 0x3baaaaab, v216\n\t"
            "v_mfma_f32_16x16x32_bf16 v[200:203], v[20:23], v[164:167], v[200:203]\n\t"
            "v_add_f32 v217, v217, v221\n\t"
            "v_mul_f32_e64 v216, v216, -v216\n\t"
            "v_mfma_f32_16x16x32_bf16 v[204:207], v[24:27], v[144:147], 0\n\t"
            "v_fmac_f32 v216, 0x3baaaaab, v217\n\t"
            "v_max_f32 v216, 0, v216\n\t"
            "v_mfma_f32_16x16x32_bf16 v[204:207], v[28:31], v[148:151], v[204:207]\n\t"
            "v_add_f32 v216, 0x3727c5ac, v216\n\t"
            "v_rsq_f32 v216, v216\n\t"
            "v_mfma_f32_16x16x32_bf16 v[204:207], v[32:35], v[152:155], v[204:207]\n\t"
            "ds_write_b32 v232, v216\n\t"
            "v_mfma_f32_16x16x32_bf16 v[204:207], v[36:39], v[156:159], v[204:207]\n\t"
            "ds_read_b32 v192, v234\n\t"
            "s_waitcnt lgkmcnt(2)\n\t"
            "v_readfirstlane_b32 s26, v249\n\t"
            "v_mfma_f32_16x16x32_bf16 v[204:207], v[40:43], v[160:163], v[204:207]\n\t"
            "ds_read_b32 v193, v234 offset:64\n\t"
            "v_mfma_f32_16x16x32_bf16 v[204:207], v[44:47], v[164:167], v[204:207]\n\t"
            "ds_read_b32 v249, v252 offset:1024\n\t"
            "v_mfma_f32_16x16x32_bf16 v[208:211], v[48:51], v[144:147], 0\n\t"
            "ds_read_b128 v[144:147], v196\n\t"
            "v_mfma_f32_16x16x32_bf16 v[208:211], v[52:55], v[148:151], v[208:211]\n\t"
            "ds_read_b128 v[148:151], v197\n\t"
            "v_mfma_f32_16x16x32_bf16 v[208:211], v[56:59], v[152:155], v[208:211]\n\t"
            "ds_read_b128 v[152:155], v196 offset:128\n\t"
            "v_mfma_f32_16x16x32_bf16 v[208:211], v[60:63], v[156:159], v[208:211]\n\t"
            "ds_read_b128 v[156:159], v197 offset:128\n\t"
            "v_mfma_f32_16x16x32_bf16 v[208:211], v[64:67], v[160:163], v[208:211]\n\t"
            "ds_read_b128 v[160:163], v196 offset:256\n\t"
            "v_mfma_f32_16x16x32_bf16 v[208:211], v[68:71], v[164:167], v[208:211]\n\t"
            "ds_read_b128 v[164:167], v197 offset:256\n\t"
            "s_waitcnt lgkmcnt(8)\n\t"
            "v_mul_f32 v200, v192, v200\n\t"
            "v_mfma_f32_16x16x32_bf16 v[212:215], v[0:3], v[168:171], 0\n\t"
            "v_mul_f32 v201, v192, v201\n\t"
            "v_mul_f32 v204, v192, v204\n\t"
            "v_mfma_f32_16x16x32_bf16 v[212:215], v[4:7], v[172:175], v[212:215]\n\t"
            "v_mul_f32 v205, v192, v205\n\t"
            "v_mul_f32 v202, v192, v202\n\t"
            "v_mfma_f32_16x16x32_bf16 v[212:215], v[8:11], v[176:179], v[212:215]\n\t"
            "v_mul_f32 v203, v192, v203\n\t"
            "v_cvt_pk_bf16_f32 v200, v200, v201\n\t"
            "v_mfma_f32_16x16x32_bf16 v[212:215], v[12:15], v[180:183], v[212:215]\n\t"
            "v_mul_f32 v206, v192, v206\n\t"
            "v_mul_f32 v207, v192, v207\n\t"
            "v_mfma_f32_16x16x32_bf16 v[212:215], v[16:19], v[184:187], v[212:215]\n\t"
            "v_cvt_pk_bf16_f32 v204, v204, v205\n\t"
            "v_cvt_pk_bf16_f32 v201, v202, v203\n\t"
            "v_mfma_f32_16x16x32_bf16 v[212:215], v[20:23], v[188:191], v[212:215]\n\t"
            "v_cvt_pk_bf16_f32 v205, v206, v207\n\t"
            "v_mul_f32 v208, v192, v208\n\t"
            "v_mfma_f32_16x16x32_bf16 v[216:219], v[24:27], v[168:171], 0\n\t"
            "ds_write_b64 v227, v[200:201]\n\t"
            "v_mfma_f32_16x16x32_bf16 v[216:219], v[28:31], v[172:175], v[216:219]\n\t"
            "ds_read_b128 v[200:203], v198 offset:18816\n\t"
            "v_mul_f32 v209, v192, v209\n\t"
            "v_mfma_f32_16x16x32_bf16 v[216:219], v[32:35], v[176:179], v[216:219]\n\t"
            "ds_write_b64 v227, v[204:205] offset:128\n\t"
            "v_mfma_f32_16x16x32_bf16 v[216:219], v[36:39], v[180:183], v[216:219]\n\t"
            "ds_read_b128 v[204:207], v198 offset:19840\n\t"
            "v_mul_f32 v210, v192, v210\n\t"
            "v_mfma_f32_16x16x32_bf16 v[216:219], v[40:43], v[184:187], v[216:219]\n\t"
            "s_add_u32 s49, s33, 19456\n\t"
            "buffer_load_dwordx4 v[76:79], v247, s[28:31], s49 offen sc1\n\t"
            "v_mfma_f32_16x16x32_bf16 v[216:219], v[44:47], v[188:191], v[216:219]\n\t"
            "s_add_u32 s49, s33, 20480\n\t"
            "buffer_load_dwordx4 v[80:83], v247, s[28:31], s49 offen sc1\n\t"
            "v_mfma_f32_16x16x32_bf16 v[220:223], v[48:51], v[168:171], 0\n\t"
            "ds_read_b128 v[168:171], v196 offset:6144\n\t"
            "s_waitcnt lgkmcnt(3)\n\t"
            "v_add_f32 v200, v200, v202\n\t"
            "v_mfma_f32_16x16x32_bf16 v[220:223], v[52:55], v[172:175], v[220:223]\n\t"
            "ds_read_b128 v[172:175], v197 offset:6144\n\t"
            "s_waitcnt lgkmcnt(2)\n\t"
            "v_add_f32 v204, v204, v206\n\t"
            "v_mfma_f32_16x16x32_bf16 v[220:223], v[56:59], v[176:179], v[220:223]\n\t"
            "ds_read_b128 v[176:179], v196 offset:6272\n\t"
            "v_add_f32 v200, v200, v204\n\t"
            "v_mfma_f32_16x16x32_bf16 v[220:223], v[60:63], v[180:183], v[220:223]\n\t"
            "ds_read_b128 v[180:183], v197 offset:6272\n\t"
            "v_add_f32 v201, v201, v203\n\t"
            "v_mfma_f32_16x16x32_bf16 v[220:223], v[64:67], v[184:187], v[220:223]\n\t"
            "ds_read_b128 v[184:187], v196 offset:6400\n\t"
            "v_add_f32 v205, v205, v207\n\t"
            "v_mfma_f32_16x16x32_bf16 v[220:223], v[68:71], v[188:191], v[220:223]\n\t"
            "ds_read_b128 v[188:191], v197 offset:6400\n\t"
            "v_mul_f32 v200, 0x3baaaaab, v200\n\t"
            "v_add_f32 v201, v201, v205\n\t"
            "v_mul_f32_e64 v200, v200, -v200\n\t"
            "v_fmac_f32 v200, 0x3baaaaab, v201\n\t"
            "v_max_f32 v200, 0, v200\n\t"
            "v_add_f32 v200, 0x3727c5ac, v200\n\t"
            "v_rsq_f32 v200, v200\n\t"
            "v_mul_f32 v211, v192, v211\n\t"
            "v_cvt_pk_bf16_f32 v208, v208, v209\n\t"
            "v_cvt_pk_bf16_f32 v209, v210, v211\n\t"
            "v_mul_f32 v212, v193, v212\n\t"
            "v_mul_f32 v213, v193, v213\n\t"
            "v_mul_f32 v214, v193, v214\n\t"
            "v_mul_f32 v215, v193, v215\n\t"
            "v_cvt_pk_bf16_f32 v212, v212, v213\n\t"
            "v_cvt_pk_bf16_f32 v213, v214, v215\n\t"
            "v_mul_f32 v216, v193, v216\n\t"
            "v_mul_f32 v217, v193, v217\n\t"
            "v_mul_f32 v218, v193, v218\n\t"
            "v_mul_f32 v219, v193, v219\n\t"
            "v_mul_f32 v220, v193, v220\n\t"
            "v_mul_f32 v221, v193, v221\n\t"
            "v_mul_f32 v222, v193, v222\n\t"
            "v_mul_f32 v223, v193, v223\n\t"
            "v_cvt_pk_bf16_f32 v216, v216, v217\n\t"
            "v_cvt_pk_bf16_f32 v217, v218, v219\n\t"
            "v_cvt_pk_bf16_f32 v220, v220, v221\n\t"
            "v_cvt_pk_bf16_f32 v221, v222, v223\n\t"
            "ds_write_b32 v232, v200\n\t"
            "v_mfma_f32_16x16x32_bf16 v[200:203], v[144:147], v[0:3], 0\n\t"
            "ds_read_b128 v[192:195], v233\n\t"
            "v_mfma_f32_16x16x32_bf16 v[200:203], v[148:151], v[4:7], v[200:203]\n\t"
            "ds_write_b64 v227, v[208:209] offset:256\n\t"
            "v_mfma_f32_16x16x32_bf16 v[200:203], v[152:155], v[8:11], v[200:203]\n\t"
            "s_add_u32 s49, s33, 21504\n\t"
            "buffer_load_dwordx4 v[84:87], v247, s[28:31], s49 offen sc1\n\t"
            "v_mfma_f32_16x16x32_bf16 v[200:203], v[156:159], v[12:15], v[200:203]\n\t"
            "s_add_u32 s49, s33, 22528\n\t"
            "buffer_load_dwordx4 v[88:91], v247, s[28:31], s49 offen sc1\n\t"
            "v_mfma_f32_16x16x32_bf16 v[200:203], v[160:163], v[16:19], v[200:203]\n\t"
            "ds_write_b64 v228, v[212:213]\n\t"
            "v_mfma_f32_16x16x32_bf16 v[200:203], v[164:167], v[20:23], v[200:203]\n\t"
            "s_add_u32 s49, s33, 23552\n\t"
            "buffer_load_dwordx4 v[92:95], v247, s[28:31], s49 offen sc1\n\t"
            "v_mfma_f32_16x16x32_bf16 v[204:207], v[144:147], v[24:27], 0\n\t"
            "s_add_u32 s49, s33, 24576\n\t"
            "buffer_load_dwordx4 v[96:99], v247, s[28:31], s49 offen sc1\n\t"
            "v_mfma_f32_16x16x32_bf16 v[204:207], v[148:151], v[28:31], v[204:207]\n\t"
            "s_add_u32 s49, s33, 25600\n\t"
            "buffer_load_dwordx4 v[100:103], v247, s[28:31], s49 offen sc1\n\t"
            "v_mfma_f32_16x16x32_bf16 v[204:207], v[152:155], v[32:35], v[204:207]\n\t"
            "ds_write_b64 v228, v[216:217] offset:128\n\t"
            "v_mfma_f32_16x16x32_bf16 v[204:207], v[156:159], v[36:39], v[204:207]\n\t"
            "s_add_u32 s49, s33, 26624\n\t"
            "buffer_load_dwordx4 v[104:107], v247, s[28:31], s49 offen sc1\n\t"
            "v_mfma_f32_16x16x32_bf16 v[204:207], v[160:163], v[40:43], v[204:207]\n\t"
            "s_add_u32 s49, s33, 27648\n\t"
            "buffer_load_dwordx4 v[108:111], v247, s[28:31], s49 offen sc1\n\t"
            "v_mfma_f32_16x16x32_bf16 v[204:207], v[164:167], v[44:47], v[204:207]\n\t"
            "s_waitcnt lgkmcnt(3)\n\t"
            "v_mul_f32 v200, v192, v200\n\t"
            "v_mul_f32 v201, v193, v201\n\t"
            "v_mfma_f32_16x16x32_bf16 v[208:211], v[144:147], v[48:51], 0\n\t"
            "ds_read_b128 v[144:147], v196 offset:12288\n\t"
            "v_mul_f32 v202, v194, v202\n\t"
            "v_mfma_f32_16x16x32_bf16 v[208:211], v[148:151], v[52:55], v[208:211]\n\t"
            "ds_read_b128 v[148:151], v197 offset:12288\n\t"
            "v_mul_f32 v203, v195, v203\n\t"
            "v_mfma_f32_16x16x32_bf16 v[208:211], v[152:155], v[56:59], v[208:211]\n\t"
            "ds_read_b128 v[152:155], v196 offset:12416\n\t"
            "v_mul_f32 v204, v192, v204\n\t"
            "v_mfma_f32_16x16x32_bf16 v[208:211], v[156:159], v[60:63], v[208:211]\n\t"
            "ds_read_b128 v[156:159], v197 offset:12416\n\t"
            "v_mul_f32 v205, v193, v205\n\t"
            "v_mfma_f32_16x16x32_bf16 v[208:211], v[160:163], v[64:67], v[208:211]\n\t"
            "ds_read_b128 v[160:163], v196 offset:12544\n\t"
            "v_mul_f32 v206, v194, v206\n\t"
            "v_mfma_f32_16x16x32_bf16 v[208:211], v[164:167], v[68:71], v[208:211]\n\t"
            "ds_read_b128 v[164:167], v197 offset:12544\n\t"
            "v_mul_f32 v207, v195, v207\n\t"
            "v_mfma_f32_16x16x32_bf16 v[212:215], v[168:171], v[0:3], 0\n\t"
            "ds_write_b64 v228, v[220:221] offset:256\n\t"
            "v_mfma_f32_16x16x32_bf16 v[212:215], v[172:175], v[4:7], v[212:215]\n\t"
            "s_add_u32 s49, s33, 28672\n\t"
            "buffer_load_dwordx4 v[112:115], v247, s[28:31], s49 offen sc1\n\t"
            "v_mfma_f32_16x16x32_bf16 v[212:215], v[176:179], v[8:11], v[212:215]\n\t"
            "v_mul_f32 v208, v192, v208\n\t"
            "v_mul_f32 v209, v193, v209\n\t"
            "v_mfma_f32_16x16x32_bf16 v[212:215], v[180:183], v[12:15], v[212:215]\n\t"
            "v_mul_f32 v210, v194, v210\n\t"
            "v_mul_f32 v211, v195, v211\n\t"
            "v_mfma_f32_16x16x32_bf16 v[212:215], v[184:187], v[16:19], v[212:215]\n\t"
            "ds_read_b128 v[192:195], v233 offset:64\n\t"
            "v_cvt_pk_bf16_f32 v200, v200, v201\n\t"
            "v_mfma_f32_16x16x32_bf16 v[212:215], v[188:191], v[20:23], v[212:215]\n\t"
            "s_add_u32 s49, s33, 29696\n\t"
            "buffer_load_dwordx4 v[116:119], v247, s[28:31], s49 offen sc1\n\t"
            "v_mfma_f32_16x16x32_bf16 v[216:219], v[168:171], v[24:27], 0\n\t"
            "s_add_u32 s49, s33, 30720\n\t"
            "buffer_load_dwordx4 v[120:123], v247, s[28:31], s49 offen sc1\n\t"
            "v_mfma_f32_16x16x32_bf16 v[216:219], v[172:175], v[28:31], v[216:219]\n\t"
            "v_cvt_pk_bf16_f32 v201, v202, v203\n\t"
            "v_cvt_pk_bf16_f32 v204, v204, v205\n\t"
            "v_mfma_f32_16x16x32_bf16 v[216:219], v[176:179], v[32:35], v[216:219]\n\t"
            "s_waitcnt lgkmcnt(0)\n\t"
            "v_mul_f32 v212, v192, v212\n\t"
            "v_mul_f32 v213, v193, v213\n\t"
            "v_mfma_f32_16x16x32_bf16 v[216:219], v[180:183], v[36:39], v[216:219]\n\t"
            "v_mul_f32 v214, v194, v214\n\t"
            "v_mul_f32 v215, v195, v215\n\t"
            "v_mfma_f32_16x16x32_bf16 v[216:219], v[184:187], v[40:43], v[216:219]\n\t"
            "v_cvt_pk_bf16_f32 v202, v212, v213\n\t"
            "v_cvt_pk_bf16_f32 v203, v214, v215\n\t"
            "v_mfma_f32_16x16x32_bf16 v[216:219], v[188:191], v[44:47], v[216:219]\n\t"
            "s_add_u32 s49, s33, 31744\n\t"
            "buffer_load_dwordx4 v[124:127], v247, s[28:31], s49 offen sc1\n\t"
            "v_mfma_f32_16x16x32_bf16 v[220:223], v[168:171], v[48:51], 0\n\t"
            "ds_read_b128 v[168:171], v254\n\t"
            "v_cvt_pk_bf16_f32 v205, v206, v207\n\t"
            "v_mfma_f32_16x16x32_bf16 v[220:223], v[172:175], v[52:55], v[220:223]\n\t"
            "ds_read_b128 v[172:175], v254 offset:64\n\t"
            "v_cvt_pk_bf16_f32 v208, v208, v209\n\t"
            "v_mfma_f32_16x16x32_bf16 v[220:223], v[176:179], v[56:59], v[220:223]\n\t"
            "s_add_u32 s49, s33, 32768\n\t"
            "buffer_load_dwordx4 v[128:131], v247, s[28:31], s49 offen sc1\n\t"
            "v_mfma_f32_16x16x32_bf16 v[220:223], v[180:183], v[60:63], v[220:223]\n\t"
            "v_mul_f32 v216, v192, v216\n\t"
            "v_mul_f32 v217, v193, v217\n\t"
            "v_mfma_f32_16x16x32_bf16 v[220:223], v[184:187], v[64:67], v[220:223]\n\t"
            "v_mul_f32 v218, v194, v218\n\t"
            "v_mul_f32 v219, v195, v219\n\t"
            "v_mfma_f32_16x16x32_bf16 v[220:223], v[188:191], v[68:71], v[220:223]\n\t"
            "v_cvt_pk_bf16_f32 v206, v216, v217\n\t"
            "v_cvt_pk_bf16_f32 v207, v218, v219\n\t"
            "v_mfma_f32_16x16x32_bf16 v[212:215], v[144:147], v[0:3], 0\n\t"
            "s_add_u32 s49, s33, 33792\n\t"
            "buffer_load_dwordx4 v[132:135], v247, s[28:31], s49 offen sc1\n\t"
            "v_mfma_f32_16x16x32_bf16 v[212:215], v[148:151], v[4:7], v[212:215]\n\t"
            "s_add_u32 s49, s33, 34816\n\t"
            "buffer_load_dwordx4 v[136:139], v247, s[28:31], s49 offen sc1\n\t"
            "v_mfma_f32_16x16x32_bf16 v[212:215], v[152:155], v[8:11], v[212:215]\n\t"
            "s_add_u32 s49, s33, 35840\n\t"
            "buffer_load_dwordx4 v[140:143], v247, s[28:31], s49 offen sc1\n\t"
            "v_mfma_f32_16x16x32_bf16 v[212:215], v[156:159], v[12:15], v[212:215]\n\t"
            "s_and_b32 s68, s26, 0x7ffffff\n\t"
            "s_add_u32 s68, s68, s78\n\t"
            "s_and_b32 s69, s26, 0x38000000\n\t"
            "s_or_b32 s69, s69, s39\n\t"
            "s_bitcmp1_b32 s26, 27\n\t"
            "s_cselect_b32 s79, 2.0, s68\n\t"
            "v_mfma_f32_16x16x32_bf16 v[212:215], v[160:163], v[16:19], v[212:215]\n\t"
            "s_and_b32 s57, s56, 3\n\t"
            "s_lshl_b32 s58, s57, 3\n\t"
            "s_lshr_b32 m0, s54, s58\n\t"
            "v_mfma_f32_16x16x32_bf16 v[212:215], v[164:167], v[20:23], v[212:215]\n\t"
            "s_lshl_b32 s58, s57, 4\n\t"
            "s_lshr_b64 s[36:37], s[50:51], s58\n\t"
            "s_and_b32 s57, s36, 0xffff\n\t"
            "s_add_u32 s57, s57, s67\n\t"
            "v_mfma_f32_16x16x32_bf16 v[216:219], v[144:147], v[24:27], 0\n\t"
            "s_lshr_b64 s[36:37], s[52:53], s58\n\t"
            "s_and_b32 s58, s36, 0xffff\n\t"
            "s_add_u32 s58, s58, s67\n\t"
            "v_mfma_f32_16x16x32_bf16 v[216:219], v[148:151], v[28:31], v[216:219]\n\t"
            "s_and_b32 s59, s50, 0xffff\n\t"
            "s_add_u32 s59, s59, s67\n\t"
            "s_and_b32 s38, s52, 0xffff\n\t"
            "s_add_u32 s38, s38, s67\n\t"
            "v_mfma_f32_16x16x32_bf16 v[216:219], v[152:155], v[32:35], v[216:219]\n\t"
            "v_cvt_pk_bf16_f32 v209, v210, v211\n\t"
            "v_mul_f32 v220, v192, v220\n\t"
            "v_mfma_f32_16x16x32_bf16 v[216:219], v[156:159], v[36:39], v[216:219]\n\t"
            "v_mul_f32 v221, v193, v221\n\t"
            "v_mul_f32 v222, v194, v222\n\t"
            "v_mfma_f32_16x16x32_bf16 v[216:219], v[160:163], v[40:43], v[216:219]\n\t"
            "v_mul_f32 v223, v195, v223\n\t"
            "v_cvt_pk_bf16_f32 v210, v220, v221\n\t"
            "v_mfma_f32_16x16x32_bf16 v[216:219], v[164:167], v[44:47], v[216:219]\n\t"
            "ds_read_b128 v[192:195], v233 offset:128\n\t"
            "v_cvt_pk_bf16_f32 v211, v222, v223\n\t"
            "v_mfma_f32_16x16x32_bf16 v[220:223], v[144:147], v[48:51], 0\n\t"
            "s_waitcnt vmcnt(18)\n\t"
            "ds_read_b64 v[144:145], v253\n\t"
            "v_mov_b64 v[176:177], 0\n\t"
            "v_mfma_f32_16x16x32_bf16 v[220:223], v[148:151], v[52:55], v[220:223]\n\t"
            "ds_read_b64 v[146:147], v253 offset:96\n\t"
            "v_mov_b64 v[178:179], 0\n\t"
            "v_mfma_f32_16x16x32_bf16 v[220:223], v[152:155], v[56:59], v[220:223]\n\t"
            "ds_read_b64 v[148:149], v253 offset:1744\n\t"
            "v_mov_b64 v[180:181], 0\n\t"
            "v_mfma_f32_16x16x32_bf16 v[220:223], v[156:159], v[60:63], v[220:223]\n\t"
            "ds_read_b64 v[150:151], v253 offset:1840\n\t"
            "v_mov_b64 v[182:183], 0\n\t"
            "v_mfma_f32_16x16x32_bf16 v[220:223], v[160:163], v[64:67], v[220:223]\n\t"
            "ds_read_b64 v[152:153], v253 offset:8\n\t"
            "v_mov_b64 v[184:185], 0\n\t"
            "v_mfma_f32_16x16x32_bf16 v[220:223], v[164:167], v[68:71], v[220:223]\n\t"
            "ds_read_b64 v[154:155], v253 offset:104\n\t"
            "v_mov_b64 v[186:187], 0\n\t"
            "v_mov_b64 v[188:189], 0\n\t"
            "v_mov_b64 v[190:191], 0\n\t"
            "v_writelane_b32 v236, s59, m0\n\t"
            "v_writelane_b32 v236, s57, 63\n\t"
            "v_xor_b32 v237, 16, v236\n\t"
            "s_waitcnt lgkmcnt(6)\n\t"
            "v_mul_f32 v212, v192, v212\n\t"
            "v_mul_f32 v213, v193, v213\n\t"
            "v_mul_f32 v214, v194, v214\n\t"
            "v_mul_f32 v215, v195, v215\n\t"
            "v_mul_f32 v216, v192, v216\n\t"
            "v_mul_f32 v217, v193, v217\n\t"
            "v_mul_f32 v218, v194, v218\n\t"
            "v_mul_f32 v219, v195, v219\n\t"
            "v_mul_f32 v220, v192, v220\n\t"
            "v_mul_f32 v221, v193, v221\n\t"
            "v_mul_f32 v222, v194, v222\n\t"
            "v_mul_f32 v223, v195, v223\n\t"
            "v_writelane_b32 v248, s38, m0\n\t"
            "v_and_b32 v249, s69, v249\n\t"
            "v_cvt_pk_bf16_f32 v212, v212, v213\n\t"
            "v_cvt_pk_bf16_f32 v216, v216, v217\n\t"
            "s_waitcnt lgkmcnt(2)\n\t"
            "v_smfmac_f32_16x16x64_bf16 v[176:179], v[168:171], v[144:151], v255\n\t"
            "ds_read_b64 v[156:157], v253 offset:1752\n\t"
            "v_cvt_pk_bf16_f32 v220, v220, v221\n\t"
            "v_cvt_pk_bf16_f32 v213, v214, v215\n\t"
            "v_cvt_pk_bf16_f32 v217, v218, v219\n\t"
            "v_cvt_pk_bf16_f32 v221, v222, v223\n\t"
            "v_writelane_b32 v248, s58, 63\n\t"
            "ds_read_b64 v[158:159], v253 offset:1848\n\t"
            "ds_read_b64 v[160:161], v253 offset:16\n\t"
            "v_cvt_pk_bf16_f32 v176, v176, v177\n\t"
            "v_cvt_pk_bf16_f32 v177, v178, v179\n\t"
            "ds_read_b64 v[162:163], v253 offset:112\n\t"
            "ds_read_b64 v[164:165], v253 offset:1760\n\t"
            "ds_read_b64 v[166:167], v253 offset:1856\n\t"
            "ds_read_b128 v[168:171], v254 offset:128\n\t"
            "ds_read_b64 v[144:145], v253 offset:24\n\t"
            "ds_read_b64 v[146:147], v253 offset:120\n\t"
            "ds_read_b64 v[148:149], v253 offset:1768\n\t"
            "ds_read_b64 v[150:151], v253 offset:1864\n\t"
            "ds_write_b128 v230, v[200:203]\n\t"
            "ds_write_b128 v230, v[204:207] offset:1536\n\t"
            "s_waitcnt lgkmcnt(11)\n\t"
            "v_smfmac_f32_16x16x64_bf16 v[180:183], v[172:175], v[152:159], v255\n\t"
            "ds_read_b128 v[172:175], v254 offset:192\n\t"
            "ds_read_b64 v[152:153], v253 offset:32\n\t"
            "ds_read_b64 v[154:155], v253 offset:128\n\t"
            "ds_read_b64 v[156:157], v253 offset:1776\n\t"
            "s_waitcnt lgkmcnt(10)\n\t"
            "v_smfmac_f32_16x16x64_bf16 v[184:187], v[168:171], v[160:167], v255\n\t"
            "ds_read_b64 v[158:159], v253 offset:1872\n\t"
            "v_mfma_f32_4x4x4_16b_bf16 v[196:199], v[176:177], v[176:177], 0\n\t"
            "ds_read_b128 v[168:171], v254 offset:256\n\t"
            "v_cvt_pk_bf16_f32 v178, v180, v181\n\t"
            "v_cvt_pk_bf16_f32 v179, v182, v183\n\t"
            "v_mov_b64 v[180:181], 0\n\t"
            "v_mov_b64 v[182:183], 0\n\t"
            "s_nop 0\n\t"
            "v_cvt_pk_bf16_f32 v184, v184, v185\n\t"
            "v_cvt_pk_bf16_f32 v185, v186, v187\n\t"
            "ds_read_b64 v[160:161], v253 offset:40\n\t"
            "ds_read_b64 v[162:163], v253 offset:136\n\t"
            "ds_read_b64 v[164:165], v253 offset:1784\n\t"
            "s_waitcnt lgkmcnt(14)\n\t"
            "ds_read_b64 v[166:167], v253 offset:1880\n\t"
            "s_waitcnt lgkmcnt(9)\n\t"
            "v_smfmac_f32_16x16x64_bf16 v[188:191], v[172:175], v[144:151], v255\n\t"
            "ds_read_b128 v[172:175], v254 offset:320\n\t"
            "v_mfma_f32_4x4x4_16b_bf16 v[196:199], v[178:179], v[178:179], v[196:199]\n\t"
            "ds_read_b64 v[144:145], v253 offset:48\n\t"
            "v_mfma_f32_16x16x32_bf16 v[192:195], v[238:241], v[176:179], 0\n\t"
            "ds_read_b64 v[146:147], v253 offset:144\n\t"
            "ds_read_b64 v[148:149], v253 offset:1792\n\t"
            "ds_read_b64 v[150:151], v253 offset:1888\n\t"
            "ds_write_b128 v236, v[176:179]\n\t"
            "v_mov_b64 v[176:177], 0\n\t"
            "v_mov_b64 v[178:179], 0\n\t"
            "v_cvt_pk_bf16_f32 v186, v188, v189\n\t"
            "v_cvt_pk_bf16_f32 v187, v190, v191\n\t"
            "s_waitcnt lgkmcnt(10)\n\t"
            "v_smfmac_f32_16x16x64_bf16 v[176:179], v[168:171], v[152:159], v255\n\t"
            "ds_read_b128 v[168:171], v254 offset:384\n\t"
            "v_mfma_f32_4x4x4_16b_bf16 v[196:199], v[184:185], v[184:185], v[196:199]\n\t"
            "ds_read_b64 v[152:153], v253 offset:56\n\t"
            "v_mov_b64 v[188:189], 0\n\t"
            "v_mov_b64 v[190:191], 0\n\t"
            "ds_read_b64 v[154:155], v253 offset:152\n\t"
            "s_waitcnt lgkmcnt(8)\n\t"
            "v_smfmac_f32_16x16x64_bf16 v[180:183], v[172:175], v[160:167], v255\n\t"
            "ds_read_b64 v[156:157], v253 offset:1800\n\t"
            "v_mfma_f32_4x4x4_16b_bf16 v[196:199], v[186:187], v[186:187], v[196:199]\n\t"
            "ds_read_b64 v[158:159], v253 offset:1896\n\t"
            "v_mfma_f32_16x16x32_bf16 v[192:195], v[238:241], v[184:187], v[192:195]\n\t"
            "ds_read_b128 v[172:175], v254 offset:448\n\t"
            "v_cvt_pk_bf16_f32 v176, v176, v177\n\t"
            "v_cvt_pk_bf16_f32 v177, v178, v179\n\t"
            "s_nop 0\n\t"
            "v_cvt_pk_bf16_f32 v178, v180, v181\n\t"
            "v_cvt_pk_bf16_f32 v179, v182, v183\n\t"
            "v_mov_b64 v[180:181], 0\n\t"
            "v_mov_b64 v[182:183], 0\n\t"
            "ds_read_b64 v[160:161], v253 offset:64\n\t"
            "ds_read_b64 v[162:163], v253 offset:160\n\t"
            "ds_read_b64 v[164:165], v253 offset:1808\n\t"
            "ds_read_b64 v[166:167], v253 offset:1904\n\t"
            "s_waitcnt lgkmcnt(14)\n\t"
            "ds_write_b128 v237, v[184:187]\n\t"
            "v_mov_b64 v[184:185], 0\n\t"
            "v_mov_b64 v[186:187], 0\n\t"
            "s_waitcnt lgkmcnt(10)\n\t"
            "s_nop 0\n\t"
            "v_smfmac_f32_16x16x64_bf16 v[184:187], v[168:171], v[144:151], v255\n\t"
            "ds_read_b128 v[168:171], v254 offset:512\n\t"
            "v_mfma_f32_4x4x4_16b_bf16 v[196:199], v[176:177], v[176:177], v[196:199]\n\t"
            "ds_read_b64 v[144:145], v253 offset:72\n\t"
            "s_waitcnt lgkmcnt(7)\n\t"
            "v_smfmac_f32_16x16x64_bf16 v[188:191], v[172:175], v[152:159], v255\n\t"
            "ds_read_b64 v[146:147], v253 offset:168\n\t"
            "v_mfma_f32_4x4x4_16b_bf16 v[196:199], v[178:179], v[178:179], v[196:199]\n\t"
            "ds_read_b64 v[148:149], v253 offset:1816\n\t"
            "v_mfma_f32_16x16x32_bf16 v[192:195], v[238:241], v[176:179], v[192:195]\n\t"
            "ds_read_b64 v[150:151], v253 offset:1912\n\t"
            "v_cvt_pk_bf16_f32 v184, v184, v185\n\t"
            "v_cvt_pk_bf16_f32 v185, v186, v187\n\t"
            "s_nop 0\n\t"
            "v_cvt_pk_bf16_f32 v186, v188, v189\n\t"
            "v_cvt_pk_bf16_f32 v187, v190, v191\n\t"
            "v_mov_b64 v[188:189], 0\n\t"
            "v_mov_b64 v[190:191], 0\n\t"
            "ds_read_b128 v[172:175], v254 offset:576\n\t"
            "ds_read_b64 v[152:153], v253 offset:80\n\t"
            "ds_read_b64 v[154:155], v253 offset:176\n\t"
            "ds_read_b64 v[156:157], v253 offset:1824\n\t"
            "ds_read_b64 v[158:159], v253 offset:1920\n\t"
            "s_waitcnt lgkmcnt(14)\n\t"
            "ds_write_b128 v236, v[176:179] offset:128\n\t"
            "v_mov_b64 v[176:177], 0\n\t"
            "v_mov_b64 v[178:179], 0\n\t"
            "s_waitcnt lgkmcnt(10)\n\t"
            "s_nop 0\n\t"
            "v_smfmac_f32_16x16x64_bf16 v[176:179], v[168:171], v[160:167], v255\n\t"
            "ds_read_b128 v[168:171], v254 offset:640\n\t"
            "v_mfma_f32_4x4x4_16b_bf16 v[196:199], v[184:185], v[184:185], v[196:199]\n\t"
            "ds_read_b64 v[160:161], v253 offset:88\n\t"
            "ds_read_b64 v[162:163], v253 offset:184\n\t"
            "ds_read_b64 v[164:165], v253 offset:1832\n\t"
            "ds_read_b64 v[166:167], v253 offset:1928\n\t"
            "s_waitcnt lgkmcnt(10)\n\t"
            "v_smfmac_f32_16x16x64_bf16 v[180:183], v[172:175], v[144:151], v255\n\t"
            "ds_read_b128 v[172:175], v254 offset:704\n\t"
            "v_mfma_f32_4x4x4_16b_bf16 v[196:199], v[186:187], v[186:187], v[196:199]\n\t"
            "ds_write_b128 v237, v[184:187] offset:128\n\t"
            "v_mfma_f32_16x16x32_bf16 v[192:195], v[238:241], v[184:187], v[192:195]\n\t"
            "v_mov_b64 v[184:185], 0\n\t"
            "v_mov_b64 v[186:187], 0\n\t"
            "v_cvt_pk_bf16_f32 v176, v176, v177\n\t"
            "v_cvt_pk_bf16_f32 v177, v178, v179\n\t"
            "v_cvt_pk_bf16_f32 v178, v180, v181\n\t"
            "v_cvt_pk_bf16_f32 v179, v182, v183\n\t"
            "ds_write_b128 v230, v[208:211] offset:3072\n\t"
            "s_waitcnt lgkmcnt(7)\n\t"
            "v_smfmac_f32_16x16x64_bf16 v[184:187], v[168:171], v[152:159], v255\n\t"
            "ds_write_b128 v236, v[176:179] offset:256\n\t"
            "v_mfma_f32_4x4x4_16b_bf16 v[196:199], v[176:177], v[176:177], v[196:199]\n\t"
            "ds_write_b64 v231, v[212:213]\n\t"
            "s_waitcnt lgkmcnt(4)\n\t"
            "v_smfmac_f32_16x16x64_bf16 v[188:191], v[172:175], v[160:167], v255\n\t"
            "s_mov_b32 exec_hi, 0x3fffff\n\t"
            "s_add_u32 m0, s66, 0\n\t"
            "s_add_u32 s49, s79, 0\n\t"
            "buffer_load_dwordx4 v249, s[72:75], s49 offen lds\n\t"
            "s_mov_b32 exec_hi, -1\n\t"
            "v_mfma_f32_4x4x4_16b_bf16 v[196:199], v[178:179], v[178:179], v[196:199]\n\t"
            "s_mov_b32 exec_hi, 0x3fffff\n\t"
            "s_add_u32 m0, s66, 872\n\t"
            "s_add_u32 s49, s68, 196224\n\t"
            "buffer_load_dwordx4 v249, s[72:75], s49 offen lds\n\t"
            "s_mov_b32 exec_hi, -1\n\t"
            "v_mfma_f32_16x16x32_bf16 v[192:195], v[238:241], v[176:179], v[192:195]\n\t"
            "s_mov_b32 exec_hi, 0x3fffff\n\t"
            "s_add_u32 m0, s66, 1744\n\t"
            "s_add_u32 s49, s68, 392448\n\t"
            "buffer_load_dwordx4 v249, s[72:75], s49 offen lds\n\t"
            "s_mov_b32 exec_hi, -1\n\t"
            "s_mov_b32 exec_hi, 0x3fffff\n\t"
            "s_add_u32 m0, s66, 2616\n\t"
            "s_add_u32 s49, s68, 588672\n\t"
            "buffer_load_dwordx4 v249, s[72:75], s49 offen lds\n\t"
            "s_mov_b32 exec_hi, -1\n\t"
            "s_mov_b32 exec_hi, 0x3fffff\n\t"
            "s_add_u32 m0, s66, 3488\n\t"
            "s_add_u32 s49, s68, 784896\n\t"
            "buffer_load_dwordx4 v249, s[72:75], s49 offen lds\n\t"
            "s_mov_b32 exec_hi, -1\n\t"
            "s_mov_b32 exec_hi, 0x3fffff\n\t"
            "s_add_u32 m0, s66, 4360\n\t"
            "s_add_u32 s49, s68, 981120\n\t"
            "buffer_load_dwordx4 v249, s[72:75], s49 offen lds\n\t"
            "s_mov_b32 exec_hi, -1\n\t"
            "v_cvt_pk_bf16_f32 v184, v184, v185\n\t"
            "v_cvt_pk_bf16_f32 v185, v186, v187\n\t"
            "v_cvt_pk_bf16_f32 v186, v188, v189\n\t"
            "v_cvt_pk_bf16_f32 v187, v190, v191\n\t"
            "v_mfma_f32_4x4x4_16b_bf16 v[196:199], v[184:185], v[184:185], v[196:199]\n\t"
            "s_mov_b32 exec_hi, 0x3fffff\n\t"
            "s_add_u32 m0, s66, 5232\n\t"
            "s_add_u32 s49, s68, 1177344\n\t"
            "buffer_load_dwordx4 v249, s[72:75], s49 offen lds\n\t"
            "s_mov_b32 exec_hi, -1\n\t"
            "v_mfma_f32_4x4x4_16b_bf16 v[196:199], v[186:187], v[186:187], v[196:199]\n\t"
            "s_mov_b32 exec_hi, 0x3fffff\n\t"
            "s_add_u32 m0, s66, 6104\n\t"
            "s_add_u32 s49, s68, 1373568\n\t"
            "buffer_load_dwordx4 v249, s[72:75], s49 offen lds\n\t"
            "s_mov_b32 exec_hi, -1\n\t"
            "v_mfma_f32_16x16x32_bf16 v[192:195], v[238:241], v[184:187], v[192:195]\n\t"
            "ds_write_b128 v237, v[184:187] offset:256\n\t"
            "ds_write_b64 v231, v[216:217] offset:1536\n\t"
            "ds_write_b64 v231, v[220:221] offset:3072\n\t"
            "s_mov_b32 exec_hi, 0x3fffff\n\t"
            "s_add_u32 m0, s66, 6976\n\t"
            "s_add_u32 s49, s68, 1569792\n\t"
            "buffer_load_dwordx4 v249, s[72:75], s49 offen lds\n\t"
            "s_mov_b32 exec_hi, -1\n\t"
            "v_cndmask_b32_e64 v193, v196, v197, s[60:61]\n\t"
            "v_cndmask_b32_e64 v193, v193, v198, s[62:63]\n\t"
            "v_cndmask_b32_e64 v193, v193, v199, s[64:65]\n\t"
            "ds_write_b64 v248, v[192:193] offset:18816\n\t"
            "s_waitcnt lgkmcnt(0)\n\t"
            "s_nop 7\n\t"
            "v_mov_b32 v144, s44\n\t"
            "v_mov_b32 v145, -1\n\t"
            "ds_write_b32 v144, v145\n\t"
            "s_cmp_ge_u32 s33, 221184\n\t"
            "s_cbranch_scc1 .Lrp%=\n\t"
            "s_barrier\n\t"
            "s_waitcnt vmcnt(9)\n\t"
            "s_nop 7\n\t"
            "v_add_u32 v232, s67, v242\n\t"
            "v_xor_b32 v233, 64, v232\n\t"
            "v_add_u32 v234, s67, v247\n\t"
            "ds_read_b128 v[144:147], v232\n\t"
            "ds_read_b128 v[148:151], v233\n\t"
            "ds_read_b128 v[152:155], v232 offset:128\n\t"
            "ds_read_b128 v[156:159], v233 offset:128\n\t"
            "ds_read_b128 v[160:163], v232 offset:256\n\t"
            "ds_read_b128 v[164:167], v233 offset:256\n\t"
            "ds_read_b128 v[176:179], v234 offset:18816\n\t"
            "ds_read_b128 v[180:183], v234 offset:19840\n\t"
            "s_waitcnt lgkmcnt(7)\n\t"
            "v_mfma_f32_16x16x32_bf16 v[200:203], v[144:147], v[0:3], 0\n\t"
            "s_waitcnt lgkmcnt(6)\n\t"
            "v_mfma_f32_16x16x32_bf16 v[200:203], v[148:151], v[4:7], v[200:203]\n\t"
            "s_waitcnt lgkmcnt(5)\n\t"
            "v_mfma_f32_16x16x32_bf16 v[200:203], v[152:155], v[8:11], v[200:203]\n\t"
            "s_waitcnt lgkmcnt(4)\n\t"
            "v_mfma_f32_16x16x32_bf16 v[200:203], v[156:159], v[12:15], v[200:203]\n\t"
            "s_waitcnt lgkmcnt(1)\n\t"
            "v_add_f32 v176, v176, v178\n\t"
            "s_waitcnt lgkmcnt(0)\n\t"
            "v_add_f32 v180, v180, v182\n\t"
            "v_mfma_f32_16x16x32_bf16 v[200:203], v[160:163], v[16:19], v[200:203]\n\t"
            "v_add_f32 v176, v176, v180\n\t"
            "v_add_f32 v177, v177, v179\n\t"
            "v_mfma_f32_16x16x32_bf16 v[200:203], v[164:167], v[20:23], v[200:203]\n\t"
            "v_add_f32 v181, v181, v183\n\t"
            "v_mul_f32 v176, 0x3baaaaab, v176\n\t"
            "v_mfma_f32_16x16x32_bf16 v[204:207], v[144:147], v[24:27], 0\n\t"
            "v_add_f32 v177, v177, v181\n\t"
            "v_mul_f32_e64 v176, v176, -v176\n\t"
            "v_mfma_f32_16x16x32_bf16 v[204:207], v[148:151], v[28:31], v[204:207]\n\t"
            "v_fmac_f32 v176, 0x3baaaaab, v177\n\t"
            "v_max_f32 v176, 0, v176\n\t"
            "v_mfma_f32_16x16x32_bf16 v[204:207], v[152:155], v[32:35], v[204:207]\n\t"
            "v_add_f32 v176, 0x3727c5ac, v176\n\t"
            "v_rsq_f32 v176, v176\n\t"
            "v_mfma_f32_16x16x32_bf16 v[204:207], v[156:159], v[36:39], v[204:207]\n\t"
            "ds_write_b32 v246, v176\n\t"
            "v_mfma_f32_16x16x32_bf16 v[204:207], v[160:163], v[40:43], v[204:207]\n\t"
            "ds_read_b128 v[228:231], v245\n\t"
            "v_mfma_f32_16x16x32_bf16 v[204:207], v[164:167], v[44:47], v[204:207]\n\t"
            "v_mfma_f32_16x16x32_bf16 v[208:211], v[144:147], v[48:51], 0\n\t"
            "v_mfma_f32_16x16x32_bf16 v[208:211], v[148:151], v[52:55], v[208:211]\n\t"
            "v_mfma_f32_16x16x32_bf16 v[208:211], v[152:155], v[56:59], v[208:211]\n\t"
            "v_mfma_f32_16x16x32_bf16 v[208:211], v[156:159], v[60:63], v[208:211]\n\t"
            "v_mfma_f32_16x16x32_bf16 v[208:211], v[160:163], v[64:67], v[208:211]\n\t"
            "v_mfma_f32_16x16x32_bf16 v[208:211], v[164:167], v[68:71], v[208:211]\n\t"
            "v_mfma_f32_16x16x32_bf16 v[212:215], v[144:147], v[72:75], 0\n\t"
            "s_waitcnt lgkmcnt(0)\n\t"
            "v_mul_f32 v200, v228, v200\n\t"
            "v_mul_f32 v201, v229, v201\n\t"
            "v_mfma_f32_16x16x32_bf16 v[212:215], v[148:151], v[76:79], v[212:215]\n\t"
            "v_mul_f32 v204, v228, v204\n\t"
            "v_mul_f32 v205, v229, v205\n\t"
            "v_mfma_f32_16x16x32_bf16 v[212:215], v[152:155], v[80:83], v[212:215]\n\t"
            "v_mul_f32 v208, v228, v208\n\t"
            "v_mul_f32 v209, v229, v209\n\t"
            "v_mfma_f32_16x16x32_bf16 v[212:215], v[156:159], v[84:87], v[212:215]\n\t"
            "v_mul_f32 v202, v230, v202\n\t"
            "v_mul_f32 v203, v231, v203\n\t"
            "v_mfma_f32_16x16x32_bf16 v[212:215], v[160:163], v[88:91], v[212:215]\n\t"
            "v_cvt_pk_bf16_f32 v200, v200, v201\n\t"
            "v_mul_f32 v206, v230, v206\n\t"
            "v_mfma_f32_16x16x32_bf16 v[212:215], v[164:167], v[92:95], v[212:215]\n\t"
            "v_mul_f32 v207, v231, v207\n\t"
            "v_cvt_pk_bf16_f32 v204, v204, v205\n\t"
            "v_mfma_f32_16x16x32_bf16 v[216:219], v[144:147], v[96:99], 0\n\t"
            "v_mul_f32 v210, v230, v210\n\t"
            "v_mul_f32 v211, v231, v211\n\t"
            "v_mfma_f32_16x16x32_bf16 v[216:219], v[148:151], v[100:103], v[216:219]\n\t"
            "v_cvt_pk_bf16_f32 v208, v208, v209\n\t"
            "v_cvt_pk_bf16_f32 v201, v202, v203\n\t"
            "v_mfma_f32_16x16x32_bf16 v[216:219], v[152:155], v[104:107], v[216:219]\n\t"
            "v_mul_f32 v212, v228, v212\n\t"
            "v_mul_f32 v213, v229, v213\n\t"
            "v_mfma_f32_16x16x32_bf16 v[216:219], v[156:159], v[108:111], v[216:219]\n\t"
            "v_mul_f32 v214, v230, v214\n\t"
            "v_mul_f32 v215, v231, v215\n\t"
            "v_mfma_f32_16x16x32_bf16 v[216:219], v[160:163], v[112:115], v[216:219]\n\t"
            "v_cvt_pk_bf16_f32 v212, v212, v213\n\t"
            "v_cvt_pk_bf16_f32 v205, v206, v207\n\t"
            "v_mfma_f32_16x16x32_bf16 v[216:219], v[164:167], v[116:119], v[216:219]\n\t"
            "v_cvt_pk_bf16_f32 v209, v210, v211\n\t"
            "v_cvt_pk_bf16_f32 v213, v214, v215\n\t"
            "v_mfma_f32_16x16x32_bf16 v[220:223], v[144:147], v[120:123], 0\n\t"
            "v_mfma_f32_16x16x32_bf16 v[220:223], v[148:151], v[124:127], v[220:223]\n\t"
            "v_mfma_f32_16x16x32_bf16 v[220:223], v[152:155], v[128:131], v[220:223]\n\t"
            "v_mfma_f32_16x16x32_bf16 v[220:223], v[156:159], v[132:135], v[220:223]\n\t"
            "s_nop 1\n\t"
            "v_mul_f32 v216, v228, v216\n\t"
            "v_mfma_f32_16x16x32_bf16 v[220:223], v[160:163], v[136:139], v[220:223]\n\t"
            "v_mul_f32 v217, v229, v217\n\t"
            "v_mul_f32 v218, v230, v218\n\t"
            "v_mfma_f32_16x16x32_bf16 v[220:223], v[164:167], v[140:143], v[220:223]\n\t"
            "v_mul_f32 v219, v231, v219\n\t"
            "v_cvt_pk_bf16_f32 v216, v216, v217\n\t"
            "v_cvt_pk_bf16_f32 v217, v218, v219\n\t"
            "s_nop 4\n\t"
            "v_mul_f32 v220, v228, v220\n\t"
            "v_mul_f32 v221, v229, v221\n\t"
            "v_mul_f32 v222, v230, v222\n\t"
            "v_mul_f32 v223, v231, v223\n\t"
            "v_cvt_pk_bf16_f32 v220, v220, v221\n\t"
            "v_cvt_pk_bf16_f32 v221, v222, v223\n\t"
            "s_nop 7\n\t"
            "s_add_u32 s45, s46, s27\n\t"
            "s_mov_b32 s46, s47\n\t"
            "s_mov_b32 s47, s68\n\t"
            "s_xor_b32 s48, s48, s71\n\t"
            "s_xor_b32 s67, s67, s71\n\t"
            "s_add_u32 s76, s76, 4\n\t"
            "s_lshl_b32 s56, s56, 2\n\t"
            "s_lshr_b32 s49, s26, 30\n\t"
            "s_or_b32 s56, s56, s49\n\t"
            "ds_read_b128 v[168:171], v254\n\t"
            "ds_read_b128 v[172:175], v254 offset:64\n\t"
            "s_waitcnt vmcnt(0)\n\t"
            "ds_read_b64 v[144:145], v253\n\t"
            "ds_read_b64 v[146:147], v253 offset:96\n\t"
            "ds_read_b64 v[148:149], v253 offset:1744\n\t"
            "ds_read_b64 v[150:151], v253 offset:1840\n\t"
            "ds_read_b64 v[152:153], v253 offset:8\n\t"
            "ds_read_b64 v[154:155], v253 offset:104\n\t"
            "ds_read_b64 v[156:157], v253 offset:1752\n\t"
            "ds_read_b64 v[158:159], v253 offset:1848\n\t"
            "ds_read_b64 v[160:161], v253 offset:16\n\t"
            "ds_read_b64 v[162:163], v253 offset:112\n\t"
            "ds_read_b64 v[164:165], v253 offset:1760\n\t"
            "ds_read_b64 v[166:167], v253 offset:1856\n\t"
            "s_waitcnt lgkmcnt(0)\n\t"
            "s_nop 7\n\t"
            ".p2align 6\n\t"
            ".Lwc_loop%=:\n\t"
            "s_setprio 3\n\t"
            "ds_read_b128 v[228:231], v245 offset:64\n\t"
            "v_mov_b64 v[176:177], 0\n\t"
            "v_mov_b64 v[178:179], 0\n\t"
            "v_mov_b32 v249, s76\n\t"
            "v_mov_b64 v[180:181], 0\n\t"
            "s_waitcnt lgkmcnt(9)\n\t"
            "v_smfmac_f32_16x16x64_bf16 v[176:179], v[168:171], v[144:151], v255\n\t"
            "ds_read_b128 v[168:171], v254 offset:128\n\t"
            "v_mov_b64 v[182:183], 0\n\t"
            "v_mov_b64 v[184:185], 0\n\t"
            "v_mov_b64 v[186:187], 0\n\t"
            "s_waitcnt lgkmcnt(6)\n\t"
            "v_smfmac_f32_16x16x64_bf16 v[180:183], v[172:175], v[152:159], v255\n\t"
            "ds_read_b32 v249, v249\n\t"
            "v_mov_b64 v[188:189], 0\n\t"
            "v_mov_b64 v[190:191], 0\n\t"
            "v_cvt_pk_bf16_f32 v176, v176, v177\n\t"
            "v_cvt_pk_bf16_f32 v177, v178, v179\n\t"
            "v_add_u32 v236, s67, v250\n\t"
            "v_add_u32 v232, s48, v242\n\t"
            "v_mfma_f32_4x4x4_16b_bf16 v[196:199], v[176:177], v[176:177], 0\n\t"
            "ds_read_b64 v[144:145], v253 offset:24\n\t"
            "v_cvt_pk_bf16_f32 v178, v180, v181\n\t"
            "v_cvt_pk_bf16_f32 v179, v182, v183\n\t"
            "s_waitcnt lgkmcnt(3)\n\t"
            "s_barrier\n\t"
            "v_mov_b64 v[180:181], 0\n\t"
            "v_mov_b64 v[182:183], 0\n\t"
            "v_xor_b32 v233, 64, v232\n\t"
            "v_add_u32 v248, s67, v251\n\t"
            "v_add_u32 v234, s67, v247\n\t"
            "ds_read_b64 v[146:147], v253 offset:120\n\t"
            "ds_read_b64 v[148:149], v253 offset:1768\n\t"
            "ds_read_b64 v[150:151], v253 offset:1864\n\t"
            "s_waitcnt lgkmcnt(5)\n\t"
            "v_smfmac_f32_16x16x64_bf16 v[184:187], v[168:171], v[160:167], v255\n\t"
            "ds_read_b128 v[172:175], v254 offset:192\n\t"
            "v_mfma_f32_4x4x4_16b_bf16 v[196:199], v[178:179], v[178:179], v[196:199]\n\t"
            "ds_read_b64 v[152:153], v253 offset:32\n\t"
            "v_mfma_f32_16x16x32_bf16 v[192:195], v[238:241], v[176:179], 0\n\t"
            "ds_read_b64 v[154:155], v253 offset:128\n\t"
            "s_waitcnt lgkmcnt(7)\n\t"
            "v_readfirstlane_b32 s26, v249\n\t"
            "ds_read_b64 v[156:157], v253 offset:1776\n\t"
            "s_and_b32 s68, s26, 0x7ffffff\n\t"
            "s_add_u32 s68, s68, s78\n\t"
            "s_and_b32 s69, s26, 0x38000000\n\t"
            "s_or_b32 s69, s69, s39\n\t"
            "s_bitcmp1_b32 s26, 27\n\t"
            "s_cselect_b32 s79, 2.0, s68\n\t"
            "ds_read_b64 v[158:159], v253 offset:1872\n\t"
            "s_and_b32 s57, s56, 3\n\t"
            "s_lshl_b32 s58, s57, 3\n\t"
            "s_lshr_b32 m0, s54, s58\n\t"
            "ds_read_b128 v[168:171], v254 offset:256\n\t"
            "s_lshl_b32 s58, s57, 4\n\t"
            "s_lshr_b64 s[36:37], s[50:51], s58\n\t"
            "s_and_b32 s57, s36, 0xffff\n\t"
            "s_add_u32 s57, s57, s67\n\t"
            "ds_read_b64 v[160:161], v253 offset:40\n\t"
            "s_lshr_b64 s[36:37], s[52:53], s58\n\t"
            "s_and_b32 s58, s36, 0xffff\n\t"
            "s_add_u32 s58, s58, s67\n\t"
            "ds_read_b64 v[162:163], v253 offset:136\n\t"
            "s_and_b32 s59, s50, 0xffff\n\t"
            "s_add_u32 s59, s59, s67\n\t"
            "s_and_b32 s38, s52, 0xffff\n\t"
            "s_add_u32 s38, s38, s67\n\t"
            "s_waitcnt lgkmcnt(7)\n\t"
            "v_smfmac_f32_16x16x64_bf16 v[188:191], v[172:175], v[144:151], v255\n\t"
            "ds_read_b64 v[164:165], v253 offset:1784\n\t"
            "v_writelane_b32 v236, s59, m0\n\t"
            "v_writelane_b32 v236, s57, 63\n\t"
            "v_cvt_pk_bf16_f32 v184, v184, v185\n\t"
            "v_cvt_pk_bf16_f32 v185, v186, v187\n\t"
            "v_xor_b32 v237, 16, v236\n\t"
            "v_writelane_b32 v248, s38, m0\n\t"
            "v_mfma_f32_4x4x4_16b_bf16 v[196:199], v[184:185], v[184:185], v[196:199]\n\t"
            "ds_read_b64 v[166:167], v253 offset:1880\n\t"
            "v_cvt_pk_bf16_f32 v186, v188, v189\n\t"
            "v_cvt_pk_bf16_f32 v187, v190, v191\n\t"
            "v_mov_b64 v[188:189], 0\n\t"
            "v_mov_b64 v[190:191], 0\n\t"
            "v_writelane_b32 v248, s58, 63\n\t"
            "ds_read_b128 v[172:175], v254 offset:320\n\t"
            "ds_read_b64 v[144:145], v253 offset:48\n\t"
            "ds_read_b64 v[146:147], v253 offset:144\n\t"
            "ds_read_b64 v[148:149], v253 offset:1792\n\t"
            "ds_read_b64 v[150:151], v253 offset:1888\n\t"
            "ds_read_b32 v249, v252 offset:1024\n\t"
            "s_waitcnt lgkmcnt(14)\n\t"
            "ds_write_b128 v236, v[176:179]\n\t"
            "v_mov_b64 v[176:177], 0\n\t"
            "v_mov_b64 v[178:179], 0\n\t"
            "s_waitcnt lgkmcnt(11)\n\t"
            "s_nop 0\n\t"
            "v_smfmac_f32_16x16x64_bf16 v[176:179], v[168:171], v[152:159], v255\n\t"
            "ds_read_b128 v[168:171], v254 offset:384\n\t"
            "v_mfma_f32_4x4x4_16b_bf16 v[196:199], v[186:187], v[186:187], v[196:199]\n\t"
            "ds_read_b64 v[152:153], v253 offset:56\n\t"
            "v_mfma_f32_16x16x32_bf16 v[192:195], v[238:241], v[184:187], v[192:195]\n\t"
            "ds_read_b64 v[154:155], v253 offset:152\n\t"
            "s_waitcnt lgkmcnt(9)\n\t"
            "v_smfmac_f32_16x16x64_bf16 v[180:183], v[172:175], v[160:167], v255\n\t"
            "ds_read_b64 v[156:157], v253 offset:1800\n\t"
            "ds_read_b64 v[158:159], v253 offset:1896\n\t"
            "v_cvt_pk_bf16_f32 v176, v176, v177\n\t"
            "v_cvt_pk_bf16_f32 v177, v178, v179\n\t"
            "s_nop 1\n\t"
            "v_mfma_f32_4x4x4_16b_bf16 v[196:199], v[176:177], v[176:177], v[196:199]\n\t"
            "ds_read_b128 v[172:175], v254 offset:448\n\t"
            "s_waitcnt lgkmcnt(7)\n\t"
            "v_and_b32 v249, s69, v249\n\t"
            "v_cvt_pk_bf16_f32 v178, v180, v181\n\t"
            "v_cvt_pk_bf16_f32 v179, v182, v183\n\t"
            "v_mov_b64 v[180:181], 0\n\t"
            "v_mov_b64 v[182:183], 0\n\t"
            "ds_read_b64 v[160:161], v253 offset:64\n\t"
            "ds_read_b64 v[162:163], v253 offset:160\n\t"
            "ds_read_b64 v[164:165], v253 offset:1808\n\t"
            "ds_read_b64 v[166:167], v253 offset:1904\n\t"
            "ds_write_b128 v237, v[184:187]\n\t"
            "v_mov_b64 v[184:185], 0\n\t"
            "v_mov_b64 v[186:187], 0\n\t"
            "s_waitcnt lgkmcnt(10)\n\t"
            "s_nop 0\n\t"
            "v_smfmac_f32_16x16x64_bf16 v[184:187], v[168:171], v[144:151], v255\n\t"
            "ds_read_b128 v[168:171], v254 offset:512\n\t"
            "v_mfma_f32_4x4x4_16b_bf16 v[196:199], v[178:179], v[178:179], v[196:199]\n\t"
            "ds_read_b64 v[144:145], v253 offset:72\n\t"
            "v_mfma_f32_16x16x32_bf16 v[192:195], v[238:241], v[176:179], v[192:195]\n\t"
            "ds_read_b64 v[146:147], v253 offset:168\n\t"
            "s_waitcnt lgkmcnt(8)\n\t"
            "v_smfmac_f32_16x16x64_bf16 v[188:191], v[172:175], v[152:159], v255\n\t"
            "ds_read_b64 v[148:149], v253 offset:1816\n\t"
            "ds_read_b64 v[150:151], v253 offset:1912\n\t"
            "v_cvt_pk_bf16_f32 v184, v184, v185\n\t"
            "v_cvt_pk_bf16_f32 v185, v186, v187\n\t"
            "s_nop 1\n\t"
            "v_mfma_f32_4x4x4_16b_bf16 v[196:199], v[184:185], v[184:185], v[196:199]\n\t"
            "ds_read_b128 v[172:175], v254 offset:576\n\t"
            "ds_read_b64 v[152:153], v253 offset:80\n\t"
            "ds_read_b64 v[154:155], v253 offset:176\n\t"
            "v_cvt_pk_bf16_f32 v186, v188, v189\n\t"
            "v_cvt_pk_bf16_f32 v187, v190, v191\n\t"
            "v_mov_b64 v[188:189], 0\n\t"
            "v_mov_b64 v[190:191], 0\n\t"
            "ds_read_b64 v[156:157], v253 offset:1824\n\t"
            "ds_read_b64 v[158:159], v253 offset:1920\n\t"
            "s_waitcnt lgkmcnt(14)\n\t"
            "ds_write_b128 v236, v[176:179] offset:128\n\t"
            "v_mov_b64 v[176:177], 0\n\t"
            "v_mov_b64 v[178:179], 0\n\t"
            "s_waitcnt lgkmcnt(10)\n\t"
            "s_nop 0\n\t"
            "v_smfmac_f32_16x16x64_bf16 v[176:179], v[168:171], v[160:167], v255\n\t"
            "ds_read_b128 v[168:171], v254 offset:640\n\t"
            "v_mfma_f32_4x4x4_16b_bf16 v[196:199], v[186:187], v[186:187], v[196:199]\n\t"
            "ds_read_b64 v[160:161], v253 offset:88\n\t"
            "v_mfma_f32_16x16x32_bf16 v[192:195], v[238:241], v[184:187], v[192:195]\n\t"
            "ds_read_b64 v[162:163], v253 offset:184\n\t"
            "s_waitcnt lgkmcnt(8)\n\t"
            "v_smfmac_f32_16x16x64_bf16 v[180:183], v[172:175], v[144:151], v255\n\t"
            "ds_read_b64 v[164:165], v253 offset:1832\n\t"
            "ds_read_b64 v[166:167], v253 offset:1928\n\t"
            "v_cvt_pk_bf16_f32 v176, v176, v177\n\t"
            "v_cvt_pk_bf16_f32 v177, v178, v179\n\t"
            "s_nop 1\n\t"
            "v_mfma_f32_4x4x4_16b_bf16 v[196:199], v[176:177], v[176:177], v[196:199]\n\t"
            "ds_read_b128 v[172:175], v254 offset:704\n\t"
            "ds_read_b128 v[144:147], v232 offset:6144\n\t"
            "ds_read_b128 v[148:151], v233 offset:6144\n\t"
            "v_cvt_pk_bf16_f32 v178, v180, v181\n\t"
            "v_cvt_pk_bf16_f32 v179, v182, v183\n\t"
            "ds_read_b128 v[180:183], v233 offset:12288\n\t"
            "ds_write_b128 v237, v[184:187] offset:128\n\t"
            "v_mov_b64 v[184:185], 0\n\t"
            "v_mov_b64 v[186:187], 0\n\t"
            "s_waitcnt lgkmcnt(9)\n\t"
            "s_nop 0\n\t"
            "v_smfmac_f32_16x16x64_bf16 v[184:187], v[168:171], v[152:159], v255\n\t"
            "ds_read_b128 v[152:155], v232 offset:6272\n\t"
            "v_mfma_f32_4x4x4_16b_bf16 v[196:199], v[178:179], v[178:179], v[196:199]\n\t"
            "ds_read_b128 v[156:159], v233 offset:6272\n\t"
            "v_mfma_f32_16x16x32_bf16 v[192:195], v[238:241], v[176:179], v[192:195]\n\t"
            "ds_write_b128 v236, v[176:179] offset:256\n\t"
            "s_waitcnt lgkmcnt(7)\n\t"
            "v_smfmac_f32_16x16x64_bf16 v[188:191], v[172:175], v[160:167], v255\n\t"
            "ds_read_b128 v[160:163], v232 offset:6400\n\t"
            "ds_read_b128 v[164:167], v233 offset:6400\n\t"
            "v_cvt_pk_bf16_f32 v184, v184, v185\n\t"
            "v_cvt_pk_bf16_f32 v185, v186, v187\n\t"
            "s_nop 1\n\t"
            "v_mfma_f32_4x4x4_16b_bf16 v[196:199], v[184:185], v[184:185], v[196:199]\n\t"
            "ds_read_b128 v[176:179], v232 offset:12288\n\t"
            "s_mov_b32 exec_hi, 0x3fffff\n\t"
            "s_add_u32 m0, s66, 0\n\t"
            "s_add_u32 s49, s79, 0\n\t"
            "buffer_load_dwordx4 v249, s[72:75], s49 offen lds\n\t"
            "s_mov_b32 exec_hi, -1\n\t"
            "v_cvt_pk_bf16_f32 v186, v188, v189\n\t"
            "v_cvt_pk_bf16_f32 v187, v190, v191\n\t"
            "s_nop 1\n\t"
            "v_mfma_f32_4x4x4_16b_bf16 v[196:199], v[186:187], v[186:187], v[196:199]\n\t"
            "ds_read_b128 v[188:191], v233 offset:12416\n\t"
            "v_mfma_f32_16x16x32_bf16 v[192:195], v[238:241], v[184:187], v[192:195]\n\t"
            "ds_write_b128 v237, v[184:187] offset:256\n\t"
            "s_waitcnt lgkmcnt(11)\n\t"
            "v_mfma_f32_16x16x32_bf16 v[224:227], v[144:147], v[0:3], 0\n\t"
            "s_setprio 3\n\t"
            "ds_read_b128 v[184:187], v232 offset:12416\n\t"
            "s_waitcnt lgkmcnt(11)\n\t"
            "v_mfma_f32_16x16x32_bf16 v[224:227], v[148:151], v[4:7], v[224:227]\n\t"
            "s_mov_b32 exec_hi, 0x3fffff\n\t"
            "s_add_u32 m0, s66, 872\n\t"
            "s_add_u32 s49, s68, 196224\n\t"
            "buffer_load_dwordx4 v249, s[72:75], s49 offen lds\n\t"
            "s_mov_b32 exec_hi, -1\n\t"
            "s_waitcnt lgkmcnt(8)\n\t"
            "v_mfma_f32_16x16x32_bf16 v[224:227], v[152:155], v[8:11], v[224:227]\n\t"
            "v_cndmask_b32_e64 v193, v196, v197, s[60:61]\n\t"
            "v_cndmask_b32_e64 v193, v193, v198, s[62:63]\n\t"
            "s_waitcnt lgkmcnt(7)\n\t"
            "v_mfma_f32_16x16x32_bf16 v[224:227], v[156:159], v[12:15], v[224:227]\n\t"
            "v_cndmask_b32_e64 v193, v193, v199, s[64:65]\n\t"
            "s_waitcnt lgkmcnt(5)\n\t"
            "v_mfma_f32_16x16x32_bf16 v[224:227], v[160:163], v[16:19], v[224:227]\n\t"
            "ds_read_b128 v[196:199], v233 offset:12544\n\t"
            "s_waitcnt lgkmcnt(5)\n\t"
            "v_mfma_f32_16x16x32_bf16 v[224:227], v[164:167], v[20:23], v[224:227]\n\t"
            "ds_write_b64 v248, v[192:193] offset:18816\n\t"
            "v_mfma_f32_16x16x32_bf16 v[168:171], v[144:147], v[24:27], 0\n\t"
            "ds_read_b128 v[192:195], v232 offset:12544\n\t"
            "v_add_u32 v232, s67, v242\n\t"
            "v_mfma_f32_16x16x32_bf16 v[168:171], v[148:151], v[28:31], v[168:171]\n\t"
            "s_mov_b32 exec_hi, 0x3fffff\n\t"
            "s_add_u32 m0, s66, 1744\n\t"
            "s_add_u32 s49, s68, 392448\n\t"
            "buffer_load_dwordx4 v249, s[72:75], s49 offen lds\n\t"
            "s_mov_b32 exec_hi, -1\n\t"
            "v_mfma_f32_16x16x32_bf16 v[168:171], v[152:155], v[32:35], v[168:171]\n\t"
            "v_mul_f32 v224, v228, v224\n\t"
            "v_mul_f32 v225, v229, v225\n\t"
            "v_mfma_f32_16x16x32_bf16 v[168:171], v[156:159], v[36:39], v[168:171]\n\t"
            "v_mul_f32 v226, v230, v226\n\t"
            "v_mul_f32 v227, v231, v227\n\t"
            "v_mfma_f32_16x16x32_bf16 v[168:171], v[160:163], v[40:43], v[168:171]\n\t"
            "v_cvt_pk_bf16_f32 v202, v224, v225\n\t"
            "v_cvt_pk_bf16_f32 v203, v226, v227\n\t"
            "v_mfma_f32_16x16x32_bf16 v[168:171], v[164:167], v[44:47], v[168:171]\n\t"
            "s_mov_b32 exec_hi, 0x3fffff\n\t"
            "s_add_u32 m0, s66, 2616\n\t"
            "s_add_u32 s49, s68, 588672\n\t"
            "buffer_load_dwordx4 v249, s[72:75], s49 offen lds\n\t"
            "s_mov_b32 exec_hi, -1\n\t"
            "v_mfma_f32_16x16x32_bf16 v[172:175], v[144:147], v[48:51], 0\n\t"
            "s_mov_b32 exec_hi, 0x3fffff\n\t"
            "s_add_u32 m0, s66, 3488\n\t"
            "s_add_u32 s49, s68, 784896\n\t"
            "buffer_load_dwordx4 v249, s[72:75], s49 offen lds\n\t"
            "s_mov_b32 exec_hi, -1\n\t"
            "v_mfma_f32_16x16x32_bf16 v[172:175], v[148:151], v[52:55], v[172:175]\n\t"
            "v_mul_f32 v168, v228, v168\n\t"
            "v_mul_f32 v169, v229, v169\n\t"
            "v_mfma_f32_16x16x32_bf16 v[172:175], v[152:155], v[56:59], v[172:175]\n\t"
            "v_mul_f32 v170, v230, v170\n\t"
            "v_mul_f32 v171, v231, v171\n\t"
            "v_mfma_f32_16x16x32_bf16 v[172:175], v[156:159], v[60:63], v[172:175]\n\t"
            "v_cvt_pk_bf16_f32 v206, v168, v169\n\t"
            "v_cvt_pk_bf16_f32 v207, v170, v171\n\t"
            "v_mfma_f32_16x16x32_bf16 v[172:175], v[160:163], v[64:67], v[172:175]\n\t"
            "s_mov_b32 exec_hi, 0x3fffff\n\t"
            "s_add_u32 m0, s66, 4360\n\t"
            "s_add_u32 s49, s68, 981120\n\t"
            "buffer_load_dwordx4 v249, s[72:75], s49 offen lds\n\t"
            "s_mov_b32 exec_hi, -1\n\t"
            "v_mfma_f32_16x16x32_bf16 v[172:175], v[164:167], v[68:71], v[172:175]\n\t"
            "s_mov_b32 exec_hi, 0x3fffff\n\t"
            "s_add_u32 m0, s66, 5232\n\t"
            "s_add_u32 s49, s68, 1177344\n\t"
            "buffer_load_dwordx4 v249, s[72:75], s49 offen lds\n\t"
            "s_mov_b32 exec_hi, -1\n\t"
            "v_mfma_f32_16x16x32_bf16 v[224:227], v[144:147], v[72:75], 0\n\t"
            "s_mov_b32 exec_hi, 0x3fffff\n\t"
            "s_add_u32 m0, s66, 6104\n\t"
            "s_add_u32 s49, s68, 1373568\n\t"
            "buffer_load_dwordx4 v249, s[72:75], s49 offen lds\n\t"
            "s_mov_b32 exec_hi, -1\n\t"
            "v_mfma_f32_16x16x32_bf16 v[224:227], v[148:151], v[76:79], v[224:227]\n\t"
            "v_mul_f32 v172, v228, v172\n\t"
            "v_mul_f32 v173, v229, v173\n\t"
            "v_mfma_f32_16x16x32_bf16 v[224:227], v[152:155], v[80:83], v[224:227]\n\t"
            "v_mul_f32 v174, v230, v174\n\t"
            "v_mul_f32 v175, v231, v175\n\t"
            "v_mfma_f32_16x16x32_bf16 v[224:227], v[156:159], v[84:87], v[224:227]\n\t"
            "v_cvt_pk_bf16_f32 v210, v172, v173\n\t"
            "v_cvt_pk_bf16_f32 v211, v174, v175\n\t"
            "v_mfma_f32_16x16x32_bf16 v[224:227], v[160:163], v[88:91], v[224:227]\n\t"
            "s_mov_b32 exec_hi, 0x3fffff\n\t"
            "s_add_u32 m0, s66, 6976\n\t"
            "s_add_u32 s49, s68, 1569792\n\t"
            "buffer_load_dwordx4 v249, s[72:75], s49 offen lds\n\t"
            "s_mov_b32 exec_hi, -1\n\t"
            "v_mfma_f32_16x16x32_bf16 v[224:227], v[164:167], v[92:95], v[224:227]\n\t"
            "s_add_u32 s45, s46, s27\n\t"
            "s_mov_b32 s46, s47\n\t"
            "s_mov_b32 s47, s68\n\t"
            "v_mfma_f32_16x16x32_bf16 v[168:171], v[144:147], v[96:99], 0\n\t"
            "s_xor_b32 s48, s48, s71\n\t"
            "s_xor_b32 s67, s67, s71\n\t"
            "s_add_u32 s76, s76, 4\n\t"
            "v_mfma_f32_16x16x32_bf16 v[168:171], v[148:151], v[100:103], v[168:171]\n\t"
            "s_lshl_b32 s56, s56, 2\n\t"
            "s_lshr_b32 s49, s26, 30\n\t"
            "s_or_b32 s56, s56, s49\n\t"
            "v_mfma_f32_16x16x32_bf16 v[168:171], v[152:155], v[104:107], v[168:171]\n\t"
            "v_mul_f32 v224, v228, v224\n\t"
            "v_mul_f32 v225, v229, v225\n\t"
            "v_mfma_f32_16x16x32_bf16 v[168:171], v[156:159], v[108:111], v[168:171]\n\t"
            "v_mul_f32 v226, v230, v226\n\t"
            "v_mul_f32 v227, v231, v227\n\t"
            "v_mfma_f32_16x16x32_bf16 v[168:171], v[160:163], v[112:115], v[168:171]\n\t"
            "v_cvt_pk_bf16_f32 v214, v224, v225\n\t"
            "v_cvt_pk_bf16_f32 v215, v226, v227\n\t"
            "v_mfma_f32_16x16x32_bf16 v[168:171], v[164:167], v[116:119], v[168:171]\n\t"
            "ds_write_b128 v243, v[200:203]\n\t"
            "v_mfma_f32_16x16x32_bf16 v[172:175], v[144:147], v[120:123], 0\n\t"
            "ds_write_b128 v243, v[204:207] offset:1536\n\t"
            "v_mfma_f32_16x16x32_bf16 v[172:175], v[148:151], v[124:127], v[172:175]\n\t"
            "ds_write_b128 v243, v[208:211] offset:3072\n\t"
            "v_mfma_f32_16x16x32_bf16 v[172:175], v[152:155], v[128:131], v[172:175]\n\t"
            "ds_write_b128 v243, v[212:215] offset:4608\n\t"
            "v_mfma_f32_16x16x32_bf16 v[172:175], v[156:159], v[132:135], v[172:175]\n\t"
            "v_mul_f32 v168, v228, v168\n\t"
            "v_mul_f32 v169, v229, v169\n\t"
            "v_mfma_f32_16x16x32_bf16 v[172:175], v[160:163], v[136:139], v[172:175]\n\t"
            "v_mul_f32 v170, v230, v170\n\t"
            "v_mul_f32 v171, v231, v171\n\t"
            "v_mfma_f32_16x16x32_bf16 v[172:175], v[164:167], v[140:143], v[172:175]\n\t"
            "v_cvt_pk_bf16_f32 v218, v168, v169\n\t"
            "v_cvt_pk_bf16_f32 v219, v170, v171\n\t"
            "s_waitcnt lgkmcnt(10)\n\t"
            "v_mfma_f32_16x16x32_bf16 v[224:227], v[176:179], v[0:3], 0\n\t"
            "ds_write_b128 v243, v[216:219] offset:6144\n\t"
            "v_mfma_f32_16x16x32_bf16 v[224:227], v[180:183], v[4:7], v[224:227]\n\t"
            "v_xor_b32 v233, 64, v232\n\t"
            "s_waitcnt lgkmcnt(8)\n\t"
            "v_mfma_f32_16x16x32_bf16 v[224:227], v[184:187], v[8:11], v[224:227]\n\t"
            "v_mul_f32 v172, v228, v172\n\t"
            "v_mul_f32 v173, v229, v173\n\t"
            "v_mfma_f32_16x16x32_bf16 v[224:227], v[188:191], v[12:15], v[224:227]\n\t"
            "v_mul_f32 v174, v230, v174\n\t"
            "v_mul_f32 v175, v231, v175\n\t"
            "s_waitcnt lgkmcnt(5)\n\t"
            "v_mfma_f32_16x16x32_bf16 v[224:227], v[192:195], v[16:19], v[224:227]\n\t"
            "ds_read_b128 v[228:231], v245 offset:128\n\t"
            "v_cvt_pk_bf16_f32 v222, v172, v173\n\t"
            "v_mfma_f32_16x16x32_bf16 v[224:227], v[196:199], v[20:23], v[224:227]\n\t"
            "v_cvt_pk_bf16_f32 v223, v174, v175\n\t"
            "v_mfma_f32_16x16x32_bf16 v[168:171], v[176:179], v[24:27], 0\n\t"
            "ds_write_b128 v243, v[220:223] offset:7680\n\t"
            "v_mfma_f32_16x16x32_bf16 v[168:171], v[180:183], v[28:31], v[168:171]\n\t"
            "v_mfma_f32_16x16x32_bf16 v[168:171], v[184:187], v[32:35], v[168:171]\n\t"
            "v_mfma_f32_16x16x32_bf16 v[168:171], v[188:191], v[36:39], v[168:171]\n\t"
            "v_mfma_f32_16x16x32_bf16 v[168:171], v[192:195], v[40:43], v[168:171]\n\t"
            "v_mfma_f32_16x16x32_bf16 v[168:171], v[196:199], v[44:47], v[168:171]\n\t"
            "s_waitcnt lgkmcnt(1)\n\t"
            "v_mul_f32 v224, v228, v224\n\t"
            "v_mfma_f32_16x16x32_bf16 v[172:175], v[176:179], v[48:51], 0\n\t"
            "v_mul_f32 v225, v229, v225\n\t"
            "v_mul_f32 v226, v230, v226\n\t"
            "v_mfma_f32_16x16x32_bf16 v[172:175], v[180:183], v[52:55], v[172:175]\n\t"
            "v_mul_f32 v227, v231, v227\n\t"
            "v_cvt_pk_bf16_f32 v224, v224, v225\n\t"
            "v_mfma_f32_16x16x32_bf16 v[172:175], v[184:187], v[56:59], v[172:175]\n\t"
            "v_cvt_pk_bf16_f32 v225, v226, v227\n\t"
            "v_mul_f32 v168, v228, v168\n\t"
            "v_mfma_f32_16x16x32_bf16 v[172:175], v[188:191], v[60:63], v[172:175]\n\t"
            "ds_write_b64 v244, v[224:225]\n\t"
            "v_mfma_f32_16x16x32_bf16 v[172:175], v[192:195], v[64:67], v[172:175]\n\t"
            "v_mul_f32 v169, v229, v169\n\t"
            "v_mul_f32 v170, v230, v170\n\t"
            "v_mfma_f32_16x16x32_bf16 v[172:175], v[196:199], v[68:71], v[172:175]\n\t"
            "v_mul_f32 v171, v231, v171\n\t"
            "v_cvt_pk_bf16_f32 v168, v168, v169\n\t"
            "v_mfma_f32_16x16x32_bf16 v[224:227], v[176:179], v[72:75], 0\n\t"
            "v_cvt_pk_bf16_f32 v169, v170, v171\n\t"
            "v_mfma_f32_16x16x32_bf16 v[224:227], v[180:183], v[76:79], v[224:227]\n\t"
            "ds_write_b64 v244, v[168:169] offset:1536\n\t"
            "v_mfma_f32_16x16x32_bf16 v[224:227], v[184:187], v[80:83], v[224:227]\n\t"
            "s_nop 0\n\t"
            "v_mul_f32 v172, v228, v172\n\t"
            "v_mul_f32 v173, v229, v173\n\t"
            "v_mfma_f32_16x16x32_bf16 v[224:227], v[188:191], v[84:87], v[224:227]\n\t"
            "v_mul_f32 v174, v230, v174\n\t"
            "v_mul_f32 v175, v231, v175\n\t"
            "v_mfma_f32_16x16x32_bf16 v[224:227], v[192:195], v[88:91], v[224:227]\n\t"
            "v_cvt_pk_bf16_f32 v172, v172, v173\n\t"
            "v_cvt_pk_bf16_f32 v173, v174, v175\n\t"
            "v_mfma_f32_16x16x32_bf16 v[224:227], v[196:199], v[92:95], v[224:227]\n\t"
            "ds_write_b64 v244, v[172:173] offset:3072\n\t"
            "v_mfma_f32_16x16x32_bf16 v[168:171], v[176:179], v[96:99], 0\n\t"
            "v_mfma_f32_16x16x32_bf16 v[168:171], v[180:183], v[100:103], v[168:171]\n\t"
            "v_mfma_f32_16x16x32_bf16 v[168:171], v[184:187], v[104:107], v[168:171]\n\t"
            "v_mfma_f32_16x16x32_bf16 v[168:171], v[188:191], v[108:111], v[168:171]\n\t"
            "v_mfma_f32_16x16x32_bf16 v[168:171], v[192:195], v[112:115], v[168:171]\n\t"
            "s_nop 1\n\t"
            "v_mul_f32 v224, v228, v224\n\t"
            "v_mfma_f32_16x16x32_bf16 v[168:171], v[196:199], v[116:119], v[168:171]\n\t"
            "v_mul_f32 v225, v229, v225\n\t"
            "v_mul_f32 v226, v230, v226\n\t"
            "v_mfma_f32_16x16x32_bf16 v[172:175], v[176:179], v[120:123], 0\n\t"
            "v_mul_f32 v227, v231, v227\n\t"
            "v_cvt_pk_bf16_f32 v224, v224, v225\n\t"
            "v_mfma_f32_16x16x32_bf16 v[172:175], v[180:183], v[124:127], v[172:175]\n\t"
            "v_cvt_pk_bf16_f32 v225, v226, v227\n\t"
            "v_mfma_f32_16x16x32_bf16 v[172:175], v[184:187], v[128:131], v[172:175]\n\t"
            "v_mul_f32 v168, v228, v168\n\t"
            "v_mul_f32 v169, v229, v169\n\t"
            "v_mfma_f32_16x16x32_bf16 v[172:175], v[188:191], v[132:135], v[172:175]\n\t"
            "v_mul_f32 v170, v230, v170\n\t"
            "v_mul_f32 v171, v231, v171\n\t"
            "v_mfma_f32_16x16x32_bf16 v[172:175], v[192:195], v[136:139], v[172:175]\n\t"
            "v_cvt_pk_bf16_f32 v168, v168, v169\n\t"
            "v_cvt_pk_bf16_f32 v169, v170, v171\n\t"
            "v_mfma_f32_16x16x32_bf16 v[172:175], v[196:199], v[140:143], v[172:175]\n\t"
            "ds_write_b64 v244, v[224:225] offset:4608\n\t"
            "ds_write_b64 v244, v[168:169] offset:6144\n\t"
            "ds_read_b128 v[168:171], v254\n\t"
            "s_nop 4\n\t"
            "v_mul_f32 v172, v228, v172\n\t"
            "v_mul_f32 v173, v229, v173\n\t"
            "v_mul_f32 v174, v230, v174\n\t"
            "v_mul_f32 v175, v231, v175\n\t"
            "v_cvt_pk_bf16_f32 v172, v172, v173\n\t"
            "v_cvt_pk_bf16_f32 v173, v174, v175\n\t"
            "ds_write_b64 v244, v[172:173] offset:7680\n\t"
            "ds_read_b128 v[172:175], v254 offset:64\n\t"
            "s_waitcnt lgkmcnt(1)\n\t"
            "s_cmp_lt_u32 s46, 0x7000000\n\t"
            "s_barrier\n\t"
            "s_cbranch_scc0 .Lvx%=\n\t"
            "ds_read_b128 v[144:147], v232\n\t"
            "ds_read_b128 v[148:151], v233\n\t"
            "ds_read_b128 v[152:155], v232 offset:128\n\t"
            "ds_read_b128 v[156:159], v233 offset:128\n\t"
            "ds_read_b128 v[160:163], v232 offset:256\n\t"
            "ds_read_b128 v[164:167], v233 offset:256\n\t"
            "ds_read_b128 v[176:179], v234 offset:18816\n\t"
            "ds_read_b128 v[180:183], v234 offset:19840\n\t"
            "s_waitcnt lgkmcnt(7)\n\t"
            "v_mfma_f32_16x16x32_bf16 v[200:203], v[144:147], v[0:3], 0\n\t"
            "s_waitcnt lgkmcnt(6)\n\t"
            "v_mfma_f32_16x16x32_bf16 v[200:203], v[148:151], v[4:7], v[200:203]\n\t"
            "s_waitcnt lgkmcnt(5)\n\t"
            "v_mfma_f32_16x16x32_bf16 v[200:203], v[152:155], v[8:11], v[200:203]\n\t"
            "s_waitcnt lgkmcnt(4)\n\t"
            "v_mfma_f32_16x16x32_bf16 v[200:203], v[156:159], v[12:15], v[200:203]\n\t"
            "s_waitcnt lgkmcnt(1)\n\t"
            "v_add_f32 v176, v176, v178\n\t"
            "s_waitcnt lgkmcnt(0)\n\t"
            "v_add_f32 v180, v180, v182\n\t"
            "v_mfma_f32_16x16x32_bf16 v[200:203], v[160:163], v[16:19], v[200:203]\n\t"
            "v_add_f32 v176, v176, v180\n\t"
            "v_add_f32 v177, v177, v179\n\t"
            "v_mfma_f32_16x16x32_bf16 v[200:203], v[164:167], v[20:23], v[200:203]\n\t"
            "v_add_f32 v181, v181, v183\n\t"
            "v_mul_f32 v176, 0x3baaaaab, v176\n\t"
            "v_mfma_f32_16x16x32_bf16 v[204:207], v[144:147], v[24:27], 0\n\t"
            "v_add_f32 v177, v177, v181\n\t"
            "v_mul_f32_e64 v176, v176, -v176\n\t"
            "v_mfma_f32_16x16x32_bf16 v[204:207], v[148:151], v[28:31], v[204:207]\n\t"
            "v_fmac_f32 v176, 0x3baaaaab, v177\n\t"
            "v_max_f32 v176, 0, v176\n\t"
            "v_mfma_f32_16x16x32_bf16 v[204:207], v[152:155], v[32:35], v[204:207]\n\t"
            "v_add_f32 v176, 0x3727c5ac, v176\n\t"
            "v_rsq_f32 v176, v176\n\t"
            "v_mfma_f32_16x16x32_bf16 v[204:207], v[156:159], v[36:39], v[204:207]\n\t"
            "ds_write_b32 v246, v176\n\t"
            "v_mfma_f32_16x16x32_bf16 v[204:207], v[160:163], v[40:43], v[204:207]\n\t"
            "ds_read_b128 v[228:231], v245\n\t"
            "v_mfma_f32_16x16x32_bf16 v[204:207], v[164:167], v[44:47], v[204:207]\n\t"
            "v_mfma_f32_16x16x32_bf16 v[208:211], v[144:147], v[48:51], 0\n\t"
            "v_mfma_f32_16x16x32_bf16 v[208:211], v[148:151], v[52:55], v[208:211]\n\t"
            "v_mfma_f32_16x16x32_bf16 v[208:211], v[152:155], v[56:59], v[208:211]\n\t"
            "v_mfma_f32_16x16x32_bf16 v[208:211], v[156:159], v[60:63], v[208:211]\n\t"
            "v_mfma_f32_16x16x32_bf16 v[208:211], v[160:163], v[64:67], v[208:211]\n\t"
            "v_mfma_f32_16x16x32_bf16 v[208:211], v[164:167], v[68:71], v[208:211]\n\t"
            "v_mfma_f32_16x16x32_bf16 v[212:215], v[144:147], v[72:75], 0\n\t"
            "s_waitcnt lgkmcnt(0)\n\t"
            "v_mul_f32 v200, v228, v200\n\t"
            "v_mul_f32 v201, v229, v201\n\t"
            "v_mfma_f32_16x16x32_bf16 v[212:215], v[148:151], v[76:79], v[212:215]\n\t"
            "v_mul_f32 v204, v228, v204\n\t"
            "v_mul_f32 v205, v229, v205\n\t"
            "v_mfma_f32_16x16x32_bf16 v[212:215], v[152:155], v[80:83], v[212:215]\n\t"
            "v_mul_f32 v208, v228, v208\n\t"
            "v_mul_f32 v209, v229, v209\n\t"
            "v_mfma_f32_16x16x32_bf16 v[212:215], v[156:159], v[84:87], v[212:215]\n\t"
            "v_mul_f32 v202, v230, v202\n\t"
            "v_mul_f32 v203, v231, v203\n\t"
            "v_mfma_f32_16x16x32_bf16 v[212:215], v[160:163], v[88:91], v[212:215]\n\t"
            "v_cvt_pk_bf16_f32 v200, v200, v201\n\t"
            "v_mul_f32 v206, v230, v206\n\t"
            "v_mfma_f32_16x16x32_bf16 v[212:215], v[164:167], v[92:95], v[212:215]\n\t"
            "v_mul_f32 v207, v231, v207\n\t"
            "v_cvt_pk_bf16_f32 v204, v204, v205\n\t"
            "v_mfma_f32_16x16x32_bf16 v[216:219], v[144:147], v[96:99], 0\n\t"
            "s_waitcnt vmcnt(0)\n\t"
            "v_mul_f32 v210, v230, v210\n\t"
            "v_mul_f32 v211, v231, v211\n\t"
            "v_mfma_f32_16x16x32_bf16 v[216:219], v[148:151], v[100:103], v[216:219]\n\t"
            "s_nop 0\n\t"
            "v_mul_f32 v212, v228, v212\n\t"
            "v_mul_f32 v213, v229, v213\n\t"
            "v_mfma_f32_16x16x32_bf16 v[216:219], v[152:155], v[104:107], v[216:219]\n\t"
            "v_cvt_pk_bf16_f32 v208, v208, v209\n\t"
            "v_mul_f32 v214, v230, v214\n\t"
            "v_mfma_f32_16x16x32_bf16 v[216:219], v[156:159], v[108:111], v[216:219]\n\t"
            "v_mul_f32 v215, v231, v215\n\t"
            "v_cvt_pk_bf16_f32 v212, v212, v213\n\t"
            "v_mfma_f32_16x16x32_bf16 v[216:219], v[160:163], v[112:115], v[216:219]\n\t"
            "v_cvt_pk_bf16_f32 v201, v202, v203\n\t"
            "v_cvt_pk_bf16_f32 v205, v206, v207\n\t"
            "v_mfma_f32_16x16x32_bf16 v[216:219], v[164:167], v[116:119], v[216:219]\n\t"
            "v_cvt_pk_bf16_f32 v209, v210, v211\n\t"
            "v_cvt_pk_bf16_f32 v213, v214, v215\n\t"
            "v_mfma_f32_16x16x32_bf16 v[220:223], v[144:147], v[120:123], 0\n\t"
            "ds_read_b64 v[144:145], v253\n\t"
            "v_mfma_f32_16x16x32_bf16 v[220:223], v[148:151], v[124:127], v[220:223]\n\t"
            "ds_read_b64 v[146:147], v253 offset:96\n\t"
            "v_mfma_f32_16x16x32_bf16 v[220:223], v[152:155], v[128:131], v[220:223]\n\t"
            "ds_read_b64 v[148:149], v253 offset:1744\n\t"
            "v_mul_f32 v216, v228, v216\n\t"
            "v_mfma_f32_16x16x32_bf16 v[220:223], v[156:159], v[132:135], v[220:223]\n\t"
            "ds_read_b64 v[150:151], v253 offset:1840\n\t"
            "v_mul_f32 v217, v229, v217\n\t"
            "v_mfma_f32_16x16x32_bf16 v[220:223], v[160:163], v[136:139], v[220:223]\n\t"
            "ds_read_b64 v[152:153], v253 offset:8\n\t"
            "v_mul_f32 v218, v230, v218\n\t"
            "v_mfma_f32_16x16x32_bf16 v[220:223], v[164:167], v[140:143], v[220:223]\n\t"
            "ds_read_b64 v[154:155], v253 offset:104\n\t"
            "v_mul_f32 v219, v231, v219\n\t"
            "v_cvt_pk_bf16_f32 v216, v216, v217\n\t"
            "v_cvt_pk_bf16_f32 v217, v218, v219\n\t"
            "ds_read_b64 v[156:157], v253 offset:1752\n\t"
            "ds_read_b64 v[158:159], v253 offset:1848\n\t"
            "ds_read_b64 v[160:161], v253 offset:16\n\t"
            "ds_read_b64 v[162:163], v253 offset:112\n\t"
            "v_mul_f32 v220, v228, v220\n\t"
            "v_mul_f32 v221, v229, v221\n\t"
            "v_mul_f32 v222, v230, v222\n\t"
            "v_mul_f32 v223, v231, v223\n\t"
            "v_cvt_pk_bf16_f32 v220, v220, v221\n\t"
            "v_cvt_pk_bf16_f32 v221, v222, v223\n\t"
            "ds_read_b64 v[164:165], v253 offset:1760\n\t"
            "ds_read_b64 v[166:167], v253 offset:1856\n\t"
            "s_branch .Lwc_loop%=\n\t"
            ".Lvx%=:\n\t"
            "s_waitcnt lgkmcnt(0)\n\t"
            "s_nop 7\n\t"
            "s_setprio 3\n\t"
            "v_add_u32 v236, s19, v252\n\t"
            "ds_read_b128 v[244:247], v236\n\t"
            "s_add_u32 s70, s33, 73728\n\t"
            "s_waitcnt lgkmcnt(0)\n\t"
            "buffer_load_dwordx4 v[200:203], v244, s[28:31], s21 offen\n\t"
            "buffer_load_dwordx4 v[204:207], v245, s[28:31], s21 offen\n\t"
            "buffer_load_dwordx4 v[208:211], v246, s[28:31], s21 offen\n\t"
            "s_add_u32 s21, s21, 6144\n\t"
            "buffer_load_dwordx4 v[212:215], v244, s[28:31], s21 offen\n\t"
            "buffer_load_dwordx4 v[216:219], v245, s[28:31], s21 offen\n\t"
            "buffer_load_dwordx4 v[220:223], v246, s[28:31], s21 offen\n\t"
            "s_add_u32 s49, s70, 0\n\t"
            "buffer_load_dwordx4 v[0:3], v247, s[28:31], s49 offen sc1\n\t"
            "s_add_u32 s49, s70, 6144\n\t"
            "buffer_load_dwordx4 v[24:27], v247, s[28:31], s49 offen sc1\n\t"
            "s_add_u32 s49, s70, 12288\n\t"
            "buffer_load_dwordx4 v[48:51], v247, s[28:31], s49 offen sc1\n\t"
            "s_add_u32 s49, s70, 18432\n\t"
            "buffer_load_dwordx4 v[72:75], v247, s[28:31], s49 offen sc1\n\t"
            "s_add_u32 s49, s70, 24576\n\t"
            "buffer_load_dwordx4 v[96:99], v247, s[28:31], s49 offen sc1\n\t"
            "s_add_u32 s49, s70, 30720\n\t"
            "buffer_load_dwordx4 v[120:123], v247, s[28:31], s49 offen sc1\n\t"
            "s_add_u32 s49, s70, 1024\n\t"
            "buffer_load_dwordx4 v[4:7], v247, s[28:31], s49 offen sc1\n\t"
            "s_add_u32 s49, s70, 7168\n\t"
            "buffer_load_dwordx4 v[28:31], v247, s[28:31], s49 offen sc1\n\t"
            "s_add_u32 s49, s70, 13312\n\t"
            "buffer_load_dwordx4 v[52:55], v247, s[28:31], s49 offen sc1\n\t"
            "s_add_u32 s49, s70, 19456\n\t"
            "buffer_load_dwordx4 v[76:79], v247, s[28:31], s49 offen sc1\n\t"
            "s_add_u32 s49, s70, 25600\n\t"
            "buffer_load_dwordx4 v[100:103], v247, s[28:31], s49 offen sc1\n\t"
            "s_add_u32 s49, s70, 31744\n\t"
            "buffer_load_dwordx4 v[124:127], v247, s[28:31], s49 offen sc1\n\t"
            "s_add_u32 s49, s70, 2048\n\t"
            "buffer_load_dwordx4 v[8:11], v247, s[28:31], s49 offen sc1\n\t"
            "s_add_u32 s49, s70, 8192\n\t"
            "buffer_load_dwordx4 v[32:35], v247, s[28:31], s49 offen sc1\n\t"
            "s_add_u32 s49, s70, 14336\n\t"
            "buffer_load_dwordx4 v[56:59], v247, s[28:31], s49 offen sc1\n\t"
            "s_add_u32 s49, s70, 20480\n\t"
            "buffer_load_dwordx4 v[80:83], v247, s[28:31], s49 offen sc1\n\t"
            "s_add_u32 s49, s70, 26624\n\t"
            "buffer_load_dwordx4 v[104:107], v247, s[28:31], s49 offen sc1\n\t"
            "s_add_u32 s49, s70, 32768\n\t"
            "buffer_load_dwordx4 v[128:131], v247, s[28:31], s49 offen sc1\n\t"
            "s_add_u32 s49, s70, 3072\n\t"
            "buffer_load_dwordx4 v[12:15], v247, s[28:31], s49 offen sc1\n\t"
            "s_add_u32 s49, s70, 9216\n\t"
            "buffer_load_dwordx4 v[36:39], v247, s[28:31], s49 offen sc1\n\t"
            "s_add_u32 s49, s70, 15360\n\t"
            "buffer_load_dwordx4 v[60:63], v247, s[28:31], s49 offen sc1\n\t"
            "s_add_u32 s49, s70, 21504\n\t"
            "buffer_load_dwordx4 v[84:87], v247, s[28:31], s49 offen sc1\n\t"
            "s_add_u32 s49, s70, 27648\n\t"
            "buffer_load_dwordx4 v[108:111], v247, s[28:31], s49 offen sc1\n\t"
            "s_add_u32 s49, s70, 33792\n\t"
            "buffer_load_dwordx4 v[132:135], v247, s[28:31], s49 offen sc1\n\t"
            "s_add_u32 s49, s70, 4096\n\t"
            "buffer_load_dwordx4 v[16:19], v247, s[28:31], s49 offen sc1\n\t"
            "s_add_u32 s49, s70, 10240\n\t"
            "buffer_load_dwordx4 v[40:43], v247, s[28:31], s49 offen sc1\n\t"
            "s_add_u32 s49, s70, 16384\n\t"
            "buffer_load_dwordx4 v[64:67], v247, s[28:31], s49 offen sc1\n\t"
            "s_add_u32 s49, s70, 22528\n\t"
            "buffer_load_dwordx4 v[88:91], v247, s[28:31], s49 offen sc1\n\t"
            "s_add_u32 s49, s70, 28672\n\t"
            "buffer_load_dwordx4 v[112:115], v247, s[28:31], s49 offen sc1\n\t"
            "s_add_u32 s49, s70, 34816\n\t"
            "buffer_load_dwordx4 v[136:139], v247, s[28:31], s49 offen sc1\n\t"
            "s_add_u32 s49, s70, 5120\n\t"
            "buffer_load_dwordx4 v[20:23], v247, s[28:31], s49 offen sc1\n\t"
            "s_add_u32 s49, s70, 11264\n\t"
            "buffer_load_dwordx4 v[44:47], v247, s[28:31], s49 offen sc1\n\t"
            "s_add_u32 s49, s70, 17408\n\t"
            "buffer_load_dwordx4 v[68:71], v247, s[28:31], s49 offen sc1\n\t"
            "s_add_u32 s49, s70, 23552\n\t"
            "buffer_load_dwordx4 v[92:95], v247, s[28:31], s49 offen sc1\n\t"
            "s_add_u32 s49, s70, 29696\n\t"
            "buffer_load_dwordx4 v[116:119], v247, s[28:31], s49 offen sc1\n\t"
            "s_add_u32 s49, s70, 35840\n\t"
            "buffer_load_dwordx4 v[140:143], v247, s[28:31], s49 offen sc1\n\t"
            "s_nop 7\n\t"
            "v_bfe_u32 v248, v247, 2, 6\n\t"
            "v_add_u32 v248, s23, v248\n\t"
            "v_bfe_u32 v249, v247, 2, 6\n\t"
            "v_add_u32 v249, s23, v249\n\t"
            "v_lshrrev_b32 v236, 4, v247\n\t"
            "v_and_b32 v236, 48, v236\n\t"
            "v_add_u32 v236, s20, v236\n\t"
            "v_add_u32 v242, s22, v242\n\t"
            "v_xor_b32 v243, 64, v242\n\t"
            "ds_read_b32 v248, v248\n\t"
            "ds_read_b32 v249, v249 offset:64\n\t"
            "s_waitcnt lgkmcnt(1)\n\t"
            "v_ashrrev_i32 v237, 31, v248\n\t"
            "v_and_b32 v237, 1177344, v237\n\t"
            "v_sub_u32 v236, v236, v237\n\t"
            "v_bfe_i32 v237, v248, 30, 1\n\t"
            "v_and_b32 v237, 2304, v237\n\t"
            "v_sub_u32 v236, v236, v237\n\t"
            "v_and_b32 v248, 0x7ffffff, v248\n\t"
            "v_add_u32 v248, v248, v236\n\t"
            "v_lshrrev_b32 v236, 4, v247\n\t"
            "v_and_b32 v236, 48, v236\n\t"
            "s_waitcnt lgkmcnt(0)\n\t"
            "v_ashrrev_i32 v237, 31, v249\n\t"
            "v_add_u32 v236, s20, v236\n\t"
            "v_and_b32 v237, 1177344, v237\n\t"
            "v_sub_u32 v236, v236, v237\n\t"
            "v_bfe_i32 v237, v249, 30, 1\n\t"
            "v_and_b32 v237, 2304, v237\n\t"
            "v_sub_u32 v236, v236, v237\n\t"
            "v_and_b32 v249, 0x7ffffff, v249\n\t"
            "v_add_u32 v249, v249, v236\n\t"
            "s_nop 7\n\t"
            "v_mov_b32 v144, s23\n\t"
            ".Ldv%=:\n\t"
            "ds_read_b128 v[148:151], v144 offset:96\n\t"
            "s_waitcnt lgkmcnt(0)\n\t"
            "v_or_b32 v148, v148, v149\n\t"
            "v_or3_b32 v148, v148, v150, v151\n\t"
            "v_cmp_gt_i32 vcc, 0, v148\n\t"
            "s_nop 4\n\t"
            "s_cbranch_vccnz .Ldv%=\n\t"
            "s_waitcnt vmcnt(0)\n\t"
            "s_nop 7\n\t"
            "ds_read_b128 v[184:187], v242 offset:53760\n\t"
            "v_lshlrev_b32 v152, 16, v200\n\t"
            "v_and_b32 v153, 0xffff0000, v200\n\t"
            "v_lshlrev_b32 v154, 16, v201\n\t"
            "v_and_b32 v155, 0xffff0000, v201\n\t"
            "v_lshlrev_b32 v156, 16, v202\n\t"
            "v_and_b32 v157, 0xffff0000, v202\n\t"
            "v_lshlrev_b32 v158, 16, v203\n\t"
            "v_and_b32 v159, 0xffff0000, v203\n\t"
            "v_lshlrev_b32 v160, 16, v204\n\t"
            "v_and_b32 v161, 0xffff0000, v204\n\t"
            "v_lshlrev_b32 v162, 16, v205\n\t"
            "v_and_b32 v163, 0xffff0000, v205\n\t"
            "v_lshlrev_b32 v164, 16, v206\n\t"
            "v_and_b32 v165, 0xffff0000, v206\n\t"
            "v_lshlrev_b32 v166, 16, v207\n\t"
            "v_and_b32 v167, 0xffff0000, v207\n\t"
            "v_lshlrev_b32 v176, 16, v208\n\t"
            "v_and_b32 v177, 0xffff0000, v208\n\t"
            "v_lshlrev_b32 v178, 16, v209\n\t"
            "v_and_b32 v179, 0xffff0000, v209\n\t"
            "v_lshlrev_b32 v180, 16, v210\n\t"
            "v_and_b32 v181, 0xffff0000, v210\n\t"
            "v_lshlrev_b32 v182, 16, v211\n\t"
            "v_and_b32 v183, 0xffff0000, v211\n\t"
            "v_lshlrev_b32 v144, 16, v212\n\t"
            "v_and_b32 v145, 0xffff0000, v212\n\t"
            "v_lshlrev_b32 v146, 16, v213\n\t"
            "v_and_b32 v147, 0xffff0000, v213\n\t"
            "v_lshlrev_b32 v148, 16, v214\n\t"
            "v_and_b32 v149, 0xffff0000, v214\n\t"
            "s_waitcnt lgkmcnt(0)\n\t"
            "v_mfma_f32_16x16x32_bf16 v[152:155], v[0:3], v[184:187], v[152:155]\n\t"
            "ds_read_b128 v[188:191], v243 offset:53760\n\t"
            "v_lshlrev_b32 v150, 16, v215\n\t"
            "v_mfma_f32_16x16x32_bf16 v[156:159], v[24:27], v[184:187], v[156:159]\n\t"
            "ds_read_b128 v[192:195], v242 offset:53888\n\t"
            "v_and_b32 v151, 0xffff0000, v215\n\t"
            "v_mfma_f32_16x16x32_bf16 v[160:163], v[48:51], v[184:187], v[160:163]\n\t"
            "v_lshlrev_b32 v168, 16, v216\n\t"
            "v_and_b32 v169, 0xffff0000, v216\n\t"
            "v_mfma_f32_16x16x32_bf16 v[164:167], v[72:75], v[184:187], v[164:167]\n\t"
            "v_lshlrev_b32 v170, 16, v217\n\t"
            "v_and_b32 v171, 0xffff0000, v217\n\t"
            "v_mfma_f32_16x16x32_bf16 v[176:179], v[96:99], v[184:187], v[176:179]\n\t"
            "v_lshlrev_b32 v172, 16, v218\n\t"
            "v_and_b32 v173, 0xffff0000, v218\n\t"
            "v_mfma_f32_16x16x32_bf16 v[180:183], v[120:123], v[184:187], v[180:183]\n\t"
            "ds_read_b128 v[184:187], v243 offset:53888\n\t"
            "v_lshlrev_b32 v174, 16, v219\n\t"
            "v_and_b32 v175, 0xffff0000, v219\n\t"
            "s_waitcnt lgkmcnt(2)\n\t"
            "v_mfma_f32_16x16x32_bf16 v[152:155], v[4:7], v[188:191], v[152:155]\n\t"
            "v_mfma_f32_16x16x32_bf16 v[156:159], v[28:31], v[188:191], v[156:159]\n\t"
            "v_mfma_f32_16x16x32_bf16 v[160:163], v[52:55], v[188:191], v[160:163]\n\t"
            "v_mfma_f32_16x16x32_bf16 v[164:167], v[76:79], v[188:191], v[164:167]\n\t"
            "v_mfma_f32_16x16x32_bf16 v[176:179], v[100:103], v[188:191], v[176:179]\n\t"
            "v_mfma_f32_16x16x32_bf16 v[180:183], v[124:127], v[188:191], v[180:183]\n\t"
            "ds_read_b128 v[188:191], v242 offset:54016\n\t"
            "s_waitcnt lgkmcnt(2)\n\t"
            "v_mfma_f32_16x16x32_bf16 v[152:155], v[8:11], v[192:195], v[152:155]\n\t"
            "v_mfma_f32_16x16x32_bf16 v[156:159], v[32:35], v[192:195], v[156:159]\n\t"
            "v_mfma_f32_16x16x32_bf16 v[160:163], v[56:59], v[192:195], v[160:163]\n\t"
            "v_mfma_f32_16x16x32_bf16 v[164:167], v[80:83], v[192:195], v[164:167]\n\t"
            "v_mfma_f32_16x16x32_bf16 v[176:179], v[104:107], v[192:195], v[176:179]\n\t"
            "v_mfma_f32_16x16x32_bf16 v[180:183], v[128:131], v[192:195], v[180:183]\n\t"
            "ds_read_b128 v[192:195], v243 offset:54016\n\t"
            "s_waitcnt lgkmcnt(2)\n\t"
            "v_mfma_f32_16x16x32_bf16 v[152:155], v[12:15], v[184:187], v[152:155]\n\t"
            "v_mfma_f32_16x16x32_bf16 v[156:159], v[36:39], v[184:187], v[156:159]\n\t"
            "v_mfma_f32_16x16x32_bf16 v[160:163], v[60:63], v[184:187], v[160:163]\n\t"
            "v_mfma_f32_16x16x32_bf16 v[164:167], v[84:87], v[184:187], v[164:167]\n\t"
            "v_mfma_f32_16x16x32_bf16 v[176:179], v[108:111], v[184:187], v[176:179]\n\t"
            "v_mfma_f32_16x16x32_bf16 v[180:183], v[132:135], v[184:187], v[180:183]\n\t"
            "ds_read_b128 v[184:187], v242 offset:59904\n\t"
            "s_waitcnt lgkmcnt(2)\n\t"
            "v_mfma_f32_16x16x32_bf16 v[152:155], v[16:19], v[188:191], v[152:155]\n\t"
            "v_mfma_f32_16x16x32_bf16 v[156:159], v[40:43], v[188:191], v[156:159]\n\t"
            "v_mfma_f32_16x16x32_bf16 v[160:163], v[64:67], v[188:191], v[160:163]\n\t"
            "v_mfma_f32_16x16x32_bf16 v[164:167], v[88:91], v[188:191], v[164:167]\n\t"
            "v_mfma_f32_16x16x32_bf16 v[176:179], v[112:115], v[188:191], v[176:179]\n\t"
            "v_mfma_f32_16x16x32_bf16 v[180:183], v[136:139], v[188:191], v[180:183]\n\t"
            "ds_read_b128 v[188:191], v243 offset:59904\n\t"
            "s_waitcnt lgkmcnt(2)\n\t"
            "v_mfma_f32_16x16x32_bf16 v[152:155], v[20:23], v[192:195], v[152:155]\n\t"
            "v_mfma_f32_16x16x32_bf16 v[156:159], v[44:47], v[192:195], v[156:159]\n\t"
            "v_mfma_f32_16x16x32_bf16 v[160:163], v[68:71], v[192:195], v[160:163]\n\t"
            "v_mfma_f32_16x16x32_bf16 v[164:167], v[92:95], v[192:195], v[164:167]\n\t"
            "v_mfma_f32_16x16x32_bf16 v[176:179], v[116:119], v[192:195], v[176:179]\n\t"
            "v_mfma_f32_16x16x32_bf16 v[180:183], v[140:143], v[192:195], v[180:183]\n\t"
            "ds_read_b128 v[192:195], v242 offset:60032\n\t"
            "s_waitcnt lgkmcnt(2)\n\t"
            "v_mfma_f32_16x16x32_bf16 v[144:147], v[0:3], v[184:187], v[144:147]\n\t"
            "v_cvt_pk_bf16_f32 v152, v152, v153\n\t"
            "v_cvt_pk_bf16_f32 v153, v154, v155\n\t"
            "v_mfma_f32_16x16x32_bf16 v[148:151], v[24:27], v[184:187], v[148:151]\n\t"
            "v_cvt_pk_bf16_f32 v160, v160, v161\n\t"
            "v_cvt_pk_bf16_f32 v161, v162, v163\n\t"
            "v_mfma_f32_16x16x32_bf16 v[168:171], v[48:51], v[184:187], v[168:171]\n\t"
            "v_cvt_pk_bf16_f32 v154, v156, v157\n\t"
            "v_cvt_pk_bf16_f32 v155, v158, v159\n\t"
            "v_mfma_f32_16x16x32_bf16 v[172:175], v[72:75], v[184:187], v[172:175]\n\t"
            "v_cvt_pk_bf16_f32 v162, v164, v165\n\t"
            "v_cvt_pk_bf16_f32 v163, v166, v167\n\t"
            "s_waitcnt lgkmcnt(1)\n\t"
            "v_mfma_f32_16x16x32_bf16 v[144:147], v[4:7], v[188:191], v[144:147]\n\t"
            "v_lshlrev_b32 v156, 16, v220\n\t"
            "v_and_b32 v157, 0xffff0000, v220\n\t"
            "v_mfma_f32_16x16x32_bf16 v[148:151], v[28:31], v[188:191], v[148:151]\n\t"
            "v_lshlrev_b32 v158, 16, v221\n\t"
            "v_and_b32 v159, 0xffff0000, v221\n\t"
            "v_mfma_f32_16x16x32_bf16 v[168:171], v[52:55], v[188:191], v[168:171]\n\t"
            "v_lshlrev_b32 v164, 16, v222\n\t"
            "v_and_b32 v165, 0xffff0000, v222\n\t"
            "v_mfma_f32_16x16x32_bf16 v[156:159], v[96:99], v[184:187], v[156:159]\n\t"
            "v_lshlrev_b32 v166, 16, v223\n\t"
            "v_and_b32 v167, 0xffff0000, v223\n\t"
            "v_mfma_f32_16x16x32_bf16 v[172:175], v[76:79], v[188:191], v[172:175]\n\t"
            "buffer_store_dwordx4 v[152:155], v248, s[40:43], 0 offen sc1\n\t"
            "v_mfma_f32_16x16x32_bf16 v[164:167], v[120:123], v[184:187], v[164:167]\n\t"
            "ds_read_b128 v[184:187], v243 offset:60032\n\t"
            "v_cvt_pk_bf16_f32 v176, v176, v177\n\t"
            "v_mfma_f32_16x16x32_bf16 v[156:159], v[100:103], v[188:191], v[156:159]\n\t"
            "buffer_store_dwordx4 v[160:163], v248, s[40:43], 0 offen offset:64 sc1\n\t"
            "v_mfma_f32_16x16x32_bf16 v[164:167], v[124:127], v[188:191], v[164:167]\n\t"
            "ds_read_b128 v[188:191], v242 offset:60160\n\t"
            "v_cvt_pk_bf16_f32 v177, v178, v179\n\t"
            "s_waitcnt lgkmcnt(2)\n\t"
            "v_mfma_f32_16x16x32_bf16 v[144:147], v[8:11], v[192:195], v[144:147]\n\t"
            "v_cvt_pk_bf16_f32 v178, v180, v181\n\t"
            "v_cvt_pk_bf16_f32 v179, v182, v183\n\t"
            "v_mfma_f32_16x16x32_bf16 v[148:151], v[32:35], v[192:195], v[148:151]\n\t"
            "buffer_store_dwordx4 v[176:179], v248, s[40:43], 0 offen offset:128 sc1\n\t"
            "v_mfma_f32_16x16x32_bf16 v[168:171], v[56:59], v[192:195], v[168:171]\n\t"
            "v_mfma_f32_16x16x32_bf16 v[172:175], v[80:83], v[192:195], v[172:175]\n\t"
            "v_mfma_f32_16x16x32_bf16 v[156:159], v[104:107], v[192:195], v[156:159]\n\t"
            "v_mfma_f32_16x16x32_bf16 v[164:167], v[128:131], v[192:195], v[164:167]\n\t"
            "ds_read_b128 v[192:195], v243 offset:60160\n\t"
            "s_waitcnt lgkmcnt(2)\n\t"
            "v_mfma_f32_16x16x32_bf16 v[144:147], v[12:15], v[184:187], v[144:147]\n\t"
            "v_mfma_f32_16x16x32_bf16 v[148:151], v[36:39], v[184:187], v[148:151]\n\t"
            "s_waitcnt lgkmcnt(1)\n\t"
            "v_mfma_f32_16x16x32_bf16 v[144:147], v[16:19], v[188:191], v[144:147]\n\t"
            "v_mfma_f32_16x16x32_bf16 v[168:171], v[60:63], v[184:187], v[168:171]\n\t"
            "v_mfma_f32_16x16x32_bf16 v[148:151], v[40:43], v[188:191], v[148:151]\n\t"
            "v_mfma_f32_16x16x32_bf16 v[172:175], v[84:87], v[184:187], v[172:175]\n\t"
            "v_mfma_f32_16x16x32_bf16 v[168:171], v[64:67], v[188:191], v[168:171]\n\t"
            "v_mfma_f32_16x16x32_bf16 v[156:159], v[108:111], v[184:187], v[156:159]\n\t"
            "s_waitcnt lgkmcnt(0)\n\t"
            "v_mfma_f32_16x16x32_bf16 v[144:147], v[20:23], v[192:195], v[144:147]\n\t"
            "v_mfma_f32_16x16x32_bf16 v[148:151], v[44:47], v[192:195], v[148:151]\n\t"
            "v_mfma_f32_16x16x32_bf16 v[172:175], v[88:91], v[188:191], v[172:175]\n\t"
            "v_mfma_f32_16x16x32_bf16 v[164:167], v[132:135], v[184:187], v[164:167]\n\t"
            "v_mfma_f32_16x16x32_bf16 v[168:171], v[68:71], v[192:195], v[168:171]\n\t"
            "v_mfma_f32_16x16x32_bf16 v[156:159], v[112:115], v[188:191], v[156:159]\n\t"
            "v_mfma_f32_16x16x32_bf16 v[172:175], v[92:95], v[192:195], v[172:175]\n\t"
            "s_nop 1\n\t"
            "v_cvt_pk_bf16_f32 v144, v144, v145\n\t"
            "v_mfma_f32_16x16x32_bf16 v[164:167], v[136:139], v[188:191], v[164:167]\n\t"
            "v_cvt_pk_bf16_f32 v145, v146, v147\n\t"
            "v_cvt_pk_bf16_f32 v146, v148, v149\n\t"
            "v_mfma_f32_16x16x32_bf16 v[156:159], v[116:119], v[192:195], v[156:159]\n\t"
            "v_cvt_pk_bf16_f32 v147, v150, v151\n\t"
            "v_cvt_pk_bf16_f32 v168, v168, v169\n\t"
            "v_mfma_f32_16x16x32_bf16 v[164:167], v[140:143], v[192:195], v[164:167]\n\t"
            "buffer_store_dwordx4 v[144:147], v249, s[40:43], 0 offen sc1\n\t"
            "v_cvt_pk_bf16_f32 v169, v170, v171\n\t"
            "v_cvt_pk_bf16_f32 v170, v172, v173\n\t"
            "v_cvt_pk_bf16_f32 v171, v174, v175\n\t"
            "s_nop 0\n\t"
            "v_cvt_pk_bf16_f32 v156, v156, v157\n\t"
            "v_cvt_pk_bf16_f32 v157, v158, v159\n\t"
            "buffer_store_dwordx4 v[168:171], v249, s[40:43], 0 offen offset:64 sc1\n\t"
            "v_cvt_pk_bf16_f32 v158, v164, v165\n\t"
            "v_cvt_pk_bf16_f32 v159, v166, v167\n\t"
            "buffer_store_dwordx4 v[156:159], v249, s[40:43], 0 offen offset:128 sc1\n\t"
            "s_nop 7\n\t"
            "s_branch .Lre%=\n\t"
            ".Lrp%=:\n\t"
            "v_add_u32 v236, s19, v252\n\t"
            "ds_write_b128 v236, v[244:247]\n\t"
            "v_add_u32 v236, s48, v244\n\t"
            "v_add_u32 v237, s48, v245\n\t"
            "v_add_u32 v248, s48, v246\n\t"
            "ds_read_b128 v[200:203], v236\n\t"
            "ds_read_b128 v[204:207], v237\n\t"
            "ds_read_b128 v[208:211], v248\n\t"
            "ds_read_b128 v[212:215], v236 offset:6144\n\t"
            "ds_read_b128 v[216:219], v237 offset:6144\n\t"
            "ds_read_b128 v[220:223], v248 offset:6144\n\t"
            "ds_read_b128 v[224:227], v236 offset:12288\n\t"
            "ds_read_b128 v[228:231], v237 offset:12288\n\t"
            "ds_read_b128 v[232:235], v248 offset:12288\n\t"
            "s_waitcnt lgkmcnt(0)\n\t"
            "s_barrier\n\t"
            "s_add_u32 s45, s46, s27\n\t"
            "s_mov_b32 s46, s47\n\t"
            "s_mov_b32 s47, s68\n\t"
            "s_xor_b32 s48, s48, s71\n\t"
            "s_xor_b32 s67, s67, s71\n\t"
            "s_add_u32 s76, s76, 4\n\t"
            "s_lshl_b32 s56, s56, 2\n\t"
            "s_lshr_b32 s49, s26, 30\n\t"
            "s_or_b32 s56, s56, s49\n\t"
            "s_cmp_ge_u32 s76, s24\n\t"
            "s_cselect_b64 vcc, -1, 0\n\t"
            "s_add_u32 s49, s33, 0\n\t"
            "buffer_load_dwordx4 v[0:3], v247, s[28:31], s49 offen sc1\n\t"
            "s_add_u32 s49, s33, 1024\n\t"
            "buffer_load_dwordx4 v[4:7], v247, s[28:31], s49 offen sc1\n\t"
            "s_add_u32 s49, s33, 2048\n\t"
            "buffer_load_dwordx4 v[8:11], v247, s[28:31], s49 offen sc1\n\t"
            "s_add_u32 s49, s33, 3072\n\t"
            "buffer_load_dwordx4 v[12:15], v247, s[28:31], s49 offen sc1\n\t"
            "s_add_u32 s49, s33, 4096\n\t"
            "buffer_load_dwordx4 v[16:19], v247, s[28:31], s49 offen sc1\n\t"
            "s_add_u32 s49, s33, 5120\n\t"
            "buffer_load_dwordx4 v[20:23], v247, s[28:31], s49 offen sc1\n\t"
            "s_add_u32 s49, s33, 6144\n\t"
            "buffer_load_dwordx4 v[24:27], v247, s[28:31], s49 offen sc1\n\t"
            "s_add_u32 s49, s33, 7168\n\t"
            "buffer_load_dwordx4 v[28:31], v247, s[28:31], s49 offen sc1\n\t"
            "s_add_u32 s49, s33, 8192\n\t"
            "buffer_load_dwordx4 v[32:35], v247, s[28:31], s49 offen sc1\n\t"
            "s_add_u32 s49, s33, 9216\n\t"
            "buffer_load_dwordx4 v[36:39], v247, s[28:31], s49 offen sc1\n\t"
            "s_add_u32 s49, s33, 10240\n\t"
            "buffer_load_dwordx4 v[40:43], v247, s[28:31], s49 offen sc1\n\t"
            "s_add_u32 s49, s33, 11264\n\t"
            "buffer_load_dwordx4 v[44:47], v247, s[28:31], s49 offen sc1\n\t"
            "s_add_u32 s49, s33, 12288\n\t"
            "buffer_load_dwordx4 v[48:51], v247, s[28:31], s49 offen sc1\n\t"
            "s_add_u32 s49, s33, 13312\n\t"
            "buffer_load_dwordx4 v[52:55], v247, s[28:31], s49 offen sc1\n\t"
            "s_add_u32 s49, s33, 14336\n\t"
            "buffer_load_dwordx4 v[56:59], v247, s[28:31], s49 offen sc1\n\t"
            "s_add_u32 s49, s33, 15360\n\t"
            "buffer_load_dwordx4 v[60:63], v247, s[28:31], s49 offen sc1\n\t"
            "s_add_u32 s49, s33, 16384\n\t"
            "buffer_load_dwordx4 v[64:67], v247, s[28:31], s49 offen sc1\n\t"
            "s_add_u32 s49, s33, 17408\n\t"
            "buffer_load_dwordx4 v[68:71], v247, s[28:31], s49 offen sc1\n\t"
            "ds_read_b128 v[168:171], v254\n\t"
            "ds_read_b128 v[172:175], v254 offset:64\n\t"
            "s_waitcnt vmcnt(0)\n\t"
            "ds_read_b64 v[144:145], v253\n\t"
            "ds_read_b64 v[146:147], v253 offset:96\n\t"
            "ds_read_b64 v[148:149], v253 offset:1744\n\t"
            "ds_read_b64 v[150:151], v253 offset:1840\n\t"
            "ds_read_b64 v[152:153], v253 offset:8\n\t"
            "ds_read_b64 v[154:155], v253 offset:104\n\t"
            "ds_read_b64 v[156:157], v253 offset:1752\n\t"
            "ds_read_b64 v[158:159], v253 offset:1848\n\t"
            "ds_read_b64 v[160:161], v253 offset:16\n\t"
            "ds_read_b64 v[162:163], v253 offset:112\n\t"
            "ds_read_b64 v[164:165], v253 offset:1760\n\t"
            "ds_read_b64 v[166:167], v253 offset:1856\n\t"
            "s_waitcnt lgkmcnt(0)\n\t"
            "s_nop 7\n\t"
            ".p2align 6\n\t"
            ".Lwb_loop%=:\n\t"
            "s_setprio 3\n\t"
            "v_mov_b64 v[176:177], 0\n\t"
            "v_mov_b64 v[178:179], 0\n\t"
            "v_mov_b32 v249, s76\n\t"
            "v_mov_b64 v[180:181], 0\n\t"
            "s_waitcnt lgkmcnt(7)\n\t"
            "v_smfmac_f32_16x16x64_bf16 v[176:179], v[168:171], v[144:151], v255\n\t"
            "ds_read_b128 v[168:171], v254 offset:128\n\t"
            "v_mov_b64 v[182:183], 0\n\t"
            "v_mov_b64 v[184:185], 0\n\t"
            "v_mov_b64 v[186:187], 0\n\t"
            "s_waitcnt lgkmcnt(5)\n\t"
            "v_smfmac_f32_16x16x64_bf16 v[180:183], v[172:175], v[152:159], v255\n\t"
            "ds_read_b32 v249, v249\n\t"
            "v_mov_b64 v[188:189], 0\n\t"
            "v_mov_b64 v[190:191], 0\n\t"
            "v_cvt_pk_bf16_f32 v176, v176, v177\n\t"
            "v_cvt_pk_bf16_f32 v177, v178, v179\n\t"
            "v_add_u32 v236, s67, v250\n\t"
            "v_add_u32 v248, s67, v251\n\t"
            "v_mfma_f32_4x4x4_16b_bf16 v[196:199], v[176:177], v[176:177], 0\n\t"
            "ds_read_b64 v[144:145], v253 offset:24\n\t"
            "v_cvt_pk_bf16_f32 v178, v180, v181\n\t"
            "v_cvt_pk_bf16_f32 v179, v182, v183\n\t"
            "s_barrier\n\t"
            "v_mov_b64 v[180:181], 0\n\t"
            "v_mov_b64 v[182:183], 0\n\t"
            "ds_read_b64 v[146:147], v253 offset:120\n\t"
            "ds_read_b64 v[148:149], v253 offset:1768\n\t"
            "ds_read_b64 v[150:151], v253 offset:1864\n\t"
            "ds_read_b128 v[172:175], v254 offset:192\n\t"
            "s_waitcnt lgkmcnt(6)\n\t"
            "v_smfmac_f32_16x16x64_bf16 v[184:187], v[168:171], v[160:167], v255\n\t"
            "ds_read_b64 v[152:153], v253 offset:32\n\t"
            "v_mfma_f32_4x4x4_16b_bf16 v[196:199], v[178:179], v[178:179], v[196:199]\n\t"
            "ds_read_b64 v[154:155], v253 offset:128\n\t"
            "v_mfma_f32_16x16x32_bf16 v[192:195], v[238:241], v[176:179], 0\n\t"
            "ds_read_b64 v[156:157], v253 offset:1776\n\t"
            "s_waitcnt lgkmcnt(8)\n\t"
            "v_readfirstlane_b32 s26, v249\n\t"
            "ds_read_b64 v[158:159], v253 offset:1872\n\t"
            "s_and_b32 s68, s26, 0x7ffffff\n\t"
            "s_add_u32 s68, s68, s78\n\t"
            "s_and_b32 s69, s26, 0x38000000\n\t"
            "s_or_b32 s69, s69, s39\n\t"
            "s_bitcmp1_b32 s26, 27\n\t"
            "s_cselect_b32 s79, 2.0, s68\n\t"
            "ds_read_b128 v[168:171], v254 offset:256\n\t"
            "s_and_b32 s57, s56, 3\n\t"
            "s_lshl_b32 s58, s57, 3\n\t"
            "s_lshr_b32 m0, s54, s58\n\t"
            "ds_read_b64 v[160:161], v253 offset:40\n\t"
            "s_lshl_b32 s58, s57, 4\n\t"
            "s_lshr_b64 s[36:37], s[50:51], s58\n\t"
            "s_and_b32 s57, s36, 0xffff\n\t"
            "s_add_u32 s57, s57, s67\n\t"
            "ds_read_b64 v[162:163], v253 offset:136\n\t"
            "s_waitcnt lgkmcnt(7)\n\t"
            "v_smfmac_f32_16x16x64_bf16 v[188:191], v[172:175], v[144:151], v255\n\t"
            "s_lshr_b64 s[36:37], s[52:53], s58\n\t"
            "s_and_b32 s58, s36, 0xffff\n\t"
            "s_add_u32 s58, s58, s67\n\t"
            "ds_read_b64 v[164:165], v253 offset:1784\n\t"
            "s_and_b32 s59, s50, 0xffff\n\t"
            "s_add_u32 s59, s59, s67\n\t"
            "s_and_b32 s38, s52, 0xffff\n\t"
            "s_add_u32 s38, s38, s67\n\t"
            "ds_read_b64 v[166:167], v253 offset:1880\n\t"
            "v_writelane_b32 v236, s59, m0\n\t"
            "v_writelane_b32 v236, s57, 63\n\t"
            "v_cvt_pk_bf16_f32 v184, v184, v185\n\t"
            "v_cvt_pk_bf16_f32 v185, v186, v187\n\t"
            "v_cvt_pk_bf16_f32 v186, v188, v189\n\t"
            "v_cvt_pk_bf16_f32 v187, v190, v191\n\t"
            "v_mfma_f32_4x4x4_16b_bf16 v[196:199], v[184:185], v[184:185], v[196:199]\n\t"
            "ds_read_b128 v[172:175], v254 offset:320\n\t"
            "v_xor_b32 v237, 16, v236\n\t"
            "v_mov_b64 v[188:189], 0\n\t"
            "v_mov_b64 v[190:191], 0\n\t"
            "v_writelane_b32 v248, s38, m0\n\t"
            "v_writelane_b32 v248, s58, 63\n\t"
            "ds_read_b64 v[144:145], v253 offset:48\n\t"
            "ds_read_b64 v[146:147], v253 offset:144\n\t"
            "ds_read_b64 v[148:149], v253 offset:1792\n\t"
            "ds_read_b64 v[150:151], v253 offset:1888\n\t"
            "ds_read_b32 v249, v252 offset:1024\n\t"
            "s_waitcnt lgkmcnt(14)\n\t"
            "ds_write_b128 v236, v[176:179]\n\t"
            "v_mov_b64 v[176:177], 0\n\t"
            "v_mov_b64 v[178:179], 0\n\t"
            "s_waitcnt lgkmcnt(11)\n\t"
            "s_nop 0\n\t"
            "v_smfmac_f32_16x16x64_bf16 v[176:179], v[168:171], v[152:159], v255\n\t"
            "ds_read_b128 v[168:171], v254 offset:384\n\t"
            "v_mfma_f32_4x4x4_16b_bf16 v[196:199], v[186:187], v[186:187], v[196:199]\n\t"
            "ds_read_b64 v[152:153], v253 offset:56\n\t"
            "v_mfma_f32_16x16x32_bf16 v[192:195], v[238:241], v[184:187], v[192:195]\n\t"
            "ds_read_b64 v[154:155], v253 offset:152\n\t"
            "s_waitcnt lgkmcnt(9)\n\t"
            "v_smfmac_f32_16x16x64_bf16 v[180:183], v[172:175], v[160:167], v255\n\t"
            "ds_read_b64 v[156:157], v253 offset:1800\n\t"
            "ds_read_b64 v[158:159], v253 offset:1896\n\t"
            "v_cvt_pk_bf16_f32 v176, v176, v177\n\t"
            "v_cvt_pk_bf16_f32 v177, v178, v179\n\t"
            "s_nop 1\n\t"
            "v_mfma_f32_4x4x4_16b_bf16 v[196:199], v[176:177], v[176:177], v[196:199]\n\t"
            "ds_read_b128 v[172:175], v254 offset:448\n\t"
            "s_waitcnt lgkmcnt(7)\n\t"
            "v_and_b32 v249, s69, v249\n\t"
            "v_cvt_pk_bf16_f32 v178, v180, v181\n\t"
            "v_cvt_pk_bf16_f32 v179, v182, v183\n\t"
            "v_mov_b64 v[180:181], 0\n\t"
            "v_mov_b64 v[182:183], 0\n\t"
            "ds_read_b64 v[160:161], v253 offset:64\n\t"
            "ds_read_b64 v[162:163], v253 offset:160\n\t"
            "ds_read_b64 v[164:165], v253 offset:1808\n\t"
            "ds_read_b64 v[166:167], v253 offset:1904\n\t"
            "ds_write_b128 v237, v[184:187]\n\t"
            "v_mov_b64 v[184:185], 0\n\t"
            "v_mov_b64 v[186:187], 0\n\t"
            "s_waitcnt lgkmcnt(10)\n\t"
            "s_nop 0\n\t"
            "v_smfmac_f32_16x16x64_bf16 v[184:187], v[168:171], v[144:151], v255\n\t"
            "ds_read_b128 v[168:171], v254 offset:512\n\t"
            "v_mfma_f32_4x4x4_16b_bf16 v[196:199], v[178:179], v[178:179], v[196:199]\n\t"
            "ds_read_b64 v[144:145], v253 offset:72\n\t"
            "v_mfma_f32_16x16x32_bf16 v[192:195], v[238:241], v[176:179], v[192:195]\n\t"
            "ds_read_b64 v[146:147], v253 offset:168\n\t"
            "s_waitcnt lgkmcnt(8)\n\t"
            "v_smfmac_f32_16x16x64_bf16 v[188:191], v[172:175], v[152:159], v255\n\t"
            "ds_read_b64 v[148:149], v253 offset:1816\n\t"
            "ds_read_b64 v[150:151], v253 offset:1912\n\t"
            "v_cvt_pk_bf16_f32 v184, v184, v185\n\t"
            "v_cvt_pk_bf16_f32 v185, v186, v187\n\t"
            "s_nop 1\n\t"
            "v_mfma_f32_4x4x4_16b_bf16 v[196:199], v[184:185], v[184:185], v[196:199]\n\t"
            "ds_read_b128 v[172:175], v254 offset:576\n\t"
            "ds_read_b64 v[152:153], v253 offset:80\n\t"
            "ds_read_b64 v[154:155], v253 offset:176\n\t"
            "v_cvt_pk_bf16_f32 v186, v188, v189\n\t"
            "v_cvt_pk_bf16_f32 v187, v190, v191\n\t"
            "v_mov_b64 v[188:189], 0\n\t"
            "v_mov_b64 v[190:191], 0\n\t"
            "ds_read_b64 v[156:157], v253 offset:1824\n\t"
            "ds_read_b64 v[158:159], v253 offset:1920\n\t"
            "s_waitcnt lgkmcnt(14)\n\t"
            "ds_write_b128 v236, v[176:179] offset:128\n\t"
            "v_mov_b64 v[176:177], 0\n\t"
            "v_mov_b64 v[178:179], 0\n\t"
            "s_waitcnt lgkmcnt(10)\n\t"
            "s_nop 0\n\t"
            "v_smfmac_f32_16x16x64_bf16 v[176:179], v[168:171], v[160:167], v255\n\t"
            "ds_read_b128 v[168:171], v254 offset:640\n\t"
            "v_mfma_f32_4x4x4_16b_bf16 v[196:199], v[186:187], v[186:187], v[196:199]\n\t"
            "ds_read_b64 v[160:161], v253 offset:88\n\t"
            "v_mfma_f32_16x16x32_bf16 v[192:195], v[238:241], v[184:187], v[192:195]\n\t"
            "ds_read_b64 v[162:163], v253 offset:184\n\t"
            "s_waitcnt lgkmcnt(8)\n\t"
            "v_smfmac_f32_16x16x64_bf16 v[180:183], v[172:175], v[144:151], v255\n\t"
            "ds_read_b64 v[164:165], v253 offset:1832\n\t"
            "ds_read_b64 v[166:167], v253 offset:1928\n\t"
            "v_cvt_pk_bf16_f32 v176, v176, v177\n\t"
            "v_cvt_pk_bf16_f32 v177, v178, v179\n\t"
            "s_nop 1\n\t"
            "v_mfma_f32_4x4x4_16b_bf16 v[196:199], v[176:177], v[176:177], v[196:199]\n\t"
            "ds_read_b128 v[172:175], v254 offset:704\n\t"
            "ds_read_b128 v[144:147], v242\n\t"
            "ds_read_b128 v[148:151], v243\n\t"
            "v_cvt_pk_bf16_f32 v178, v180, v181\n\t"
            "v_cvt_pk_bf16_f32 v179, v182, v183\n\t"
            "v_lshlrev_b32 v180, 16, v202\n\t"
            "v_and_b32 v181, 0xffff0000, v202\n\t"
            "v_lshlrev_b32 v182, 16, v203\n\t"
            "v_and_b32 v183, 0xffff0000, v203\n\t"
            "ds_write_b128 v237, v[184:187] offset:128\n\t"
            "v_mov_b64 v[184:185], 0\n\t"
            "v_mov_b64 v[186:187], 0\n\t"
            "s_waitcnt lgkmcnt(8)\n\t"
            "s_nop 0\n\t"
            "v_smfmac_f32_16x16x64_bf16 v[184:187], v[168:171], v[152:159], v255\n\t"
            "ds_read_b128 v[152:155], v242 offset:128\n\t"
            "v_mfma_f32_4x4x4_16b_bf16 v[196:199], v[178:179], v[178:179], v[196:199]\n\t"
            "ds_read_b128 v[156:159], v243 offset:128\n\t"
            "v_mfma_f32_16x16x32_bf16 v[192:195], v[238:241], v[176:179], v[192:195]\n\t"
            "ds_read_b128 v[168:171], v242 offset:6144\n\t"
            "s_waitcnt lgkmcnt(6)\n\t"
            "v_smfmac_f32_16x16x64_bf16 v[188:191], v[172:175], v[160:167], v255\n\t"
            "ds_read_b128 v[160:163], v242 offset:256\n\t"
            "ds_read_b128 v[164:167], v243 offset:256\n\t"
            "v_cvt_pk_bf16_f32 v184, v184, v185\n\t"
            "v_cvt_pk_bf16_f32 v185, v186, v187\n\t"
            "s_nop 1\n\t"
            "v_mfma_f32_4x4x4_16b_bf16 v[196:199], v[184:185], v[184:185], v[196:199]\n\t"
            "ds_read_b128 v[172:175], v243 offset:6144\n\t"
            "s_mov_b32 exec_hi, 0x3fffff\n\t"
            "s_add_u32 m0, s66, 0\n\t"
            "s_add_u32 s49, s79, 0\n\t"
            "buffer_load_dwordx4 v249, s[72:75], s49 offen lds\n\t"
            "s_mov_b32 exec_hi, -1\n\t"
            "v_cvt_pk_bf16_f32 v186, v188, v189\n\t"
            "v_cvt_pk_bf16_f32 v187, v190, v191\n\t"
            "v_lshlrev_b32 v188, 16, v206\n\t"
            "v_and_b32 v189, 0xffff0000, v206\n\t"
            "v_mfma_f32_4x4x4_16b_bf16 v[196:199], v[186:187], v[186:187], v[196:199]\n\t"
            "ds_write_b128 v236, v[176:179] offset:256\n\t"
            "v_mfma_f32_16x16x32_bf16 v[192:195], v[238:241], v[184:187], v[192:195]\n\t"
            "v_lshlrev_b32 v176, 16, v200\n\t"
            "v_and_b32 v177, 0xffff0000, v200\n\t"
            "v_lshlrev_b32 v178, 16, v201\n\t"
            "v_and_b32 v179, 0xffff0000, v201\n\t"
            "v_lshlrev_b32 v190, 16, v207\n\t"
            "v_and_b32 v191, 0xffff0000, v207\n\t"
            "s_waitcnt lgkmcnt(9)\n\t"
            "v_mfma_f32_16x16x32_bf16 v[176:179], v[0:3], v[144:147], v[176:179]\n\t"
            "s_setprio 3\n\t"
            "s_mov_b32 exec_hi, 0x3fffff\n\t"
            "s_add_u32 m0, s66, 872\n\t"
            "s_add_u32 s49, s68, 196224\n\t"
            "buffer_load_dwordx4 v249, s[72:75], s49 offen lds\n\t"
            "s_mov_b32 exec_hi, -1\n\t"
            "s_waitcnt lgkmcnt(8)\n\t"
            "v_mfma_f32_16x16x32_bf16 v[176:179], v[4:7], v[148:151], v[176:179]\n\t"
            "s_mov_b32 exec_hi, 0x3fffff\n\t"
            "s_add_u32 m0, s66, 1744\n\t"
            "s_add_u32 s49, s68, 392448\n\t"
            "buffer_load_dwordx4 v249, s[72:75], s49 offen lds\n\t"
            "s_mov_b32 exec_hi, -1\n\t"
            "s_waitcnt lgkmcnt(6)\n\t"
            "v_mfma_f32_16x16x32_bf16 v[176:179], v[8:11], v[152:155], v[176:179]\n\t"
            "s_mov_b32 exec_hi, 0x3fffff\n\t"
            "s_add_u32 m0, s66, 2616\n\t"
            "s_add_u32 s49, s68, 588672\n\t"
            "buffer_load_dwordx4 v249, s[72:75], s49 offen lds\n\t"
            "s_mov_b32 exec_hi, -1\n\t"
            "s_waitcnt lgkmcnt(5)\n\t"
            "v_mfma_f32_16x16x32_bf16 v[176:179], v[12:15], v[156:159], v[176:179]\n\t"
            "s_mov_b32 exec_hi, 0x3fffff\n\t"
            "s_add_u32 m0, s66, 3488\n\t"
            "s_add_u32 s49, s68, 784896\n\t"
            "buffer_load_dwordx4 v249, s[72:75], s49 offen lds\n\t"
            "s_mov_b32 exec_hi, -1\n\t"
            "s_waitcnt lgkmcnt(3)\n\t"
            "v_mfma_f32_16x16x32_bf16 v[176:179], v[16:19], v[160:163], v[176:179]\n\t"
            "ds_write_b128 v237, v[184:187] offset:256\n\t"
            "s_waitcnt lgkmcnt(3)\n\t"
            "v_mfma_f32_16x16x32_bf16 v[176:179], v[20:23], v[164:167], v[176:179]\n\t"
            "v_lshlrev_b32 v184, 16, v204\n\t"
            "v_and_b32 v185, 0xffff0000, v204\n\t"
            "v_mfma_f32_16x16x32_bf16 v[180:183], v[24:27], v[144:147], v[180:183]\n\t"
            "v_lshlrev_b32 v186, 16, v205\n\t"
            "v_and_b32 v187, 0xffff0000, v205\n\t"
            "v_mfma_f32_16x16x32_bf16 v[180:183], v[28:31], v[148:151], v[180:183]\n\t"
            "s_mov_b32 exec_hi, 0x3fffff\n\t"
            "s_add_u32 m0, s66, 4360\n\t"
            "s_add_u32 s49, s68, 981120\n\t"
            "buffer_load_dwordx4 v249, s[72:75], s49 offen lds\n\t"
            "s_mov_b32 exec_hi, -1\n\t"
            "v_mfma_f32_16x16x32_bf16 v[180:183], v[32:35], v[152:155], v[180:183]\n\t"
            "s_mov_b32 exec_hi, 0x3fffff\n\t"
            "s_add_u32 m0, s66, 5232\n\t"
            "s_add_u32 s49, s68, 1177344\n\t"
            "buffer_load_dwordx4 v249, s[72:75], s49 offen lds\n\t"
            "s_mov_b32 exec_hi, -1\n\t"
            "v_mfma_f32_16x16x32_bf16 v[180:183], v[36:39], v[156:159], v[180:183]\n\t"
            "v_cndmask_b32_e64 v193, v196, v197, s[60:61]\n\t"
            "v_cndmask_b32_e64 v193, v193, v198, s[62:63]\n\t"
            "v_mfma_f32_16x16x32_bf16 v[180:183], v[40:43], v[160:163], v[180:183]\n\t"
            "s_mov_b32 exec_hi, 0x3fffff\n\t"
            "s_add_u32 m0, s66, 6104\n\t"
            "s_add_u32 s49, s68, 1373568\n\t"
            "buffer_load_dwordx4 v249, s[72:75], s49 offen lds\n\t"
            "s_mov_b32 exec_hi, -1\n\t"
            "v_mfma_f32_16x16x32_bf16 v[180:183], v[44:47], v[164:167], v[180:183]\n\t"
            "v_cndmask_b32_e64 v193, v193, v199, s[64:65]\n\t"
            "v_lshlrev_b32 v194, 16, v209\n\t"
            "v_mfma_f32_16x16x32_bf16 v[184:187], v[48:51], v[144:147], v[184:187]\n\t"
            "ds_read_b128 v[196:199], v252\n\t"
            "v_and_b32 v195, 0xffff0000, v209\n\t"
            "v_mfma_f32_16x16x32_bf16 v[184:187], v[52:55], v[148:151], v[184:187]\n\t"
            "s_mov_b32 exec_hi, 0x3fffff\n\t"
            "s_add_u32 m0, s66, 6976\n\t"
            "s_add_u32 s49, s68, 1569792\n\t"
            "buffer_load_dwordx4 v249, s[72:75], s49 offen lds\n\t"
            "s_mov_b32 exec_hi, -1\n\t"
            "v_mfma_f32_16x16x32_bf16 v[184:187], v[56:59], v[152:155], v[184:187]\n\t"
            "s_bfe_u32 s57, s56, 0x20004\n\t"
            "s_lshl_b32 s58, s57, 3\n\t"
            "s_lshr_b32 s58, s55, s58\n\t"
            "s_lshl_b64 s[36:37], s[34:35], s58\n\t"
            "v_mfma_f32_16x16x32_bf16 v[184:187], v[60:63], v[156:159], v[184:187]\n\t"
            "s_bitcmp1_b32 s57, 1\n\t"
            "s_cselect_b32 s58, 1177344, 0\n\t"
            "s_bitcmp1_b32 s57, 0\n\t"
            "s_cselect_b32 s57, 2304, 0\n\t"
            "s_add_u32 s57, s57, s58\n\t"
            "v_mfma_f32_16x16x32_bf16 v[184:187], v[64:67], v[160:163], v[184:187]\n\t"
            "ds_write_b64 v248, v[192:193] offset:18816\n\t"
            "v_mfma_f32_16x16x32_bf16 v[184:187], v[68:71], v[164:167], v[184:187]\n\t"
            "v_lshlrev_b32 v192, 16, v208\n\t"
            "v_and_b32 v193, 0xffff0000, v208\n\t"
            "v_mfma_f32_16x16x32_bf16 v[188:191], v[72:75], v[144:147], v[188:191]\n\t"
            "s_waitcnt lgkmcnt(1)\n\t"
            "s_mov_b64 exec, s[36:37]\n\t"
            "v_add_u32 v196, s57, v196\n\t"
            "s_mov_b64 exec, -1\n\t"
            "v_mfma_f32_16x16x32_bf16 v[188:191], v[76:79], v[148:151], v[188:191]\n\t"
            "v_cvt_pk_bf16_f32 v176, v176, v177\n\t"
            "v_cvt_pk_bf16_f32 v177, v178, v179\n\t"
            "v_mfma_f32_16x16x32_bf16 v[188:191], v[80:83], v[152:155], v[188:191]\n\t"
            "v_cvt_pk_bf16_f32 v178, v180, v181\n\t"
            "v_cvt_pk_bf16_f32 v179, v182, v183\n\t"
            "v_mfma_f32_16x16x32_bf16 v[188:191], v[84:87], v[156:159], v[188:191]\n\t"
            "s_cbranch_vccnz .Lst0_%=\n\t"
            "buffer_store_dwordx4 v[176:179], v196, s[40:43], s45 offen\n\t"
            ".Lsr0_%=:\n\t"
            "v_mfma_f32_16x16x32_bf16 v[188:191], v[88:91], v[160:163], v[188:191]\n\t"
            "s_nop 0\n\t"
            "v_lshlrev_b32 v176, 16, v210\n\t"
            "v_and_b32 v177, 0xffff0000, v210\n\t"
            "v_mfma_f32_16x16x32_bf16 v[188:191], v[92:95], v[164:167], v[188:191]\n\t"
            "v_lshlrev_b32 v178, 16, v211\n\t"
            "v_and_b32 v179, 0xffff0000, v211\n\t"
            "v_mfma_f32_16x16x32_bf16 v[192:195], v[96:99], v[144:147], v[192:195]\n\t"
            "v_lshlrev_b32 v180, 16, v212\n\t"
            "v_and_b32 v181, 0xffff0000, v212\n\t"
            "v_mfma_f32_16x16x32_bf16 v[192:195], v[100:103], v[148:151], v[192:195]\n\t"
            "v_lshlrev_b32 v182, 16, v213\n\t"
            "v_and_b32 v183, 0xffff0000, v213\n\t"
            "v_mfma_f32_16x16x32_bf16 v[192:195], v[104:107], v[152:155], v[192:195]\n\t"
            "v_cvt_pk_bf16_f32 v184, v184, v185\n\t"
            "v_cvt_pk_bf16_f32 v185, v186, v187\n\t"
            "v_mfma_f32_16x16x32_bf16 v[192:195], v[108:111], v[156:159], v[192:195]\n\t"
            "v_cvt_pk_bf16_f32 v186, v188, v189\n\t"
            "v_cvt_pk_bf16_f32 v187, v190, v191\n\t"
            "v_mfma_f32_16x16x32_bf16 v[192:195], v[112:115], v[160:163], v[192:195]\n\t"
            "s_cbranch_vccnz .Lst1_%=\n\t"
            "buffer_store_dwordx4 v[184:187], v196, s[40:43], s45 offen offset:64\n\t"
            ".Lsr1_%=:\n\t"
            "v_mfma_f32_16x16x32_bf16 v[192:195], v[116:119], v[164:167], v[192:195]\n\t"
            "s_nop 0\n\t"
            "v_lshlrev_b32 v184, 16, v214\n\t"
            "v_and_b32 v185, 0xffff0000, v214\n\t"
            "v_mfma_f32_16x16x32_bf16 v[176:179], v[120:123], v[144:147], v[176:179]\n\t"
            "ds_read_b128 v[144:147], v242 offset:6272\n\t"
            "v_lshlrev_b32 v186, 16, v215\n\t"
            "v_mfma_f32_16x16x32_bf16 v[176:179], v[124:127], v[148:151], v[176:179]\n\t"
            "ds_read_b128 v[148:151], v243 offset:6272\n\t"
            "v_and_b32 v187, 0xffff0000, v215\n\t"
            "v_mfma_f32_16x16x32_bf16 v[176:179], v[128:131], v[152:155], v[176:179]\n\t"
            "ds_read_b128 v[152:155], v242 offset:6400\n\t"
            "v_lshlrev_b32 v188, 16, v216\n\t"
            "v_mfma_f32_16x16x32_bf16 v[176:179], v[132:135], v[156:159], v[176:179]\n\t"
            "ds_read_b128 v[156:159], v243 offset:6400\n\t"
            "v_and_b32 v189, 0xffff0000, v216\n\t"
            "v_mfma_f32_16x16x32_bf16 v[176:179], v[136:139], v[160:163], v[176:179]\n\t"
            "ds_read_b128 v[160:163], v242 offset:12288\n\t"
            "v_lshlrev_b32 v190, 16, v217\n\t"
            "v_mfma_f32_16x16x32_bf16 v[176:179], v[140:143], v[164:167], v[176:179]\n\t"
            "ds_read_b128 v[164:167], v243 offset:12288\n\t"
            "v_and_b32 v191, 0xffff0000, v217\n\t"
            "v_mfma_f32_16x16x32_bf16 v[180:183], v[0:3], v[168:171], v[180:183]\n\t"
            "v_cvt_pk_bf16_f32 v192, v192, v193\n\t"
            "v_cvt_pk_bf16_f32 v193, v194, v195\n\t"
            "v_mfma_f32_16x16x32_bf16 v[180:183], v[4:7], v[172:175], v[180:183]\n\t"
            "v_mov_b32 v249, s76\n\t"
            "v_add_u32 v236, s48, v244\n\t"
            "v_cvt_pk_bf16_f32 v194, v176, v177\n\t"
            "s_waitcnt lgkmcnt(5)\n\t"
            "v_mfma_f32_16x16x32_bf16 v[180:183], v[8:11], v[144:147], v[180:183]\n\t"
            "ds_read_b32 v249, v249 offset:4\n\t"
            "v_cvt_pk_bf16_f32 v195, v178, v179\n\t"
            "s_waitcnt lgkmcnt(5)\n\t"
            "v_mfma_f32_16x16x32_bf16 v[180:183], v[12:15], v[148:151], v[180:183]\n\t"
            "ds_read_b128 v[200:203], v236\n\t"
            "v_lshlrev_b32 v176, 16, v220\n\t"
            "s_waitcnt lgkmcnt(5)\n\t"
            "v_mfma_f32_16x16x32_bf16 v[180:183], v[16:19], v[152:155], v[180:183]\n\t"
            "ds_read_b128 v[212:215], v236 offset:6144\n\t"
            "v_and_b32 v177, 0xffff0000, v220\n\t"
            "s_waitcnt lgkmcnt(5)\n\t"
            "v_mfma_f32_16x16x32_bf16 v[180:183], v[20:23], v[156:159], v[180:183]\n\t"
            "s_cbranch_vccnz .Lst2_%=\n\t"
            "buffer_store_dwordx4 v[192:195], v196, s[40:43], s45 offen offset:128\n\t"
            ".Lsr2_%=:\n\t"
            "v_mfma_f32_16x16x32_bf16 v[184:187], v[24:27], v[168:171], v[184:187]\n\t"
            "s_nop 0\n\t"
            "v_lshlrev_b32 v192, 16, v218\n\t"
            "v_and_b32 v193, 0xffff0000, v218\n\t"
            "v_mfma_f32_16x16x32_bf16 v[184:187], v[28:31], v[172:175], v[184:187]\n\t"
            "v_lshlrev_b32 v194, 16, v219\n\t"
            "v_and_b32 v195, 0xffff0000, v219\n\t"
            "v_mfma_f32_16x16x32_bf16 v[184:187], v[32:35], v[144:147], v[184:187]\n\t"
            "v_lshlrev_b32 v178, 16, v221\n\t"
            "v_and_b32 v179, 0xffff0000, v221\n\t"
            "v_mfma_f32_16x16x32_bf16 v[184:187], v[36:39], v[148:151], v[184:187]\n\t"
            "v_cvt_pk_bf16_f32 v180, v180, v181\n\t"
            "v_cvt_pk_bf16_f32 v181, v182, v183\n\t"
            "v_mfma_f32_16x16x32_bf16 v[184:187], v[40:43], v[152:155], v[184:187]\n\t"
            "s_waitcnt lgkmcnt(2)\n\t"
            "v_readfirstlane_b32 s44, v249\n\t"
            "v_add_u32 v237, s48, v245\n\t"
            "v_mfma_f32_16x16x32_bf16 v[184:187], v[44:47], v[156:159], v[184:187]\n\t"
            "s_and_b32 s70, s44, 0x7ffffff\n\t"
            "s_add_u32 s70, s70, s77\n\t"
            "s_and_b32 s44, s44, 0x08000000\n\t"
            "s_or_b32 s44, s44, s39\n\t"
            "v_mfma_f32_16x16x32_bf16 v[188:191], v[48:51], v[168:171], v[188:191]\n\t"
            "ds_read_b32 v249, v252 offset:1028\n\t"
            "v_add_u32 v248, s48, v246\n\t"
            "v_mfma_f32_16x16x32_bf16 v[188:191], v[52:55], v[172:175], v[188:191]\n\t"
            "ds_read_b128 v[204:207], v237\n\t"
            "v_cvt_pk_bf16_f32 v182, v184, v185\n\t"
            "v_mfma_f32_16x16x32_bf16 v[188:191], v[56:59], v[144:147], v[188:191]\n\t"
            "ds_read_b128 v[208:211], v248\n\t"
            "v_cvt_pk_bf16_f32 v183, v186, v187\n\t"
            "v_mfma_f32_16x16x32_bf16 v[188:191], v[60:63], v[148:151], v[188:191]\n\t"
            "ds_read_b128 v[216:219], v237 offset:6144\n\t"
            "v_lshlrev_b32 v184, 16, v224\n\t"
            "v_mfma_f32_16x16x32_bf16 v[188:191], v[64:67], v[152:155], v[188:191]\n\t"
            "s_cbranch_vccnz .Lst3_%=\n\t"
            "buffer_store_dwordx4 v[180:183], v197, s[40:43], s45 offen\n\t"
            ".Lsr3_%=:\n\t"
            "v_mfma_f32_16x16x32_bf16 v[188:191], v[68:71], v[156:159], v[188:191]\n\t"
            "s_nop 0\n\t"
            "v_lshlrev_b32 v180, 16, v222\n\t"
            "v_and_b32 v181, 0xffff0000, v222\n\t"
            "v_mfma_f32_16x16x32_bf16 v[192:195], v[72:75], v[168:171], v[192:195]\n\t"
            "v_lshlrev_b32 v182, 16, v223\n\t"
            "v_and_b32 v183, 0xffff0000, v223\n\t"
            "v_mfma_f32_16x16x32_bf16 v[192:195], v[76:79], v[172:175], v[192:195]\n\t"
            "ds_read_b128 v[220:223], v248 offset:6144\n\t"
            "v_and_b32 v185, 0xffff0000, v224\n\t"
            "v_mfma_f32_16x16x32_bf16 v[192:195], v[80:83], v[144:147], v[192:195]\n\t"
            "v_lshlrev_b32 v186, 16, v225\n\t"
            "v_and_b32 v187, 0xffff0000, v225\n\t"
            "v_mfma_f32_16x16x32_bf16 v[192:195], v[84:87], v[148:151], v[192:195]\n\t"
            "v_cvt_pk_bf16_f32 v188, v188, v189\n\t"
            "v_cvt_pk_bf16_f32 v189, v190, v191\n\t"
            "v_mfma_f32_16x16x32_bf16 v[192:195], v[88:91], v[152:155], v[192:195]\n\t"
            "s_waitcnt lgkmcnt(4)\n\t"
            "v_and_b32 v249, s44, v249\n\t"
            "v_mfma_f32_16x16x32_bf16 v[192:195], v[92:95], v[156:159], v[192:195]\n\t"
            "v_mfma_f32_16x16x32_bf16 v[176:179], v[96:99], v[168:171], v[176:179]\n\t"
            "v_mfma_f32_16x16x32_bf16 v[176:179], v[100:103], v[172:175], v[176:179]\n\t"
            "v_mfma_f32_16x16x32_bf16 v[176:179], v[104:107], v[144:147], v[176:179]\n\t"
            "v_mfma_f32_16x16x32_bf16 v[176:179], v[108:111], v[148:151], v[176:179]\n\t"
            "v_mfma_f32_16x16x32_bf16 v[176:179], v[112:115], v[152:155], v[176:179]\n\t"
            "v_mfma_f32_16x16x32_bf16 v[176:179], v[116:119], v[156:159], v[176:179]\n\t"
            "s_nop 1\n\t"
            "v_cvt_pk_bf16_f32 v190, v192, v193\n\t"
            "v_mfma_f32_16x16x32_bf16 v[180:183], v[120:123], v[168:171], v[180:183]\n\t"
            "ds_read_b128 v[168:171], v242 offset:12416\n\t"
            "v_cvt_pk_bf16_f32 v191, v194, v195\n\t"
            "v_mfma_f32_16x16x32_bf16 v[180:183], v[124:127], v[172:175], v[180:183]\n\t"
            "ds_read_b128 v[172:175], v243 offset:12416\n\t"
            "v_lshlrev_b32 v192, 16, v228\n\t"
            "v_mfma_f32_16x16x32_bf16 v[180:183], v[128:131], v[144:147], v[180:183]\n\t"
            "ds_read_b128 v[144:147], v242 offset:12544\n\t"
            "v_and_b32 v193, 0xffff0000, v228\n\t"
            "v_mfma_f32_16x16x32_bf16 v[180:183], v[132:135], v[148:151], v[180:183]\n\t"
            "ds_read_b128 v[148:151], v243 offset:12544\n\t"
            "v_lshlrev_b32 v194, 16, v229\n\t"
            "v_mfma_f32_16x16x32_bf16 v[180:183], v[136:139], v[152:155], v[180:183]\n\t"
            "s_waitcnt lgkmcnt(0)\n\t"
            "s_barrier\n\t"
            "s_cbranch_vccnz .Lst4_%=\n\t"
            "buffer_store_dwordx4 v[188:191], v197, s[40:43], s45 offen offset:64\n\t"
            ".Lsr4_%=:\n\t"
            "v_mfma_f32_16x16x32_bf16 v[180:183], v[140:143], v[156:159], v[180:183]\n\t"
            "s_nop 0\n\t"
            "v_lshlrev_b32 v188, 16, v226\n\t"
            "v_and_b32 v189, 0xffff0000, v226\n\t"
            "v_mfma_f32_16x16x32_bf16 v[184:187], v[0:3], v[160:163], v[184:187]\n\t"
            "v_lshlrev_b32 v190, 16, v227\n\t"
            "v_and_b32 v191, 0xffff0000, v227\n\t"
            "v_mfma_f32_16x16x32_bf16 v[184:187], v[4:7], v[164:167], v[184:187]\n\t"
            "ds_read_b128 v[224:227], v236 offset:12288\n\t"
            "v_and_b32 v195, 0xffff0000, v229\n\t"
            "v_mfma_f32_16x16x32_bf16 v[184:187], v[8:11], v[168:171], v[184:187]\n\t"
            "v_cvt_pk_bf16_f32 v176, v176, v177\n\t"
            "v_cvt_pk_bf16_f32 v177, v178, v179\n\t"
            "v_mfma_f32_16x16x32_bf16 v[184:187], v[12:15], v[172:175], v[184:187]\n\t"
            "v_cvt_pk_bf16_f32 v178, v180, v181\n\t"
            "v_cvt_pk_bf16_f32 v179, v182, v183\n\t"
            "v_mfma_f32_16x16x32_bf16 v[184:187], v[16:19], v[144:147], v[184:187]\n\t"
            "s_cbranch_vccnz .Lst5_%=\n\t"
            "buffer_store_dwordx4 v[176:179], v197, s[40:43], s45 offen offset:128\n\t"
            ".Lsr5_%=:\n\t"
            "v_mfma_f32_16x16x32_bf16 v[184:187], v[20:23], v[148:151], v[184:187]\n\t"
            "s_nop 0\n\t"
            "v_lshlrev_b32 v176, 16, v230\n\t"
            "v_and_b32 v177, 0xffff0000, v230\n\t"
            "v_mfma_f32_16x16x32_bf16 v[188:191], v[24:27], v[160:163], v[188:191]\n\t"
            "v_lshlrev_b32 v178, 16, v231\n\t"
            "v_and_b32 v179, 0xffff0000, v231\n\t"
            "v_mfma_f32_16x16x32_bf16 v[188:191], v[28:31], v[164:167], v[188:191]\n\t"
            "ds_read_b128 v[228:231], v237 offset:12288\n\t"
            "v_lshlrev_b32 v180, 16, v232\n\t"
            "v_mfma_f32_16x16x32_bf16 v[188:191], v[32:35], v[168:171], v[188:191]\n\t"
            "v_and_b32 v181, 0xffff0000, v232\n\t"
            "v_lshlrev_b32 v182, 16, v233\n\t"
            "v_mfma_f32_16x16x32_bf16 v[188:191], v[36:39], v[172:175], v[188:191]\n\t"
            "v_and_b32 v183, 0xffff0000, v233\n\t"
            "v_cvt_pk_bf16_f32 v184, v184, v185\n\t"
            "v_mfma_f32_16x16x32_bf16 v[188:191], v[40:43], v[144:147], v[188:191]\n\t"
            "v_cvt_pk_bf16_f32 v185, v186, v187\n\t"
            "v_mfma_f32_16x16x32_bf16 v[188:191], v[44:47], v[148:151], v[188:191]\n\t"
            "v_mfma_f32_16x16x32_bf16 v[192:195], v[48:51], v[160:163], v[192:195]\n\t"
            "v_mfma_f32_16x16x32_bf16 v[192:195], v[52:55], v[164:167], v[192:195]\n\t"
            "v_mfma_f32_16x16x32_bf16 v[192:195], v[56:59], v[168:171], v[192:195]\n\t"
            "v_mfma_f32_16x16x32_bf16 v[192:195], v[60:63], v[172:175], v[192:195]\n\t"
            "v_mfma_f32_16x16x32_bf16 v[192:195], v[64:67], v[144:147], v[192:195]\n\t"
            "v_mfma_f32_16x16x32_bf16 v[192:195], v[68:71], v[148:151], v[192:195]\n\t"
            "s_nop 1\n\t"
            "v_cvt_pk_bf16_f32 v186, v188, v189\n\t"
            "v_mfma_f32_16x16x32_bf16 v[176:179], v[72:75], v[160:163], v[176:179]\n\t"
            "v_cvt_pk_bf16_f32 v187, v190, v191\n\t"
            "v_mfma_f32_16x16x32_bf16 v[176:179], v[76:79], v[164:167], v[176:179]\n\t"
            "s_cbranch_vccnz .Lst6_%=\n\t"
            "buffer_store_dwordx4 v[184:187], v198, s[40:43], s45 offen\n\t"
            ".Lsr6_%=:\n\t"
            "v_mfma_f32_16x16x32_bf16 v[176:179], v[80:83], v[168:171], v[176:179]\n\t"
            "s_nop 0\n\t"
            "v_lshlrev_b32 v184, 16, v234\n\t"
            "v_and_b32 v185, 0xffff0000, v234\n\t"
            "v_mfma_f32_16x16x32_bf16 v[176:179], v[84:87], v[172:175], v[176:179]\n\t"
            "v_lshlrev_b32 v186, 16, v235\n\t"
            "v_and_b32 v187, 0xffff0000, v235\n\t"
            "v_mfma_f32_16x16x32_bf16 v[176:179], v[88:91], v[144:147], v[176:179]\n\t"
            "ds_read_b128 v[232:235], v248 offset:12288\n\t"
            "v_cvt_pk_bf16_f32 v192, v192, v193\n\t"
            "v_mfma_f32_16x16x32_bf16 v[176:179], v[92:95], v[148:151], v[176:179]\n\t"
            "v_cvt_pk_bf16_f32 v193, v194, v195\n\t"
            "v_mfma_f32_16x16x32_bf16 v[180:183], v[96:99], v[160:163], v[180:183]\n\t"
            "v_mfma_f32_16x16x32_bf16 v[180:183], v[100:103], v[164:167], v[180:183]\n\t"
            "v_mfma_f32_16x16x32_bf16 v[180:183], v[104:107], v[168:171], v[180:183]\n\t"
            "v_mfma_f32_16x16x32_bf16 v[180:183], v[108:111], v[172:175], v[180:183]\n\t"
            "v_mfma_f32_16x16x32_bf16 v[180:183], v[112:115], v[144:147], v[180:183]\n\t"
            "s_nop 1\n\t"
            "v_cvt_pk_bf16_f32 v194, v176, v177\n\t"
            "v_mfma_f32_16x16x32_bf16 v[180:183], v[116:119], v[148:151], v[180:183]\n\t"
            "v_cvt_pk_bf16_f32 v195, v178, v179\n\t"
            "v_mfma_f32_16x16x32_bf16 v[184:187], v[120:123], v[160:163], v[184:187]\n\t"
            "s_cbranch_vccnz .Lst7_%=\n\t"
            "buffer_store_dwordx4 v[192:195], v198, s[40:43], s45 offen offset:64\n\t"
            ".Lsr7_%=:\n\t"
            "v_mfma_f32_16x16x32_bf16 v[184:187], v[124:127], v[164:167], v[184:187]\n\t"
            "s_waitcnt vmcnt(8)\n\t"
            "ds_read_b64 v[152:153], v253 offset:8\n\t"
            "v_mfma_f32_16x16x32_bf16 v[184:187], v[128:131], v[168:171], v[184:187]\n\t"
            "ds_read_b128 v[168:171], v254\n\t"
            "v_cvt_pk_bf16_f32 v180, v180, v181\n\t"
            "v_mfma_f32_16x16x32_bf16 v[184:187], v[132:135], v[172:175], v[184:187]\n\t"
            "ds_read_b128 v[172:175], v254 offset:64\n\t"
            "v_cvt_pk_bf16_f32 v181, v182, v183\n\t"
            "v_mfma_f32_16x16x32_bf16 v[184:187], v[136:139], v[144:147], v[184:187]\n\t"
            "ds_read_b64 v[144:145], v253\n\t"
            "v_mfma_f32_16x16x32_bf16 v[184:187], v[140:143], v[148:151], v[184:187]\n\t"
            "ds_read_b64 v[146:147], v253 offset:96\n\t"
            "ds_read_b64 v[148:149], v253 offset:1744\n\t"
            "ds_read_b64 v[150:151], v253 offset:1840\n\t"
            "ds_read_b64 v[154:155], v253 offset:104\n\t"
            "ds_read_b64 v[156:157], v253 offset:1752\n\t"
            "ds_read_b64 v[158:159], v253 offset:1848\n\t"
            "ds_read_b64 v[160:161], v253 offset:16\n\t"
            "ds_read_b64 v[162:163], v253 offset:112\n\t"
            "v_cvt_pk_bf16_f32 v182, v184, v185\n\t"
            "v_cvt_pk_bf16_f32 v183, v186, v187\n\t"
            "s_waitcnt lgkmcnt(14)\n\t"
            "ds_read_b64 v[164:165], v253 offset:1760\n\t"
            "s_waitcnt lgkmcnt(14)\n\t"
            "ds_read_b64 v[166:167], v253 offset:1856\n\t"
            "s_cbranch_vccnz .Lst8_%=\n\t"
            "buffer_store_dwordx4 v[180:183], v198, s[40:43], s45 offen offset:128\n\t"
            ".Lsr8_%=:\n\t"
            "s_mov_b32 m0, s32\n\t"
            "s_add_u32 s49, s70, s25\n\t"
            "buffer_load_dword v249, s[72:75], s70 offen lds\n\t"
            "buffer_load_dword v249, s[72:75], s49 offen lds\n\t"
            "s_add_u32 s45, s46, s27\n\t"
            "s_mov_b32 s46, s47\n\t"
            "s_mov_b32 s47, s68\n\t"
            "s_xor_b32 s48, s48, s71\n\t"
            "s_xor_b32 s67, s67, s71\n\t"
            "s_add_u32 s76, s76, 4\n\t"
            "s_lshl_b32 s56, s56, 2\n\t"
            "s_lshr_b32 s49, s26, 30\n\t"
            "s_or_b32 s56, s56, s49\n\t"
            "s_cmp_ge_u32 s76, s24\n\t"
            "s_cselect_b64 vcc, -1, 0\n\t"
            "s_cmp_lt_u32 s46, 0x7000000\n\t"
            "s_cbranch_scc1 .Lwb_loop%=\n\t"
            "s_branch .Lsx%=\n\t"
            ".Lst0_%=:\n\t"
            "buffer_store_dwordx4 v[176:179], v196, s[40:43], s45 offen sc1\n\t"
            "s_branch .Lsr0_%=\n\t"
            ".Lst1_%=:\n\t"
            "buffer_store_dwordx4 v[184:187], v196, s[40:43], s45 offen offset:64 sc1\n\t"
            "s_branch .Lsr1_%=\n\t"
            ".Lst2_%=:\n\t"
            "buffer_store_dwordx4 v[192:195], v196, s[40:43], s45 offen offset:128 sc1\n\t"
            "s_branch .Lsr2_%=\n\t"
            ".Lst3_%=:\n\t"
            "buffer_store_dwordx4 v[180:183], v197, s[40:43], s45 offen sc1\n\t"
            "s_branch .Lsr3_%=\n\t"
            ".Lst4_%=:\n\t"
            "buffer_store_dwordx4 v[188:191], v197, s[40:43], s45 offen offset:64 sc1\n\t"
            "s_branch .Lsr4_%=\n\t"
            ".Lst5_%=:\n\t"
            "buffer_store_dwordx4 v[176:179], v197, s[40:43], s45 offen offset:128 sc1\n\t"
            "s_branch .Lsr5_%=\n\t"
            ".Lst6_%=:\n\t"
            "buffer_store_dwordx4 v[184:187], v198, s[40:43], s45 offen sc1\n\t"
            "s_branch .Lsr6_%=\n\t"
            ".Lst7_%=:\n\t"
            "buffer_store_dwordx4 v[192:195], v198, s[40:43], s45 offen offset:64 sc1\n\t"
            "s_branch .Lsr7_%=\n\t"
            ".Lst8_%=:\n\t"
            "buffer_store_dwordx4 v[180:183], v198, s[40:43], s45 offen offset:128 sc1\n\t"
            "s_branch .Lsr8_%=\n\t"
            ".Lsx%=:\n\t"
            "s_waitcnt lgkmcnt(0)\n\t"
            "s_nop 7\n\t"
            "s_setprio 3\n\t"
            "s_nop 7\n\t"
            "ds_read_b128 v[196:199], v252\n\t"
            "s_bfe_u32 s57, s56, 0x20004\n\t"
            "s_lshl_b32 s58, s57, 3\n\t"
            "s_lshr_b32 s58, s55, s58\n\t"
            "s_lshl_b64 s[36:37], s[34:35], s58\n\t"
            "s_bitcmp1_b32 s57, 1\n\t"
            "s_cselect_b32 s58, 1177344, 0\n\t"
            "s_bitcmp1_b32 s57, 0\n\t"
            "s_cselect_b32 s57, 2304, 0\n\t"
            "s_add_u32 s57, s57, s58\n\t"
            "s_waitcnt lgkmcnt(0)\n\t"
            "s_mov_b64 exec, s[36:37]\n\t"
            "v_add_u32 v196, s57, v196\n\t"
            "s_mov_b64 exec, -1\n\t"
            "s_nop 7\n\t"
            "v_mov_b32 v144, s23\n\t"
            ".Ldp%=:\n\t"
            "ds_read_b128 v[148:151], v144 offset:96\n\t"
            "s_waitcnt lgkmcnt(0)\n\t"
            "v_or_b32 v148, v148, v149\n\t"
            "v_or3_b32 v148, v148, v150, v151\n\t"
            "v_cmp_gt_i32 vcc, 0, v148\n\t"
            "s_nop 4\n\t"
            "s_cbranch_vccnz .Ldp%=\n\t"
            "s_nop 7\n\t"
            "ds_read_b128 v[184:187], v242\n\t"
            "v_lshlrev_b32 v152, 16, v200\n\t"
            "v_and_b32 v153, 0xffff0000, v200\n\t"
            "v_lshlrev_b32 v154, 16, v201\n\t"
            "v_and_b32 v155, 0xffff0000, v201\n\t"
            "v_lshlrev_b32 v156, 16, v202\n\t"
            "v_and_b32 v157, 0xffff0000, v202\n\t"
            "v_lshlrev_b32 v158, 16, v203\n\t"
            "v_and_b32 v159, 0xffff0000, v203\n\t"
            "v_lshlrev_b32 v160, 16, v204\n\t"
            "v_and_b32 v161, 0xffff0000, v204\n\t"
            "v_lshlrev_b32 v162, 16, v205\n\t"
            "v_and_b32 v163, 0xffff0000, v205\n\t"
            "v_lshlrev_b32 v164, 16, v206\n\t"
            "v_and_b32 v165, 0xffff0000, v206\n\t"
            "v_lshlrev_b32 v166, 16, v207\n\t"
            "v_and_b32 v167, 0xffff0000, v207\n\t"
            "v_lshlrev_b32 v176, 16, v208\n\t"
            "v_and_b32 v177, 0xffff0000, v208\n\t"
            "v_lshlrev_b32 v178, 16, v209\n\t"
            "v_and_b32 v179, 0xffff0000, v209\n\t"
            "v_lshlrev_b32 v180, 16, v210\n\t"
            "v_and_b32 v181, 0xffff0000, v210\n\t"
            "v_lshlrev_b32 v182, 16, v211\n\t"
            "v_and_b32 v183, 0xffff0000, v211\n\t"
            "v_lshlrev_b32 v144, 16, v212\n\t"
            "v_and_b32 v145, 0xffff0000, v212\n\t"
            "v_lshlrev_b32 v146, 16, v213\n\t"
            "v_and_b32 v147, 0xffff0000, v213\n\t"
            "v_lshlrev_b32 v148, 16, v214\n\t"
            "v_and_b32 v149, 0xffff0000, v214\n\t"
            "s_waitcnt lgkmcnt(0)\n\t"
            "v_mfma_f32_16x16x32_bf16 v[152:155], v[0:3], v[184:187], v[152:155]\n\t"
            "ds_read_b128 v[188:191], v243\n\t"
            "v_lshlrev_b32 v150, 16, v215\n\t"
            "v_mfma_f32_16x16x32_bf16 v[156:159], v[24:27], v[184:187], v[156:159]\n\t"
            "ds_read_b128 v[192:195], v242 offset:128\n\t"
            "v_and_b32 v151, 0xffff0000, v215\n\t"
            "v_mfma_f32_16x16x32_bf16 v[160:163], v[48:51], v[184:187], v[160:163]\n\t"
            "v_lshlrev_b32 v168, 16, v216\n\t"
            "v_and_b32 v169, 0xffff0000, v216\n\t"
            "v_mfma_f32_16x16x32_bf16 v[164:167], v[72:75], v[184:187], v[164:167]\n\t"
            "v_lshlrev_b32 v170, 16, v217\n\t"
            "v_and_b32 v171, 0xffff0000, v217\n\t"
            "v_mfma_f32_16x16x32_bf16 v[176:179], v[96:99], v[184:187], v[176:179]\n\t"
            "v_lshlrev_b32 v172, 16, v218\n\t"
            "v_and_b32 v173, 0xffff0000, v218\n\t"
            "v_mfma_f32_16x16x32_bf16 v[180:183], v[120:123], v[184:187], v[180:183]\n\t"
            "ds_read_b128 v[184:187], v243 offset:128\n\t"
            "v_lshlrev_b32 v174, 16, v219\n\t"
            "v_and_b32 v175, 0xffff0000, v219\n\t"
            "s_waitcnt lgkmcnt(2)\n\t"
            "v_mfma_f32_16x16x32_bf16 v[152:155], v[4:7], v[188:191], v[152:155]\n\t"
            "v_mfma_f32_16x16x32_bf16 v[156:159], v[28:31], v[188:191], v[156:159]\n\t"
            "v_mfma_f32_16x16x32_bf16 v[160:163], v[52:55], v[188:191], v[160:163]\n\t"
            "v_mfma_f32_16x16x32_bf16 v[164:167], v[76:79], v[188:191], v[164:167]\n\t"
            "v_mfma_f32_16x16x32_bf16 v[176:179], v[100:103], v[188:191], v[176:179]\n\t"
            "v_mfma_f32_16x16x32_bf16 v[180:183], v[124:127], v[188:191], v[180:183]\n\t"
            "ds_read_b128 v[188:191], v242 offset:256\n\t"
            "s_waitcnt lgkmcnt(2)\n\t"
            "v_mfma_f32_16x16x32_bf16 v[152:155], v[8:11], v[192:195], v[152:155]\n\t"
            "v_mfma_f32_16x16x32_bf16 v[156:159], v[32:35], v[192:195], v[156:159]\n\t"
            "v_mfma_f32_16x16x32_bf16 v[160:163], v[56:59], v[192:195], v[160:163]\n\t"
            "v_mfma_f32_16x16x32_bf16 v[164:167], v[80:83], v[192:195], v[164:167]\n\t"
            "v_mfma_f32_16x16x32_bf16 v[176:179], v[104:107], v[192:195], v[176:179]\n\t"
            "v_mfma_f32_16x16x32_bf16 v[180:183], v[128:131], v[192:195], v[180:183]\n\t"
            "ds_read_b128 v[192:195], v243 offset:256\n\t"
            "s_waitcnt lgkmcnt(2)\n\t"
            "v_mfma_f32_16x16x32_bf16 v[152:155], v[12:15], v[184:187], v[152:155]\n\t"
            "v_mfma_f32_16x16x32_bf16 v[156:159], v[36:39], v[184:187], v[156:159]\n\t"
            "v_mfma_f32_16x16x32_bf16 v[160:163], v[60:63], v[184:187], v[160:163]\n\t"
            "v_mfma_f32_16x16x32_bf16 v[164:167], v[84:87], v[184:187], v[164:167]\n\t"
            "v_mfma_f32_16x16x32_bf16 v[176:179], v[108:111], v[184:187], v[176:179]\n\t"
            "v_mfma_f32_16x16x32_bf16 v[180:183], v[132:135], v[184:187], v[180:183]\n\t"
            "ds_read_b128 v[184:187], v242 offset:6144\n\t"
            "s_waitcnt lgkmcnt(2)\n\t"
            "v_mfma_f32_16x16x32_bf16 v[152:155], v[16:19], v[188:191], v[152:155]\n\t"
            "v_mfma_f32_16x16x32_bf16 v[156:159], v[40:43], v[188:191], v[156:159]\n\t"
            "v_mfma_f32_16x16x32_bf16 v[160:163], v[64:67], v[188:191], v[160:163]\n\t"
            "v_mfma_f32_16x16x32_bf16 v[164:167], v[88:91], v[188:191], v[164:167]\n\t"
            "v_mfma_f32_16x16x32_bf16 v[176:179], v[112:115], v[188:191], v[176:179]\n\t"
            "v_mfma_f32_16x16x32_bf16 v[180:183], v[136:139], v[188:191], v[180:183]\n\t"
            "ds_read_b128 v[188:191], v243 offset:6144\n\t"
            "s_waitcnt lgkmcnt(2)\n\t"
            "v_mfma_f32_16x16x32_bf16 v[152:155], v[20:23], v[192:195], v[152:155]\n\t"
            "v_mfma_f32_16x16x32_bf16 v[156:159], v[44:47], v[192:195], v[156:159]\n\t"
            "v_mfma_f32_16x16x32_bf16 v[160:163], v[68:71], v[192:195], v[160:163]\n\t"
            "v_mfma_f32_16x16x32_bf16 v[164:167], v[92:95], v[192:195], v[164:167]\n\t"
            "v_mfma_f32_16x16x32_bf16 v[176:179], v[116:119], v[192:195], v[176:179]\n\t"
            "v_mfma_f32_16x16x32_bf16 v[180:183], v[140:143], v[192:195], v[180:183]\n\t"
            "ds_read_b128 v[192:195], v242 offset:6272\n\t"
            "s_waitcnt lgkmcnt(2)\n\t"
            "v_mfma_f32_16x16x32_bf16 v[144:147], v[0:3], v[184:187], v[144:147]\n\t"
            "v_cvt_pk_bf16_f32 v152, v152, v153\n\t"
            "v_cvt_pk_bf16_f32 v153, v154, v155\n\t"
            "v_mfma_f32_16x16x32_bf16 v[148:151], v[24:27], v[184:187], v[148:151]\n\t"
            "v_cvt_pk_bf16_f32 v160, v160, v161\n\t"
            "v_cvt_pk_bf16_f32 v161, v162, v163\n\t"
            "v_mfma_f32_16x16x32_bf16 v[168:171], v[48:51], v[184:187], v[168:171]\n\t"
            "v_cvt_pk_bf16_f32 v154, v156, v157\n\t"
            "v_cvt_pk_bf16_f32 v155, v158, v159\n\t"
            "v_mfma_f32_16x16x32_bf16 v[172:175], v[72:75], v[184:187], v[172:175]\n\t"
            "v_cvt_pk_bf16_f32 v162, v164, v165\n\t"
            "v_cvt_pk_bf16_f32 v163, v166, v167\n\t"
            "s_waitcnt lgkmcnt(1)\n\t"
            "v_mfma_f32_16x16x32_bf16 v[144:147], v[4:7], v[188:191], v[144:147]\n\t"
            "v_lshlrev_b32 v156, 16, v220\n\t"
            "v_and_b32 v157, 0xffff0000, v220\n\t"
            "v_mfma_f32_16x16x32_bf16 v[148:151], v[28:31], v[188:191], v[148:151]\n\t"
            "v_lshlrev_b32 v158, 16, v221\n\t"
            "v_and_b32 v159, 0xffff0000, v221\n\t"
            "v_mfma_f32_16x16x32_bf16 v[168:171], v[52:55], v[188:191], v[168:171]\n\t"
            "v_lshlrev_b32 v164, 16, v222\n\t"
            "v_and_b32 v165, 0xffff0000, v222\n\t"
            "v_mfma_f32_16x16x32_bf16 v[156:159], v[96:99], v[184:187], v[156:159]\n\t"
            "v_lshlrev_b32 v166, 16, v223\n\t"
            "v_and_b32 v167, 0xffff0000, v223\n\t"
            "v_mfma_f32_16x16x32_bf16 v[172:175], v[76:79], v[188:191], v[172:175]\n\t"
            "buffer_store_dwordx4 v[152:155], v196, s[40:43], s45 offen sc1\n\t"
            "v_mfma_f32_16x16x32_bf16 v[164:167], v[120:123], v[184:187], v[164:167]\n\t"
            "ds_read_b128 v[184:187], v243 offset:6272\n\t"
            "v_cvt_pk_bf16_f32 v176, v176, v177\n\t"
            "v_mfma_f32_16x16x32_bf16 v[156:159], v[100:103], v[188:191], v[156:159]\n\t"
            "buffer_store_dwordx4 v[160:163], v196, s[40:43], s45 offen offset:64 sc1\n\t"
            "v_mfma_f32_16x16x32_bf16 v[164:167], v[124:127], v[188:191], v[164:167]\n\t"
            "ds_read_b128 v[188:191], v242 offset:6400\n\t"
            "v_cvt_pk_bf16_f32 v177, v178, v179\n\t"
            "s_waitcnt lgkmcnt(2)\n\t"
            "v_mfma_f32_16x16x32_bf16 v[144:147], v[8:11], v[192:195], v[144:147]\n\t"
            "v_cvt_pk_bf16_f32 v178, v180, v181\n\t"
            "v_cvt_pk_bf16_f32 v179, v182, v183\n\t"
            "v_mfma_f32_16x16x32_bf16 v[148:151], v[32:35], v[192:195], v[148:151]\n\t"
            "buffer_store_dwordx4 v[176:179], v196, s[40:43], s45 offen offset:128 sc1\n\t"
            "v_mfma_f32_16x16x32_bf16 v[168:171], v[56:59], v[192:195], v[168:171]\n\t"
            "v_lshlrev_b32 v152, 16, v224\n\t"
            "v_and_b32 v153, 0xffff0000, v224\n\t"
            "v_mfma_f32_16x16x32_bf16 v[172:175], v[80:83], v[192:195], v[172:175]\n\t"
            "v_lshlrev_b32 v154, 16, v225\n\t"
            "v_and_b32 v155, 0xffff0000, v225\n\t"
            "v_mfma_f32_16x16x32_bf16 v[156:159], v[104:107], v[192:195], v[156:159]\n\t"
            "v_lshlrev_b32 v180, 16, v226\n\t"
            "v_and_b32 v181, 0xffff0000, v226\n\t"
            "v_mfma_f32_16x16x32_bf16 v[164:167], v[128:131], v[192:195], v[164:167]\n\t"
            "ds_read_b128 v[192:195], v243 offset:6400\n\t"
            "v_lshlrev_b32 v182, 16, v227\n\t"
            "s_waitcnt lgkmcnt(2)\n\t"
            "v_mfma_f32_16x16x32_bf16 v[144:147], v[12:15], v[184:187], v[144:147]\n\t"
            "v_and_b32 v183, 0xffff0000, v227\n\t"
            "v_lshlrev_b32 v160, 16, v228\n\t"
            "v_mfma_f32_16x16x32_bf16 v[148:151], v[36:39], v[184:187], v[148:151]\n\t"
            "v_and_b32 v161, 0xffff0000, v228\n\t"
            "v_lshlrev_b32 v162, 16, v229\n\t"
            "s_waitcnt lgkmcnt(1)\n\t"
            "v_mfma_f32_16x16x32_bf16 v[144:147], v[16:19], v[188:191], v[144:147]\n\t"
            "v_and_b32 v163, 0xffff0000, v229\n\t"
            "v_lshlrev_b32 v176, 16, v230\n\t"
            "v_mfma_f32_16x16x32_bf16 v[168:171], v[60:63], v[184:187], v[168:171]\n\t"
            "v_and_b32 v177, 0xffff0000, v230\n\t"
            "v_lshlrev_b32 v178, 16, v231\n\t"
            "v_mfma_f32_16x16x32_bf16 v[148:151], v[40:43], v[188:191], v[148:151]\n\t"
            "v_and_b32 v179, 0xffff0000, v231\n\t"
            "v_mfma_f32_16x16x32_bf16 v[172:175], v[84:87], v[184:187], v[172:175]\n\t"
            "v_mfma_f32_16x16x32_bf16 v[168:171], v[64:67], v[188:191], v[168:171]\n\t"
            "v_mfma_f32_16x16x32_bf16 v[156:159], v[108:111], v[184:187], v[156:159]\n\t"
            "s_waitcnt lgkmcnt(0)\n\t"
            "v_mfma_f32_16x16x32_bf16 v[144:147], v[20:23], v[192:195], v[144:147]\n\t"
            "v_mfma_f32_16x16x32_bf16 v[148:151], v[44:47], v[192:195], v[148:151]\n\t"
            "v_mfma_f32_16x16x32_bf16 v[164:167], v[132:135], v[184:187], v[164:167]\n\t"
            "ds_read_b128 v[184:187], v242 offset:12288\n\t"
            "v_mfma_f32_16x16x32_bf16 v[172:175], v[88:91], v[188:191], v[172:175]\n\t"
            "v_mfma_f32_16x16x32_bf16 v[168:171], v[68:71], v[192:195], v[168:171]\n\t"
            "v_mfma_f32_16x16x32_bf16 v[156:159], v[112:115], v[188:191], v[156:159]\n\t"
            "s_nop 1\n\t"
            "v_cvt_pk_bf16_f32 v144, v144, v145\n\t"
            "v_mfma_f32_16x16x32_bf16 v[164:167], v[136:139], v[188:191], v[164:167]\n\t"
            "ds_read_b128 v[188:191], v243 offset:12288\n\t"
            "v_cvt_pk_bf16_f32 v145, v146, v147\n\t"
            "v_mfma_f32_16x16x32_bf16 v[172:175], v[92:95], v[192:195], v[172:175]\n\t"
            "v_cvt_pk_bf16_f32 v146, v148, v149\n\t"
            "v_cvt_pk_bf16_f32 v147, v150, v151\n\t"
            "v_mfma_f32_16x16x32_bf16 v[156:159], v[116:119], v[192:195], v[156:159]\n\t"
            "buffer_store_dwordx4 v[144:147], v197, s[40:43], s45 offen sc1\n\t"
            "v_mfma_f32_16x16x32_bf16 v[164:167], v[140:143], v[192:195], v[164:167]\n\t"
            "ds_read_b128 v[192:195], v242 offset:12416\n\t"
            "v_cvt_pk_bf16_f32 v168, v168, v169\n\t"
            "s_waitcnt lgkmcnt(2)\n\t"
            "v_mfma_f32_16x16x32_bf16 v[152:155], v[0:3], v[184:187], v[152:155]\n\t"
            "v_cvt_pk_bf16_f32 v169, v170, v171\n\t"
            "v_cvt_pk_bf16_f32 v170, v172, v173\n\t"
            "v_mfma_f32_16x16x32_bf16 v[180:183], v[24:27], v[184:187], v[180:183]\n\t"
            "v_cvt_pk_bf16_f32 v171, v174, v175\n\t"
            "v_cvt_pk_bf16_f32 v156, v156, v157\n\t"
            "v_mfma_f32_16x16x32_bf16 v[160:163], v[48:51], v[184:187], v[160:163]\n\t"
            "buffer_store_dwordx4 v[168:171], v197, s[40:43], s45 offen offset:64 sc1\n\t"
            "v_mfma_f32_16x16x32_bf16 v[176:179], v[72:75], v[184:187], v[176:179]\n\t"
            "v_cvt_pk_bf16_f32 v157, v158, v159\n\t"
            "v_cvt_pk_bf16_f32 v158, v164, v165\n\t"
            "s_waitcnt lgkmcnt(1)\n\t"
            "v_mfma_f32_16x16x32_bf16 v[152:155], v[4:7], v[188:191], v[152:155]\n\t"
            "v_cvt_pk_bf16_f32 v159, v166, v167\n\t"
            "v_lshlrev_b32 v164, 16, v232\n\t"
            "v_mfma_f32_16x16x32_bf16 v[180:183], v[28:31], v[188:191], v[180:183]\n\t"
            "buffer_store_dwordx4 v[156:159], v197, s[40:43], s45 offen offset:128 sc1\n\t"
            "v_mfma_f32_16x16x32_bf16 v[160:163], v[52:55], v[188:191], v[160:163]\n\t"
            "v_and_b32 v165, 0xffff0000, v232\n\t"
            "v_lshlrev_b32 v166, 16, v233\n\t"
            "v_mfma_f32_16x16x32_bf16 v[176:179], v[76:79], v[188:191], v[176:179]\n\t"
            "v_and_b32 v167, 0xffff0000, v233\n\t"
            "v_lshlrev_b32 v156, 16, v234\n\t"
            "s_waitcnt lgkmcnt(0)\n\t"
            "v_mfma_f32_16x16x32_bf16 v[152:155], v[8:11], v[192:195], v[152:155]\n\t"
            "v_and_b32 v157, 0xffff0000, v234\n\t"
            "v_lshlrev_b32 v158, 16, v235\n\t"
            "v_mfma_f32_16x16x32_bf16 v[164:167], v[96:99], v[184:187], v[164:167]\n\t"
            "v_and_b32 v159, 0xffff0000, v235\n\t"
            "v_mfma_f32_16x16x32_bf16 v[164:167], v[100:103], v[188:191], v[164:167]\n\t"
            "s_nop 0\n\t"
            "v_mfma_f32_16x16x32_bf16 v[156:159], v[120:123], v[184:187], v[156:159]\n\t"
            "ds_read_b128 v[184:187], v243 offset:12416\n\t"
            "v_mfma_f32_16x16x32_bf16 v[156:159], v[124:127], v[188:191], v[156:159]\n\t"
            "ds_read_b128 v[188:191], v242 offset:12544\n\t"
            "v_mfma_f32_16x16x32_bf16 v[180:183], v[32:35], v[192:195], v[180:183]\n\t"
            "v_mfma_f32_16x16x32_bf16 v[160:163], v[56:59], v[192:195], v[160:163]\n\t"
            "v_mfma_f32_16x16x32_bf16 v[176:179], v[80:83], v[192:195], v[176:179]\n\t"
            "v_mfma_f32_16x16x32_bf16 v[164:167], v[104:107], v[192:195], v[164:167]\n\t"
            "v_mfma_f32_16x16x32_bf16 v[156:159], v[128:131], v[192:195], v[156:159]\n\t"
            "ds_read_b128 v[192:195], v243 offset:12544\n\t"
            "s_waitcnt lgkmcnt(2)\n\t"
            "v_mfma_f32_16x16x32_bf16 v[152:155], v[12:15], v[184:187], v[152:155]\n\t"
            "v_mfma_f32_16x16x32_bf16 v[180:183], v[36:39], v[184:187], v[180:183]\n\t"
            "s_waitcnt lgkmcnt(1)\n\t"
            "v_mfma_f32_16x16x32_bf16 v[152:155], v[16:19], v[188:191], v[152:155]\n\t"
            "v_mfma_f32_16x16x32_bf16 v[160:163], v[60:63], v[184:187], v[160:163]\n\t"
            "v_mfma_f32_16x16x32_bf16 v[180:183], v[40:43], v[188:191], v[180:183]\n\t"
            "v_mfma_f32_16x16x32_bf16 v[176:179], v[84:87], v[184:187], v[176:179]\n\t"
            "s_waitcnt lgkmcnt(0)\n\t"
            "v_mfma_f32_16x16x32_bf16 v[152:155], v[20:23], v[192:195], v[152:155]\n\t"
            "v_mfma_f32_16x16x32_bf16 v[160:163], v[64:67], v[188:191], v[160:163]\n\t"
            "v_mfma_f32_16x16x32_bf16 v[180:183], v[44:47], v[192:195], v[180:183]\n\t"
            "v_mfma_f32_16x16x32_bf16 v[164:167], v[108:111], v[184:187], v[164:167]\n\t"
            "v_mfma_f32_16x16x32_bf16 v[176:179], v[88:91], v[188:191], v[176:179]\n\t"
            "v_mfma_f32_16x16x32_bf16 v[156:159], v[132:135], v[184:187], v[156:159]\n\t"
            "v_mfma_f32_16x16x32_bf16 v[160:163], v[68:71], v[192:195], v[160:163]\n\t"
            "s_nop 1\n\t"
            "v_cvt_pk_bf16_f32 v152, v152, v153\n\t"
            "v_mfma_f32_16x16x32_bf16 v[164:167], v[112:115], v[188:191], v[164:167]\n\t"
            "v_cvt_pk_bf16_f32 v153, v154, v155\n\t"
            "v_mfma_f32_16x16x32_bf16 v[176:179], v[92:95], v[192:195], v[176:179]\n\t"
            "v_cvt_pk_bf16_f32 v154, v180, v181\n\t"
            "v_cvt_pk_bf16_f32 v155, v182, v183\n\t"
            "v_mfma_f32_16x16x32_bf16 v[156:159], v[136:139], v[188:191], v[156:159]\n\t"
            "buffer_store_dwordx4 v[152:155], v198, s[40:43], s45 offen sc1\n\t"
            "v_mfma_f32_16x16x32_bf16 v[164:167], v[116:119], v[192:195], v[164:167]\n\t"
            "v_cvt_pk_bf16_f32 v160, v160, v161\n\t"
            "v_cvt_pk_bf16_f32 v161, v162, v163\n\t"
            "v_mfma_f32_16x16x32_bf16 v[156:159], v[140:143], v[192:195], v[156:159]\n\t"
            "v_cvt_pk_bf16_f32 v162, v176, v177\n\t"
            "v_cvt_pk_bf16_f32 v163, v178, v179\n\t"
            "buffer_store_dwordx4 v[160:163], v198, s[40:43], s45 offen offset:64 sc1\n\t"
            "s_nop 1\n\t"
            "v_cvt_pk_bf16_f32 v164, v164, v165\n\t"
            "v_cvt_pk_bf16_f32 v165, v166, v167\n\t"
            "s_nop 0\n\t"
            "v_cvt_pk_bf16_f32 v166, v156, v157\n\t"
            "v_cvt_pk_bf16_f32 v167, v158, v159\n\t"
            "buffer_store_dwordx4 v[164:167], v198, s[40:43], s45 offen offset:128 sc1\n\t"
            "s_nop 7\n\t"
            ".Lre%=:\n\t"
            : "+{v[224:227]}"(f0), "+{v[228:231]}"(f1), "+{v[232:235]}"(f2), "+{v242}"(r242), "+{v243}"(r243), "+{v[244:247]}"(r244),
              "+{s45}"(s45), "+{s46}"(s46), "+{s47}"(s47), "+{s48}"(s48), "+{s67}"(s67), "+{s76}"(s76), "+{s56}"(s56), "+{s44}"(s44), "+{s70}"(s70), "+{s21}"(s21)
            : B_VIN, B_SIN
            : "v0", "v1", "v2", "v3", "v4", "v5", "v6", "v7", "v8", "v9", "v10", "v11", "v12", "v13", "v14", "v15", "v16", "v17", "v18", "v19", "v20", "v21", "v22", "v23", "v24", "v25", "v26", "v27", "v28", "v29", "v30", "v31", "v32", "v33", "v34", "v35", "v36", "v37", "v38", "v39", "v40", "v41", "v42", "v43", "v44", "v45", "v46", "v47", "v48", "v49", "v50", "v51", "v52", "v53", "v54", "v55", "v56", "v57", "v58", "v59", "v60", "v61", "v62", "v63", "v64", "v65", "v66", "v67", "v68", "v69", "v70", "v71", "v72", "v73", "v74", "v75", "v76", "v77", "v78", "v79", "v80", "v81", "v82", "v83", "v84", "v85", "v86", "v87", "v88", "v89", "v90", "v91", "v92", "v93", "v94", "v95", "v96", "v97", "v98", "v99", "v100", "v101", "v102", "v103", "v104", "v105", "v106", "v107", "v108", "v109", "v110", "v111", "v112", "v113", "v114", "v115", "v116", "v117", "v118", "v119", "v120", "v121", "v122", "v123", "v124", "v125", "v126", "v127", "v128", "v129", "v130", "v131", "v132", "v133", "v134", "v135", "v136", "v137", "v138", "v139", "v140", "v141", "v142", "v143", "v144", "v145", "v146", "v147", "v148", "v149", "v150", "v151", "v152", "v153", "v154", "v155", "v156", "v157", "v158", "v159", "v160", "v161", "v162", "v163", "v164", "v165", "v166", "v167", "v168", "v169", "v170", "v171", "v172", "v173", "v174", "v175", "v176", "v177", "v178", "v179", "v180", "v181", "v182", "v183", "v184", "v185", "v186", "v187", "v188", "v189", "v190", "v191", "v192", "v193", "v194", "v195", "v196", "v197", "v198", "v199", "v200", "v201", "v202", "v203", "v204", "v205", "v206", "v207", "v208", "v209", "v210", "v211", "v212", "v213", "v214", "v215", "v216", "v217", "v218", "v219", "v220", "v221", "v222", "v223", "v236", "v237", "v248", "v249", "s26", "s68", "s69", "s79", "s36", "s37", "s38", "s57", "s58", "s59", "s49", "vcc", "scc", "m0", "memory");
        }
        asm volatile("wk_code_end1:");
        asm volatile("wk_code_end2:");

    }
}

extern "C" void kernel_launch(void* const* d_in, const int* in_sizes, int n_in,
                              void* d_out, int out_size, void* d_ws, size_t ws_size,
                              hipStream_t stream) {
    const unsigned short* x = (const unsigned short*)d_in[0];
    const unsigned short* dw = (const unsigned short*)d_in[1];
    const unsigned short* wqkv = (const unsigned short*)d_in[2];
    const unsigned short* wo = (const unsigned short*)d_in[3];
    unsigned short* out = (unsigned short*)d_out;
    u32x4* wf = (u32x4*)d_ws;
    win_attn_kernel<<<GRID, 512, 0, stream>>>(x, dw, wqkv, wo, wf, out);
}
```

```cpp
#include <hip/hip_runtime.h>
#include <hip/hip_bf16.h>
#include <stdint.h>


typedef unsigned u32x4 __attribute__((ext_vector_type(4)));

constexpr int IMG = 511;
constexpr int CH = 192;
constexpr int NWX = 73;
constexpr int NWIN = NWX * NWX;
constexpr int GRID = 256;
constexpr int NSHORT = 21 * GRID - NWIN;
constexpr int ROWBYTES = IMG * CH * 2;
constexpr int XBIAS = (IMG + 1) * CH * 2;
constexpr unsigned OOB = 0x40000000u;

constexpr int X_OFF = 32768;
constexpr int X1_OFF = 53632;
constexpr int O_OFF = 74496;
constexpr int V_OFF = 92928;
constexpr int KQL_OFF = 111360;
constexpr int OL_OFF = 128256;
constexpr int AT_OFF = 137472;
constexpr int CT_OFF = AT_OFF + 19584;
constexpr int RS_OFF = CT_OFF + 4096;
constexpr int WL_OFF = RS_OFF + 2304 + 64;
constexpr int LDS_BYTES = WL_OFF + 128;
#define RS_BASE (RS_OFF + (wave < 4 ? wave * 384 : 768 + wave * 192))
constexpr int TP_OFF = X1_OFF + 8192;
constexpr int STL_OFF = O_OFF + 12288;
#define LB_OFF(b) ((b) < 7 ? V_OFF + (b) * 6160 : X1_OFF + ((b) - 7) * 6160)
#define LPOS(wc) (((wc) & 1) ? 0 : 6)
constexpr unsigned CROW = 0x060a0230u;
constexpr unsigned CLANE = 0x00132C3Fu;

constexpr unsigned long long TOKMAP[6] = {0x20001c190f2a2202ull, 0x052517121f061a13ull, 0x181e08151d0b260cull, 0x1b2b2903102d0901ull, 0x04162e0e2824142cull, 0x110a072127232f0dull};
constexpr unsigned long long ROWMAP[8] = {0x08321b240f1c0006ull, 0x23310e2a09170d14ull, 0x210c2f030a273418ull, 0x1d2b11221f043739ull, 0x3234363a103e192dull, 0x291a20022c3f1605ull, 0x303a3c2628122e15ull, 0x39253d1e01070b13ull};

constexpr int WF_BYTES = 8 * 36 * 1024;
constexpr int EP_OFF = WF_BYTES;
constexpr int CN_OFF = EP_OFF + 256, FL_OFF = EP_OFF + 512;
constexpr int SH_OFF = EP_OFF + 1024;
constexpr int WS_BYTES = SH_OFF + GRID * 12288;
constexpr int GO_OFF = WL_OFF + 124;
constexpr int PG_OFF = WL_OFF + 96;
constexpr int SPIN_MAX = 1 << 20;

#define BAR() asm volatile("s_waitcnt lgkmcnt(0)\n\ts_barrier" ::: "memory")
#define OPQ(x) asm volatile("" : "+v"(x))
__host__ __device__ __forceinline__ constexpr unsigned byte_of(unsigned long long a, unsigned long long b, int i) {
    return (unsigned)(((i & 8) ? b : a) >> (8 * (i & 7))) & 0xFFu;
}
__host__ __device__ __forceinline__ constexpr void seq_to_win(int seq, int& wy, int& wx) {
    const bool last = seq >= 5110;
    const int s = last ? 35 : seq / 146, r = seq - 146 * s;
    const int kx = last ? r / 6 : r >> 2, pos = r - (last ? 6 : 4) * kx;
    const bool low = last && pos >= (kx == 36 ? 2 : 4);
    wy = low ? 72 : 2 * s + (pos & 1);
    wx = 2 * kx + (low ? (kx == 36 ? 0 : pos - 4) : pos >> 1);
}
__host__ __device__ __forceinline__ constexpr int win_to_seq(int wy, int wx) {
    const int kx = wx >> 1;
    return wy == 72 ? 5110 + 6 * kx + (kx == 36 ? 2 : 4 + (wx & 1)) : 146 * (wy >> 1) + (wy >= 70 ? 6 : 4) * kx + 2 * (wx & 1) + (wy & 1);
}
__host__ __device__ __forceinline__ constexpr int block_of(int wy, int wx) { return wy >= 70 ? 1295 + (wx & ~1) + (wy == 72) : 37 * (wy >> 1) + (wx >> 1); }
__host__ __device__ __forceinline__ constexpr void block_pos(int b, int& ky, int& kx) {
    if (b >= 1295) { kx = (b - 1295) >> 1; ky = 70 + 2 * ((b - 1295) & 1); }
    else { const int s = b / 37; kx = b - 37 * s; ky = 2 * s; }
}
__host__ __device__ __forceinline__ constexpr unsigned wl_entry(int seq) {
    int wy = 0, wx = 0;
    seq_to_win(seq, wy, wx);
    return (unsigned)((wy * 7 * IMG + wx * 7) * (CH * 2)) | (wy == 0 ? 0x08000000u : 0u) | (wx == 0 ? 0x10000000u : 0u) |
           (wx == NWX - 1 ? 0x20000000u : 0u) | ((unsigned)(wx & 1) << 30) | ((unsigned)(wy & 1) << 31);
}
__host__ __device__ __forceinline__ constexpr unsigned dma_off(int lane) {
    const int col = (lane * 43) >> 8;
    return (unsigned)(col * (CH * 2 - 96) + 16 * lane) | (col == 0 ? 0x10000000u : 0u) | (col == 8 ? 0x20000000u : 0u);
}

__host__ __device__ __forceinline__ constexpr unsigned pf_off(int lane) {
    const int l = lane < 63 ? lane : 62, r = l / 21;
    return (unsigned)(r * ROWBYTES + (l - 21 * r) * 128) | (r == 0 ? 0x08000000u : 0u);
}
constexpr int RECN = 80;
struct Tables { unsigned rec[GRID][RECN]; unsigned img[768]; };
constexpr Tables make_tables() {
    Tables t = {};
    for (int bx = 0; bx < GRID; ++bx) {
        unsigned* r = t.rec[bx];
        const int q0 = (bx & 7) * 32 + (bx >> 3), cnt = q0 < NSHORT ? 20 : 21, start = q0 < NSHORT ? 20 * q0 : 21 * q0 - NSHORT;
        for (int i = 0; i < 32; ++i) r[i] = i < cnt ? wl_entry(start + i) : 0x7ffffffu;
        int wy = 0, wx = 0;
        seq_to_win(start, wy, wx);
        const int key0 = block_of(wy, wx);
        seq_to_win(start + cnt - 1, wy, wx);
        const int nblk = block_of(wy, wx) - key0 + 1;
        for (int b = 0; b < 8; ++b) {
            int ky = 0, kx = 0;
            block_pos(key0 + b, ky, kx);
            r[64 + b] = b < nblk ? (unsigned)(((7 * ky + 5) * IMG + 14 * kx + 5) * (CH * 2) + XBIAS) | (kx == 36 ? 1u : 0u) : OOB;
            for (int c = 0; c < 4; ++c) {
                const int y = ky + (c >> 1), x = 2 * kx + (c & 1), i = win_to_seq(y, x) - start;
                const bool ok = b < nblk && y < NWX && x < NWX && i >= 0 && i < cnt;
                r[32 + 4 * b + c] = ok ? (unsigned)(((7 * y + LPOS(y)) * IMG + 7 * x + LPOS(x)) * (CH * 2) + i) : OOB + 47;
            }
        }
        r[72] = q0; r[73] = cnt;
    }
    for (int lane = 0; lane < 64; ++lane) {
        for (int e = 0; e < 3; ++e) {
            const int tk = byte_of(TOKMAP[2 * e], TOKMAP[2 * e + 1], lane & 15);
            t.img[192 + 4 * lane + e] = (unsigned)((tk / 7) * ROWBYTES + (tk % 7) * (CH * 2)) + 16 * (lane >> 4);
        }
        t.img[448 + 4 * lane] = dma_off(lane);
        t.img[449 + 4 * lane] = pf_off(lane);
    }
    return t;
}
static __constant__ const Tables TAB = make_tables();
typedef __attribute__((address_space(1))) unsigned gu32;
typedef __attribute__((address_space(1))) unsigned long long gu64;
#define RLX_AGENT __ATOMIC_RELAXED, __HIP_MEMORY_SCOPE_AGENT
#define TAG_OF(e) (((unsigned long long)~((e) + 1u) << 32) | ((e) + 1u))

__global__ __launch_bounds__(512, 2) void win_attn_kernel(const unsigned short* __restrict__ x, const unsigned short* __restrict__ dw,
                                                          const unsigned short* __restrict__ wqkv, const unsigned short* __restrict__ wo,
                                                          u32x4* wf, unsigned short* __restrict__ out) {
    __shared__ __attribute__((aligned(16))) char smem[LDS_BYTES];

    asm volatile("wk_code_begin:");
    const int tid = threadIdx.x;
    const int wave = __builtin_amdgcn_readfirstlane(tid >> 6);
    const int w = wave & 3;
    const unsigned ldsb = (unsigned)(size_t)smem;
#define ROLE_SETUP                                                                                 \
    int bx = blockIdx.x;                                                                           \
    asm volatile("" : "+s"(bx));                                                                   \
    bx = __builtin_amdgcn_readfirstlane(bx);                                                       \
    const unsigned* rec = TAB.rec[bx];                                                             \
    const unsigned en0 = rec[0];                                                                   \
    const int q0 = rec[72], cnt = rec[73];
    asm volatile(
        "s_load_dwordx8 s[24:31], %[ka], 0x0\n\t"
        "s_load_dwordx2 s[32:33], %[ka], 0x20\n\t"
        "s_mul_i32 s36, %[bx], %[recb]\n\t"
        "s_xor_b32 s37, %[wave], 4\n\t"
        "s_lshl2_add_u32 s38, s37, s36\n\t"
        "s_load_dword s38, %[tab], s38 offset:0x100\n\t"
        "s_load_dword s39, %[tab], s36\n\t"
        "v_mbcnt_lo_u32_b32 v1, -1, 0\n\t"
        "v_mbcnt_hi_u32_b32 v1, -1, v1\n\t"
        "s_and_b32 s44, %[bx], 7\n\t"
        "s_lshl_b32 s44, s44, 5\n\t"
        "s_lshr_b32 s45, %[bx], 3\n\t"
        "s_add_u32 s44, s44, s45\n\t"
        "v_lshlrev_b32 v2, 4, v1\n\t"
        "s_mov_b32 s43, 0x20000\n\t"
        "s_cmp_ge_u32 s44, 36\n\t"
        "s_cbranch_scc1 .Lnp%=\n\t"
        "s_mul_i32 s45, s44, 6\n\t"
        "s_lshl1_add_u32 s46, s44, 210\n\t"
        "s_cmp_ge_u32 %[wave], 6\n\t"
        "s_cselect_b64 vcc, -1, 0\n\t"
        "s_cselect_b32 s45, s46, s45\n\t"
        "s_movk_i32 s52, 1152\n\t"
        "s_cselect_b32 s52, 384, s52\n\t"
        "s_cselect_b32 s53, 0, 0xbbaaaaab\n\t"
        "s_add_u32 s45, s45, %[wave]\n\t"
        "s_mul_i32 s46, s45, 171\n\t"
        "s_lshr_b32 s46, s46, 10\n\t"
        "s_mul_i32 s47, s46, 6\n\t"
        "s_sub_u32 s47, s45, s47\n\t"
        "s_mul_i32 s48, s46, 43\n\t"
        "s_lshr_b32 s48, s48, 8\n\t"
        "s_mul_i32 s49, s48, 6\n\t"
        "s_sub_u32 s49, s46, s49\n\t"
        "s_mul_i32 s50, s48, 48\n\t"
        "s_cmp_lt_u32 s49, 3\n\t"
        "s_cselect_b32 s34, 1.0, 0x3e553b94\n\t"
        "s_movk_i32 s51, -48\n\t"
        "s_cselect_b32 s51, 192, s51\n\t"
        "s_cmp_lt_u32 s48, 4\n\t"
        "s_cselect_b32 s51, s51, s50\n\t"
        "s_cselect_b32 s34, s34, 1.0\n\t"
        "s_add_u32 s51, s51, s50\n\t"
        "s_lshl4_add_u32 s51, s49, s51\n\t"
        "s_and_b32 s49, s49, 1\n\t"
        "s_mul_i32 s49, s49, 12\n\t"
        "s_add_u32 s49, s49, 576\n\t"
        "s_cmp_ge_u32 %[wave], 6\n\t"
        "s_cselect_b32 s49, s49, 0\n\t"
        "s_sub_u32 s51, s51, s49\n\t"
        "s_and_b32 s49, s47, 1\n\t"
        "s_mul_i32 s49, s49, 88\n\t"
        "s_lshl3_add_u32 s47, s47, s49\n\t"
        "v_lshrrev_b32 v3, 5, v1\n\t"
        "v_mul_u32_u24 v3, 48, v3\n\t"
        "v_lshrrev_b32 v4, 1, v1\n\t"
        "v_and_or_b32 v3, v4, 8, v3\n\t"
        "v_add_u32 v3, s47, v3\n\t"
        "v_mul_u32_u24 v3, s52, v3\n\t"
        "v_and_b32 v4, 15, v1\n\t"
        "v_and_b32 v5, 12, v1\n\t"
        "v_cndmask_b32 v5, 0, v5, vcc\n\t"
        "v_add3_u32 v5, v4, v5, s51\n\t"
        "v_lshl_add_u32 v3, v5, 1, v3\n\t"
        "v_mov_b64 v[8:9], 0\n\t"
        "v_mov_b64 v[10:11], 0\n\t"
        "v_mov_b64 v[12:13], 0\n\t"
        "v_mov_b64 v[14:15], 0\n\t"
        "s_mul_i32 s42, s52, 192\n\t"
        "s_cmp_ge_u32 %[wave], 6\n\t"
        "s_waitcnt lgkmcnt(0)\n\t"
        "s_cselect_b64 s[40:41], s[30:31], s[28:29]\n\t"
        "s_and_b32 s41, s41, 0xffff\n\t"
        "buffer_load_short_d16_hi v8, v3, s[40:43], 0 offen\n\t"
        "buffer_load_short_d16_hi v9, v3, s[40:43], s52 offen\n\t"
        "s_lshl_b32 s46, s52, 1\n\t"
        "buffer_load_short_d16_hi v10, v3, s[40:43], s46 offen\n\t"
        "s_add_u32 s46, s46, s52\n\t"
        "buffer_load_short_d16_hi v11, v3, s[40:43], s46 offen\n\t"
        "s_add_u32 s46, s46, s52\n\t"
        "buffer_load_short_d16_hi v12, v3, s[40:43], s46 offen\n\t"
        "s_add_u32 s46, s46, s52\n\t"
        "buffer_load_short_d16_hi v13, v3, s[40:43], s46 offen\n\t"
        "s_add_u32 s46, s46, s52\n\t"
        "buffer_load_short_d16_hi v14, v3, s[40:43], s46 offen\n\t"
        "s_add_u32 s46, s46, s52\n\t"
        "buffer_load_short_d16_hi v15, v3, s[40:43], s46 offen\n\t"
        "v_mov_b32 v6, %[ep]\n\t"
        "global_load_dword v7, v6, s[32:33] sc1\n\t"
        "v_mov_b32 v5, %[red]\n\t"
        "v_mad_u32_u24 v4, v4, %[c144], v5\n\t"
        "v_lshrrev_b32 v5, 2, v1\n\t"
        "v_and_b32 v5, 12, v5\n\t"
        "s_lshl_b32 s46, %[wave], 4\n\t"
        "v_add3_u32 v5, v4, v5, s46\n\t"
        "s_mov_b32 s40, s32\n\t"
        "s_and_b32 s41, s33, 0xffff\n\t"
        "s_mov_b32 s42, %[wsb]\n\t"
        "s_lshl_b32 s45, s45, 10\n\t"
        "s_waitcnt vmcnt(1)\n\t"
        "v_add_f32 v16, v8, v10\n\t"
        "v_add_f32 v17, v9, v11\n\t"
        "v_add_f32 v18, v12, v14\n\t"
        "v_add_f32 v19, v13, v15\n\t"
        "v_add_f32 v16, v16, v18\n\t"
        "v_add_f32 v17, v17, v19\n\t"
        "v_add_f32 v16, v16, v17\n\t"
        "ds_write_b32 v5, v16\n\t"
        "s_waitcnt lgkmcnt(0)\n\t"
        "s_barrier\n\t"
        "ds_read_b128 v[16:19], v4\n\t"
        "ds_read_b128 v[20:23], v4 offset:16\n\t"
        "ds_read_b128 v[24:27], v4 offset:32\n\t"
        "ds_read_b128 v[28:31], v4 offset:48\n\t"
        "ds_read_b128 v[32:35], v4 offset:64\n\t"
        "ds_read_b128 v[36:39], v4 offset:80\n\t"
        "s_waitcnt lgkmcnt(4)\n\t"
        "v_add_f32 v16, v16, v18\n\t"
        "v_add_f32 v17, v17, v19\n\t"
        "v_add_f32 v20, v20, v22\n\t"
        "v_add_f32 v21, v21, v23\n\t"
        "s_waitcnt lgkmcnt(2)\n\t"
        "v_add_f32 v24, v24, v26\n\t"
        "v_add_f32 v25, v25, v27\n\t"
        "v_add_f32 v28, v28, v30\n\t"
        "v_add_f32 v29, v29, v31\n\t"
        "v_add_f32 v16, v16, v20\n\t"
        "v_add_f32 v17, v17, v21\n\t"
        "s_waitcnt lgkmcnt(0)\n\t"
        "v_add_f32 v32, v32, v34\n\t"
        "v_add_f32 v33, v33, v35\n\t"
        "v_add_f32 v36, v36, v38\n\t"
        "v_add_f32 v37, v37, v39\n\t"
        "v_add_f32 v24, v24, v28\n\t"
        "v_add_f32 v25, v25, v29\n\t"
        "v_add_f32 v32, v32, v36\n\t"
        "v_add_f32 v33, v33, v37\n\t"
        "v_add_f32 v16, v16, v24\n\t"
        "v_add_f32 v17, v17, v25\n\t"
        "v_add_f32 v16, v16, v32\n\t"
        "v_add_f32 v17, v17, v33\n\t"
        "v_add_f32 v16, v16, v17\n\t"
        "v_mul_f32 v16, s53, v16\n\t"
        "v_mul_f32 v16, s34, v16\n\t"
        "v_fma_f32 v8, v8, s34, v16\n\t"
        "v_fma_f32 v9, v9, s34, v16\n\t"
        "v_fma_f32 v10, v10, s34, v16\n\t"
        "v_fma_f32 v11, v11, s34, v16\n\t"
        "v_fma_f32 v12, v12, s34, v16\n\t"
        "v_fma_f32 v13, v13, s34, v16\n\t"
        "v_fma_f32 v14, v14, s34, v16\n\t"
        "v_fma_f32 v15, v15, s34, v16\n\t"
        "v_cvt_pk_bf16_f32 v8, v8, v9\n\t"
        "v_cvt_pk_bf16_f32 v9, v10, v11\n\t"
        "v_cvt_pk_bf16_f32 v10, v12, v13\n\t"
        "v_cvt_pk_bf16_f32 v11, v14, v15\n\t"
        "buffer_store_dwordx4 v[8:11], v2, s[40:43], s45 offen sc1\n\t"
        "s_or_b32 s46, s44, s37\n\t"
        "s_cmp_lg_u32 s46, 0\n\t"
        "s_cbranch_scc1 .Lnc%=\n\t"
        "v_mov_b32 v3, 0\n\t"
        "global_store_dword v6, v3, s[32:33] offset:256 sc1\n\t"
        ".Lnc%=:\n\t"
        "v_lshl_add_u32 v6, s44, 3, v6\n\t"
        "s_waitcnt vmcnt(0)\n\t"
        "s_barrier\n\t"
        "s_cmp_lg_u32 %[wave], 0\n\t"
        "s_cbranch_scc1 .Lnp%=\n\t"
        "v_add_u32 v4, 1, v7\n\t"
        "v_not_b32 v5, v4\n\t"
        "s_mov_b64 exec, 1\n\t"
        "global_store_dwordx2 v6, v[4:5], s[32:33] offset:512 sc1\n\t"
        "s_mov_b64 exec, -1\n\t"
        ".Lnp%=:\n\t"
        "s_getpc_b64 s[40:41]\n\t"
        ".Lwk_here%=:\n\t"
        "s_sub_u32 s40, s40, .Lwk_here%=-wk_code_begin\n\t"
        "s_subb_u32 s41, s41, 0\n\t"
        "s_and_b32 s41, s41, 0xffff\n\t"
        "s_mov_b32 s42, wk_code_end-wk_code_begin\n\t"
        "s_max_u32 s42, s42, wk_code_end1-wk_code_begin\n\t"
        "s_max_u32 s42, s42, wk_code_end2-wk_code_begin\n\t"
        "s_add_u32 m0, %[lds], %[sink]\n\t"
        "v_lshlrev_b32 v3, 7, v1\n\t"
        "s_lshl_b32 s45, %[wave], 13\n\t"
        "buffer_load_dword v3, s[40:43], s45 offen lds\n\t"
        "s_mov_b64 s[40:41], %[tab]\n\t"
        "s_and_b32 s41, s41, 0xffff\n\t"
        "s_mov_b32 s42, %[tabb]\n\t"
        "s_lshl_b32 s46, %[wave], 10\n\t"
        "s_add_u32 s47, s46, %[imgs]\n\t"
        "s_add_u32 s48, s46, %[imgd]\n\t"
        "s_lshl_b32 s49, %[wave], 8\n\t"
        "s_add_u32 s49, s49, %[mapd]\n\t"
        "s_add_u32 s50, s36, 128\n\t"
        "s_mov_b64 vcc, -1\n\t"
        "s_cmp_eq_u32 %[wave], 0\n\t"
        "s_cselect_b32 s47, s36, s47\n\t"
        "s_cselect_b32 s48, %[wld], s48\n\t"
        "s_cselect_b64 vcc, 0xff, vcc\n\t"
        "s_cmp_ge_u32 %[wave], 4\n\t"
        "s_cselect_b32 s47, s50, s47\n\t"
        "s_cselect_b32 s48, s49, s48\n\t"
        "s_cselect_b64 exec, 0xff, vcc\n\t"
        "s_add_u32 m0, %[lds], s48\n\t"
        "s_and_b32 s49, %[wave], 3\n\t"
        "buffer_load_dwordx4 v2, s[40:43], s47 offen lds\n\t"
        "s_mov_b64 exec, -1\n\t"
        "v_mul_u32_u24 v3, 43, v1\n\t"
        "v_lshrrev_b32 v3, 8, v3\n\t"
        "v_mad_u32_u24 v4, v3, %[c288], v2\n\t"
        "s_mul_i32 s49, s49, 96\n\t"
        "s_mul_i32 s50, s37, 6160\n\t"
        "s_add_u32 s50, s50, %[lb0]\n\t"
        "s_cmp_eq_u32 s37, 7\n\t"
        "s_cselect_b32 s50, %[lb7], s50\n\t"
        "s_add_u32 s46, s46, %[tp]\n\t"
        "s_waitcnt lgkmcnt(0)\n\t"
        "s_cmp_ge_u32 %[wave], 4\n\t"
        "s_cbranch_scc1 .Lib%=\n\t"
        "s_mov_b32 s40, s26\n\t"
        "s_and_b32 s41, s27, 0xffff\n\t"
        "s_movk_i32 s42, 3456\n\t"
        "s_add_u32 m0, %[lds], s46\n\t"
        "s_nop 0\n\t"
        "buffer_load_dwordx4 v4, s[40:43], s49 offen lds\n\t"
        ".Lib%=:\n\t"
        "s_sub_u32 s40, s24, %[xb]\n\t"
        "s_subb_u32 s41, s25, 0\n\t"
        "s_and_b32 s41, s41, 0xffff\n\t"
        "s_mov_b32 s42, %[xsz]\n\t"
        "s_cmp_eq_u32 s38, 2.0\n\t"
        "s_cbranch_scc1 .Lic%=\n\t"
        "s_mov_b32 exec_hi, 0xffff\n\t"
        "s_add_u32 m0, %[lds], s50\n\t"
        "s_mov_b32 s46, 4\n\t"
        "s_bitcmp1_b32 s38, 0\n\t"
        "s_cselect_b32 s47, 2.0, s38\n\t"
        "s_bitset0_b32 s38, 0\n\t"
        ".Lid%=:\n\t"
        "buffer_load_dwordx4 v2, s[40:43], s38 offen lds\n\t"
        "buffer_load_dwordx4 v2, s[40:43], s47 offen offset:768 lds\n\t"
        "s_add_u32 m0, m0, 1536\n\t"
        "s_add_u32 s38, s38, %[rb]\n\t"
        "s_add_u32 s47, s47, %[rb]\n\t"
        "s_sub_u32 s46, s46, 1\n\t"
        "s_cmp_lg_u32 s46, 0\n\t"
        "s_cbranch_scc1 .Lid%=\n\t"
        ".Lic%=:\n\t"
        "s_cmp_lt_u32 %[wave], 4\n\t"
        "s_cbranch_scc1 .Lie%=\n\t"
        "s_mov_b32 exec_hi, 0x3fffff\n\t"
        "s_and_b32 s46, s39, 0x10000000\n\t"
        "s_and_b32 s47, s39, 0x20000000\n\t"
        "v_cmp_eq_u32 vcc, 0, v3\n\t"
        "v_mov_b32 v5, s46\n\t"
        "v_mov_b32 v6, s47\n\t"
        "v_cndmask_b32 v5, 0, v5, vcc\n\t"
        "v_cmp_eq_u32 vcc, 8, v3\n\t"
        "s_and_b32 s46, s39, 0x7ffffff\n\t"
        "s_add_u32 s46, s46, s49\n\t"
        "v_cndmask_b32 v6, 0, v6, vcc\n\t"
        "v_or3_b32 v4, v4, v5, v6\n\t"
        "s_bitcmp1_b32 s39, 27\n\t"
        "s_cselect_b32 s47, 2.0, s46\n\t"
        "s_lshl_b32 s45, s37, 13\n\t"
        "s_add_u32 m0, %[lds], s45\n\t"
        "s_mov_b32 s48, 8\n\t"
        "buffer_load_dwordx4 v4, s[40:43], s47 offen lds\n\t"
        ".Lia%=:\n\t"
        "s_add_u32 m0, m0, 872\n\t"
        "s_add_u32 s46, s46, %[rb]\n\t"
        "s_sub_u32 s48, s48, 1\n\t"
        "buffer_load_dwordx4 v4, s[40:43], s46 offen lds\n\t"
        "s_cmp_lg_u32 s48, 0\n\t"
        "s_cbranch_scc1 .Lia%=\n\t"
        ".Lie%=:\n\t"
        "s_mov_b64 exec, -1\n\t"
        :: [ka] "s"(__builtin_amdgcn_kernarg_segment_ptr()), [bx] "s"(blockIdx.x), [wave] "s"(wave), [tab] "s"(&TAB), [lds] "s"(ldsb),
           [red] "s"(ldsb + O_OFF), [c144] "s"(144), [c288] "s"(288), [recb] "n"(RECN * 4), [ep] "n"(EP_OFF), [wsb] "n"(WS_BYTES), [sink] "n"(X_OFF + 18432),
           [tabb] "n"(sizeof(Tables)), [imgs] "n"(GRID * RECN * 4 - 1024), [imgd] "n"(AT_OFF + 18816 - 1024), [mapd] "n"(TP_OFF + 3072), [wld] "n"(WL_OFF),
           [lb0] "n"(LB_OFF(0)), [lb7] "n"(LB_OFF(7)), [tp] "n"(TP_OFF), [xb] "n"(XBIAS), [xsz] "n"(IMG * IMG * CH * 2 + XBIAS), [rb] "n"(ROWBYTES)
        : "v1", "v2", "v3", "v4", "v5", "v6", "v7", "v8", "v9", "v10", "v11", "v12", "v13", "v14", "v15", "v16", "v17", "v18", "v19", "v20", "v21", "v22", "v23", "v24", "v25", "v26", "v27", "v28", "v29", "v30", "v31", "v32", "v33", "v34", "v35", "v36", "v37", "v38", "v39", "s24", "s25", "s26", "s27", "s28", "s29", "s30", "s31", "s32", "s33", "s34", "s36", "s37", "s38", "s39", "s40", "s41", "s42", "s43", "s44", "s45", "s46", "s47", "s48", "s49", "s50", "s51", "s52", "s53", "m0", "vcc", "scc", "memory");
#define WAIT_GO                                                                                    \
        {                                                                                          \
            unsigned go_;                                                                          \
            asm volatile(".Lgo%=:\n\tds_read_b32 %0, %1\n\ts_waitcnt lgkmcnt(0)\n\tv_cmp_ne_u32 vcc, -1, %0\n\ts_nop 4\n\ts_cbranch_vccnz .Lgo%=" \
                         : "=&v"(go_) : "v"(ldsb + GO_OFF) : "vcc", "memory");                     \
        }
#define LANE_SETUP                                                                                 \
    int lane;                                                                                      \
    asm volatile("v_mbcnt_lo_u32_b32 %0, -1, 0\n\tv_mbcnt_hi_u32_b32 %0, -1, %0" : "=v"(lane));    \
    __builtin_assume(lane >= 0 && lane < 64);                                                      \
    const int g = lane >> 4, l15 = lane & 15, sw = l15 >> 1;                                       \
    int fE = X_OFF + l15 * 384 + ((g ^ sw) << 4);                                                  \
    OPQ(fE);
#define XPAT(r) (((r) < 48 ? (r) : 48) * 384 + (((2 * w) ^ (((r) >> 1) & 7)) << 4))
#define SPAT(r) ((w >> 1) * 1024 + (w & 1) * 8 + (r) * 16)
#define CONV_CONSTS(var) \
            const unsigned long long rlo = g == 0 ? ROWMAP[0] : (g == 1 ? ROWMAP[2] : (g == 2 ? ROWMAP[4] : ROWMAP[6])); \
            const unsigned long long rhi = g == 0 ? ROWMAP[1] : (g == 1 ? ROWMAP[3] : (g == 2 ? ROWMAP[5] : ROWMAP[7])); \
            int xrow = byte_of(rlo, rhi, l15); \
            if (lane == (int)((CLANE >> (8 * (var))) & 0xFF)) xrow = 48; \
            if (lane == 63) xrow = (CROW >> (8 * (var))) & 0xFF; \
            const unsigned pXw_ = XPAT(xrow); \
            const unsigned pSTw_ = (w >> 1) * 1024 + (w & 1) * 8 + xrow * 16; \
            const int oy = (0x5320 >> (4 * (l15 >> 2))) & 15, ox = (0x5320 >> (4 * (l15 & 3))) & 15; \
            const unsigned aB_ = ldsb + w * 8192 + (oy + (g >> 1)) * 872 + (ox + 2 * (g & 1)) * 96; \
            const int dyi = g - (l15 >> 3); \
            const unsigned aA_ = ldsb + AT_OFF + (l15 & 3) * 16 + \
                                 ((dyi < 0 || dyi > 2) ? 18816 : (dyi == 0 ? 0 : (dyi == 1 ? 6208 : 12544)) + ((l15 >> 2) & 1) * 3072 + w * 768); \
            const unsigned idx_ = (l15 & 3) * 0x5555u; \
            const unsigned se = (0 || l15 == 4 * g) ? 0x3F803F80u : 0u; \
            const u32x4 sel4 = {se, se, se, se};

    if (wave < 4) {
        ROLE_SETUP
        LANE_SETUP
        asm volatile("s_waitcnt vmcnt(0)" ::: "memory");
        if (lane < 48) {
            unsigned tp[9];
#pragma unroll
            for (int k = 0; k < 9; ++k) tp[k] = *(const unsigned short*)(smem + TP_OFF + w * 1024 + k * 96 + lane * 2);
            tp[4] |= 0x3F800000u;
#pragma unroll
            for (int dy = 0; dy < 3; ++dy) {
                char* p = smem + AT_OFF + (dy == 0 ? 0 : (dy == 1 ? 6208 : 12544)) + w * 768 + lane * 16;
                *(u32x4*)p = (u32x4){tp[3 * dy], tp[3 * dy + 1], tp[3 * dy + 2], 0u};
                *(u32x4*)(p + 3072) = (u32x4){0u, tp[3 * dy], tp[3 * dy + 1], tp[3 * dy + 2]};
            }
        }
        BAR();
        {
            const int dW = X_OFF + l15 * 384 + (((2 * w + (g >> 1)) ^ sw) << 4) + 8 * (g & 1);
            const unsigned rsb = ldsb + RS_BASE;
            const unsigned pE_ = fE - X_OFF, aO_ = ldsb + dW + (O_OFF - X_OFF), aZ_ = ldsb + AT_OFF + 18816, aVB_ = ldsb + V_OFF + w * 4608 + 1024 + lane * 8;
            unsigned aRSw_ = rsb + lane * 4;
            const unsigned pST_ = lane * 16, aPLw_ = rsb + lane * 2, aRSr_ = rsb + l15 * 4;
            const unsigned g16_ = g * 16, aPLr_ = (l15 & 3) ? aZ_ : rsb + g * 8;
            const unsigned loVL_ = ((2 * w + (l15 >> 3)) << 4) + (l15 & 7) * 2, loOL_ = ((2 * w + (g >> 1)) << 4) + 8 * (g & 1);
            const unsigned on = (l15 & 3) ? 0u : 0x3F803F80u;
            const u32x4 ones4 = {on, on, on, on};
            const unsigned snk = ldsb + O_OFF + 15360 + w * 192, kr = ldsb + KQL_OFF + l15 * 800;
            const bool ok1 = 16 + l15 < cnt, dg = g == (l15 >> 2);
            u32x4 pl = {kr + w * 192 + g * 16, (ok1 ? kr + 12800 + w * 192 : snk) + g * 16, dg ? kr + 768 + w * 4 : snk, dg && ok1 ? kr + 12800 + 768 + w * 4 : snk};
            unsigned pcl, pch, len;
            asm volatile("s_getpc_b64 s[36:37]\n.Lwk_h2%=:\n\ts_sub_u32 s36, s36, .Lwk_h2%=-wk_code_begin\n\ts_subb_u32 s37, s37, 0\n\t"
                         "s_mov_b32 %2, wk_code_end-wk_code_begin\n\ts_max_u32 %2, %2, wk_code_end1-wk_code_begin" : "={s36}"(pcl), "={s37}"(pch), "=s"(len) :: "scc");
            pcl = __builtin_amdgcn_readfirstlane(pcl); pch = __builtin_amdgcn_readfirstlane(pch); len = __builtin_amdgcn_readfirstlane(len);
            const unsigned long long tb = w == 0 ? ((unsigned long long)pch << 32) | pcl : (unsigned long long)wf;
            const u32x4 tr = {(unsigned)tb, (unsigned)(tb >> 32) & 0xffffu, w == 0 ? len : (w == 1 ? (unsigned)WF_BYTES : SH_OFF + bx * 12288 + 12288), 0x00020000u};
            const unsigned to = w == 0 ? ((bx >> 3) % 6) * 8192 : (w == 1 ? 6 * 36864 + ((bx >> 3) % 9) * 8192 : SH_OFF + bx * 12288 + (w - 2) * 8192);
            asm volatile("" :: "{v244}"(pE_), "{v245}"(aO_), "{v246}"(aZ_), "{v247}"(aVB_), "{v248}"(pST_), "{v249}"(aRSw_), "{v250}"(aPLw_), "{v251}"(aRSr_), "{v252}"(g16_), "{v253}"(aPLr_), "{v254}"(loVL_), "{v255}"(loOL_), "{v[240:243]}"(ones4), "{v[232:235]}"(pl), "{s[36:39]}"(tr), "{s41}"(to));
            if (w == 0) {
                gu64* fp = (gu64*)((char*)wf + (lane < 36 ? FL_OFF + lane * 8 : EP_OFF));
#pragma unroll 1
                for (int it = 0; it < SPIN_MAX; ++it) {
                    const unsigned long long f = __hip_atomic_load(fp, RLX_AGENT);
                    const unsigned ep = __builtin_amdgcn_readlane((unsigned)f, 63);
                    if ((__builtin_amdgcn_ballot_w64(f != TAG_OF(ep)) & 0xfffffffffull) == 0ull) break;
                    __builtin_amdgcn_s_sleep(1);
                }
                __builtin_amdgcn_fence(__ATOMIC_ACQUIRE, "wavefront");
                asm volatile("ds_write_b32 %0, %1" :: "v"(ldsb + GO_OFF), "v"(~0u) : "memory");
            } else WAIT_GO
            __builtin_amdgcn_s_setprio(2);
            unsigned s44 = cnt, s48 = ldsb + X_OFF, s70 = 0;
            unsigned s72 = ldsb + KQL_OFF + w * 192, s73 = ldsb + OL_OFF, s77 = ldsb + OL_OFF, s76 = ldsb + KQL_OFF + 768 + w * 4;
            const unsigned long long wb = (unsigned long long)wf;
            const u32x4 wr = {(unsigned)wb, (unsigned)(wb >> 32) & 0xffffu, (unsigned)WS_BYTES, 0x00020000u};
            asm volatile(
            "s_setprio 2\n\t"
            "s_add_u32 s49, s33, 0\n\t"
            "buffer_load_dwordx4 v[0:3], v248, s[28:31], s49 offen sc1\n\t"
            "s_add_u32 s49, s33, 1024\n\t"
            "buffer_load_dwordx4 v[4:7], v248, s[28:31], s49 offen sc1\n\t"
            "s_add_u32 s49, s33, 2048\n\t"
            "buffer_load_dwordx4 v[8:11], v248, s[28:31], s49 offen sc1\n\t"
            "s_add_u32 s49, s33, 3072\n\t"
            "buffer_load_dwordx4 v[12:15], v248, s[28:31], s49 offen sc1\n\t"
            "s_add_u32 s49, s33, 4096\n\t"
            "buffer_load_dwordx4 v[16:19], v248, s[28:31], s49 offen sc1\n\t"
            "s_add_u32 s49, s33, 5120\n\t"
            "buffer_load_dwordx4 v[20:23], v248, s[28:31], s49 offen sc1\n\t"
            "s_add_u32 s49, s33, 6144\n\t"
            "buffer_load_dwordx4 v[24:27], v248, s[28:31], s49 offen sc1\n\t"
            "s_add_u32 s49, s33, 7168\n\t"
            "buffer_load_dwordx4 v[28:31], v248, s[28:31], s49 offen sc1\n\t"
            "s_add_u32 s49, s33, 8192\n\t"
            "buffer_load_dwordx4 v[32:35], v248, s[28:31], s49 offen sc1\n\t"
            "s_add_u32 s49, s33, 9216\n\t"
            "buffer_load_dwordx4 v[36:39], v248, s[28:31], s49 offen sc1\n\t"
            "s_add_u32 s49, s33, 10240\n\t"
            "buffer_load_dwordx4 v[40:43], v248, s[28:31], s49 offen sc1\n\t"
            "s_add_u32 s49, s33, 11264\n\t"
            "buffer_load_dwordx4 v[44:47], v248, s[28:31], s49 offen sc1\n\t"
            "s_add_u32 s49, s33, 12288\n\t"
            "buffer_load_dwordx4 v[48:51], v248, s[28:31], s49 offen sc1\n\t"
            "s_add_u32 s49, s33, 13312\n\t"
            "buffer_load_dwordx4 v[52:55], v248, s[28:31], s49 offen sc1\n\t"
            "s_add_u32 s49, s33, 14336\n\t"
            "buffer_load_dwordx4 v[56:59], v248, s[28:31], s49 offen sc1\n\t"
            "s_add_u32 s49, s33, 15360\n\t"
            "buffer_load_dwordx4 v[60:63], v248, s[28:31], s49 offen sc1\n\t"
            "v_mov_b32 v224, s46\n\t"
            ".Ls1%=:\n\t"
            "ds_read_b128 v[220:223], v224\n\t"
            "s_waitcnt lgkmcnt(0)\n\t"
            "v_and_b32 v220, v220, v221\n\t"
            "v_and_b32 v222, v222, v223\n\t"
            "v_and_b32 v220, v220, v222\n\t"
            "s_nop 0\n\t"
            "v_readfirstlane_b32 s49, v220\n\t"
            "s_nop 3\n\t"
            "s_bitcmp1_b32 s49, 27\n\t"
            "s_cbranch_scc0 .Ls1%=\n\t"
            "s_nop 7\n\t"
            "v_add_u32 v237, s35, v248\n\t"
            "v_add_u32 v238, s56, v244\n\t"
            "v_xor_b32 v239, 64, v238\n\t"
            "v_add_u32 v236, s56, v248\n\t"
            "ds_read_b128 v[200:203], v237\n\t"
            "ds_read_b128 v[204:207], v237 offset:1024\n\t"
            "ds_read_b128 v[144:147], v238\n\t"
            "ds_read_b128 v[148:151], v239\n\t"
            "ds_read_b128 v[152:155], v238 offset:128\n\t"
            "ds_read_b128 v[156:159], v239 offset:128\n\t"
            "ds_read_b128 v[160:163], v238 offset:256\n\t"
            "ds_read_b128 v[164:167], v239 offset:256\n\t"
            "ds_read_b128 v[168:171], v238 offset:6144\n\t"
            "ds_read_b128 v[172:175], v239 offset:6144\n\t"
            "ds_read_b128 v[176:179], v238 offset:6272\n\t"
            "ds_read_b128 v[180:183], v239 offset:6272\n\t"
            "ds_read_b128 v[184:187], v238 offset:6400\n\t"
            "ds_read_b128 v[188:191], v239 offset:6400\n\t"
            "ds_read_b128 v[208:211], v237 offset:2048\n\t"
            "s_waitcnt lgkmcnt(14)\n\t"
            "ds_read_b128 v[224:227], v236 offset:12288\n\t"
            "s_waitcnt lgkmcnt(14)\n\t"
            "ds_read_b128 v[228:231], v236 offset:13312\n\t"
            "s_add_u32 s49, s33, 16384\n\t"
            "buffer_load_dwordx4 v[64:67], v248, s[28:31], s49 offen sc1\n\t"
            "s_add_u32 s49, s33, 17408\n\t"
            "buffer_load_dwordx4 v[68:71], v248, s[28:31], s49 offen sc1\n\t"
            "s_waitcnt vmcnt(12)\n\t"
            "s_waitcnt lgkmcnt(14)\n\t"
            "v_mfma_f32_16x16x32_bf16 v[192:195], v[0:3], v[144:147], 0\n\t"
            "buffer_store_dwordx4 v[200:203], v248, s[28:31], s34 offen\n\t"
            "s_waitcnt lgkmcnt(8)\n\t"
            "v_mfma_f32_16x16x32_bf16 v[212:215], v[0:3], v[168:171], 0\n\t"
            "buffer_store_dwordx4 v[204:207], v248, s[28:31], s34 offen offset:1024\n\t"
            "v_mfma_f32_16x16x32_bf16 v[192:195], v[4:7], v[148:151], v[192:195]\n\t"
            "s_waitcnt lgkmcnt(2)\n\t"
            "buffer_store_dwordx4 v[208:211], v248, s[28:31], s34 offen offset:2048\n\t"
            "v_mfma_f32_16x16x32_bf16 v[212:215], v[4:7], v[172:175], v[212:215]\n\t"
            "s_add_u32 s49, s33, 18432\n\t"
            "buffer_load_dwordx4 v[72:75], v248, s[28:31], s49 offen sc1\n\t"
            "v_mfma_f32_16x16x32_bf16 v[192:195], v[8:11], v[152:155], v[192:195]\n\t"
            "s_add_u32 s49, s33, 19456\n\t"
            "buffer_load_dwordx4 v[76:79], v248, s[28:31], s49 offen sc1\n\t"
            "v_mfma_f32_16x16x32_bf16 v[212:215], v[8:11], v[176:179], v[212:215]\n\t"
            "s_add_u32 s49, s33, 20480\n\t"
            "buffer_load_dwordx4 v[80:83], v248, s[28:31], s49 offen sc1\n\t"
            "v_mfma_f32_16x16x32_bf16 v[192:195], v[12:15], v[156:159], v[192:195]\n\t"
            "s_add_u32 s49, s33, 21504\n\t"
            "buffer_load_dwordx4 v[84:87], v248, s[28:31], s49 offen sc1\n\t"
            "v_mfma_f32_16x16x32_bf16 v[212:215], v[12:15], v[180:183], v[212:215]\n\t"
            "s_add_u32 s49, s33, 22528\n\t"
            "buffer_load_dwordx4 v[88:91], v248, s[28:31], s49 offen sc1\n\t"
            "v_mfma_f32_16x16x32_bf16 v[192:195], v[16:19], v[160:163], v[192:195]\n\t"
            "s_add_u32 s49, s33, 23552\n\t"
            "buffer_load_dwordx4 v[92:95], v248, s[28:31], s49 offen sc1\n\t"
            "v_mfma_f32_16x16x32_bf16 v[212:215], v[16:19], v[184:187], v[212:215]\n\t"
            "s_waitcnt lgkmcnt(1)\n\t"
            "v_add_f32 v224, v224, v226\n\t"
            "s_waitcnt lgkmcnt(0)\n\t"
            "v_add_f32 v228, v228, v230\n\t"
            "v_mfma_f32_16x16x32_bf16 v[192:195], v[20:23], v[164:167], v[192:195]\n\t"
            "v_add_f32 v224, v224, v228\n\t"
            "v_add_f32 v225, v225, v227\n\t"
            "v_mfma_f32_16x16x32_bf16 v[212:215], v[20:23], v[188:191], v[212:215]\n\t"
            "s_waitcnt vmcnt(15)\n\t"
            "s_add_u32 s49, s33, 24576\n\t"
            "buffer_load_dwordx4 v[96:99], v248, s[28:31], s49 offen sc1\n\t"
            "v_mfma_f32_16x16x32_bf16 v[196:199], v[24:27], v[144:147], 0\n\t"
            "s_add_u32 s49, s33, 25600\n\t"
            "buffer_load_dwordx4 v[100:103], v248, s[28:31], s49 offen sc1\n\t"
            "v_mfma_f32_16x16x32_bf16 v[216:219], v[24:27], v[168:171], 0\n\t"
            "s_add_u32 s49, s33, 26624\n\t"
            "buffer_load_dwordx4 v[104:107], v248, s[28:31], s49 offen sc1\n\t"
            "v_mfma_f32_16x16x32_bf16 v[196:199], v[28:31], v[148:151], v[196:199]\n\t"
            "s_add_u32 s49, s33, 27648\n\t"
            "buffer_load_dwordx4 v[108:111], v248, s[28:31], s49 offen sc1\n\t"
            "v_mfma_f32_16x16x32_bf16 v[216:219], v[28:31], v[172:175], v[216:219]\n\t"
            "s_add_u32 s49, s33, 28672\n\t"
            "buffer_load_dwordx4 v[112:115], v248, s[28:31], s49 offen sc1\n\t"
            "v_mfma_f32_16x16x32_bf16 v[196:199], v[32:35], v[152:155], v[196:199]\n\t"
            "s_add_u32 s49, s33, 29696\n\t"
            "buffer_load_dwordx4 v[116:119], v248, s[28:31], s49 offen sc1\n\t"
            "v_mfma_f32_16x16x32_bf16 v[216:219], v[32:35], v[176:179], v[216:219]\n\t"
            "v_add_f32 v229, v229, v231\n\t"
            "v_mul_f32 v224, 0x3baaaaab, v224\n\t"
            "v_mfma_f32_16x16x32_bf16 v[196:199], v[36:39], v[156:159], v[196:199]\n\t"
            "v_add_f32 v225, v225, v229\n\t"
            "v_mul_f32_e64 v224, v224, -v224\n\t"
            "v_mfma_f32_16x16x32_bf16 v[216:219], v[36:39], v[180:183], v[216:219]\n\t"
            "v_fmac_f32 v224, 0x3baaaaab, v225\n\t"
            "v_max_f32 v224, 0, v224\n\t"
            "v_mfma_f32_16x16x32_bf16 v[196:199], v[40:43], v[160:163], v[196:199]\n\t"
            "v_add_f32 v224, 0x3727c5ac, v224\n\t"
            "v_rsq_f32 v224, v224\n\t"
            "v_mfma_f32_16x16x32_bf16 v[216:219], v[40:43], v[184:187], v[216:219]\n\t"
            "ds_write_b32 v249, v224\n\t"
            "v_mfma_f32_16x16x32_bf16 v[196:199], v[44:47], v[164:167], v[196:199]\n\t"
            "ds_read_b32 v236, v251\n\t"
            "v_mfma_f32_16x16x32_bf16 v[216:219], v[44:47], v[188:191], v[216:219]\n\t"
            "ds_read_b32 v237, v251 offset:64\n\t"
            "s_waitcnt vmcnt(15)\n\t"
            "v_mfma_f32_16x16x32_bf16 v[200:203], v[48:51], v[144:147], 0\n\t"
            "s_add_u32 s49, s33, 30720\n\t"
            "buffer_load_dwordx4 v[120:123], v248, s[28:31], s49 offen sc1\n\t"
            "v_mfma_f32_16x16x32_bf16 v[220:223], v[48:51], v[168:171], 0\n\t"
            "s_add_u32 s49, s33, 31744\n\t"
            "buffer_load_dwordx4 v[124:127], v248, s[28:31], s49 offen sc1\n\t"
            "v_mfma_f32_16x16x32_bf16 v[200:203], v[52:55], v[148:151], v[200:203]\n\t"
            "s_add_u32 s49, s33, 32768\n\t"
            "buffer_load_dwordx4 v[128:131], v248, s[28:31], s49 offen sc1\n\t"
            "v_mfma_f32_16x16x32_bf16 v[220:223], v[52:55], v[172:175], v[220:223]\n\t"
            "s_add_u32 s49, s33, 33792\n\t"
            "buffer_load_dwordx4 v[132:135], v248, s[28:31], s49 offen sc1\n\t"
            "v_mfma_f32_16x16x32_bf16 v[200:203], v[56:59], v[152:155], v[200:203]\n\t"
            "s_add_u32 s49, s33, 34816\n\t"
            "buffer_load_dwordx4 v[136:139], v248, s[28:31], s49 offen sc1\n\t"
            "v_mfma_f32_16x16x32_bf16 v[220:223], v[56:59], v[176:179], v[220:223]\n\t"
            "s_add_u32 s49, s33, 35840\n\t"
            "buffer_load_dwordx4 v[140:143], v248, s[28:31], s49 offen sc1\n\t"
            "s_setprio 0\n\t"
            "v_mfma_f32_16x16x32_bf16 v[200:203], v[60:63], v[156:159], v[200:203]\n\t"
            "s_waitcnt lgkmcnt(1)\n\t"
            "v_mul_f32 v192, v236, v192\n\t"
            "v_mul_f32 v193, v236, v193\n\t"
            "v_mfma_f32_16x16x32_bf16 v[220:223], v[60:63], v[180:183], v[220:223]\n\t"
            "s_waitcnt lgkmcnt(0)\n\t"
            "v_mul_f32 v212, v237, v212\n\t"
            "v_mul_f32 v213, v237, v213\n\t"
            "v_mfma_f32_16x16x32_bf16 v[200:203], v[64:67], v[160:163], v[200:203]\n\t"
            "v_mul_f32 v194, v236, v194\n\t"
            "v_mul_f32 v195, v236, v195\n\t"
            "v_mfma_f32_16x16x32_bf16 v[220:223], v[64:67], v[184:187], v[220:223]\n\t"
            "v_cvt_pk_bf16_f32 v192, v192, v193\n\t"
            "v_mul_f32 v214, v237, v214\n\t"
            "v_mfma_f32_16x16x32_bf16 v[200:203], v[68:71], v[164:167], v[200:203]\n\t"
            "v_mul_f32 v215, v237, v215\n\t"
            "v_cvt_pk_bf16_f32 v212, v212, v213\n\t"
            "v_mfma_f32_16x16x32_bf16 v[220:223], v[68:71], v[188:191], v[220:223]\n\t"
            "s_waitcnt vmcnt(12)\n\t"
            "v_cvt_pk_bf16_f32 v193, v194, v195\n\t"
            "v_mfma_f32_16x16x32_bf16 v[204:207], v[72:75], v[144:147], 0\n\t"
            "v_cvt_pk_bf16_f32 v213, v214, v215\n\t"
            "v_mul_f32 v196, v236, v196\n\t"
            "v_mfma_f32_16x16x32_bf16 v[224:227], v[72:75], v[168:171], 0\n\t"
            "v_mul_f32 v197, v236, v197\n\t"
            "v_mul_f32 v198, v236, v198\n\t"
            "v_mfma_f32_16x16x32_bf16 v[204:207], v[76:79], v[148:151], v[204:207]\n\t"
            "v_mul_f32 v199, v236, v199\n\t"
            "v_mul_f32 v216, v237, v216\n\t"
            "v_mfma_f32_16x16x32_bf16 v[224:227], v[76:79], v[172:175], v[224:227]\n\t"
            "v_mul_f32 v217, v237, v217\n\t"
            "v_mul_f32 v218, v237, v218\n\t"
            "v_mfma_f32_16x16x32_bf16 v[204:207], v[80:83], v[152:155], v[204:207]\n\t"
            "v_mul_f32 v219, v237, v219\n\t"
            "v_cvt_pk_bf16_f32 v194, v196, v197\n\t"
            "v_mfma_f32_16x16x32_bf16 v[224:227], v[80:83], v[176:179], v[224:227]\n\t"
            "v_cvt_pk_bf16_f32 v195, v198, v199\n\t"
            "v_cvt_pk_bf16_f32 v214, v216, v217\n\t"
            "v_mfma_f32_16x16x32_bf16 v[204:207], v[84:87], v[156:159], v[204:207]\n\t"
            "v_cvt_pk_bf16_f32 v215, v218, v219\n\t"
            "v_mul_f32 v200, v236, v200\n\t"
            "v_mfma_f32_16x16x32_bf16 v[224:227], v[84:87], v[180:183], v[224:227]\n\t"
            "v_mul_f32 v201, v236, v201\n\t"
            "v_mul_f32 v220, v237, v220\n\t"
            "v_mfma_f32_16x16x32_bf16 v[204:207], v[88:91], v[160:163], v[204:207]\n\t"
            "v_mul_f32 v221, v237, v221\n\t"
            "v_mul_f32 v202, v236, v202\n\t"
            "v_mfma_f32_16x16x32_bf16 v[224:227], v[88:91], v[184:187], v[224:227]\n\t"
            "v_mul_f32 v203, v236, v203\n\t"
            "v_cvt_pk_bf16_f32 v200, v200, v201\n\t"
            "v_mfma_f32_16x16x32_bf16 v[204:207], v[92:95], v[164:167], v[204:207]\n\t"
            "v_mul_f32 v222, v237, v222\n\t"
            "v_mul_f32 v223, v237, v223\n\t"
            "v_mfma_f32_16x16x32_bf16 v[224:227], v[92:95], v[188:191], v[224:227]\n\t"
            "s_waitcnt vmcnt(6)\n\t"
            "v_cvt_pk_bf16_f32 v220, v220, v221\n\t"
            "v_mfma_f32_16x16x32_bf16 v[196:199], v[96:99], v[144:147], 0\n\t"
            "v_cvt_pk_bf16_f32 v201, v202, v203\n\t"
            "v_cvt_pk_bf16_f32 v221, v222, v223\n\t"
            "v_mfma_f32_16x16x32_bf16 v[216:219], v[96:99], v[168:171], 0\n\t"
            "v_mul_f32 v204, v236, v204\n\t"
            "v_mul_f32 v205, v236, v205\n\t"
            "v_mfma_f32_16x16x32_bf16 v[196:199], v[100:103], v[148:151], v[196:199]\n\t"
            "v_mul_f32 v224, v237, v224\n\t"
            "v_mul_f32 v225, v237, v225\n\t"
            "v_mfma_f32_16x16x32_bf16 v[216:219], v[100:103], v[172:175], v[216:219]\n\t"
            "v_mul_f32 v206, v236, v206\n\t"
            "v_mul_f32 v207, v236, v207\n\t"
            "v_mfma_f32_16x16x32_bf16 v[196:199], v[104:107], v[152:155], v[196:199]\n\t"
            "v_cvt_pk_bf16_f32 v204, v204, v205\n\t"
            "v_mul_f32 v226, v237, v226\n\t"
            "v_mfma_f32_16x16x32_bf16 v[216:219], v[104:107], v[176:179], v[216:219]\n\t"
            "v_mul_f32 v227, v237, v227\n\t"
            "v_cvt_pk_bf16_f32 v224, v224, v225\n\t"
            "v_mfma_f32_16x16x32_bf16 v[196:199], v[108:111], v[156:159], v[196:199]\n\t"
            "v_cvt_pk_bf16_f32 v205, v206, v207\n\t"
            "v_cvt_pk_bf16_f32 v225, v226, v227\n\t"
            "v_mfma_f32_16x16x32_bf16 v[216:219], v[108:111], v[180:183], v[216:219]\n\t"
            "ds_write_b128 v232, v[192:195]\n\t"
            "v_mfma_f32_16x16x32_bf16 v[196:199], v[112:115], v[160:163], v[196:199]\n\t"
            "ds_write_b128 v233, v[212:215]\n\t"
            "v_mfma_f32_16x16x32_bf16 v[216:219], v[112:115], v[184:187], v[216:219]\n\t"
            "v_mfma_f32_16x16x32_bf16 v[196:199], v[116:119], v[164:167], v[196:199]\n\t"
            "v_mfma_f32_16x16x32_bf16 v[216:219], v[116:119], v[188:191], v[216:219]\n\t"
            "s_waitcnt vmcnt(0)\n\t"
            "v_mfma_f32_16x16x32_bf16 v[208:211], v[120:123], v[144:147], 0\n\t"
            "v_mfma_f32_16x16x32_bf16 v[228:231], v[120:123], v[168:171], 0\n\t"
            "v_mfma_f32_16x16x32_bf16 v[208:211], v[124:127], v[148:151], v[208:211]\n\t"
            "v_mfma_f32_16x16x32_bf16 v[228:231], v[124:127], v[172:175], v[228:231]\n\t"
            "s_nop 1\n\t"
            "v_mul_f32 v196, v236, v196\n\t"
            "v_mfma_f32_16x16x32_bf16 v[208:211], v[128:131], v[152:155], v[208:211]\n\t"
            "v_mul_f32 v197, v236, v197\n\t"
            "v_mul_f32 v198, v236, v198\n\t"
            "v_mfma_f32_16x16x32_bf16 v[228:231], v[128:131], v[176:179], v[228:231]\n\t"
            "v_mul_f32 v199, v236, v199\n\t"
            "v_mul_f32 v216, v237, v216\n\t"
            "v_mfma_f32_16x16x32_bf16 v[208:211], v[132:135], v[156:159], v[208:211]\n\t"
            "v_mul_f32 v217, v237, v217\n\t"
            "v_mul_f32 v218, v237, v218\n\t"
            "v_mfma_f32_16x16x32_bf16 v[228:231], v[132:135], v[180:183], v[228:231]\n\t"
            "v_mul_f32 v219, v237, v219\n\t"
            "v_cvt_pk_bf16_f32 v206, v196, v197\n\t"
            "v_mfma_f32_16x16x32_bf16 v[208:211], v[136:139], v[160:163], v[208:211]\n\t"
            "v_cvt_pk_bf16_f32 v207, v198, v199\n\t"
            "v_cvt_pk_bf16_f32 v226, v216, v217\n\t"
            "v_mfma_f32_16x16x32_bf16 v[228:231], v[136:139], v[184:187], v[228:231]\n\t"
            "v_cvt_pk_bf16_f32 v227, v218, v219\n\t"
            "v_mfma_f32_16x16x32_bf16 v[208:211], v[140:143], v[164:167], v[208:211]\n\t"
            "ds_write_b128 v232, v[204:207] offset:64\n\t"
            "v_mfma_f32_16x16x32_bf16 v[228:231], v[140:143], v[188:191], v[228:231]\n\t"
            "ds_write_b128 v233, v[224:227] offset:64\n\t"
            "s_nop 4\n\t"
            "v_mul_f32 v208, v236, v208\n\t"
            "v_mul_f32 v209, v236, v209\n\t"
            "v_mul_f32 v210, v236, v210\n\t"
            "v_mul_f32 v211, v236, v211\n\t"
            "v_cvt_pk_bf16_f32 v202, v208, v209\n\t"
            "v_cvt_pk_bf16_f32 v203, v210, v211\n\t"
            "v_mul_f32 v228, v237, v228\n\t"
            "v_mul_f32 v229, v237, v229\n\t"
            "v_mfma_f32_16x16x16_bf16 v[196:199], v[200:201], v[202:203], 0\n\t"
            "v_mul_f32 v230, v237, v230\n\t"
            "v_mul_f32 v231, v237, v231\n\t"
            "v_cvt_pk_bf16_f32 v222, v228, v229\n\t"
            "v_cvt_pk_bf16_f32 v223, v230, v231\n\t"
            "s_nop 1\n\t"
            "v_mfma_f32_16x16x16_bf16 v[216:219], v[220:221], v[222:223], 0\n\t"
            "ds_write_b128 v232, v[200:203] offset:128\n\t"
            "v_mfma_f32_16x16x32_bf16 v[196:199], v[192:195], v[204:207], v[196:199]\n\t"
            "ds_write_b128 v233, v[220:223] offset:128\n\t"
            "s_nop 2\n\t"
            "v_mfma_f32_16x16x32_bf16 v[216:219], v[212:215], v[224:227], v[216:219]\n\t"
            "s_nop 2\n\t"
            "v_cndmask_b32_e64 v196, v196, v197, s[50:51]\n\t"
            "v_cndmask_b32_e64 v196, v196, v198, s[52:53]\n\t"
            "v_cndmask_b32_e64 v196, v196, v199, s[54:55]\n\t"
            "ds_write_b32 v234, v196\n\t"
            "s_nop 0\n\t"
            "v_cndmask_b32_e64 v216, v216, v217, s[50:51]\n\t"
            "v_cndmask_b32_e64 v216, v216, v218, s[52:53]\n\t"
            "v_cndmask_b32_e64 v216, v216, v219, s[54:55]\n\t"
            "ds_write_b32 v235, v216\n\t"
            "s_waitcnt lgkmcnt(0)\n\t"
            "s_nop 7\n\t"
            "s_cmp_lg_u32 s33, 0\n\t"
            "s_cbranch_scc1 .Lar%=\n\t"
            "s_mov_b64 exec, 1\n\t"
            "v_mov_b32 v216, 1\n\t"
            "s_mov_b32 s49, 295168\n\t"
            "buffer_atomic_add v216, off, s[28:31], s49\n\t"
            "s_mov_b64 exec, -1\n\t"
            ".Lar%=:\n\t"
            "v_mov_b32 v224, s46\n\t"
            ".Ls2%=:\n\t"
            "ds_read_b128 v[220:223], v224\n\t"
            "s_waitcnt lgkmcnt(0)\n\t"
            "v_and_b32 v220, v220, v221\n\t"
            "v_and_b32 v222, v222, v223\n\t"
            "v_and_b32 v220, v220, v222\n\t"
            "s_nop 0\n\t"
            "v_readfirstlane_b32 s49, v220\n\t"
            "s_nop 3\n\t"
            "s_bitcmp1_b32 s49, 31\n\t"
            "s_cbranch_scc0 .Ls2%=\n\t"
            "s_waitcnt lgkmcnt(0)\n\t"
            "s_nop 7\n\t"
            "s_setprio 0\n\t"
            "v_add_u32 v232, s48, v244\n\t"
            "v_add_u32 v208, s48, v248\n\t"
            "v_xor_b32 v233, 64, v232\n\t"
            "v_add_u32 v234, s72, v252\n\t"
            "v_cndmask_b32_e64 v235, v246, v234, s[50:51]\n\t"
            "v_cndmask_b32_e64 v234, v246, v234, s[26:27]\n\t"
            "v_mov_b32 v239, s76\n\t"
            "ds_read_b128 v[216:219], v232\n\t"
            "ds_read_b128 v[204:207], v208 offset:18816\n\t"
            "ds_read_b128 v[208:211], v208 offset:19840\n\t"
            "ds_read_b128 v[220:223], v233\n\t"
            "ds_read_b128 v[224:227], v232 offset:128\n\t"
            "ds_read_b128 v[188:191], v233 offset:6272\n\t"
            "ds_read_b128 v[192:195], v232 offset:6400\n\t"
            "ds_read_b128 v[200:203], v233 offset:6400\n\t"
            "ds_read_b32 v239, v239\n\t"
            "s_waitcnt lgkmcnt(8)\n\t"
            "v_mfma_f32_16x16x32_bf16 v[148:151], v[72:75], v[216:219], 0\n\t"
            "s_waitcnt lgkmcnt(7)\n\t"
            "v_add_f32 v204, v204, v206\n\t"
            "v_add_f32 v205, v205, v207\n\t"
            "s_waitcnt lgkmcnt(6)\n\t"
            "v_add_f32 v208, v208, v210\n\t"
            "v_add_f32 v209, v209, v211\n\t"
            "v_add_f32 v204, v204, v208\n\t"
            "v_add_f32 v205, v205, v209\n\t"
            "v_mfma_f32_16x16x32_bf16 v[208:211], v[96:99], v[216:219], 0\n\t"
            "v_mul_f32 v204, 0x3baaaaab, v204\n\t"
            "v_mul_f32_e64 v204, v204, -v204\n\t"
            "v_mfma_f32_16x16x32_bf16 v[212:215], v[120:123], v[216:219], 0\n\t"
            "v_fmac_f32 v204, 0x3baaaaab, v205\n\t"
            "v_max_f32 v204, 0, v204\n\t"
            "v_mfma_f32_16x16x32_bf16 v[144:147], v[0:3], v[216:219], 0\n\t"
            "v_add_f32 v204, 0x3727c5ac, v204\n\t"
            "v_rsq_f32 v204, v204\n\t"
            "s_nop 0\n\t"
            "ds_write_b32 v249, v204\n\t"
            "v_mfma_f32_16x16x32_bf16 v[204:207], v[24:27], v[216:219], 0\n\t"
            "ds_read_b32 v236, v251\n\t"
            "v_mfma_f32_16x16x32_bf16 v[152:155], v[48:51], v[216:219], 0\n\t"
            "ds_read_b128 v[216:219], v233 offset:128\n\t"
            "s_waitcnt lgkmcnt(8)\n\t"
            "v_mfma_f32_16x16x32_bf16 v[148:151], v[76:79], v[220:223], v[148:151]\n\t"
            "ds_read_b32 v237, v251 offset:64\n\t"
            "v_mfma_f32_16x16x32_bf16 v[208:211], v[100:103], v[220:223], v[208:211]\n\t"
            "ds_read_b32 v238, v251 offset:128\n\t"
            "v_mfma_f32_16x16x32_bf16 v[212:215], v[124:127], v[220:223], v[212:215]\n\t"
            "v_mfma_f32_16x16x32_bf16 v[144:147], v[4:7], v[220:223], v[144:147]\n\t"
            "v_mfma_f32_16x16x32_bf16 v[204:207], v[28:31], v[220:223], v[204:207]\n\t"
            "v_mfma_f32_16x16x32_bf16 v[152:155], v[52:55], v[220:223], v[152:155]\n\t"
            "ds_read_b128 v[220:223], v232 offset:256\n\t"
            "s_waitcnt lgkmcnt(10)\n\t"
            "v_mfma_f32_16x16x32_bf16 v[144:147], v[8:11], v[224:227], v[144:147]\n\t"
            "v_mfma_f32_16x16x32_bf16 v[204:207], v[32:35], v[224:227], v[204:207]\n\t"
            "v_mfma_f32_16x16x32_bf16 v[152:155], v[56:59], v[224:227], v[152:155]\n\t"
            "v_mfma_f32_16x16x32_bf16 v[148:151], v[80:83], v[224:227], v[148:151]\n\t"
            "v_mfma_f32_16x16x32_bf16 v[208:211], v[104:107], v[224:227], v[208:211]\n\t"
            "v_mfma_f32_16x16x32_bf16 v[212:215], v[128:131], v[224:227], v[212:215]\n\t"
            "ds_read_b128 v[224:227], v233 offset:256\n\t"
            "s_waitcnt lgkmcnt(4)\n\t"
            "v_mfma_f32_16x16x32_bf16 v[148:151], v[84:87], v[216:219], v[148:151]\n\t"
            "v_mfma_f32_16x16x32_bf16 v[208:211], v[108:111], v[216:219], v[208:211]\n\t"
            "v_mfma_f32_16x16x32_bf16 v[212:215], v[132:135], v[216:219], v[212:215]\n\t"
            "v_mfma_f32_16x16x32_bf16 v[144:147], v[12:15], v[216:219], v[144:147]\n\t"
            "v_mfma_f32_16x16x32_bf16 v[204:207], v[36:39], v[216:219], v[204:207]\n\t"
            "v_mfma_f32_16x16x32_bf16 v[152:155], v[60:63], v[216:219], v[152:155]\n\t"
            "ds_read_b128 v[216:219], v232 offset:6144\n\t"
            "s_waitcnt lgkmcnt(2)\n\t"
            "v_mfma_f32_16x16x32_bf16 v[148:151], v[88:91], v[220:223], v[148:151]\n\t"
            "v_mfma_f32_16x16x32_bf16 v[208:211], v[112:115], v[220:223], v[208:211]\n\t"
            "v_mfma_f32_16x16x32_bf16 v[212:215], v[136:139], v[220:223], v[212:215]\n\t"
            "v_mfma_f32_16x16x32_bf16 v[144:147], v[16:19], v[220:223], v[144:147]\n\t"
            "v_mfma_f32_16x16x32_bf16 v[204:207], v[40:43], v[220:223], v[204:207]\n\t"
            "v_mfma_f32_16x16x32_bf16 v[152:155], v[64:67], v[220:223], v[152:155]\n\t"
            "ds_read_b128 v[220:223], v233 offset:6144\n\t"
            "s_waitcnt lgkmcnt(2)\n\t"
            "v_mfma_f32_16x16x32_bf16 v[144:147], v[20:23], v[224:227], v[144:147]\n\t"
            "v_mfma_f32_16x16x32_bf16 v[204:207], v[44:47], v[224:227], v[204:207]\n\t"
            "v_mfma_f32_16x16x32_bf16 v[152:155], v[68:71], v[224:227], v[152:155]\n\t"
            "v_mfma_f32_16x16x32_bf16 v[148:151], v[92:95], v[224:227], v[148:151]\n\t"
            "v_mfma_f32_16x16x32_bf16 v[208:211], v[116:119], v[224:227], v[208:211]\n\t"
            "v_mfma_f32_16x16x32_bf16 v[212:215], v[140:143], v[224:227], v[212:215]\n\t"
            "ds_read_b128 v[224:227], v232 offset:6272\n\t"
            "s_waitcnt lgkmcnt(2)\n\t"
            "v_mfma_f32_16x16x32_bf16 v[160:163], v[72:75], v[216:219], 0\n\t"
            "v_mul_f32 v144, v236, v144\n\t"
            "v_mul_f32 v145, v236, v145\n\t"
            "v_mul_f32 v146, v236, v146\n\t"
            "s_waitcnt lgkmcnt(1)\n\t"
            "v_mfma_f32_16x16x32_bf16 v[160:163], v[76:79], v[220:223], v[160:163]\n\t"
            "v_mul_f32 v152, v236, v152\n\t"
            "v_mul_f32 v153, v236, v153\n\t"
            "v_mul_f32 v154, v236, v154\n\t"
            "v_mul_f32 v155, v236, v155\n\t"
            "v_cvt_pk_bf16_f32 v152, v152, v153\n\t"
            "v_cvt_pk_bf16_f32 v153, v154, v155\n\t"
            "v_mul_f32 v212, v236, v212\n\t"
            "v_mul_f32 v213, v236, v213\n\t"
            "v_mul_f32 v214, v236, v214\n\t"
            "v_mul_f32 v215, v236, v215\n\t"
            "v_cvt_pk_bf16_f32 v154, v212, v213\n\t"
            "v_cvt_pk_bf16_f32 v155, v214, v215\n\t"
            "v_mul_f32 v147, v236, v147\n\t"
            "v_cvt_pk_bf16_f32 v144, v144, v145\n\t"
            "v_mul_f32 v148, v236, v148\n\t"
            "v_mul_f32 v149, v236, v149\n\t"
            "v_cvt_pk_bf16_f32 v145, v146, v147\n\t"
            "v_mul_f32 v204, v236, v204\n\t"
            "v_mul_f32 v205, v236, v205\n\t"
            "v_mul_f32 v206, v236, v206\n\t"
            "v_mul_f32 v207, v236, v207\n\t"
            "v_mul_f32 v150, v236, v150\n\t"
            "s_waitcnt lgkmcnt(0)\n\t"
            "v_mfma_f32_16x16x32_bf16 v[160:163], v[80:83], v[224:227], v[160:163]\n\t"
            "ds_read_b128 v[212:215], v232 offset:12416\n\t"
            "v_mul_f32 v151, v236, v151\n\t"
            "v_mfma_f32_16x16x32_bf16 v[160:163], v[84:87], v[188:191], v[160:163]\n\t"
            "v_cvt_pk_bf16_f32 v148, v148, v149\n\t"
            "v_cvt_pk_bf16_f32 v146, v204, v205\n\t"
            "v_mfma_f32_16x16x32_bf16 v[160:163], v[88:91], v[192:195], v[160:163]\n\t"
            "v_cvt_pk_bf16_f32 v147, v206, v207\n\t"
            "v_cvt_pk_bf16_f32 v149, v150, v151\n\t"
            "v_mfma_f32_16x16x32_bf16 v[160:163], v[92:95], v[200:203], v[160:163]\n\t"
            "ds_read_b128 v[204:207], v232 offset:12288\n\t"
            "v_mul_f32 v208, v236, v208\n\t"
            "v_mfma_f32_16x16x16_bf16 v[180:183], v[152:153], v[154:155], 0\n\t"
            "v_mul_f32 v209, v236, v209\n\t"
            "v_mul_f32 v210, v236, v210\n\t"
            "v_mul_f32 v211, v236, v211\n\t"
            "v_mfma_f32_16x16x32_bf16 v[196:199], v[24:27], v[216:219], 0\n\t"
            "v_cvt_pk_bf16_f32 v150, v208, v209\n\t"
            "v_cvt_pk_bf16_f32 v151, v210, v211\n\t"
            "v_mfma_f32_16x16x32_bf16 v[196:199], v[28:31], v[220:223], v[196:199]\n\t"
            "ds_read_b128 v[208:211], v233 offset:12288\n\t"
            "v_mul_f32 v160, v237, v160\n\t"
            "v_mfma_f32_16x16x32_bf16 v[196:199], v[32:35], v[224:227], v[196:199]\n\t"
            "v_mul_f32 v161, v237, v161\n\t"
            "v_mul_f32 v162, v237, v162\n\t"
            "v_mfma_f32_16x16x32_bf16 v[196:199], v[36:39], v[188:191], v[196:199]\n\t"
            "v_mul_f32 v163, v237, v163\n\t"
            "v_cvt_pk_bf16_f32 v160, v160, v161\n\t"
            "v_mfma_f32_16x16x32_bf16 v[196:199], v[40:43], v[192:195], v[196:199]\n\t"
            "v_cvt_pk_bf16_f32 v161, v162, v163\n\t"
            "v_mfma_f32_16x16x32_bf16 v[196:199], v[44:47], v[200:203], v[196:199]\n\t"
            "v_mfma_f32_16x16x32_bf16 v[156:159], v[0:3], v[216:219], 0\n\t"
            "v_mfma_f32_16x16x32_bf16 v[156:159], v[4:7], v[220:223], v[156:159]\n\t"
            "v_mfma_f32_16x16x32_bf16 v[156:159], v[8:11], v[224:227], v[156:159]\n\t"
            "v_mfma_f32_16x16x32_bf16 v[156:159], v[12:15], v[188:191], v[156:159]\n\t"
            "v_mfma_f32_16x16x32_bf16 v[156:159], v[16:19], v[192:195], v[156:159]\n\t"
            "v_mfma_f32_16x16x32_bf16 v[156:159], v[20:23], v[200:203], v[156:159]\n\t"
            "s_nop 1\n\t"
            "v_mul_f32 v196, v237, v196\n\t"
            "v_mfma_f32_16x16x32_bf16 v[164:167], v[48:51], v[216:219], 0\n\t"
            "v_mul_f32 v197, v237, v197\n\t"
            "v_mul_f32 v198, v237, v198\n\t"
            "v_mfma_f32_16x16x32_bf16 v[164:167], v[52:55], v[220:223], v[164:167]\n\t"
            "v_mul_f32 v199, v237, v199\n\t"
            "v_mfma_f32_16x16x32_bf16 v[164:167], v[56:59], v[224:227], v[164:167]\n\t"
            "v_mul_f32 v156, v237, v156\n\t"
            "v_mul_f32 v157, v237, v157\n\t"
            "v_mfma_f32_16x16x32_bf16 v[164:167], v[60:63], v[188:191], v[164:167]\n\t"
            "v_mul_f32 v158, v237, v158\n\t"
            "v_mul_f32 v159, v237, v159\n\t"
            "v_mfma_f32_16x16x32_bf16 v[164:167], v[64:67], v[192:195], v[164:167]\n\t"
            "v_cvt_pk_bf16_f32 v156, v156, v157\n\t"
            "v_cvt_pk_bf16_f32 v157, v158, v159\n\t"
            "v_mfma_f32_16x16x32_bf16 v[164:167], v[68:71], v[200:203], v[164:167]\n\t"
            "v_cvt_pk_bf16_f32 v158, v196, v197\n\t"
            "v_cvt_pk_bf16_f32 v159, v198, v199\n\t"
            "v_mfma_f32_16x16x32_bf16 v[228:231], v[96:99], v[216:219], 0\n\t"
            "v_mfma_f32_16x16x32_bf16 v[228:231], v[100:103], v[220:223], v[228:231]\n\t"
            "v_mfma_f32_16x16x32_bf16 v[228:231], v[104:107], v[224:227], v[228:231]\n\t"
            "v_mfma_f32_16x16x32_bf16 v[228:231], v[108:111], v[188:191], v[228:231]\n\t"
            "s_nop 1\n\t"
            "v_mul_f32 v164, v237, v164\n\t"
            "v_mfma_f32_16x16x32_bf16 v[228:231], v[112:115], v[192:195], v[228:231]\n\t"
            "v_mul_f32 v165, v237, v165\n\t"
            "v_mul_f32 v166, v237, v166\n\t"
            "v_mfma_f32_16x16x32_bf16 v[228:231], v[116:119], v[200:203], v[228:231]\n\t"
            "v_mul_f32 v167, v237, v167\n\t"
            "v_cvt_pk_bf16_f32 v164, v164, v165\n\t"
            "v_mfma_f32_16x16x32_bf16 v[180:183], v[144:147], v[148:151], v[180:183]\n\t"
            "v_cvt_pk_bf16_f32 v165, v166, v167\n\t"
            "v_mfma_f32_16x16x32_bf16 v[196:199], v[120:123], v[216:219], 0\n\t"
            "ds_read_b128 v[216:219], v233 offset:12416\n\t"
            "v_mfma_f32_16x16x32_bf16 v[196:199], v[124:127], v[220:223], v[196:199]\n\t"
            "ds_read_b128 v[220:223], v232 offset:12544\n\t"
            "v_mul_f32 v228, v237, v228\n\t"
            "v_mfma_f32_16x16x32_bf16 v[196:199], v[128:131], v[224:227], v[196:199]\n\t"
            "ds_read_b128 v[224:227], v233 offset:12544\n\t"
            "v_mul_f32 v229, v237, v229\n\t"
            "v_mfma_f32_16x16x32_bf16 v[196:199], v[132:135], v[188:191], v[196:199]\n\t"
            "v_mul_f32 v230, v237, v230\n\t"
            "v_mul_f32 v231, v237, v231\n\t"
            "v_mfma_f32_16x16x32_bf16 v[196:199], v[136:139], v[192:195], v[196:199]\n\t"
            "v_cvt_pk_bf16_f32 v162, v228, v229\n\t"
            "v_cvt_pk_bf16_f32 v163, v230, v231\n\t"
            "v_mfma_f32_16x16x32_bf16 v[196:199], v[140:143], v[200:203], v[196:199]\n\t"
            "v_exp_f32 v180, v180\n\t"
            "s_waitcnt lgkmcnt(4)\n\t"
            "v_mfma_f32_16x16x32_bf16 v[176:179], v[48:51], v[204:207], 0\n\t"
            "v_exp_f32 v181, v181\n\t"
            "s_waitcnt lgkmcnt(3)\n\t"
            "v_mfma_f32_16x16x32_bf16 v[176:179], v[52:55], v[208:211], v[176:179]\n\t"
            "v_exp_f32 v182, v182\n\t"
            "v_mfma_f32_16x16x32_bf16 v[176:179], v[56:59], v[212:215], v[176:179]\n\t"
            "v_exp_f32 v183, v183\n\t"
            "s_waitcnt lgkmcnt(2)\n\t"
            "v_mfma_f32_16x16x32_bf16 v[176:179], v[60:63], v[216:219], v[176:179]\n\t"
            "v_mul_f32 v196, v237, v196\n\t"
            "v_mul_f32 v197, v237, v197\n\t"
            "s_waitcnt lgkmcnt(1)\n\t"
            "v_mfma_f32_16x16x32_bf16 v[176:179], v[64:67], v[220:223], v[176:179]\n\t"
            "v_mul_f32 v198, v237, v198\n\t"
            "v_mul_f32 v199, v237, v199\n\t"
            "s_waitcnt lgkmcnt(0)\n\t"
            "v_mfma_f32_16x16x32_bf16 v[176:179], v[68:71], v[224:227], v[176:179]\n\t"
            "v_cvt_pk_bf16_f32 v166, v196, v197\n\t"
            "v_cvt_pk_bf16_f32 v167, v198, v199\n\t"
            "v_cvt_pk_bf16_f32 v180, v180, v181\n\t"
            "v_cvt_pk_bf16_f32 v181, v182, v183\n\t"
            "v_mfma_f32_16x16x16_bf16 v[188:191], v[152:153], v[166:167], 0\n\t"
            "v_mfma_f32_16x16x32_bf16 v[168:171], v[0:3], v[204:207], 0\n\t"
            "s_nop 1\n\t"
            "v_mul_f32 v176, v238, v176\n\t"
            "v_mfma_f32_16x16x32_bf16 v[168:171], v[4:7], v[208:211], v[168:171]\n\t"
            "v_mul_f32 v177, v238, v177\n\t"
            "v_mul_f32 v178, v238, v178\n\t"
            "v_mfma_f32_16x16x32_bf16 v[168:171], v[8:11], v[212:215], v[168:171]\n\t"
            "v_mul_f32 v179, v238, v179\n\t"
            "v_cvt_pk_bf16_f32 v176, v176, v177\n\t"
            "v_mfma_f32_16x16x32_bf16 v[168:171], v[12:15], v[216:219], v[168:171]\n\t"
            "v_cvt_pk_bf16_f32 v177, v178, v179\n\t"
            "v_mfma_f32_16x16x32_bf16 v[168:171], v[16:19], v[220:223], v[168:171]\n\t"
            "v_mfma_f32_16x16x32_bf16 v[168:171], v[20:23], v[224:227], v[168:171]\n\t"
            "v_mfma_f32_16x16x16_bf16 v[192:195], v[164:165], v[166:167], 0\n\t"
            "v_mfma_f32_16x16x32_bf16 v[188:191], v[144:147], v[160:163], v[188:191]\n\t"
            "v_mfma_f32_16x16x16_bf16 v[184:187], v[164:165], v[154:155], 0\n\t"
            "s_nop 3\n\t"
            "v_mfma_f32_16x16x32_bf16 v[192:195], v[156:159], v[160:163], v[192:195]\n\t"
            "v_mfma_f32_16x16x32_bf16 v[172:175], v[72:75], v[204:207], 0\n\t"
            "v_mfma_f32_16x16x32_bf16 v[172:175], v[76:79], v[208:211], v[172:175]\n\t"
            "v_mul_f32 v168, v238, v168\n\t"
            "v_mfma_f32_16x16x32_bf16 v[172:175], v[80:83], v[212:215], v[172:175]\n\t"
            "v_mul_f32 v169, v238, v169\n\t"
            "v_mul_f32 v170, v238, v170\n\t"
            "v_mfma_f32_16x16x32_bf16 v[172:175], v[84:87], v[216:219], v[172:175]\n\t"
            "v_exp_f32 v188, v188\n\t"
            "v_mfma_f32_16x16x32_bf16 v[172:175], v[88:91], v[220:223], v[172:175]\n\t"
            "v_exp_f32 v189, v189\n\t"
            "v_mfma_f32_16x16x32_bf16 v[172:175], v[92:95], v[224:227], v[172:175]\n\t"
            "v_exp_f32 v190, v190\n\t"
            "v_mfma_f32_16x16x32_bf16 v[184:187], v[156:159], v[148:151], v[184:187]\n\t"
            "v_exp_f32 v191, v191\n\t"
            "v_mfma_f32_16x16x32_bf16 v[200:203], v[120:123], v[204:207], 0\n\t"
            "v_exp_f32 v192, v192\n\t"
            "v_mfma_f32_16x16x32_bf16 v[200:203], v[124:127], v[208:211], v[200:203]\n\t"
            "v_exp_f32 v193, v193\n\t"
            "v_mfma_f32_16x16x32_bf16 v[200:203], v[128:131], v[212:215], v[200:203]\n\t"
            "v_exp_f32 v194, v194\n\t"
            "v_mfma_f32_16x16x32_bf16 v[200:203], v[132:135], v[216:219], v[200:203]\n\t"
            "v_exp_f32 v184, v184\n\t"
            "v_mfma_f32_16x16x32_bf16 v[200:203], v[136:139], v[220:223], v[200:203]\n\t"
            "v_exp_f32 v185, v185\n\t"
            "v_mfma_f32_16x16x32_bf16 v[200:203], v[140:143], v[224:227], v[200:203]\n\t"
            "v_exp_f32 v186, v186\n\t"
            "v_exp_f32 v187, v187\n\t"
            "v_cvt_pk_bf16_f32 v182, v184, v185\n\t"
            "v_cvt_pk_bf16_f32 v183, v186, v187\n\t"
            "v_mfma_f32_16x16x16_bf16 v[184:187], v[176:177], v[154:155], 0\n\t"
            "v_exp_f32 v195, v195\n\t"
            "v_cvt_pk_bf16_f32 v188, v188, v189\n\t"
            "v_mfma_f32_16x16x32_bf16 v[196:199], v[24:27], v[204:207], 0\n\t"
            "v_cvt_pk_bf16_f32 v189, v190, v191\n\t"
            "v_cvt_pk_bf16_f32 v190, v192, v193\n\t"
            "v_mfma_f32_16x16x32_bf16 v[196:199], v[28:31], v[208:211], v[196:199]\n\t"
            "v_cvt_pk_bf16_f32 v191, v194, v195\n\t"
            "v_mul_f32 v171, v238, v171\n\t"
            "v_mfma_f32_16x16x32_bf16 v[196:199], v[32:35], v[212:215], v[196:199]\n\t"
            "v_cvt_pk_bf16_f32 v168, v168, v169\n\t"
            "v_cvt_pk_bf16_f32 v169, v170, v171\n\t"
            "v_mfma_f32_16x16x32_bf16 v[196:199], v[36:39], v[216:219], v[196:199]\n\t"
            "v_mul_f32 v172, v238, v172\n\t"
            "v_mul_f32 v173, v238, v173\n\t"
            "v_mfma_f32_16x16x32_bf16 v[196:199], v[40:43], v[220:223], v[196:199]\n\t"
            "v_mul_f32 v200, v238, v200\n\t"
            "v_mul_f32 v201, v238, v201\n\t"
            "v_mfma_f32_16x16x32_bf16 v[196:199], v[44:47], v[224:227], v[196:199]\n\t"
            "v_mul_f32 v202, v238, v202\n\t"
            "v_mul_f32 v203, v238, v203\n\t"
            "v_mfma_f32_16x16x32_bf16 v[228:231], v[96:99], v[204:207], 0\n\t"
            "ds_read_b64 v[204:205], v235 offset:136\n\t"
            "v_mul_f32 v174, v238, v174\n\t"
            "v_mfma_f32_16x16x32_bf16 v[228:231], v[100:103], v[208:211], v[228:231]\n\t"
            "ds_read_b64 v[206:207], v234 offset:128\n\t"
            "v_mul_f32 v175, v238, v175\n\t"
            "v_mfma_f32_16x16x32_bf16 v[228:231], v[104:107], v[212:215], v[228:231]\n\t"
            "ds_read_b128 v[208:211], v234\n\t"
            "v_mul_f32 v196, v238, v196\n\t"
            "v_mfma_f32_16x16x32_bf16 v[228:231], v[108:111], v[216:219], v[228:231]\n\t"
            "ds_read_b128 v[212:215], v235 offset:64\n\t"
            "v_mul_f32 v197, v238, v197\n\t"
            "v_mfma_f32_16x16x32_bf16 v[228:231], v[112:115], v[220:223], v[228:231]\n\t"
            "v_mul_f32 v198, v238, v198\n\t"
            "v_mul_f32 v199, v238, v199\n\t"
            "v_mfma_f32_16x16x32_bf16 v[228:231], v[116:119], v[224:227], v[228:231]\n\t"
            "v_cvt_pk_bf16_f32 v170, v196, v197\n\t"
            "v_cvt_pk_bf16_f32 v171, v198, v199\n\t"
            "v_mfma_f32_16x16x16_bf16 v[192:195], v[176:177], v[166:167], 0\n\t"
            "v_cvt_pk_bf16_f32 v172, v172, v173\n\t"
            "v_cvt_pk_bf16_f32 v178, v200, v201\n\t"
            "v_cvt_pk_bf16_f32 v179, v202, v203\n\t"
            "v_mfma_f32_16x16x32_bf16 v[184:187], v[168:171], v[148:151], v[184:187]\n\t"
            "v_cvt_pk_bf16_f32 v173, v174, v175\n\t"
            "v_mul_f32 v228, v238, v228\n\t"
            "v_mfma_f32_16x16x32_bf16 v[192:195], v[168:171], v[160:163], v[192:195]\n\t"
            "v_mul_f32 v229, v238, v229\n\t"
            "v_mul_f32 v230, v238, v230\n\t"
            "v_mul_f32 v231, v238, v231\n\t"
            "s_waitcnt lgkmcnt(2)\n\t"
            "v_mfma_f32_16x16x32_bf16 v[216:219], v[204:207], v[152:155], 0\n\t"
            "v_cvt_pk_bf16_f32 v174, v228, v229\n\t"
            "v_cvt_pk_bf16_f32 v175, v230, v231\n\t"
            "s_waitcnt lgkmcnt(1)\n\t"
            "v_mfma_f32_16x16x32_bf16 v[216:219], v[208:211], v[148:151], v[216:219]\n\t"
            "v_exp_f32 v184, v184\n\t"
            "s_waitcnt lgkmcnt(0)\n\t"
            "v_mfma_f32_16x16x32_bf16 v[216:219], v[212:215], v[144:147], v[216:219]\n\t"
            "v_exp_f32 v185, v185\n\t"
            "v_mfma_f32_16x16x32_bf16 v[220:223], v[204:207], v[164:167], 0\n\t"
            "v_exp_f32 v186, v186\n\t"
            "v_mfma_f32_16x16x32_bf16 v[220:223], v[208:211], v[160:163], v[220:223]\n\t"
            "v_exp_f32 v187, v187\n\t"
            "v_mfma_f32_16x16x32_bf16 v[220:223], v[212:215], v[156:159], v[220:223]\n\t"
            "v_cvt_pk_bf16_f32 v184, v184, v185\n\t"
            "v_cvt_pk_bf16_f32 v185, v186, v187\n\t"
            "v_mfma_f32_16x16x16_bf16 v[228:231], v[164:165], v[178:179], 0\n\t"
            "v_exp_f32 v216, v216\n\t"
            "v_mov_b32 v187, 0\n\t"
            "v_mfma_f32_16x16x16_bf16 v[200:203], v[176:177], v[178:179], 0\n\t"
            "v_cvt_pk_bf16_f32 v216, v216, 0\n\t"
            "v_cndmask_b32_e64 v186, 0, v216, s[58:59]\n\t"
            "v_exp_f32 v192, v192\n\t"
            "v_mfma_f32_16x16x32_bf16 v[224:227], v[204:207], v[176:179], 0\n\t"
            "v_exp_f32 v193, v193\n\t"
            "v_mfma_f32_16x16x32_bf16 v[224:227], v[208:211], v[172:175], v[224:227]\n\t"
            "v_exp_f32 v194, v194\n\t"
            "v_mfma_f32_16x16x32_bf16 v[224:227], v[212:215], v[168:171], v[224:227]\n\t"
            "v_exp_f32 v195, v195\n\t"
            "v_mfma_f32_16x16x16_bf16 v[196:199], v[152:153], v[178:179], 0\n\t"
            "v_exp_f32 v220, v220\n\t"
            "v_cvt_pk_bf16_f32 v192, v192, v193\n\t"
            "v_mfma_f32_16x16x32_bf16 v[204:207], v[240:243], v[180:183], 0\n\t"
            "v_cvt_pk_bf16_f32 v193, v194, v195\n\t"
            "v_cvt_pk_bf16_f32 v220, v220, 0\n\t"
            "v_mfma_f32_16x16x32_bf16 v[204:207], v[240:243], v[184:187], v[204:207]\n\t"
            "v_cndmask_b32_e64 v194, 0, v220, s[58:59]\n\t"
            "v_mov_b32 v195, 0\n\t"
            "v_mfma_f32_16x16x32_bf16 v[200:203], v[168:171], v[172:175], v[200:203]\n\t"
            "v_cndmask_b32_e64 v235, v217, v221, s[60:61]\n\t"
            "v_cndmask_b32_e64 v235, v235, v225, s[62:63]\n\t"
            "v_mfma_f32_16x16x32_bf16 v[208:211], v[240:243], v[188:191], 0\n\t"
            "v_cndmask_b32_e64 v235, v235, v239, s[64:65]\n\t"
            "v_cmp_nlt_f32_e64 s[24:25], |v235|, s23\n\t"
            "v_mfma_f32_16x16x32_bf16 v[208:211], v[240:243], v[192:195], v[208:211]\n\t"
            "v_exp_f32 v235, v235\n\t"
            "v_mfma_f32_16x16x32_bf16 v[196:199], v[144:147], v[172:175], v[196:199]\n\t"
            "v_exp_f32 v200, v200\n\t"
            "v_mfma_f32_16x16x32_bf16 v[228:231], v[156:159], v[172:175], v[228:231]\n\t"
            "v_exp_f32 v201, v201\n\t"
            "v_exp_f32 v202, v202\n\t"
            "v_exp_f32 v203, v203\n\t"
            "v_exp_f32 v224, v224\n\t"
            "v_cvt_pk_bf16_f32 v235, v235, 0\n\t"
            "v_cvt_pk_bf16_f32 v200, v200, v201\n\t"
            "v_exp_f32 v196, v196\n\t"
            "v_exp_f32 v197, v197\n\t"
            "v_exp_f32 v198, v198\n\t"
            "v_exp_f32 v199, v199\n\t"
            "v_exp_f32 v228, v228\n\t"
            "v_exp_f32 v229, v229\n\t"
            "v_exp_f32 v230, v230\n\t"
            "v_exp_f32 v231, v231\n\t"
            "v_cvt_pk_bf16_f32 v196, v196, v197\n\t"
            "v_cvt_pk_bf16_f32 v197, v198, v199\n\t"
            "v_cvt_pk_bf16_f32 v198, v228, v229\n\t"
            "v_cvt_pk_bf16_f32 v199, v230, v231\n\t"
            "v_cndmask_b32_e64 v235, 0, v235, s[66:67]\n\t"
            "v_cvt_pk_bf16_f32 v201, v202, v203\n\t"
            "v_mfma_f32_16x16x32_bf16 v[212:215], v[240:243], v[196:199], 0\n\t"
            "v_cvt_pk_bf16_f32 v224, v224, 0\n\t"
            "v_cndmask_b32_e64 v202, 0, v224, s[58:59]\n\t"
            "v_mov_b32 v203, 0\n\t"
            "v_add_u32 v205, 0xe0800000, v204\n\t"
            "v_add_u32 v209, 0xe0800000, v208\n\t"
            "v_mfma_f32_16x16x32_bf16 v[212:215], v[240:243], v[200:203], v[212:215]\n\t"
            "ds_write_b16 v250, v235 offset:256\n\t"
            "v_rcp_f32 v236, v204\n\t"
            "v_rcp_f32 v237, v208\n\t"
            "s_nop 4\n\t"
            "v_add_u32 v213, 0xe0800000, v212\n\t"
            "v_rcp_f32 v238, v212\n\t"
            "v_or3_b32 v205, v205, v209, v213\n\t"
            "v_cmp_le_u32 vcc, 2.0, v205\n\t"
            "s_waitcnt lgkmcnt(0)\n\t"
            "s_nop 7\n\t"
            "s_branch .Lwa_in%=\n\t"
            ".p2align 6\n\t"
            ".Lwa_loop%=:\n\t"
            "ds_read_b64 v[156:157], v247\n\t"
            "v_add_u32 v232, s48, v244\n\t"
            "v_add_u32 v208, s48, v248\n\t"
            "v_add_u32 v235, s45, v248\n\t"
            "v_xor_b32 v234, s73, v254\n\t"
            "v_xor_b32 v233, 64, v232\n\t"
            "v_mov_b32 v159, 0\n\t"
            "v_mov_b32 v167, 0\n\t"
            "v_mov_b32 v163, 0\n\t"
            "v_mov_b32 v239, s76\n\t"
            "ds_read_b128 v[216:219], v232\n\t"
            "ds_read_b128 v[204:207], v208 offset:18816\n\t"
            "ds_read_b128 v[208:211], v208 offset:19840\n\t"
            "ds_read_b128 v[168:171], v235\n\t"
            "ds_read_u16 v158, v234\n\t"
            "ds_read_b128 v[220:223], v233\n\t"
            "ds_read_b128 v[172:175], v235 offset:1536\n\t"
            "ds_read_b64 v[164:165], v247 offset:1536\n\t"
            "ds_read_u16 v166, v234 offset:128\n\t"
            "ds_read_b128 v[176:179], v235 offset:3072\n\t"
            "ds_read_b64 v[160:161], v247 offset:3072\n\t"
            "ds_read_u16 v162, v234 offset:256\n\t"
            "v_add_u32 v234, s72, v252\n\t"
            "v_cndmask_b32_e64 v235, v246, v234, s[50:51]\n\t"
            "v_cndmask_b32_e64 v234, v246, v234, s[26:27]\n\t"
            "ds_read_b128 v[224:227], v232 offset:128\n\t"
            "s_waitcnt lgkmcnt(14)\n\t"
            "ds_read_b32 v239, v239\n\t"
            "s_waitcnt lgkmcnt(13)\n\t"
            "v_mfma_f32_16x16x32_bf16 v[148:151], v[72:75], v[216:219], 0\n\t"
            "s_waitcnt lgkmcnt(12)\n\t"
            "v_add_f32 v204, v204, v206\n\t"
            "v_add_f32 v205, v205, v207\n\t"
            "s_waitcnt lgkmcnt(11)\n\t"
            "v_add_f32 v208, v208, v210\n\t"
            "v_add_f32 v209, v209, v211\n\t"
            "v_add_f32 v204, v204, v208\n\t"
            "v_add_f32 v205, v205, v209\n\t"
            "v_mfma_f32_16x16x32_bf16 v[208:211], v[96:99], v[216:219], 0\n\t"
            "v_mul_f32 v204, 0x3baaaaab, v204\n\t"
            "v_mul_f32_e64 v204, v204, -v204\n\t"
            "v_mfma_f32_16x16x32_bf16 v[212:215], v[120:123], v[216:219], 0\n\t"
            "v_fmac_f32 v204, 0x3baaaaab, v205\n\t"
            "v_max_f32 v204, 0, v204\n\t"
            "s_waitcnt lgkmcnt(10)\n\t"
            "v_mfma_f32_16x16x32_bf16 v[228:231], v[168:171], v[180:183], 0\n\t"
            "v_add_f32 v204, 0x3727c5ac, v204\n\t"
            "v_rsq_f32 v204, v204\n\t"
            "s_waitcnt lgkmcnt(9)\n\t"
            "v_mfma_f32_16x16x32_bf16 v[228:231], v[156:159], v[184:187], v[228:231]\n\t"
            "ds_write_b32 v249, v204\n\t"
            "v_mfma_f32_16x16x32_bf16 v[144:147], v[0:3], v[216:219], 0\n\t"
            "v_mfma_f32_16x16x32_bf16 v[204:207], v[24:27], v[216:219], 0\n\t"
            "v_mfma_f32_16x16x32_bf16 v[152:155], v[48:51], v[216:219], 0\n\t"
            "ds_read_b128 v[216:219], v233 offset:128\n\t"
            "s_waitcnt lgkmcnt(10)\n\t"
            "v_mfma_f32_16x16x32_bf16 v[148:151], v[76:79], v[220:223], v[148:151]\n\t"
            "s_nop 0\n\t"
            "v_mul_f32 v228, v236, v228\n\t"
            "v_mfma_f32_16x16x32_bf16 v[208:211], v[100:103], v[220:223], v[208:211]\n\t"
            "v_mul_f32 v229, v236, v229\n\t"
            "v_mul_f32 v230, v236, v230\n\t"
            "v_mfma_f32_16x16x32_bf16 v[212:215], v[124:127], v[220:223], v[212:215]\n\t"
            "v_mul_f32 v231, v236, v231\n\t"
            "v_cvt_pk_bf16_f32 v228, v228, v229\n\t"
            "v_mfma_f32_16x16x32_bf16 v[144:147], v[4:7], v[220:223], v[144:147]\n\t"
            "v_cvt_pk_bf16_f32 v229, v230, v231\n\t"
            "v_mfma_f32_16x16x32_bf16 v[204:207], v[28:31], v[220:223], v[204:207]\n\t"
            "ds_write_b64 v245, v[228:229]\n\t"
            "v_mfma_f32_16x16x32_bf16 v[152:155], v[52:55], v[220:223], v[152:155]\n\t"
            "ds_read_b128 v[220:223], v232 offset:256\n\t"
            "s_waitcnt lgkmcnt(11)\n\t"
            "v_mfma_f32_16x16x32_bf16 v[228:231], v[172:175], v[180:183], 0\n\t"
            "s_waitcnt lgkmcnt(9)\n\t"
            "v_mfma_f32_16x16x32_bf16 v[228:231], v[164:167], v[184:187], v[228:231]\n\t"
            "s_waitcnt lgkmcnt(8)\n\t"
            "v_mfma_f32_16x16x32_bf16 v[180:183], v[176:179], v[180:183], 0\n\t"
            "s_waitcnt lgkmcnt(6)\n\t"
            "v_mfma_f32_16x16x32_bf16 v[180:183], v[160:163], v[184:187], v[180:183]\n\t"
            "s_waitcnt lgkmcnt(5)\n\t"
            "v_mfma_f32_16x16x32_bf16 v[144:147], v[8:11], v[224:227], v[144:147]\n\t"
            "v_mfma_f32_16x16x32_bf16 v[204:207], v[32:35], v[224:227], v[204:207]\n\t"
            "v_mfma_f32_16x16x32_bf16 v[152:155], v[56:59], v[224:227], v[152:155]\n\t"
            "v_mfma_f32_16x16x32_bf16 v[148:151], v[80:83], v[224:227], v[148:151]\n\t"
            "v_mul_f32 v228, v236, v228\n\t"
            "v_mfma_f32_16x16x32_bf16 v[208:211], v[104:107], v[224:227], v[208:211]\n\t"
            "v_mul_f32 v229, v236, v229\n\t"
            "v_mul_f32 v230, v236, v230\n\t"
            "v_mfma_f32_16x16x32_bf16 v[212:215], v[128:131], v[224:227], v[212:215]\n\t"
            "ds_read_b128 v[224:227], v233 offset:256\n\t"
            "v_mul_f32 v231, v236, v231\n\t"
            "v_mfma_f32_16x16x32_bf16 v[184:187], v[168:171], v[188:191], 0\n\t"
            "v_cvt_pk_bf16_f32 v228, v228, v229\n\t"
            "v_cvt_pk_bf16_f32 v229, v230, v231\n\t"
            "v_mfma_f32_16x16x32_bf16 v[184:187], v[156:159], v[192:195], v[184:187]\n\t"
            "ds_write_b64 v245, v[228:229] offset:128\n\t"
            "v_mfma_f32_16x16x32_bf16 v[228:231], v[172:175], v[188:191], 0\n\t"
            "v_mul_f32 v180, v236, v180\n\t"
            "v_mul_f32 v181, v236, v181\n\t"
            "v_mfma_f32_16x16x32_bf16 v[228:231], v[164:167], v[192:195], v[228:231]\n\t"
            "v_mul_f32 v182, v236, v182\n\t"
            "v_mul_f32 v183, v236, v183\n\t"
            "s_waitcnt lgkmcnt(4)\n\t"
            "v_mfma_f32_16x16x32_bf16 v[148:151], v[84:87], v[216:219], v[148:151]\n\t"
            "ds_read_b32 v236, v251\n\t"
            "v_cvt_pk_bf16_f32 v180, v180, v181\n\t"
            "v_mfma_f32_16x16x32_bf16 v[208:211], v[108:111], v[216:219], v[208:211]\n\t"
            "v_cvt_pk_bf16_f32 v181, v182, v183\n\t"
            "v_mul_f32 v184, v237, v184\n\t"
            "v_mfma_f32_16x16x32_bf16 v[212:215], v[132:135], v[216:219], v[212:215]\n\t"
            "ds_write_b64 v245, v[180:181] offset:256\n\t"
            "v_mfma_f32_16x16x32_bf16 v[144:147], v[12:15], v[216:219], v[144:147]\n\t"
            "v_mul_f32 v185, v237, v185\n\t"
            "v_mul_f32 v186, v237, v186\n\t"
            "v_mfma_f32_16x16x32_bf16 v[204:207], v[36:39], v[216:219], v[204:207]\n\t"
            "v_mul_f32 v187, v237, v187\n\t"
            "v_cvt_pk_bf16_f32 v184, v184, v185\n\t"
            "v_mfma_f32_16x16x32_bf16 v[152:155], v[60:63], v[216:219], v[152:155]\n\t"
            "ds_read_b128 v[216:219], v232 offset:6144\n\t"
            "v_cvt_pk_bf16_f32 v185, v186, v187\n\t"
            "v_mfma_f32_16x16x32_bf16 v[180:183], v[168:171], v[196:199], 0\n\t"
            "ds_read_b64 v[186:187], v253 offset:352\n\t"
            "v_mul_f32 v228, v237, v228\n\t"
            "v_mfma_f32_16x16x32_bf16 v[180:183], v[156:159], v[200:203], v[180:183]\n\t"
            "ds_write_b64 v245, v[184:185] offset:6144\n\t"
            "s_waitcnt lgkmcnt(7)\n\t"
            "v_mfma_f32_16x16x32_bf16 v[148:151], v[88:91], v[220:223], v[148:151]\n\t"
            "ds_read_b64 v[184:185], v253 offset:320\n\t"
            "v_mul_f32 v229, v237, v229\n\t"
            "v_mfma_f32_16x16x32_bf16 v[208:211], v[112:115], v[220:223], v[208:211]\n\t"
            "v_mul_f32 v230, v237, v230\n\t"
            "v_mul_f32 v231, v237, v231\n\t"
            "v_mfma_f32_16x16x32_bf16 v[212:215], v[136:139], v[220:223], v[212:215]\n\t"
            "v_mul_f32 v180, v238, v180\n\t"
            "v_mul_f32 v181, v238, v181\n\t"
            "v_mfma_f32_16x16x32_bf16 v[188:191], v[176:179], v[188:191], 0\n\t"
            "v_mul_f32 v182, v238, v182\n\t"
            "v_mul_f32 v183, v238, v183\n\t"
            "v_mfma_f32_16x16x32_bf16 v[188:191], v[160:163], v[192:195], v[188:191]\n\t"
            "ds_read_b128 v[192:195], v232 offset:6400\n\t"
            "v_cvt_pk_bf16_f32 v180, v180, v181\n\t"
            "v_mfma_f32_16x16x32_bf16 v[144:147], v[16:19], v[220:223], v[144:147]\n\t"
            "v_cvt_pk_bf16_f32 v181, v182, v183\n\t"
            "v_cvt_pk_bf16_f32 v228, v228, v229\n\t"
            "v_mfma_f32_16x16x32_bf16 v[204:207], v[40:43], v[220:223], v[204:207]\n\t"
            "ds_read_b64 v[182:183], v253 offset:288\n\t"
            "v_cvt_pk_bf16_f32 v229, v230, v231\n\t"
            "v_mfma_f32_16x16x32_bf16 v[152:155], v[64:67], v[220:223], v[152:155]\n\t"
            "ds_read_b128 v[220:223], v233 offset:6144\n\t"
            "v_mul_f32 v188, v237, v188\n\t"
            "s_waitcnt lgkmcnt(10)\n\t"
            "v_mfma_f32_16x16x32_bf16 v[144:147], v[20:23], v[224:227], v[144:147]\n\t"
            "ds_write_b64 v245, v[180:181] offset:12288\n\t"
            "v_mfma_f32_16x16x32_bf16 v[204:207], v[44:47], v[224:227], v[204:207]\n\t"
            "ds_read_b64 v[180:181], v253 offset:256\n\t"
            "v_mul_f32 v189, v237, v189\n\t"
            "v_mfma_f32_16x16x32_bf16 v[152:155], v[68:71], v[224:227], v[152:155]\n\t"
            "ds_write_b64 v245, v[228:229] offset:6272\n\t"
            "v_mfma_f32_16x16x32_bf16 v[148:151], v[92:95], v[224:227], v[148:151]\n\t"
            "v_mul_f32 v190, v237, v190\n\t"
            "v_mul_f32 v191, v237, v191\n\t"
            "v_mfma_f32_16x16x32_bf16 v[208:211], v[116:119], v[224:227], v[208:211]\n\t"
            "ds_read_b32 v237, v251 offset:64\n\t"
            "v_cvt_pk_bf16_f32 v188, v188, v189\n\t"
            "v_mfma_f32_16x16x32_bf16 v[212:215], v[140:143], v[224:227], v[212:215]\n\t"
            "ds_read_b128 v[224:227], v232 offset:6272\n\t"
            "v_cvt_pk_bf16_f32 v189, v190, v191\n\t"
            "v_mfma_f32_16x16x32_bf16 v[228:231], v[172:175], v[196:199], 0\n\t"
            "s_waitcnt lgkmcnt(14)\n\t"
            "ds_write_b64 v245, v[188:189] offset:6400\n\t"
            "v_mfma_f32_16x16x32_bf16 v[228:231], v[164:167], v[200:203], v[228:231]\n\t"
            "s_waitcnt lgkmcnt(14)\n\t"
            "ds_read_b128 v[188:191], v233 offset:6272\n\t"
            "v_mul_f32 v152, v236, v152\n\t"
            "v_mfma_f32_16x16x32_bf16 v[196:199], v[176:179], v[196:199], 0\n\t"
            "v_mul_f32 v153, v236, v153\n\t"
            "v_mul_f32 v154, v236, v154\n\t"
            "v_mfma_f32_16x16x32_bf16 v[196:199], v[160:163], v[200:203], v[196:199]\n\t"
            "s_waitcnt lgkmcnt(14)\n\t"
            "ds_read_b128 v[200:203], v233 offset:6400\n\t"
            "v_mul_f32 v155, v236, v155\n\t"
            "s_waitcnt lgkmcnt(6)\n\t"
            "v_mfma_f32_16x16x32_bf16 v[168:171], v[168:171], v[180:183], 0\n\t"
            "v_mul_f32 v228, v238, v228\n\t"
            "v_mul_f32 v229, v238, v229\n\t"
            "v_mfma_f32_16x16x32_bf16 v[168:171], v[156:159], v[184:187], v[168:171]\n\t"
            "v_mul_f32 v230, v238, v230\n\t"
            "v_mul_f32 v231, v238, v231\n\t"
            "v_mfma_f32_16x16x32_bf16 v[176:179], v[176:179], v[180:183], 0\n\t"
            "v_cvt_pk_bf16_f32 v228, v228, v229\n\t"
            "v_cvt_pk_bf16_f32 v229, v230, v231\n\t"
            "v_mfma_f32_16x16x32_bf16 v[176:179], v[160:163], v[184:187], v[176:179]\n\t"
            "ds_write_b64 v245, v[228:229] offset:12416\n\t"
            "v_mfma_f32_16x16x32_bf16 v[228:231], v[240:243], v[180:183], 0\n\t"
            "v_cvt_pk_bf16_f32 v152, v152, v153\n\t"
            "v_cvt_pk_bf16_f32 v153, v154, v155\n\t"
            "v_mfma_f32_16x16x32_bf16 v[228:231], v[240:243], v[184:187], v[228:231]\n\t"
            "v_mul_f32 v212, v236, v212\n\t"
            "v_mul_f32 v213, v236, v213\n\t"
            "v_mfma_f32_16x16x32_bf16 v[160:163], v[72:75], v[216:219], 0\n\t"
            "v_mul_f32 v214, v236, v214\n\t"
            "v_mul_f32 v215, v236, v215\n\t"
            "v_mfma_f32_16x16x32_bf16 v[160:163], v[76:79], v[220:223], v[160:163]\n\t"
            "v_cvt_pk_bf16_f32 v154, v212, v213\n\t"
            "v_cvt_pk_bf16_f32 v155, v214, v215\n\t"
            "s_waitcnt lgkmcnt(4)\n\t"
            "v_mfma_f32_16x16x32_bf16 v[160:163], v[80:83], v[224:227], v[160:163]\n\t"
            "ds_read_b128 v[212:215], v232 offset:12416\n\t"
            "v_mul_f32 v196, v238, v196\n\t"
            "s_waitcnt lgkmcnt(3)\n\t"
            "v_mfma_f32_16x16x32_bf16 v[160:163], v[84:87], v[188:191], v[160:163]\n\t"
            "v_mul_f32 v197, v238, v197\n\t"
            "v_mul_f32 v198, v238, v198\n\t"
            "v_mfma_f32_16x16x32_bf16 v[160:163], v[88:91], v[192:195], v[160:163]\n\t"
            "v_mul_f32 v199, v238, v199\n\t"
            "v_cvt_pk_bf16_f32 v196, v196, v197\n\t"
            "s_waitcnt lgkmcnt(2)\n\t"
            "v_mfma_f32_16x16x32_bf16 v[160:163], v[92:95], v[200:203], v[160:163]\n\t"
            "ds_read_b32 v238, v251 offset:128\n\t"
            "v_cvt_pk_bf16_f32 v197, v198, v199\n\t"
            "v_mfma_f32_16x16x32_bf16 v[172:175], v[172:175], v[180:183], 0\n\t"
            "ds_write_b64 v245, v[196:197] offset:12544\n\t"
            "v_mfma_f32_16x16x32_bf16 v[172:175], v[164:167], v[184:187], v[172:175]\n\t"
            "v_rcp_f32 v229, v228\n\t"
            "v_mfma_f32_16x16x16_bf16 v[180:183], v[152:153], v[154:155], 0\n\t"
            "v_mul_f32 v168, v229, v168\n\t"
            "v_mul_f32 v169, v229, v169\n\t"
            "v_mul_f32 v176, v229, v176\n\t"
            "v_mfma_f32_16x16x32_bf16 v[196:199], v[24:27], v[216:219], 0\n\t"
            "v_mul_f32 v177, v229, v177\n\t"
            "v_mul_f32 v170, v229, v170\n\t"
            "v_mfma_f32_16x16x32_bf16 v[196:199], v[28:31], v[220:223], v[196:199]\n\t"
            "v_mul_f32 v172, v229, v172\n\t"
            "v_mul_f32 v173, v229, v173\n\t"
            "v_mfma_f32_16x16x32_bf16 v[196:199], v[32:35], v[224:227], v[196:199]\n\t"
            "v_mul_f32 v171, v229, v171\n\t"
            "v_cvt_pk_bf16_f32 v168, v168, v169\n\t"
            "v_mfma_f32_16x16x32_bf16 v[196:199], v[36:39], v[188:191], v[196:199]\n\t"
            "v_mul_f32 v174, v229, v174\n\t"
            "v_mul_f32 v175, v229, v175\n\t"
            "v_mfma_f32_16x16x32_bf16 v[196:199], v[40:43], v[192:195], v[196:199]\n\t"
            "v_cvt_pk_bf16_f32 v172, v172, v173\n\t"
            "v_mul_f32 v178, v229, v178\n\t"
            "v_mfma_f32_16x16x32_bf16 v[196:199], v[44:47], v[200:203], v[196:199]\n\t"
            "v_mul_f32 v179, v229, v179\n\t"
            "v_cvt_pk_bf16_f32 v176, v176, v177\n\t"
            "v_mfma_f32_16x16x32_bf16 v[156:159], v[0:3], v[216:219], 0\n\t"
            "v_xor_b32 v230, s73, v255\n\t"
            "v_cvt_pk_bf16_f32 v169, v170, v171\n\t"
            "v_mfma_f32_16x16x32_bf16 v[156:159], v[4:7], v[220:223], v[156:159]\n\t"
            "v_cvt_pk_bf16_f32 v173, v174, v175\n\t"
            "v_cvt_pk_bf16_f32 v177, v178, v179\n\t"
            "v_mfma_f32_16x16x32_bf16 v[156:159], v[8:11], v[224:227], v[156:159]\n\t"
            "s_mov_b64 exec, s[68:69]\n\t"
            "ds_write_b64 v230, v[168:169]\n\t"
            "ds_write_b64 v230, v[172:173] offset:128\n\t"
            "ds_write_b64 v230, v[176:177] offset:256\n\t"
            "s_mov_b64 exec, -1\n\t"
            "v_mfma_f32_16x16x32_bf16 v[156:159], v[12:15], v[188:191], v[156:159]\n\t"
            "v_mul_f32 v144, v236, v144\n\t"
            "v_mul_f32 v145, v236, v145\n\t"
            "v_mfma_f32_16x16x32_bf16 v[156:159], v[16:19], v[192:195], v[156:159]\n\t"
            "v_mul_f32 v146, v236, v146\n\t"
            "v_mul_f32 v147, v236, v147\n\t"
            "v_mfma_f32_16x16x32_bf16 v[156:159], v[20:23], v[200:203], v[156:159]\n\t"
            "v_cvt_pk_bf16_f32 v144, v144, v145\n\t"
            "v_mul_f32 v148, v236, v148\n\t"
            "v_mfma_f32_16x16x32_bf16 v[164:167], v[48:51], v[216:219], 0\n\t"
            "v_mul_f32 v149, v236, v149\n\t"
            "v_cvt_pk_bf16_f32 v145, v146, v147\n\t"
            "v_mfma_f32_16x16x32_bf16 v[164:167], v[52:55], v[220:223], v[164:167]\n\t"
            "v_mul_f32 v204, v236, v204\n\t"
            "v_mul_f32 v205, v236, v205\n\t"
            "v_mfma_f32_16x16x32_bf16 v[164:167], v[56:59], v[224:227], v[164:167]\n\t"
            "v_mul_f32 v206, v236, v206\n\t"
            "v_mul_f32 v207, v236, v207\n\t"
            "v_mfma_f32_16x16x32_bf16 v[164:167], v[60:63], v[188:191], v[164:167]\n\t"
            "s_waitcnt lgkmcnt(0)\n\t"
            "s_barrier\n\t"
            "s_setprio 2\n\t"
            "v_mul_f32 v150, v236, v150\n\t"
            "v_mul_f32 v151, v236, v151\n\t"
            "v_mfma_f32_16x16x32_bf16 v[164:167], v[64:67], v[192:195], v[164:167]\n\t"
            "v_cvt_pk_bf16_f32 v148, v148, v149\n\t"
            "v_cvt_pk_bf16_f32 v146, v204, v205\n\t"
            "v_mfma_f32_16x16x32_bf16 v[164:167], v[68:71], v[200:203], v[164:167]\n\t"
            "v_cvt_pk_bf16_f32 v147, v206, v207\n\t"
            "v_cvt_pk_bf16_f32 v149, v150, v151\n\t"
            "v_mfma_f32_16x16x32_bf16 v[228:231], v[96:99], v[216:219], 0\n\t"
            "ds_read_b128 v[204:207], v232 offset:12288\n\t"
            "v_mul_f32 v208, v236, v208\n\t"
            "v_mfma_f32_16x16x32_bf16 v[228:231], v[100:103], v[220:223], v[228:231]\n\t"
            "v_mul_f32 v209, v236, v209\n\t"
            "v_mul_f32 v210, v236, v210\n\t"
            "v_mfma_f32_16x16x32_bf16 v[228:231], v[104:107], v[224:227], v[228:231]\n\t"
            "v_mul_f32 v211, v236, v211\n\t"
            "v_cvt_pk_bf16_f32 v150, v208, v209\n\t"
            "v_mfma_f32_16x16x32_bf16 v[228:231], v[108:111], v[188:191], v[228:231]\n\t"
            "v_cvt_pk_bf16_f32 v151, v210, v211\n\t"
            "v_mul_f32 v156, v237, v156\n\t"
            "v_mfma_f32_16x16x32_bf16 v[228:231], v[112:115], v[192:195], v[228:231]\n\t"
            "ds_read_b128 v[208:211], v233 offset:12288\n\t"
            "v_mul_f32 v157, v237, v157\n\t"
            "v_mfma_f32_16x16x32_bf16 v[228:231], v[116:119], v[200:203], v[228:231]\n\t"
            "v_mul_f32 v158, v237, v158\n\t"
            "v_mul_f32 v159, v237, v159\n\t"
            "v_mfma_f32_16x16x32_bf16 v[180:183], v[144:147], v[148:151], v[180:183]\n\t"
            "v_cvt_pk_bf16_f32 v156, v156, v157\n\t"
            "v_cvt_pk_bf16_f32 v157, v158, v159\n\t"
            "v_mul_f32 v196, v237, v196\n\t"
            "v_mul_f32 v197, v237, v197\n\t"
            "v_mul_f32 v198, v237, v198\n\t"
            "v_mul_f32 v199, v237, v199\n\t"
            "v_cvt_pk_bf16_f32 v158, v196, v197\n\t"
            "v_cvt_pk_bf16_f32 v159, v198, v199\n\t"
            "v_mfma_f32_16x16x32_bf16 v[196:199], v[120:123], v[216:219], 0\n\t"
            "ds_read_b128 v[216:219], v233 offset:12416\n\t"
            "v_mul_f32 v164, v237, v164\n\t"
            "v_mfma_f32_16x16x32_bf16 v[196:199], v[124:127], v[220:223], v[196:199]\n\t"
            "ds_read_b128 v[220:223], v232 offset:12544\n\t"
            "v_mul_f32 v165, v237, v165\n\t"
            "v_mfma_f32_16x16x32_bf16 v[196:199], v[128:131], v[224:227], v[196:199]\n\t"
            "ds_read_b128 v[224:227], v233 offset:12544\n\t"
            "v_mul_f32 v166, v237, v166\n\t"
            "v_mfma_f32_16x16x32_bf16 v[196:199], v[132:135], v[188:191], v[196:199]\n\t"
            "v_mul_f32 v167, v237, v167\n\t"
            "v_cvt_pk_bf16_f32 v164, v164, v165\n\t"
            "v_mfma_f32_16x16x32_bf16 v[196:199], v[136:139], v[192:195], v[196:199]\n\t"
            "v_cvt_pk_bf16_f32 v165, v166, v167\n\t"
            "v_mul_f32 v160, v237, v160\n\t"
            "v_mfma_f32_16x16x32_bf16 v[196:199], v[140:143], v[200:203], v[196:199]\n\t"
            "v_mul_f32 v161, v237, v161\n\t"
            "v_mul_f32 v162, v237, v162\n\t"
            "s_waitcnt lgkmcnt(4)\n\t"
            "v_mfma_f32_16x16x32_bf16 v[176:179], v[48:51], v[204:207], 0\n\t"
            "v_mul_f32 v163, v237, v163\n\t"
            "v_cvt_pk_bf16_f32 v160, v160, v161\n\t"
            "s_waitcnt lgkmcnt(3)\n\t"
            "v_mfma_f32_16x16x32_bf16 v[176:179], v[52:55], v[208:211], v[176:179]\n\t"
            "v_cvt_pk_bf16_f32 v161, v162, v163\n\t"
            "v_mul_f32 v228, v237, v228\n\t"
            "v_mfma_f32_16x16x32_bf16 v[176:179], v[56:59], v[212:215], v[176:179]\n\t"
            "v_mul_f32 v196, v237, v196\n\t"
            "v_mul_f32 v197, v237, v197\n\t"
            "s_waitcnt lgkmcnt(2)\n\t"
            "v_mfma_f32_16x16x32_bf16 v[176:179], v[60:63], v[216:219], v[176:179]\n\t"
            "v_mul_f32 v198, v237, v198\n\t"
            "v_mul_f32 v199, v237, v199\n\t"
            "s_waitcnt lgkmcnt(1)\n\t"
            "v_mfma_f32_16x16x32_bf16 v[176:179], v[64:67], v[220:223], v[176:179]\n\t"
            "v_cvt_pk_bf16_f32 v166, v196, v197\n\t"
            "v_cvt_pk_bf16_f32 v167, v198, v199\n\t"
            "s_waitcnt lgkmcnt(0)\n\t"
            "v_mfma_f32_16x16x32_bf16 v[176:179], v[68:71], v[224:227], v[176:179]\n\t"
            "v_mul_f32 v229, v237, v229\n\t"
            "v_mul_f32 v230, v237, v230\n\t"
            "v_mfma_f32_16x16x16_bf16 v[188:191], v[152:153], v[166:167], 0\n\t"
            "v_mul_f32 v231, v237, v231\n\t"
            "v_cvt_pk_bf16_f32 v162, v228, v229\n\t"
            "v_cvt_pk_bf16_f32 v163, v230, v231\n\t"
            "v_mfma_f32_16x16x32_bf16 v[168:171], v[0:3], v[204:207], 0\n\t"
            "v_exp_f32 v180, v180\n\t"
            "v_mfma_f32_16x16x32_bf16 v[168:171], v[4:7], v[208:211], v[168:171]\n\t"
            "v_exp_f32 v181, v181\n\t"
            "v_mfma_f32_16x16x32_bf16 v[168:171], v[8:11], v[212:215], v[168:171]\n\t"
            "v_exp_f32 v182, v182\n\t"
            "v_mfma_f32_16x16x32_bf16 v[168:171], v[12:15], v[216:219], v[168:171]\n\t"
            "v_exp_f32 v183, v183\n\t"
            "v_mfma_f32_16x16x32_bf16 v[168:171], v[16:19], v[220:223], v[168:171]\n\t"
            "v_mul_f32 v176, v238, v176\n\t"
            "v_mul_f32 v177, v238, v177\n\t"
            "v_mfma_f32_16x16x32_bf16 v[168:171], v[20:23], v[224:227], v[168:171]\n\t"
            "v_mul_f32 v178, v238, v178\n\t"
            "v_mul_f32 v179, v238, v179\n\t"
            "v_mfma_f32_16x16x16_bf16 v[192:195], v[164:165], v[166:167], 0\n\t"
            "v_cvt_pk_bf16_f32 v176, v176, v177\n\t"
            "v_cvt_pk_bf16_f32 v180, v180, v181\n\t"
            "v_cvt_pk_bf16_f32 v177, v178, v179\n\t"
            "v_mfma_f32_16x16x32_bf16 v[188:191], v[144:147], v[160:163], v[188:191]\n\t"
            "v_cvt_pk_bf16_f32 v181, v182, v183\n\t"
            "v_mul_f32 v168, v238, v168\n\t"
            "v_mfma_f32_16x16x16_bf16 v[184:187], v[164:165], v[154:155], 0\n\t"
            "v_mul_f32 v169, v238, v169\n\t"
            "v_mul_f32 v170, v238, v170\n\t"
            "v_mul_f32 v171, v238, v171\n\t"
            "v_mfma_f32_16x16x32_bf16 v[192:195], v[156:159], v[160:163], v[192:195]\n\t"
            "s_nop 0\n\t"
            "v_exp_f32 v188, v188\n\t"
            "v_mfma_f32_16x16x32_bf16 v[172:175], v[72:75], v[204:207], 0\n\t"
            "v_exp_f32 v189, v189\n\t"
            "v_mfma_f32_16x16x32_bf16 v[172:175], v[76:79], v[208:211], v[172:175]\n\t"
            "v_exp_f32 v190, v190\n\t"
            "v_mfma_f32_16x16x32_bf16 v[172:175], v[80:83], v[212:215], v[172:175]\n\t"
            "v_exp_f32 v191, v191\n\t"
            "v_mfma_f32_16x16x32_bf16 v[172:175], v[84:87], v[216:219], v[172:175]\n\t"
            "v_exp_f32 v192, v192\n\t"
            "v_mfma_f32_16x16x32_bf16 v[172:175], v[88:91], v[220:223], v[172:175]\n\t"
            "v_exp_f32 v193, v193\n\t"
            "v_mfma_f32_16x16x32_bf16 v[172:175], v[92:95], v[224:227], v[172:175]\n\t"
            "v_exp_f32 v194, v194\n\t"
            "v_mfma_f32_16x16x32_bf16 v[184:187], v[156:159], v[148:151], v[184:187]\n\t"
            "v_exp_f32 v195, v195\n\t"
            "v_mfma_f32_16x16x32_bf16 v[200:203], v[120:123], v[204:207], 0\n\t"
            "v_cvt_pk_bf16_f32 v188, v188, v189\n\t"
            "v_cvt_pk_bf16_f32 v189, v190, v191\n\t"
            "v_mfma_f32_16x16x32_bf16 v[200:203], v[124:127], v[208:211], v[200:203]\n\t"
            "v_cvt_pk_bf16_f32 v190, v192, v193\n\t"
            "v_cvt_pk_bf16_f32 v191, v194, v195\n\t"
            "v_mfma_f32_16x16x32_bf16 v[200:203], v[128:131], v[212:215], v[200:203]\n\t"
            "v_exp_f32 v184, v184\n\t"
            "v_mfma_f32_16x16x32_bf16 v[200:203], v[132:135], v[216:219], v[200:203]\n\t"
            "v_exp_f32 v185, v185\n\t"
            "v_mfma_f32_16x16x32_bf16 v[200:203], v[136:139], v[220:223], v[200:203]\n\t"
            "v_exp_f32 v186, v186\n\t"
            "v_mfma_f32_16x16x32_bf16 v[200:203], v[140:143], v[224:227], v[200:203]\n\t"
            "v_exp_f32 v187, v187\n\t"
            "v_cvt_pk_bf16_f32 v182, v184, v185\n\t"
            "v_cvt_pk_bf16_f32 v183, v186, v187\n\t"
            "v_mfma_f32_16x16x16_bf16 v[184:187], v[176:177], v[154:155], 0\n\t"
            "v_cvt_pk_bf16_f32 v168, v168, v169\n\t"
            "v_cvt_pk_bf16_f32 v169, v170, v171\n\t"
            "v_mul_f32 v172, v238, v172\n\t"
            "v_mfma_f32_16x16x32_bf16 v[196:199], v[24:27], v[204:207], 0\n\t"
            "s_setprio 0\n\t"
            "v_mul_f32 v173, v238, v173\n\t"
            "v_mul_f32 v200, v238, v200\n\t"
            "v_mfma_f32_16x16x32_bf16 v[196:199], v[28:31], v[208:211], v[196:199]\n\t"
            "v_mul_f32 v201, v238, v201\n\t"
            "v_mul_f32 v202, v238, v202\n\t"
            "v_mfma_f32_16x16x32_bf16 v[196:199], v[32:35], v[212:215], v[196:199]\n\t"
            "v_mul_f32 v203, v238, v203\n\t"
            "v_mul_f32 v174, v238, v174\n\t"
            "v_mfma_f32_16x16x32_bf16 v[196:199], v[36:39], v[216:219], v[196:199]\n\t"
            "v_mul_f32 v175, v238, v175\n\t"
            "v_cvt_pk_bf16_f32 v172, v172, v173\n\t"
            "v_mfma_f32_16x16x32_bf16 v[196:199], v[40:43], v[220:223], v[196:199]\n\t"
            "v_cvt_pk_bf16_f32 v178, v200, v201\n\t"
            "v_cvt_pk_bf16_f32 v179, v202, v203\n\t"
            "v_mfma_f32_16x16x32_bf16 v[196:199], v[44:47], v[224:227], v[196:199]\n\t"
            "v_cvt_pk_bf16_f32 v173, v174, v175\n\t"
            "v_mfma_f32_16x16x32_bf16 v[228:231], v[96:99], v[204:207], 0\n\t"
            "ds_read_b64 v[204:205], v235 offset:136\n\t"
            "v_mfma_f32_16x16x32_bf16 v[228:231], v[100:103], v[208:211], v[228:231]\n\t"
            "ds_read_b64 v[206:207], v234 offset:128\n\t"
            "v_mfma_f32_16x16x32_bf16 v[228:231], v[104:107], v[212:215], v[228:231]\n\t"
            "ds_read_b128 v[208:211], v234\n\t"
            "v_mfma_f32_16x16x32_bf16 v[228:231], v[108:111], v[216:219], v[228:231]\n\t"
            "ds_read_b128 v[212:215], v235 offset:64\n\t"
            "v_mul_f32 v196, v238, v196\n\t"
            "v_mfma_f32_16x16x32_bf16 v[228:231], v[112:115], v[220:223], v[228:231]\n\t"
            "v_mul_f32 v197, v238, v197\n\t"
            "v_mul_f32 v198, v238, v198\n\t"
            "v_mfma_f32_16x16x32_bf16 v[228:231], v[116:119], v[224:227], v[228:231]\n\t"
            "v_mul_f32 v199, v238, v199\n\t"
            "v_cvt_pk_bf16_f32 v170, v196, v197\n\t"
            "v_mfma_f32_16x16x16_bf16 v[192:195], v[176:177], v[166:167], 0\n\t"
            "v_cvt_pk_bf16_f32 v171, v198, v199\n\t"
            "s_nop 1\n\t"
            "v_mfma_f32_16x16x32_bf16 v[184:187], v[168:171], v[148:151], v[184:187]\n\t"
            "s_nop 0\n\t"
            "v_mul_f32 v228, v238, v228\n\t"
            "v_mul_f32 v229, v238, v229\n\t"
            "v_mul_f32 v230, v238, v230\n\t"
            "v_mul_f32 v231, v238, v231\n\t"
            "v_mfma_f32_16x16x32_bf16 v[192:195], v[168:171], v[160:163], v[192:195]\n\t"
            "v_cvt_pk_bf16_f32 v174, v228, v229\n\t"
            "v_cvt_pk_bf16_f32 v175, v230, v231\n\t"
            "s_waitcnt lgkmcnt(2)\n\t"
            "v_mfma_f32_16x16x32_bf16 v[216:219], v[204:207], v[152:155], 0\n\t"
            "v_exp_f32 v184, v184\n\t"
            "s_waitcnt lgkmcnt(1)\n\t"
            "v_mfma_f32_16x16x32_bf16 v[216:219], v[208:211], v[148:151], v[216:219]\n\t"
            "v_exp_f32 v185, v185\n\t"
            "s_waitcnt lgkmcnt(0)\n\t"
            "v_mfma_f32_16x16x32_bf16 v[216:219], v[212:215], v[144:147], v[216:219]\n\t"
            "v_exp_f32 v186, v186\n\t"
            "v_mfma_f32_16x16x32_bf16 v[220:223], v[204:207], v[164:167], 0\n\t"
            "v_exp_f32 v187, v187\n\t"
            "v_mfma_f32_16x16x32_bf16 v[220:223], v[208:211], v[160:163], v[220:223]\n\t"
            "v_cvt_pk_bf16_f32 v184, v184, v185\n\t"
            "v_cvt_pk_bf16_f32 v185, v186, v187\n\t"
            "v_mfma_f32_16x16x32_bf16 v[220:223], v[212:215], v[156:159], v[220:223]\n\t"
            "s_nop 0\n\t"
            "v_exp_f32 v216, v216\n\t"
            "v_mfma_f32_16x16x16_bf16 v[228:231], v[164:165], v[178:179], 0\n\t"
            "v_cvt_pk_bf16_f32 v216, v216, 0\n\t"
            "v_cndmask_b32_e64 v186, 0, v216, s[58:59]\n\t"
            "v_mov_b32 v187, 0\n\t"
            "v_mfma_f32_16x16x16_bf16 v[200:203], v[176:177], v[178:179], 0\n\t"
            "v_exp_f32 v192, v192\n\t"
            "v_exp_f32 v193, v193\n\t"
            "v_mfma_f32_16x16x32_bf16 v[224:227], v[204:207], v[176:179], 0\n\t"
            "v_exp_f32 v194, v194\n\t"
            "v_mfma_f32_16x16x32_bf16 v[224:227], v[208:211], v[172:175], v[224:227]\n\t"
            "v_exp_f32 v195, v195\n\t"
            "v_mfma_f32_16x16x32_bf16 v[224:227], v[212:215], v[168:171], v[224:227]\n\t"
            "v_exp_f32 v220, v220\n\t"
            "v_mfma_f32_16x16x16_bf16 v[196:199], v[152:153], v[178:179], 0\n\t"
            "v_cvt_pk_bf16_f32 v192, v192, v193\n\t"
            "v_cvt_pk_bf16_f32 v193, v194, v195\n\t"
            "v_cvt_pk_bf16_f32 v220, v220, 0\n\t"
            "v_mfma_f32_16x16x32_bf16 v[204:207], v[240:243], v[180:183], 0\n\t"
            "v_cndmask_b32_e64 v194, 0, v220, s[58:59]\n\t"
            "v_mov_b32 v195, 0\n\t"
            "v_mfma_f32_16x16x32_bf16 v[204:207], v[240:243], v[184:187], v[204:207]\n\t"
            "v_cndmask_b32_e64 v235, v217, v221, s[60:61]\n\t"
            "v_cndmask_b32_e64 v235, v235, v225, s[62:63]\n\t"
            "v_mfma_f32_16x16x32_bf16 v[200:203], v[168:171], v[172:175], v[200:203]\n\t"
            "v_cndmask_b32_e64 v235, v235, v239, s[64:65]\n\t"
            "v_cmp_nlt_f32_e64 s[24:25], |v235|, s23\n\t"
            "v_mfma_f32_16x16x32_bf16 v[208:211], v[240:243], v[188:191], 0\n\t"
            "v_exp_f32 v235, v235\n\t"
            "v_mfma_f32_16x16x32_bf16 v[208:211], v[240:243], v[192:195], v[208:211]\n\t"
            "v_exp_f32 v224, v224\n\t"
            "v_mfma_f32_16x16x32_bf16 v[196:199], v[144:147], v[172:175], v[196:199]\n\t"
            "s_nop 0\n\t"
            "v_exp_f32 v200, v200\n\t"
            "v_mfma_f32_16x16x32_bf16 v[228:231], v[156:159], v[172:175], v[228:231]\n\t"
            "v_exp_f32 v201, v201\n\t"
            "v_exp_f32 v202, v202\n\t"
            "v_exp_f32 v203, v203\n\t"
            "v_cvt_pk_bf16_f32 v235, v235, 0\n\t"
            "v_cvt_pk_bf16_f32 v200, v200, v201\n\t"
            "v_cndmask_b32_e64 v235, 0, v235, s[66:67]\n\t"
            "v_exp_f32 v196, v196\n\t"
            "v_exp_f32 v197, v197\n\t"
            "v_exp_f32 v198, v198\n\t"
            "v_exp_f32 v199, v199\n\t"
            "v_exp_f32 v228, v228\n\t"
            "v_exp_f32 v229, v229\n\t"
            "v_exp_f32 v230, v230\n\t"
            "v_exp_f32 v231, v231\n\t"
            "v_cvt_pk_bf16_f32 v196, v196, v197\n\t"
            "v_cvt_pk_bf16_f32 v197, v198, v199\n\t"
            "v_cvt_pk_bf16_f32 v198, v228, v229\n\t"
            "v_cvt_pk_bf16_f32 v199, v230, v231\n\t"
            "v_cvt_pk_bf16_f32 v201, v202, v203\n\t"
            "v_cvt_pk_bf16_f32 v224, v224, 0\n\t"
            "v_mfma_f32_16x16x32_bf16 v[212:215], v[240:243], v[196:199], 0\n\t"
            "ds_write_b16 v250, v235 offset:256\n\t"
            "v_cndmask_b32_e64 v202, 0, v224, s[58:59]\n\t"
            "v_mov_b32 v203, 0\n\t"
            "v_add_u32 v205, 0xe0800000, v204\n\t"
            "v_add_u32 v209, 0xe0800000, v208\n\t"
            "v_mfma_f32_16x16x32_bf16 v[212:215], v[240:243], v[200:203], v[212:215]\n\t"
            "v_rcp_f32 v236, v204\n\t"
            "v_rcp_f32 v237, v208\n\t"
            "s_nop 5\n\t"
            "v_add_u32 v213, 0xe0800000, v212\n\t"
            "v_rcp_f32 v238, v212\n\t"
            "v_or3_b32 v205, v205, v209, v213\n\t"
            "v_cmp_le_u32 vcc, 2.0, v205\n\t"
            ".Lwa_in%=:\n\t"
            "s_or_b64 vcc, vcc, s[24:25]\n\t"
            "s_cbranch_scc1 .Lwa_slow%=\n\t"
            ".Lwa_ok%=:\n\t"
            "s_bfe_u32 s49, s70, 0x30001\n\t"
            "s_lshl_b32 s49, s49, 4\n\t"
            "s_or_b32 s73, s77, s49\n\t"
            "s_add_u32 s70, s70, 1\n\t"
            "s_add_u32 s72, s72, 800\n\t"
            "s_add_u32 s76, s76, 800\n\t"
            "s_add_u32 s77, s77, 384\n\t"
            "s_xor_b32 s48, s48, s47\n\t"
            "s_sub_u32 s44, s44, 1\n\t"
            "s_cmp_gt_u32 s44, 2\n\t"
            "s_barrier\n\t"
            "s_setprio 0\n\t"
            "s_cbranch_scc1 .Lwa_loop%=\n\t"
            "s_cmp_eq_u32 s44, 0\n\t"
            "s_cbranch_scc1 .Lwa_done%=\n\t"
            "s_cmp_lg_u32 s44, 2\n\t"
            "s_cbranch_scc1 .Lwa_loop%=\n\t"
            "s_mov_b32 m0, s40\n\t"
            "v_lshlrev_b32 v234, 3, v248\n\t"
            "buffer_load_dword v234, s[36:39], s41 offen sc1 lds\n\t"
            "s_branch .Lwa_loop%=\n\t"
            ".Lwa_slow%=:\n\t"
            "s_waitcnt lgkmcnt(0)\n\t"
            "s_nop 7\n\t"
            "v_mfma_f32_16x16x16_bf16 v[180:183], v[152:153], v[154:155], 0\n\t"
            "v_add_u32 v234, s72, v252\n\t"
            "v_cndmask_b32_e64 v235, v246, v234, s[50:51]\n\t"
            "v_cndmask_b32_e64 v234, v246, v234, s[26:27]\n\t"
            "v_mfma_f32_16x16x16_bf16 v[228:231], v[164:165], v[154:155], 0\n\t"
            "ds_read_b64 v[216:217], v235 offset:136\n\t"
            "ds_read_b64 v[218:219], v234 offset:128\n\t"
            "v_mfma_f32_16x16x16_bf16 v[184:187], v[176:177], v[154:155], 0\n\t"
            "ds_read_b128 v[220:223], v234\n\t"
            "ds_read_b128 v[224:227], v235 offset:64\n\t"
            "v_mfma_f32_16x16x32_bf16 v[180:183], v[144:147], v[148:151], v[180:183]\n\t"
            "v_mfma_f32_16x16x32_bf16 v[228:231], v[156:159], v[148:151], v[228:231]\n\t"
            "s_nop 1\n\t"
            "v_mfma_f32_16x16x32_bf16 v[184:187], v[168:171], v[148:151], v[184:187]\n\t"
            "v_mfma_f32_16x16x16_bf16 v[188:191], v[152:153], v[166:167], 0\n\t"
            "v_mfma_f32_16x16x16_bf16 v[192:195], v[176:177], v[166:167], 0\n\t"
            "v_mfma_f32_16x16x16_bf16 v[196:199], v[152:153], v[178:179], 0\n\t"
            "v_mfma_f32_16x16x16_bf16 v[200:203], v[176:177], v[178:179], 0\n\t"
            "s_waitcnt lgkmcnt(2)\n\t"
            "v_mfma_f32_16x16x32_bf16 v[204:207], v[216:219], v[152:155], 0\n\t"
            "s_waitcnt lgkmcnt(1)\n\t"
            "v_mfma_f32_16x16x32_bf16 v[204:207], v[220:223], v[148:151], v[204:207]\n\t"
            "s_waitcnt lgkmcnt(0)\n\t"
            "v_mfma_f32_16x16x32_bf16 v[204:207], v[224:227], v[144:147], v[204:207]\n\t"
            "v_mfma_f32_16x16x32_bf16 v[208:211], v[216:219], v[164:167], 0\n\t"
            "v_mfma_f32_16x16x32_bf16 v[208:211], v[220:223], v[160:163], v[208:211]\n\t"
            "v_mfma_f32_16x16x32_bf16 v[208:211], v[224:227], v[156:159], v[208:211]\n\t"
            "v_mfma_f32_16x16x32_bf16 v[188:191], v[144:147], v[160:163], v[188:191]\n\t"
            "v_mfma_f32_16x16x32_bf16 v[192:195], v[168:171], v[160:163], v[192:195]\n\t"
            "v_mfma_f32_16x16x32_bf16 v[212:215], v[216:219], v[176:179], 0\n\t"
            "s_nop 1\n\t"
            "v_max3_f32 v232, v204, v180, v181\n\t"
            "v_mfma_f32_16x16x32_bf16 v[212:215], v[220:223], v[172:175], v[212:215]\n\t"
            "v_max3_f32 v232, v232, v182, v183\n\t"
            "v_max3_f32 v232, v232, v228, v229\n\t"
            "v_mfma_f32_16x16x32_bf16 v[212:215], v[224:227], v[168:171], v[212:215]\n\t"
            "v_max3_f32 v232, v232, v230, v231\n\t"
            "v_max3_f32 v232, v232, v184, v185\n\t"
            "v_mfma_f32_16x16x32_bf16 v[196:199], v[144:147], v[172:175], v[196:199]\n\t"
            "v_max3_f32 v232, v232, v186, v187\n\t"
            "v_mov_b32 v233, v232\n\t"
            "v_mfma_f32_16x16x32_bf16 v[200:203], v[168:171], v[172:175], v[200:203]\n\t"
            "s_nop 0\n\t"
            "v_permlane32_swap_b32 v232, v233\n\t"
            "v_max_f32 v232, v232, v233\n\t"
            "v_mov_b32 v233, v232\n\t"
            "v_max3_f32 v234, v205, v209, v213\n\t"
            "v_max_f32 v234, v234, v239\n\t"
            "v_permlane16_swap_b32 v232, v233\n\t"
            "v_max_f32 v232, v232, v233\n\t"
            "v_sub_f32 v180, v180, v232\n\t"
            "v_sub_f32 v181, v181, v232\n\t"
            "v_exp_f32 v180, v180\n\t"
            "v_exp_f32 v181, v181\n\t"
            "v_sub_f32 v182, v182, v232\n\t"
            "v_sub_f32 v183, v183, v232\n\t"
            "v_exp_f32 v182, v182\n\t"
            "v_exp_f32 v183, v183\n\t"
            "v_sub_f32 v228, v228, v232\n\t"
            "v_sub_f32 v229, v229, v232\n\t"
            "v_sub_f32 v230, v230, v232\n\t"
            "v_sub_f32 v231, v231, v232\n\t"
            "v_exp_f32 v228, v228\n\t"
            "v_exp_f32 v229, v229\n\t"
            "v_exp_f32 v230, v230\n\t"
            "v_exp_f32 v231, v231\n\t"
            "v_cvt_pk_bf16_f32 v180, v180, v181\n\t"
            "v_cvt_pk_bf16_f32 v181, v182, v183\n\t"
            "v_cvt_pk_bf16_f32 v182, v228, v229\n\t"
            "v_cvt_pk_bf16_f32 v183, v230, v231\n\t"
            "v_mfma_f32_16x16x16_bf16 v[228:231], v[164:165], v[166:167], 0\n\t"
            "v_sub_f32 v184, v184, v232\n\t"
            "v_sub_f32 v185, v185, v232\n\t"
            "v_sub_f32 v186, v186, v232\n\t"
            "v_sub_f32 v187, v187, v232\n\t"
            "v_sub_f32 v204, v204, v232\n\t"
            "v_max3_f32 v232, v208, v188, v189\n\t"
            "v_max3_f32 v232, v232, v190, v191\n\t"
            "v_mfma_f32_16x16x32_bf16 v[228:231], v[156:159], v[160:163], v[228:231]\n\t"
            "v_max_f32_dpp v234, v234, v234 row_ror:8 row_mask:0xf bank_mask:0xf\n\t"
            "v_cndmask_b32_e64 v235, v205, v209, s[60:61]\n\t"
            "v_cndmask_b32_e64 v235, v235, v213, s[62:63]\n\t"
            "v_max_f32_dpp v234, v234, v234 row_ror:4 row_mask:0xf bank_mask:0xf\n\t"
            "v_cndmask_b32_e64 v235, v235, v239, s[64:65]\n\t"
            "v_exp_f32 v184, v184\n\t"
            "v_max_f32_dpp v234, v234, v234 row_ror:2 row_mask:0xf bank_mask:0xf\n\t"
            "v_exp_f32 v185, v185\n\t"
            "v_max3_f32 v232, v232, v228, v229\n\t"
            "v_max3_f32 v232, v232, v230, v231\n\t"
            "v_max3_f32 v232, v232, v192, v193\n\t"
            "v_max3_f32 v232, v232, v194, v195\n\t"
            "v_mov_b32 v233, v232\n\t"
            "v_max_f32_dpp v234, v234, v234 row_ror:1 row_mask:0xf bank_mask:0xf\n\t"
            "v_sub_f32 v235, v235, v234\n\t"
            "v_permlane32_swap_b32 v232, v233\n\t"
            "v_max_f32 v232, v232, v233\n\t"
            "v_mov_b32 v233, v232\n\t"
            "v_exp_f32 v235, v235\n\t"
            "v_exp_f32 v186, v186\n\t"
            "v_permlane16_swap_b32 v232, v233\n\t"
            "v_max_f32 v232, v232, v233\n\t"
            "v_sub_f32 v188, v188, v232\n\t"
            "v_sub_f32 v189, v189, v232\n\t"
            "v_exp_f32 v188, v188\n\t"
            "v_exp_f32 v189, v189\n\t"
            "v_sub_f32 v190, v190, v232\n\t"
            "v_sub_f32 v191, v191, v232\n\t"
            "v_exp_f32 v190, v190\n\t"
            "v_exp_f32 v191, v191\n\t"
            "v_sub_f32 v228, v228, v232\n\t"
            "v_sub_f32 v229, v229, v232\n\t"
            "v_sub_f32 v230, v230, v232\n\t"
            "v_sub_f32 v231, v231, v232\n\t"
            "v_exp_f32 v228, v228\n\t"
            "v_exp_f32 v229, v229\n\t"
            "v_exp_f32 v230, v230\n\t"
            "v_exp_f32 v231, v231\n\t"
            "v_cvt_pk_bf16_f32 v188, v188, v189\n\t"
            "v_cvt_pk_bf16_f32 v189, v190, v191\n\t"
            "v_cvt_pk_bf16_f32 v190, v228, v229\n\t"
            "v_cvt_pk_bf16_f32 v191, v230, v231\n\t"
            "v_mfma_f32_16x16x16_bf16 v[228:231], v[164:165], v[178:179], 0\n\t"
            "v_sub_f32 v192, v192, v232\n\t"
            "v_sub_f32 v193, v193, v232\n\t"
            "v_sub_f32 v194, v194, v232\n\t"
            "v_sub_f32 v195, v195, v232\n\t"
            "v_sub_f32 v208, v208, v232\n\t"
            "v_max3_f32 v232, v212, v196, v197\n\t"
            "v_max3_f32 v232, v232, v198, v199\n\t"
            "v_mfma_f32_16x16x32_bf16 v[228:231], v[156:159], v[172:175], v[228:231]\n\t"
            "v_exp_f32 v192, v192\n\t"
            "v_exp_f32 v193, v193\n\t"
            "v_exp_f32 v187, v187\n\t"
            "v_exp_f32 v204, v204\n\t"
            "v_exp_f32 v194, v194\n\t"
            "v_exp_f32 v195, v195\n\t"
            "v_exp_f32 v208, v208\n\t"
            "v_cvt_pk_bf16_f32 v184, v184, v185\n\t"
            "v_max3_f32 v232, v232, v228, v229\n\t"
            "v_max3_f32 v232, v232, v230, v231\n\t"
            "v_max3_f32 v232, v232, v200, v201\n\t"
            "v_max3_f32 v232, v232, v202, v203\n\t"
            "v_mov_b32 v233, v232\n\t"
            "v_cvt_pk_bf16_f32 v192, v192, v193\n\t"
            "v_cvt_pk_bf16_f32 v235, v235, 0\n\t"
            "v_permlane32_swap_b32 v232, v233\n\t"
            "v_max_f32 v232, v232, v233\n\t"
            "v_mov_b32 v233, v232\n\t"
            "v_cvt_pk_bf16_f32 v185, v186, v187\n\t"
            "v_cvt_pk_bf16_f32 v204, v204, 0\n\t"
            "v_permlane16_swap_b32 v232, v233\n\t"
            "v_max_f32 v232, v232, v233\n\t"
            "v_sub_f32 v196, v196, v232\n\t"
            "v_sub_f32 v197, v197, v232\n\t"
            "v_exp_f32 v196, v196\n\t"
            "v_exp_f32 v197, v197\n\t"
            "v_sub_f32 v198, v198, v232\n\t"
            "v_sub_f32 v199, v199, v232\n\t"
            "v_exp_f32 v198, v198\n\t"
            "v_exp_f32 v199, v199\n\t"
            "v_sub_f32 v228, v228, v232\n\t"
            "v_sub_f32 v229, v229, v232\n\t"
            "v_sub_f32 v230, v230, v232\n\t"
            "v_sub_f32 v231, v231, v232\n\t"
            "v_sub_f32 v200, v200, v232\n\t"
            "v_sub_f32 v201, v201, v232\n\t"
            "v_exp_f32 v228, v228\n\t"
            "v_exp_f32 v229, v229\n\t"
            "v_exp_f32 v230, v230\n\t"
            "v_exp_f32 v231, v231\n\t"
            "v_exp_f32 v200, v200\n\t"
            "v_exp_f32 v201, v201\n\t"
            "v_sub_f32 v202, v202, v232\n\t"
            "v_sub_f32 v203, v203, v232\n\t"
            "v_sub_f32 v212, v212, v232\n\t"
            "v_cvt_pk_bf16_f32 v196, v196, v197\n\t"
            "v_exp_f32 v202, v202\n\t"
            "v_exp_f32 v203, v203\n\t"
            "v_exp_f32 v212, v212\n\t"
            "v_cvt_pk_bf16_f32 v197, v198, v199\n\t"
            "v_cvt_pk_bf16_f32 v198, v228, v229\n\t"
            "v_cvt_pk_bf16_f32 v199, v230, v231\n\t"
            "v_cvt_pk_bf16_f32 v200, v200, v201\n\t"
            "v_cvt_pk_bf16_f32 v193, v194, v195\n\t"
            "v_cvt_pk_bf16_f32 v208, v208, 0\n\t"
            "v_cvt_pk_bf16_f32 v201, v202, v203\n\t"
            "v_cvt_pk_bf16_f32 v212, v212, 0\n\t"
            "v_cndmask_b32_e64 v235, 0, v235, s[66:67]\n\t"
            "v_cndmask_b32_e64 v186, 0, v204, s[58:59]\n\t"
            "v_mfma_f32_16x16x32_bf16 v[204:207], v[240:243], v[180:183], 0\n\t"
            "v_cndmask_b32_e64 v194, 0, v208, s[58:59]\n\t"
            "v_cndmask_b32_e64 v202, 0, v212, s[58:59]\n\t"
            "v_mfma_f32_16x16x32_bf16 v[208:211], v[240:243], v[188:191], 0\n\t"
            "ds_write_b16 v250, v235 offset:256\n\t"
            "v_mfma_f32_16x16x32_bf16 v[212:215], v[240:243], v[196:199], 0\n\t"
            "v_mov_b32 v187, 0\n\t"
            "v_mov_b32 v195, 0\n\t"
            "v_mov_b32 v203, 0\n\t"
            "v_mfma_f32_16x16x32_bf16 v[204:207], v[240:243], v[184:187], v[204:207]\n\t"
            "v_mfma_f32_16x16x32_bf16 v[208:211], v[240:243], v[192:195], v[208:211]\n\t"
            "v_mfma_f32_16x16x32_bf16 v[212:215], v[240:243], v[200:203], v[212:215]\n\t"
            "s_nop 5\n\t"
            "v_rcp_f32 v236, v204\n\t"
            "v_rcp_f32 v237, v208\n\t"
            "v_rcp_f32 v238, v212\n\t"
            "s_waitcnt lgkmcnt(0)\n\t"
            "s_nop 7\n\t"
            "s_branch .Lwa_ok%=\n\t"
            ".Lwa_done%=:\n\t"
            "s_setprio 2\n\t"
            "s_waitcnt lgkmcnt(0)\n\t"
            "s_nop 7\n\t"
            "ds_read_b64 v[156:157], v247\n\t"
            "v_add_u32 v235, s45, v248\n\t"
            "v_xor_b32 v234, s73, v254\n\t"
            "v_mov_b32 v159, 0\n\t"
            "v_mov_b32 v163, 0\n\t"
            "v_mov_b32 v167, 0\n\t"
            "ds_read_b128 v[168:171], v235\n\t"
            "ds_read_u16 v158, v234\n\t"
            "ds_read_b128 v[172:175], v235 offset:1536\n\t"
            "ds_read_b128 v[176:179], v235 offset:3072\n\t"
            "ds_read_b64 v[160:161], v247 offset:3072\n\t"
            "ds_read_u16 v162, v234 offset:256\n\t"
            "ds_read_b64 v[164:165], v247 offset:1536\n\t"
            "ds_read_u16 v166, v234 offset:128\n\t"
            "s_waitcnt lgkmcnt(7)\n\t"
            "v_mfma_f32_16x16x32_bf16 v[228:231], v[168:171], v[180:183], 0\n\t"
            "s_waitcnt lgkmcnt(6)\n\t"
            "v_mfma_f32_16x16x32_bf16 v[228:231], v[156:159], v[184:187], v[228:231]\n\t"
            "s_nop 7\n\t"
            "v_mul_f32 v228, v236, v228\n\t"
            "v_mul_f32 v229, v236, v229\n\t"
            "v_mul_f32 v230, v236, v230\n\t"
            "v_mul_f32 v231, v236, v231\n\t"
            "v_cvt_pk_bf16_f32 v228, v228, v229\n\t"
            "v_cvt_pk_bf16_f32 v229, v230, v231\n\t"
            "ds_write_b64 v245, v[228:229]\n\t"
            "s_waitcnt lgkmcnt(6)\n\t"
            "v_mfma_f32_16x16x32_bf16 v[228:231], v[172:175], v[180:183], 0\n\t"
            "s_waitcnt lgkmcnt(5)\n\t"
            "v_mfma_f32_16x16x32_bf16 v[180:183], v[176:179], v[180:183], 0\n\t"
            "s_waitcnt lgkmcnt(3)\n\t"
            "v_mfma_f32_16x16x32_bf16 v[180:183], v[160:163], v[184:187], v[180:183]\n\t"
            "s_waitcnt lgkmcnt(1)\n\t"
            "v_mfma_f32_16x16x32_bf16 v[228:231], v[164:167], v[184:187], v[228:231]\n\t"
            "v_mfma_f32_16x16x32_bf16 v[184:187], v[168:171], v[188:191], 0\n\t"
            "v_mfma_f32_16x16x32_bf16 v[184:187], v[156:159], v[192:195], v[184:187]\n\t"
            "s_nop 3\n\t"
            "v_mul_f32 v180, v236, v180\n\t"
            "v_mul_f32 v181, v236, v181\n\t"
            "v_mul_f32 v182, v236, v182\n\t"
            "v_mul_f32 v183, v236, v183\n\t"
            "v_cvt_pk_bf16_f32 v180, v180, v181\n\t"
            "v_cvt_pk_bf16_f32 v181, v182, v183\n\t"
            "v_mul_f32 v228, v236, v228\n\t"
            "v_mul_f32 v229, v236, v229\n\t"
            "v_mul_f32 v230, v236, v230\n\t"
            "v_mul_f32 v231, v236, v231\n\t"
            "v_cvt_pk_bf16_f32 v228, v228, v229\n\t"
            "v_cvt_pk_bf16_f32 v229, v230, v231\n\t"
            "v_mul_f32 v184, v237, v184\n\t"
            "v_mul_f32 v185, v237, v185\n\t"
            "v_mul_f32 v186, v237, v186\n\t"
            "v_mul_f32 v187, v237, v187\n\t"
            "v_cvt_pk_bf16_f32 v184, v184, v185\n\t"
            "v_cvt_pk_bf16_f32 v185, v186, v187\n\t"
            "ds_read_b64 v[186:187], v253 offset:352\n\t"
            "ds_write_b64 v245, v[180:181] offset:256\n\t"
            "v_mfma_f32_16x16x32_bf16 v[180:183], v[168:171], v[196:199], 0\n\t"
            "ds_write_b64 v245, v[228:229] offset:128\n\t"
            "v_mfma_f32_16x16x32_bf16 v[180:183], v[156:159], v[200:203], v[180:183]\n\t"
            "ds_write_b64 v245, v[184:185] offset:6144\n\t"
            "v_mfma_f32_16x16x32_bf16 v[228:231], v[172:175], v[188:191], 0\n\t"
            "ds_read_b64 v[184:185], v253 offset:320\n\t"
            "v_mfma_f32_16x16x32_bf16 v[228:231], v[164:167], v[192:195], v[228:231]\n\t"
            "v_mfma_f32_16x16x32_bf16 v[188:191], v[176:179], v[188:191], 0\n\t"
            "v_mfma_f32_16x16x32_bf16 v[188:191], v[160:163], v[192:195], v[188:191]\n\t"
            "s_nop 1\n\t"
            "v_mul_f32 v180, v238, v180\n\t"
            "v_mul_f32 v181, v238, v181\n\t"
            "v_mul_f32 v182, v238, v182\n\t"
            "v_mul_f32 v183, v238, v183\n\t"
            "v_cvt_pk_bf16_f32 v180, v180, v181\n\t"
            "v_cvt_pk_bf16_f32 v181, v182, v183\n\t"
            "v_mul_f32 v228, v237, v228\n\t"
            "v_mul_f32 v229, v237, v229\n\t"
            "v_mul_f32 v230, v237, v230\n\t"
            "v_mul_f32 v231, v237, v231\n\t"
            "v_cvt_pk_bf16_f32 v228, v228, v229\n\t"
            "v_cvt_pk_bf16_f32 v229, v230, v231\n\t"
            "v_mul_f32 v188, v237, v188\n\t"
            "v_mul_f32 v189, v237, v189\n\t"
            "v_mul_f32 v190, v237, v190\n\t"
            "v_mul_f32 v191, v237, v191\n\t"
            "v_cvt_pk_bf16_f32 v188, v188, v189\n\t"
            "v_cvt_pk_bf16_f32 v189, v190, v191\n\t"
            "ds_read_b64 v[182:183], v253 offset:288\n\t"
            "ds_write_b64 v245, v[180:181] offset:12288\n\t"
            "ds_read_b64 v[180:181], v253 offset:256\n\t"
            "ds_write_b64 v245, v[228:229] offset:6272\n\t"
            "v_mfma_f32_16x16x32_bf16 v[228:231], v[172:175], v[196:199], 0\n\t"
            "ds_write_b64 v245, v[188:189] offset:6400\n\t"
            "v_mfma_f32_16x16x32_bf16 v[228:231], v[164:167], v[200:203], v[228:231]\n\t"
            "v_mfma_f32_16x16x32_bf16 v[196:199], v[176:179], v[196:199], 0\n\t"
            "v_mfma_f32_16x16x32_bf16 v[196:199], v[160:163], v[200:203], v[196:199]\n\t"
            "s_nop 5\n\t"
            "v_mul_f32 v228, v238, v228\n\t"
            "s_waitcnt lgkmcnt(2)\n\t"
            "v_mfma_f32_16x16x32_bf16 v[168:171], v[168:171], v[180:183], 0\n\t"
            "v_mfma_f32_16x16x32_bf16 v[172:175], v[172:175], v[180:183], 0\n\t"
            "v_mfma_f32_16x16x32_bf16 v[176:179], v[176:179], v[180:183], 0\n\t"
            "v_mul_f32 v229, v238, v229\n\t"
            "v_mfma_f32_16x16x32_bf16 v[168:171], v[156:159], v[184:187], v[168:171]\n\t"
            "v_mul_f32 v230, v238, v230\n\t"
            "v_mul_f32 v231, v238, v231\n\t"
            "v_mfma_f32_16x16x32_bf16 v[172:175], v[164:167], v[184:187], v[172:175]\n\t"
            "v_cvt_pk_bf16_f32 v228, v228, v229\n\t"
            "v_cvt_pk_bf16_f32 v229, v230, v231\n\t"
            "v_mfma_f32_16x16x32_bf16 v[176:179], v[160:163], v[184:187], v[176:179]\n\t"
            "ds_write_b64 v245, v[228:229] offset:12416\n\t"
            "v_mfma_f32_16x16x32_bf16 v[228:231], v[240:243], v[180:183], 0\n\t"
            "v_mul_f32 v196, v238, v196\n\t"
            "v_mul_f32 v197, v238, v197\n\t"
            "v_mfma_f32_16x16x32_bf16 v[228:231], v[240:243], v[184:187], v[228:231]\n\t"
            "v_mul_f32 v198, v238, v198\n\t"
            "v_mul_f32 v199, v238, v199\n\t"
            "v_cvt_pk_bf16_f32 v196, v196, v197\n\t"
            "v_cvt_pk_bf16_f32 v197, v198, v199\n\t"
            "ds_write_b64 v245, v[196:197] offset:12544\n\t"
            "s_nop 2\n\t"
            "v_rcp_f32 v229, v228\n\t"
            "s_nop 0\n\t"
            "v_mul_f32 v168, v229, v168\n\t"
            "v_mul_f32 v169, v229, v169\n\t"
            "v_mul_f32 v172, v229, v172\n\t"
            "v_mul_f32 v173, v229, v173\n\t"
            "v_mul_f32 v176, v229, v176\n\t"
            "v_mul_f32 v177, v229, v177\n\t"
            "v_mul_f32 v170, v229, v170\n\t"
            "v_mul_f32 v171, v229, v171\n\t"
            "v_cvt_pk_bf16_f32 v168, v168, v169\n\t"
            "v_mul_f32 v174, v229, v174\n\t"
            "v_mul_f32 v175, v229, v175\n\t"
            "v_cvt_pk_bf16_f32 v172, v172, v173\n\t"
            "v_mul_f32 v178, v229, v178\n\t"
            "v_mul_f32 v179, v229, v179\n\t"
            "v_cvt_pk_bf16_f32 v176, v176, v177\n\t"
            "v_xor_b32 v230, s73, v255\n\t"
            "v_cvt_pk_bf16_f32 v169, v170, v171\n\t"
            "v_cvt_pk_bf16_f32 v173, v174, v175\n\t"
            "v_cvt_pk_bf16_f32 v177, v178, v179\n\t"
            "s_mov_b64 exec, s[68:69]\n\t"
            "ds_write_b64 v230, v[168:169]\n\t"
            "ds_write_b64 v230, v[172:173] offset:128\n\t"
            "ds_write_b64 v230, v[176:177] offset:256\n\t"
            "s_mov_b64 exec, -1\n\t"
            "s_waitcnt lgkmcnt(0)\n\t"
            "v_mov_b32 v144, s42\n\t"
            "v_bfrev_b32 v145, -2\n\t"
            "ds_write_b32 v144, v145\n\t"
            "s_waitcnt lgkmcnt(0)\n\t"
            "s_nop 7\n\t"
            : "+{v[232:235]}"(pl), "+{v249}"(aRSw_),
              "+{s44}"(s44), "+{s48}"(s48), "+{s70}"(s70), "+{s72}"(s72), "+{s73}"(s73), "+{s76}"(s76), "+{s77}"(s77)
            : "{v244}"(pE_), "{v245}"(aO_), "{v246}"(aZ_), "{v247}"(aVB_), "{v248}"(pST_), "{v250}"(aPLw_), "{v251}"(aRSr_), "{v252}"(g16_), "{v253}"(aPLr_), "{v254}"(loVL_), "{v255}"(loOL_), "{v[240:243]}"(ones4), "{s47}"((ldsb + X_OFF) ^ (ldsb + X1_OFF)),
              "{s[58:59]}"(0xFFFFull), "{s[60:61]}"(0xFFFF0000ull), "{s[62:63]}"(0xFFFF00000000ull), "{s[64:65]}"(0xFFFF000000000000ull),
              "{s[66:67]}"(0x1FFFFFFFFFFFFull), "{s[68:69]}"(0x0001000100010001ull), "{s[26:27]}"(0x1111111111111111ull), "{s23}"(64.f), "{s57}"(RS_OFF + 768 + (4 + (w >> 1)) * 192 - RS_BASE), "{s45}"(ldsb + V_OFF + w * 4608),
              "{s[28:31]}"(wr), "{s33}"(wave * 36864), "{s46}"(ldsb + PG_OFF), "{s42}"(ldsb + PG_OFF + w * 4), "{s56}"(ldsb + O_OFF), "{s[36:39]}"(tr), "{s41}"(to), "{s40}"(ldsb + X_OFF + 18432), "{s34}"(SH_OFF + bx * 12288 + w * 3072), "{s35}"(ldsb + O_OFF + w * 3072),
              "{s[50:51]}"(0x2222222222222222ull), "{s[52:53]}"(0x4444444444444444ull), "{s[54:55]}"(0x8888888888888888ull)
            : "s24", "s25", "v0", "v1", "v2", "v3", "v4", "v5", "v6", "v7", "v8", "v9", "v10", "v11", "v12", "v13", "v14", "v15", "v16", "v17", "v18", "v19", "v20", "v21", "v22", "v23", "v24", "v25", "v26", "v27", "v28", "v29", "v30", "v31", "v32", "v33", "v34", "v35", "v36", "v37", "v38", "v39", "v40", "v41", "v42", "v43", "v44", "v45", "v46", "v47", "v48", "v49", "v50", "v51", "v52", "v53", "v54", "v55", "v56", "v57", "v58", "v59", "v60", "v61", "v62", "v63", "v64", "v65", "v66", "v67", "v68", "v69", "v70", "v71", "v72", "v73", "v74", "v75", "v76", "v77", "v78", "v79", "v80", "v81", "v82", "v83", "v84", "v85", "v86", "v87", "v88", "v89", "v90", "v91", "v92", "v93", "v94", "v95", "v96", "v97", "v98", "v99", "v100", "v101", "v102", "v103", "v104", "v105", "v106", "v107", "v108", "v109", "v110", "v111", "v112", "v113", "v114", "v115", "v116", "v117", "v118", "v119", "v120", "v121", "v122", "v123", "v124", "v125", "v126", "v127", "v128", "v129", "v130", "v131", "v132", "v133", "v134", "v135", "v136", "v137", "v138", "v139", "v140", "v141", "v142", "v143", "v144", "v145", "v146", "v147", "v148", "v149", "v150", "v151", "v152", "v153", "v154", "v155", "v156", "v157", "v158", "v159", "v160", "v161", "v162", "v163", "v164", "v165", "v166", "v167", "v168", "v169", "v170", "v171", "v172", "v173", "v174", "v175", "v176", "v177", "v178", "v179", "v180", "v181", "v182", "v183", "v184", "v185", "v186", "v187", "v188", "v189", "v190", "v191", "v192", "v193", "v194", "v195", "v196", "v197", "v198", "v199", "v200", "v201", "v202", "v203", "v204", "v205", "v206", "v207", "v208", "v209", "v210", "v211", "v212", "v213", "v214", "v215", "v216", "v217", "v218", "v219", "v220", "v221", "v222", "v223", "v224", "v225", "v226", "v227", "v228", "v229", "v230", "v231", "v236", "v237", "v238", "v239", "s49", "vcc", "scc", "m0", "memory");
        }
        asm volatile("wk_code_end:");

        if (q0 == 40 && wave == 0) {
#pragma unroll 1
            for (int it = 0; it < SPIN_MAX && __hip_atomic_load((gu32*)((char*)wf + CN_OFF), RLX_AGENT) != (unsigned)GRID; ++it) __builtin_amdgcn_s_sleep(8);
            __hip_atomic_store((gu32*)((char*)wf + EP_OFF), __hip_atomic_load((gu32*)((char*)wf + EP_OFF), RLX_AGENT) + 1u, RLX_AGENT);
        }
    } else {
        ROLE_SETUP
        {
            LANE_SETUP
            const bool isV = wave < 6;
            CONV_CONSTS(0)
            const unsigned aCA_ = ldsb + CT_OFF + lane * 16, rsb = ldsb + RS_BASE, pE = fE - X_OFF;
            const unsigned aRSw = (lane < 48 ? rsb : ldsb + RS_OFF + 2304 - 192) + lane * 4, aRSr = rsb + (lane & 48);
            unsigned pr[3];
#pragma unroll
            for (int k = 0; k < 3; ++k) {
                const int c8 = 12 * (w - 2) + 4 * k + g;
                const int ws = c8 / 6, r6 = c8 - 6 * ws;
                pr[k] = l15 * 384 + (r6 >> 1) * 128 + (((2 * ws + (r6 & 1)) ^ sw) << 4);
            }
            unsigned r242 = isV ? pE : ldsb + O_OFF + pE, r243 = isV ? ldsb + V_OFF + w * 9216 + lane * 16 : (ldsb + O_OFF + pE) ^ 64;
            u32x4 r244 = {isV ? ldsb + V_OFF + w * 9216 + 1024 + lane * 8 : pr[0], isV ? aRSr : pr[1], isV ? aRSw : pr[2], (unsigned)lane * 16};
            const int hd = isV ? 2 * w : 2 * w - 3;
            const unsigned vq = ldsb + OL_OFF + l15 * 384 + (((2 * hd + (g >> 1)) ^ sw) << 4) + 8 * (g & 1);
            u32x4 f1 = {16 + l15 < cnt ? vq + 6144 : ldsb + OL_OFF + 22 * 384 + g * 8, pE, ldsb + V_OFF + hd * 4608 + lane * 16, ldsb + V_OFF + hd * 4608 + 1024 + lane * 8};
            u32x4 f2 = {aRSw, aRSr, rsb + l15 * 4, 0u};
            const unsigned lB = ldsb + LB_OFF(l15 & 7) + (g >> 1) * 1536 + (g & 1) * 768 + 96 * w;
            const unsigned long long ob = (unsigned long long)out, xb = (unsigned long long)((const char*)x - XBIAS), wb = (unsigned long long)wf;
            const u32x4 orr = {(unsigned)ob, (unsigned)(ob >> 32) & 0xffffu, (unsigned)(IMG * IMG * CH * 2), 0x00020000u};
            const u32x4 xr = {(unsigned)xb, (unsigned)(xb >> 32) & 0xffffu, (unsigned)(IMG * IMG * CH * 2 + XBIAS), 0x00020000u};
            const u32x4 wr = {(unsigned)wb, (unsigned)(wb >> 32) & 0xffffu, (unsigned)WS_BYTES, 0x00020000u};
            unsigned s45 = OOB, s46 = OOB, s47 = (en0 & 0x7ffffffu) + 96 * w;
            unsigned s48 = ldsb + X1_OFF, s67 = ldsb + X_OFF, s76 = ldsb + WL_OFF + 4, s56 = en0 >> 30;
            unsigned s21 = SH_OFF + bx * 12288, s44 = ldsb + PG_OFF + w * 4, s70 = isV ? wave * 36864 : (wave - 2) * 36864 + 18432;
            unsigned long long xp = 0, sp = 0;
#pragma unroll
            for (int v = 0; v < 4; ++v) {
                const unsigned r = (CROW >> (8 * v)) & 0xFF;
                xp |= (unsigned long long)XPAT(r) << (16 * v);
                sp |= (unsigned long long)SPAT(r) << (16 * v);
            }
#define B_VIN "{v250}"(pXw_), "{v251}"(pSTw_), "{v252}"(aCA_), "{v253}"(aB_), "{v254}"(aA_), "{v255}"(idx_), "{v[238:241]}"(sel4)
#define B_SIN "{s[40:43]}"(orr), "{s[72:75]}"(xr), "{s39}"(0x401fffffu), "{s[28:31]}"(wr), "{s33}"(wave * 36864), "{s78}"(96 * w), \
              "{s27}"(96 * w - 384), "{s[50:51]}"(xp), "{s[52:53]}"(sp), "{s54}"(CLANE), "{s55}"(CROW & 0x0F0F0F0Fu), "{s[34:35]}"(0x0001000100010001ull), \
              "{s[60:61]}"(0x2222222222222222ull), "{s[62:63]}"(0x4444444444444444ull), "{s[64:65]}"(0x8888888888888888ull), \
              "{s77}"(w == 2 ? 768 : 6 * ROWBYTES + 768), "{s25}"(w == 2 ? 3 * ROWBYTES : OOB), "{s32}"(ldsb + X_OFF + 18432), \
              "{s24}"(ldsb + WL_OFF + 12 + 4 * (cnt - 5)), "{s66}"(ldsb + w * 8192), "{s71}"((ldsb + X_OFF) ^ (ldsb + X1_OFF)), \
              "{s23}"(ldsb + WL_OFF), "{s22}"(ldsb + O_OFF), \
              "{s20}"(6 * ROWBYTES + 2304 + 192 * (w & 1)), "{s19}"(2048 + (w & 1) * 1024)
            asm volatile("" :: B_VIN, "{v242}"(r242), "{v243}"(r243), "{v[244:247]}"(r244), "{v[228:231]}"(f1), "{v[232:235]}"(f2), "v"(lB), "v"(vq));
            asm volatile("" :: B_SIN);
            asm volatile("s_waitcnt vmcnt(9)" ::: "memory");
            unsigned lm = *(const unsigned*)(smem + TP_OFF + 4096 + w * 256 + ((l15 & 7) * 4 + g) * 4);
            lm = l15 < 8 ? lm : OOB + 47;
            const int row = lm & 63;
            u32x4 f0 = {XPAT(row) + (unsigned)(O_OFF - X_OFF), SPAT(row) + (unsigned)(STL_OFF - X_OFF - 18816), lB, vq};
            asm volatile(
            "s_setprio 3\n\t"
            "s_waitcnt lgkmcnt(0)\n\t"
            "s_barrier\n\t"
            "s_nop 7\n\t"
            "ds_read_b128 v[168:171], v254\n\t"
            "v_mov_b64 v[176:177], 0\n\t"
            "v_mov_b64 v[178:179], 0\n\t"
            "v_mov_b64 v[180:181], 0\n\t"
            "v_mov_b64 v[182:183], 0\n\t"
            "v_mov_b64 v[184:185], 0\n\t"
            "v_mov_b64 v[186:187], 0\n\t"
            "v_mov_b64 v[188:189], 0\n\t"
            "v_mov_b64 v[190:191], 0\n\t"
            "v_add_u32 v236, s67, v224\n\t"
            "v_xor_b32 v237, 16, v236\n\t"
            "v_add_u32 v248, s67, v225\n\t"
            "ds_read_b64 v[144:145], v226\n\t"
            "ds_read_b64 v[146:147], v226 offset:384\n\t"
            "ds_read_b64 v[148:149], v226 offset:3072\n\t"
            "ds_read_b64 v[150:151], v226 offset:3456\n\t"
            "ds_read_b128 v[172:175], v254 offset:64\n\t"
            "ds_read_b64 v[152:153], v226 offset:8\n\t"
            "ds_read_b64 v[154:155], v226 offset:392\n\t"
            "ds_read_b64 v[156:157], v226 offset:3080\n\t"
            "ds_read_b64 v[158:159], v226 offset:3464\n\t"
            "ds_read_b64 v[160:161], v226 offset:16\n\t"
            "ds_read_b64 v[162:163], v226 offset:400\n\t"
            "ds_read_b64 v[164:165], v226 offset:3088\n\t"
            "ds_read_b64 v[166:167], v226 offset:3472\n\t"
            "s_waitcnt lgkmcnt(9)\n\t"
            "v_smfmac_f32_16x16x64_bf16 v[176:179], v[168:171], v[144:151], v255\n\t"
            "ds_read_b128 v[168:171], v254 offset:128\n\t"
            "ds_read_b64 v[144:145], v226 offset:24\n\t"
            "ds_read_b64 v[146:147], v226 offset:408\n\t"
            "ds_read_b64 v[148:149], v226 offset:3096\n\t"
            "s_waitcnt lgkmcnt(8)\n\t"
            "v_smfmac_f32_16x16x64_bf16 v[180:183], v[172:175], v[152:159], v255\n\t"
            "ds_read_b64 v[150:151], v226 offset:3480\n\t"
            "ds_read_b128 v[172:175], v254 offset:192\n\t"
            "ds_read_b64 v[152:153], v226 offset:32\n\t"
            "v_cvt_pk_bf16_f32 v176, v176, v177\n\t"
            "v_cvt_pk_bf16_f32 v177, v178, v179\n\t"
            "s_nop 1\n\t"
            "v_mfma_f32_4x4x4_16b_bf16 v[196:199], v[176:177], v[176:177], 0\n\t"
            "ds_read_b64 v[154:155], v226 offset:416\n\t"
            "ds_read_b64 v[156:157], v226 offset:3104\n\t"
            "v_cvt_pk_bf16_f32 v178, v180, v181\n\t"
            "v_cvt_pk_bf16_f32 v179, v182, v183\n\t"
            "v_mov_b64 v[180:181], 0\n\t"
            "v_mov_b64 v[182:183], 0\n\t"
            "v_mfma_f32_16x16x32_bf16 v[192:195], v[238:241], v[176:179], 0\n\t"
            "ds_read_b64 v[158:159], v226 offset:3488\n\t"
            "s_waitcnt lgkmcnt(9)\n\t"
            "v_smfmac_f32_16x16x64_bf16 v[184:187], v[168:171], v[160:167], v255\n\t"
            "ds_read_b128 v[168:171], v254 offset:256\n\t"
            "v_mfma_f32_4x4x4_16b_bf16 v[196:199], v[178:179], v[178:179], v[196:199]\n\t"
            "ds_read_b64 v[160:161], v226 offset:40\n\t"
            "ds_read_b64 v[162:163], v226 offset:424\n\t"
            "ds_read_b64 v[164:165], v226 offset:3112\n\t"
            "ds_read_b64 v[166:167], v226 offset:3496\n\t"
            "s_waitcnt lgkmcnt(9)\n\t"
            "v_smfmac_f32_16x16x64_bf16 v[188:191], v[172:175], v[144:151], v255\n\t"
            "ds_read_b128 v[172:175], v254 offset:320\n\t"
            "v_cvt_pk_bf16_f32 v184, v184, v185\n\t"
            "v_cvt_pk_bf16_f32 v185, v186, v187\n\t"
            "s_nop 1\n\t"
            "v_mfma_f32_4x4x4_16b_bf16 v[196:199], v[184:185], v[184:185], v[196:199]\n\t"
            "ds_read_b64 v[144:145], v226 offset:48\n\t"
            "ds_read_b64 v[146:147], v226 offset:432\n\t"
            "ds_read_b64 v[148:149], v226 offset:3120\n\t"
            "ds_read_b64 v[150:151], v226 offset:3504\n\t"
            "v_cvt_pk_bf16_f32 v186, v188, v189\n\t"
            "v_cvt_pk_bf16_f32 v187, v190, v191\n\t"
            "v_mov_b64 v[188:189], 0\n\t"
            "v_mov_b64 v[190:191], 0\n\t"
            "v_mfma_f32_16x16x32_bf16 v[192:195], v[238:241], v[184:187], v[192:195]\n\t"
            "ds_write_b128 v236, v[176:179]\n\t"
            "v_mfma_f32_4x4x4_16b_bf16 v[196:199], v[186:187], v[186:187], v[196:199]\n\t"
            "v_mov_b64 v[176:177], 0\n\t"
            "v_mov_b64 v[178:179], 0\n\t"
            "s_waitcnt lgkmcnt(10)\n\t"
            "s_nop 0\n\t"
            "v_smfmac_f32_16x16x64_bf16 v[176:179], v[168:171], v[152:159], v255\n\t"
            "ds_read_b128 v[168:171], v254 offset:384\n\t"
            "s_waitcnt lgkmcnt(6)\n\t"
            "v_smfmac_f32_16x16x64_bf16 v[180:183], v[172:175], v[160:167], v255\n\t"
            "ds_read_b64 v[152:153], v226 offset:56\n\t"
            "ds_read_b64 v[154:155], v226 offset:440\n\t"
            "ds_read_b64 v[156:157], v226 offset:3128\n\t"
            "ds_read_b64 v[158:159], v226 offset:3512\n\t"
            "ds_read_b128 v[172:175], v254 offset:448\n\t"
            "ds_read_b64 v[160:161], v226 offset:64\n\t"
            "v_cvt_pk_bf16_f32 v176, v176, v177\n\t"
            "v_cvt_pk_bf16_f32 v177, v178, v179\n\t"
            "v_cvt_pk_bf16_f32 v178, v180, v181\n\t"
            "v_cvt_pk_bf16_f32 v179, v182, v183\n\t"
            "v_mfma_f32_4x4x4_16b_bf16 v[196:199], v[176:177], v[176:177], v[196:199]\n\t"
            "ds_read_b64 v[162:163], v226 offset:448\n\t"
            "v_mfma_f32_16x16x32_bf16 v[192:195], v[238:241], v[176:179], v[192:195]\n\t"
            "ds_read_b64 v[164:165], v226 offset:3136\n\t"
            "v_mov_b64 v[180:181], 0\n\t"
            "v_mfma_f32_4x4x4_16b_bf16 v[196:199], v[178:179], v[178:179], v[196:199]\n\t"
            "ds_read_b64 v[166:167], v226 offset:3520\n\t"
            "v_mov_b64 v[182:183], 0\n\t"
            "s_waitcnt lgkmcnt(14)\n\t"
            "ds_write_b128 v237, v[184:187]\n\t"
            "v_mov_b64 v[184:185], 0\n\t"
            "v_mov_b64 v[186:187], 0\n\t"
            "s_waitcnt lgkmcnt(10)\n\t"
            "s_nop 0\n\t"
            "v_smfmac_f32_16x16x64_bf16 v[184:187], v[168:171], v[144:151], v255\n\t"
            "ds_read_b128 v[168:171], v254 offset:512\n\t"
            "s_waitcnt lgkmcnt(6)\n\t"
            "v_smfmac_f32_16x16x64_bf16 v[188:191], v[172:175], v[152:159], v255\n\t"
            "ds_read_b64 v[144:145], v226 offset:72\n\t"
            "ds_read_b64 v[146:147], v226 offset:456\n\t"
            "ds_read_b64 v[148:149], v226 offset:3144\n\t"
            "ds_read_b64 v[150:151], v226 offset:3528\n\t"
            "ds_read_b128 v[172:175], v254 offset:576\n\t"
            "ds_read_b64 v[152:153], v226 offset:80\n\t"
            "v_cvt_pk_bf16_f32 v184, v184, v185\n\t"
            "v_cvt_pk_bf16_f32 v185, v186, v187\n\t"
            "v_cvt_pk_bf16_f32 v186, v188, v189\n\t"
            "v_cvt_pk_bf16_f32 v187, v190, v191\n\t"
            "v_mfma_f32_4x4x4_16b_bf16 v[196:199], v[184:185], v[184:185], v[196:199]\n\t"
            "ds_read_b64 v[154:155], v226 offset:464\n\t"
            "v_mfma_f32_16x16x32_bf16 v[192:195], v[238:241], v[184:187], v[192:195]\n\t"
            "ds_read_b64 v[156:157], v226 offset:3152\n\t"
            "v_mov_b64 v[188:189], 0\n\t"
            "v_mfma_f32_4x4x4_16b_bf16 v[196:199], v[186:187], v[186:187], v[196:199]\n\t"
            "ds_read_b64 v[158:159], v226 offset:3536\n\t"
            "v_mov_b64 v[190:191], 0\n\t"
            "s_waitcnt lgkmcnt(14)\n\t"
            "ds_write_b128 v236, v[176:179] offset:128\n\t"
            "v_mov_b64 v[176:177], 0\n\t"
            "v_mov_b64 v[178:179], 0\n\t"
            "s_waitcnt lgkmcnt(10)\n\t"
            "s_nop 0\n\t"
            "v_smfmac_f32_16x16x64_bf16 v[176:179], v[168:171], v[160:167], v255\n\t"
            "ds_read_b128 v[168:171], v254 offset:640\n\t"
            "s_waitcnt lgkmcnt(6)\n\t"
            "v_smfmac_f32_16x16x64_bf16 v[180:183], v[172:175], v[144:151], v255\n\t"
            "ds_read_b64 v[160:161], v226 offset:88\n\t"
            "ds_read_b64 v[162:163], v226 offset:472\n\t"
            "ds_read_b64 v[164:165], v226 offset:3160\n\t"
            "ds_read_b64 v[166:167], v226 offset:3544\n\t"
            "ds_read_b128 v[172:175], v254 offset:704\n\t"
            "ds_write_b128 v237, v[184:187] offset:128\n\t"
            "v_mov_b64 v[184:185], 0\n\t"
            "v_mov_b64 v[186:187], 0\n\t"
            "v_cvt_pk_bf16_f32 v176, v176, v177\n\t"
            "v_cvt_pk_bf16_f32 v177, v178, v179\n\t"
            "v_cvt_pk_bf16_f32 v178, v180, v181\n\t"
            "v_cvt_pk_bf16_f32 v179, v182, v183\n\t"
            "v_mfma_f32_4x4x4_16b_bf16 v[196:199], v[176:177], v[176:177], v[196:199]\n\t"
            "ds_write_b128 v236, v[176:179] offset:256\n\t"
            "s_waitcnt lgkmcnt(7)\n\t"
            "v_smfmac_f32_16x16x64_bf16 v[184:187], v[168:171], v[152:159], v255\n\t"
            "v_mfma_f32_4x4x4_16b_bf16 v[196:199], v[178:179], v[178:179], v[196:199]\n\t"
            "v_mfma_f32_16x16x32_bf16 v[192:195], v[238:241], v[176:179], v[192:195]\n\t"
            "s_waitcnt lgkmcnt(2)\n\t"
            "v_smfmac_f32_16x16x64_bf16 v[188:191], v[172:175], v[160:167], v255\n\t"
            "s_nop 3\n\t"
            "v_cvt_pk_bf16_f32 v184, v184, v185\n\t"
            "v_cvt_pk_bf16_f32 v185, v186, v187\n\t"
            "s_nop 1\n\t"
            "v_mfma_f32_4x4x4_16b_bf16 v[196:199], v[184:185], v[184:185], v[196:199]\n\t"
            "v_cvt_pk_bf16_f32 v186, v188, v189\n\t"
            "v_cvt_pk_bf16_f32 v187, v190, v191\n\t"
            "s_nop 1\n\t"
            "v_mfma_f32_4x4x4_16b_bf16 v[196:199], v[186:187], v[186:187], v[196:199]\n\t"
            "ds_write_b128 v237, v[184:187] offset:256\n\t"
            "v_mfma_f32_16x16x32_bf16 v[192:195], v[238:241], v[184:187], v[192:195]\n\t"
            "s_nop 7\n\t"
            "v_cndmask_b32_e64 v193, v196, v197, s[60:61]\n\t"
            "v_cndmask_b32_e64 v193, v193, v198, s[62:63]\n\t"
            "v_cndmask_b32_e64 v193, v193, v199, s[64:65]\n\t"
            "ds_write_b64 v248, v[192:193] offset:18816\n\t"
            "s_waitcnt lgkmcnt(0)\n\t"
            "s_nop 7\n\t"
            "v_mov_b32 v144, s44\n\t"
            "v_mov_b32 v145, 0x0fffffff\n\t"
            "ds_write_b32 v144, v145\n\t"
            "s_nop 7\n\t"
            "ds_read_b128 v[168:171], v254\n\t"
            "s_waitcnt vmcnt(0)\n\t"
            "ds_read_b64 v[144:145], v253\n\t"
            "v_mov_b64 v[176:177], 0\n\t"
            "v_mov_b64 v[178:179], 0\n\t"
            "v_mov_b64 v[180:181], 0\n\t"
            "v_mov_b64 v[182:183], 0\n\t"
            "v_mov_b32 v249, s76\n\t"
            "v_mov_b64 v[184:185], 0\n\t"
            "v_mov_b64 v[186:187], 0\n\t"
            "v_mov_b64 v[188:189], 0\n\t"
            "v_mov_b64 v[190:191], 0\n\t"
            "v_add_u32 v236, s67, v250\n\t"
            "v_add_u32 v248, s67, v251\n\t"
            "ds_read_b64 v[146:147], v253 offset:96\n\t"
            "ds_read_b64 v[148:149], v253 offset:1744\n\t"
            "ds_read_b64 v[150:151], v253 offset:1840\n\t"
            "ds_read_b128 v[172:175], v254 offset:64\n\t"
            "ds_read_b64 v[152:153], v253 offset:8\n\t"
            "ds_read_b64 v[154:155], v253 offset:104\n\t"
            "ds_read_b64 v[156:157], v253 offset:1752\n\t"
            "ds_read_b64 v[158:159], v253 offset:1848\n\t"
            "ds_read_b64 v[160:161], v253 offset:16\n\t"
            "ds_read_b64 v[162:163], v253 offset:112\n\t"
            "ds_read_b64 v[164:165], v253 offset:1760\n\t"
            "ds_read_b64 v[166:167], v253 offset:1856\n\t"
            "ds_read_b32 v249, v249\n\t"
            "s_waitcnt lgkmcnt(10)\n\t"
            "v_smfmac_f32_16x16x64_bf16 v[176:179], v[168:171], v[144:151], v255\n\t"
            "ds_read_b128 v[168:171], v254 offset:128\n\t"
            "ds_read_b64 v[144:145], v253 offset:24\n\t"
            "ds_read_b64 v[146:147], v253 offset:120\n\t"
            "ds_read_b64 v[148:149], v253 offset:1768\n\t"
            "s_waitcnt lgkmcnt(9)\n\t"
            "v_smfmac_f32_16x16x64_bf16 v[180:183], v[172:175], v[152:159], v255\n\t"
            "ds_read_b64 v[150:151], v253 offset:1864\n\t"
            "ds_read_b128 v[172:175], v254 offset:192\n\t"
            "ds_read_b64 v[152:153], v253 offset:32\n\t"
            "v_cvt_pk_bf16_f32 v176, v176, v177\n\t"
            "v_cvt_pk_bf16_f32 v177, v178, v179\n\t"
            "s_waitcnt lgkmcnt(7)\n\t"
            "v_readfirstlane_b32 s26, v249\n\t"
            "v_mfma_f32_4x4x4_16b_bf16 v[196:199], v[176:177], v[176:177], 0\n\t"
            "ds_read_b64 v[154:155], v253 offset:128\n\t"
            "s_and_b32 s68, s26, 0x7ffffff\n\t"
            "s_add_u32 s68, s68, s78\n\t"
            "s_and_b32 s69, s26, 0x38000000\n\t"
            "s_or_b32 s69, s69, s39\n\t"
            "s_bitcmp1_b32 s26, 27\n\t"
            "s_cselect_b32 s79, 2.0, s68\n\t"
            "ds_read_b64 v[156:157], v253 offset:1776\n\t"
            "s_and_b32 s57, s56, 3\n\t"
            "s_lshl_b32 s58, s57, 3\n\t"
            "s_lshr_b32 m0, s54, s58\n\t"
            "s_waitcnt lgkmcnt(8)\n\t"
            "v_smfmac_f32_16x16x64_bf16 v[184:187], v[168:171], v[160:167], v255\n\t"
            "s_lshl_b32 s58, s57, 4\n\t"
            "s_lshr_b64 s[36:37], s[50:51], s58\n\t"
            "s_and_b32 s57, s36, 0xffff\n\t"
            "s_add_u32 s57, s57, s67\n\t"
            "ds_read_b64 v[158:159], v253 offset:1872\n\t"
            "s_lshr_b64 s[36:37], s[52:53], s58\n\t"
            "s_and_b32 s58, s36, 0xffff\n\t"
            "s_add_u32 s58, s58, s67\n\t"
            "ds_read_b128 v[168:171], v254 offset:256\n\t"
            "s_and_b32 s59, s50, 0xffff\n\t"
            "s_add_u32 s59, s59, s67\n\t"
            "s_and_b32 s38, s52, 0xffff\n\t"
            "s_add_u32 s38, s38, s67\n\t"
            "s_waitcnt lgkmcnt(5)\n\t"
            "v_smfmac_f32_16x16x64_bf16 v[188:191], v[172:175], v[144:151], v255\n\t"
            "ds_read_b64 v[160:161], v253 offset:40\n\t"
            "v_writelane_b32 v236, s59, m0\n\t"
            "v_writelane_b32 v236, s57, 63\n\t"
            "v_cvt_pk_bf16_f32 v178, v180, v181\n\t"
            "v_cvt_pk_bf16_f32 v179, v182, v183\n\t"
            "v_mov_b64 v[180:181], 0\n\t"
            "v_mov_b64 v[182:183], 0\n\t"
            "v_mfma_f32_16x16x32_bf16 v[192:195], v[238:241], v[176:179], 0\n\t"
            "ds_read_b64 v[162:163], v253 offset:136\n\t"
            "v_cvt_pk_bf16_f32 v184, v184, v185\n\t"
            "v_mfma_f32_4x4x4_16b_bf16 v[196:199], v[178:179], v[178:179], v[196:199]\n\t"
            "ds_read_b64 v[164:165], v253 offset:1784\n\t"
            "v_cvt_pk_bf16_f32 v185, v186, v187\n\t"
            "v_xor_b32 v237, 16, v236\n\t"
            "v_cvt_pk_bf16_f32 v186, v188, v189\n\t"
            "v_mfma_f32_4x4x4_16b_bf16 v[196:199], v[184:185], v[184:185], v[196:199]\n\t"
            "ds_read_b64 v[166:167], v253 offset:1880\n\t"
            "v_cvt_pk_bf16_f32 v187, v190, v191\n\t"
            "v_mov_b64 v[188:189], 0\n\t"
            "v_mov_b64 v[190:191], 0\n\t"
            "v_mfma_f32_16x16x32_bf16 v[192:195], v[238:241], v[184:187], v[192:195]\n\t"
            "ds_read_b128 v[172:175], v254 offset:320\n\t"
            "v_writelane_b32 v248, s38, m0\n\t"
            "v_mfma_f32_4x4x4_16b_bf16 v[196:199], v[186:187], v[186:187], v[196:199]\n\t"
            "ds_read_b64 v[144:145], v253 offset:48\n\t"
            "v_writelane_b32 v248, s58, 63\n\t"
            "ds_read_b64 v[146:147], v253 offset:144\n\t"
            "ds_read_b64 v[148:149], v253 offset:1792\n\t"
            "ds_read_b64 v[150:151], v253 offset:1888\n\t"
            "ds_write_b128 v236, v[176:179]\n\t"
            "v_mov_b64 v[176:177], 0\n\t"
            "v_mov_b64 v[178:179], 0\n\t"
            "s_waitcnt lgkmcnt(10)\n\t"
            "s_nop 0\n\t"
            "v_smfmac_f32_16x16x64_bf16 v[176:179], v[168:171], v[152:159], v255\n\t"
            "ds_read_b128 v[168:171], v254 offset:384\n\t"
            "ds_read_b64 v[152:153], v253 offset:56\n\t"
            "ds_read_b64 v[154:155], v253 offset:152\n\t"
            "s_waitcnt lgkmcnt(8)\n\t"
            "v_smfmac_f32_16x16x64_bf16 v[180:183], v[172:175], v[160:167], v255\n\t"
            "ds_read_b64 v[156:157], v253 offset:1800\n\t"
            "ds_read_b64 v[158:159], v253 offset:1896\n\t"
            "ds_read_b128 v[172:175], v254 offset:448\n\t"
            "ds_read_b64 v[160:161], v253 offset:64\n\t"
            "v_cvt_pk_bf16_f32 v176, v176, v177\n\t"
            "v_cvt_pk_bf16_f32 v177, v178, v179\n\t"
            "s_nop 1\n\t"
            "v_mfma_f32_4x4x4_16b_bf16 v[196:199], v[176:177], v[176:177], v[196:199]\n\t"
            "ds_read_b64 v[162:163], v253 offset:160\n\t"
            "v_cvt_pk_bf16_f32 v178, v180, v181\n\t"
            "v_cvt_pk_bf16_f32 v179, v182, v183\n\t"
            "v_mov_b64 v[180:181], 0\n\t"
            "v_mov_b64 v[182:183], 0\n\t"
            "v_mfma_f32_16x16x32_bf16 v[192:195], v[238:241], v[176:179], v[192:195]\n\t"
            "ds_read_b64 v[164:165], v253 offset:1808\n\t"
            "v_mfma_f32_4x4x4_16b_bf16 v[196:199], v[178:179], v[178:179], v[196:199]\n\t"
            "ds_read_b64 v[166:167], v253 offset:1904\n\t"
            "s_waitcnt lgkmcnt(14)\n\t"
            "ds_write_b128 v237, v[184:187]\n\t"
            "v_mov_b64 v[184:185], 0\n\t"
            "v_mov_b64 v[186:187], 0\n\t"
            "s_waitcnt lgkmcnt(10)\n\t"
            "s_nop 0\n\t"
            "v_smfmac_f32_16x16x64_bf16 v[184:187], v[168:171], v[144:151], v255\n\t"
            "ds_read_b128 v[168:171], v254 offset:512\n\t"
            "s_waitcnt lgkmcnt(6)\n\t"
            "v_smfmac_f32_16x16x64_bf16 v[188:191], v[172:175], v[152:159], v255\n\t"
            "ds_read_b64 v[144:145], v253 offset:72\n\t"
            "ds_read_b64 v[146:147], v253 offset:168\n\t"
            "ds_read_b64 v[148:149], v253 offset:1816\n\t"
            "ds_read_b64 v[150:151], v253 offset:1912\n\t"
            "ds_read_b128 v[172:175], v254 offset:576\n\t"
            "ds_read_b64 v[152:153], v253 offset:80\n\t"
            "v_cvt_pk_bf16_f32 v184, v184, v185\n\t"
            "v_cvt_pk_bf16_f32 v185, v186, v187\n\t"
            "v_cvt_pk_bf16_f32 v186, v188, v189\n\t"
            "v_cvt_pk_bf16_f32 v187, v190, v191\n\t"
            "v_mfma_f32_4x4x4_16b_bf16 v[196:199], v[184:185], v[184:185], v[196:199]\n\t"
            "ds_read_b64 v[154:155], v253 offset:176\n\t"
            "v_mfma_f32_16x16x32_bf16 v[192:195], v[238:241], v[184:187], v[192:195]\n\t"
            "ds_read_b64 v[156:157], v253 offset:1824\n\t"
            "v_mov_b64 v[188:189], 0\n\t"
            "v_mfma_f32_4x4x4_16b_bf16 v[196:199], v[186:187], v[186:187], v[196:199]\n\t"
            "ds_read_b64 v[158:159], v253 offset:1920\n\t"
            "v_mov_b64 v[190:191], 0\n\t"
            "s_waitcnt lgkmcnt(14)\n\t"
            "ds_write_b128 v236, v[176:179] offset:128\n\t"
            "v_mov_b64 v[176:177], 0\n\t"
            "v_mov_b64 v[178:179], 0\n\t"
            "s_waitcnt lgkmcnt(10)\n\t"
            "s_nop 0\n\t"
            "v_smfmac_f32_16x16x64_bf16 v[176:179], v[168:171], v[160:167], v255\n\t"
            "ds_read_b128 v[168:171], v254 offset:640\n\t"
            "s_waitcnt lgkmcnt(6)\n\t"
            "v_smfmac_f32_16x16x64_bf16 v[180:183], v[172:175], v[144:151], v255\n\t"
            "ds_read_b64 v[160:161], v253 offset:88\n\t"
            "ds_read_b64 v[162:163], v253 offset:184\n\t"
            "ds_read_b64 v[164:165], v253 offset:1832\n\t"
            "ds_read_b64 v[166:167], v253 offset:1928\n\t"
            "ds_read_b128 v[172:175], v254 offset:704\n\t"
            "ds_write_b128 v237, v[184:187] offset:128\n\t"
            "v_mov_b64 v[184:185], 0\n\t"
            "v_mov_b64 v[186:187], 0\n\t"
            "v_cvt_pk_bf16_f32 v176, v176, v177\n\t"
            "v_cvt_pk_bf16_f32 v177, v178, v179\n\t"
            "v_cvt_pk_bf16_f32 v178, v180, v181\n\t"
            "v_cvt_pk_bf16_f32 v179, v182, v183\n\t"
            "v_mfma_f32_4x4x4_16b_bf16 v[196:199], v[176:177], v[176:177], v[196:199]\n\t"
            "ds_write_b128 v236, v[176:179] offset:256\n\t"
            "s_waitcnt lgkmcnt(7)\n\t"
            "v_smfmac_f32_16x16x64_bf16 v[184:187], v[168:171], v[152:159], v255\n\t"
            "v_mfma_f32_4x4x4_16b_bf16 v[196:199], v[178:179], v[178:179], v[196:199]\n\t"
            "v_mfma_f32_16x16x32_bf16 v[192:195], v[238:241], v[176:179], v[192:195]\n\t"
            "s_waitcnt lgkmcnt(2)\n\t"
            "v_smfmac_f32_16x16x64_bf16 v[188:191], v[172:175], v[160:167], v255\n\t"
            "s_nop 3\n\t"
            "v_cvt_pk_bf16_f32 v184, v184, v185\n\t"
            "v_cvt_pk_bf16_f32 v185, v186, v187\n\t"
            "s_nop 1\n\t"
            "v_mfma_f32_4x4x4_16b_bf16 v[196:199], v[184:185], v[184:185], v[196:199]\n\t"
            "v_cvt_pk_bf16_f32 v186, v188, v189\n\t"
            "v_cvt_pk_bf16_f32 v187, v190, v191\n\t"
            "s_nop 1\n\t"
            "v_mfma_f32_4x4x4_16b_bf16 v[196:199], v[186:187], v[186:187], v[196:199]\n\t"
            "ds_write_b128 v237, v[184:187] offset:256\n\t"
            "v_mfma_f32_16x16x32_bf16 v[192:195], v[238:241], v[184:187], v[192:195]\n\t"
            "s_nop 7\n\t"
            "v_cndmask_b32_e64 v193, v196, v197, s[60:61]\n\t"
            "v_cndmask_b32_e64 v193, v193, v198, s[62:63]\n\t"
            "v_cndmask_b32_e64 v193, v193, v199, s[64:65]\n\t"
            "ds_write_b64 v248, v[192:193] offset:18816\n\t"
            "s_waitcnt lgkmcnt(0)\n\t"
            "s_nop 7\n\t"
            "v_mov_b32 v144, s44\n\t"
            "v_mov_b32 v145, 0xbfffffff\n\t"
            "ds_write_b32 v144, v145\n\t"
            "s_setprio 1\n\t"
            "v_mov_b32 v144, s23\n\t"
            ".Lgo%=:\n\t"
            "ds_read_b32 v145, v144 offset:124\n\t"
            "s_waitcnt lgkmcnt(0)\n\t"
            "v_cmp_ne_u32 vcc, -1, v145\n\t"
            "s_nop 4\n\t"
            "s_cbranch_vccnz .Lgo%=\n\t"
            "s_nop 7\n\t"
            "ds_read_b32 v249, v252 offset:1024\n\t"
            "s_add_u32 s49, s70, 0\n\t"
            "buffer_load_dwordx4 v[0:3], v247, s[28:31], s49 offen sc1\n\t"
            "s_add_u32 s49, s70, 1024\n\t"
            "buffer_load_dwordx4 v[4:7], v247, s[28:31], s49 offen sc1\n\t"
            "s_add_u32 s49, s70, 2048\n\t"
            "buffer_load_dwordx4 v[8:11], v247, s[28:31], s49 offen sc1\n\t"
            "s_add_u32 s49, s70, 3072\n\t"
            "buffer_load_dwordx4 v[12:15], v247, s[28:31], s49 offen sc1\n\t"
            "s_add_u32 s49, s70, 4096\n\t"
            "buffer_load_dwordx4 v[16:19], v247, s[28:31], s49 offen sc1\n\t"
            "s_add_u32 s49, s70, 5120\n\t"
            "buffer_load_dwordx4 v[20:23], v247, s[28:31], s49 offen sc1\n\t"
            "s_add_u32 s49, s70, 6144\n\t"
            "buffer_load_dwordx4 v[24:27], v247, s[28:31], s49 offen sc1\n\t"
            "s_add_u32 s49, s70, 7168\n\t"
            "buffer_load_dwordx4 v[28:31], v247, s[28:31], s49 offen sc1\n\t"
            "s_add_u32 s49, s70, 8192\n\t"
            "buffer_load_dwordx4 v[32:35], v247, s[28:31], s49 offen sc1\n\t"
            "s_add_u32 s49, s70, 9216\n\t"
            "buffer_load_dwordx4 v[36:39], v247, s[28:31], s49 offen sc1\n\t"
            "s_add_u32 s49, s70, 10240\n\t"
            "buffer_load_dwordx4 v[40:43], v247, s[28:31], s49 offen sc1\n\t"
            "s_add_u32 s49, s70, 11264\n\t"
            "buffer_load_dwordx4 v[44:47], v247, s[28:31], s49 offen sc1\n\t"
            "s_add_u32 s49, s70, 12288\n\t"
            "buffer_load_dwordx4 v[48:51], v247, s[28:31], s49 offen sc1\n\t"
            "s_add_u32 s49, s70, 13312\n\t"
            "buffer_load_dwordx4 v[52:55], v247, s[28:31], s49 offen sc1\n\t"
            "s_add_u32 s49, s70, 14336\n\t"
            "buffer_load_dwordx4 v[56:59], v247, s[28:31], s49 offen sc1\n\t"
            "s_add_u32 s49, s70, 15360\n\t"
            "buffer_load_dwordx4 v[60:63], v247, s[28:31], s49 offen sc1\n\t"
            "s_add_u32 s49, s70, 16384\n\t"
            "buffer_load_dwordx4 v[64:67], v247, s[28:31], s49 offen sc1\n\t"
            "s_add_u32 s49, s70, 17408\n\t"
            "buffer_load_dwordx4 v[68:71], v247, s[28:31], s49 offen sc1\n\t"
            "s_waitcnt lgkmcnt(0)\n\t"
            "v_and_b32 v249, s69, v249\n\t"
            "s_mov_b32 exec_hi, 0x3fffff\n\t"
            "s_add_u32 m0, s66, 0\n\t"
            "s_add_u32 s49, s79, 0\n\t"
            "buffer_load_dwordx4 v249, s[72:75], s49 offen lds\n\t"
            "s_mov_b32 exec_hi, -1\n\t"
            "s_mov_b32 exec_hi, 0x3fffff\n\t"
            "s_add_u32 m0, s66, 872\n\t"
            "s_add_u32 s49, s68, 196224\n\t"
            "buffer_load_dwordx4 v249, s[72:75], s49 offen lds\n\t"
            "s_mov_b32 exec_hi, -1\n\t"
            "s_mov_b32 exec_hi, 0x3fffff\n\t"
            "s_add_u32 m0, s66, 1744\n\t"
            "s_add_u32 s49, s68, 392448\n\t"
            "buffer_load_dwordx4 v249, s[72:75], s49 offen lds\n\t"
            "s_mov_b32 exec_hi, -1\n\t"
            "s_mov_b32 exec_hi, 0x3fffff\n\t"
            "s_add_u32 m0, s66, 2616\n\t"
            "s_add_u32 s49, s68, 588672\n\t"
            "buffer_load_dwordx4 v249, s[72:75], s49 offen lds\n\t"
            "s_mov_b32 exec_hi, -1\n\t"
            "s_mov_b32 exec_hi, 0x3fffff\n\t"
            "s_add_u32 m0, s66, 3488\n\t"
            "s_add_u32 s49, s68, 784896\n\t"
            "buffer_load_dwordx4 v249, s[72:75], s49 offen lds\n\t"
            "s_mov_b32 exec_hi, -1\n\t"
            "s_mov_b32 exec_hi, 0x3fffff\n\t"
            "s_add_u32 m0, s66, 4360\n\t"
            "s_add_u32 s49, s68, 981120\n\t"
            "buffer_load_dwordx4 v249, s[72:75], s49 offen lds\n\t"
            "s_mov_b32 exec_hi, -1\n\t"
            "s_mov_b32 exec_hi, 0x3fffff\n\t"
            "s_add_u32 m0, s66, 5232\n\t"
            "s_add_u32 s49, s68, 1177344\n\t"
            "buffer_load_dwordx4 v249, s[72:75], s49 offen lds\n\t"
            "s_mov_b32 exec_hi, -1\n\t"
            "s_mov_b32 exec_hi, 0x3fffff\n\t"
            "s_add_u32 m0, s66, 6104\n\t"
            "s_add_u32 s49, s68, 1373568\n\t"
            "buffer_load_dwordx4 v249, s[72:75], s49 offen lds\n\t"
            "s_mov_b32 exec_hi, -1\n\t"
            "s_mov_b32 exec_hi, 0x3fffff\n\t"
            "s_add_u32 m0, s66, 6976\n\t"
            "s_add_u32 s49, s68, 1569792\n\t"
            "buffer_load_dwordx4 v249, s[72:75], s49 offen lds\n\t"
            "s_mov_b32 exec_hi, -1\n\t"
            "s_add_u32 s45, s46, s27\n\t"
            "s_mov_b32 s46, s47\n\t"
            "s_mov_b32 s47, s68\n\t"
            "s_xor_b32 s48, s48, s71\n\t"
            "s_xor_b32 s67, s67, s71\n\t"
            "s_add_u32 s76, s76, 4\n\t"
            "s_lshl_b32 s56, s56, 2\n\t"
            "s_lshr_b32 s49, s26, 30\n\t"
            "s_or_b32 s56, s56, s49\n\t"
            "s_cmp_ge_u32 s76, s24\n\t"
            "s_cselect_b64 vcc, -1, 0\n\t"
            "s_nop 7\n\t"
            "v_mov_b32 v144, s23\n\t"
            ".Lpg%=:\n\t"
            "ds_read_b128 v[148:151], v144 offset:96\n\t"
            "s_waitcnt lgkmcnt(0)\n\t"
            "v_and_b32 v148, v148, v149\n\t"
            "v_and_b32 v150, v150, v151\n\t"
            "v_and_b32 v148, v148, v150\n\t"
            "v_cmp_le_i32 vcc, 0, v148\n\t"
            "s_nop 4\n\t"
            "s_cbranch_vccnz .Lpg%=\n\t"
            "s_setprio 3\n\t"
            "s_nop 7\n\t"
            "v_add_u32 v196, s22, v229\n\t"
            "v_xor_b32 v197, 64, v196\n\t"
            "v_add_u32 v198, s22, v247\n\t"
            "v_mov_b32 v249, s76\n\t"
            "v_add_u32 v236, s67, v250\n\t"
            "v_add_u32 v248, s67, v251\n\t"
            "ds_read_b128 v[144:147], v196\n\t"
            "ds_read_b128 v[148:151], v197\n\t"
            "ds_read_b128 v[152:155], v196 offset:128\n\t"
            "ds_read_b128 v[156:159], v197 offset:128\n\t"
            "ds_read_b128 v[160:163], v196 offset:256\n\t"
            "ds_read_b128 v[164:167], v197 offset:256\n\t"
            "ds_read_b128 v[216:219], v198 offset:12288\n\t"
            "ds_read_b128 v[220:223], v198 offset:13312\n\t"
            "v_add_u32 v198, s48, v247\n\t"
            "ds_read_b128 v[168:171], v196 offset:6144\n\t"
            "ds_read_b128 v[172:175], v197 offset:6144\n\t"
            "ds_read_b128 v[176:179], v196 offset:6272\n\t"
            "ds_read_b128 v[180:183], v197 offset:6272\n\t"
            "ds_read_b128 v[184:187], v196 offset:6400\n\t"
            "v_add_u32 v196, s48, v229\n\t"
            "ds_read_b128 v[188:191], v197 offset:6400\n\t"
            "s_waitcnt vmcnt(9)\n\t"
            "s_waitcnt lgkmcnt(13)\n\t"
            "v_mfma_f32_16x16x32_bf16 v[200:203], v[0:3], v[144:147], 0\n\t"
            "ds_read_b32 v249, v249\n\t"
            "v_xor_b32 v197, 64, v196\n\t"
            "s_waitcnt lgkmcnt(13)\n\t"
            "v_mfma_f32_16x16x32_bf16 v[200:203], v[4:7], v[148:151], v[200:203]\n\t"
            "s_add_u32 s49, s33, 18432\n\t"
            "buffer_load_dwordx4 v[72:75], v247, s[28:31], s49 offen sc1\n\t"
            "s_waitcnt lgkmcnt(12)\n\t"
            "v_mfma_f32_16x16x32_bf16 v[200:203], v[8:11], v[152:155], v[200:203]\n\t"
            "s_waitcnt lgkmcnt(8)\n\t"
            "v_add_f32 v216, v216, v218\n\t"
            "s_waitcnt lgkmcnt(7)\n\t"
            "v_add_f32 v220, v220, v222\n\t"
            "v_mfma_f32_16x16x32_bf16 v[200:203], v[12:15], v[156:159], v[200:203]\n\t"
            "v_add_f32 v216, v216, v220\n\t"
            "v_add_f32 v217, v217, v219\n\t"
            "v_mfma_f32_16x16x32_bf16 v[200:203], v[16:19], v[160:163], v[200:203]\n\t"
            "v_add_f32 v221, v221, v223\n\t"
            "v_mul_f32 v216, 0x3baaaaab, v216\n\t"
            "v_mfma_f32_16x16x32_bf16 v[200:203], v[20:23], v[164:167], v[200:203]\n\t"
            "v_add_f32 v217, v217, v221\n\t"
            "v_mul_f32_e64 v216, v216, -v216\n\t"
            "v_mfma_f32_16x16x32_bf16 v[204:207], v[24:27], v[144:147], 0\n\t"
            "v_fmac_f32 v216, 0x3baaaaab, v217\n\t"
            "v_max_f32 v216, 0, v216\n\t"
            "v_mfma_f32_16x16x32_bf16 v[204:207], v[28:31], v[148:151], v[204:207]\n\t"
            "v_add_f32 v216, 0x3727c5ac, v216\n\t"
            "v_rsq_f32 v216, v216\n\t"
            "v_mfma_f32_16x16x32_bf16 v[204:207], v[32:35], v[152:155], v[204:207]\n\t"
            "ds_write_b32 v232, v216\n\t"
            "v_mfma_f32_16x16x32_bf16 v[204:207], v[36:39], v[156:159], v[204:207]\n\t"
            "ds_read_b32 v192, v234\n\t"
            "s_waitcnt lgkmcnt(2)\n\t"
            "v_readfirstlane_b32 s26, v249\n\t"
            "v_mfma_f32_16x16x32_bf16 v[204:207], v[40:43], v[160:163], v[204:207]\n\t"
            "ds_read_b32 v193, v234 offset:64\n\t"
            "v_mfma_f32_16x16x32_bf16 v[204:207], v[44:47], v[164:167], v[204:207]\n\t"
            "ds_read_b32 v249, v252 offset:1024\n\t"
            "v_mfma_f32_16x16x32_bf16 v[208:211], v[48:51], v[144:147], 0\n\t"
            "ds_read_b128 v[144:147], v196\n\t"
            "v_mfma_f32_16x16x32_bf16 v[208:211], v[52:55], v[148:151], v[208:211]\n\t"
            "ds_read_b128 v[148:151], v197\n\t"
            "v_mfma_f32_16x16x32_bf16 v[208:211], v[56:59], v[152:155], v[208:211]\n\t"
            "ds_read_b128 v[152:155], v196 offset:128\n\t"
            "v_mfma_f32_16x16x32_bf16 v[208:211], v[60:63], v[156:159], v[208:211]\n\t"
            "ds_read_b128 v[156:159], v197 offset:128\n\t"
            "v_mfma_f32_16x16x32_bf16 v[208:211], v[64:67], v[160:163], v[208:211]\n\t"
            "ds_read_b128 v[160:163], v196 offset:256\n\t"
            "v_mfma_f32_16x16x32_bf16 v[208:211], v[68:71], v[164:167], v[208:211]\n\t"
            "ds_read_b128 v[164:167], v197 offset:256\n\t"
            "s_waitcnt lgkmcnt(8)\n\t"
            "v_mul_f32 v200, v192, v200\n\t"
            "v_mfma_f32_16x16x32_bf16 v[212:215], v[0:3], v[168:171], 0\n\t"
            "v_mul_f32 v201, v192, v201\n\t"
            "v_mul_f32 v204, v192, v204\n\t"
            "v_mfma_f32_16x16x32_bf16 v[212:215], v[4:7], v[172:175], v[212:215]\n\t"
            "v_mul_f32 v205, v192, v205\n\t"
            "v_mul_f32 v202, v192, v202\n\t"
            "v_mfma_f32_16x16x32_bf16 v[212:215], v[8:11], v[176:179], v[212:215]\n\t"
            "v_mul_f32 v203, v192, v203\n\t"
            "v_cvt_pk_bf16_f32 v200, v200, v201\n\t"
            "v_mfma_f32_16x16x32_bf16 v[212:215], v[12:15], v[180:183], v[212:215]\n\t"
            "v_mul_f32 v206, v192, v206\n\t"
            "v_mul_f32 v207, v192, v207\n\t"
            "v_mfma_f32_16x16x32_bf16 v[212:215], v[16:19], v[184:187], v[212:215]\n\t"
            "v_cvt_pk_bf16_f32 v204, v204, v205\n\t"
            "v_cvt_pk_bf16_f32 v201, v202, v203\n\t"
            "v_mfma_f32_16x16x32_bf16 v[212:215], v[20:23], v[188:191], v[212:215]\n\t"
            "v_cvt_pk_bf16_f32 v205, v206, v207\n\t"
            "v_mul_f32 v208, v192, v208\n\t"
            "v_mfma_f32_16x16x32_bf16 v[216:219], v[24:27], v[168:171], 0\n\t"
            "ds_write_b64 v227, v[200:201]\n\t"
            "v_mfma_f32_16x16x32_bf16 v[216:219], v[28:31], v[172:175], v[216:219]\n\t"
            "ds_read_b128 v[200:203], v198 offset:18816\n\t"
            "v_mul_f32 v209, v192, v209\n\t"
            "v_mfma_f32_16x16x32_bf16 v[216:219], v[32:35], v[176:179], v[216:219]\n\t"
            "ds_write_b64 v227, v[204:205] offset:128\n\t"
            "v_mfma_f32_16x16x32_bf16 v[216:219], v[36:39], v[180:183], v[216:219]\n\t"
            "ds_read_b128 v[204:207], v198 offset:19840\n\t"
            "v_mul_f32 v210, v192, v210\n\t"
            "v_mfma_f32_16x16x32_bf16 v[216:219], v[40:43], v[184:187], v[216:219]\n\t"
            "s_add_u32 s49, s33, 19456\n\t"
            "buffer_load_dwordx4 v[76:79], v247, s[28:31], s49 offen sc1\n\t"
            "v_mfma_f32_16x16x32_bf16 v[216:219], v[44:47], v[188:191], v[216:219]\n\t"
            "s_add_u32 s49, s33, 20480\n\t"
            "buffer_load_dwordx4 v[80:83], v247, s[28:31], s49 offen sc1\n\t"
            "v_mfma_f32_16x16x32_bf16 v[220:223], v[48:51], v[168:171], 0\n\t"
            "ds_read_b128 v[168:171], v196 offset:6144\n\t"
            "s_waitcnt lgkmcnt(3)\n\t"
            "v_add_f32 v200, v200, v202\n\t"
            "v_mfma_f32_16x16x32_bf16 v[220:223], v[52:55], v[172:175], v[220:223]\n\t"
            "ds_read_b128 v[172:175], v197 offset:6144\n\t"
            "s_waitcnt lgkmcnt(2)\n\t"
            "v_add_f32 v204, v204, v206\n\t"
            "v_mfma_f32_16x16x32_bf16 v[220:223], v[56:59], v[176:179], v[220:223]\n\t"
            "ds_read_b128 v[176:179], v196 offset:6272\n\t"
            "v_add_f32 v200, v200, v204\n\t"
            "v_mfma_f32_16x16x32_bf16 v[220:223], v[60:63], v[180:183], v[220:223]\n\t"
            "ds_read_b128 v[180:183], v197 offset:6272\n\t"
            "v_add_f32 v201, v201, v203\n\t"
            "v_mfma_f32_16x16x32_bf16 v[220:223], v[64:67], v[184:187], v[220:223]\n\t"
            "ds_read_b128 v[184:187], v196 offset:6400\n\t"
            "v_add_f32 v205, v205, v207\n\t"
            "v_mfma_f32_16x16x32_bf16 v[220:223], v[68:71], v[188:191], v[220:223]\n\t"
            "ds_read_b128 v[188:191], v197 offset:6400\n\t"
            "v_mul_f32 v200, 0x3baaaaab, v200\n\t"
            "v_add_f32 v201, v201, v205\n\t"
            "v_mul_f32_e64 v200, v200, -v200\n\t"
            "v_fmac_f32 v200, 0x3baaaaab, v201\n\t"
            "v_max_f32 v200, 0, v200\n\t"
            "v_add_f32 v200, 0x3727c5ac, v200\n\t"
            "v_rsq_f32 v200, v200\n\t"
            "v_mul_f32 v211, v192, v211\n\t"
            "v_cvt_pk_bf16_f32 v208, v208, v209\n\t"
            "v_cvt_pk_bf16_f32 v209, v210, v211\n\t"
            "v_mul_f32 v212, v193, v212\n\t"
            "v_mul_f32 v213, v193, v213\n\t"
            "v_mul_f32 v214, v193, v214\n\t"
            "v_mul_f32 v215, v193, v215\n\t"
            "v_cvt_pk_bf16_f32 v212, v212, v213\n\t"
            "v_cvt_pk_bf16_f32 v213, v214, v215\n\t"
            "v_mul_f32 v216, v193, v216\n\t"
            "v_mul_f32 v217, v193, v217\n\t"
            "v_mul_f32 v218, v193, v218\n\t"
            "v_mul_f32 v219, v193, v219\n\t"
            "v_mul_f32 v220, v193, v220\n\t"
            "v_mul_f32 v221, v193, v221\n\t"
            "v_mul_f32 v222, v193, v222\n\t"
            "v_mul_f32 v223, v193, v223\n\t"
            "v_cvt_pk_bf16_f32 v216, v216, v217\n\t"
            "v_cvt_pk_bf16_f32 v217, v218, v219\n\t"
            "v_cvt_pk_bf16_f32 v220, v220, v221\n\t"
            "v_cvt_pk_bf16_f32 v221, v222, v223\n\t"
            "ds_write_b32 v232, v200\n\t"
            "v_mfma_f32_16x16x32_bf16 v[200:203], v[144:147], v[0:3], 0\n\t"
            "ds_read_b128 v[192:195], v233\n\t"
            "v_mfma_f32_16x16x32_bf16 v[200:203], v[148:151], v[4:7], v[200:203]\n\t"
            "ds_write_b64 v227, v[208:209] offset:256\n\t"
            "v_mfma_f32_16x16x32_bf16 v[200:203], v[152:155], v[8:11], v[200:203]\n\t"
            "s_add_u32 s49, s33, 21504\n\t"
            "buffer_load_dwordx4 v[84:87], v247, s[28:31], s49 offen sc1\n\t"
            "v_mfma_f32_16x16x32_bf16 v[200:203], v[156:159], v[12:15], v[200:203]\n\t"
            "s_add_u32 s49, s33, 22528\n\t"
            "buffer_load_dwordx4 v[88:91], v247, s[28:31], s49 offen sc1\n\t"
            "v_mfma_f32_16x16x32_bf16 v[200:203], v[160:163], v[16:19], v[200:203]\n\t"
            "ds_write_b64 v228, v[212:213]\n\t"
            "v_mfma_f32_16x16x32_bf16 v[200:203], v[164:167], v[20:23], v[200:203]\n\t"
            "s_add_u32 s49, s33, 23552\n\t"
            "buffer_load_dwordx4 v[92:95], v247, s[28:31], s49 offen sc1\n\t"
            "v_mfma_f32_16x16x32_bf16 v[204:207], v[144:147], v[24:27], 0\n\t"
            "s_add_u32 s49, s33, 24576\n\t"
            "buffer_load_dwordx4 v[96:99], v247, s[28:31], s49 offen sc1\n\t"
            "v_mfma_f32_16x16x32_bf16 v[204:207], v[148:151], v[28:31], v[204:207]\n\t"
            "s_add_u32 s49, s33, 25600\n\t"
            "buffer_load_dwordx4 v[100:103], v247, s[28:31], s49 offen sc1\n\t"
            "v_mfma_f32_16x16x32_bf16 v[204:207], v[152:155], v[32:35], v[204:207]\n\t"
            "ds_write_b64 v228, v[216:217] offset:128\n\t"
            "v_mfma_f32_16x16x32_bf16 v[204:207], v[156:159], v[36:39], v[204:207]\n\t"
            "s_add_u32 s49, s33, 26624\n\t"
            "buffer_load_dwordx4 v[104:107], v247, s[28:31], s49 offen sc1\n\t"
            "v_mfma_f32_16x16x32_bf16 v[204:207], v[160:163], v[40:43], v[204:207]\n\t"
            "s_add_u32 s49, s33, 27648\n\t"
            "buffer_load_dwordx4 v[108:111], v247, s[28:31], s49 offen sc1\n\t"
            "v_mfma_f32_16x16x32_bf16 v[204:207], v[164:167], v[44:47], v[204:207]\n\t"
            "s_waitcnt lgkmcnt(3)\n\t"
            "v_mul_f32 v200, v192, v200\n\t"
            "v_mul_f32 v201, v193, v201\n\t"
            "v_mfma_f32_16x16x32_bf16 v[208:211], v[144:147], v[48:51], 0\n\t"
            "ds_read_b128 v[144:147], v196 offset:12288\n\t"
            "v_mul_f32 v202, v194, v202\n\t"
            "v_mfma_f32_16x16x32_bf16 v[208:211], v[148:151], v[52:55], v[208:211]\n\t"
            "ds_read_b128 v[148:151], v197 offset:12288\n\t"
            "v_mul_f32 v203, v195, v203\n\t"
            "v_mfma_f32_16x16x32_bf16 v[208:211], v[152:155], v[56:59], v[208:211]\n\t"
            "ds_read_b128 v[152:155], v196 offset:12416\n\t"
            "v_mul_f32 v204, v192, v204\n\t"
            "v_mfma_f32_16x16x32_bf16 v[208:211], v[156:159], v[60:63], v[208:211]\n\t"
            "ds_read_b128 v[156:159], v197 offset:12416\n\t"
            "v_mul_f32 v205, v193, v205\n\t"
            "v_mfma_f32_16x16x32_bf16 v[208:211], v[160:163], v[64:67], v[208:211]\n\t"
            "ds_read_b128 v[160:163], v196 offset:12544\n\t"
            "v_mul_f32 v206, v194, v206\n\t"
            "v_mfma_f32_16x16x32_bf16 v[208:211], v[164:167], v[68:71], v[208:211]\n\t"
            "ds_read_b128 v[164:167], v197 offset:12544\n\t"
            "v_mul_f32 v207, v195, v207\n\t"
            "v_mfma_f32_16x16x32_bf16 v[212:215], v[168:171], v[0:3], 0\n\t"
            "ds_write_b64 v228, v[220:221] offset:256\n\t"
            "v_mfma_f32_16x16x32_bf16 v[212:215], v[172:175], v[4:7], v[212:215]\n\t"
            "s_add_u32 s49, s33, 28672\n\t"
            "buffer_load_dwordx4 v[112:115], v247, s[28:31], s49 offen sc1\n\t"
            "v_mfma_f32_16x16x32_bf16 v[212:215], v[176:179], v[8:11], v[212:215]\n\t"
            "v_mul_f32 v208, v192, v208\n\t"
            "v_mul_f32 v209, v193, v209\n\t"
            "v_mfma_f32_16x16x32_bf16 v[212:215], v[180:183], v[12:15], v[212:215]\n\t"
            "v_mul_f32 v210, v194, v210\n\t"
            "v_mul_f32 v211, v195, v211\n\t"
            "v_mfma_f32_16x16x32_bf16 v[212:215], v[184:187], v[16:19], v[212:215]\n\t"
            "ds_read_b128 v[192:195], v233 offset:64\n\t"
            "v_cvt_pk_bf16_f32 v200, v200, v201\n\t"
            "v_mfma_f32_16x16x32_bf16 v[212:215], v[188:191], v[20:23], v[212:215]\n\t"
            "s_add_u32 s49, s33, 29696\n\t"
            "buffer_load_dwordx4 v[116:119], v247, s[28:31], s49 offen sc1\n\t"
            "v_mfma_f32_16x16x32_bf16 v[216:219], v[168:171], v[24:27], 0\n\t"
            "s_add_u32 s49, s33, 30720\n\t"
            "buffer_load_dwordx4 v[120:123], v247, s[28:31], s49 offen sc1\n\t"
            "v_mfma_f32_16x16x32_bf16 v[216:219], v[172:175], v[28:31], v[216:219]\n\t"
            "v_cvt_pk_bf16_f32 v201, v202, v203\n\t"
            "v_cvt_pk_bf16_f32 v204, v204, v205\n\t"
            "v_mfma_f32_16x16x32_bf16 v[216:219], v[176:179], v[32:35], v[216:219]\n\t"
            "s_waitcnt lgkmcnt(0)\n\t"
            "v_mul_f32 v212, v192, v212\n\t"
            "v_mul_f32 v213, v193, v213\n\t"
            "v_mfma_f32_16x16x32_bf16 v[216:219], v[180:183], v[36:39], v[216:219]\n\t"
            "v_mul_f32 v214, v194, v214\n\t"
            "v_mul_f32 v215, v195, v215\n\t"
            "v_mfma_f32_16x16x32_bf16 v[216:219], v[184:187], v[40:43], v[216:219]\n\t"
            "v_cvt_pk_bf16_f32 v202, v212, v213\n\t"
            "v_cvt_pk_bf16_f32 v203, v214, v215\n\t"
            "v_mfma_f32_16x16x32_bf16 v[216:219], v[188:191], v[44:47], v[216:219]\n\t"
            "s_add_u32 s49, s33, 31744\n\t"
            "buffer_load_dwordx4 v[124:127], v247, s[28:31], s49 offen sc1\n\t"
            "v_mfma_f32_16x16x32_bf16 v[220:223], v[168:171], v[48:51], 0\n\t"
            "ds_read_b128 v[168:171], v254\n\t"
            "v_cvt_pk_bf16_f32 v205, v206, v207\n\t"
            "v_mfma_f32_16x16x32_bf16 v[220:223], v[172:175], v[52:55], v[220:223]\n\t"
            "ds_read_b128 v[172:175], v254 offset:64\n\t"
            "v_cvt_pk_bf16_f32 v208, v208, v209\n\t"
            "v_mfma_f32_16x16x32_bf16 v[220:223], v[176:179], v[56:59], v[220:223]\n\t"
            "s_add_u32 s49, s33, 32768\n\t"
            "buffer_load_dwordx4 v[128:131], v247, s[28:31], s49 offen sc1\n\t"
            "v_mfma_f32_16x16x32_bf16 v[220:223], v[180:183], v[60:63], v[220:223]\n\t"
            "v_mul_f32 v216, v192, v216\n\t"
            "v_mul_f32 v217, v193, v217\n\t"
            "v_mfma_f32_16x16x32_bf16 v[220:223], v[184:187], v[64:67], v[220:223]\n\t"
            "v_mul_f32 v218, v194, v218\n\t"
            "v_mul_f32 v219, v195, v219\n\t"
            "v_mfma_f32_16x16x32_bf16 v[220:223], v[188:191], v[68:71], v[220:223]\n\t"
            "v_cvt_pk_bf16_f32 v206, v216, v217\n\t"
            "v_cvt_pk_bf16_f32 v207, v218, v219\n\t"
            "v_mfma_f32_16x16x32_bf16 v[212:215], v[144:147], v[0:3], 0\n\t"
            "s_add_u32 s49, s33, 33792\n\t"
            "buffer_load_dwordx4 v[132:135], v247, s[28:31], s49 offen sc1\n\t"
            "v_mfma_f32_16x16x32_bf16 v[212:215], v[148:151], v[4:7], v[212:215]\n\t"
            "s_add_u32 s49, s33, 34816\n\t"
            "buffer_load_dwordx4 v[136:139], v247, s[28:31], s49 offen sc1\n\t"
            "v_mfma_f32_16x16x32_bf16 v[212:215], v[152:155], v[8:11], v[212:215]\n\t"
            "s_add_u32 s49, s33, 35840\n\t"
            "buffer_load_dwordx4 v[140:143], v247, s[28:31], s49 offen sc1\n\t"
            "v_mfma_f32_16x16x32_bf16 v[212:215], v[156:159], v[12:15], v[212:215]\n\t"
            "s_and_b32 s68, s26, 0x7ffffff\n\t"
            "s_add_u32 s68, s68, s78\n\t"
            "s_and_b32 s69, s26, 0x38000000\n\t"
            "s_or_b32 s69, s69, s39\n\t"
            "s_bitcmp1_b32 s26, 27\n\t"
            "s_cselect_b32 s79, 2.0, s68\n\t"
            "v_mfma_f32_16x16x32_bf16 v[212:215], v[160:163], v[16:19], v[212:215]\n\t"
            "s_and_b32 s57, s56, 3\n\t"
            "s_lshl_b32 s58, s57, 3\n\t"
            "s_lshr_b32 m0, s54, s58\n\t"
            "v_mfma_f32_16x16x32_bf16 v[212:215], v[164:167], v[20:23], v[212:215]\n\t"
            "s_lshl_b32 s58, s57, 4\n\t"
            "s_lshr_b64 s[36:37], s[50:51], s58\n\t"
            "s_and_b32 s57, s36, 0xffff\n\t"
            "s_add_u32 s57, s57, s67\n\t"
            "v_mfma_f32_16x16x32_bf16 v[216:219], v[144:147], v[24:27], 0\n\t"
            "s_lshr_b64 s[36:37], s[52:53], s58\n\t"
            "s_and_b32 s58, s36, 0xffff\n\t"
            "s_add_u32 s58, s58, s67\n\t"
            "v_mfma_f32_16x16x32_bf16 v[216:219], v[148:151], v[28:31], v[216:219]\n\t"
            "s_and_b32 s59, s50, 0xffff\n\t"
            "s_add_u32 s59, s59, s67\n\t"
            "s_and_b32 s38, s52, 0xffff\n\t"
            "s_add_u32 s38, s38, s67\n\t"
            "v_mfma_f32_16x16x32_bf16 v[216:219], v[152:155], v[32:35], v[216:219]\n\t"
            "v_cvt_pk_bf16_f32 v209, v210, v211\n\t"
            "v_mul_f32 v220, v192, v220\n\t"
            "v_mfma_f32_16x16x32_bf16 v[216:219], v[156:159], v[36:39], v[216:219]\n\t"
            "v_mul_f32 v221, v193, v221\n\t"
            "v_mul_f32 v222, v194, v222\n\t"
            "v_mfma_f32_16x16x32_bf16 v[216:219], v[160:163], v[40:43], v[216:219]\n\t"
            "v_mul_f32 v223, v195, v223\n\t"
            "v_cvt_pk_bf16_f32 v210, v220, v221\n\t"
            "v_mfma_f32_16x16x32_bf16 v[216:219], v[164:167], v[44:47], v[216:219]\n\t"
            "ds_read_b128 v[192:195], v233 offset:128\n\t"
            "v_cvt_pk_bf16_f32 v211, v222, v223\n\t"
            "v_mfma_f32_16x16x32_bf16 v[220:223], v[144:147], v[48:51], 0\n\t"
            "s_waitcnt vmcnt(18)\n\t"
            "ds_read_b64 v[144:145], v253\n\t"
            "v_mov_b64 v[176:177], 0\n\t"
            "v_mfma_f32_16x16x32_bf16 v[220:223], v[148:151], v[52:55], v[220:223]\n\t"
            "ds_read_b64 v[146:147], v253 offset:96\n\t"
            "v_mov_b64 v[178:179], 0\n\t"
            "v_mfma_f32_16x16x32_bf16 v[220:223], v[152:155], v[56:59], v[220:223]\n\t"
            "ds_read_b64 v[148:149], v253 offset:1744\n\t"
            "v_mov_b64 v[180:181], 0\n\t"
            "v_mfma_f32_16x16x32_bf16 v[220:223], v[156:159], v[60:63], v[220:223]\n\t"
            "ds_read_b64 v[150:151], v253 offset:1840\n\t"
            "v_mov_b64 v[182:183], 0\n\t"
            "v_mfma_f32_16x16x32_bf16 v[220:223], v[160:163], v[64:67], v[220:223]\n\t"
            "ds_read_b64 v[152:153], v253 offset:8\n\t"
            "v_mov_b64 v[184:185], 0\n\t"
            "v_mfma_f32_16x16x32_bf16 v[220:223], v[164:167], v[68:71], v[220:223]\n\t"
            "ds_read_b64 v[154:155], v253 offset:104\n\t"
            "v_mov_b64 v[186:187], 0\n\t"
            "v_mov_b64 v[188:189], 0\n\t"
            "v_mov_b64 v[190:191], 0\n\t"
            "v_writelane_b32 v236, s59, m0\n\t"
            "v_writelane_b32 v236, s57, 63\n\t"
            "v_xor_b32 v237, 16, v236\n\t"
            "s_waitcnt lgkmcnt(6)\n\t"
            "v_mul_f32 v212, v192, v212\n\t"
            "v_mul_f32 v213, v193, v213\n\t"
            "v_mul_f32 v214, v194, v214\n\t"
            "v_mul_f32 v215, v195, v215\n\t"
            "v_mul_f32 v216, v192, v216\n\t"
            "v_mul_f32 v217, v193, v217\n\t"
            "v_mul_f32 v218, v194, v218\n\t"
            "v_mul_f32 v219, v195, v219\n\t"
            "v_mul_f32 v220, v192, v220\n\t"
            "v_mul_f32 v221, v193, v221\n\t"
            "v_mul_f32 v222, v194, v222\n\t"
            "v_mul_f32 v223, v195, v223\n\t"
            "v_writelane_b32 v248, s38, m0\n\t"
            "v_and_b32 v249, s69, v249\n\t"
            "v_cvt_pk_bf16_f32 v212, v212, v213\n\t"
            "v_cvt_pk_bf16_f32 v216, v216, v217\n\t"
            "s_waitcnt lgkmcnt(2)\n\t"
            "v_smfmac_f32_16x16x64_bf16 v[176:179], v[168:171], v[144:151], v255\n\t"
            "ds_read_b64 v[156:157], v253 offset:1752\n\t"
            "v_cvt_pk_bf16_f32 v220, v220, v221\n\t"
            "v_cvt_pk_bf16_f32 v213, v214, v215\n\t"
            "v_cvt_pk_bf16_f32 v217, v218, v219\n\t"
            "v_cvt_pk_bf16_f32 v221, v222, v223\n\t"
            "v_writelane_b32 v248, s58, 63\n\t"
            "ds_read_b64 v[158:159], v253 offset:1848\n\t"
            "ds_read_b64 v[160:161], v253 offset:16\n\t"
            "v_cvt_pk_bf16_f32 v176, v176, v177\n\t"
            "v_cvt_pk_bf16_f32 v177, v178, v179\n\t"
            "ds_read_b64 v[162:163], v253 offset:112\n\t"
            "ds_read_b64 v[164:165], v253 offset:1760\n\t"
            "ds_read_b64 v[166:167], v253 offset:1856\n\t"
            "ds_read_b128 v[168:171], v254 offset:128\n\t"
            "ds_read_b64 v[144:145], v253 offset:24\n\t"
            "ds_read_b64 v[146:147], v253 offset:120\n\t"
            "ds_read_b64 v[148:149], v253 offset:1768\n\t"
            "ds_read_b64 v[150:151], v253 offset:1864\n\t"
            "ds_write_b128 v230, v[200:203]\n\t"
            "ds_write_b128 v230, v[204:207] offset:1536\n\t"
            "s_waitcnt lgkmcnt(11)\n\t"
            "v_smfmac_f32_16x16x64_bf16 v[180:183], v[172:175], v[152:159], v255\n\t"
            "ds_read_b128 v[172:175], v254 offset:192\n\t"
            "ds_read_b64 v[152:153], v253 offset:32\n\t"
            "ds_read_b64 v[154:155], v253 offset:128\n\t"
            "ds_read_b64 v[156:157], v253 offset:1776\n\t"
            "s_waitcnt lgkmcnt(10)\n\t"
            "v_smfmac_f32_16x16x64_bf16 v[184:187], v[168:171], v[160:167], v255\n\t"
            "ds_read_b64 v[158:159], v253 offset:1872\n\t"
            "v_mfma_f32_4x4x4_16b_bf16 v[196:199], v[176:177], v[176:177], 0\n\t"
            "ds_read_b128 v[168:171], v254 offset:256\n\t"
            "v_cvt_pk_bf16_f32 v178, v180, v181\n\t"
            "v_cvt_pk_bf16_f32 v179, v182, v183\n\t"
            "v_mov_b64 v[180:181], 0\n\t"
            "v_mov_b64 v[182:183], 0\n\t"
            "s_nop 0\n\t"
            "v_cvt_pk_bf16_f32 v184, v184, v185\n\t"
            "v_cvt_pk_bf16_f32 v185, v186, v187\n\t"
            "ds_read_b64 v[160:161], v253 offset:40\n\t"
            "ds_read_b64 v[162:163], v253 offset:136\n\t"
            "ds_read_b64 v[164:165], v253 offset:1784\n\t"
            "s_waitcnt lgkmcnt(14)\n\t"
            "ds_read_b64 v[166:167], v253 offset:1880\n\t"
            "s_waitcnt lgkmcnt(9)\n\t"
            "v_smfmac_f32_16x16x64_bf16 v[188:191], v[172:175], v[144:151], v255\n\t"
            "ds_read_b128 v[172:175], v254 offset:320\n\t"
            "v_mfma_f32_4x4x4_16b_bf16 v[196:199], v[178:179], v[178:179], v[196:199]\n\t"
            "ds_read_b64 v[144:145], v253 offset:48\n\t"
            "v_mfma_f32_16x16x32_bf16 v[192:195], v[238:241], v[176:179], 0\n\t"
            "ds_read_b64 v[146:147], v253 offset:144\n\t"
            "ds_read_b64 v[148:149], v253 offset:1792\n\t"
            "ds_read_b64 v[150:151], v253 offset:1888\n\t"
            "ds_write_b128 v236, v[176:179]\n\t"
            "v_mov_b64 v[176:177], 0\n\t"
            "v_mov_b64 v[178:179], 0\n\t"
            "v_cvt_pk_bf16_f32 v186, v188, v189\n\t"
            "v_cvt_pk_bf16_f32 v187, v190, v191\n\t"
            "s_waitcnt lgkmcnt(10)\n\t"
            "v_smfmac_f32_16x16x64_bf16 v[176:179], v[168:171], v[152:159], v255\n\t"
            "ds_read_b128 v[168:171], v254 offset:384\n\t"
            "v_mfma_f32_4x4x4_16b_bf16 v[196:199], v[184:185], v[184:185], v[196:199]\n\t"
            "ds_read_b64 v[152:153], v253 offset:56\n\t"
            "v_mov_b64 v[188:189], 0\n\t"
            "v_mov_b64 v[190:191], 0\n\t"
            "ds_read_b64 v[154:155], v253 offset:152\n\t"
            "s_waitcnt lgkmcnt(8)\n\t"
            "v_smfmac_f32_16x16x64_bf16 v[180:183], v[172:175], v[160:167], v255\n\t"
            "ds_read_b64 v[156:157], v253 offset:1800\n\t"
            "v_mfma_f32_4x4x4_16b_bf16 v[196:199], v[186:187], v[186:187], v[196:199]\n\t"
            "ds_read_b64 v[158:159], v253 offset:1896\n\t"
            "v_mfma_f32_16x16x32_bf16 v[192:195], v[238:241], v[184:187], v[192:195]\n\t"
            "ds_read_b128 v[172:175], v254 offset:448\n\t"
            "v_cvt_pk_bf16_f32 v176, v176, v177\n\t"
            "v_cvt_pk_bf16_f32 v177, v178, v179\n\t"
            "s_nop 0\n\t"
            "v_cvt_pk_bf16_f32 v178, v180, v181\n\t"
            "v_cvt_pk_bf16_f32 v179, v182, v183\n\t"
            "v_mov_b64 v[180:181], 0\n\t"
            "v_mov_b64 v[182:183], 0\n\t"
            "ds_read_b64 v[160:161], v253 offset:64\n\t"
            "ds_read_b64 v[162:163], v253 offset:160\n\t"
            "ds_read_b64 v[164:165], v253 offset:1808\n\t"
            "ds_read_b64 v[166:167], v253 offset:1904\n\t"
            "s_waitcnt lgkmcnt(14)\n\t"
            "ds_write_b128 v237, v[184:187]\n\t"
            "v_mov_b64 v[184:185], 0\n\t"
            "v_mov_b64 v[186:187], 0\n\t"
            "s_waitcnt lgkmcnt(10)\n\t"
            "s_nop 0\n\t"
            "v_smfmac_f32_16x16x64_bf16 v[184:187], v[168:171], v[144:151], v255\n\t"
            "ds_read_b128 v[168:171], v254 offset:512\n\t"
            "v_mfma_f32_4x4x4_16b_bf16 v[196:199], v[176:177], v[176:177], v[196:199]\n\t"
            "ds_read_b64 v[144:145], v253 offset:72\n\t"
            "s_waitcnt lgkmcnt(7)\n\t"
            "v_smfmac_f32_16x16x64_bf16 v[188:191], v[172:175], v[152:159], v255\n\t"
            "ds_read_b64 v[146:147], v253 offset:168\n\t"
            "v_mfma_f32_4x4x4_16b_bf16 v[196:199], v[178:179], v[178:179], v[196:199]\n\t"
            "ds_read_b64 v[148:149], v253 offset:1816\n\t"
            "v_mfma_f32_16x16x32_bf16 v[192:195], v[238:241], v[176:179], v[192:195]\n\t"
            "ds_read_b64 v[150:151], v253 offset:1912\n\t"
            "v_cvt_pk_bf16_f32 v184, v184, v185\n\t"
            "v_cvt_pk_bf16_f32 v185, v186, v187\n\t"
            "s_nop 0\n\t"
            "v_cvt_pk_bf16_f32 v186, v188, v189\n\t"
            "v_cvt_pk_bf16_f32 v187, v190, v191\n\t"
            "v_mov_b64 v[188:189], 0\n\t"
            "v_mov_b64 v[190:191], 0\n\t"
            "ds_read_b128 v[172:175], v254 offset:576\n\t"
            "ds_read_b64 v[152:153], v253 offset:80\n\t"
            "ds_read_b64 v[154:155], v253 offset:176\n\t"
            "ds_read_b64 v[156:157], v253 offset:1824\n\t"
            "ds_read_b64 v[158:159], v253 offset:1920\n\t"
            "s_waitcnt lgkmcnt(14)\n\t"
            "ds_write_b128 v236, v[176:179] offset:128\n\t"
            "v_mov_b64 v[176:177], 0\n\t"
            "v_mov_b64 v[178:179], 0\n\t"
            "s_waitcnt lgkmcnt(10)\n\t"
            "s_nop 0\n\t"
            "v_smfmac_f32_16x16x64_bf16 v[176:179], v[168:171], v[160:167], v255\n\t"
            "ds_read_b128 v[168:171], v254 offset:640\n\t"
            "v_mfma_f32_4x4x4_16b_bf16 v[196:199], v[184:185], v[184:185], v[196:199]\n\t"
            "ds_read_b64 v[160:161], v253 offset:88\n\t"
            "ds_read_b64 v[162:163], v253 offset:184\n\t"
            "ds_read_b64 v[164:165], v253 offset:1832\n\t"
            "ds_read_b64 v[166:167], v253 offset:1928\n\t"
            "s_waitcnt lgkmcnt(10)\n\t"
            "v_smfmac_f32_16x16x64_bf16 v[180:183], v[172:175], v[144:151], v255\n\t"
            "ds_read_b128 v[172:175], v254 offset:704\n\t"
            "v_mfma_f32_4x4x4_16b_bf16 v[196:199], v[186:187], v[186:187], v[196:199]\n\t"
            "ds_write_b128 v237, v[184:187] offset:128\n\t"
            "v_mfma_f32_16x16x32_bf16 v[192:195], v[238:241], v[184:187], v[192:195]\n\t"
            "v_mov_b64 v[184:185], 0\n\t"
            "v_mov_b64 v[186:187], 0\n\t"
            "v_cvt_pk_bf16_f32 v176, v176, v177\n\t"
            "v_cvt_pk_bf16_f32 v177, v178, v179\n\t"
            "v_cvt_pk_bf16_f32 v178, v180, v181\n\t"
            "v_cvt_pk_bf16_f32 v179, v182, v183\n\t"
            "ds_write_b128 v230, v[208:211] offset:3072\n\t"
            "s_waitcnt lgkmcnt(7)\n\t"
            "v_smfmac_f32_16x16x64_bf16 v[184:187], v[168:171], v[152:159], v255\n\t"
            "ds_write_b128 v236, v[176:179] offset:256\n\t"
            "v_mfma_f32_4x4x4_16b_bf16 v[196:199], v[176:177], v[176:177], v[196:199]\n\t"
            "ds_write_b64 v231, v[212:213]\n\t"
            "s_waitcnt lgkmcnt(4)\n\t"
            "v_smfmac_f32_16x16x64_bf16 v[188:191], v[172:175], v[160:167], v255\n\t"
            "s_mov_b32 exec_hi, 0x3fffff\n\t"
            "s_add_u32 m0, s66, 0\n\t"
            "s_add_u32 s49, s79, 0\n\t"
            "buffer_load_dwordx4 v249, s[72:75], s49 offen lds\n\t"
            "s_mov_b32 exec_hi, -1\n\t"
            "v_mfma_f32_4x4x4_16b_bf16 v[196:199], v[178:179], v[178:179], v[196:199]\n\t"
            "s_mov_b32 exec_hi, 0x3fffff\n\t"
            "s_add_u32 m0, s66, 872\n\t"
            "s_add_u32 s49, s68, 196224\n\t"
            "buffer_load_dwordx4 v249, s[72:75], s49 offen lds\n\t"
            "s_mov_b32 exec_hi, -1\n\t"
            "v_mfma_f32_16x16x32_bf16 v[192:195], v[238:241], v[176:179], v[192:195]\n\t"
            "s_mov_b32 exec_hi, 0x3fffff\n\t"
            "s_add_u32 m0, s66, 1744\n\t"
            "s_add_u32 s49, s68, 392448\n\t"
            "buffer_load_dwordx4 v249, s[72:75], s49 offen lds\n\t"
            "s_mov_b32 exec_hi, -1\n\t"
            "s_mov_b32 exec_hi, 0x3fffff\n\t"
            "s_add_u32 m0, s66, 2616\n\t"
            "s_add_u32 s49, s68, 588672\n\t"
            "buffer_load_dwordx4 v249, s[72:75], s49 offen lds\n\t"
            "s_mov_b32 exec_hi, -1\n\t"
            "s_mov_b32 exec_hi, 0x3fffff\n\t"
            "s_add_u32 m0, s66, 3488\n\t"
            "s_add_u32 s49, s68, 784896\n\t"
            "buffer_load_dwordx4 v249, s[72:75], s49 offen lds\n\t"
            "s_mov_b32 exec_hi, -1\n\t"
            "s_mov_b32 exec_hi, 0x3fffff\n\t"
            "s_add_u32 m0, s66, 4360\n\t"
            "s_add_u32 s49, s68, 981120\n\t"
            "buffer_load_dwordx4 v249, s[72:75], s49 offen lds\n\t"
            "s_mov_b32 exec_hi, -1\n\t"
            "v_cvt_pk_bf16_f32 v184, v184, v185\n\t"
            "v_cvt_pk_bf16_f32 v185, v186, v187\n\t"
            "v_cvt_pk_bf16_f32 v186, v188, v189\n\t"
            "v_cvt_pk_bf16_f32 v187, v190, v191\n\t"
            "v_mfma_f32_4x4x4_16b_bf16 v[196:199], v[184:185], v[184:185], v[196:199]\n\t"
            "s_mov_b32 exec_hi, 0x3fffff\n\t"
            "s_add_u32 m0, s66, 5232\n\t"
            "s_add_u32 s49, s68, 1177344\n\t"
            "buffer_load_dwordx4 v249, s[72:75], s49 offen lds\n\t"
            "s_mov_b32 exec_hi, -1\n\t"
            "v_mfma_f32_4x4x4_16b_bf16 v[196:199], v[186:187], v[186:187], v[196:199]\n\t"
            "s_mov_b32 exec_hi, 0x3fffff\n\t"
            "s_add_u32 m0, s66, 6104\n\t"
            "s_add_u32 s49, s68, 1373568\n\t"
            "buffer_load_dwordx4 v249, s[72:75], s49 offen lds\n\t"
            "s_mov_b32 exec_hi, -1\n\t"
            "v_mfma_f32_16x16x32_bf16 v[192:195], v[238:241], v[184:187], v[192:195]\n\t"
            "ds_write_b128 v237, v[184:187] offset:256\n\t"
            "ds_write_b64 v231, v[216:217] offset:1536\n\t"
            "ds_write_b64 v231, v[220:221] offset:3072\n\t"
            "s_mov_b32 exec_hi, 0x3fffff\n\t"
            "s_add_u32 m0, s66, 6976\n\t"
            "s_add_u32 s49, s68, 1569792\n\t"
            "buffer_load_dwordx4 v249, s[72:75], s49 offen lds\n\t"
            "s_mov_b32 exec_hi, -1\n\t"
            "v_cndmask_b32_e64 v193, v196, v197, s[60:61]\n\t"
            "v_cndmask_b32_e64 v193, v193, v198, s[62:63]\n\t"
            "v_cndmask_b32_e64 v193, v193, v199, s[64:65]\n\t"
            "ds_write_b64 v248, v[192:193] offset:18816\n\t"
            "s_waitcnt lgkmcnt(0)\n\t"
            "s_nop 7\n\t"
            "v_mov_b32 v144, s44\n\t"
            "v_mov_b32 v145, -1\n\t"
            "ds_write_b32 v144, v145\n\t"
            "s_cmp_ge_u32 s33, 221184\n\t"
            "s_cbranch_scc1 .Lrp%=\n\t"
            "s_barrier\n\t"
            "s_waitcnt vmcnt(9)\n\t"
            "s_nop 7\n\t"
            "v_add_u32 v232, s67, v242\n\t"
            "v_xor_b32 v233, 64, v232\n\t"
            "v_add_u32 v234, s67, v247\n\t"
            "ds_read_b128 v[144:147], v232\n\t"
            "ds_read_b128 v[148:151], v233\n\t"
            "ds_read_b128 v[152:155], v232 offset:128\n\t"
            "ds_read_b128 v[156:159], v233 offset:128\n\t"
            "ds_read_b128 v[160:163], v232 offset:256\n\t"
            "ds_read_b128 v[164:167], v233 offset:256\n\t"
            "ds_read_b128 v[176:179], v234 offset:18816\n\t"
            "ds_read_b128 v[180:183], v234 offset:19840\n\t"
            "s_waitcnt lgkmcnt(7)\n\t"
            "v_mfma_f32_16x16x32_bf16 v[200:203], v[144:147], v[0:3], 0\n\t"
            "s_waitcnt lgkmcnt(6)\n\t"
            "v_mfma_f32_16x16x32_bf16 v[200:203], v[148:151], v[4:7], v[200:203]\n\t"
            "s_waitcnt lgkmcnt(5)\n\t"
            "v_mfma_f32_16x16x32_bf16 v[200:203], v[152:155], v[8:11], v[200:203]\n\t"
            "s_waitcnt lgkmcnt(4)\n\t"
            "v_mfma_f32_16x16x32_bf16 v[200:203], v[156:159], v[12:15], v[200:203]\n\t"
            "s_waitcnt lgkmcnt(1)\n\t"
            "v_add_f32 v176, v176, v178\n\t"
            "s_waitcnt lgkmcnt(0)\n\t"
            "v_add_f32 v180, v180, v182\n\t"
            "v_mfma_f32_16x16x32_bf16 v[200:203], v[160:163], v[16:19], v[200:203]\n\t"
            "v_add_f32 v176, v176, v180\n\t"
            "v_add_f32 v177, v177, v179\n\t"
            "v_mfma_f32_16x16x32_bf16 v[200:203], v[164:167], v[20:23], v[200:203]\n\t"
            "v_add_f32 v181, v181, v183\n\t"
            "v_mul_f32 v176, 0x3baaaaab, v176\n\t"
            "v_mfma_f32_16x16x32_bf16 v[204:207], v[144:147], v[24:27], 0\n\t"
            "v_add_f32 v177, v177, v181\n\t"
            "v_mul_f32_e64 v176, v176, -v176\n\t"
            "v_mfma_f32_16x16x32_bf16 v[204:207], v[148:151], v[28:31], v[204:207]\n\t"
            "v_fmac_f32 v176, 0x3baaaaab, v177\n\t"
            "v_max_f32 v176, 0, v176\n\t"
            "v_mfma_f32_16x16x32_bf16 v[204:207], v[152:155], v[32:35], v[204:207]\n\t"
            "v_add_f32 v176, 0x3727c5ac, v176\n\t"
            "v_rsq_f32 v176, v176\n\t"
            "v_mfma_f32_16x16x32_bf16 v[204:207], v[156:159], v[36:39], v[204:207]\n\t"
            "ds_write_b32 v246, v176\n\t"
            "v_mfma_f32_16x16x32_bf16 v[204:207], v[160:163], v[40:43], v[204:207]\n\t"
            "ds_read_b128 v[228:231], v245\n\t"
            "v_mfma_f32_16x16x32_bf16 v[204:207], v[164:167], v[44:47], v[204:207]\n\t"
            "v_mfma_f32_16x16x32_bf16 v[208:211], v[144:147], v[48:51], 0\n\t"
            "v_mfma_f32_16x16x32_bf16 v[208:211], v[148:151], v[52:55], v[208:211]\n\t"
            "v_mfma_f32_16x16x32_bf16 v[208:211], v[152:155], v[56:59], v[208:211]\n\t"
            "v_mfma_f32_16x16x32_bf16 v[208:211], v[156:159], v[60:63], v[208:211]\n\t"
            "v_mfma_f32_16x16x32_bf16 v[208:211], v[160:163], v[64:67], v[208:211]\n\t"
            "v_mfma_f32_16x16x32_bf16 v[208:211], v[164:167], v[68:71], v[208:211]\n\t"
            "v_mfma_f32_16x16x32_bf16 v[212:215], v[144:147], v[72:75], 0\n\t"
            "s_waitcnt lgkmcnt(0)\n\t"
            "v_mul_f32 v200, v228, v200\n\t"
            "v_mul_f32 v201, v229, v201\n\t"
            "v_mfma_f32_16x16x32_bf16 v[212:215], v[148:151], v[76:79], v[212:215]\n\t"
            "v_mul_f32 v204, v228, v204\n\t"
            "v_mul_f32 v205, v229, v205\n\t"
            "v_mfma_f32_16x16x32_bf16 v[212:215], v[152:155], v[80:83], v[212:215]\n\t"
            "v_mul_f32 v208, v228, v208\n\t"
            "v_mul_f32 v209, v229, v209\n\t"
            "v_mfma_f32_16x16x32_bf16 v[212:215], v[156:159], v[84:87], v[212:215]\n\t"
            "v_mul_f32 v202, v230, v202\n\t"
            "v_mul_f32 v203, v231, v203\n\t"
            "v_mfma_f32_16x16x32_bf16 v[212:215], v[160:163], v[88:91], v[212:215]\n\t"
            "v_cvt_pk_bf16_f32 v200, v200, v201\n\t"
            "v_mul_f32 v206, v230, v206\n\t"
            "v_mfma_f32_16x16x32_bf16 v[212:215], v[164:167], v[92:95], v[212:215]\n\t"
            "v_mul_f32 v207, v231, v207\n\t"
            "v_cvt_pk_bf16_f32 v204, v204, v205\n\t"
            "v_mfma_f32_16x16x32_bf16 v[216:219], v[144:147], v[96:99], 0\n\t"
            "v_mul_f32 v210, v230, v210\n\t"
            "v_mul_f32 v211, v231, v211\n\t"
            "v_mfma_f32_16x16x32_bf16 v[216:219], v[148:151], v[100:103], v[216:219]\n\t"
            "v_cvt_pk_bf16_f32 v208, v208, v209\n\t"
            "v_cvt_pk_bf16_f32 v201, v202, v203\n\t"
            "v_mfma_f32_16x16x32_bf16 v[216:219], v[152:155], v[104:107], v[216:219]\n\t"
            "v_mul_f32 v212, v228, v212\n\t"
            "v_mul_f32 v213, v229, v213\n\t"
            "v_mfma_f32_16x16x32_bf16 v[216:219], v[156:159], v[108:111], v[216:219]\n\t"
            "v_mul_f32 v214, v230, v214\n\t"
            "v_mul_f32 v215, v231, v215\n\t"
            "v_mfma_f32_16x16x32_bf16 v[216:219], v[160:163], v[112:115], v[216:219]\n\t"
            "v_cvt_pk_bf16_f32 v212, v212, v213\n\t"
            "v_cvt_pk_bf16_f32 v205, v206, v207\n\t"
            "v_mfma_f32_16x16x32_bf16 v[216:219], v[164:167], v[116:119], v[216:219]\n\t"
            "v_cvt_pk_bf16_f32 v209, v210, v211\n\t"
            "v_cvt_pk_bf16_f32 v213, v214, v215\n\t"
            "v_mfma_f32_16x16x32_bf16 v[220:223], v[144:147], v[120:123], 0\n\t"
            "v_mfma_f32_16x16x32_bf16 v[220:223], v[148:151], v[124:127], v[220:223]\n\t"
            "v_mfma_f32_16x16x32_bf16 v[220:223], v[152:155], v[128:131], v[220:223]\n\t"
            "v_mfma_f32_16x16x32_bf16 v[220:223], v[156:159], v[132:135], v[220:223]\n\t"
            "s_nop 1\n\t"
            "v_mul_f32 v216, v228, v216\n\t"
            "v_mfma_f32_16x16x32_bf16 v[220:223], v[160:163], v[136:139], v[220:223]\n\t"
            "v_mul_f32 v217, v229, v217\n\t"
            "v_mul_f32 v218, v230, v218\n\t"
            "v_mfma_f32_16x16x32_bf16 v[220:223], v[164:167], v[140:143], v[220:223]\n\t"
            "v_mul_f32 v219, v231, v219\n\t"
            "v_cvt_pk_bf16_f32 v216, v216, v217\n\t"
            "v_cvt_pk_bf16_f32 v217, v218, v219\n\t"
            "s_nop 4\n\t"
            "v_mul_f32 v220, v228, v220\n\t"
            "v_mul_f32 v221, v229, v221\n\t"
            "v_mul_f32 v222, v230, v222\n\t"
            "v_mul_f32 v223, v231, v223\n\t"
            "v_cvt_pk_bf16_f32 v220, v220, v221\n\t"
            "v_cvt_pk_bf16_f32 v221, v222, v223\n\t"
            "s_nop 7\n\t"
            "s_add_u32 s45, s46, s27\n\t"
            "s_mov_b32 s46, s47\n\t"
            "s_mov_b32 s47, s68\n\t"
            "s_xor_b32 s48, s48, s71\n\t"
            "s_xor_b32 s67, s67, s71\n\t"
            "s_add_u32 s76, s76, 4\n\t"
            "s_lshl_b32 s56, s56, 2\n\t"
            "s_lshr_b32 s49, s26, 30\n\t"
            "s_or_b32 s56, s56, s49\n\t"
            "ds_read_b128 v[168:171], v254\n\t"
            "ds_read_b128 v[172:175], v254 offset:64\n\t"
            "s_waitcnt vmcnt(0)\n\t"
            "ds_read_b64 v[144:145], v253\n\t"
            "ds_read_b64 v[146:147], v253 offset:96\n\t"
            "ds_read_b64 v[148:149], v253 offset:1744\n\t"
            "ds_read_b64 v[150:151], v253 offset:1840\n\t"
            "ds_read_b64 v[152:153], v253 offset:8\n\t"
            "ds_read_b64 v[154:155], v253 offset:104\n\t"
            "ds_read_b64 v[156:157], v253 offset:1752\n\t"
            "ds_read_b64 v[158:159], v253 offset:1848\n\t"
            "ds_read_b64 v[160:161], v253 offset:16\n\t"
            "ds_read_b64 v[162:163], v253 offset:112\n\t"
            "ds_read_b64 v[164:165], v253 offset:1760\n\t"
            "ds_read_b64 v[166:167], v253 offset:1856\n\t"
            "s_waitcnt lgkmcnt(0)\n\t"
            "s_nop 7\n\t"
            ".p2align 6\n\t"
            ".Lwc_loop%=:\n\t"
            "s_setprio 3\n\t"
            "ds_read_b128 v[228:231], v245 offset:64\n\t"
            "v_mov_b64 v[176:177], 0\n\t"
            "v_mov_b64 v[178:179], 0\n\t"
            "v_mov_b32 v249, s76\n\t"
            "v_mov_b64 v[180:181], 0\n\t"
            "s_waitcnt lgkmcnt(9)\n\t"
            "v_smfmac_f32_16x16x64_bf16 v[176:179], v[168:171], v[144:151], v255\n\t"
            "ds_read_b128 v[168:171], v254 offset:128\n\t"
            "v_mov_b64 v[182:183], 0\n\t"
            "v_mov_b64 v[184:185], 0\n\t"
            "v_mov_b64 v[186:187], 0\n\t"
            "s_waitcnt lgkmcnt(6)\n\t"
            "v_smfmac_f32_16x16x64_bf16 v[180:183], v[172:175], v[152:159], v255\n\t"
            "ds_read_b32 v249, v249\n\t"
            "v_mov_b64 v[188:189], 0\n\t"
            "v_mov_b64 v[190:191], 0\n\t"
            "v_cvt_pk_bf16_f32 v176, v176, v177\n\t"
            "v_cvt_pk_bf16_f32 v177, v178, v179\n\t"
            "v_add_u32 v236, s67, v250\n\t"
            "v_add_u32 v232, s48, v242\n\t"
            "v_mfma_f32_4x4x4_16b_bf16 v[196:199], v[176:177], v[176:177], 0\n\t"
            "ds_read_b64 v[144:145], v253 offset:24\n\t"
            "v_cvt_pk_bf16_f32 v178, v180, v181\n\t"
            "v_cvt_pk_bf16_f32 v179, v182, v183\n\t"
            "s_waitcnt lgkmcnt(3)\n\t"
            "s_barrier\n\t"
            "v_mov_b64 v[180:181], 0\n\t"
            "v_mov_b64 v[182:183], 0\n\t"
            "v_xor_b32 v233, 64, v232\n\t"
            "v_add_u32 v248, s67, v251\n\t"
            "v_add_u32 v234, s67, v247\n\t"
            "ds_read_b64 v[146:147], v253 offset:120\n\t"
            "ds_read_b64 v[148:149], v253 offset:1768\n\t"
            "ds_read_b64 v[150:151], v253 offset:1864\n\t"
            "s_waitcnt lgkmcnt(5)\n\t"
            "v_smfmac_f32_16x16x64_bf16 v[184:187], v[168:171], v[160:167], v255\n\t"
            "ds_read_b128 v[172:175], v254 offset:192\n\t"
            "v_mfma_f32_4x4x4_16b_bf16 v[196:199], v[178:179], v[178:179], v[196:199]\n\t"
            "ds_read_b64 v[152:153], v253 offset:32\n\t"
            "v_mfma_f32_16x16x32_bf16 v[192:195], v[238:241], v[176:179], 0\n\t"
            "ds_read_b64 v[154:155], v253 offset:128\n\t"
            "s_waitcnt lgkmcnt(7)\n\t"
            "v_readfirstlane_b32 s26, v249\n\t"
            "ds_read_b64 v[156:157], v253 offset:1776\n\t"
            "s_and_b32 s68, s26, 0x7ffffff\n\t"
            "s_add_u32 s68, s68, s78\n\t"
            "s_and_b32 s69, s26, 0x38000000\n\t"
            "s_or_b32 s69, s69, s39\n\t"
            "s_bitcmp1_b32 s26, 27\n\t"
            "s_cselect_b32 s79, 2.0, s68\n\t"
            "ds_read_b64 v[158:159], v253 offset:1872\n\t"
            "s_and_b32 s57, s56, 3\n\t"
            "s_lshl_b32 s58, s57, 3\n\t"
            "s_lshr_b32 m0, s54, s58\n\t"
            "ds_read_b128 v[168:171], v254 offset:256\n\t"
            "s_lshl_b32 s58, s57, 4\n\t"
            "s_lshr_b64 s[36:37], s[50:51], s58\n\t"
            "s_and_b32 s57, s36, 0xffff\n\t"
            "s_add_u32 s57, s57, s67\n\t"
            "ds_read_b64 v[160:161], v253 offset:40\n\t"
            "s_lshr_b64 s[36:37], s[52:53], s58\n\t"
            "s_and_b32 s58, s36, 0xffff\n\t"
            "s_add_u32 s58, s58, s67\n\t"
            "ds_read_b64 v[162:163], v253 offset:136\n\t"
            "s_and_b32 s59, s50, 0xffff\n\t"
            "s_add_u32 s59, s59, s67\n\t"
            "s_and_b32 s38, s52, 0xffff\n\t"
            "s_add_u32 s38, s38, s67\n\t"
            "s_waitcnt lgkmcnt(7)\n\t"
            "v_smfmac_f32_16x16x64_bf16 v[188:191], v[172:175], v[144:151], v255\n\t"
            "ds_read_b64 v[164:165], v253 offset:1784\n\t"
            "v_writelane_b32 v236, s59, m0\n\t"
            "v_writelane_b32 v236, s57, 63\n\t"
            "v_cvt_pk_bf16_f32 v184, v184, v185\n\t"
            "v_cvt_pk_bf16_f32 v185, v186, v187\n\t"
            "v_xor_b32 v237, 16, v236\n\t"
            "v_writelane_b32 v248, s38, m0\n\t"
            "v_mfma_f32_4x4x4_16b_bf16 v[196:199], v[184:185], v[184:185], v[196:199]\n\t"
            "ds_read_b64 v[166:167], v253 offset:1880\n\t"
            "v_cvt_pk_bf16_f32 v186, v188, v189\n\t"
            "v_cvt_pk_bf16_f32 v187, v190, v191\n\t"
            "v_mov_b64 v[188:189], 0\n\t"
            "v_mov_b64 v[190:191], 0\n\t"
            "v_writelane_b32 v248, s58, 63\n\t"
            "ds_read_b128 v[172:175], v254 offset:320\n\t"
            "ds_read_b64 v[144:145], v253 offset:48\n\t"
            "ds_read_b64 v[146:147], v253 offset:144\n\t"
            "ds_read_b64 v[148:149], v253 offset:1792\n\t"
            "ds_read_b64 v[150:151], v253 offset:1888\n\t"
            "ds_read_b32 v249, v252 offset:1024\n\t"
            "s_waitcnt lgkmcnt(14)\n\t"
            "ds_write_b128 v236, v[176:179]\n\t"
            "v_mov_b64 v[176:177], 0\n\t"
            "v_mov_b64 v[178:179], 0\n\t"
            "s_waitcnt lgkmcnt(11)\n\t"
            "s_nop 0\n\t"
            "v_smfmac_f32_16x16x64_bf16 v[176:179], v[168:171], v[152:159], v255\n\t"
            "ds_read_b128 v[168:171], v254 offset:384\n\t"
            "v_mfma_f32_4x4x4_16b_bf16 v[196:199], v[186:187], v[186:187], v[196:199]\n\t"
            "ds_read_b64 v[152:153], v253 offset:56\n\t"
            "v_mfma_f32_16x16x32_bf16 v[192:195], v[238:241], v[184:187], v[192:195]\n\t"
            "ds_read_b64 v[154:155], v253 offset:152\n\t"
            "s_waitcnt lgkmcnt(9)\n\t"
            "v_smfmac_f32_16x16x64_bf16 v[180:183], v[172:175], v[160:167], v255\n\t"
            "ds_read_b64 v[156:157], v253 offset:1800\n\t"
            "ds_read_b64 v[158:159], v253 offset:1896\n\t"
            "v_cvt_pk_bf16_f32 v176, v176, v177\n\t"
            "v_cvt_pk_bf16_f32 v177, v178, v179\n\t"
            "s_nop 1\n\t"
            "v_mfma_f32_4x4x4_16b_bf16 v[196:199], v[176:177], v[176:177], v[196:199]\n\t"
            "ds_read_b128 v[172:175], v254 offset:448\n\t"
            "s_waitcnt lgkmcnt(7)\n\t"
            "v_and_b32 v249, s69, v249\n\t"
            "v_cvt_pk_bf16_f32 v178, v180, v181\n\t"
            "v_cvt_pk_bf16_f32 v179, v182, v183\n\t"
            "v_mov_b64 v[180:181], 0\n\t"
            "v_mov_b64 v[182:183], 0\n\t"
            "ds_read_b64 v[160:161], v253 offset:64\n\t"
            "ds_read_b64 v[162:163], v253 offset:160\n\t"
            "ds_read_b64 v[164:165], v253 offset:1808\n\t"
            "ds_read_b64 v[166:167], v253 offset:1904\n\t"
            "ds_write_b128 v237, v[184:187]\n\t"
            "v_mov_b64 v[184:185], 0\n\t"
            "v_mov_b64 v[186:187], 0\n\t"
            "s_waitcnt lgkmcnt(10)\n\t"
            "s_nop 0\n\t"
            "v_smfmac_f32_16x16x64_bf16 v[184:187], v[168:171], v[144:151], v255\n\t"
            "ds_read_b128 v[168:171], v254 offset:512\n\t"
            "v_mfma_f32_4x4x4_16b_bf16 v[196:199], v[178:179], v[178:179], v[196:199]\n\t"
            "ds_read_b64 v[144:145], v253 offset:72\n\t"
            "v_mfma_f32_16x16x32_bf16 v[192:195], v[238:241], v[176:179], v[192:195]\n\t"
            "ds_read_b64 v[146:147], v253 offset:168\n\t"
            "s_waitcnt lgkmcnt(8)\n\t"
            "v_smfmac_f32_16x16x64_bf16 v[188:191], v[172:175], v[152:159], v255\n\t"
            "ds_read_b64 v[148:149], v253 offset:1816\n\t"
            "ds_read_b64 v[150:151], v253 offset:1912\n\t"
            "v_cvt_pk_bf16_f32 v184, v184, v185\n\t"
            "v_cvt_pk_bf16_f32 v185, v186, v187\n\t"
            "s_nop 1\n\t"
            "v_mfma_f32_4x4x4_16b_bf16 v[196:199], v[184:185], v[184:185], v[196:199]\n\t"
            "ds_read_b128 v[172:175], v254 offset:576\n\t"
            "ds_read_b64 v[152:153], v253 offset:80\n\t"
            "ds_read_b64 v[154:155], v253 offset:176\n\t"
            "v_cvt_pk_bf16_f32 v186, v188, v189\n\t"
            "v_cvt_pk_bf16_f32 v187, v190, v191\n\t"
            "v_mov_b64 v[188:189], 0\n\t"
            "v_mov_b64 v[190:191], 0\n\t"
            "ds_read_b64 v[156:157], v253 offset:1824\n\t"
            "ds_read_b64 v[158:159], v253 offset:1920\n\t"
            "s_waitcnt lgkmcnt(14)\n\t"
            "ds_write_b128 v236, v[176:179] offset:128\n\t"
            "v_mov_b64 v[176:177], 0\n\t"
            "v_mov_b64 v[178:179], 0\n\t"
            "s_waitcnt lgkmcnt(10)\n\t"
            "s_nop 0\n\t"
            "v_smfmac_f32_16x16x64_bf16 v[176:179], v[168:171], v[160:167], v255\n\t"
            "ds_read_b128 v[168:171], v254 offset:640\n\t"
            "v_mfma_f32_4x4x4_16b_bf16 v[196:199], v[186:187], v[186:187], v[196:199]\n\t"
            "ds_read_b64 v[160:161], v253 offset:88\n\t"
            "v_mfma_f32_16x16x32_bf16 v[192:195], v[238:241], v[184:187], v[192:195]\n\t"
            "ds_read_b64 v[162:163], v253 offset:184\n\t"
            "s_waitcnt lgkmcnt(8)\n\t"
            "v_smfmac_f32_16x16x64_bf16 v[180:183], v[172:175], v[144:151], v255\n\t"
            "ds_read_b64 v[164:165], v253 offset:1832\n\t"
            "ds_read_b64 v[166:167], v253 offset:1928\n\t"
            "v_cvt_pk_bf16_f32 v176, v176, v177\n\t"
            "v_cvt_pk_bf16_f32 v177, v178, v179\n\t"
            "s_nop 1\n\t"
            "v_mfma_f32_4x4x4_16b_bf16 v[196:199], v[176:177], v[176:177], v[196:199]\n\t"
            "ds_read_b128 v[172:175], v254 offset:704\n\t"
            "ds_read_b128 v[144:147], v232 offset:6144\n\t"
            "ds_read_b128 v[148:151], v233 offset:6144\n\t"
            "v_cvt_pk_bf16_f32 v178, v180, v181\n\t"
            "v_cvt_pk_bf16_f32 v179, v182, v183\n\t"
            "ds_read_b128 v[180:183], v233 offset:12288\n\t"
            "ds_write_b128 v237, v[184:187] offset:128\n\t"
            "v_mov_b64 v[184:185], 0\n\t"
            "v_mov_b64 v[186:187], 0\n\t"
            "s_waitcnt lgkmcnt(9)\n\t"
            "s_nop 0\n\t"
            "v_smfmac_f32_16x16x64_bf16 v[184:187], v[168:171], v[152:159], v255\n\t"
            "ds_read_b128 v[152:155], v232 offset:6272\n\t"
            "v_mfma_f32_4x4x4_16b_bf16 v[196:199], v[178:179], v[178:179], v[196:199]\n\t"
            "ds_read_b128 v[156:159], v233 offset:6272\n\t"
            "v_mfma_f32_16x16x32_bf16 v[192:195], v[238:241], v[176:179], v[192:195]\n\t"
            "ds_write_b128 v236, v[176:179] offset:256\n\t"
            "s_waitcnt lgkmcnt(7)\n\t"
            "v_smfmac_f32_16x16x64_bf16 v[188:191], v[172:175], v[160:167], v255\n\t"
            "ds_read_b128 v[160:163], v232 offset:6400\n\t"
            "ds_read_b128 v[164:167], v233 offset:6400\n\t"
            "v_cvt_pk_bf16_f32 v184, v184, v185\n\t"
            "v_cvt_pk_bf16_f32 v185, v186, v187\n\t"
            "s_nop 1\n\t"
            "v_mfma_f32_4x4x4_16b_bf16 v[196:199], v[184:185], v[184:185], v[196:199]\n\t"
            "ds_read_b128 v[176:179], v232 offset:12288\n\t"
            "s_mov_b32 exec_hi, 0x3fffff\n\t"
            "s_add_u32 m0, s66, 0\n\t"
            "s_add_u32 s49, s79, 0\n\t"
            "buffer_load_dwordx4 v249, s[72:75], s49 offen lds\n\t"
            "s_mov_b32 exec_hi, -1\n\t"
            "v_cvt_pk_bf16_f32 v186, v188, v189\n\t"
            "v_cvt_pk_bf16_f32 v187, v190, v191\n\t"
            "s_nop 1\n\t"
            "v_mfma_f32_4x4x4_16b_bf16 v[196:199], v[186:187], v[186:187], v[196:199]\n\t"
            "ds_read_b128 v[188:191], v233 offset:12416\n\t"
            "v_mfma_f32_16x16x32_bf16 v[192:195], v[238:241], v[184:187], v[192:195]\n\t"
            "ds_write_b128 v237, v[184:187] offset:256\n\t"
            "s_waitcnt lgkmcnt(11)\n\t"
            "v_mfma_f32_16x16x32_bf16 v[224:227], v[144:147], v[0:3], 0\n\t"
            "s_setprio 3\n\t"
            "ds_read_b128 v[184:187], v232 offset:12416\n\t"
            "s_waitcnt lgkmcnt(11)\n\t"
            "v_mfma_f32_16x16x32_bf16 v[224:227], v[148:151], v[4:7], v[224:227]\n\t"
            "s_mov_b32 exec_hi, 0x3fffff\n\t"
            "s_add_u32 m0, s66, 872\n\t"
            "s_add_u32 s49, s68, 196224\n\t"
            "buffer_load_dwordx4 v249, s[72:75], s49 offen lds\n\t"
            "s_mov_b32 exec_hi, -1\n\t"
            "s_waitcnt lgkmcnt(8)\n\t"
            "v_mfma_f32_16x16x32_bf16 v[224:227], v[152:155], v[8:11], v[224:227]\n\t"
            "v_cndmask_b32_e64 v193, v196, v197, s[60:61]\n\t"
            "v_cndmask_b32_e64 v193, v193, v198, s[62:63]\n\t"
            "s_waitcnt lgkmcnt(7)\n\t"
            "v_mfma_f32_16x16x32_bf16 v[224:227], v[156:159], v[12:15], v[224:227]\n\t"
            "v_cndmask_b32_e64 v193, v193, v199, s[64:65]\n\t"
            "s_waitcnt lgkmcnt(5)\n\t"
            "v_mfma_f32_16x16x32_bf16 v[224:227], v[160:163], v[16:19], v[224:227]\n\t"
            "ds_read_b128 v[196:199], v233 offset:12544\n\t"
            "s_waitcnt lgkmcnt(5)\n\t"
            "v_mfma_f32_16x16x32_bf16 v[224:227], v[164:167], v[20:23], v[224:227]\n\t"
            "ds_write_b64 v248, v[192:193] offset:18816\n\t"
            "v_mfma_f32_16x16x32_bf16 v[168:171], v[144:147], v[24:27], 0\n\t"
            "ds_read_b128 v[192:195], v232 offset:12544\n\t"
            "v_add_u32 v232, s67, v242\n\t"
            "v_mfma_f32_16x16x32_bf16 v[168:171], v[148:151], v[28:31], v[168:171]\n\t"
            "s_mov_b32 exec_hi, 0x3fffff\n\t"
            "s_add_u32 m0, s66, 1744\n\t"
            "s_add_u32 s49, s68, 392448\n\t"
            "buffer_load_dwordx4 v249, s[72:75], s49 offen lds\n\t"
            "s_mov_b32 exec_hi, -1\n\t"
            "v_mfma_f32_16x16x32_bf16 v[168:171], v[152:155], v[32:35], v[168:171]\n\t"
            "v_mul_f32 v224, v228, v224\n\t"
            "v_mul_f32 v225, v229, v225\n\t"
            "v_mfma_f32_16x16x32_bf16 v[168:171], v[156:159], v[36:39], v[168:171]\n\t"
            "v_mul_f32 v226, v230, v226\n\t"
            "v_mul_f32 v227, v231, v227\n\t"
            "v_mfma_f32_16x16x32_bf16 v[168:171], v[160:163], v[40:43], v[168:171]\n\t"
            "v_cvt_pk_bf16_f32 v202, v224, v225\n\t"
            "v_cvt_pk_bf16_f32 v203, v226, v227\n\t"
            "v_mfma_f32_16x16x32_bf16 v[168:171], v[164:167], v[44:47], v[168:171]\n\t"
            "s_mov_b32 exec_hi, 0x3fffff\n\t"
            "s_add_u32 m0, s66, 2616\n\t"
            "s_add_u32 s49, s68, 588672\n\t"
            "buffer_load_dwordx4 v249, s[72:75], s49 offen lds\n\t"
            "s_mov_b32 exec_hi, -1\n\t"
            "v_mfma_f32_16x16x32_bf16 v[172:175], v[144:147], v[48:51], 0\n\t"
            "s_mov_b32 exec_hi, 0x3fffff\n\t"
            "s_add_u32 m0, s66, 3488\n\t"
            "s_add_u32 s49, s68, 784896\n\t"
            "buffer_load_dwordx4 v249, s[72:75], s49 offen lds\n\t"
            "s_mov_b32 exec_hi, -1\n\t"
            "v_mfma_f32_16x16x32_bf16 v[172:175], v[148:151], v[52:55], v[172:175]\n\t"
            "v_mul_f32 v168, v228, v168\n\t"
            "v_mul_f32 v169, v229, v169\n\t"
            "v_mfma_f32_16x16x32_bf16 v[172:175], v[152:155], v[56:59], v[172:175]\n\t"
            "v_mul_f32 v170, v230, v170\n\t"
            "v_mul_f32 v171, v231, v171\n\t"
            "v_mfma_f32_16x16x32_bf16 v[172:175], v[156:159], v[60:63], v[172:175]\n\t"
            "v_cvt_pk_bf16_f32 v206, v168, v169\n\t"
            "v_cvt_pk_bf16_f32 v207, v170, v171\n\t"
            "v_mfma_f32_16x16x32_bf16 v[172:175], v[160:163], v[64:67], v[172:175]\n\t"
            "s_mov_b32 exec_hi, 0x3fffff\n\t"
            "s_add_u32 m0, s66, 4360\n\t"
            "s_add_u32 s49, s68, 981120\n\t"
            "buffer_load_dwordx4 v249, s[72:75], s49 offen lds\n\t"
            "s_mov_b32 exec_hi, -1\n\t"
            "v_mfma_f32_16x16x32_bf16 v[172:175], v[164:167], v[68:71], v[172:175]\n\t"
            "s_mov_b32 exec_hi, 0x3fffff\n\t"
            "s_add_u32 m0, s66, 5232\n\t"
            "s_add_u32 s49, s68, 1177344\n\t"
            "buffer_load_dwordx4 v249, s[72:75], s49 offen lds\n\t"
            "s_mov_b32 exec_hi, -1\n\t"
            "v_mfma_f32_16x16x32_bf16 v[224:227], v[144:147], v[72:75], 0\n\t"
            "s_mov_b32 exec_hi, 0x3fffff\n\t"
            "s_add_u32 m0, s66, 6104\n\t"
            "s_add_u32 s49, s68, 1373568\n\t"
            "buffer_load_dwordx4 v249, s[72:75], s49 offen lds\n\t"
            "s_mov_b32 exec_hi, -1\n\t"
            "v_mfma_f32_16x16x32_bf16 v[224:227], v[148:151], v[76:79], v[224:227]\n\t"
            "v_mul_f32 v172, v228, v172\n\t"
            "v_mul_f32 v173, v229, v173\n\t"
            "v_mfma_f32_16x16x32_bf16 v[224:227], v[152:155], v[80:83], v[224:227]\n\t"
            "v_mul_f32 v174, v230, v174\n\t"
            "v_mul_f32 v175, v231, v175\n\t"
            "v_mfma_f32_16x16x32_bf16 v[224:227], v[156:159], v[84:87], v[224:227]\n\t"
            "v_cvt_pk_bf16_f32 v210, v172, v173\n\t"
            "v_cvt_pk_bf16_f32 v211, v174, v175\n\t"
            "v_mfma_f32_16x16x32_bf16 v[224:227], v[160:163], v[88:91], v[224:227]\n\t"
            "s_mov_b32 exec_hi, 0x3fffff\n\t"
            "s_add_u32 m0, s66, 6976\n\t"
            "s_add_u32 s49, s68, 1569792\n\t"
            "buffer_load_dwordx4 v249, s[72:75], s49 offen lds\n\t"
            "s_mov_b32 exec_hi, -1\n\t"
            "v_mfma_f32_16x16x32_bf16 v[224:227], v[164:167], v[92:95], v[224:227]\n\t"
            "s_add_u32 s45, s46, s27\n\t"
            "s_mov_b32 s46, s47\n\t"
            "s_mov_b32 s47, s68\n\t"
            "v_mfma_f32_16x16x32_bf16 v[168:171], v[144:147], v[96:99], 0\n\t"
            "s_xor_b32 s48, s48, s71\n\t"
            "s_xor_b32 s67, s67, s71\n\t"
            "s_add_u32 s76, s76, 4\n\t"
            "v_mfma_f32_16x16x32_bf16 v[168:171], v[148:151], v[100:103], v[168:171]\n\t"
            "s_lshl_b32 s56, s56, 2\n\t"
            "s_lshr_b32 s49, s26, 30\n\t"
            "s_or_b32 s56, s56, s49\n\t"
            "v_mfma_f32_16x16x32_bf16 v[168:171], v[152:155], v[104:107], v[168:171]\n\t"
            "v_mul_f32 v224, v228, v224\n\t"
            "v_mul_f32 v225, v229, v225\n\t"
            "v_mfma_f32_16x16x32_bf16 v[168:171], v[156:159], v[108:111], v[168:171]\n\t"
            "v_mul_f32 v226, v230, v226\n\t"
            "v_mul_f32 v227, v231, v227\n\t"
            "v_mfma_f32_16x16x32_bf16 v[168:171], v[160:163], v[112:115], v[168:171]\n\t"
            "v_cvt_pk_bf16_f32 v214, v224, v225\n\t"
            "v_cvt_pk_bf16_f32 v215, v226, v227\n\t"
            "v_mfma_f32_16x16x32_bf16 v[168:171], v[164:167], v[116:119], v[168:171]\n\t"
            "ds_write_b128 v243, v[200:203]\n\t"
            "v_mfma_f32_16x16x32_bf16 v[172:175], v[144:147], v[120:123], 0\n\t"
            "ds_write_b128 v243, v[204:207] offset:1536\n\t"
            "v_mfma_f32_16x16x32_bf16 v[172:175], v[148:151], v[124:127], v[172:175]\n\t"
            "ds_write_b128 v243, v[208:211] offset:3072\n\t"
            "v_mfma_f32_16x16x32_bf16 v[172:175], v[152:155], v[128:131], v[172:175]\n\t"
            "ds_write_b128 v243, v[212:215] offset:4608\n\t"
            "v_mfma_f32_16x16x32_bf16 v[172:175], v[156:159], v[132:135], v[172:175]\n\t"
            "v_mul_f32 v168, v228, v168\n\t"
            "v_mul_f32 v169, v229, v169\n\t"
            "v_mfma_f32_16x16x32_bf16 v[172:175], v[160:163], v[136:139], v[172:175]\n\t"
            "v_mul_f32 v170, v230, v170\n\t"
            "v_mul_f32 v171, v231, v171\n\t"
            "v_mfma_f32_16x16x32_bf16 v[172:175], v[164:167], v[140:143], v[172:175]\n\t"
            "v_cvt_pk_bf16_f32 v218, v168, v169\n\t"
            "v_cvt_pk_bf16_f32 v219, v170, v171\n\t"
            "s_waitcnt lgkmcnt(10)\n\t"
            "v_mfma_f32_16x16x32_bf16 v[224:227], v[176:179], v[0:3], 0\n\t"
            "ds_write_b128 v243, v[216:219] offset:6144\n\t"
            "v_mfma_f32_16x16x32_bf16 v[224:227], v[180:183], v[4:7], v[224:227]\n\t"
            "v_xor_b32 v233, 64, v232\n\t"
            "s_waitcnt lgkmcnt(8)\n\t"
            "v_mfma_f32_16x16x32_bf16 v[224:227], v[184:187], v[8:11], v[224:227]\n\t"
            "v_mul_f32 v172, v228, v172\n\t"
            "v_mul_f32 v173, v229, v173\n\t"
            "v_mfma_f32_16x16x32_bf16 v[224:227], v[188:191], v[12:15], v[224:227]\n\t"
            "v_mul_f32 v174, v230, v174\n\t"
            "v_mul_f32 v175, v231, v175\n\t"
            "s_waitcnt lgkmcnt(5)\n\t"
            "v_mfma_f32_16x16x32_bf16 v[224:227], v[192:195], v[16:19], v[224:227]\n\t"
            "ds_read_b128 v[228:231], v245 offset:128\n\t"
            "v_cvt_pk_bf16_f32 v222, v172, v173\n\t"
            "v_mfma_f32_16x16x32_bf16 v[224:227], v[196:199], v[20:23], v[224:227]\n\t"
            "v_cvt_pk_bf16_f32 v223, v174, v175\n\t"
            "v_mfma_f32_16x16x32_bf16 v[168:171], v[176:179], v[24:27], 0\n\t"
            "ds_write_b128 v243, v[220:223] offset:7680\n\t"
            "v_mfma_f32_16x16x32_bf16 v[168:171], v[180:183], v[28:31], v[168:171]\n\t"
            "v_mfma_f32_16x16x32_bf16 v[168:171], v[184:187], v[32:35], v[168:171]\n\t"
            "v_mfma_f32_16x16x32_bf16 v[168:171], v[188:191], v[36:39], v[168:171]\n\t"
            "v_mfma_f32_16x16x32_bf16 v[168:171], v[192:195], v[40:43], v[168:171]\n\t"
            "v_mfma_f32_16x16x32_bf16 v[168:171], v[196:199], v[44:47], v[168:171]\n\t"
            "s_waitcnt lgkmcnt(1)\n\t"
            "v_mul_f32 v224, v228, v224\n\t"
            "v_mfma_f32_16x16x32_bf16 v[172:175], v[176:179], v[48:51], 0\n\t"
            "v_mul_f32 v225, v229, v225\n\t"
            "v_mul_f32 v226, v230, v226\n\t"
            "v_mfma_f32_16x16x32_bf16 v[172:175], v[180:183], v[52:55], v[172:175]\n\t"
            "v_mul_f32 v227, v231, v227\n\t"
            "v_cvt_pk_bf16_f32 v224, v224, v225\n\t"
            "v_mfma_f32_16x16x32_bf16 v[172:175], v[184:187], v[56:59], v[172:175]\n\t"
            "v_cvt_pk_bf16_f32 v225, v226, v227\n\t"
            "v_mul_f32 v168, v228, v168\n\t"
            "v_mfma_f32_16x16x32_bf16 v[172:175], v[188:191], v[60:63], v[172:175]\n\t"
            "ds_write_b64 v244, v[224:225]\n\t"
            "v_mfma_f32_16x16x32_bf16 v[172:175], v[192:195], v[64:67], v[172:175]\n\t"
            "v_mul_f32 v169, v229, v169\n\t"
            "v_mul_f32 v170, v230, v170\n\t"
            "v_mfma_f32_16x16x32_bf16 v[172:175], v[196:199], v[68:71], v[172:175]\n\t"
            "v_mul_f32 v171, v231, v171\n\t"
            "v_cvt_pk_bf16_f32 v168, v168, v169\n\t"
            "v_mfma_f32_16x16x32_bf16 v[224:227], v[176:179], v[72:75], 0\n\t"
            "v_cvt_pk_bf16_f32 v169, v170, v171\n\t"
            "v_mfma_f32_16x16x32_bf16 v[224:227], v[180:183], v[76:79], v[224:227]\n\t"
            "ds_write_b64 v244, v[168:169] offset:1536\n\t"
            "v_mfma_f32_16x16x32_bf16 v[224:227], v[184:187], v[80:83], v[224:227]\n\t"
            "s_nop 0\n\t"
            "v_mul_f32 v172, v228, v172\n\t"
            "v_mul_f32 v173, v229, v173\n\t"
            "v_mfma_f32_16x16x32_bf16 v[224:227], v[188:191], v[84:87], v[224:227]\n\t"
            "v_mul_f32 v174, v230, v174\n\t"
            "v_mul_f32 v175, v231, v175\n\t"
            "v_mfma_f32_16x16x32_bf16 v[224:227], v[192:195], v[88:91], v[224:227]\n\t"
            "v_cvt_pk_bf16_f32 v172, v172, v173\n\t"
            "v_cvt_pk_bf16_f32 v173, v174, v175\n\t"
            "v_mfma_f32_16x16x32_bf16 v[224:227], v[196:199], v[92:95], v[224:227]\n\t"
            "ds_write_b64 v244, v[172:173] offset:3072\n\t"
            "v_mfma_f32_16x16x32_bf16 v[168:171], v[176:179], v[96:99], 0\n\t"
            "v_mfma_f32_16x16x32_bf16 v[168:171], v[180:183], v[100:103], v[168:171]\n\t"
            "v_mfma_f32_16x16x32_bf16 v[168:171], v[184:187], v[104:107], v[168:171]\n\t"
            "v_mfma_f32_16x16x32_bf16 v[168:171], v[188:191], v[108:111], v[168:171]\n\t"
            "v_mfma_f32_16x16x32_bf16 v[168:171], v[192:195], v[112:115], v[168:171]\n\t"
            "s_nop 1\n\t"
            "v_mul_f32 v224, v228, v224\n\t"
            "v_mfma_f32_16x16x32_bf16 v[168:171], v[196:199], v[116:119], v[168:171]\n\t"
            "v_mul_f32 v225, v229, v225\n\t"
            "v_mul_f32 v226, v230, v226\n\t"
            "v_mfma_f32_16x16x32_bf16 v[172:175], v[176:179], v[120:123], 0\n\t"
            "v_mul_f32 v227, v231, v227\n\t"
            "v_cvt_pk_bf16_f32 v224, v224, v225\n\t"
            "v_mfma_f32_16x16x32_bf16 v[172:175], v[180:183], v[124:127], v[172:175]\n\t"
            "v_cvt_pk_bf16_f32 v225, v226, v227\n\t"
            "v_mfma_f32_16x16x32_bf16 v[172:175], v[184:187], v[128:131], v[172:175]\n\t"
            "v_mul_f32 v168, v228, v168\n\t"
            "v_mul_f32 v169, v229, v169\n\t"
            "v_mfma_f32_16x16x32_bf16 v[172:175], v[188:191], v[132:135], v[172:175]\n\t"
            "v_mul_f32 v170, v230, v170\n\t"
            "v_mul_f32 v171, v231, v171\n\t"
            "v_mfma_f32_16x16x32_bf16 v[172:175], v[192:195], v[136:139], v[172:175]\n\t"
            "v_cvt_pk_bf16_f32 v168, v168, v169\n\t"
            "v_cvt_pk_bf16_f32 v169, v170, v171\n\t"
            "v_mfma_f32_16x16x32_bf16 v[172:175], v[196:199], v[140:143], v[172:175]\n\t"
            "ds_write_b64 v244, v[224:225] offset:4608\n\t"
            "ds_write_b64 v244, v[168:169] offset:6144\n\t"
            "ds_read_b128 v[168:171], v254\n\t"
            "s_nop 4\n\t"
            "v_mul_f32 v172, v228, v172\n\t"
            "v_mul_f32 v173, v229, v173\n\t"
            "v_mul_f32 v174, v230, v174\n\t"
            "v_mul_f32 v175, v231, v175\n\t"
            "v_cvt_pk_bf16_f32 v172, v172, v173\n\t"
            "v_cvt_pk_bf16_f32 v173, v174, v175\n\t"
            "ds_write_b64 v244, v[172:173] offset:7680\n\t"
            "ds_read_b128 v[172:175], v254 offset:64\n\t"
            "s_waitcnt lgkmcnt(1)\n\t"
            "s_cmp_lt_u32 s46, 0x7000000\n\t"
            "s_barrier\n\t"
            "s_cbranch_scc0 .Lvx%=\n\t"
            "ds_read_b128 v[144:147], v232\n\t"
            "ds_read_b128 v[148:151], v233\n\t"
            "ds_read_b128 v[152:155], v232 offset:128\n\t"
            "ds_read_b128 v[156:159], v233 offset:128\n\t"
            "ds_read_b128 v[160:163], v232 offset:256\n\t"
            "ds_read_b128 v[164:167], v233 offset:256\n\t"
            "ds_read_b128 v[176:179], v234 offset:18816\n\t"
            "ds_read_b128 v[180:183], v234 offset:19840\n\t"
            "s_waitcnt lgkmcnt(7)\n\t"
            "v_mfma_f32_16x16x32_bf16 v[200:203], v[144:147], v[0:3], 0\n\t"
            "s_waitcnt lgkmcnt(6)\n\t"
            "v_mfma_f32_16x16x32_bf16 v[200:203], v[148:151], v[4:7], v[200:203]\n\t"
            "s_waitcnt lgkmcnt(5)\n\t"
            "v_mfma_f32_16x16x32_bf16 v[200:203], v[152:155], v[8:11], v[200:203]\n\t"
            "s_waitcnt lgkmcnt(4)\n\t"
            "v_mfma_f32_16x16x32_bf16 v[200:203], v[156:159], v[12:15], v[200:203]\n\t"
            "s_waitcnt lgkmcnt(1)\n\t"
            "v_add_f32 v176, v176, v178\n\t"
            "s_waitcnt lgkmcnt(0)\n\t"
            "v_add_f32 v180, v180, v182\n\t"
            "v_mfma_f32_16x16x32_bf16 v[200:203], v[160:163], v[16:19], v[200:203]\n\t"
            "v_add_f32 v176, v176, v180\n\t"
            "v_add_f32 v177, v177, v179\n\t"
            "v_mfma_f32_16x16x32_bf16 v[200:203], v[164:167], v[20:23], v[200:203]\n\t"
            "v_add_f32 v181, v181, v183\n\t"
            "v_mul_f32 v176, 0x3baaaaab, v176\n\t"
            "v_mfma_f32_16x16x32_bf16 v[204:207], v[144:147], v[24:27], 0\n\t"
            "v_add_f32 v177, v177, v181\n\t"
            "v_mul_f32_e64 v176, v176, -v176\n\t"
            "v_mfma_f32_16x16x32_bf16 v[204:207], v[148:151], v[28:31], v[204:207]\n\t"
            "v_fmac_f32 v176, 0x3baaaaab, v177\n\t"
            "v_max_f32 v176, 0, v176\n\t"
            "v_mfma_f32_16x16x32_bf16 v[204:207], v[152:155], v[32:35], v[204:207]\n\t"
            "v_add_f32 v176, 0x3727c5ac, v176\n\t"
            "v_rsq_f32 v176, v176\n\t"
            "v_mfma_f32_16x16x32_bf16 v[204:207], v[156:159], v[36:39], v[204:207]\n\t"
            "ds_write_b32 v246, v176\n\t"
            "v_mfma_f32_16x16x32_bf16 v[204:207], v[160:163], v[40:43], v[204:207]\n\t"
            "ds_read_b128 v[228:231], v245\n\t"
            "v_mfma_f32_16x16x32_bf16 v[204:207], v[164:167], v[44:47], v[204:207]\n\t"
            "v_mfma_f32_16x16x32_bf16 v[208:211], v[144:147], v[48:51], 0\n\t"
            "v_mfma_f32_16x16x32_bf16 v[208:211], v[148:151], v[52:55], v[208:211]\n\t"
            "v_mfma_f32_16x16x32_bf16 v[208:211], v[152:155], v[56:59], v[208:211]\n\t"
            "v_mfma_f32_16x16x32_bf16 v[208:211], v[156:159], v[60:63], v[208:211]\n\t"
            "v_mfma_f32_16x16x32_bf16 v[208:211], v[160:163], v[64:67], v[208:211]\n\t"
            "v_mfma_f32_16x16x32_bf16 v[208:211], v[164:167], v[68:71], v[208:211]\n\t"
            "v_mfma_f32_16x16x32_bf16 v[212:215], v[144:147], v[72:75], 0\n\t"
            "s_waitcnt lgkmcnt(0)\n\t"
            "v_mul_f32 v200, v228, v200\n\t"
            "v_mul_f32 v201, v229, v201\n\t"
            "v_mfma_f32_16x16x32_bf16 v[212:215], v[148:151], v[76:79], v[212:215]\n\t"
            "v_mul_f32 v204, v228, v204\n\t"
            "v_mul_f32 v205, v229, v205\n\t"
            "v_mfma_f32_16x16x32_bf16 v[212:215], v[152:155], v[80:83], v[212:215]\n\t"
            "v_mul_f32 v208, v228, v208\n\t"
            "v_mul_f32 v209, v229, v209\n\t"
            "v_mfma_f32_16x16x32_bf16 v[212:215], v[156:159], v[84:87], v[212:215]\n\t"
            "v_mul_f32 v202, v230, v202\n\t"
            "v_mul_f32 v203, v231, v203\n\t"
            "v_mfma_f32_16x16x32_bf16 v[212:215], v[160:163], v[88:91], v[212:215]\n\t"
            "v_cvt_pk_bf16_f32 v200, v200, v201\n\t"
            "v_mul_f32 v206, v230, v206\n\t"
            "v_mfma_f32_16x16x32_bf16 v[212:215], v[164:167], v[92:95], v[212:215]\n\t"
            "v_mul_f32 v207, v231, v207\n\t"
            "v_cvt_pk_bf16_f32 v204, v204, v205\n\t"
            "v_mfma_f32_16x16x32_bf16 v[216:219], v[144:147], v[96:99], 0\n\t"
            "s_waitcnt vmcnt(0)\n\t"
            "v_mul_f32 v210, v230, v210\n\t"
            "v_mul_f32 v211, v231, v211\n\t"
            "v_mfma_f32_16x16x32_bf16 v[216:219], v[148:151], v[100:103], v[216:219]\n\t"
            "s_nop 0\n\t"
            "v_mul_f32 v212, v228, v212\n\t"
            "v_mul_f32 v213, v229, v213\n\t"
            "v_mfma_f32_16x16x32_bf16 v[216:219], v[152:155], v[104:107], v[216:219]\n\t"
            "v_cvt_pk_bf16_f32 v208, v208, v209\n\t"
            "v_mul_f32 v214, v230, v214\n\t"
            "v_mfma_f32_16x16x32_bf16 v[216:219], v[156:159], v[108:111], v[216:219]\n\t"
            "v_mul_f32 v215, v231, v215\n\t"
            "v_cvt_pk_bf16_f32 v212, v212, v213\n\t"
            "v_mfma_f32_16x16x32_bf16 v[216:219], v[160:163], v[112:115], v[216:219]\n\t"
            "v_cvt_pk_bf16_f32 v201, v202, v203\n\t"
            "v_cvt_pk_bf16_f32 v205, v206, v207\n\t"
            "v_mfma_f32_16x16x32_bf16 v[216:219], v[164:167], v[116:119], v[216:219]\n\t"
            "v_cvt_pk_bf16_f32 v209, v210, v211\n\t"
            "v_cvt_pk_bf16_f32 v213, v214, v215\n\t"
            "v_mfma_f32_16x16x32_bf16 v[220:223], v[144:147], v[120:123], 0\n\t"
            "ds_read_b64 v[144:145], v253\n\t"
            "v_mfma_f32_16x16x32_bf16 v[220:223], v[148:151], v[124:127], v[220:223]\n\t"
            "ds_read_b64 v[146:147], v253 offset:96\n\t"
            "v_mfma_f32_16x16x32_bf16 v[220:223], v[152:155], v[128:131], v[220:223]\n\t"
            "ds_read_b64 v[148:149], v253 offset:1744\n\t"
            "v_mul_f32 v216, v228, v216\n\t"
            "v_mfma_f32_16x16x32_bf16 v[220:223], v[156:159], v[132:135], v[220:223]\n\t"
            "ds_read_b64 v[150:151], v253 offset:1840\n\t"
            "v_mul_f32 v217, v229, v217\n\t"
            "v_mfma_f32_16x16x32_bf16 v[220:223], v[160:163], v[136:139], v[220:223]\n\t"
            "ds_read_b64 v[152:153], v253 offset:8\n\t"
            "v_mul_f32 v218, v230, v218\n\t"
            "v_mfma_f32_16x16x32_bf16 v[220:223], v[164:167], v[140:143], v[220:223]\n\t"
            "ds_read_b64 v[154:155], v253 offset:104\n\t"
            "v_mul_f32 v219, v231, v219\n\t"
            "v_cvt_pk_bf16_f32 v216, v216, v217\n\t"
            "v_cvt_pk_bf16_f32 v217, v218, v219\n\t"
            "ds_read_b64 v[156:157], v253 offset:1752\n\t"
            "ds_read_b64 v[158:159], v253 offset:1848\n\t"
            "ds_read_b64 v[160:161], v253 offset:16\n\t"
            "ds_read_b64 v[162:163], v253 offset:112\n\t"
            "v_mul_f32 v220, v228, v220\n\t"
            "v_mul_f32 v221, v229, v221\n\t"
            "v_mul_f32 v222, v230, v222\n\t"
            "v_mul_f32 v223, v231, v223\n\t"
            "v_cvt_pk_bf16_f32 v220, v220, v221\n\t"
            "v_cvt_pk_bf16_f32 v221, v222, v223\n\t"
            "ds_read_b64 v[164:165], v253 offset:1760\n\t"
            "ds_read_b64 v[166:167], v253 offset:1856\n\t"
            "s_branch .Lwc_loop%=\n\t"
            ".Lvx%=:\n\t"
            "s_waitcnt lgkmcnt(0)\n\t"
            "s_nop 7\n\t"
            "s_setprio 3\n\t"
            "v_add_u32 v236, s19, v252\n\t"
            "ds_read_b128 v[244:247], v236\n\t"
            "s_add_u32 s70, s33, 73728\n\t"
            "s_waitcnt lgkmcnt(0)\n\t"
            "buffer_load_dwordx4 v[200:203], v244, s[28:31], s21 offen\n\t"
            "buffer_load_dwordx4 v[204:207], v245, s[28:31], s21 offen\n\t"
            "buffer_load_dwordx4 v[208:211], v246, s[28:31], s21 offen\n\t"
            "s_add_u32 s21, s21, 6144\n\t"
            "buffer_load_dwordx4 v[212:215], v244, s[28:31], s21 offen\n\t"
            "buffer_load_dwordx4 v[216:219], v245, s[28:31], s21 offen\n\t"
            "buffer_load_dwordx4 v[220:223], v246, s[28:31], s21 offen\n\t"
            "s_nop 7\n\t"
            "s_add_u32 s49, s70, 0\n\t"
            "buffer_load_dwordx4 v[0:3], v247, s[28:31], s49 offen sc1\n\t"
            "s_add_u32 s49, s70, 1024\n\t"
            "buffer_load_dwordx4 v[4:7], v247, s[28:31], s49 offen sc1\n\t"
            "s_add_u32 s49, s70, 2048\n\t"
            "buffer_load_dwordx4 v[8:11], v247, s[28:31], s49 offen sc1\n\t"
            "s_add_u32 s49, s70, 3072\n\t"
            "buffer_load_dwordx4 v[12:15], v247, s[28:31], s49 offen sc1\n\t"
            "s_add_u32 s49, s70, 4096\n\t"
            "buffer_load_dwordx4 v[16:19], v247, s[28:31], s49 offen sc1\n\t"
            "s_add_u32 s49, s70, 5120\n\t"
            "buffer_load_dwordx4 v[20:23], v247, s[28:31], s49 offen sc1\n\t"
            "s_add_u32 s49, s70, 6144\n\t"
            "buffer_load_dwordx4 v[24:27], v247, s[28:31], s49 offen sc1\n\t"
            "s_add_u32 s49, s70, 7168\n\t"
            "buffer_load_dwordx4 v[28:31], v247, s[28:31], s49 offen sc1\n\t"
            "s_add_u32 s49, s70, 8192\n\t"
            "buffer_load_dwordx4 v[32:35], v247, s[28:31], s49 offen sc1\n\t"
            "s_add_u32 s49, s70, 9216\n\t"
            "buffer_load_dwordx4 v[36:39], v247, s[28:31], s49 offen sc1\n\t"
            "s_add_u32 s49, s70, 10240\n\t"
            "buffer_load_dwordx4 v[40:43], v247, s[28:31], s49 offen sc1\n\t"
            "s_add_u32 s49, s70, 11264\n\t"
            "buffer_load_dwordx4 v[44:47], v247, s[28:31], s49 offen sc1\n\t"
            "s_add_u32 s49, s70, 12288\n\t"
            "buffer_load_dwordx4 v[48:51], v247, s[28:31], s49 offen sc1\n\t"
            "s_add_u32 s49, s70, 13312\n\t"
            "buffer_load_dwordx4 v[52:55], v247, s[28:31], s49 offen sc1\n\t"
            "s_add_u32 s49, s70, 14336\n\t"
            "buffer_load_dwordx4 v[56:59], v247, s[28:31], s49 offen sc1\n\t"
            "s_add_u32 s49, s70, 15360\n\t"
            "buffer_load_dwordx4 v[60:63], v247, s[28:31], s49 offen sc1\n\t"
            "s_add_u32 s49, s70, 16384\n\t"
            "buffer_load_dwordx4 v[64:67], v247, s[28:31], s49 offen sc1\n\t"
            "s_add_u32 s49, s70, 17408\n\t"
            "buffer_load_dwordx4 v[68:71], v247, s[28:31], s49 offen sc1\n\t"
            "s_add_u32 s49, s70, 18432\n\t"
            "buffer_load_dwordx4 v[72:75], v247, s[28:31], s49 offen sc1\n\t"
            "s_add_u32 s49, s70, 19456\n\t"
            "buffer_load_dwordx4 v[76:79], v247, s[28:31], s49 offen sc1\n\t"
            "s_add_u32 s49, s70, 20480\n\t"
            "buffer_load_dwordx4 v[80:83], v247, s[28:31], s49 offen sc1\n\t"
            "s_add_u32 s49, s70, 21504\n\t"
            "buffer_load_dwordx4 v[84:87], v247, s[28:31], s49 offen sc1\n\t"
            "s_add_u32 s49, s70, 22528\n\t"
            "buffer_load_dwordx4 v[88:91], v247, s[28:31], s49 offen sc1\n\t"
            "s_add_u32 s49, s70, 23552\n\t"
            "buffer_load_dwordx4 v[92:95], v247, s[28:31], s49 offen sc1\n\t"
            "s_waitcnt vmcnt(18)\n\t"
            "s_add_u32 s49, s70, 24576\n\t"
            "buffer_load_dwordx4 v[96:99], v247, s[28:31], s49 offen sc1\n\t"
            "s_add_u32 s49, s70, 25600\n\t"
            "buffer_load_dwordx4 v[100:103], v247, s[28:31], s49 offen sc1\n\t"
            "v_add_u32 v242, s22, v242\n\t"
            "v_xor_b32 v243, 64, v242\n\t"
            "v_lshlrev_b32 v192, 16, v200\n\t"
            "v_and_b32 v193, 0xffff0000, v200\n\t"
            "v_lshlrev_b32 v194, 16, v201\n\t"
            "v_and_b32 v195, 0xffff0000, v201\n\t"
            "v_lshlrev_b32 v196, 16, v202\n\t"
            "v_and_b32 v197, 0xffff0000, v202\n\t"
            "v_lshlrev_b32 v198, 16, v203\n\t"
            "v_and_b32 v199, 0xffff0000, v203\n\t"
            "v_bfe_u32 v248, v247, 2, 6\n\t"
            "v_add_u32 v248, s23, v248\n\t"
            "v_lshlrev_b32 v224, 16, v204\n\t"
            "v_and_b32 v225, 0xffff0000, v204\n\t"
            "v_lshlrev_b32 v226, 16, v205\n\t"
            "v_and_b32 v227, 0xffff0000, v205\n\t"
            "v_lshrrev_b32 v236, 4, v247\n\t"
            "v_and_b32 v236, 48, v236\n\t"
            "v_add_u32 v236, s20, v236\n\t"
            "v_bfe_u32 v249, v247, 2, 6\n\t"
            "v_lshlrev_b32 v228, 16, v206\n\t"
            "v_and_b32 v229, 0xffff0000, v206\n\t"
            "v_lshlrev_b32 v230, 16, v207\n\t"
            "v_and_b32 v231, 0xffff0000, v207\n\t"
            "v_add_u32 v249, s23, v249\n\t"
            "ds_read_b128 v[144:147], v242 offset:53760\n\t"
            "ds_read_b128 v[148:151], v243 offset:53760\n\t"
            "ds_read_b128 v[152:155], v242 offset:53888\n\t"
            "ds_read_b128 v[156:159], v243 offset:53888\n\t"
            "ds_read_b128 v[160:163], v242 offset:54016\n\t"
            "ds_read_b128 v[164:167], v243 offset:54016\n\t"
            "ds_read_b32 v248, v248\n\t"
            "ds_read_b32 v249, v249 offset:64\n\t"
            "s_add_u32 s49, s70, 26624\n\t"
            "buffer_load_dwordx4 v[104:107], v247, s[28:31], s49 offen sc1\n\t"
            "s_add_u32 s49, s70, 27648\n\t"
            "buffer_load_dwordx4 v[108:111], v247, s[28:31], s49 offen sc1\n\t"
            "s_waitcnt lgkmcnt(7)\n\t"
            "v_mfma_f32_16x16x32_bf16 v[192:195], v[0:3], v[144:147], v[192:195]\n\t"
            "s_add_u32 s49, s70, 28672\n\t"
            "buffer_load_dwordx4 v[112:115], v247, s[28:31], s49 offen sc1\n\t"
            "s_waitcnt lgkmcnt(6)\n\t"
            "v_mfma_f32_16x16x32_bf16 v[192:195], v[4:7], v[148:151], v[192:195]\n\t"
            "s_add_u32 s49, s70, 29696\n\t"
            "buffer_load_dwordx4 v[116:119], v247, s[28:31], s49 offen sc1\n\t"
            "s_waitcnt lgkmcnt(5)\n\t"
            "v_mfma_f32_16x16x32_bf16 v[192:195], v[8:11], v[152:155], v[192:195]\n\t"
            "s_waitcnt vmcnt(18)\n\t"
            "s_add_u32 s49, s70, 30720\n\t"
            "buffer_load_dwordx4 v[120:123], v247, s[28:31], s49 offen sc1\n\t"
            "s_waitcnt lgkmcnt(4)\n\t"
            "v_mfma_f32_16x16x32_bf16 v[192:195], v[12:15], v[156:159], v[192:195]\n\t"
            "s_add_u32 s49, s70, 31744\n\t"
            "buffer_load_dwordx4 v[124:127], v247, s[28:31], s49 offen sc1\n\t"
            "s_waitcnt lgkmcnt(3)\n\t"
            "v_mfma_f32_16x16x32_bf16 v[192:195], v[16:19], v[160:163], v[192:195]\n\t"
            "s_add_u32 s49, s70, 32768\n\t"
            "buffer_load_dwordx4 v[128:131], v247, s[28:31], s49 offen sc1\n\t"
            "s_waitcnt lgkmcnt(2)\n\t"
            "v_mfma_f32_16x16x32_bf16 v[192:195], v[20:23], v[164:167], v[192:195]\n\t"
            "s_add_u32 s49, s70, 33792\n\t"
            "buffer_load_dwordx4 v[132:135], v247, s[28:31], s49 offen sc1\n\t"
            "v_mfma_f32_16x16x32_bf16 v[196:199], v[24:27], v[144:147], v[196:199]\n\t"
            "s_add_u32 s49, s70, 34816\n\t"
            "buffer_load_dwordx4 v[136:139], v247, s[28:31], s49 offen sc1\n\t"
            "v_mfma_f32_16x16x32_bf16 v[196:199], v[28:31], v[148:151], v[196:199]\n\t"
            "s_add_u32 s49, s70, 35840\n\t"
            "buffer_load_dwordx4 v[140:143], v247, s[28:31], s49 offen sc1\n\t"
            "v_mfma_f32_16x16x32_bf16 v[196:199], v[32:35], v[152:155], v[196:199]\n\t"
            "s_waitcnt vmcnt(18)\n\t"
            "s_waitcnt lgkmcnt(1)\n\t"
            "v_ashrrev_i32 v237, 31, v248\n\t"
            "v_mfma_f32_16x16x32_bf16 v[196:199], v[36:39], v[156:159], v[196:199]\n\t"
            "v_and_b32 v237, 1177344, v237\n\t"
            "v_sub_u32 v236, v236, v237\n\t"
            "v_mfma_f32_16x16x32_bf16 v[196:199], v[40:43], v[160:163], v[196:199]\n\t"
            "v_bfe_i32 v237, v248, 30, 1\n\t"
            "v_and_b32 v237, 2304, v237\n\t"
            "v_mfma_f32_16x16x32_bf16 v[196:199], v[44:47], v[164:167], v[196:199]\n\t"
            "v_cvt_pk_bf16_f32 v192, v192, v193\n\t"
            "v_sub_u32 v236, v236, v237\n\t"
            "v_mfma_f32_16x16x32_bf16 v[224:227], v[48:51], v[144:147], v[224:227]\n\t"
            "v_and_b32 v248, 0x7ffffff, v248\n\t"
            "v_cvt_pk_bf16_f32 v193, v194, v195\n\t"
            "v_mfma_f32_16x16x32_bf16 v[224:227], v[52:55], v[148:151], v[224:227]\n\t"
            "v_add_u32 v248, v248, v236\n\t"
            "v_lshrrev_b32 v236, 4, v247\n\t"
            "v_mfma_f32_16x16x32_bf16 v[224:227], v[56:59], v[152:155], v[224:227]\n\t"
            "v_cvt_pk_bf16_f32 v194, v196, v197\n\t"
            "v_cvt_pk_bf16_f32 v195, v198, v199\n\t"
            "v_mfma_f32_16x16x32_bf16 v[224:227], v[60:63], v[156:159], v[224:227]\n\t"
            "buffer_store_dwordx4 v[192:195], v248, s[40:43], 0 offen sc1\n\t"
            "v_mfma_f32_16x16x32_bf16 v[224:227], v[64:67], v[160:163], v[224:227]\n\t"
            "s_waitcnt vmcnt(13)\n\t"
            "v_and_b32 v236, 48, v236\n\t"
            "s_waitcnt lgkmcnt(0)\n\t"
            "v_ashrrev_i32 v237, 31, v249\n\t"
            "v_mfma_f32_16x16x32_bf16 v[224:227], v[68:71], v[164:167], v[224:227]\n\t"
            "v_add_u32 v236, s20, v236\n\t"
            "v_and_b32 v237, 1177344, v237\n\t"
            "v_mfma_f32_16x16x32_bf16 v[228:231], v[72:75], v[144:147], v[228:231]\n\t"
            "v_sub_u32 v236, v236, v237\n\t"
            "v_bfe_i32 v237, v249, 30, 1\n\t"
            "v_mfma_f32_16x16x32_bf16 v[228:231], v[76:79], v[148:151], v[228:231]\n\t"
            "v_and_b32 v237, 2304, v237\n\t"
            "v_sub_u32 v236, v236, v237\n\t"
            "v_mfma_f32_16x16x32_bf16 v[228:231], v[80:83], v[152:155], v[228:231]\n\t"
            "v_cvt_pk_bf16_f32 v224, v224, v225\n\t"
            "v_cvt_pk_bf16_f32 v225, v226, v227\n\t"
            "v_mfma_f32_16x16x32_bf16 v[228:231], v[84:87], v[156:159], v[228:231]\n\t"
            "v_and_b32 v249, 0x7ffffff, v249\n\t"
            "v_add_u32 v249, v249, v236\n\t"
            "v_mfma_f32_16x16x32_bf16 v[228:231], v[88:91], v[160:163], v[228:231]\n\t"
            "v_mfma_f32_16x16x32_bf16 v[228:231], v[92:95], v[164:167], v[228:231]\n\t"
            "s_nop 7\n\t"
            "v_cvt_pk_bf16_f32 v226, v228, v229\n\t"
            "v_cvt_pk_bf16_f32 v227, v230, v231\n\t"
            "buffer_store_dwordx4 v[224:227], v248, s[40:43], 0 offen offset:64 sc1\n\t"
            "s_nop 7\n\t"
            "v_mov_b32 v168, s23\n\t"
            ".Ldv%=:\n\t"
            "ds_read_b128 v[172:175], v168 offset:96\n\t"
            "s_waitcnt lgkmcnt(0)\n\t"
            "v_or_b32 v172, v172, v173\n\t"
            "v_or3_b32 v172, v172, v174, v175\n\t"
            "v_cmp_gt_i32 vcc, 0, v172\n\t"
            "s_nop 4\n\t"
            "s_cbranch_vccnz .Ldv%=\n\t"
            "s_nop 7\n\t"
            "ds_read_b128 v[168:171], v242 offset:59904\n\t"
            "v_lshlrev_b32 v232, 16, v212\n\t"
            "v_and_b32 v233, 0xffff0000, v212\n\t"
            "v_lshlrev_b32 v234, 16, v213\n\t"
            "v_and_b32 v235, 0xffff0000, v213\n\t"
            "s_waitcnt vmcnt(32)\n\t"
            "v_lshlrev_b32 v192, 16, v214\n\t"
            "v_and_b32 v193, 0xffff0000, v214\n\t"
            "v_lshlrev_b32 v194, 16, v215\n\t"
            "v_and_b32 v195, 0xffff0000, v215\n\t"
            "v_lshlrev_b32 v196, 16, v216\n\t"
            "v_and_b32 v197, 0xffff0000, v216\n\t"
            "v_lshlrev_b32 v198, 16, v217\n\t"
            "v_and_b32 v199, 0xffff0000, v217\n\t"
            "v_lshlrev_b32 v224, 16, v218\n\t"
            "v_and_b32 v225, 0xffff0000, v218\n\t"
            "v_lshlrev_b32 v226, 16, v219\n\t"
            "v_and_b32 v227, 0xffff0000, v219\n\t"
            "v_lshlrev_b32 v228, 16, v208\n\t"
            "v_and_b32 v229, 0xffff0000, v208\n\t"
            "v_lshlrev_b32 v230, 16, v209\n\t"
            "v_and_b32 v231, 0xffff0000, v209\n\t"
            "ds_read_b128 v[172:175], v243 offset:59904\n\t"
            "ds_read_b128 v[176:179], v242 offset:60032\n\t"
            "ds_read_b128 v[180:183], v243 offset:60032\n\t"
            "ds_read_b128 v[184:187], v242 offset:60160\n\t"
            "s_waitcnt lgkmcnt(4)\n\t"
            "v_mfma_f32_16x16x32_bf16 v[232:235], v[0:3], v[168:171], v[232:235]\n\t"
            "ds_read_b128 v[188:191], v243 offset:60160\n\t"
            "s_waitcnt lgkmcnt(4)\n\t"
            "v_mfma_f32_16x16x32_bf16 v[232:235], v[4:7], v[172:175], v[232:235]\n\t"
            "s_waitcnt lgkmcnt(3)\n\t"
            "v_mfma_f32_16x16x32_bf16 v[232:235], v[8:11], v[176:179], v[232:235]\n\t"
            "s_waitcnt vmcnt(26)\n\t"
            "s_waitcnt lgkmcnt(2)\n\t"
            "v_mfma_f32_16x16x32_bf16 v[232:235], v[12:15], v[180:183], v[232:235]\n\t"
            "s_waitcnt lgkmcnt(1)\n\t"
            "v_mfma_f32_16x16x32_bf16 v[232:235], v[16:19], v[184:187], v[232:235]\n\t"
            "s_waitcnt lgkmcnt(0)\n\t"
            "v_mfma_f32_16x16x32_bf16 v[232:235], v[20:23], v[188:191], v[232:235]\n\t"
            "v_mfma_f32_16x16x32_bf16 v[192:195], v[24:27], v[168:171], v[192:195]\n\t"
            "v_mfma_f32_16x16x32_bf16 v[192:195], v[28:31], v[172:175], v[192:195]\n\t"
            "v_mfma_f32_16x16x32_bf16 v[192:195], v[32:35], v[176:179], v[192:195]\n\t"
            "s_waitcnt vmcnt(20)\n\t"
            "v_mfma_f32_16x16x32_bf16 v[192:195], v[36:39], v[180:183], v[192:195]\n\t"
            "v_mfma_f32_16x16x32_bf16 v[192:195], v[40:43], v[184:187], v[192:195]\n\t"
            "s_nop 1\n\t"
            "v_cvt_pk_bf16_f32 v232, v232, v233\n\t"
            "v_mfma_f32_16x16x32_bf16 v[192:195], v[44:47], v[188:191], v[192:195]\n\t"
            "v_cvt_pk_bf16_f32 v233, v234, v235\n\t"
            "v_mfma_f32_16x16x32_bf16 v[196:199], v[48:51], v[168:171], v[196:199]\n\t"
            "v_mfma_f32_16x16x32_bf16 v[196:199], v[52:55], v[172:175], v[196:199]\n\t"
            "v_mfma_f32_16x16x32_bf16 v[196:199], v[56:59], v[176:179], v[196:199]\n\t"
            "v_mfma_f32_16x16x32_bf16 v[196:199], v[60:63], v[180:183], v[196:199]\n\t"
            "v_mfma_f32_16x16x32_bf16 v[196:199], v[64:67], v[184:187], v[196:199]\n\t"
            "s_nop 1\n\t"
            "v_cvt_pk_bf16_f32 v234, v192, v193\n\t"
            "v_mfma_f32_16x16x32_bf16 v[196:199], v[68:71], v[188:191], v[196:199]\n\t"
            "v_cvt_pk_bf16_f32 v235, v194, v195\n\t"
            "v_lshlrev_b32 v192, 16, v220\n\t"
            "v_and_b32 v193, 0xffff0000, v220\n\t"
            "v_lshlrev_b32 v194, 16, v221\n\t"
            "v_and_b32 v195, 0xffff0000, v221\n\t"
            "buffer_store_dwordx4 v[232:235], v249, s[40:43], 0 offen sc1\n\t"
            "s_waitcnt vmcnt(15)\n\t"
            "v_mfma_f32_16x16x32_bf16 v[224:227], v[72:75], v[168:171], v[224:227]\n\t"
            "v_cvt_pk_bf16_f32 v196, v196, v197\n\t"
            "v_cvt_pk_bf16_f32 v197, v198, v199\n\t"
            "v_mfma_f32_16x16x32_bf16 v[224:227], v[76:79], v[172:175], v[224:227]\n\t"
            "v_lshlrev_b32 v232, 16, v210\n\t"
            "v_and_b32 v233, 0xffff0000, v210\n\t"
            "v_mfma_f32_16x16x32_bf16 v[224:227], v[80:83], v[176:179], v[224:227]\n\t"
            "s_waitcnt vmcnt(9)\n\t"
            "v_lshlrev_b32 v234, 16, v211\n\t"
            "v_mfma_f32_16x16x32_bf16 v[224:227], v[84:87], v[180:183], v[224:227]\n\t"
            "v_and_b32 v235, 0xffff0000, v211\n\t"
            "v_mfma_f32_16x16x32_bf16 v[224:227], v[88:91], v[184:187], v[224:227]\n\t"
            "v_mfma_f32_16x16x32_bf16 v[224:227], v[92:95], v[188:191], v[224:227]\n\t"
            "v_mfma_f32_16x16x32_bf16 v[228:231], v[96:99], v[144:147], v[228:231]\n\t"
            "v_mfma_f32_16x16x32_bf16 v[228:231], v[100:103], v[148:151], v[228:231]\n\t"
            "v_mfma_f32_16x16x32_bf16 v[228:231], v[104:107], v[152:155], v[228:231]\n\t"
            "v_mfma_f32_16x16x32_bf16 v[228:231], v[108:111], v[156:159], v[228:231]\n\t"
            "v_mfma_f32_16x16x32_bf16 v[228:231], v[112:115], v[160:163], v[228:231]\n\t"
            "v_mfma_f32_16x16x32_bf16 v[228:231], v[116:119], v[164:167], v[228:231]\n\t"
            "s_nop 1\n\t"
            "v_cvt_pk_bf16_f32 v198, v224, v225\n\t"
            "v_cvt_pk_bf16_f32 v199, v226, v227\n\t"
            "buffer_store_dwordx4 v[196:199], v249, s[40:43], 0 offen offset:64 sc1\n\t"
            "s_waitcnt vmcnt(4)\n\t"
            "v_mfma_f32_16x16x32_bf16 v[232:235], v[120:123], v[144:147], v[232:235]\n\t"
            "v_lshlrev_b32 v196, 16, v222\n\t"
            "v_and_b32 v197, 0xffff0000, v222\n\t"
            "v_mfma_f32_16x16x32_bf16 v[232:235], v[124:127], v[148:151], v[232:235]\n\t"
            "v_cvt_pk_bf16_f32 v228, v228, v229\n\t"
            "v_cvt_pk_bf16_f32 v229, v230, v231\n\t"
            "v_mfma_f32_16x16x32_bf16 v[232:235], v[128:131], v[152:155], v[232:235]\n\t"
            "s_waitcnt vmcnt(10)\n\t"
            "v_lshlrev_b32 v198, 16, v223\n\t"
            "v_mfma_f32_16x16x32_bf16 v[232:235], v[132:135], v[156:159], v[232:235]\n\t"
            "v_and_b32 v199, 0xffff0000, v223\n\t"
            "v_mfma_f32_16x16x32_bf16 v[232:235], v[136:139], v[160:163], v[232:235]\n\t"
            "v_mfma_f32_16x16x32_bf16 v[232:235], v[140:143], v[164:167], v[232:235]\n\t"
            "v_mfma_f32_16x16x32_bf16 v[192:195], v[96:99], v[168:171], v[192:195]\n\t"
            "v_mfma_f32_16x16x32_bf16 v[192:195], v[100:103], v[172:175], v[192:195]\n\t"
            "v_mfma_f32_16x16x32_bf16 v[192:195], v[104:107], v[176:179], v[192:195]\n\t"
            "v_mfma_f32_16x16x32_bf16 v[192:195], v[108:111], v[180:183], v[192:195]\n\t"
            "v_mfma_f32_16x16x32_bf16 v[192:195], v[112:115], v[184:187], v[192:195]\n\t"
            "v_mfma_f32_16x16x32_bf16 v[192:195], v[116:119], v[188:191], v[192:195]\n\t"
            "s_nop 1\n\t"
            "v_cvt_pk_bf16_f32 v230, v232, v233\n\t"
            "v_cvt_pk_bf16_f32 v231, v234, v235\n\t"
            "buffer_store_dwordx4 v[228:231], v248, s[40:43], 0 offen offset:128 sc1\n\t"
            "s_waitcnt vmcnt(5)\n\t"
            "v_mfma_f32_16x16x32_bf16 v[196:199], v[120:123], v[168:171], v[196:199]\n\t"
            "v_mfma_f32_16x16x32_bf16 v[196:199], v[124:127], v[172:175], v[196:199]\n\t"
            "v_cvt_pk_bf16_f32 v192, v192, v193\n\t"
            "v_mfma_f32_16x16x32_bf16 v[196:199], v[128:131], v[176:179], v[196:199]\n\t"
            "v_cvt_pk_bf16_f32 v193, v194, v195\n\t"
            "v_mfma_f32_16x16x32_bf16 v[196:199], v[132:135], v[180:183], v[196:199]\n\t"
            "v_mfma_f32_16x16x32_bf16 v[196:199], v[136:139], v[184:187], v[196:199]\n\t"
            "v_mfma_f32_16x16x32_bf16 v[196:199], v[140:143], v[188:191], v[196:199]\n\t"
            "s_nop 7\n\t"
            "v_cvt_pk_bf16_f32 v194, v196, v197\n\t"
            "v_cvt_pk_bf16_f32 v195, v198, v199\n\t"
            "buffer_store_dwordx4 v[192:195], v249, s[40:43], 0 offen offset:128 sc1\n\t"
            "s_nop 7\n\t"
            "s_branch .Lre%=\n\t"
            ".Lrp%=:\n\t"
            "v_add_u32 v236, s19, v252\n\t"
            "ds_write_b128 v236, v[244:247]\n\t"
            "v_add_u32 v236, s48, v244\n\t"
            "v_add_u32 v237, s48, v245\n\t"
            "v_add_u32 v248, s48, v246\n\t"
            "ds_read_b128 v[200:203], v236\n\t"
            "ds_read_b128 v[204:207], v237\n\t"
            "ds_read_b128 v[208:211], v248\n\t"
            "ds_read_b128 v[212:215], v236 offset:6144\n\t"
            "ds_read_b128 v[216:219], v237 offset:6144\n\t"
            "ds_read_b128 v[220:223], v248 offset:6144\n\t"
            "ds_read_b128 v[224:227], v236 offset:12288\n\t"
            "ds_read_b128 v[228:231], v237 offset:12288\n\t"
            "ds_read_b128 v[232:235], v248 offset:12288\n\t"
            "s_waitcnt lgkmcnt(0)\n\t"
            "s_barrier\n\t"
            "s_add_u32 s45, s46, s27\n\t"
            "s_mov_b32 s46, s47\n\t"
            "s_mov_b32 s47, s68\n\t"
            "s_xor_b32 s48, s48, s71\n\t"
            "s_xor_b32 s67, s67, s71\n\t"
            "s_add_u32 s76, s76, 4\n\t"
            "s_lshl_b32 s56, s56, 2\n\t"
            "s_lshr_b32 s49, s26, 30\n\t"
            "s_or_b32 s56, s56, s49\n\t"
            "s_cmp_ge_u32 s76, s24\n\t"
            "s_cselect_b64 vcc, -1, 0\n\t"
            "s_add_u32 s49, s33, 0\n\t"
            "buffer_load_dwordx4 v[0:3], v247, s[28:31], s49 offen sc1\n\t"
            "s_add_u32 s49, s33, 1024\n\t"
            "buffer_load_dwordx4 v[4:7], v247, s[28:31], s49 offen sc1\n\t"
            "s_add_u32 s49, s33, 2048\n\t"
            "buffer_load_dwordx4 v[8:11], v247, s[28:31], s49 offen sc1\n\t"
            "s_add_u32 s49, s33, 3072\n\t"
            "buffer_load_dwordx4 v[12:15], v247, s[28:31], s49 offen sc1\n\t"
            "s_add_u32 s49, s33, 4096\n\t"
            "buffer_load_dwordx4 v[16:19], v247, s[28:31], s49 offen sc1\n\t"
            "s_add_u32 s49, s33, 5120\n\t"
            "buffer_load_dwordx4 v[20:23], v247, s[28:31], s49 offen sc1\n\t"
            "s_add_u32 s49, s33, 6144\n\t"
            "buffer_load_dwordx4 v[24:27], v247, s[28:31], s49 offen sc1\n\t"
            "s_add_u32 s49, s33, 7168\n\t"
            "buffer_load_dwordx4 v[28:31], v247, s[28:31], s49 offen sc1\n\t"
            "s_add_u32 s49, s33, 8192\n\t"
            "buffer_load_dwordx4 v[32:35], v247, s[28:31], s49 offen sc1\n\t"
            "s_add_u32 s49, s33, 9216\n\t"
            "buffer_load_dwordx4 v[36:39], v247, s[28:31], s49 offen sc1\n\t"
            "s_add_u32 s49, s33, 10240\n\t"
            "buffer_load_dwordx4 v[40:43], v247, s[28:31], s49 offen sc1\n\t"
            "s_add_u32 s49, s33, 11264\n\t"
            "buffer_load_dwordx4 v[44:47], v247, s[28:31], s49 offen sc1\n\t"
            "s_add_u32 s49, s33, 12288\n\t"
            "buffer_load_dwordx4 v[48:51], v247, s[28:31], s49 offen sc1\n\t"
            "s_add_u32 s49, s33, 13312\n\t"
            "buffer_load_dwordx4 v[52:55], v247, s[28:31], s49 offen sc1\n\t"
            "s_add_u32 s49, s33, 14336\n\t"
            "buffer_load_dwordx4 v[56:59], v247, s[28:31], s49 offen sc1\n\t"
            "s_add_u32 s49, s33, 15360\n\t"
            "buffer_load_dwordx4 v[60:63], v247, s[28:31], s49 offen sc1\n\t"
            "s_add_u32 s49, s33, 16384\n\t"
            "buffer_load_dwordx4 v[64:67], v247, s[28:31], s49 offen sc1\n\t"
            "s_add_u32 s49, s33, 17408\n\t"
            "buffer_load_dwordx4 v[68:71], v247, s[28:31], s49 offen sc1\n\t"
            "ds_read_b128 v[168:171], v254\n\t"
            "ds_read_b128 v[172:175], v254 offset:64\n\t"
            "s_waitcnt vmcnt(0)\n\t"
            "ds_read_b64 v[144:145], v253\n\t"
            "ds_read_b64 v[146:147], v253 offset:96\n\t"
            "ds_read_b64 v[148:149], v253 offset:1744\n\t"
            "ds_read_b64 v[150:151], v253 offset:1840\n\t"
            "ds_read_b64 v[152:153], v253 offset:8\n\t"
            "ds_read_b64 v[154:155], v253 offset:104\n\t"
            "ds_read_b64 v[156:157], v253 offset:1752\n\t"
            "ds_read_b64 v[158:159], v253 offset:1848\n\t"
            "ds_read_b64 v[160:161], v253 offset:16\n\t"
            "ds_read_b64 v[162:163], v253 offset:112\n\t"
            "ds_read_b64 v[164:165], v253 offset:1760\n\t"
            "ds_read_b64 v[166:167], v253 offset:1856\n\t"
            "s_waitcnt lgkmcnt(0)\n\t"
            "s_nop 7\n\t"
            ".p2align 6\n\t"
            ".Lwb_loop%=:\n\t"
            "s_setprio 3\n\t"
            "v_mov_b64 v[176:177], 0\n\t"
            "v_mov_b64 v[178:179], 0\n\t"
            "v_mov_b32 v249, s76\n\t"
            "v_mov_b64 v[180:181], 0\n\t"
            "s_waitcnt lgkmcnt(7)\n\t"
            "v_smfmac_f32_16x16x64_bf16 v[176:179], v[168:171], v[144:151], v255\n\t"
            "ds_read_b128 v[168:171], v254 offset:128\n\t"
            "v_mov_b64 v[182:183], 0\n\t"
            "v_mov_b64 v[184:185], 0\n\t"
            "v_mov_b64 v[186:187], 0\n\t"
            "s_waitcnt lgkmcnt(5)\n\t"
            "v_smfmac_f32_16x16x64_bf16 v[180:183], v[172:175], v[152:159], v255\n\t"
            "ds_read_b32 v249, v249\n\t"
            "v_mov_b64 v[188:189], 0\n\t"
            "v_mov_b64 v[190:191], 0\n\t"
            "v_cvt_pk_bf16_f32 v176, v176, v177\n\t"
            "v_cvt_pk_bf16_f32 v177, v178, v179\n\t"
            "v_add_u32 v236, s67, v250\n\t"
            "v_add_u32 v248, s67, v251\n\t"
            "v_mfma_f32_4x4x4_16b_bf16 v[196:199], v[176:177], v[176:177], 0\n\t"
            "ds_read_b64 v[144:145], v253 offset:24\n\t"
            "v_cvt_pk_bf16_f32 v178, v180, v181\n\t"
            "v_cvt_pk_bf16_f32 v179, v182, v183\n\t"
            "s_barrier\n\t"
            "v_mov_b64 v[180:181], 0\n\t"
            "v_mov_b64 v[182:183], 0\n\t"
            "ds_read_b64 v[146:147], v253 offset:120\n\t"
            "ds_read_b64 v[148:149], v253 offset:1768\n\t"
            "ds_read_b64 v[150:151], v253 offset:1864\n\t"
            "ds_read_b128 v[172:175], v254 offset:192\n\t"
            "s_waitcnt lgkmcnt(6)\n\t"
            "v_smfmac_f32_16x16x64_bf16 v[184:187], v[168:171], v[160:167], v255\n\t"
            "ds_read_b64 v[152:153], v253 offset:32\n\t"
            "v_mfma_f32_4x4x4_16b_bf16 v[196:199], v[178:179], v[178:179], v[196:199]\n\t"
            "ds_read_b64 v[154:155], v253 offset:128\n\t"
            "v_mfma_f32_16x16x32_bf16 v[192:195], v[238:241], v[176:179], 0\n\t"
            "ds_read_b64 v[156:157], v253 offset:1776\n\t"
            "s_waitcnt lgkmcnt(8)\n\t"
            "v_readfirstlane_b32 s26, v249\n\t"
            "ds_read_b64 v[158:159], v253 offset:1872\n\t"
            "s_and_b32 s68, s26, 0x7ffffff\n\t"
            "s_add_u32 s68, s68, s78\n\t"
            "s_and_b32 s69, s26, 0x38000000\n\t"
            "s_or_b32 s69, s69, s39\n\t"
            "s_bitcmp1_b32 s26, 27\n\t"
            "s_cselect_b32 s79, 2.0, s68\n\t"
            "ds_read_b128 v[168:171], v254 offset:256\n\t"
            "s_and_b32 s57, s56, 3\n\t"
            "s_lshl_b32 s58, s57, 3\n\t"
            "s_lshr_b32 m0, s54, s58\n\t"
            "ds_read_b64 v[160:161], v253 offset:40\n\t"
            "s_lshl_b32 s58, s57, 4\n\t"
            "s_lshr_b64 s[36:37], s[50:51], s58\n\t"
            "s_and_b32 s57, s36, 0xffff\n\t"
            "s_add_u32 s57, s57, s67\n\t"
            "ds_read_b64 v[162:163], v253 offset:136\n\t"
            "s_waitcnt lgkmcnt(7)\n\t"
            "v_smfmac_f32_16x16x64_bf16 v[188:191], v[172:175], v[144:151], v255\n\t"
            "s_lshr_b64 s[36:37], s[52:53], s58\n\t"
            "s_and_b32 s58, s36, 0xffff\n\t"
            "s_add_u32 s58, s58, s67\n\t"
            "ds_read_b64 v[164:165], v253 offset:1784\n\t"
            "s_and_b32 s59, s50, 0xffff\n\t"
            "s_add_u32 s59, s59, s67\n\t"
            "s_and_b32 s38, s52, 0xffff\n\t"
            "s_add_u32 s38, s38, s67\n\t"
            "ds_read_b64 v[166:167], v253 offset:1880\n\t"
            "v_writelane_b32 v236, s59, m0\n\t"
            "v_writelane_b32 v236, s57, 63\n\t"
            "v_cvt_pk_bf16_f32 v184, v184, v185\n\t"
            "v_cvt_pk_bf16_f32 v185, v186, v187\n\t"
            "v_cvt_pk_bf16_f32 v186, v188, v189\n\t"
            "v_cvt_pk_bf16_f32 v187, v190, v191\n\t"
            "v_mfma_f32_4x4x4_16b_bf16 v[196:199], v[184:185], v[184:185], v[196:199]\n\t"
            "ds_read_b128 v[172:175], v254 offset:320\n\t"
            "v_xor_b32 v237, 16, v236\n\t"
            "v_mov_b64 v[188:189], 0\n\t"
            "v_mov_b64 v[190:191], 0\n\t"
            "v_writelane_b32 v248, s38, m0\n\t"
            "v_writelane_b32 v248, s58, 63\n\t"
            "ds_read_b64 v[144:145], v253 offset:48\n\t"
            "ds_read_b64 v[146:147], v253 offset:144\n\t"
            "ds_read_b64 v[148:149], v253 offset:1792\n\t"
            "ds_read_b64 v[150:151], v253 offset:1888\n\t"
            "ds_read_b32 v249, v252 offset:1024\n\t"
            "s_waitcnt lgkmcnt(14)\n\t"
            "ds_write_b128 v236, v[176:179]\n\t"
            "v_mov_b64 v[176:177], 0\n\t"
            "v_mov_b64 v[178:179], 0\n\t"
            "s_waitcnt lgkmcnt(11)\n\t"
            "s_nop 0\n\t"
            "v_smfmac_f32_16x16x64_bf16 v[176:179], v[168:171], v[152:159], v255\n\t"
            "ds_read_b128 v[168:171], v254 offset:384\n\t"
            "v_mfma_f32_4x4x4_16b_bf16 v[196:199], v[186:187], v[186:187], v[196:199]\n\t"
            "ds_read_b64 v[152:153], v253 offset:56\n\t"
            "v_mfma_f32_16x16x32_bf16 v[192:195], v[238:241], v[184:187], v[192:195]\n\t"
            "ds_read_b64 v[154:155], v253 offset:152\n\t"
            "s_waitcnt lgkmcnt(9)\n\t"
            "v_smfmac_f32_16x16x64_bf16 v[180:183], v[172:175], v[160:167], v255\n\t"
            "ds_read_b64 v[156:157], v253 offset:1800\n\t"
            "ds_read_b64 v[158:159], v253 offset:1896\n\t"
            "v_cvt_pk_bf16_f32 v176, v176, v177\n\t"
            "v_cvt_pk_bf16_f32 v177, v178, v179\n\t"
            "s_nop 1\n\t"
            "v_mfma_f32_4x4x4_16b_bf16 v[196:199], v[176:177], v[176:177], v[196:199]\n\t"
            "ds_read_b128 v[172:175], v254 offset:448\n\t"
            "s_waitcnt lgkmcnt(7)\n\t"
            "v_and_b32 v249, s69, v249\n\t"
            "v_cvt_pk_bf16_f32 v178, v180, v181\n\t"
            "v_cvt_pk_bf16_f32 v179, v182, v183\n\t"
            "v_mov_b64 v[180:181], 0\n\t"
            "v_mov_b64 v[182:183], 0\n\t"
            "ds_read_b64 v[160:161], v253 offset:64\n\t"
            "ds_read_b64 v[162:163], v253 offset:160\n\t"
            "ds_read_b64 v[164:165], v253 offset:1808\n\t"
            "ds_read_b64 v[166:167], v253 offset:1904\n\t"
            "ds_write_b128 v237, v[184:187]\n\t"
            "v_mov_b64 v[184:185], 0\n\t"
            "v_mov_b64 v[186:187], 0\n\t"
            "s_waitcnt lgkmcnt(10)\n\t"
            "s_nop 0\n\t"
            "v_smfmac_f32_16x16x64_bf16 v[184:187], v[168:171], v[144:151], v255\n\t"
            "ds_read_b128 v[168:171], v254 offset:512\n\t"
            "v_mfma_f32_4x4x4_16b_bf16 v[196:199], v[178:179], v[178:179], v[196:199]\n\t"
            "ds_read_b64 v[144:145], v253 offset:72\n\t"
            "v_mfma_f32_16x16x32_bf16 v[192:195], v[238:241], v[176:179], v[192:195]\n\t"
            "ds_read_b64 v[146:147], v253 offset:168\n\t"
            "s_waitcnt lgkmcnt(8)\n\t"
            "v_smfmac_f32_16x16x64_bf16 v[188:191], v[172:175], v[152:159], v255\n\t"
            "ds_read_b64 v[148:149], v253 offset:1816\n\t"
            "ds_read_b64 v[150:151], v253 offset:1912\n\t"
            "v_cvt_pk_bf16_f32 v184, v184, v185\n\t"
            "v_cvt_pk_bf16_f32 v185, v186, v187\n\t"
            "s_nop 1\n\t"
            "v_mfma_f32_4x4x4_16b_bf16 v[196:199], v[184:185], v[184:185], v[196:199]\n\t"
            "ds_read_b128 v[172:175], v254 offset:576\n\t"
            "ds_read_b64 v[152:153], v253 offset:80\n\t"
            "ds_read_b64 v[154:155], v253 offset:176\n\t"
            "v_cvt_pk_bf16_f32 v186, v188, v189\n\t"
            "v_cvt_pk_bf16_f32 v187, v190, v191\n\t"
            "v_mov_b64 v[188:189], 0\n\t"
            "v_mov_b64 v[190:191], 0\n\t"
            "ds_read_b64 v[156:157], v253 offset:1824\n\t"
            "ds_read_b64 v[158:159], v253 offset:1920\n\t"
            "s_waitcnt lgkmcnt(14)\n\t"
            "ds_write_b128 v236, v[176:179] offset:128\n\t"
            "v_mov_b64 v[176:177], 0\n\t"
            "v_mov_b64 v[178:179], 0\n\t"
            "s_waitcnt lgkmcnt(10)\n\t"
            "s_nop 0\n\t"
            "v_smfmac_f32_16x16x64_bf16 v[176:179], v[168:171], v[160:167], v255\n\t"
            "ds_read_b128 v[168:171], v254 offset:640\n\t"
            "v_mfma_f32_4x4x4_16b_bf16 v[196:199], v[186:187], v[186:187], v[196:199]\n\t"
            "ds_read_b64 v[160:161], v253 offset:88\n\t"
            "v_mfma_f32_16x16x32_bf16 v[192:195], v[238:241], v[184:187], v[192:195]\n\t"
            "ds_read_b64 v[162:163], v253 offset:184\n\t"
            "s_waitcnt lgkmcnt(8)\n\t"
            "v_smfmac_f32_16x16x64_bf16 v[180:183], v[172:175], v[144:151], v255\n\t"
            "ds_read_b64 v[164:165], v253 offset:1832\n\t"
            "ds_read_b64 v[166:167], v253 offset:1928\n\t"
            "v_cvt_pk_bf16_f32 v176, v176, v177\n\t"
            "v_cvt_pk_bf16_f32 v177, v178, v179\n\t"
            "s_nop 1\n\t"
            "v_mfma_f32_4x4x4_16b_bf16 v[196:199], v[176:177], v[176:177], v[196:199]\n\t"
            "ds_read_b128 v[172:175], v254 offset:704\n\t"
            "ds_read_b128 v[144:147], v242\n\t"
            "ds_read_b128 v[148:151], v243\n\t"
            "v_cvt_pk_bf16_f32 v178, v180, v181\n\t"
            "v_cvt_pk_bf16_f32 v179, v182, v183\n\t"
            "v_lshlrev_b32 v180, 16, v202\n\t"
            "v_and_b32 v181, 0xffff0000, v202\n\t"
            "v_lshlrev_b32 v182, 16, v203\n\t"
            "v_and_b32 v183, 0xffff0000, v203\n\t"
            "ds_write_b128 v237, v[184:187] offset:128\n\t"
            "v_mov_b64 v[184:185], 0\n\t"
            "v_mov_b64 v[186:187], 0\n\t"
            "s_waitcnt lgkmcnt(8)\n\t"
            "s_nop 0\n\t"
            "v_smfmac_f32_16x16x64_bf16 v[184:187], v[168:171], v[152:159], v255\n\t"
            "ds_read_b128 v[152:155], v242 offset:128\n\t"
            "v_mfma_f32_4x4x4_16b_bf16 v[196:199], v[178:179], v[178:179], v[196:199]\n\t"
            "ds_read_b128 v[156:159], v243 offset:128\n\t"
            "v_mfma_f32_16x16x32_bf16 v[192:195], v[238:241], v[176:179], v[192:195]\n\t"
            "ds_read_b128 v[168:171], v242 offset:6144\n\t"
            "s_waitcnt lgkmcnt(6)\n\t"
            "v_smfmac_f32_16x16x64_bf16 v[188:191], v[172:175], v[160:167], v255\n\t"
            "ds_read_b128 v[160:163], v242 offset:256\n\t"
            "ds_read_b128 v[164:167], v243 offset:256\n\t"
            "v_cvt_pk_bf16_f32 v184, v184, v185\n\t"
            "v_cvt_pk_bf16_f32 v185, v186, v187\n\t"
            "s_nop 1\n\t"
            "v_mfma_f32_4x4x4_16b_bf16 v[196:199], v[184:185], v[184:185], v[196:199]\n\t"
            "ds_read_b128 v[172:175], v243 offset:6144\n\t"
            "s_mov_b32 exec_hi, 0x3fffff\n\t"
            "s_add_u32 m0, s66, 0\n\t"
            "s_add_u32 s49, s79, 0\n\t"
            "buffer_load_dwordx4 v249, s[72:75], s49 offen lds\n\t"
            "s_mov_b32 exec_hi, -1\n\t"
            "v_cvt_pk_bf16_f32 v186, v188, v189\n\t"
            "v_cvt_pk_bf16_f32 v187, v190, v191\n\t"
            "v_lshlrev_b32 v188, 16, v206\n\t"
            "v_and_b32 v189, 0xffff0000, v206\n\t"
            "v_mfma_f32_4x4x4_16b_bf16 v[196:199], v[186:187], v[186:187], v[196:199]\n\t"
            "ds_write_b128 v236, v[176:179] offset:256\n\t"
            "v_mfma_f32_16x16x32_bf16 v[192:195], v[238:241], v[184:187], v[192:195]\n\t"
            "v_lshlrev_b32 v176, 16, v200\n\t"
            "v_and_b32 v177, 0xffff0000, v200\n\t"
            "v_lshlrev_b32 v178, 16, v201\n\t"
            "v_and_b32 v179, 0xffff0000, v201\n\t"
            "v_lshlrev_b32 v190, 16, v207\n\t"
            "v_and_b32 v191, 0xffff0000, v207\n\t"
            "s_waitcnt lgkmcnt(9)\n\t"
            "v_mfma_f32_16x16x32_bf16 v[176:179], v[0:3], v[144:147], v[176:179]\n\t"
            "s_setprio 3\n\t"
            "s_mov_b32 exec_hi, 0x3fffff\n\t"
            "s_add_u32 m0, s66, 872\n\t"
            "s_add_u32 s49, s68, 196224\n\t"
            "buffer_load_dwordx4 v249, s[72:75], s49 offen lds\n\t"
            "s_mov_b32 exec_hi, -1\n\t"
            "s_waitcnt lgkmcnt(8)\n\t"
            "v_mfma_f32_16x16x32_bf16 v[176:179], v[4:7], v[148:151], v[176:179]\n\t"
            "s_mov_b32 exec_hi, 0x3fffff\n\t"
            "s_add_u32 m0, s66, 1744\n\t"
            "s_add_u32 s49, s68, 392448\n\t"
            "buffer_load_dwordx4 v249, s[72:75], s49 offen lds\n\t"
            "s_mov_b32 exec_hi, -1\n\t"
            "s_waitcnt lgkmcnt(6)\n\t"
            "v_mfma_f32_16x16x32_bf16 v[176:179], v[8:11], v[152:155], v[176:179]\n\t"
            "s_mov_b32 exec_hi, 0x3fffff\n\t"
            "s_add_u32 m0, s66, 2616\n\t"
            "s_add_u32 s49, s68, 588672\n\t"
            "buffer_load_dwordx4 v249, s[72:75], s49 offen lds\n\t"
            "s_mov_b32 exec_hi, -1\n\t"
            "s_waitcnt lgkmcnt(5)\n\t"
            "v_mfma_f32_16x16x32_bf16 v[176:179], v[12:15], v[156:159], v[176:179]\n\t"
            "s_mov_b32 exec_hi, 0x3fffff\n\t"
            "s_add_u32 m0, s66, 3488\n\t"
            "s_add_u32 s49, s68, 784896\n\t"
            "buffer_load_dwordx4 v249, s[72:75], s49 offen lds\n\t"
            "s_mov_b32 exec_hi, -1\n\t"
            "s_waitcnt lgkmcnt(3)\n\t"
            "v_mfma_f32_16x16x32_bf16 v[176:179], v[16:19], v[160:163], v[176:179]\n\t"
            "ds_write_b128 v237, v[184:187] offset:256\n\t"
            "s_waitcnt lgkmcnt(3)\n\t"
            "v_mfma_f32_16x16x32_bf16 v[176:179], v[20:23], v[164:167], v[176:179]\n\t"
            "v_lshlrev_b32 v184, 16, v204\n\t"
            "v_and_b32 v185, 0xffff0000, v204\n\t"
            "v_mfma_f32_16x16x32_bf16 v[180:183], v[24:27], v[144:147], v[180:183]\n\t"
            "v_lshlrev_b32 v186, 16, v205\n\t"
            "v_and_b32 v187, 0xffff0000, v205\n\t"
            "v_mfma_f32_16x16x32_bf16 v[180:183], v[28:31], v[148:151], v[180:183]\n\t"
            "s_mov_b32 exec_hi, 0x3fffff\n\t"
            "s_add_u32 m0, s66, 4360\n\t"
            "s_add_u32 s49, s68, 981120\n\t"
            "buffer_load_dwordx4 v249, s[72:75], s49 offen lds\n\t"
            "s_mov_b32 exec_hi, -1\n\t"
            "v_mfma_f32_16x16x32_bf16 v[180:183], v[32:35], v[152:155], v[180:183]\n\t"
            "s_mov_b32 exec_hi, 0x3fffff\n\t"
            "s_add_u32 m0, s66, 5232\n\t"
            "s_add_u32 s49, s68, 1177344\n\t"
            "buffer_load_dwordx4 v249, s[72:75], s49 offen lds\n\t"
            "s_mov_b32 exec_hi, -1\n\t"
            "v_mfma_f32_16x16x32_bf16 v[180:183], v[36:39], v[156:159], v[180:183]\n\t"
            "v_cndmask_b32_e64 v193, v196, v197, s[60:61]\n\t"
            "v_cndmask_b32_e64 v193, v193, v198, s[62:63]\n\t"
            "v_mfma_f32_16x16x32_bf16 v[180:183], v[40:43], v[160:163], v[180:183]\n\t"
            "s_mov_b32 exec_hi, 0x3fffff\n\t"
            "s_add_u32 m0, s66, 6104\n\t"
            "s_add_u32 s49, s68, 1373568\n\t"
            "buffer_load_dwordx4 v249, s[72:75], s49 offen lds\n\t"
            "s_mov_b32 exec_hi, -1\n\t"
            "v_mfma_f32_16x16x32_bf16 v[180:183], v[44:47], v[164:167], v[180:183]\n\t"
            "v_cndmask_b32_e64 v193, v193, v199, s[64:65]\n\t"
            "v_lshlrev_b32 v194, 16, v209\n\t"
            "v_mfma_f32_16x16x32_bf16 v[184:187], v[48:51], v[144:147], v[184:187]\n\t"
            "ds_read_b128 v[196:199], v252\n\t"
            "v_and_b32 v195, 0xffff0000, v209\n\t"
            "v_mfma_f32_16x16x32_bf16 v[184:187], v[52:55], v[148:151], v[184:187]\n\t"
            "s_mov_b32 exec_hi, 0x3fffff\n\t"
            "s_add_u32 m0, s66, 6976\n\t"
            "s_add_u32 s49, s68, 1569792\n\t"
            "buffer_load_dwordx4 v249, s[72:75], s49 offen lds\n\t"
            "s_mov_b32 exec_hi, -1\n\t"
            "v_mfma_f32_16x16x32_bf16 v[184:187], v[56:59], v[152:155], v[184:187]\n\t"
            "s_bfe_u32 s57, s56, 0x20004\n\t"
            "s_lshl_b32 s58, s57, 3\n\t"
            "s_lshr_b32 s58, s55, s58\n\t"
            "s_lshl_b64 s[36:37], s[34:35], s58\n\t"
            "v_mfma_f32_16x16x32_bf16 v[184:187], v[60:63], v[156:159], v[184:187]\n\t"
            "s_bitcmp1_b32 s57, 1\n\t"
            "s_cselect_b32 s58, 1177344, 0\n\t"
            "s_bitcmp1_b32 s57, 0\n\t"
            "s_cselect_b32 s57, 2304, 0\n\t"
            "s_add_u32 s57, s57, s58\n\t"
            "v_mfma_f32_16x16x32_bf16 v[184:187], v[64:67], v[160:163], v[184:187]\n\t"
            "ds_write_b64 v248, v[192:193] offset:18816\n\t"
            "v_mfma_f32_16x16x32_bf16 v[184:187], v[68:71], v[164:167], v[184:187]\n\t"
            "v_lshlrev_b32 v192, 16, v208\n\t"
            "v_and_b32 v193, 0xffff0000, v208\n\t"
            "v_mfma_f32_16x16x32_bf16 v[188:191], v[72:75], v[144:147], v[188:191]\n\t"
            "s_waitcnt lgkmcnt(1)\n\t"
            "s_mov_b64 exec, s[36:37]\n\t"
            "v_add_u32 v196, s57, v196\n\t"
            "s_mov_b64 exec, -1\n\t"
            "v_mfma_f32_16x16x32_bf16 v[188:191], v[76:79], v[148:151], v[188:191]\n\t"
            "v_cvt_pk_bf16_f32 v176, v176, v177\n\t"
            "v_cvt_pk_bf16_f32 v177, v178, v179\n\t"
            "v_mfma_f32_16x16x32_bf16 v[188:191], v[80:83], v[152:155], v[188:191]\n\t"
            "v_cvt_pk_bf16_f32 v178, v180, v181\n\t"
            "v_cvt_pk_bf16_f32 v179, v182, v183\n\t"
            "v_mfma_f32_16x16x32_bf16 v[188:191], v[84:87], v[156:159], v[188:191]\n\t"
            "s_cbranch_vccnz .Lst0_%=\n\t"
            "buffer_store_dwordx4 v[176:179], v196, s[40:43], s45 offen\n\t"
            ".Lsr0_%=:\n\t"
            "v_mfma_f32_16x16x32_bf16 v[188:191], v[88:91], v[160:163], v[188:191]\n\t"
            "s_nop 0\n\t"
            "v_lshlrev_b32 v176, 16, v210\n\t"
            "v_and_b32 v177, 0xffff0000, v210\n\t"
            "v_mfma_f32_16x16x32_bf16 v[188:191], v[92:95], v[164:167], v[188:191]\n\t"
            "v_lshlrev_b32 v178, 16, v211\n\t"
            "v_and_b32 v179, 0xffff0000, v211\n\t"
            "v_mfma_f32_16x16x32_bf16 v[192:195], v[96:99], v[144:147], v[192:195]\n\t"
            "v_lshlrev_b32 v180, 16, v212\n\t"
            "v_and_b32 v181, 0xffff0000, v212\n\t"
            "v_mfma_f32_16x16x32_bf16 v[192:195], v[100:103], v[148:151], v[192:195]\n\t"
            "v_lshlrev_b32 v182, 16, v213\n\t"
            "v_and_b32 v183, 0xffff0000, v213\n\t"
            "v_mfma_f32_16x16x32_bf16 v[192:195], v[104:107], v[152:155], v[192:195]\n\t"
            "v_cvt_pk_bf16_f32 v184, v184, v185\n\t"
            "v_cvt_pk_bf16_f32 v185, v186, v187\n\t"
            "v_mfma_f32_16x16x32_bf16 v[192:195], v[108:111], v[156:159], v[192:195]\n\t"
            "v_cvt_pk_bf16_f32 v186, v188, v189\n\t"
            "v_cvt_pk_bf16_f32 v187, v190, v191\n\t"
            "v_mfma_f32_16x16x32_bf16 v[192:195], v[112:115], v[160:163], v[192:195]\n\t"
            "s_cbranch_vccnz .Lst1_%=\n\t"
            "buffer_store_dwordx4 v[184:187], v196, s[40:43], s45 offen offset:64\n\t"
            ".Lsr1_%=:\n\t"
            "v_mfma_f32_16x16x32_bf16 v[192:195], v[116:119], v[164:167], v[192:195]\n\t"
            "s_nop 0\n\t"
            "v_lshlrev_b32 v184, 16, v214\n\t"
            "v_and_b32 v185, 0xffff0000, v214\n\t"
            "v_mfma_f32_16x16x32_bf16 v[176:179], v[120:123], v[144:147], v[176:179]\n\t"
            "ds_read_b128 v[144:147], v242 offset:6272\n\t"
            "v_lshlrev_b32 v186, 16, v215\n\t"
            "v_mfma_f32_16x16x32_bf16 v[176:179], v[124:127], v[148:151], v[176:179]\n\t"
            "ds_read_b128 v[148:151], v243 offset:6272\n\t"
            "v_and_b32 v187, 0xffff0000, v215\n\t"
            "v_mfma_f32_16x16x32_bf16 v[176:179], v[128:131], v[152:155], v[176:179]\n\t"
            "ds_read_b128 v[152:155], v242 offset:6400\n\t"
            "v_lshlrev_b32 v188, 16, v216\n\t"
            "v_mfma_f32_16x16x32_bf16 v[176:179], v[132:135], v[156:159], v[176:179]\n\t"
            "ds_read_b128 v[156:159], v243 offset:6400\n\t"
            "v_and_b32 v189, 0xffff0000, v216\n\t"
            "v_mfma_f32_16x16x32_bf16 v[176:179], v[136:139], v[160:163], v[176:179]\n\t"
            "ds_read_b128 v[160:163], v242 offset:12288\n\t"
            "v_lshlrev_b32 v190, 16, v217\n\t"
            "v_mfma_f32_16x16x32_bf16 v[176:179], v[140:143], v[164:167], v[176:179]\n\t"
            "ds_read_b128 v[164:167], v243 offset:12288\n\t"
            "v_and_b32 v191, 0xffff0000, v217\n\t"
            "v_mfma_f32_16x16x32_bf16 v[180:183], v[0:3], v[168:171], v[180:183]\n\t"
            "v_cvt_pk_bf16_f32 v192, v192, v193\n\t"
            "v_cvt_pk_bf16_f32 v193, v194, v195\n\t"
            "v_mfma_f32_16x16x32_bf16 v[180:183], v[4:7], v[172:175], v[180:183]\n\t"
            "v_mov_b32 v249, s76\n\t"
            "v_add_u32 v236, s48, v244\n\t"
            "v_cvt_pk_bf16_f32 v194, v176, v177\n\t"
            "s_waitcnt lgkmcnt(5)\n\t"
            "v_mfma_f32_16x16x32_bf16 v[180:183], v[8:11], v[144:147], v[180:183]\n\t"
            "ds_read_b32 v249, v249 offset:4\n\t"
            "v_cvt_pk_bf16_f32 v195, v178, v179\n\t"
            "s_waitcnt lgkmcnt(5)\n\t"
            "v_mfma_f32_16x16x32_bf16 v[180:183], v[12:15], v[148:151], v[180:183]\n\t"
            "ds_read_b128 v[200:203], v236\n\t"
            "v_lshlrev_b32 v176, 16, v220\n\t"
            "s_waitcnt lgkmcnt(5)\n\t"
            "v_mfma_f32_16x16x32_bf16 v[180:183], v[16:19], v[152:155], v[180:183]\n\t"
            "ds_read_b128 v[212:215], v236 offset:6144\n\t"
            "v_and_b32 v177, 0xffff0000, v220\n\t"
            "s_waitcnt lgkmcnt(5)\n\t"
            "v_mfma_f32_16x16x32_bf16 v[180:183], v[20:23], v[156:159], v[180:183]\n\t"
            "s_cbranch_vccnz .Lst2_%=\n\t"
            "buffer_store_dwordx4 v[192:195], v196, s[40:43], s45 offen offset:128\n\t"
            ".Lsr2_%=:\n\t"
            "v_mfma_f32_16x16x32_bf16 v[184:187], v[24:27], v[168:171], v[184:187]\n\t"
            "s_nop 0\n\t"
            "v_lshlrev_b32 v192, 16, v218\n\t"
            "v_and_b32 v193, 0xffff0000, v218\n\t"
            "v_mfma_f32_16x16x32_bf16 v[184:187], v[28:31], v[172:175], v[184:187]\n\t"
            "v_lshlrev_b32 v194, 16, v219\n\t"
            "v_and_b32 v195, 0xffff0000, v219\n\t"
            "v_mfma_f32_16x16x32_bf16 v[184:187], v[32:35], v[144:147], v[184:187]\n\t"
            "v_lshlrev_b32 v178, 16, v221\n\t"
            "v_and_b32 v179, 0xffff0000, v221\n\t"
            "v_mfma_f32_16x16x32_bf16 v[184:187], v[36:39], v[148:151], v[184:187]\n\t"
            "v_cvt_pk_bf16_f32 v180, v180, v181\n\t"
            "v_cvt_pk_bf16_f32 v181, v182, v183\n\t"
            "v_mfma_f32_16x16x32_bf16 v[184:187], v[40:43], v[152:155], v[184:187]\n\t"
            "s_waitcnt lgkmcnt(2)\n\t"
            "v_readfirstlane_b32 s44, v249\n\t"
            "v_add_u32 v237, s48, v245\n\t"
            "v_mfma_f32_16x16x32_bf16 v[184:187], v[44:47], v[156:159], v[184:187]\n\t"
            "s_and_b32 s70, s44, 0x7ffffff\n\t"
            "s_add_u32 s70, s70, s77\n\t"
            "s_and_b32 s44, s44, 0x08000000\n\t"
            "s_or_b32 s44, s44, s39\n\t"
            "v_mfma_f32_16x16x32_bf16 v[188:191], v[48:51], v[168:171], v[188:191]\n\t"
            "ds_read_b32 v249, v252 offset:1028\n\t"
            "v_add_u32 v248, s48, v246\n\t"
            "v_mfma_f32_16x16x32_bf16 v[188:191], v[52:55], v[172:175], v[188:191]\n\t"
            "ds_read_b128 v[204:207], v237\n\t"
            "v_cvt_pk_bf16_f32 v182, v184, v185\n\t"
            "v_mfma_f32_16x16x32_bf16 v[188:191], v[56:59], v[144:147], v[188:191]\n\t"
            "ds_read_b128 v[208:211], v248\n\t"
            "v_cvt_pk_bf16_f32 v183, v186, v187\n\t"
            "v_mfma_f32_16x16x32_bf16 v[188:191], v[60:63], v[148:151], v[188:191]\n\t"
            "ds_read_b128 v[216:219], v237 offset:6144\n\t"
            "v_lshlrev_b32 v184, 16, v224\n\t"
            "v_mfma_f32_16x16x32_bf16 v[188:191], v[64:67], v[152:155], v[188:191]\n\t"
            "s_cbranch_vccnz .Lst3_%=\n\t"
            "buffer_store_dwordx4 v[180:183], v197, s[40:43], s45 offen\n\t"
            ".Lsr3_%=:\n\t"
            "v_mfma_f32_16x16x32_bf16 v[188:191], v[68:71], v[156:159], v[188:191]\n\t"
            "s_nop 0\n\t"
            "v_lshlrev_b32 v180, 16, v222\n\t"
            "v_and_b32 v181, 0xffff0000, v222\n\t"
            "v_mfma_f32_16x16x32_bf16 v[192:195], v[72:75], v[168:171], v[192:195]\n\t"
            "v_lshlrev_b32 v182, 16, v223\n\t"
            "v_and_b32 v183, 0xffff0000, v223\n\t"
            "v_mfma_f32_16x16x32_bf16 v[192:195], v[76:79], v[172:175], v[192:195]\n\t"
            "ds_read_b128 v[220:223], v248 offset:6144\n\t"
            "v_and_b32 v185, 0xffff0000, v224\n\t"
            "v_mfma_f32_16x16x32_bf16 v[192:195], v[80:83], v[144:147], v[192:195]\n\t"
            "v_lshlrev_b32 v186, 16, v225\n\t"
            "v_and_b32 v187, 0xffff0000, v225\n\t"
            "v_mfma_f32_16x16x32_bf16 v[192:195], v[84:87], v[148:151], v[192:195]\n\t"
            "v_cvt_pk_bf16_f32 v188, v188, v189\n\t"
            "v_cvt_pk_bf16_f32 v189, v190, v191\n\t"
            "v_mfma_f32_16x16x32_bf16 v[192:195], v[88:91], v[152:155], v[192:195]\n\t"
            "s_waitcnt lgkmcnt(4)\n\t"
            "v_and_b32 v249, s44, v249\n\t"
            "v_mfma_f32_16x16x32_bf16 v[192:195], v[92:95], v[156:159], v[192:195]\n\t"
            "v_mfma_f32_16x16x32_bf16 v[176:179], v[96:99], v[168:171], v[176:179]\n\t"
            "v_mfma_f32_16x16x32_bf16 v[176:179], v[100:103], v[172:175], v[176:179]\n\t"
            "v_mfma_f32_16x16x32_bf16 v[176:179], v[104:107], v[144:147], v[176:179]\n\t"
            "v_mfma_f32_16x16x32_bf16 v[176:179], v[108:111], v[148:151], v[176:179]\n\t"
            "v_mfma_f32_16x16x32_bf16 v[176:179], v[112:115], v[152:155], v[176:179]\n\t"
            "v_mfma_f32_16x16x32_bf16 v[176:179], v[116:119], v[156:159], v[176:179]\n\t"
            "s_nop 1\n\t"
            "v_cvt_pk_bf16_f32 v190, v192, v193\n\t"
            "v_mfma_f32_16x16x32_bf16 v[180:183], v[120:123], v[168:171], v[180:183]\n\t"
            "ds_read_b128 v[168:171], v242 offset:12416\n\t"
            "v_cvt_pk_bf16_f32 v191, v194, v195\n\t"
            "v_mfma_f32_16x16x32_bf16 v[180:183], v[124:127], v[172:175], v[180:183]\n\t"
            "ds_read_b128 v[172:175], v243 offset:12416\n\t"
            "v_lshlrev_b32 v192, 16, v228\n\t"
            "v_mfma_f32_16x16x32_bf16 v[180:183], v[128:131], v[144:147], v[180:183]\n\t"
            "ds_read_b128 v[144:147], v242 offset:12544\n\t"
            "v_and_b32 v193, 0xffff0000, v228\n\t"
            "v_mfma_f32_16x16x32_bf16 v[180:183], v[132:135], v[148:151], v[180:183]\n\t"
            "ds_read_b128 v[148:151], v243 offset:12544\n\t"
            "v_lshlrev_b32 v194, 16, v229\n\t"
            "v_mfma_f32_16x16x32_bf16 v[180:183], v[136:139], v[152:155], v[180:183]\n\t"
            "s_waitcnt lgkmcnt(0)\n\t"
            "s_barrier\n\t"
            "s_cbranch_vccnz .Lst4_%=\n\t"
            "buffer_store_dwordx4 v[188:191], v197, s[40:43], s45 offen offset:64\n\t"
            ".Lsr4_%=:\n\t"
            "v_mfma_f32_16x16x32_bf16 v[180:183], v[140:143], v[156:159], v[180:183]\n\t"
            "s_nop 0\n\t"
            "v_lshlrev_b32 v188, 16, v226\n\t"
            "v_and_b32 v189, 0xffff0000, v226\n\t"
            "v_mfma_f32_16x16x32_bf16 v[184:187], v[0:3], v[160:163], v[184:187]\n\t"
            "v_lshlrev_b32 v190, 16, v227\n\t"
            "v_and_b32 v191, 0xffff0000, v227\n\t"
            "v_mfma_f32_16x16x32_bf16 v[184:187], v[4:7], v[164:167], v[184:187]\n\t"
            "ds_read_b128 v[224:227], v236 offset:12288\n\t"
            "v_and_b32 v195, 0xffff0000, v229\n\t"
            "v_mfma_f32_16x16x32_bf16 v[184:187], v[8:11], v[168:171], v[184:187]\n\t"
            "v_cvt_pk_bf16_f32 v176, v176, v177\n\t"
            "v_cvt_pk_bf16_f32 v177, v178, v179\n\t"
            "v_mfma_f32_16x16x32_bf16 v[184:187], v[12:15], v[172:175], v[184:187]\n\t"
            "v_cvt_pk_bf16_f32 v178, v180, v181\n\t"
            "v_cvt_pk_bf16_f32 v179, v182, v183\n\t"
            "v_mfma_f32_16x16x32_bf16 v[184:187], v[16:19], v[144:147], v[184:187]\n\t"
            "s_cbranch_vccnz .Lst5_%=\n\t"
            "buffer_store_dwordx4 v[176:179], v197, s[40:43], s45 offen offset:128\n\t"
            ".Lsr5_%=:\n\t"
            "v_mfma_f32_16x16x32_bf16 v[184:187], v[20:23], v[148:151], v[184:187]\n\t"
            "s_nop 0\n\t"
            "v_lshlrev_b32 v176, 16, v230\n\t"
            "v_and_b32 v177, 0xffff0000, v230\n\t"
            "v_mfma_f32_16x16x32_bf16 v[188:191], v[24:27], v[160:163], v[188:191]\n\t"
            "v_lshlrev_b32 v178, 16, v231\n\t"
            "v_and_b32 v179, 0xffff0000, v231\n\t"
            "v_mfma_f32_16x16x32_bf16 v[188:191], v[28:31], v[164:167], v[188:191]\n\t"
            "ds_read_b128 v[228:231], v237 offset:12288\n\t"
            "v_lshlrev_b32 v180, 16, v232\n\t"
            "v_mfma_f32_16x16x32_bf16 v[188:191], v[32:35], v[168:171], v[188:191]\n\t"
            "v_and_b32 v181, 0xffff0000, v232\n\t"
            "v_lshlrev_b32 v182, 16, v233\n\t"
            "v_mfma_f32_16x16x32_bf16 v[188:191], v[36:39], v[172:175], v[188:191]\n\t"
            "v_and_b32 v183, 0xffff0000, v233\n\t"
            "v_cvt_pk_bf16_f32 v184, v184, v185\n\t"
            "v_mfma_f32_16x16x32_bf16 v[188:191], v[40:43], v[144:147], v[188:191]\n\t"
            "v_cvt_pk_bf16_f32 v185, v186, v187\n\t"
            "v_mfma_f32_16x16x32_bf16 v[188:191], v[44:47], v[148:151], v[188:191]\n\t"
            "v_mfma_f32_16x16x32_bf16 v[192:195], v[48:51], v[160:163], v[192:195]\n\t"
            "v_mfma_f32_16x16x32_bf16 v[192:195], v[52:55], v[164:167], v[192:195]\n\t"
            "v_mfma_f32_16x16x32_bf16 v[192:195], v[56:59], v[168:171], v[192:195]\n\t"
            "v_mfma_f32_16x16x32_bf16 v[192:195], v[60:63], v[172:175], v[192:195]\n\t"
            "v_mfma_f32_16x16x32_bf16 v[192:195], v[64:67], v[144:147], v[192:195]\n\t"
            "v_mfma_f32_16x16x32_bf16 v[192:195], v[68:71], v[148:151], v[192:195]\n\t"
            "s_nop 1\n\t"
            "v_cvt_pk_bf16_f32 v186, v188, v189\n\t"
            "v_mfma_f32_16x16x32_bf16 v[176:179], v[72:75], v[160:163], v[176:179]\n\t"
            "v_cvt_pk_bf16_f32 v187, v190, v191\n\t"
            "v_mfma_f32_16x16x32_bf16 v[176:179], v[76:79], v[164:167], v[176:179]\n\t"
            "s_cbranch_vccnz .Lst6_%=\n\t"
            "buffer_store_dwordx4 v[184:187], v198, s[40:43], s45 offen\n\t"
            ".Lsr6_%=:\n\t"
            "v_mfma_f32_16x16x32_bf16 v[176:179], v[80:83], v[168:171], v[176:179]\n\t"
            "s_nop 0\n\t"
            "v_lshlrev_b32 v184, 16, v234\n\t"
            "v_and_b32 v185, 0xffff0000, v234\n\t"
            "v_mfma_f32_16x16x32_bf16 v[176:179], v[84:87], v[172:175], v[176:179]\n\t"
            "v_lshlrev_b32 v186, 16, v235\n\t"
            "v_and_b32 v187, 0xffff0000, v235\n\t"
            "v_mfma_f32_16x16x32_bf16 v[176:179], v[88:91], v[144:147], v[176:179]\n\t"
            "ds_read_b128 v[232:235], v248 offset:12288\n\t"
            "v_cvt_pk_bf16_f32 v192, v192, v193\n\t"
            "v_mfma_f32_16x16x32_bf16 v[176:179], v[92:95], v[148:151], v[176:179]\n\t"
            "v_cvt_pk_bf16_f32 v193, v194, v195\n\t"
            "v_mfma_f32_16x16x32_bf16 v[180:183], v[96:99], v[160:163], v[180:183]\n\t"
            "v_mfma_f32_16x16x32_bf16 v[180:183], v[100:103], v[164:167], v[180:183]\n\t"
            "v_mfma_f32_16x16x32_bf16 v[180:183], v[104:107], v[168:171], v[180:183]\n\t"
            "v_mfma_f32_16x16x32_bf16 v[180:183], v[108:111], v[172:175], v[180:183]\n\t"
            "v_mfma_f32_16x16x32_bf16 v[180:183], v[112:115], v[144:147], v[180:183]\n\t"
            "s_nop 1\n\t"
            "v_cvt_pk_bf16_f32 v194, v176, v177\n\t"
            "v_mfma_f32_16x16x32_bf16 v[180:183], v[116:119], v[148:151], v[180:183]\n\t"
            "v_cvt_pk_bf16_f32 v195, v178, v179\n\t"
            "v_mfma_f32_16x16x32_bf16 v[184:187], v[120:123], v[160:163], v[184:187]\n\t"
            "s_cbranch_vccnz .Lst7_%=\n\t"
            "buffer_store_dwordx4 v[192:195], v198, s[40:43], s45 offen offset:64\n\t"
            ".Lsr7_%=:\n\t"
            "v_mfma_f32_16x16x32_bf16 v[184:187], v[124:127], v[164:167], v[184:187]\n\t"
            "s_waitcnt vmcnt(8)\n\t"
            "ds_read_b64 v[152:153], v253 offset:8\n\t"
            "v_mfma_f32_16x16x32_bf16 v[184:187], v[128:131], v[168:171], v[184:187]\n\t"
            "ds_read_b128 v[168:171], v254\n\t"
            "v_cvt_pk_bf16_f32 v180, v180, v181\n\t"
            "v_mfma_f32_16x16x32_bf16 v[184:187], v[132:135], v[172:175], v[184:187]\n\t"
            "ds_read_b128 v[172:175], v254 offset:64\n\t"
            "v_cvt_pk_bf16_f32 v181, v182, v183\n\t"
            "v_mfma_f32_16x16x32_bf16 v[184:187], v[136:139], v[144:147], v[184:187]\n\t"
            "ds_read_b64 v[144:145], v253\n\t"
            "v_mfma_f32_16x16x32_bf16 v[184:187], v[140:143], v[148:151], v[184:187]\n\t"
            "ds_read_b64 v[146:147], v253 offset:96\n\t"
            "ds_read_b64 v[148:149], v253 offset:1744\n\t"
            "ds_read_b64 v[150:151], v253 offset:1840\n\t"
            "ds_read_b64 v[154:155], v253 offset:104\n\t"
            "ds_read_b64 v[156:157], v253 offset:1752\n\t"
            "ds_read_b64 v[158:159], v253 offset:1848\n\t"
            "ds_read_b64 v[160:161], v253 offset:16\n\t"
            "ds_read_b64 v[162:163], v253 offset:112\n\t"
            "v_cvt_pk_bf16_f32 v182, v184, v185\n\t"
            "v_cvt_pk_bf16_f32 v183, v186, v187\n\t"
            "s_waitcnt lgkmcnt(14)\n\t"
            "ds_read_b64 v[164:165], v253 offset:1760\n\t"
            "s_waitcnt lgkmcnt(14)\n\t"
            "ds_read_b64 v[166:167], v253 offset:1856\n\t"
            "s_cbranch_vccnz .Lst8_%=\n\t"
            "buffer_store_dwordx4 v[180:183], v198, s[40:43], s45 offen offset:128\n\t"
            ".Lsr8_%=:\n\t"
            "s_mov_b32 m0, s32\n\t"
            "s_add_u32 s49, s70, s25\n\t"
            "buffer_load_dword v249, s[72:75], s70 offen lds\n\t"
            "buffer_load_dword v249, s[72:75], s49 offen lds\n\t"
            "s_add_u32 s45, s46, s27\n\t"
            "s_mov_b32 s46, s47\n\t"
            "s_mov_b32 s47, s68\n\t"
            "s_xor_b32 s48, s48, s71\n\t"
            "s_xor_b32 s67, s67, s71\n\t"
            "s_add_u32 s76, s76, 4\n\t"
            "s_lshl_b32 s56, s56, 2\n\t"
            "s_lshr_b32 s49, s26, 30\n\t"
            "s_or_b32 s56, s56, s49\n\t"
            "s_cmp_ge_u32 s76, s24\n\t"
            "s_cselect_b64 vcc, -1, 0\n\t"
            "s_cmp_lt_u32 s46, 0x7000000\n\t"
            "s_cbranch_scc1 .Lwb_loop%=\n\t"
            "s_branch .Lsx%=\n\t"
            ".Lst0_%=:\n\t"
            "buffer_store_dwordx4 v[176:179], v196, s[40:43], s45 offen sc1\n\t"
            "s_branch .Lsr0_%=\n\t"
            ".Lst1_%=:\n\t"
            "buffer_store_dwordx4 v[184:187], v196, s[40:43], s45 offen offset:64 sc1\n\t"
            "s_branch .Lsr1_%=\n\t"
            ".Lst2_%=:\n\t"
            "buffer_store_dwordx4 v[192:195], v196, s[40:43], s45 offen offset:128 sc1\n\t"
            "s_branch .Lsr2_%=\n\t"
            ".Lst3_%=:\n\t"
            "buffer_store_dwordx4 v[180:183], v197, s[40:43], s45 offen sc1\n\t"
            "s_branch .Lsr3_%=\n\t"
            ".Lst4_%=:\n\t"
            "buffer_store_dwordx4 v[188:191], v197, s[40:43], s45 offen offset:64 sc1\n\t"
            "s_branch .Lsr4_%=\n\t"
            ".Lst5_%=:\n\t"
            "buffer_store_dwordx4 v[176:179], v197, s[40:43], s45 offen offset:128 sc1\n\t"
            "s_branch .Lsr5_%=\n\t"
            ".Lst6_%=:\n\t"
            "buffer_store_dwordx4 v[184:187], v198, s[40:43], s45 offen sc1\n\t"
            "s_branch .Lsr6_%=\n\t"
            ".Lst7_%=:\n\t"
            "buffer_store_dwordx4 v[192:195], v198, s[40:43], s45 offen offset:64 sc1\n\t"
            "s_branch .Lsr7_%=\n\t"
            ".Lst8_%=:\n\t"
            "buffer_store_dwordx4 v[180:183], v198, s[40:43], s45 offen offset:128 sc1\n\t"
            "s_branch .Lsr8_%=\n\t"
            ".Lsx%=:\n\t"
            "s_waitcnt lgkmcnt(0)\n\t"
            "s_nop 7\n\t"
            "s_setprio 3\n\t"
            "s_nop 7\n\t"
            "ds_read_b128 v[196:199], v252\n\t"
            "s_bfe_u32 s57, s56, 0x20004\n\t"
            "s_lshl_b32 s58, s57, 3\n\t"
            "s_lshr_b32 s58, s55, s58\n\t"
            "s_lshl_b64 s[36:37], s[34:35], s58\n\t"
            "s_bitcmp1_b32 s57, 1\n\t"
            "s_cselect_b32 s58, 1177344, 0\n\t"
            "s_bitcmp1_b32 s57, 0\n\t"
            "s_cselect_b32 s57, 2304, 0\n\t"
            "s_add_u32 s57, s57, s58\n\t"
            "s_waitcnt lgkmcnt(0)\n\t"
            "s_mov_b64 exec, s[36:37]\n\t"
            "v_add_u32 v196, s57, v196\n\t"
            "s_mov_b64 exec, -1\n\t"
            "s_nop 7\n\t"
            "v_mov_b32 v144, s23\n\t"
            ".Ldp%=:\n\t"
            "ds_read_b128 v[148:151], v144 offset:96\n\t"
            "s_waitcnt lgkmcnt(0)\n\t"
            "v_or_b32 v148, v148, v149\n\t"
            "v_or3_b32 v148, v148, v150, v151\n\t"
            "v_cmp_gt_i32 vcc, 0, v148\n\t"
            "s_nop 4\n\t"
            "s_cbranch_vccnz .Ldp%=\n\t"
            "s_nop 7\n\t"
            "ds_read_b128 v[184:187], v242\n\t"
            "v_lshlrev_b32 v152, 16, v200\n\t"
            "v_and_b32 v153, 0xffff0000, v200\n\t"
            "v_lshlrev_b32 v154, 16, v201\n\t"
            "v_and_b32 v155, 0xffff0000, v201\n\t"
            "v_lshlrev_b32 v156, 16, v202\n\t"
            "v_and_b32 v157, 0xffff0000, v202\n\t"
            "v_lshlrev_b32 v158, 16, v203\n\t"
            "v_and_b32 v159, 0xffff0000, v203\n\t"
            "v_lshlrev_b32 v160, 16, v204\n\t"
            "v_and_b32 v161, 0xffff0000, v204\n\t"
            "v_lshlrev_b32 v162, 16, v205\n\t"
            "v_and_b32 v163, 0xffff0000, v205\n\t"
            "v_lshlrev_b32 v164, 16, v206\n\t"
            "v_and_b32 v165, 0xffff0000, v206\n\t"
            "v_lshlrev_b32 v166, 16, v207\n\t"
            "v_and_b32 v167, 0xffff0000, v207\n\t"
            "v_lshlrev_b32 v176, 16, v208\n\t"
            "v_and_b32 v177, 0xffff0000, v208\n\t"
            "v_lshlrev_b32 v178, 16, v209\n\t"
            "v_and_b32 v179, 0xffff0000, v209\n\t"
            "v_lshlrev_b32 v180, 16, v210\n\t"
            "v_and_b32 v181, 0xffff0000, v210\n\t"
            "v_lshlrev_b32 v182, 16, v211\n\t"
            "v_and_b32 v183, 0xffff0000, v211\n\t"
            "v_lshlrev_b32 v144, 16, v212\n\t"
            "v_and_b32 v145, 0xffff0000, v212\n\t"
            "v_lshlrev_b32 v146, 16, v213\n\t"
            "v_and_b32 v147, 0xffff0000, v213\n\t"
            "v_lshlrev_b32 v148, 16, v214\n\t"
            "v_and_b32 v149, 0xffff0000, v214\n\t"
            "s_waitcnt lgkmcnt(0)\n\t"
            "v_mfma_f32_16x16x32_bf16 v[152:155], v[0:3], v[184:187], v[152:155]\n\t"
            "ds_read_b128 v[188:191], v243\n\t"
            "v_lshlrev_b32 v150, 16, v215\n\t"
            "v_mfma_f32_16x16x32_bf16 v[156:159], v[24:27], v[184:187], v[156:159]\n\t"
            "ds_read_b128 v[192:195], v242 offset:128\n\t"
            "v_and_b32 v151, 0xffff0000, v215\n\t"
            "v_mfma_f32_16x16x32_bf16 v[160:163], v[48:51], v[184:187], v[160:163]\n\t"
            "v_lshlrev_b32 v168, 16, v216\n\t"
            "v_and_b32 v169, 0xffff0000, v216\n\t"
            "v_mfma_f32_16x16x32_bf16 v[164:167], v[72:75], v[184:187], v[164:167]\n\t"
            "v_lshlrev_b32 v170, 16, v217\n\t"
            "v_and_b32 v171, 0xffff0000, v217\n\t"
            "v_mfma_f32_16x16x32_bf16 v[176:179], v[96:99], v[184:187], v[176:179]\n\t"
            "v_lshlrev_b32 v172, 16, v218\n\t"
            "v_and_b32 v173, 0xffff0000, v218\n\t"
            "v_mfma_f32_16x16x32_bf16 v[180:183], v[120:123], v[184:187], v[180:183]\n\t"
            "ds_read_b128 v[184:187], v243 offset:128\n\t"
            "v_lshlrev_b32 v174, 16, v219\n\t"
            "v_and_b32 v175, 0xffff0000, v219\n\t"
            "s_waitcnt lgkmcnt(2)\n\t"
            "v_mfma_f32_16x16x32_bf16 v[152:155], v[4:7], v[188:191], v[152:155]\n\t"
            "v_mfma_f32_16x16x32_bf16 v[156:159], v[28:31], v[188:191], v[156:159]\n\t"
            "v_mfma_f32_16x16x32_bf16 v[160:163], v[52:55], v[188:191], v[160:163]\n\t"
            "v_mfma_f32_16x16x32_bf16 v[164:167], v[76:79], v[188:191], v[164:167]\n\t"
            "v_mfma_f32_16x16x32_bf16 v[176:179], v[100:103], v[188:191], v[176:179]\n\t"
            "v_mfma_f32_16x16x32_bf16 v[180:183], v[124:127], v[188:191], v[180:183]\n\t"
            "ds_read_b128 v[188:191], v242 offset:256\n\t"
            "s_waitcnt lgkmcnt(2)\n\t"
            "v_mfma_f32_16x16x32_bf16 v[152:155], v[8:11], v[192:195], v[152:155]\n\t"
            "v_mfma_f32_16x16x32_bf16 v[156:159], v[32:35], v[192:195], v[156:159]\n\t"
            "v_mfma_f32_16x16x32_bf16 v[160:163], v[56:59], v[192:195], v[160:163]\n\t"
            "v_mfma_f32_16x16x32_bf16 v[164:167], v[80:83], v[192:195], v[164:167]\n\t"
            "v_mfma_f32_16x16x32_bf16 v[176:179], v[104:107], v[192:195], v[176:179]\n\t"
            "v_mfma_f32_16x16x32_bf16 v[180:183], v[128:131], v[192:195], v[180:183]\n\t"
            "ds_read_b128 v[192:195], v243 offset:256\n\t"
            "s_waitcnt lgkmcnt(2)\n\t"
            "v_mfma_f32_16x16x32_bf16 v[152:155], v[12:15], v[184:187], v[152:155]\n\t"
            "v_mfma_f32_16x16x32_bf16 v[156:159], v[36:39], v[184:187], v[156:159]\n\t"
            "v_mfma_f32_16x16x32_bf16 v[160:163], v[60:63], v[184:187], v[160:163]\n\t"
            "v_mfma_f32_16x16x32_bf16 v[164:167], v[84:87], v[184:187], v[164:167]\n\t"
            "v_mfma_f32_16x16x32_bf16 v[176:179], v[108:111], v[184:187], v[176:179]\n\t"
            "v_mfma_f32_16x16x32_bf16 v[180:183], v[132:135], v[184:187], v[180:183]\n\t"
            "ds_read_b128 v[184:187], v242 offset:6144\n\t"
            "s_waitcnt lgkmcnt(2)\n\t"
            "v_mfma_f32_16x16x32_bf16 v[152:155], v[16:19], v[188:191], v[152:155]\n\t"
            "v_mfma_f32_16x16x32_bf16 v[156:159], v[40:43], v[188:191], v[156:159]\n\t"
            "v_mfma_f32_16x16x32_bf16 v[160:163], v[64:67], v[188:191], v[160:163]\n\t"
            "v_mfma_f32_16x16x32_bf16 v[164:167], v[88:91], v[188:191], v[164:167]\n\t"
            "v_mfma_f32_16x16x32_bf16 v[176:179], v[112:115], v[188:191], v[176:179]\n\t"
            "v_mfma_f32_16x16x32_bf16 v[180:183], v[136:139], v[188:191], v[180:183]\n\t"
            "ds_read_b128 v[188:191], v243 offset:6144\n\t"
            "s_waitcnt lgkmcnt(2)\n\t"
            "v_mfma_f32_16x16x32_bf16 v[152:155], v[20:23], v[192:195], v[152:155]\n\t"
            "v_mfma_f32_16x16x32_bf16 v[156:159], v[44:47], v[192:195], v[156:159]\n\t"
            "v_mfma_f32_16x16x32_bf16 v[160:163], v[68:71], v[192:195], v[160:163]\n\t"
            "v_mfma_f32_16x16x32_bf16 v[164:167], v[92:95], v[192:195], v[164:167]\n\t"
            "v_mfma_f32_16x16x32_bf16 v[176:179], v[116:119], v[192:195], v[176:179]\n\t"
            "v_mfma_f32_16x16x32_bf16 v[180:183], v[140:143], v[192:195], v[180:183]\n\t"
            "ds_read_b128 v[192:195], v242 offset:6272\n\t"
            "s_waitcnt lgkmcnt(2)\n\t"
            "v_mfma_f32_16x16x32_bf16 v[144:147], v[0:3], v[184:187], v[144:147]\n\t"
            "v_cvt_pk_bf16_f32 v152, v152, v153\n\t"
            "v_cvt_pk_bf16_f32 v153, v154, v155\n\t"
            "v_mfma_f32_16x16x32_bf16 v[148:151], v[24:27], v[184:187], v[148:151]\n\t"
            "v_cvt_pk_bf16_f32 v160, v160, v161\n\t"
            "v_cvt_pk_bf16_f32 v161, v162, v163\n\t"
            "v_mfma_f32_16x16x32_bf16 v[168:171], v[48:51], v[184:187], v[168:171]\n\t"
            "v_cvt_pk_bf16_f32 v154, v156, v157\n\t"
            "v_cvt_pk_bf16_f32 v155, v158, v159\n\t"
            "v_mfma_f32_16x16x32_bf16 v[172:175], v[72:75], v[184:187], v[172:175]\n\t"
            "v_cvt_pk_bf16_f32 v162, v164, v165\n\t"
            "v_cvt_pk_bf16_f32 v163, v166, v167\n\t"
            "s_waitcnt lgkmcnt(1)\n\t"
            "v_mfma_f32_16x16x32_bf16 v[144:147], v[4:7], v[188:191], v[144:147]\n\t"
            "v_lshlrev_b32 v156, 16, v220\n\t"
            "v_and_b32 v157, 0xffff0000, v220\n\t"
            "v_mfma_f32_16x16x32_bf16 v[148:151], v[28:31], v[188:191], v[148:151]\n\t"
            "v_lshlrev_b32 v158, 16, v221\n\t"
            "v_and_b32 v159, 0xffff0000, v221\n\t"
            "v_mfma_f32_16x16x32_bf16 v[168:171], v[52:55], v[188:191], v[168:171]\n\t"
            "v_lshlrev_b32 v164, 16, v222\n\t"
            "v_and_b32 v165, 0xffff0000, v222\n\t"
            "v_mfma_f32_16x16x32_bf16 v[156:159], v[96:99], v[184:187], v[156:159]\n\t"
            "v_lshlrev_b32 v166, 16, v223\n\t"
            "v_and_b32 v167, 0xffff0000, v223\n\t"
            "v_mfma_f32_16x16x32_bf16 v[172:175], v[76:79], v[188:191], v[172:175]\n\t"
            "buffer_store_dwordx4 v[152:155], v196, s[40:43], s45 offen sc1\n\t"
            "v_mfma_f32_16x16x32_bf16 v[164:167], v[120:123], v[184:187], v[164:167]\n\t"
            "ds_read_b128 v[184:187], v243 offset:6272\n\t"
            "v_cvt_pk_bf16_f32 v176, v176, v177\n\t"
            "v_mfma_f32_16x16x32_bf16 v[156:159], v[100:103], v[188:191], v[156:159]\n\t"
            "buffer_store_dwordx4 v[160:163], v196, s[40:43], s45 offen offset:64 sc1\n\t"
            "v_mfma_f32_16x16x32_bf16 v[164:167], v[124:127], v[188:191], v[164:167]\n\t"
            "ds_read_b128 v[188:191], v242 offset:6400\n\t"
            "v_cvt_pk_bf16_f32 v177, v178, v179\n\t"
            "s_waitcnt lgkmcnt(2)\n\t"
            "v_mfma_f32_16x16x32_bf16 v[144:147], v[8:11], v[192:195], v[144:147]\n\t"
            "v_cvt_pk_bf16_f32 v178, v180, v181\n\t"
            "v_cvt_pk_bf16_f32 v179, v182, v183\n\t"
            "v_mfma_f32_16x16x32_bf16 v[148:151], v[32:35], v[192:195], v[148:151]\n\t"
            "buffer_store_dwordx4 v[176:179], v196, s[40:43], s45 offen offset:128 sc1\n\t"
            "v_mfma_f32_16x16x32_bf16 v[168:171], v[56:59], v[192:195], v[168:171]\n\t"
            "v_lshlrev_b32 v152, 16, v224\n\t"
            "v_and_b32 v153, 0xffff0000, v224\n\t"
            "v_mfma_f32_16x16x32_bf16 v[172:175], v[80:83], v[192:195], v[172:175]\n\t"
            "v_lshlrev_b32 v154, 16, v225\n\t"
            "v_and_b32 v155, 0xffff0000, v225\n\t"
            "v_mfma_f32_16x16x32_bf16 v[156:159], v[104:107], v[192:195], v[156:159]\n\t"
            "v_lshlrev_b32 v180, 16, v226\n\t"
            "v_and_b32 v181, 0xffff0000, v226\n\t"
            "v_mfma_f32_16x16x32_bf16 v[164:167], v[128:131], v[192:195], v[164:167]\n\t"
            "ds_read_b128 v[192:195], v243 offset:6400\n\t"
            "v_lshlrev_b32 v182, 16, v227\n\t"
            "s_waitcnt lgkmcnt(2)\n\t"
            "v_mfma_f32_16x16x32_bf16 v[144:147], v[12:15], v[184:187], v[144:147]\n\t"
            "v_and_b32 v183, 0xffff0000, v227\n\t"
            "v_lshlrev_b32 v160, 16, v228\n\t"
            "v_mfma_f32_16x16x32_bf16 v[148:151], v[36:39], v[184:187], v[148:151]\n\t"
            "v_and_b32 v161, 0xffff0000, v228\n\t"
            "v_lshlrev_b32 v162, 16, v229\n\t"
            "s_waitcnt lgkmcnt(1)\n\t"
            "v_mfma_f32_16x16x32_bf16 v[144:147], v[16:19], v[188:191], v[144:147]\n\t"
            "v_and_b32 v163, 0xffff0000, v229\n\t"
            "v_lshlrev_b32 v176, 16, v230\n\t"
            "v_mfma_f32_16x16x32_bf16 v[168:171], v[60:63], v[184:187], v[168:171]\n\t"
            "v_and_b32 v177, 0xffff0000, v230\n\t"
            "v_lshlrev_b32 v178, 16, v231\n\t"
            "v_mfma_f32_16x16x32_bf16 v[148:151], v[40:43], v[188:191], v[148:151]\n\t"
            "v_and_b32 v179, 0xffff0000, v231\n\t"
            "v_mfma_f32_16x16x32_bf16 v[172:175], v[84:87], v[184:187], v[172:175]\n\t"
            "v_mfma_f32_16x16x32_bf16 v[168:171], v[64:67], v[188:191], v[168:171]\n\t"
            "v_mfma_f32_16x16x32_bf16 v[156:159], v[108:111], v[184:187], v[156:159]\n\t"
            "s_waitcnt lgkmcnt(0)\n\t"
            "v_mfma_f32_16x16x32_bf16 v[144:147], v[20:23], v[192:195], v[144:147]\n\t"
            "v_mfma_f32_16x16x32_bf16 v[148:151], v[44:47], v[192:195], v[148:151]\n\t"
            "v_mfma_f32_16x16x32_bf16 v[164:167], v[132:135], v[184:187], v[164:167]\n\t"
            "ds_read_b128 v[184:187], v242 offset:12288\n\t"
            "v_mfma_f32_16x16x32_bf16 v[172:175], v[88:91], v[188:191], v[172:175]\n\t"
            "v_mfma_f32_16x16x32_bf16 v[168:171], v[68:71], v[192:195], v[168:171]\n\t"
            "v_mfma_f32_16x16x32_bf16 v[156:159], v[112:115], v[188:191], v[156:159]\n\t"
            "s_nop 1\n\t"
            "v_cvt_pk_bf16_f32 v144, v144, v145\n\t"
            "v_mfma_f32_16x16x32_bf16 v[164:167], v[136:139], v[188:191], v[164:167]\n\t"
            "ds_read_b128 v[188:191], v243 offset:12288\n\t"
            "v_cvt_pk_bf16_f32 v145, v146, v147\n\t"
            "v_mfma_f32_16x16x32_bf16 v[172:175], v[92:95], v[192:195], v[172:175]\n\t"
            "v_cvt_pk_bf16_f32 v146, v148, v149\n\t"
            "v_cvt_pk_bf16_f32 v147, v150, v151\n\t"
            "v_mfma_f32_16x16x32_bf16 v[156:159], v[116:119], v[192:195], v[156:159]\n\t"
            "buffer_store_dwordx4 v[144:147], v197, s[40:43], s45 offen sc1\n\t"
            "v_mfma_f32_16x16x32_bf16 v[164:167], v[140:143], v[192:195], v[164:167]\n\t"
            "ds_read_b128 v[192:195], v242 offset:12416\n\t"
            "v_cvt_pk_bf16_f32 v168, v168, v169\n\t"
            "s_waitcnt lgkmcnt(2)\n\t"
            "v_mfma_f32_16x16x32_bf16 v[152:155], v[0:3], v[184:187], v[152:155]\n\t"
            "v_cvt_pk_bf16_f32 v169, v170, v171\n\t"
            "v_cvt_pk_bf16_f32 v170, v172, v173\n\t"
            "v_mfma_f32_16x16x32_bf16 v[180:183], v[24:27], v[184:187], v[180:183]\n\t"
            "v_cvt_pk_bf16_f32 v171, v174, v175\n\t"
            "v_cvt_pk_bf16_f32 v156, v156, v157\n\t"
            "v_mfma_f32_16x16x32_bf16 v[160:163], v[48:51], v[184:187], v[160:163]\n\t"
            "buffer_store_dwordx4 v[168:171], v197, s[40:43], s45 offen offset:64 sc1\n\t"
            "v_mfma_f32_16x16x32_bf16 v[176:179], v[72:75], v[184:187], v[176:179]\n\t"
            "v_cvt_pk_bf16_f32 v157, v158, v159\n\t"
            "v_cvt_pk_bf16_f32 v158, v164, v165\n\t"
            "s_waitcnt lgkmcnt(1)\n\t"
            "v_mfma_f32_16x16x32_bf16 v[152:155], v[4:7], v[188:191], v[152:155]\n\t"
            "v_cvt_pk_bf16_f32 v159, v166, v167\n\t"
            "v_lshlrev_b32 v164, 16, v232\n\t"
            "v_mfma_f32_16x16x32_bf16 v[180:183], v[28:31], v[188:191], v[180:183]\n\t"
            "buffer_store_dwordx4 v[156:159], v197, s[40:43], s45 offen offset:128 sc1\n\t"
            "v_mfma_f32_16x16x32_bf16 v[160:163], v[52:55], v[188:191], v[160:163]\n\t"
            "v_and_b32 v165, 0xffff0000, v232\n\t"
            "v_lshlrev_b32 v166, 16, v233\n\t"
            "v_mfma_f32_16x16x32_bf16 v[176:179], v[76:79], v[188:191], v[176:179]\n\t"
            "v_and_b32 v167, 0xffff0000, v233\n\t"
            "v_lshlrev_b32 v156, 16, v234\n\t"
            "s_waitcnt lgkmcnt(0)\n\t"
            "v_mfma_f32_16x16x32_bf16 v[152:155], v[8:11], v[192:195], v[152:155]\n\t"
            "v_and_b32 v157, 0xffff0000, v234\n\t"
            "v_lshlrev_b32 v158, 16, v235\n\t"
            "v_mfma_f32_16x16x32_bf16 v[164:167], v[96:99], v[184:187], v[164:167]\n\t"
            "v_and_b32 v159, 0xffff0000, v235\n\t"
            "v_mfma_f32_16x16x32_bf16 v[164:167], v[100:103], v[188:191], v[164:167]\n\t"
            "s_nop 0\n\t"
            "v_mfma_f32_16x16x32_bf16 v[156:159], v[120:123], v[184:187], v[156:159]\n\t"
            "ds_read_b128 v[184:187], v243 offset:12416\n\t"
            "v_mfma_f32_16x16x32_bf16 v[156:159], v[124:127], v[188:191], v[156:159]\n\t"
            "ds_read_b128 v[188:191], v242 offset:12544\n\t"
            "v_mfma_f32_16x16x32_bf16 v[180:183], v[32:35], v[192:195], v[180:183]\n\t"
            "v_mfma_f32_16x16x32_bf16 v[160:163], v[56:59], v[192:195], v[160:163]\n\t"
            "v_mfma_f32_16x16x32_bf16 v[176:179], v[80:83], v[192:195], v[176:179]\n\t"
            "v_mfma_f32_16x16x32_bf16 v[164:167], v[104:107], v[192:195], v[164:167]\n\t"
            "v_mfma_f32_16x16x32_bf16 v[156:159], v[128:131], v[192:195], v[156:159]\n\t"
            "ds_read_b128 v[192:195], v243 offset:12544\n\t"
            "s_waitcnt lgkmcnt(2)\n\t"
            "v_mfma_f32_16x16x32_bf16 v[152:155], v[12:15], v[184:187], v[152:155]\n\t"
            "v_mfma_f32_16x16x32_bf16 v[180:183], v[36:39], v[184:187], v[180:183]\n\t"
            "s_waitcnt lgkmcnt(1)\n\t"
            "v_mfma_f32_16x16x32_bf16 v[152:155], v[16:19], v[188:191], v[152:155]\n\t"
            "v_mfma_f32_16x16x32_bf16 v[160:163], v[60:63], v[184:187], v[160:163]\n\t"
            "v_mfma_f32_16x16x32_bf16 v[180:183], v[40:43], v[188:191], v[180:183]\n\t"
            "v_mfma_f32_16x16x32_bf16 v[176:179], v[84:87], v[184:187], v[176:179]\n\t"
            "s_waitcnt lgkmcnt(0)\n\t"
            "v_mfma_f32_16x16x32_bf16 v[152:155], v[20:23], v[192:195], v[152:155]\n\t"
            "v_mfma_f32_16x16x32_bf16 v[160:163], v[64:67], v[188:191], v[160:163]\n\t"
            "v_mfma_f32_16x16x32_bf16 v[180:183], v[44:47], v[192:195], v[180:183]\n\t"
            "v_mfma_f32_16x16x32_bf16 v[164:167], v[108:111], v[184:187], v[164:167]\n\t"
            "v_mfma_f32_16x16x32_bf16 v[176:179], v[88:91], v[188:191], v[176:179]\n\t"
            "v_mfma_f32_16x16x32_bf16 v[156:159], v[132:135], v[184:187], v[156:159]\n\t"
            "v_mfma_f32_16x16x32_bf16 v[160:163], v[68:71], v[192:195], v[160:163]\n\t"
            "s_nop 1\n\t"
            "v_cvt_pk_bf16_f32 v152, v152, v153\n\t"
            "v_mfma_f32_16x16x32_bf16 v[164:167], v[112:115], v[188:191], v[164:167]\n\t"
            "v_cvt_pk_bf16_f32 v153, v154, v155\n\t"
            "v_mfma_f32_16x16x32_bf16 v[176:179], v[92:95], v[192:195], v[176:179]\n\t"
            "v_cvt_pk_bf16_f32 v154, v180, v181\n\t"
            "v_cvt_pk_bf16_f32 v155, v182, v183\n\t"
            "v_mfma_f32_16x16x32_bf16 v[156:159], v[136:139], v[188:191], v[156:159]\n\t"
            "buffer_store_dwordx4 v[152:155], v198, s[40:43], s45 offen sc1\n\t"
            "v_mfma_f32_16x16x32_bf16 v[164:167], v[116:119], v[192:195], v[164:167]\n\t"
            "v_cvt_pk_bf16_f32 v160, v160, v161\n\t"
            "v_cvt_pk_bf16_f32 v161, v162, v163\n\t"
            "v_mfma_f32_16x16x32_bf16 v[156:159], v[140:143], v[192:195], v[156:159]\n\t"
            "v_cvt_pk_bf16_f32 v162, v176, v177\n\t"
            "v_cvt_pk_bf16_f32 v163, v178, v179\n\t"
            "buffer_store_dwordx4 v[160:163], v198, s[40:43], s45 offen offset:64 sc1\n\t"
            "s_nop 1\n\t"
            "v_cvt_pk_bf16_f32 v164, v164, v165\n\t"
            "v_cvt_pk_bf16_f32 v165, v166, v167\n\t"
            "s_nop 0\n\t"
            "v_cvt_pk_bf16_f32 v166, v156, v157\n\t"
            "v_cvt_pk_bf16_f32 v167, v158, v159\n\t"
            "buffer_store_dwordx4 v[164:167], v198, s[40:43], s45 offen offset:128 sc1\n\t"
            "s_nop 7\n\t"
            ".Lre%=:\n\t"
            : "+{v[224:227]}"(f0), "+{v[228:231]}"(f1), "+{v[232:235]}"(f2), "+{v242}"(r242), "+{v243}"(r243), "+{v[244:247]}"(r244),
              "+{s45}"(s45), "+{s46}"(s46), "+{s47}"(s47), "+{s48}"(s48), "+{s67}"(s67), "+{s76}"(s76), "+{s56}"(s56), "+{s44}"(s44), "+{s70}"(s70), "+{s21}"(s21)
            : B_VIN, B_SIN
            : "v0", "v1", "v2", "v3", "v4", "v5", "v6", "v7", "v8", "v9", "v10", "v11", "v12", "v13", "v14", "v15", "v16", "v17", "v18", "v19", "v20", "v21", "v22", "v23", "v24", "v25", "v26", "v27", "v28", "v29", "v30", "v31", "v32", "v33", "v34", "v35", "v36", "v37", "v38", "v39", "v40", "v41", "v42", "v43", "v44", "v45", "v46", "v47", "v48", "v49", "v50", "v51", "v52", "v53", "v54", "v55", "v56", "v57", "v58", "v59", "v60", "v61", "v62", "v63", "v64", "v65", "v66", "v67", "v68", "v69", "v70", "v71", "v72", "v73", "v74", "v75", "v76", "v77", "v78", "v79", "v80", "v81", "v82", "v83", "v84", "v85", "v86", "v87", "v88", "v89", "v90", "v91", "v92", "v93", "v94", "v95", "v96", "v97", "v98", "v99", "v100", "v101", "v102", "v103", "v104", "v105", "v106", "v107", "v108", "v109", "v110", "v111", "v112", "v113", "v114", "v115", "v116", "v117", "v118", "v119", "v120", "v121", "v122", "v123", "v124", "v125", "v126", "v127", "v128", "v129", "v130", "v131", "v132", "v133", "v134", "v135", "v136", "v137", "v138", "v139", "v140", "v141", "v142", "v143", "v144", "v145", "v146", "v147", "v148", "v149", "v150", "v151", "v152", "v153", "v154", "v155", "v156", "v157", "v158", "v159", "v160", "v161", "v162", "v163", "v164", "v165", "v166", "v167", "v168", "v169", "v170", "v171", "v172", "v173", "v174", "v175", "v176", "v177", "v178", "v179", "v180", "v181", "v182", "v183", "v184", "v185", "v186", "v187", "v188", "v189", "v190", "v191", "v192", "v193", "v194", "v195", "v196", "v197", "v198", "v199", "v200", "v201", "v202", "v203", "v204", "v205", "v206", "v207", "v208", "v209", "v210", "v211", "v212", "v213", "v214", "v215", "v216", "v217", "v218", "v219", "v220", "v221", "v222", "v223", "v236", "v237", "v248", "v249", "s26", "s68", "s69", "s79", "s36", "s37", "s38", "s57", "s58", "s59", "s49", "vcc", "scc", "m0", "memory");
        }
        asm volatile("wk_code_end1:");
        asm volatile("wk_code_end2:");

    }
}

extern "C" void kernel_launch(void* const* d_in, const int* in_sizes, int n_in,
                              void* d_out, int out_size, void* d_ws, size_t ws_size,
                              hipStream_t stream) {
    const unsigned short* x = (const unsigned short*)d_in[0];
    const unsigned short* dw = (const unsigned short*)d_in[1];
    const unsigned short* wqkv = (const unsigned short*)d_in[2];
    const unsigned short* wo = (const unsigned short*)d_in[3];
    unsigned short* out = (unsigned short*)d_out;
    u32x4* wf = (u32x4*)d_ws;
    win_attn_kernel<<<GRID, 512, 0, stream>>>(x, dw, wqkv, wo, wf, out);
}
```

```cpp
#include <hip/hip_runtime.h>
#include <hip/hip_bf16.h>
#include <stdint.h>


typedef unsigned u32x4 __attribute__((ext_vector_type(4)));

constexpr int IMG = 511;
constexpr int CH = 192;
constexpr int NWX = 73;
constexpr int NWIN = NWX * NWX;
constexpr int GRID = 256;
constexpr int NSHORT = 21 * GRID - NWIN;
constexpr int ROWBYTES = IMG * CH * 2;
constexpr int XBIAS = (IMG + 1) * CH * 2;
constexpr unsigned OOB = 0x40000000u;

constexpr int X_OFF = 32768;
constexpr int X1_OFF = 53632;
constexpr int O_OFF = 74496;
constexpr int V_OFF = 92928;
constexpr int KQL_OFF = 111360;
constexpr int OL_OFF = 128256;
constexpr int AT_OFF = 137472;
constexpr int CT_OFF = AT_OFF + 19584;
constexpr int RS_OFF = CT_OFF + 4096;
constexpr int WL_OFF = RS_OFF + 2304 + 64;
constexpr int LDS_BYTES = WL_OFF + 128;
#define RS_BASE (RS_OFF + (wave < 4 ? wave * 384 : 768 + wave * 192))
constexpr int TP_OFF = X1_OFF + 8192;
constexpr int STL_OFF = O_OFF + 12288;
#define LB_OFF(b) ((b) < 7 ? V_OFF + (b) * 6160 : X1_OFF + ((b) - 7) * 6160)
#define LPOS(wc) (((wc) & 1) ? 0 : 6)
constexpr unsigned CROW = 0x060a0230u;
constexpr unsigned CLANE = 0x00132C3Fu;

constexpr unsigned long long TOKMAP[6] = {0x20001c190f2a2202ull, 0x052517121f061a13ull, 0x181e08151d0b260cull, 0x1b2b2903102d0901ull, 0x04162e0e2824142cull, 0x110a072127232f0dull};
constexpr unsigned long long ROWMAP[8] = {0x08321b240f1c0006ull, 0x23310e2a09170d14ull, 0x210c2f030a273418ull, 0x1d2b11221f043739ull, 0x3234363a103e192dull, 0x291a20022c3f1605ull, 0x303a3c2628122e15ull, 0x39253d1e01070b13ull};

constexpr int WF_BYTES = 8 * 36 * 1024;
constexpr int EP_OFF = WF_BYTES;
constexpr int CN_OFF = EP_OFF + 256, FL_OFF = EP_OFF + 512;
constexpr int SH_OFF = EP_OFF + 1024;
constexpr int WS_BYTES = SH_OFF + GRID * 12288;
constexpr int GO_OFF = WL_OFF + 124;
constexpr int PG_OFF = WL_OFF + 96;
constexpr int SPIN_MAX = 1 << 20;

#define BAR() asm volatile("s_waitcnt lgkmcnt(0)\n\ts_barrier" ::: "memory")
#define OPQ(x) asm volatile("" : "+v"(x))
__host__ __device__ __forceinline__ constexpr unsigned byte_of(unsigned long long a, unsigned long long b, int i) {
    return (unsigned)(((i & 8) ? b : a) >> (8 * (i & 7))) & 0xFFu;
}
__host__ __device__ __forceinline__ constexpr void seq_to_win(int seq, int& wy, int& wx) {
    const bool last = seq >= 5110;
    const int s = last ? 35 : seq / 146, r = seq - 146 * s;
    const int kx = last ? r / 6 : r >> 2, pos = r - (last ? 6 : 4) * kx;
    const bool low = last && pos >= (kx == 36 ? 2 : 4);
    wy = low ? 72 : 2 * s + (pos & 1);
    wx = 2 * kx + (low ? (kx == 36 ? 0 : pos - 4) : pos >> 1);
}
__host__ __device__ __forceinline__ constexpr int win_to_seq(int wy, int wx) {
    const int kx = wx >> 1;
    return wy == 72 ? 5110 + 6 * kx + (kx == 36 ? 2 : 4 + (wx & 1)) : 146 * (wy >> 1) + (wy >= 70 ? 6 : 4) * kx + 2 * (wx & 1) + (wy & 1);
}
__host__ __device__ __forceinline__ constexpr int block_of(int wy, int wx) { return wy >= 70 ? 1295 + (wx & ~1) + (wy == 72) : 37 * (wy >> 1) + (wx >> 1); }
__host__ __device__ __forceinline__ constexpr void block_pos(int b, int& ky, int& kx) {
    if (b >= 1295) { kx = (b - 1295) >> 1; ky = 70 + 2 * ((b - 1295) & 1); }
    else { const int s = b / 37; kx = b - 37 * s; ky = 2 * s; }
}
__host__ __device__ __forceinline__ constexpr unsigned wl_entry(int seq) {
    int wy = 0, wx = 0;
    seq_to_win(seq, wy, wx);
    return (unsigned)((wy * 7 * IMG + wx * 7) * (CH * 2)) | (wy == 0 ? 0x08000000u : 0u) | (wx == 0 ? 0x10000000u : 0u) |
           (wx == NWX - 1 ? 0x20000000u : 0u) | ((unsigned)(wx & 1) << 30) | ((unsigned)(wy & 1) << 31);
}
__host__ __device__ __forceinline__ constexpr unsigned dma_off(int lane) {
    const int col = (lane * 43) >> 8;
    return (unsigned)(col * (CH * 2 - 96) + 16 * lane) | (col == 0 ? 0x10000000u : 0u) | (col == 8 ? 0x20000000u : 0u);
}

__host__ __device__ __forceinline__ constexpr unsigned pf_off(int lane) {
    const int l = lane < 63 ? lane : 62, r = l / 21;
    return (unsigned)(r * ROWBYTES + (l - 21 * r) * 128) | (r == 0 ? 0x08000000u : 0u);
}
constexpr int RECN = 80;
struct Tables { unsigned rec[GRID][RECN]; unsigned img[768]; };
constexpr Tables make_tables() {
    Tables t = {};
    for (int bx = 0; bx < GRID; ++bx) {
        unsigned* r = t.rec[bx];
        const int q0 = (bx & 7) * 32 + (bx >> 3), cnt = q0 < NSHORT ? 20 : 21, start = NWIN - cnt - (q0 < NSHORT ? 20 * q0 : 21 * q0 - NSHORT);
        for (int i = 0; i < 32; ++i) r[i] = i < cnt ? wl_entry(start + i) : 0x7ffffffu;
        int wy = 0, wx = 0;
        seq_to_win(start, wy, wx);
        const int key0 = block_of(wy, wx);
        seq_to_win(start + cnt - 1, wy, wx);
        const int nblk = block_of(wy, wx) - key0 + 1;
        for (int b = 0; b < 8; ++b) {
            int ky = 0, kx = 0;
            block_pos(key0 + b, ky, kx);
            r[64 + b] = b < nblk ? (unsigned)(((7 * ky + 5) * IMG + 14 * kx + 5) * (CH * 2) + XBIAS) | (kx == 36 ? 1u : 0u) : OOB;
            for (int c = 0; c < 4; ++c) {
                const int y = ky + (c >> 1), x = 2 * kx + (c & 1), i = win_to_seq(y, x) - start;
                const bool ok = b < nblk && y < NWX && x < NWX && i >= 0 && i < cnt;
                r[32 + 4 * b + c] = ok ? (unsigned)(((7 * y + LPOS(y)) * IMG + 7 * x + LPOS(x)) * (CH * 2) + i) : OOB + 47;
            }
        }
        r[72] = q0; r[73] = cnt;
    }
    for (int lane = 0; lane < 64; ++lane) {
        for (int e = 0; e < 3; ++e) {
            const int tk = byte_of(TOKMAP[2 * e], TOKMAP[2 * e + 1], lane & 15);
            t.img[192 + 4 * lane + e] = (unsigned)((tk / 7) * ROWBYTES + (tk % 7) * (CH * 2)) + 16 * (lane >> 4);
        }
        t.img[448 + 4 * lane] = dma_off(lane);
        t.img[449 + 4 * lane] = pf_off(lane);
    }
    return t;
}
static __constant__ const Tables TAB = make_tables();
typedef __attribute__((address_space(1))) unsigned gu32;
typedef __attribute__((address_space(1))) unsigned long long gu64;
#define RLX_AGENT __ATOMIC_RELAXED, __HIP_MEMORY_SCOPE_AGENT
#define TAG_OF(e) (((unsigned long long)~((e) + 1u) << 32) | ((e) + 1u))

__global__ __launch_bounds__(512, 2) void win_attn_kernel(const unsigned short* __restrict__ x, const unsigned short* __restrict__ dw,
                                                          const unsigned short* __restrict__ wqkv, const unsigned short* __restrict__ wo,
                                                          u32x4* wf, unsigned short* __restrict__ out) {
    __shared__ __attribute__((aligned(16))) char smem[LDS_BYTES];

    asm volatile("wk_code_begin:");
    const int tid = threadIdx.x;
    const int wave = __builtin_amdgcn_readfirstlane(tid >> 6);
    const int w = wave & 3;
    const unsigned ldsb = (unsigned)(size_t)smem;
#define ROLE_SETUP                                                                                 \
    int bx = blockIdx.x;                                                                           \
    asm volatile("" : "+s"(bx));                                                                   \
    bx = __builtin_amdgcn_readfirstlane(bx);                                                       \
    const unsigned* rec = TAB.rec[bx];                                                             \
    const unsigned en0 = rec[0];                                                                   \
    const int q0 = rec[72], cnt = rec[73];
    asm volatile(
        "s_load_dwordx8 s[24:31], %[ka], 0x0\n\t"
        "s_load_dwordx2 s[32:33], %[ka], 0x20\n\t"
        "s_mul_i32 s36, %[bx], %[recb]\n\t"
        "s_xor_b32 s37, %[wave], 4\n\t"
        "s_lshl2_add_u32 s38, s37, s36\n\t"
        "s_load_dword s38, %[tab], s38 offset:0x100\n\t"
        "s_load_dword s39, %[tab], s36\n\t"
        "v_mbcnt_lo_u32_b32 v1, -1, 0\n\t"
        "v_mbcnt_hi_u32_b32 v1, -1, v1\n\t"
        "s_and_b32 s44, %[bx], 7\n\t"
        "s_lshl_b32 s44, s44, 5\n\t"
        "s_lshr_b32 s45, %[bx], 3\n\t"
        "s_add_u32 s44, s44, s45\n\t"
        "v_lshlrev_b32 v2, 4, v1\n\t"
        "s_mov_b32 s43, 0x20000\n\t"
        "s_cmp_ge_u32 s44, 36\n\t"
        "s_cbranch_scc1 .Lnp%=\n\t"
        "s_mul_i32 s45, s44, 6\n\t"
        "s_lshl1_add_u32 s46, s44, 210\n\t"
        "s_cmp_ge_u32 %[wave], 6\n\t"
        "s_cselect_b64 vcc, -1, 0\n\t"
        "s_cselect_b32 s45, s46, s45\n\t"
        "s_movk_i32 s52, 1152\n\t"
        "s_cselect_b32 s52, 384, s52\n\t"
        "s_cselect_b32 s53, 0, 0xbbaaaaab\n\t"
        "s_add_u32 s45, s45, %[wave]\n\t"
        "s_mul_i32 s46, s45, 171\n\t"
        "s_lshr_b32 s46, s46, 10\n\t"
        "s_mul_i32 s47, s46, 6\n\t"
        "s_sub_u32 s47, s45, s47\n\t"
        "s_mul_i32 s48, s46, 43\n\t"
        "s_lshr_b32 s48, s48, 8\n\t"
        "s_mul_i32 s49, s48, 6\n\t"
        "s_sub_u32 s49, s46, s49\n\t"
        "s_mul_i32 s50, s48, 48\n\t"
        "s_cmp_lt_u32 s49, 3\n\t"
        "s_cselect_b32 s34, 1.0, 0x3e553b94\n\t"
        "s_movk_i32 s51, -48\n\t"
        "s_cselect_b32 s51, 192, s51\n\t"
        "s_cmp_lt_u32 s48, 4\n\t"
        "s_cselect_b32 s51, s51, s50\n\t"
        "s_cselect_b32 s34, s34, 1.0\n\t"
        "s_add_u32 s51, s51, s50\n\t"
        "s_lshl4_add_u32 s51, s49, s51\n\t"
        "s_and_b32 s49, s49, 1\n\t"
        "s_mul_i32 s49, s49, 12\n\t"
        "s_add_u32 s49, s49, 576\n\t"
        "s_cmp_ge_u32 %[wave], 6\n\t"
        "s_cselect_b32 s49, s49, 0\n\t"
        "s_sub_u32 s51, s51, s49\n\t"
        "s_and_b32 s49, s47, 1\n\t"
        "s_mul_i32 s49, s49, 88\n\t"
        "s_lshl3_add_u32 s47, s47, s49\n\t"
        "v_lshrrev_b32 v3, 5, v1\n\t"
        "v_mul_u32_u24 v3, 48, v3\n\t"
        "v_lshrrev_b32 v4, 1, v1\n\t"
        "v_and_or_b32 v3, v4, 8, v3\n\t"
        "v_add_u32 v3, s47, v3\n\t"
        "v_mul_u32_u24 v3, s52, v3\n\t"
        "v_and_b32 v4, 15, v1\n\t"
        "v_and_b32 v5, 12, v1\n\t"
        "v_cndmask_b32 v5, 0, v5, vcc\n\t"
        "v_add3_u32 v5, v4, v5, s51\n\t"
        "v_lshl_add_u32 v3, v5, 1, v3\n\t"
        "v_mov_b64 v[8:9], 0\n\t"
        "v_mov_b64 v[10:11], 0\n\t"
        "v_mov_b64 v[12:13], 0\n\t"
        "v_mov_b64 v[14:15], 0\n\t"
        "s_mul_i32 s42, s52, 192\n\t"
        "s_cmp_ge_u32 %[wave], 6\n\t"
        "s_waitcnt lgkmcnt(0)\n\t"
        "s_cselect_b64 s[40:41], s[30:31], s[28:29]\n\t"
        "s_and_b32 s41, s41, 0xffff\n\t"
        "buffer_load_short_d16_hi v8, v3, s[40:43], 0 offen\n\t"
        "buffer_load_short_d16_hi v9, v3, s[40:43], s52 offen\n\t"
        "s_lshl_b32 s46, s52, 1\n\t"
        "buffer_load_short_d16_hi v10, v3, s[40:43], s46 offen\n\t"
        "s_add_u32 s46, s46, s52\n\t"
        "buffer_load_short_d16_hi v11, v3, s[40:43], s46 offen\n\t"
        "s_add_u32 s46, s46, s52\n\t"
        "buffer_load_short_d16_hi v12, v3, s[40:43], s46 offen\n\t"
        "s_add_u32 s46, s46, s52\n\t"
        "buffer_load_short_d16_hi v13, v3, s[40:43], s46 offen\n\t"
        "s_add_u32 s46, s46, s52\n\t"
        "buffer_load_short_d16_hi v14, v3, s[40:43], s46 offen\n\t"
        "s_add_u32 s46, s46, s52\n\t"
        "buffer_load_short_d16_hi v15, v3, s[40:43], s46 offen\n\t"
        "v_mov_b32 v6, %[ep]\n\t"
        "global_load_dword v7, v6, s[32:33] sc1\n\t"
        "v_mov_b32 v5, %[red]\n\t"
        "v_mad_u32_u24 v4, v4, %[c144], v5\n\t"
        "v_lshrrev_b32 v5, 2, v1\n\t"
        "v_and_b32 v5, 12, v5\n\t"
        "s_lshl_b32 s46, %[wave], 4\n\t"
        "v_add3_u32 v5, v4, v5, s46\n\t"
        "s_mov_b32 s40, s32\n\t"
        "s_and_b32 s41, s33, 0xffff\n\t"
        "s_mov_b32 s42, %[wsb]\n\t"
        "s_lshl_b32 s45, s45, 10\n\t"
        "s_waitcnt vmcnt(1)\n\t"
        "v_add_f32 v16, v8, v10\n\t"
        "v_add_f32 v17, v9, v11\n\t"
        "v_add_f32 v18, v12, v14\n\t"
        "v_add_f32 v19, v13, v15\n\t"
        "v_add_f32 v16, v16, v18\n\t"
        "v_add_f32 v17, v17, v19\n\t"
        "v_add_f32 v16, v16, v17\n\t"
        "ds_write_b32 v5, v16\n\t"
        "s_waitcnt lgkmcnt(0)\n\t"
        "s_barrier\n\t"
        "ds_read_b128 v[16:19], v4\n\t"
        "ds_read_b128 v[20:23], v4 offset:16\n\t"
        "ds_read_b128 v[24:27], v4 offset:32\n\t"
        "ds_read_b128 v[28:31], v4 offset:48\n\t"
        "ds_read_b128 v[32:35], v4 offset:64\n\t"
        "ds_read_b128 v[36:39], v4 offset:80\n\t"
        "s_waitcnt lgkmcnt(4)\n\t"
        "v_add_f32 v16, v16, v18\n\t"
        "v_add_f32 v17, v17, v19\n\t"
        "v_add_f32 v20, v20, v22\n\t"
        "v_add_f32 v21, v21, v23\n\t"
        "s_waitcnt lgkmcnt(2)\n\t"
        "v_add_f32 v24, v24, v26\n\t"
        "v_add_f32 v25, v25, v27\n\t"
        "v_add_f32 v28, v28, v30\n\t"
        "v_add_f32 v29, v29, v31\n\t"
        "v_add_f32 v16, v16, v20\n\t"
        "v_add_f32 v17, v17, v21\n\t"
        "s_waitcnt lgkmcnt(0)\n\t"
        "v_add_f32 v32, v32, v34\n\t"
        "v_add_f32 v33, v33, v35\n\t"
        "v_add_f32 v36, v36, v38\n\t"
        "v_add_f32 v37, v37, v39\n\t"
        "v_add_f32 v24, v24, v28\n\t"
        "v_add_f32 v25, v25, v29\n\t"
        "v_add_f32 v32, v32, v36\n\t"
        "v_add_f32 v33, v33, v37\n\t"
        "v_add_f32 v16, v16, v24\n\t"
        "v_add_f32 v17, v17, v25\n\t"
        "v_add_f32 v16, v16, v32\n\t"
        "v_add_f32 v17, v17, v33\n\t"
        "v_add_f32 v16, v16, v17\n\t"
        "v_mul_f32 v16, s53, v16\n\t"
        "v_mul_f32 v16, s34, v16\n\t"
        "v_fma_f32 v8, v8, s34, v16\n\t"
        "v_fma_f32 v9, v9, s34, v16\n\t"
        "v_fma_f32 v10, v10, s34, v16\n\t"
        "v_fma_f32 v11, v11, s34, v16\n\t"
        "v_fma_f32 v12, v12, s34, v16\n\t"
        "v_fma_f32 v13, v13, s34, v16\n\t"
        "v_fma_f32 v14, v14, s34, v16\n\t"
        "v_fma_f32 v15, v15, s34, v16\n\t"
        "v_cvt_pk_bf16_f32 v8, v8, v9\n\t"
        "v_cvt_pk_bf16_f32 v9, v10, v11\n\t"
        "v_cvt_pk_bf16_f32 v10, v12, v13\n\t"
        "v_cvt_pk_bf16_f32 v11, v14, v15\n\t"
        "buffer_store_dwordx4 v[8:11], v2, s[40:43], s45 offen sc1\n\t"
        "s_or_b32 s46, s44, s37\n\t"
        "s_cmp_lg_u32 s46, 0\n\t"
        "s_cbranch_scc1 .Lnc%=\n\t"
        "v_mov_b32 v3, 0\n\t"
        "global_store_dword v6, v3, s[32:33] offset:256 sc1\n\t"
        ".Lnc%=:\n\t"
        "v_lshl_add_u32 v6, s44, 3, v6\n\t"
        "s_waitcnt vmcnt(0)\n\t"
        "s_barrier\n\t"
        "s_cmp_lg_u32 %[wave], 0\n\t"
        "s_cbranch_scc1 .Lnp%=\n\t"
        "v_add_u32 v4, 1, v7\n\t"
        "v_not_b32 v5, v4\n\t"
        "s_mov_b64 exec, 1\n\t"
        "global_store_dwordx2 v6, v[4:5], s[32:33] offset:512 sc1\n\t"
        "s_mov_b64 exec, -1\n\t"
        ".Lnp%=:\n\t"
        "s_getpc_b64 s[40:41]\n\t"
        ".Lwk_here%=:\n\t"
        "s_sub_u32 s40, s40, .Lwk_here%=-wk_code_begin\n\t"
        "s_subb_u32 s41, s41, 0\n\t"
        "s_and_b32 s41, s41, 0xffff\n\t"
        "s_mov_b32 s42, wk_code_end-wk_code_begin\n\t"
        "s_max_u32 s42, s42, wk_code_end1-wk_code_begin\n\t"
        "s_max_u32 s42, s42, wk_code_end2-wk_code_begin\n\t"
        "s_add_u32 m0, %[lds], %[sink]\n\t"
        "v_lshlrev_b32 v3, 7, v1\n\t"
        "s_lshl_b32 s45, %[wave], 13\n\t"
        "buffer_load_dword v3, s[40:43], s45 offen lds\n\t"
        "s_mov_b64 s[40:41], %[tab]\n\t"
        "s_and_b32 s41, s41, 0xffff\n\t"
        "s_mov_b32 s42, %[tabb]\n\t"
        "s_lshl_b32 s46, %[wave], 10\n\t"
        "s_add_u32 s47, s46, %[imgs]\n\t"
        "s_add_u32 s48, s46, %[imgd]\n\t"
        "s_lshl_b32 s49, %[wave], 8\n\t"
        "s_add_u32 s49, s49, %[mapd]\n\t"
        "s_add_u32 s50, s36, 128\n\t"
        "s_mov_b64 vcc, -1\n\t"
        "s_cmp_eq_u32 %[wave], 0\n\t"
        "s_cselect_b32 s47, s36, s47\n\t"
        "s_cselect_b32 s48, %[wld], s48\n\t"
        "s_cselect_b64 vcc, 0xff, vcc\n\t"
        "s_cmp_ge_u32 %[wave], 4\n\t"
        "s_cselect_b32 s47, s50, s47\n\t"
        "s_cselect_b32 s48, s49, s48\n\t"
        "s_cselect_b64 exec, 0xff, vcc\n\t"
        "s_add_u32 m0, %[lds], s48\n\t"
        "s_and_b32 s49, %[wave], 3\n\t"
        "buffer_load_dwordx4 v2, s[40:43], s47 offen lds\n\t"
        "s_mov_b64 exec, -1\n\t"
        "v_mul_u32_u24 v3, 43, v1\n\t"
        "v_lshrrev_b32 v3, 8, v3\n\t"
        "v_mad_u32_u24 v4, v3, %[c288], v2\n\t"
        "s_mul_i32 s49, s49, 96\n\t"
        "s_mul_i32 s50, s37, 6160\n\t"
        "s_add_u32 s50, s50, %[lb0]\n\t"
        "s_cmp_eq_u32 s37, 7\n\t"
        "s_cselect_b32 s50, %[lb7], s50\n\t"
        "s_add_u32 s46, s46, %[tp]\n\t"
        "s_waitcnt lgkmcnt(0)\n\t"
        "s_cmp_ge_u32 %[wave], 4\n\t"
        "s_cbranch_scc1 .Lib%=\n\t"
        "s_mov_b32 s40, s26\n\t"
        "s_and_b32 s41, s27, 0xffff\n\t"
        "s_movk_i32 s42, 3456\n\t"
        "s_add_u32 m0, %[lds], s46\n\t"
        "s_nop 0\n\t"
        "buffer_load_dwordx4 v4, s[40:43], s49 offen lds\n\t"
        ".Lib%=:\n\t"
        "s_sub_u32 s40, s24, %[xb]\n\t"
        "s_subb_u32 s41, s25, 0\n\t"
        "s_and_b32 s41, s41, 0xffff\n\t"
        "s_mov_b32 s42, %[xsz]\n\t"
        "s_cmp_eq_u32 s38, 2.0\n\t"
        "s_cbranch_scc1 .Lic%=\n\t"
        "s_mov_b32 exec_hi, 0xffff\n\t"
        "s_add_u32 m0, %[lds], s50\n\t"
        "s_mov_b32 s46, 4\n\t"
        "s_bitcmp1_b32 s38, 0\n\t"
        "s_cselect_b32 s47, 2.0, s38\n\t"
        "s_bitset0_b32 s38, 0\n\t"
        ".Lid%=:\n\t"
        "buffer_load_dwordx4 v2, s[40:43], s38 offen lds\n\t"
        "buffer_load_dwordx4 v2, s[40:43], s47 offen offset:768 lds\n\t"
        "s_add_u32 m0, m0, 1536\n\t"
        "s_add_u32 s38, s38, %[rb]\n\t"
        "s_add_u32 s47, s47, %[rb]\n\t"
        "s_sub_u32 s46, s46, 1\n\t"
        "s_cmp_lg_u32 s46, 0\n\t"
        "s_cbranch_scc1 .Lid%=\n\t"
        ".Lic%=:\n\t"
        "s_cmp_lt_u32 %[wave], 4\n\t"
        "s_cbranch_scc1 .Lie%=\n\t"
        "s_mov_b32 exec_hi, 0x3fffff\n\t"
        "s_and_b32 s46, s39, 0x10000000\n\t"
        "s_and_b32 s47, s39, 0x20000000\n\t"
        "v_cmp_eq_u32 vcc, 0, v3\n\t"
        "v_mov_b32 v5, s46\n\t"
        "v_mov_b32 v6, s47\n\t"
        "v_cndmask_b32 v5, 0, v5, vcc\n\t"
        "v_cmp_eq_u32 vcc, 8, v3\n\t"
        "s_and_b32 s46, s39, 0x7ffffff\n\t"
        "s_add_u32 s46, s46, s49\n\t"
        "v_cndmask_b32 v6, 0, v6, vcc\n\t"
        "v_or3_b32 v4, v4, v5, v6\n\t"
        "s_bitcmp1_b32 s39, 27\n\t"
        "s_cselect_b32 s47, 2.0, s46\n\t"
        "s_lshl_b32 s45, s37, 13\n\t"
        "s_add_u32 m0, %[lds], s45\n\t"
        "s_mov_b32 s48, 8\n\t"
        "buffer_load_dwordx4 v4, s[40:43], s47 offen lds\n\t"
        ".Lia%=:\n\t"
        "s_add_u32 m0, m0, 872\n\t"
        "s_add_u32 s46, s46, %[rb]\n\t"
        "s_sub_u32 s48, s48, 1\n\t"
        "buffer_load_dwordx4 v4, s[40:43], s46 offen lds\n\t"
        "s_cmp_lg_u32 s48, 0\n\t"
        "s_cbranch_scc1 .Lia%=\n\t"
        ".Lie%=:\n\t"
        "s_mov_b64 exec, -1\n\t"
        :: [ka] "s"(__builtin_amdgcn_kernarg_segment_ptr()), [bx] "s"(blockIdx.x), [wave] "s"(wave), [tab] "s"(&TAB), [lds] "s"(ldsb),
           [red] "s"(ldsb + O_OFF), [c144] "s"(144), [c288] "s"(288), [recb] "n"(RECN * 4), [ep] "n"(EP_OFF), [wsb] "n"(WS_BYTES), [sink] "n"(X_OFF + 18432),
           [tabb] "n"(sizeof(Tables)), [imgs] "n"(GRID * RECN * 4 - 1024), [imgd] "n"(AT_OFF + 18816 - 1024), [mapd] "n"(TP_OFF + 3072), [wld] "n"(WL_OFF),
           [lb0] "n"(LB_OFF(0)), [lb7] "n"(LB_OFF(7)), [tp] "n"(TP_OFF), [xb] "n"(XBIAS), [xsz] "n"(IMG * IMG * CH * 2 + XBIAS), [rb] "n"(ROWBYTES)
        : "v1", "v2", "v3", "v4", "v5", "v6", "v7", "v8", "v9", "v10", "v11", "v12", "v13", "v14", "v15", "v16", "v17", "v18", "v19", "v20", "v21", "v22", "v23", "v24", "v25", "v26", "v27", "v28", "v29", "v30", "v31", "v32", "v33", "v34", "v35", "v36", "v37", "v38", "v39", "s24", "s25", "s26", "s27", "s28", "s29", "s30", "s31", "s32", "s33", "s34", "s36", "s37", "s38", "s39", "s40", "s41", "s42", "s43", "s44", "s45", "s46", "s47", "s48", "s49", "s50", "s51", "s52", "s53", "m0", "vcc", "scc", "memory");
#define WAIT_GO                                                                                    \
        {                                                                                          \
            unsigned go_;                                                                          \
            asm volatile(".Lgo%=:\n\tds_read_b32 %0, %1\n\ts_waitcnt lgkmcnt(0)\n\tv_cmp_ne_u32 vcc, -1, %0\n\ts_nop 4\n\ts_cbranch_vccnz .Lgo%=" \
                         : "=&v"(go_) : "v"(ldsb + GO_OFF) : "vcc", "memory");                     \
        }
#define LANE_SETUP                                                                                 \
    int lane;                                                                                      \
    asm volatile("v_mbcnt_lo_u32_b32 %0, -1, 0\n\tv_mbcnt_hi_u32_b32 %0, -1, %0" : "=v"(lane));    \
    __builtin_assume(lane >= 0 && lane < 64);                                                      \
    const int g = lane >> 4, l15 = lane & 15, sw = l15 >> 1;                                       \
    int fE = X_OFF + l15 * 384 + ((g ^ sw) << 4);                                                  \
    OPQ(fE);
#define XPAT(r) (((r) < 48 ? (r) : 48) * 384 + (((2 * w) ^ (((r) >> 1) & 7)) << 4))
#define SPAT(r) ((w >> 1) * 1024 + (w & 1) * 8 + (r) * 16)
#define CONV_CONSTS(var) \
            const unsigned long long rlo = g == 0 ? ROWMAP[0] : (g == 1 ? ROWMAP[2] : (g == 2 ? ROWMAP[4] : ROWMAP[6])); \
            const unsigned long long rhi = g == 0 ? ROWMAP[1] : (g == 1 ? ROWMAP[3] : (g == 2 ? ROWMAP[5] : ROWMAP[7])); \
            int xrow = byte_of(rlo, rhi, l15); \
            if (lane == (int)((CLANE >> (8 * (var))) & 0xFF)) xrow = 48; \
            if (lane == 63) xrow = (CROW >> (8 * (var))) & 0xFF; \
            const unsigned pXw_ = XPAT(xrow); \
            const unsigned pSTw_ = (w >> 1) * 1024 + (w & 1) * 8 + xrow * 16; \
            const int oy = (0x5320 >> (4 * (l15 >> 2))) & 15, ox = (0x5320 >> (4 * (l15 & 3))) & 15; \
            const unsigned aB_ = ldsb + w * 8192 + (oy + (g >> 1)) * 872 + (ox + 2 * (g & 1)) * 96; \
            const int dyi = g - (l15 >> 3); \
            const unsigned aA_ = ldsb + AT_OFF + (l15 & 3) * 16 + \
                                 ((dyi < 0 || dyi > 2) ? 18816 : (dyi == 0 ? 0 : (dyi == 1 ? 6208 : 12544)) + ((l15 >> 2) & 1) * 3072 + w * 768); \
            const unsigned idx_ = (l15 & 3) * 0x5555u; \
            const unsigned se = (2 || l15 == 4 * g) ? 0x3F803F80u : 0u; \
            const u32x4 sel4 = {se, se, se, se};

    if (wave < 4) {
        ROLE_SETUP
        LANE_SETUP
        asm volatile("s_waitcnt vmcnt(0)" ::: "memory");
        if (lane < 48) {
            unsigned tp[9];
#pragma unroll
            for (int k = 0; k < 9; ++k) tp[k] = *(const unsigned short*)(smem + TP_OFF + w * 1024 + k * 96 + lane * 2);
            tp[4] |= 0x3F800000u;
#pragma unroll
            for (int dy = 0; dy < 3; ++dy) {
                char* p = smem + AT_OFF + (dy == 0 ? 0 : (dy == 1 ? 6208 : 12544)) + w * 768 + lane * 16;
                *(u32x4*)p = (u32x4){tp[3 * dy], tp[3 * dy + 1], tp[3 * dy + 2], 0u};
                *(u32x4*)(p + 3072) = (u32x4){0u, tp[3 * dy], tp[3 * dy + 1], tp[3 * dy + 2]};
            }
        }
        BAR();
        {
            const int dW = X_OFF + l15 * 384 + (((2 * w + (g >> 1)) ^ sw) << 4) + 8 * (g & 1);
            const unsigned rsb = ldsb + RS_BASE;
            const unsigned pE_ = fE - X_OFF, aO_ = ldsb + dW + (O_OFF - X_OFF), aZ_ = ldsb + AT_OFF + 18816, aVB_ = ldsb + V_OFF + w * 4608 + 1024 + lane * 8;
            unsigned aRSw_ = rsb + lane * 4;
            const unsigned pST_ = lane * 16, aPLw_ = rsb + lane * 2, aRSr_ = rsb + l15 * 4;
            const unsigned g16_ = g * 16, aPLr_ = (l15 & 3) ? aZ_ : rsb + g * 8;
            const unsigned loVL_ = ((2 * w + (l15 >> 3)) << 4) + (l15 & 7) * 2, loOL_ = ((2 * w + (g >> 1)) << 4) + 8 * (g & 1);
            const unsigned on = (l15 & 3) ? 0u : 0x3F803F80u;
            const u32x4 ones4 = {on, on, on, on};
            const unsigned snk = ldsb + O_OFF + 15360 + w * 192, kr = ldsb + KQL_OFF + l15 * 800;
            const bool ok1 = 16 + l15 < cnt, dg = g == (l15 >> 2);
            u32x4 pl = {kr + w * 192 + g * 16, (ok1 ? kr + 12800 + w * 192 : snk) + g * 16, dg ? kr + 768 + w * 4 : snk, dg && ok1 ? kr + 12800 + 768 + w * 4 : snk};
            unsigned pcl, pch, len;
            asm volatile("s_getpc_b64 s[36:37]\n.Lwk_h2%=:\n\ts_sub_u32 s36, s36, .Lwk_h2%=-wk_code_begin\n\ts_subb_u32 s37, s37, 0\n\t"
                         "s_mov_b32 %2, wk_code_end-wk_code_begin\n\ts_max_u32 %2, %2, wk_code_end1-wk_code_begin" : "={s36}"(pcl), "={s37}"(pch), "=s"(len) :: "scc");
            pcl = __builtin_amdgcn_readfirstlane(pcl); pch = __builtin_amdgcn_readfirstlane(pch); len = __builtin_amdgcn_readfirstlane(len);
            const unsigned long long tb = w == 0 ? ((unsigned long long)pch << 32) | pcl : (unsigned long long)wf;
            const u32x4 tr = {(unsigned)tb, (unsigned)(tb >> 32) & 0xffffu, w == 0 ? len : (w == 1 ? (unsigned)WF_BYTES : SH_OFF + bx * 12288 + 12288), 0x00020000u};
            const unsigned to = w == 0 ? ((bx >> 3) % 6) * 8192 : (w == 1 ? 6 * 36864 + ((bx >> 3) % 9) * 8192 : SH_OFF + bx * 12288 + (w - 2) * 8192);
            asm volatile("" :: "{v244}"(pE_), "{v245}"(aO_), "{v246}"(aZ_), "{v247}"(aVB_), "{v248}"(pST_), "{v249}"(aRSw_), "{v250}"(aPLw_), "{v251}"(aRSr_), "{v252}"(g16_), "{v253}"(aPLr_), "{v254}"(loVL_), "{v255}"(loOL_), "{v[240:243]}"(ones4), "{v[232:235]}"(pl), "{s[36:39]}"(tr), "{s41}"(to));
            if (w == 0) {
                gu64* fp = (gu64*)((char*)wf + (lane < 36 ? FL_OFF + lane * 8 : EP_OFF));
#pragma unroll 1
                for (int it = 0; it < SPIN_MAX; ++it) {
                    const unsigned long long f = __hip_atomic_load(fp, RLX_AGENT);
                    const unsigned ep = __builtin_amdgcn_readlane((unsigned)f, 63);
                    if ((__builtin_amdgcn_ballot_w64(f != TAG_OF(ep)) & 0xfffffffffull) == 0ull) break;
                    __builtin_amdgcn_s_sleep(1);
                }
                __builtin_amdgcn_fence(__ATOMIC_ACQUIRE, "wavefront");
                asm volatile("ds_write_b32 %0, %1" :: "v"(ldsb + GO_OFF), "v"(~0u) : "memory");
            } else WAIT_GO
            __builtin_amdgcn_s_setprio(2);
            unsigned s44 = cnt, s48 = ldsb + X_OFF, s70 = 0;
            unsigned s72 = ldsb + KQL_OFF + w * 192, s73 = ldsb + OL_OFF, s77 = ldsb + OL_OFF, s76 = ldsb + KQL_OFF + 768 + w * 4;
            const unsigned long long wb = (unsigned long long)wf;
            const u32x4 wr = {(unsigned)wb, (unsigned)(wb >> 32) & 0xffffu, (unsigned)WS_BYTES, 0x00020000u};
            asm volatile(
            "s_setprio 2\n\t"
            "s_add_u32 s49, s33, 0\n\t"
            "buffer_load_dwordx4 v[0:3], v248, s[28:31], s49 offen sc1\n\t"
            "s_add_u32 s49, s33, 1024\n\t"
            "buffer_load_dwordx4 v[4:7], v248, s[28:31], s49 offen sc1\n\t"
            "s_add_u32 s49, s33, 2048\n\t"
            "buffer_load_dwordx4 v[8:11], v248, s[28:31], s49 offen sc1\n\t"
            "s_add_u32 s49, s33, 3072\n\t"
            "buffer_load_dwordx4 v[12:15], v248, s[28:31], s49 offen sc1\n\t"
            "s_add_u32 s49, s33, 4096\n\t"
            "buffer_load_dwordx4 v[16:19], v248, s[28:31], s49 offen sc1\n\t"
            "s_add_u32 s49, s33, 5120\n\t"
            "buffer_load_dwordx4 v[20:23], v248, s[28:31], s49 offen sc1\n\t"
            "s_add_u32 s49, s33, 6144\n\t"
            "buffer_load_dwordx4 v[24:27], v248, s[28:31], s49 offen sc1\n\t"
            "s_add_u32 s49, s33, 7168\n\t"
            "buffer_load_dwordx4 v[28:31], v248, s[28:31], s49 offen sc1\n\t"
            "s_add_u32 s49, s33, 8192\n\t"
            "buffer_load_dwordx4 v[32:35], v248, s[28:31], s49 offen sc1\n\t"
            "s_add_u32 s49, s33, 9216\n\t"
            "buffer_load_dwordx4 v[36:39], v248, s[28:31], s49 offen sc1\n\t"
            "s_add_u32 s49, s33, 10240\n\t"
            "buffer_load_dwordx4 v[40:43], v248, s[28:31], s49 offen sc1\n\t"
            "s_add_u32 s49, s33, 11264\n\t"
            "buffer_load_dwordx4 v[44:47], v248, s[28:31], s49 offen sc1\n\t"
            "s_add_u32 s49, s33, 12288\n\t"
            "buffer_load_dwordx4 v[48:51], v248, s[28:31], s49 offen sc1\n\t"
            "s_add_u32 s49, s33, 13312\n\t"
            "buffer_load_dwordx4 v[52:55], v248, s[28:31], s49 offen sc1\n\t"
            "s_add_u32 s49, s33, 14336\n\t"
            "buffer_load_dwordx4 v[56:59], v248, s[28:31], s49 offen sc1\n\t"
            "s_add_u32 s49, s33, 15360\n\t"
            "buffer_load_dwordx4 v[60:63], v248, s[28:31], s49 offen sc1\n\t"
            "v_mov_b32 v224, s46\n\t"
            ".Ls1%=:\n\t"
            "ds_read_b128 v[220:223], v224\n\t"
            "s_waitcnt lgkmcnt(0)\n\t"
            "v_and_b32 v220, v220, v221\n\t"
            "v_and_b32 v222, v222, v223\n\t"
            "v_and_b32 v220, v220, v222\n\t"
            "s_nop 0\n\t"
            "v_readfirstlane_b32 s49, v220\n\t"
            "s_nop 3\n\t"
            "s_bitcmp1_b32 s49, 27\n\t"
            "s_cbranch_scc0 .Ls1%=\n\t"
            "s_nop 7\n\t"
            "v_add_u32 v237, s35, v248\n\t"
            "v_add_u32 v238, s56, v244\n\t"
            "v_xor_b32 v239, 64, v238\n\t"
            "v_add_u32 v236, s56, v248\n\t"
            "ds_read_b128 v[200:203], v237\n\t"
            "ds_read_b128 v[204:207], v237 offset:1024\n\t"
            "ds_read_b128 v[144:147], v238\n\t"
            "ds_read_b128 v[148:151], v239\n\t"
            "ds_read_b128 v[152:155], v238 offset:128\n\t"
            "ds_read_b128 v[156:159], v239 offset:128\n\t"
            "ds_read_b128 v[160:163], v238 offset:256\n\t"
            "ds_read_b128 v[164:167], v239 offset:256\n\t"
            "ds_read_b128 v[168:171], v238 offset:6144\n\t"
            "ds_read_b128 v[172:175], v239 offset:6144\n\t"
            "ds_read_b128 v[176:179], v238 offset:6272\n\t"
            "ds_read_b128 v[180:183], v239 offset:6272\n\t"
            "ds_read_b128 v[184:187], v238 offset:6400\n\t"
            "ds_read_b128 v[188:191], v239 offset:6400\n\t"
            "ds_read_b128 v[208:211], v237 offset:2048\n\t"
            "s_waitcnt lgkmcnt(14)\n\t"
            "ds_read_b128 v[224:227], v236 offset:12288\n\t"
            "s_waitcnt lgkmcnt(14)\n\t"
            "ds_read_b128 v[228:231], v236 offset:13312\n\t"
            "s_add_u32 s49, s33, 16384\n\t"
            "buffer_load_dwordx4 v[64:67], v248, s[28:31], s49 offen sc1\n\t"
            "s_add_u32 s49, s33, 17408\n\t"
            "buffer_load_dwordx4 v[68:71], v248, s[28:31], s49 offen sc1\n\t"
            "s_waitcnt vmcnt(12)\n\t"
            "s_waitcnt lgkmcnt(14)\n\t"
            "v_mfma_f32_16x16x32_bf16 v[192:195], v[0:3], v[144:147], 0\n\t"
            "buffer_store_dwordx4 v[200:203], v248, s[28:31], s34 offen\n\t"
            "s_waitcnt lgkmcnt(8)\n\t"
            "v_mfma_f32_16x16x32_bf16 v[212:215], v[0:3], v[168:171], 0\n\t"
            "buffer_store_dwordx4 v[204:207], v248, s[28:31], s34 offen offset:1024\n\t"
            "v_mfma_f32_16x16x32_bf16 v[192:195], v[4:7], v[148:151], v[192:195]\n\t"
            "s_waitcnt lgkmcnt(2)\n\t"
            "buffer_store_dwordx4 v[208:211], v248, s[28:31], s34 offen offset:2048\n\t"
            "v_mfma_f32_16x16x32_bf16 v[212:215], v[4:7], v[172:175], v[212:215]\n\t"
            "s_add_u32 s49, s33, 18432\n\t"
            "buffer_load_dwordx4 v[72:75], v248, s[28:31], s49 offen sc1\n\t"
            "v_mfma_f32_16x16x32_bf16 v[192:195], v[8:11], v[152:155], v[192:195]\n\t"
            "s_add_u32 s49, s33, 19456\n\t"
            "buffer_load_dwordx4 v[76:79], v248, s[28:31], s49 offen sc1\n\t"
            "v_mfma_f32_16x16x32_bf16 v[212:215], v[8:11], v[176:179], v[212:215]\n\t"
            "s_add_u32 s49, s33, 20480\n\t"
            "buffer_load_dwordx4 v[80:83], v248, s[28:31], s49 offen sc1\n\t"
            "v_mfma_f32_16x16x32_bf16 v[192:195], v[12:15], v[156:159], v[192:195]\n\t"
            "s_add_u32 s49, s33, 21504\n\t"
            "buffer_load_dwordx4 v[84:87], v248, s[28:31], s49 offen sc1\n\t"
            "v_mfma_f32_16x16x32_bf16 v[212:215], v[12:15], v[180:183], v[212:215]\n\t"
            "s_add_u32 s49, s33, 22528\n\t"
            "buffer_load_dwordx4 v[88:91], v248, s[28:31], s49 offen sc1\n\t"
            "v_mfma_f32_16x16x32_bf16 v[192:195], v[16:19], v[160:163], v[192:195]\n\t"
            "s_add_u32 s49, s33, 23552\n\t"
            "buffer_load_dwordx4 v[92:95], v248, s[28:31], s49 offen sc1\n\t"
            "v_mfma_f32_16x16x32_bf16 v[212:215], v[16:19], v[184:187], v[212:215]\n\t"
            "s_waitcnt lgkmcnt(1)\n\t"
            "v_add_f32 v224, v224, v226\n\t"
            "s_waitcnt lgkmcnt(0)\n\t"
            "v_add_f32 v228, v228, v230\n\t"
            "v_mfma_f32_16x16x32_bf16 v[192:195], v[20:23], v[164:167], v[192:195]\n\t"
            "v_add_f32 v224, v224, v228\n\t"
            "v_add_f32 v225, v225, v227\n\t"
            "v_mfma_f32_16x16x32_bf16 v[212:215], v[20:23], v[188:191], v[212:215]\n\t"
            "s_waitcnt vmcnt(15)\n\t"
            "s_add_u32 s49, s33, 24576\n\t"
            "buffer_load_dwordx4 v[96:99], v248, s[28:31], s49 offen sc1\n\t"
            "v_mfma_f32_16x16x32_bf16 v[196:199], v[24:27], v[144:147], 0\n\t"
            "s_add_u32 s49, s33, 25600\n\t"
            "buffer_load_dwordx4 v[100:103], v248, s[28:31], s49 offen sc1\n\t"
            "v_mfma_f32_16x16x32_bf16 v[216:219], v[24:27], v[168:171], 0\n\t"
            "s_add_u32 s49, s33, 26624\n\t"
            "buffer_load_dwordx4 v[104:107], v248, s[28:31], s49 offen sc1\n\t"
            "v_mfma_f32_16x16x32_bf16 v[196:199], v[28:31], v[148:151], v[196:199]\n\t"
            "s_add_u32 s49, s33, 27648\n\t"
            "buffer_load_dwordx4 v[108:111], v248, s[28:31], s49 offen sc1\n\t"
            "v_mfma_f32_16x16x32_bf16 v[216:219], v[28:31], v[172:175], v[216:219]\n\t"
            "s_add_u32 s49, s33, 28672\n\t"
            "buffer_load_dwordx4 v[112:115], v248, s[28:31], s49 offen sc1\n\t"
            "v_mfma_f32_16x16x32_bf16 v[196:199], v[32:35], v[152:155], v[196:199]\n\t"
            "s_add_u32 s49, s33, 29696\n\t"
            "buffer_load_dwordx4 v[116:119], v248, s[28:31], s49 offen sc1\n\t"
            "v_mfma_f32_16x16x32_bf16 v[216:219], v[32:35], v[176:179], v[216:219]\n\t"
            "v_add_f32 v229, v229, v231\n\t"
            "v_mul_f32 v224, 0x3baaaaab, v224\n\t"
            "v_mfma_f32_16x16x32_bf16 v[196:199], v[36:39], v[156:159], v[196:199]\n\t"
            "v_add_f32 v225, v225, v229\n\t"
            "v_mul_f32_e64 v224, v224, -v224\n\t"
            "v_mfma_f32_16x16x32_bf16 v[216:219], v[36:39], v[180:183], v[216:219]\n\t"
            "v_fmac_f32 v224, 0x3baaaaab, v225\n\t"
            "v_max_f32 v224, 0, v224\n\t"
            "v_mfma_f32_16x16x32_bf16 v[196:199], v[40:43], v[160:163], v[196:199]\n\t"
            "v_add_f32 v224, 0x3727c5ac, v224\n\t"
            "v_rsq_f32 v224, v224\n\t"
            "v_mfma_f32_16x16x32_bf16 v[216:219], v[40:43], v[184:187], v[216:219]\n\t"
            "ds_write_b32 v249, v224\n\t"
            "v_mfma_f32_16x16x32_bf16 v[196:199], v[44:47], v[164:167], v[196:199]\n\t"
            "ds_read_b32 v236, v251\n\t"
            "v_mfma_f32_16x16x32_bf16 v[216:219], v[44:47], v[188:191], v[216:219]\n\t"
            "ds_read_b32 v237, v251 offset:64\n\t"
            "s_waitcnt vmcnt(15)\n\t"
            "v_mfma_f32_16x16x32_bf16 v[200:203], v[48:51], v[144:147], 0\n\t"
            "s_add_u32 s49, s33, 30720\n\t"
            "buffer_load_dwordx4 v[120:123], v248, s[28:31], s49 offen sc1\n\t"
            "v_mfma_f32_16x16x32_bf16 v[220:223], v[48:51], v[168:171], 0\n\t"
            "s_add_u32 s49, s33, 31744\n\t"
            "buffer_load_dwordx4 v[124:127], v248, s[28:31], s49 offen sc1\n\t"
            "v_mfma_f32_16x16x32_bf16 v[200:203], v[52:55], v[148:151], v[200:203]\n\t"
            "s_add_u32 s49, s33, 32768\n\t"
            "buffer_load_dwordx4 v[128:131], v248, s[28:31], s49 offen sc1\n\t"
            "v_mfma_f32_16x16x32_bf16 v[220:223], v[52:55], v[172:175], v[220:223]\n\t"
            "s_add_u32 s49, s33, 33792\n\t"
            "buffer_load_dwordx4 v[132:135], v248, s[28:31], s49 offen sc1\n\t"
            "v_mfma_f32_16x16x32_bf16 v[200:203], v[56:59], v[152:155], v[200:203]\n\t"
            "s_add_u32 s49, s33, 34816\n\t"
            "buffer_load_dwordx4 v[136:139], v248, s[28:31], s49 offen sc1\n\t"
            "v_mfma_f32_16x16x32_bf16 v[220:223], v[56:59], v[176:179], v[220:223]\n\t"
            "s_add_u32 s49, s33, 35840\n\t"
            "buffer_load_dwordx4 v[140:143], v248, s[28:31], s49 offen sc1\n\t"
            "s_setprio 0\n\t"
            "v_mfma_f32_16x16x32_bf16 v[200:203], v[60:63], v[156:159], v[200:203]\n\t"
            "s_waitcnt lgkmcnt(1)\n\t"
            "v_mul_f32 v192, v236, v192\n\t"
            "v_mul_f32 v193, v236, v193\n\t"
            "v_mfma_f32_16x16x32_bf16 v[220:223], v[60:63], v[180:183], v[220:223]\n\t"
            "s_waitcnt lgkmcnt(0)\n\t"
            "v_mul_f32 v212, v237, v212\n\t"
            "v_mul_f32 v213, v237, v213\n\t"
            "v_mfma_f32_16x16x32_bf16 v[200:203], v[64:67], v[160:163], v[200:203]\n\t"
            "v_mul_f32 v194, v236, v194\n\t"
            "v_mul_f32 v195, v236, v195\n\t"
            "v_mfma_f32_16x16x32_bf16 v[220:223], v[64:67], v[184:187], v[220:223]\n\t"
            "v_cvt_pk_bf16_f32 v192, v192, v193\n\t"
            "v_mul_f32 v214, v237, v214\n\t"
            "v_mfma_f32_16x16x32_bf16 v[200:203], v[68:71], v[164:167], v[200:203]\n\t"
            "v_mul_f32 v215, v237, v215\n\t"
            "v_cvt_pk_bf16_f32 v212, v212, v213\n\t"
            "v_mfma_f32_16x16x32_bf16 v[220:223], v[68:71], v[188:191], v[220:223]\n\t"
            "s_waitcnt vmcnt(12)\n\t"
            "v_cvt_pk_bf16_f32 v193, v194, v195\n\t"
            "v_mfma_f32_16x16x32_bf16 v[204:207], v[72:75], v[144:147], 0\n\t"
            "v_cvt_pk_bf16_f32 v213, v214, v215\n\t"
            "v_mul_f32 v196, v236, v196\n\t"
            "v_mfma_f32_16x16x32_bf16 v[224:227], v[72:75], v[168:171], 0\n\t"
            "v_mul_f32 v197, v236, v197\n\t"
            "v_mul_f32 v198, v236, v198\n\t"
            "v_mfma_f32_16x16x32_bf16 v[204:207], v[76:79], v[148:151], v[204:207]\n\t"
            "v_mul_f32 v199, v236, v199\n\t"
            "v_mul_f32 v216, v237, v216\n\t"
            "v_mfma_f32_16x16x32_bf16 v[224:227], v[76:79], v[172:175], v[224:227]\n\t"
            "v_mul_f32 v217, v237, v217\n\t"
            "v_mul_f32 v218, v237, v218\n\t"
            "v_mfma_f32_16x16x32_bf16 v[204:207], v[80:83], v[152:155], v[204:207]\n\t"
            "v_mul_f32 v219, v237, v219\n\t"
            "v_cvt_pk_bf16_f32 v194, v196, v197\n\t"
            "v_mfma_f32_16x16x32_bf16 v[224:227], v[80:83], v[176:179], v[224:227]\n\t"
            "v_cvt_pk_bf16_f32 v195, v198, v199\n\t"
            "v_cvt_pk_bf16_f32 v214, v216, v217\n\t"
            "v_mfma_f32_16x16x32_bf16 v[204:207], v[84:87], v[156:159], v[204:207]\n\t"
            "v_cvt_pk_bf16_f32 v215, v218, v219\n\t"
            "v_mul_f32 v200, v236, v200\n\t"
            "v_mfma_f32_16x16x32_bf16 v[224:227], v[84:87], v[180:183], v[224:227]\n\t"
            "v_mul_f32 v201, v236, v201\n\t"
            "v_mul_f32 v220, v237, v220\n\t"
            "v_mfma_f32_16x16x32_bf16 v[204:207], v[88:91], v[160:163], v[204:207]\n\t"
            "v_mul_f32 v221, v237, v221\n\t"
            "v_mul_f32 v202, v236, v202\n\t"
            "v_mfma_f32_16x16x32_bf16 v[224:227], v[88:91], v[184:187], v[224:227]\n\t"
            "v_mul_f32 v203, v236, v203\n\t"
            "v_cvt_pk_bf16_f32 v200, v200, v201\n\t"
            "v_mfma_f32_16x16x32_bf16 v[204:207], v[92:95], v[164:167], v[204:207]\n\t"
            "v_mul_f32 v222, v237, v222\n\t"
            "v_mul_f32 v223, v237, v223\n\t"
            "v_mfma_f32_16x16x32_bf16 v[224:227], v[92:95], v[188:191], v[224:227]\n\t"
            "s_waitcnt vmcnt(6)\n\t"
            "v_cvt_pk_bf16_f32 v220, v220, v221\n\t"
            "v_mfma_f32_16x16x32_bf16 v[196:199], v[96:99], v[144:147], 0\n\t"
            "v_cvt_pk_bf16_f32 v201, v202, v203\n\t"
            "v_cvt_pk_bf16_f32 v221, v222, v223\n\t"
            "v_mfma_f32_16x16x32_bf16 v[216:219], v[96:99], v[168:171], 0\n\t"
            "v_mul_f32 v204, v236, v204\n\t"
            "v_mul_f32 v205, v236, v205\n\t"
            "v_mfma_f32_16x16x32_bf16 v[196:199], v[100:103], v[148:151], v[196:199]\n\t"
            "v_mul_f32 v224, v237, v224\n\t"
            "v_mul_f32 v225, v237, v225\n\t"
            "v_mfma_f32_16x16x32_bf16 v[216:219], v[100:103], v[172:175], v[216:219]\n\t"
            "v_mul_f32 v206, v236, v206\n\t"
            "v_mul_f32 v207, v236, v207\n\t"
            "v_mfma_f32_16x16x32_bf16 v[196:199], v[104:107], v[152:155], v[196:199]\n\t"
            "v_cvt_pk_bf16_f32 v204, v204, v205\n\t"
            "v_mul_f32 v226, v237, v226\n\t"
            "v_mfma_f32_16x16x32_bf16 v[216:219], v[104:107], v[176:179], v[216:219]\n\t"
            "v_mul_f32 v227, v237, v227\n\t"
            "v_cvt_pk_bf16_f32 v224, v224, v225\n\t"
            "v_mfma_f32_16x16x32_bf16 v[196:199], v[108:111], v[156:159], v[196:199]\n\t"
            "v_cvt_pk_bf16_f32 v205, v206, v207\n\t"
            "v_cvt_pk_bf16_f32 v225, v226, v227\n\t"
            "v_mfma_f32_16x16x32_bf16 v[216:219], v[108:111], v[180:183], v[216:219]\n\t"
            "ds_write_b128 v232, v[192:195]\n\t"
            "v_mfma_f32_16x16x32_bf16 v[196:199], v[112:115], v[160:163], v[196:199]\n\t"
            "ds_write_b128 v233, v[212:215]\n\t"
            "v_mfma_f32_16x16x32_bf16 v[216:219], v[112:115], v[184:187], v[216:219]\n\t"
            "v_mfma_f32_16x16x32_bf16 v[196:199], v[116:119], v[164:167], v[196:199]\n\t"
            "v_mfma_f32_16x16x32_bf16 v[216:219], v[116:119], v[188:191], v[216:219]\n\t"
            "s_waitcnt vmcnt(0)\n\t"
            "v_mfma_f32_16x16x32_bf16 v[208:211], v[120:123], v[144:147], 0\n\t"
            "v_mfma_f32_16x16x32_bf16 v[228:231], v[120:123], v[168:171], 0\n\t"
            "v_mfma_f32_16x16x32_bf16 v[208:211], v[124:127], v[148:151], v[208:211]\n\t"
            "v_mfma_f32_16x16x32_bf16 v[228:231], v[124:127], v[172:175], v[228:231]\n\t"
            "s_nop 1\n\t"
            "v_mul_f32 v196, v236, v196\n\t"
            "v_mfma_f32_16x16x32_bf16 v[208:211], v[128:131], v[152:155], v[208:211]\n\t"
            "v_mul_f32 v197, v236, v197\n\t"
            "v_mul_f32 v198, v236, v198\n\t"
            "v_mfma_f32_16x16x32_bf16 v[228:231], v[128:131], v[176:179], v[228:231]\n\t"
            "v_mul_f32 v199, v236, v199\n\t"
            "v_mul_f32 v216, v237, v216\n\t"
            "v_mfma_f32_16x16x32_bf16 v[208:211], v[132:135], v[156:159], v[208:211]\n\t"
            "v_mul_f32 v217, v237, v217\n\t"
            "v_mul_f32 v218, v237, v218\n\t"
            "v_mfma_f32_16x16x32_bf16 v[228:231], v[132:135], v[180:183], v[228:231]\n\t"
            "v_mul_f32 v219, v237, v219\n\t"
            "v_cvt_pk_bf16_f32 v206, v196, v197\n\t"
            "v_mfma_f32_16x16x32_bf16 v[208:211], v[136:139], v[160:163], v[208:211]\n\t"
            "v_cvt_pk_bf16_f32 v207, v198, v199\n\t"
            "v_cvt_pk_bf16_f32 v226, v216, v217\n\t"
            "v_mfma_f32_16x16x32_bf16 v[228:231], v[136:139], v[184:187], v[228:231]\n\t"
            "v_cvt_pk_bf16_f32 v227, v218, v219\n\t"
            "v_mfma_f32_16x16x32_bf16 v[208:211], v[140:143], v[164:167], v[208:211]\n\t"
            "ds_write_b128 v232, v[204:207] offset:64\n\t"
            "v_mfma_f32_16x16x32_bf16 v[228:231], v[140:143], v[188:191], v[228:231]\n\t"
            "ds_write_b128 v233, v[224:227] offset:64\n\t"
            "s_nop 4\n\t"
            "v_mul_f32 v208, v236, v208\n\t"
            "v_mul_f32 v209, v236, v209\n\t"
            "v_mul_f32 v210, v236, v210\n\t"
            "v_mul_f32 v211, v236, v211\n\t"
            "v_cvt_pk_bf16_f32 v202, v208, v209\n\t"
            "v_cvt_pk_bf16_f32 v203, v210, v211\n\t"
            "v_mul_f32 v228, v237, v228\n\t"
            "v_mul_f32 v229, v237, v229\n\t"
            "v_mfma_f32_16x16x16_bf16 v[196:199], v[200:201], v[202:203], 0\n\t"
            "v_mul_f32 v230, v237, v230\n\t"
            "v_mul_f32 v231, v237, v231\n\t"
            "v_cvt_pk_bf16_f32 v222, v228, v229\n\t"
            "v_cvt_pk_bf16_f32 v223, v230, v231\n\t"
            "s_nop 1\n\t"
            "v_mfma_f32_16x16x16_bf16 v[216:219], v[220:221], v[222:223], 0\n\t"
            "ds_write_b128 v232, v[200:203] offset:128\n\t"
            "v_mfma_f32_16x16x32_bf16 v[196:199], v[192:195], v[204:207], v[196:199]\n\t"
            "ds_write_b128 v233, v[220:223] offset:128\n\t"
            "s_nop 2\n\t"
            "v_mfma_f32_16x16x32_bf16 v[216:219], v[212:215], v[224:227], v[216:219]\n\t"
            "s_nop 2\n\t"
            "v_cndmask_b32_e64 v196, v196, v197, s[50:51]\n\t"
            "v_cndmask_b32_e64 v196, v196, v198, s[52:53]\n\t"
            "v_cndmask_b32_e64 v196, v196, v199, s[54:55]\n\t"
            "ds_write_b32 v234, v196\n\t"
            "s_nop 0\n\t"
            "v_cndmask_b32_e64 v216, v216, v217, s[50:51]\n\t"
            "v_cndmask_b32_e64 v216, v216, v218, s[52:53]\n\t"
            "v_cndmask_b32_e64 v216, v216, v219, s[54:55]\n\t"
            "ds_write_b32 v235, v216\n\t"
            "s_waitcnt lgkmcnt(0)\n\t"
            "s_nop 7\n\t"
            "s_cmp_lg_u32 s33, 0\n\t"
            "s_cbranch_scc1 .Lar%=\n\t"
            "s_mov_b64 exec, 1\n\t"
            "v_mov_b32 v216, 1\n\t"
            "s_mov_b32 s49, 295168\n\t"
            "buffer_atomic_add v216, off, s[28:31], s49\n\t"
            "s_mov_b64 exec, -1\n\t"
            ".Lar%=:\n\t"
            "v_mov_b32 v224, s46\n\t"
            ".Ls2%=:\n\t"
            "ds_read_b128 v[220:223], v224\n\t"
            "s_waitcnt lgkmcnt(0)\n\t"
            "v_and_b32 v220, v220, v221\n\t"
            "v_and_b32 v222, v222, v223\n\t"
            "v_and_b32 v220, v220, v222\n\t"
            "s_nop 0\n\t"
            "v_readfirstlane_b32 s49, v220\n\t"
            "s_nop 3\n\t"
            "s_bitcmp1_b32 s49, 31\n\t"
            "s_cbranch_scc0 .Ls2%=\n\t"
            "s_waitcnt lgkmcnt(0)\n\t"
            "s_nop 7\n\t"
            "s_setprio 0\n\t"
            "v_add_u32 v232, s48, v244\n\t"
            "v_add_u32 v208, s48, v248\n\t"
            "v_xor_b32 v233, 64, v232\n\t"
            "v_add_u32 v234, s72, v252\n\t"
            "v_cndmask_b32_e64 v235, v246, v234, s[50:51]\n\t"
            "v_cndmask_b32_e64 v234, v246, v234, s[26:27]\n\t"
            "v_mov_b32 v239, s76\n\t"
            "ds_read_b128 v[216:219], v232\n\t"
            "ds_read_b128 v[204:207], v208 offset:18816\n\t"
            "ds_read_b128 v[208:211], v208 offset:19840\n\t"
            "ds_read_b128 v[220:223], v233\n\t"
            "ds_read_b128 v[224:227], v232 offset:128\n\t"
            "ds_read_b128 v[188:191], v233 offset:6272\n\t"
            "ds_read_b128 v[192:195], v232 offset:6400\n\t"
            "ds_read_b128 v[200:203], v233 offset:6400\n\t"
            "ds_read_b32 v239, v239\n\t"
            "s_waitcnt lgkmcnt(8)\n\t"
            "v_mfma_f32_16x16x32_bf16 v[148:151], v[72:75], v[216:219], 0\n\t"
            "s_waitcnt lgkmcnt(7)\n\t"
            "v_add_f32 v204, v204, v206\n\t"
            "v_add_f32 v205, v205, v207\n\t"
            "s_waitcnt lgkmcnt(6)\n\t"
            "v_add_f32 v208, v208, v210\n\t"
            "v_add_f32 v209, v209, v211\n\t"
            "v_add_f32 v204, v204, v208\n\t"
            "v_add_f32 v205, v205, v209\n\t"
            "v_mfma_f32_16x16x32_bf16 v[208:211], v[96:99], v[216:219], 0\n\t"
            "v_mul_f32 v204, 0x3baaaaab, v204\n\t"
            "v_mul_f32_e64 v204, v204, -v204\n\t"
            "v_mfma_f32_16x16x32_bf16 v[212:215], v[120:123], v[216:219], 0\n\t"
            "v_fmac_f32 v204, 0x3baaaaab, v205\n\t"
            "v_max_f32 v204, 0, v204\n\t"
            "v_mfma_f32_16x16x32_bf16 v[144:147], v[0:3], v[216:219], 0\n\t"
            "v_add_f32 v204, 0x3727c5ac, v204\n\t"
            "v_rsq_f32 v204, v204\n\t"
            "s_nop 0\n\t"
            "ds_write_b32 v249, v204\n\t"
            "v_mfma_f32_16x16x32_bf16 v[204:207], v[24:27], v[216:219], 0\n\t"
            "ds_read_b32 v236, v251\n\t"
            "v_mfma_f32_16x16x32_bf16 v[152:155], v[48:51], v[216:219], 0\n\t"
            "ds_read_b128 v[216:219], v233 offset:128\n\t"
            "s_waitcnt lgkmcnt(8)\n\t"
            "v_mfma_f32_16x16x32_bf16 v[148:151], v[76:79], v[220:223], v[148:151]\n\t"
            "ds_read_b32 v237, v251 offset:64\n\t"
            "v_mfma_f32_16x16x32_bf16 v[208:211], v[100:103], v[220:223], v[208:211]\n\t"
            "ds_read_b32 v238, v251 offset:128\n\t"
            "v_mfma_f32_16x16x32_bf16 v[212:215], v[124:127], v[220:223], v[212:215]\n\t"
            "v_mfma_f32_16x16x32_bf16 v[144:147], v[4:7], v[220:223], v[144:147]\n\t"
            "v_mfma_f32_16x16x32_bf16 v[204:207], v[28:31], v[220:223], v[204:207]\n\t"
            "v_mfma_f32_16x16x32_bf16 v[152:155], v[52:55], v[220:223], v[152:155]\n\t"
            "ds_read_b128 v[220:223], v232 offset:256\n\t"
            "s_waitcnt lgkmcnt(10)\n\t"
            "v_mfma_f32_16x16x32_bf16 v[144:147], v[8:11], v[224:227], v[144:147]\n\t"
            "v_mfma_f32_16x16x32_bf16 v[204:207], v[32:35], v[224:227], v[204:207]\n\t"
            "v_mfma_f32_16x16x32_bf16 v[152:155], v[56:59], v[224:227], v[152:155]\n\t"
            "v_mfma_f32_16x16x32_bf16 v[148:151], v[80:83], v[224:227], v[148:151]\n\t"
            "v_mfma_f32_16x16x32_bf16 v[208:211], v[104:107], v[224:227], v[208:211]\n\t"
            "v_mfma_f32_16x16x32_bf16 v[212:215], v[128:131], v[224:227], v[212:215]\n\t"
            "ds_read_b128 v[224:227], v233 offset:256\n\t"
            "s_waitcnt lgkmcnt(4)\n\t"
            "v_mfma_f32_16x16x32_bf16 v[148:151], v[84:87], v[216:219], v[148:151]\n\t"
            "v_mfma_f32_16x16x32_bf16 v[208:211], v[108:111], v[216:219], v[208:211]\n\t"
            "v_mfma_f32_16x16x32_bf16 v[212:215], v[132:135], v[216:219], v[212:215]\n\t"
            "v_mfma_f32_16x16x32_bf16 v[144:147], v[12:15], v[216:219], v[144:147]\n\t"
            "v_mfma_f32_16x16x32_bf16 v[204:207], v[36:39], v[216:219], v[204:207]\n\t"
            "v_mfma_f32_16x16x32_bf16 v[152:155], v[60:63], v[216:219], v[152:155]\n\t"
            "ds_read_b128 v[216:219], v232 offset:6144\n\t"
            "s_waitcnt lgkmcnt(2)\n\t"
            "v_mfma_f32_16x16x32_bf16 v[148:151], v[88:91], v[220:223], v[148:151]\n\t"
            "v_mfma_f32_16x16x32_bf16 v[208:211], v[112:115], v[220:223], v[208:211]\n\t"
            "v_mfma_f32_16x16x32_bf16 v[212:215], v[136:139], v[220:223], v[212:215]\n\t"
            "v_mfma_f32_16x16x32_bf16 v[144:147], v[16:19], v[220:223], v[144:147]\n\t"
            "v_mfma_f32_16x16x32_bf16 v[204:207], v[40:43], v[220:223], v[204:207]\n\t"
            "v_mfma_f32_16x16x32_bf16 v[152:155], v[64:67], v[220:223], v[152:155]\n\t"
            "ds_read_b128 v[220:223], v233 offset:6144\n\t"
            "s_waitcnt lgkmcnt(2)\n\t"
            "v_mfma_f32_16x16x32_bf16 v[144:147], v[20:23], v[224:227], v[144:147]\n\t"
            "v_mfma_f32_16x16x32_bf16 v[204:207], v[44:47], v[224:227], v[204:207]\n\t"
            "v_mfma_f32_16x16x32_bf16 v[152:155], v[68:71], v[224:227], v[152:155]\n\t"
            "v_mfma_f32_16x16x32_bf16 v[148:151], v[92:95], v[224:227], v[148:151]\n\t"
            "v_mfma_f32_16x16x32_bf16 v[208:211], v[116:119], v[224:227], v[208:211]\n\t"
            "v_mfma_f32_16x16x32_bf16 v[212:215], v[140:143], v[224:227], v[212:215]\n\t"
            "ds_read_b128 v[224:227], v232 offset:6272\n\t"
            "s_waitcnt lgkmcnt(2)\n\t"
            "v_mfma_f32_16x16x32_bf16 v[160:163], v[72:75], v[216:219], 0\n\t"
            "v_mul_f32 v144, v236, v144\n\t"
            "v_mul_f32 v145, v236, v145\n\t"
            "v_mul_f32 v146, v236, v146\n\t"
            "s_waitcnt lgkmcnt(1)\n\t"
            "v_mfma_f32_16x16x32_bf16 v[160:163], v[76:79], v[220:223], v[160:163]\n\t"
            "v_mul_f32 v152, v236, v152\n\t"
            "v_mul_f32 v153, v236, v153\n\t"
            "v_mul_f32 v154, v236, v154\n\t"
            "v_mul_f32 v155, v236, v155\n\t"
            "v_cvt_pk_bf16_f32 v152, v152, v153\n\t"
            "v_cvt_pk_bf16_f32 v153, v154, v155\n\t"
            "v_mul_f32 v212, v236, v212\n\t"
            "v_mul_f32 v213, v236, v213\n\t"
            "v_mul_f32 v214, v236, v214\n\t"
            "v_mul_f32 v215, v236, v215\n\t"
            "v_cvt_pk_bf16_f32 v154, v212, v213\n\t"
            "v_cvt_pk_bf16_f32 v155, v214, v215\n\t"
            "v_mul_f32 v147, v236, v147\n\t"
            "v_cvt_pk_bf16_f32 v144, v144, v145\n\t"
            "v_mul_f32 v148, v236, v148\n\t"
            "v_mul_f32 v149, v236, v149\n\t"
            "v_cvt_pk_bf16_f32 v145, v146, v147\n\t"
            "v_mul_f32 v204, v236, v204\n\t"
            "v_mul_f32 v205, v236, v205\n\t"
            "v_mul_f32 v206, v236, v206\n\t"
            "v_mul_f32 v207, v236, v207\n\t"
            "v_mul_f32 v150, v236, v150\n\t"
            "s_waitcnt lgkmcnt(0)\n\t"
            "v_mfma_f32_16x16x32_bf16 v[160:163], v[80:83], v[224:227], v[160:163]\n\t"
            "ds_read_b128 v[212:215], v232 offset:12416\n\t"
            "v_mul_f32 v151, v236, v151\n\t"
            "v_mfma_f32_16x16x32_bf16 v[160:163], v[84:87], v[188:191], v[160:163]\n\t"
            "v_cvt_pk_bf16_f32 v148, v148, v149\n\t"
            "v_cvt_pk_bf16_f32 v146, v204, v205\n\t"
            "v_mfma_f32_16x16x32_bf16 v[160:163], v[88:91], v[192:195], v[160:163]\n\t"
            "v_cvt_pk_bf16_f32 v147, v206, v207\n\t"
            "v_cvt_pk_bf16_f32 v149, v150, v151\n\t"
            "v_mfma_f32_16x16x32_bf16 v[160:163], v[92:95], v[200:203], v[160:163]\n\t"
            "ds_read_b128 v[204:207], v232 offset:12288\n\t"
            "v_mul_f32 v208, v236, v208\n\t"
            "v_mul_f32 v209, v236, v209\n\t"
            "v_mul_f32 v210, v236, v210\n\t"
            "v_mfma_f32_16x16x16_bf16 v[180:183], v[152:153], v[154:155], 0\n\t"
            "v_mul_f32 v211, v236, v211\n\t"
            "v_cvt_pk_bf16_f32 v150, v208, v209\n\t"
            "v_cvt_pk_bf16_f32 v151, v210, v211\n\t"
            "v_mfma_f32_16x16x32_bf16 v[196:199], v[24:27], v[216:219], 0\n\t"
            "ds_read_b128 v[208:211], v233 offset:12288\n\t"
            "v_mul_f32 v160, v237, v160\n\t"
            "v_mfma_f32_16x16x32_bf16 v[196:199], v[28:31], v[220:223], v[196:199]\n\t"
            "v_mul_f32 v161, v237, v161\n\t"
            "v_mul_f32 v162, v237, v162\n\t"
            "v_mfma_f32_16x16x32_bf16 v[196:199], v[32:35], v[224:227], v[196:199]\n\t"
            "v_mul_f32 v163, v237, v163\n\t"
            "v_cvt_pk_bf16_f32 v160, v160, v161\n\t"
            "v_mfma_f32_16x16x32_bf16 v[196:199], v[36:39], v[188:191], v[196:199]\n\t"
            "v_cvt_pk_bf16_f32 v161, v162, v163\n\t"
            "v_mfma_f32_16x16x32_bf16 v[196:199], v[40:43], v[192:195], v[196:199]\n\t"
            "v_mfma_f32_16x16x32_bf16 v[196:199], v[44:47], v[200:203], v[196:199]\n\t"
            "v_mfma_f32_16x16x32_bf16 v[156:159], v[0:3], v[216:219], 0\n\t"
            "v_mfma_f32_16x16x32_bf16 v[156:159], v[4:7], v[220:223], v[156:159]\n\t"
            "v_mfma_f32_16x16x32_bf16 v[156:159], v[8:11], v[224:227], v[156:159]\n\t"
            "v_mfma_f32_16x16x32_bf16 v[156:159], v[12:15], v[188:191], v[156:159]\n\t"
            "v_mfma_f32_16x16x32_bf16 v[156:159], v[16:19], v[192:195], v[156:159]\n\t"
            "v_mfma_f32_16x16x32_bf16 v[156:159], v[20:23], v[200:203], v[156:159]\n\t"
            "s_nop 1\n\t"
            "v_mul_f32 v196, v237, v196\n\t"
            "v_mfma_f32_16x16x32_bf16 v[164:167], v[48:51], v[216:219], 0\n\t"
            "v_mul_f32 v197, v237, v197\n\t"
            "v_mul_f32 v198, v237, v198\n\t"
            "v_mfma_f32_16x16x32_bf16 v[164:167], v[52:55], v[220:223], v[164:167]\n\t"
            "v_mul_f32 v199, v237, v199\n\t"
            "v_mfma_f32_16x16x32_bf16 v[164:167], v[56:59], v[224:227], v[164:167]\n\t"
            "v_mul_f32 v156, v237, v156\n\t"
            "v_mul_f32 v157, v237, v157\n\t"
            "v_mfma_f32_16x16x32_bf16 v[164:167], v[60:63], v[188:191], v[164:167]\n\t"
            "v_mul_f32 v158, v237, v158\n\t"
            "v_mul_f32 v159, v237, v159\n\t"
            "v_mfma_f32_16x16x32_bf16 v[164:167], v[64:67], v[192:195], v[164:167]\n\t"
            "v_cvt_pk_bf16_f32 v156, v156, v157\n\t"
            "v_cvt_pk_bf16_f32 v157, v158, v159\n\t"
            "v_mfma_f32_16x16x32_bf16 v[164:167], v[68:71], v[200:203], v[164:167]\n\t"
            "v_cvt_pk_bf16_f32 v158, v196, v197\n\t"
            "v_cvt_pk_bf16_f32 v159, v198, v199\n\t"
            "v_mfma_f32_16x16x32_bf16 v[228:231], v[96:99], v[216:219], 0\n\t"
            "v_mfma_f32_16x16x32_bf16 v[228:231], v[100:103], v[220:223], v[228:231]\n\t"
            "v_mfma_f32_16x16x32_bf16 v[228:231], v[104:107], v[224:227], v[228:231]\n\t"
            "v_mfma_f32_16x16x32_bf16 v[228:231], v[108:111], v[188:191], v[228:231]\n\t"
            "s_nop 1\n\t"
            "v_mul_f32 v164, v237, v164\n\t"
            "v_mfma_f32_16x16x32_bf16 v[228:231], v[112:115], v[192:195], v[228:231]\n\t"
            "v_mul_f32 v165, v237, v165\n\t"
            "v_mul_f32 v166, v237, v166\n\t"
            "v_mfma_f32_16x16x32_bf16 v[228:231], v[116:119], v[200:203], v[228:231]\n\t"
            "v_mul_f32 v167, v237, v167\n\t"
            "v_cvt_pk_bf16_f32 v164, v164, v165\n\t"
            "v_mfma_f32_16x16x32_bf16 v[180:183], v[144:147], v[148:151], v[180:183]\n\t"
            "v_cvt_pk_bf16_f32 v165, v166, v167\n\t"
            "v_mfma_f32_16x16x32_bf16 v[196:199], v[120:123], v[216:219], 0\n\t"
            "ds_read_b128 v[216:219], v233 offset:12416\n\t"
            "v_mfma_f32_16x16x32_bf16 v[196:199], v[124:127], v[220:223], v[196:199]\n\t"
            "ds_read_b128 v[220:223], v232 offset:12544\n\t"
            "v_mul_f32 v228, v237, v228\n\t"
            "v_mfma_f32_16x16x32_bf16 v[196:199], v[128:131], v[224:227], v[196:199]\n\t"
            "ds_read_b128 v[224:227], v233 offset:12544\n\t"
            "v_mul_f32 v229, v237, v229\n\t"
            "v_mfma_f32_16x16x32_bf16 v[196:199], v[132:135], v[188:191], v[196:199]\n\t"
            "v_mul_f32 v230, v237, v230\n\t"
            "v_mul_f32 v231, v237, v231\n\t"
            "v_mfma_f32_16x16x32_bf16 v[196:199], v[136:139], v[192:195], v[196:199]\n\t"
            "v_cvt_pk_bf16_f32 v162, v228, v229\n\t"
            "v_cvt_pk_bf16_f32 v163, v230, v231\n\t"
            "v_mfma_f32_16x16x32_bf16 v[196:199], v[140:143], v[200:203], v[196:199]\n\t"
            "v_exp_f32 v180, v180\n\t"
            "s_waitcnt lgkmcnt(4)\n\t"
            "v_mfma_f32_16x16x32_bf16 v[176:179], v[48:51], v[204:207], 0\n\t"
            "v_exp_f32 v181, v181\n\t"
            "s_waitcnt lgkmcnt(3)\n\t"
            "v_mfma_f32_16x16x32_bf16 v[176:179], v[52:55], v[208:211], v[176:179]\n\t"
            "v_exp_f32 v182, v182\n\t"
            "v_mfma_f32_16x16x32_bf16 v[176:179], v[56:59], v[212:215], v[176:179]\n\t"
            "v_exp_f32 v183, v183\n\t"
            "s_waitcnt lgkmcnt(2)\n\t"
            "v_mfma_f32_16x16x32_bf16 v[176:179], v[60:63], v[216:219], v[176:179]\n\t"
            "v_mul_f32 v196, v237, v196\n\t"
            "v_mul_f32 v197, v237, v197\n\t"
            "s_waitcnt lgkmcnt(1)\n\t"
            "v_mfma_f32_16x16x32_bf16 v[176:179], v[64:67], v[220:223], v[176:179]\n\t"
            "v_mul_f32 v198, v237, v198\n\t"
            "v_mul_f32 v199, v237, v199\n\t"
            "s_waitcnt lgkmcnt(0)\n\t"
            "v_mfma_f32_16x16x32_bf16 v[176:179], v[68:71], v[224:227], v[176:179]\n\t"
            "v_cvt_pk_bf16_f32 v166, v196, v197\n\t"
            "v_cvt_pk_bf16_f32 v167, v198, v199\n\t"
            "v_cvt_pk_bf16_f32 v180, v180, v181\n\t"
            "v_cvt_pk_bf16_f32 v181, v182, v183\n\t"
            "v_mfma_f32_16x16x16_bf16 v[188:191], v[152:153], v[166:167], 0\n\t"
            "v_mfma_f32_16x16x16_bf16 v[192:195], v[164:165], v[166:167], 0\n\t"
            "s_nop 1\n\t"
            "v_mul_f32 v176, v238, v176\n\t"
            "v_mfma_f32_16x16x16_bf16 v[184:187], v[164:165], v[154:155], 0\n\t"
            "v_mul_f32 v177, v238, v177\n\t"
            "v_mul_f32 v178, v238, v178\n\t"
            "v_mul_f32 v179, v238, v179\n\t"
            "v_mfma_f32_16x16x32_bf16 v[168:171], v[0:3], v[204:207], 0\n\t"
            "v_cvt_pk_bf16_f32 v176, v176, v177\n\t"
            "v_cvt_pk_bf16_f32 v177, v178, v179\n\t"
            "v_mfma_f32_16x16x32_bf16 v[168:171], v[4:7], v[208:211], v[168:171]\n\t"
            "v_mfma_f32_16x16x32_bf16 v[168:171], v[8:11], v[212:215], v[168:171]\n\t"
            "v_mfma_f32_16x16x32_bf16 v[168:171], v[12:15], v[216:219], v[168:171]\n\t"
            "v_mfma_f32_16x16x32_bf16 v[168:171], v[16:19], v[220:223], v[168:171]\n\t"
            "v_mfma_f32_16x16x32_bf16 v[168:171], v[20:23], v[224:227], v[168:171]\n\t"
            "v_mfma_f32_16x16x32_bf16 v[188:191], v[144:147], v[160:163], v[188:191]\n\t"
            "v_mfma_f32_16x16x32_bf16 v[200:203], v[120:123], v[204:207], 0\n\t"
            "v_mfma_f32_16x16x32_bf16 v[200:203], v[124:127], v[208:211], v[200:203]\n\t"
            "v_mfma_f32_16x16x32_bf16 v[200:203], v[128:131], v[212:215], v[200:203]\n\t"
            "v_mfma_f32_16x16x32_bf16 v[200:203], v[132:135], v[216:219], v[200:203]\n\t"
            "v_mfma_f32_16x16x32_bf16 v[200:203], v[136:139], v[220:223], v[200:203]\n\t"
            "s_nop 1\n\t"
            "v_mul_f32 v168, v238, v168\n\t"
            "v_mfma_f32_16x16x32_bf16 v[200:203], v[140:143], v[224:227], v[200:203]\n\t"
            "v_exp_f32 v188, v188\n\t"
            "v_mfma_f32_16x16x32_bf16 v[192:195], v[156:159], v[160:163], v[192:195]\n\t"
            "v_exp_f32 v189, v189\n\t"
            "v_mfma_f32_16x16x32_bf16 v[172:175], v[72:75], v[204:207], 0\n\t"
            "v_exp_f32 v190, v190\n\t"
            "v_mfma_f32_16x16x32_bf16 v[172:175], v[76:79], v[208:211], v[172:175]\n\t"
            "v_exp_f32 v191, v191\n\t"
            "v_mfma_f32_16x16x32_bf16 v[172:175], v[80:83], v[212:215], v[172:175]\n\t"
            "v_cvt_pk_bf16_f32 v188, v188, v189\n\t"
            "v_cvt_pk_bf16_f32 v189, v190, v191\n\t"
            "v_mfma_f32_16x16x32_bf16 v[172:175], v[84:87], v[216:219], v[172:175]\n\t"
            "v_exp_f32 v192, v192\n\t"
            "v_mfma_f32_16x16x32_bf16 v[172:175], v[88:91], v[220:223], v[172:175]\n\t"
            "v_exp_f32 v193, v193\n\t"
            "v_mfma_f32_16x16x32_bf16 v[172:175], v[92:95], v[224:227], v[172:175]\n\t"
            "v_exp_f32 v194, v194\n\t"
            "v_mfma_f32_16x16x32_bf16 v[184:187], v[156:159], v[148:151], v[184:187]\n\t"
            "v_exp_f32 v195, v195\n\t"
            "v_mfma_f32_16x16x32_bf16 v[196:199], v[24:27], v[204:207], 0\n\t"
            "v_cvt_pk_bf16_f32 v190, v192, v193\n\t"
            "v_cvt_pk_bf16_f32 v191, v194, v195\n\t"
            "v_mfma_f32_16x16x32_bf16 v[196:199], v[28:31], v[208:211], v[196:199]\n\t"
            "v_mul_f32 v169, v238, v169\n\t"
            "v_mul_f32 v170, v238, v170\n\t"
            "v_mfma_f32_16x16x32_bf16 v[196:199], v[32:35], v[212:215], v[196:199]\n\t"
            "v_exp_f32 v184, v184\n\t"
            "v_mfma_f32_16x16x32_bf16 v[196:199], v[36:39], v[216:219], v[196:199]\n\t"
            "v_exp_f32 v185, v185\n\t"
            "v_mfma_f32_16x16x32_bf16 v[196:199], v[40:43], v[220:223], v[196:199]\n\t"
            "v_exp_f32 v186, v186\n\t"
            "v_mfma_f32_16x16x32_bf16 v[196:199], v[44:47], v[224:227], v[196:199]\n\t"
            "v_exp_f32 v187, v187\n\t"
            "v_cvt_pk_bf16_f32 v182, v184, v185\n\t"
            "v_cvt_pk_bf16_f32 v183, v186, v187\n\t"
            "v_mfma_f32_16x16x16_bf16 v[184:187], v[176:177], v[154:155], 0\n\t"
            "v_mul_f32 v171, v238, v171\n\t"
            "v_cvt_pk_bf16_f32 v168, v168, v169\n\t"
            "v_cvt_pk_bf16_f32 v169, v170, v171\n\t"
            "v_mfma_f32_16x16x16_bf16 v[192:195], v[176:177], v[166:167], 0\n\t"
            "v_mul_f32 v196, v238, v196\n\t"
            "v_mul_f32 v197, v238, v197\n\t"
            "v_mul_f32 v198, v238, v198\n\t"
            "v_mfma_f32_16x16x32_bf16 v[228:231], v[96:99], v[204:207], 0\n\t"
            "ds_read_b64 v[204:205], v235 offset:136\n\t"
            "v_mul_f32 v199, v238, v199\n\t"
            "v_mfma_f32_16x16x32_bf16 v[228:231], v[100:103], v[208:211], v[228:231]\n\t"
            "ds_read_b64 v[206:207], v234 offset:128\n\t"
            "v_cvt_pk_bf16_f32 v170, v196, v197\n\t"
            "v_mfma_f32_16x16x32_bf16 v[228:231], v[104:107], v[212:215], v[228:231]\n\t"
            "ds_read_b128 v[208:211], v234\n\t"
            "v_cvt_pk_bf16_f32 v171, v198, v199\n\t"
            "v_mfma_f32_16x16x32_bf16 v[228:231], v[108:111], v[216:219], v[228:231]\n\t"
            "ds_read_b128 v[212:215], v235 offset:64\n\t"
            "v_mul_f32 v172, v238, v172\n\t"
            "v_mfma_f32_16x16x32_bf16 v[228:231], v[112:115], v[220:223], v[228:231]\n\t"
            "v_mul_f32 v173, v238, v173\n\t"
            "v_mul_f32 v200, v238, v200\n\t"
            "v_mfma_f32_16x16x32_bf16 v[228:231], v[116:119], v[224:227], v[228:231]\n\t"
            "v_mul_f32 v201, v238, v201\n\t"
            "v_mul_f32 v202, v238, v202\n\t"
            "v_mfma_f32_16x16x32_bf16 v[184:187], v[168:171], v[148:151], v[184:187]\n\t"
            "v_mul_f32 v203, v238, v203\n\t"
            "v_mul_f32 v174, v238, v174\n\t"
            "v_mfma_f32_16x16x32_bf16 v[192:195], v[168:171], v[160:163], v[192:195]\n\t"
            "v_mul_f32 v175, v238, v175\n\t"
            "v_cvt_pk_bf16_f32 v172, v172, v173\n\t"
            "v_cvt_pk_bf16_f32 v178, v200, v201\n\t"
            "v_cvt_pk_bf16_f32 v179, v202, v203\n\t"
            "v_cvt_pk_bf16_f32 v173, v174, v175\n\t"
            "v_mul_f32 v228, v238, v228\n\t"
            "v_mul_f32 v229, v238, v229\n\t"
            "s_waitcnt lgkmcnt(2)\n\t"
            "v_mfma_f32_16x16x32_bf16 v[216:219], v[204:207], v[152:155], 0\n\t"
            "v_mul_f32 v230, v238, v230\n\t"
            "v_mul_f32 v231, v238, v231\n\t"
            "s_waitcnt lgkmcnt(1)\n\t"
            "v_mfma_f32_16x16x32_bf16 v[216:219], v[208:211], v[148:151], v[216:219]\n\t"
            "v_cvt_pk_bf16_f32 v174, v228, v229\n\t"
            "v_cvt_pk_bf16_f32 v175, v230, v231\n\t"
            "s_waitcnt lgkmcnt(0)\n\t"
            "v_mfma_f32_16x16x32_bf16 v[216:219], v[212:215], v[144:147], v[216:219]\n\t"
            "v_exp_f32 v184, v184\n\t"
            "v_mfma_f32_16x16x32_bf16 v[220:223], v[204:207], v[164:167], 0\n\t"
            "v_exp_f32 v185, v185\n\t"
            "v_mfma_f32_16x16x32_bf16 v[220:223], v[208:211], v[160:163], v[220:223]\n\t"
            "v_exp_f32 v186, v186\n\t"
            "v_mfma_f32_16x16x32_bf16 v[220:223], v[212:215], v[156:159], v[220:223]\n\t"
            "v_exp_f32 v187, v187\n\t"
            "v_cvt_pk_bf16_f32 v184, v184, v185\n\t"
            "v_exp_f32 v216, v216\n\t"
            "v_mfma_f32_16x16x16_bf16 v[228:231], v[164:165], v[178:179], 0\n\t"
            "v_cvt_pk_bf16_f32 v185, v186, v187\n\t"
            "v_cvt_pk_bf16_f32 v216, v216, 0\n\t"
            "v_cndmask_b32_e64 v186, 0, v216, s[58:59]\n\t"
            "v_mfma_f32_16x16x16_bf16 v[200:203], v[176:177], v[178:179], 0\n\t"
            "v_mov_b32 v187, 0\n\t"
            "v_cndmask_b32_e64 v235, v217, v221, s[60:61]\n\t"
            "v_exp_f32 v192, v192\n\t"
            "v_mfma_f32_16x16x16_bf16 v[196:199], v[152:153], v[178:179], 0\n\t"
            "v_exp_f32 v193, v193\n\t"
            "v_exp_f32 v194, v194\n\t"
            "v_mfma_f32_16x16x32_bf16 v[224:227], v[204:207], v[176:179], 0\n\t"
            "v_exp_f32 v195, v195\n\t"
            "v_mfma_f32_16x16x32_bf16 v[224:227], v[208:211], v[172:175], v[224:227]\n\t"
            "v_exp_f32 v220, v220\n\t"
            "v_mfma_f32_16x16x32_bf16 v[224:227], v[212:215], v[168:171], v[224:227]\n\t"
            "v_cvt_pk_bf16_f32 v192, v192, v193\n\t"
            "v_cvt_pk_bf16_f32 v193, v194, v195\n\t"
            "v_mfma_f32_16x16x32_bf16 v[204:207], v[240:243], v[180:183], 0\n\t"
            "v_cvt_pk_bf16_f32 v220, v220, 0\n\t"
            "v_cndmask_b32_e64 v194, 0, v220, s[58:59]\n\t"
            "v_mfma_f32_16x16x32_bf16 v[204:207], v[240:243], v[184:187], v[204:207]\n\t"
            "v_mov_b32 v195, 0\n\t"
            "v_mfma_f32_16x16x32_bf16 v[200:203], v[168:171], v[172:175], v[200:203]\n\t"
            "v_exp_f32 v224, v224\n\t"
            "v_mfma_f32_16x16x32_bf16 v[196:199], v[144:147], v[172:175], v[196:199]\n\t"
            "v_cvt_pk_bf16_f32 v224, v224, 0\n\t"
            "v_cndmask_b32_e64 v235, v235, v225, s[62:63]\n\t"
            "v_mfma_f32_16x16x32_bf16 v[228:231], v[156:159], v[172:175], v[228:231]\n\t"
            "v_cndmask_b32_e64 v235, v235, v239, s[64:65]\n\t"
            "v_cmp_nlt_f32_e64 s[24:25], |v235|, s23\n\t"
            "v_exp_f32 v235, v235\n\t"
            "v_exp_f32 v200, v200\n\t"
            "v_exp_f32 v201, v201\n\t"
            "v_exp_f32 v196, v196\n\t"
            "v_exp_f32 v197, v197\n\t"
            "v_exp_f32 v198, v198\n\t"
            "v_exp_f32 v199, v199\n\t"
            "v_exp_f32 v228, v228\n\t"
            "v_exp_f32 v229, v229\n\t"
            "v_exp_f32 v230, v230\n\t"
            "v_exp_f32 v231, v231\n\t"
            "v_cvt_pk_bf16_f32 v196, v196, v197\n\t"
            "v_cvt_pk_bf16_f32 v197, v198, v199\n\t"
            "v_cvt_pk_bf16_f32 v198, v228, v229\n\t"
            "v_cvt_pk_bf16_f32 v199, v230, v231\n\t"
            "v_exp_f32 v202, v202\n\t"
            "v_exp_f32 v203, v203\n\t"
            "v_mfma_f32_16x16x32_bf16 v[212:215], v[240:243], v[196:199], 0\n\t"
            "v_cvt_pk_bf16_f32 v200, v200, v201\n\t"
            "v_cvt_pk_bf16_f32 v201, v202, v203\n\t"
            "v_cndmask_b32_e64 v202, 0, v224, s[58:59]\n\t"
            "v_mov_b32 v203, 0\n\t"
            "v_cvt_pk_bf16_f32 v235, v235, 0\n\t"
            "v_cndmask_b32_e64 v235, 0, v235, s[66:67]\n\t"
            "v_mfma_f32_16x16x32_bf16 v[212:215], v[240:243], v[200:203], v[212:215]\n\t"
            "ds_write_b16 v250, v235 offset:256\n\t"
            "v_mfma_f32_16x16x32_bf16 v[208:211], v[240:243], v[188:191], 0\n\t"
            "v_add_u32 v205, 0xe0800000, v204\n\t"
            "v_rcp_f32 v236, v204\n\t"
            "v_mfma_f32_16x16x32_bf16 v[208:211], v[240:243], v[192:195], v[208:211]\n\t"
            "s_nop 2\n\t"
            "v_add_u32 v213, 0xe0800000, v212\n\t"
            "v_rcp_f32 v238, v212\n\t"
            "s_nop 2\n\t"
            "v_add_u32 v209, 0xe0800000, v208\n\t"
            "v_rcp_f32 v237, v208\n\t"
            "v_or3_b32 v205, v205, v209, v213\n\t"
            "v_cmp_le_u32 vcc, 2.0, v205\n\t"
            "s_waitcnt lgkmcnt(0)\n\t"
            "s_nop 7\n\t"
            "s_branch .Lwa_in%=\n\t"
            ".p2align 6\n\t"
            ".Lwa_loop%=:\n\t"
            "ds_read_b64 v[156:157], v247\n\t"
            "v_add_u32 v232, s48, v244\n\t"
            "v_add_u32 v208, s48, v248\n\t"
            "v_add_u32 v235, s45, v248\n\t"
            "v_xor_b32 v234, s73, v254\n\t"
            "v_xor_b32 v233, 64, v232\n\t"
            "v_mov_b32 v159, 0\n\t"
            "v_mov_b32 v167, 0\n\t"
            "v_mov_b32 v163, 0\n\t"
            "v_mov_b32 v239, s76\n\t"
            "ds_read_b128 v[216:219], v232\n\t"
            "ds_read_b128 v[204:207], v208 offset:18816\n\t"
            "ds_read_b128 v[208:211], v208 offset:19840\n\t"
            "ds_read_b128 v[168:171], v235\n\t"
            "ds_read_u16 v158, v234\n\t"
            "ds_read_b128 v[220:223], v233\n\t"
            "ds_read_b128 v[172:175], v235 offset:1536\n\t"
            "ds_read_b64 v[164:165], v247 offset:1536\n\t"
            "ds_read_u16 v166, v234 offset:128\n\t"
            "ds_read_b128 v[176:179], v235 offset:3072\n\t"
            "ds_read_b64 v[160:161], v247 offset:3072\n\t"
            "ds_read_u16 v162, v234 offset:256\n\t"
            "v_add_u32 v234, s72, v252\n\t"
            "v_cndmask_b32_e64 v235, v246, v234, s[50:51]\n\t"
            "v_cndmask_b32_e64 v234, v246, v234, s[26:27]\n\t"
            "ds_read_b128 v[224:227], v232 offset:128\n\t"
            "s_waitcnt lgkmcnt(14)\n\t"
            "ds_read_b32 v239, v239\n\t"
            "s_waitcnt lgkmcnt(13)\n\t"
            "v_mfma_f32_16x16x32_bf16 v[148:151], v[72:75], v[216:219], 0\n\t"
            "s_waitcnt lgkmcnt(12)\n\t"
            "v_add_f32 v204, v204, v206\n\t"
            "v_add_f32 v205, v205, v207\n\t"
            "s_waitcnt lgkmcnt(11)\n\t"
            "v_add_f32 v208, v208, v210\n\t"
            "v_add_f32 v209, v209, v211\n\t"
            "v_add_f32 v204, v204, v208\n\t"
            "v_add_f32 v205, v205, v209\n\t"
            "v_mfma_f32_16x16x32_bf16 v[208:211], v[96:99], v[216:219], 0\n\t"
            "v_mul_f32 v204, 0x3baaaaab, v204\n\t"
            "v_mul_f32_e64 v204, v204, -v204\n\t"
            "v_mfma_f32_16x16x32_bf16 v[212:215], v[120:123], v[216:219], 0\n\t"
            "v_fmac_f32 v204, 0x3baaaaab, v205\n\t"
            "v_max_f32 v204, 0, v204\n\t"
            "s_waitcnt lgkmcnt(10)\n\t"
            "v_mfma_f32_16x16x32_bf16 v[228:231], v[168:171], v[180:183], 0\n\t"
            "v_add_f32 v204, 0x3727c5ac, v204\n\t"
            "s_waitcnt lgkmcnt(9)\n\t"
            "v_mfma_f32_16x16x32_bf16 v[228:231], v[156:159], v[184:187], v[228:231]\n\t"
            "v_rsq_f32 v204, v204\n\t"
            "v_mfma_f32_16x16x32_bf16 v[144:147], v[0:3], v[216:219], 0\n\t"
            "ds_write_b32 v249, v204\n\t"
            "v_mfma_f32_16x16x32_bf16 v[204:207], v[24:27], v[216:219], 0\n\t"
            "v_mfma_f32_16x16x32_bf16 v[152:155], v[48:51], v[216:219], 0\n\t"
            "ds_read_b128 v[216:219], v233 offset:128\n\t"
            "s_waitcnt lgkmcnt(10)\n\t"
            "v_mfma_f32_16x16x32_bf16 v[148:151], v[76:79], v[220:223], v[148:151]\n\t"
            "v_mul_f32 v228, v236, v228\n\t"
            "v_mul_f32 v229, v236, v229\n\t"
            "v_mfma_f32_16x16x32_bf16 v[208:211], v[100:103], v[220:223], v[208:211]\n\t"
            "v_mul_f32 v230, v236, v230\n\t"
            "v_mul_f32 v231, v236, v231\n\t"
            "v_mfma_f32_16x16x32_bf16 v[212:215], v[124:127], v[220:223], v[212:215]\n\t"
            "v_cvt_pk_bf16_f32 v228, v228, v229\n\t"
            "v_cvt_pk_bf16_f32 v229, v230, v231\n\t"
            "v_mfma_f32_16x16x32_bf16 v[144:147], v[4:7], v[220:223], v[144:147]\n\t"
            "ds_write_b64 v245, v[228:229]\n\t"
            "v_mfma_f32_16x16x32_bf16 v[204:207], v[28:31], v[220:223], v[204:207]\n\t"
            "v_mfma_f32_16x16x32_bf16 v[152:155], v[52:55], v[220:223], v[152:155]\n\t"
            "ds_read_b128 v[220:223], v232 offset:256\n\t"
            "s_waitcnt lgkmcnt(11)\n\t"
            "v_mfma_f32_16x16x32_bf16 v[228:231], v[172:175], v[180:183], 0\n\t"
            "s_waitcnt lgkmcnt(9)\n\t"
            "v_mfma_f32_16x16x32_bf16 v[228:231], v[164:167], v[184:187], v[228:231]\n\t"
            "s_waitcnt lgkmcnt(8)\n\t"
            "v_mfma_f32_16x16x32_bf16 v[180:183], v[176:179], v[180:183], 0\n\t"
            "s_waitcnt lgkmcnt(6)\n\t"
            "v_mfma_f32_16x16x32_bf16 v[180:183], v[160:163], v[184:187], v[180:183]\n\t"
            "s_waitcnt lgkmcnt(5)\n\t"
            "v_mfma_f32_16x16x32_bf16 v[144:147], v[8:11], v[224:227], v[144:147]\n\t"
            "v_mfma_f32_16x16x32_bf16 v[204:207], v[32:35], v[224:227], v[204:207]\n\t"
            "v_mfma_f32_16x16x32_bf16 v[152:155], v[56:59], v[224:227], v[152:155]\n\t"
            "v_mfma_f32_16x16x32_bf16 v[148:151], v[80:83], v[224:227], v[148:151]\n\t"
            "v_mul_f32 v228, v236, v228\n\t"
            "v_mfma_f32_16x16x32_bf16 v[208:211], v[104:107], v[224:227], v[208:211]\n\t"
            "v_mul_f32 v229, v236, v229\n\t"
            "v_mul_f32 v230, v236, v230\n\t"
            "v_mfma_f32_16x16x32_bf16 v[212:215], v[128:131], v[224:227], v[212:215]\n\t"
            "ds_read_b128 v[224:227], v233 offset:256\n\t"
            "v_mul_f32 v231, v236, v231\n\t"
            "v_mfma_f32_16x16x32_bf16 v[184:187], v[168:171], v[188:191], 0\n\t"
            "v_cvt_pk_bf16_f32 v228, v228, v229\n\t"
            "v_cvt_pk_bf16_f32 v229, v230, v231\n\t"
            "v_mfma_f32_16x16x32_bf16 v[184:187], v[156:159], v[192:195], v[184:187]\n\t"
            "ds_write_b64 v245, v[228:229] offset:128\n\t"
            "v_mfma_f32_16x16x32_bf16 v[228:231], v[172:175], v[188:191], 0\n\t"
            "v_mul_f32 v180, v236, v180\n\t"
            "v_mul_f32 v181, v236, v181\n\t"
            "v_mfma_f32_16x16x32_bf16 v[228:231], v[164:167], v[192:195], v[228:231]\n\t"
            "v_mul_f32 v182, v236, v182\n\t"
            "v_mul_f32 v183, v236, v183\n\t"
            "s_waitcnt lgkmcnt(4)\n\t"
            "v_mfma_f32_16x16x32_bf16 v[148:151], v[84:87], v[216:219], v[148:151]\n\t"
            "ds_read_b32 v236, v251\n\t"
            "v_cvt_pk_bf16_f32 v180, v180, v181\n\t"
            "v_mfma_f32_16x16x32_bf16 v[208:211], v[108:111], v[216:219], v[208:211]\n\t"
            "v_cvt_pk_bf16_f32 v181, v182, v183\n\t"
            "v_mul_f32 v184, v237, v184\n\t"
            "v_mfma_f32_16x16x32_bf16 v[212:215], v[132:135], v[216:219], v[212:215]\n\t"
            "ds_write_b64 v245, v[180:181] offset:256\n\t"
            "v_mfma_f32_16x16x32_bf16 v[144:147], v[12:15], v[216:219], v[144:147]\n\t"
            "v_mul_f32 v185, v237, v185\n\t"
            "v_mul_f32 v186, v237, v186\n\t"
            "v_mfma_f32_16x16x32_bf16 v[204:207], v[36:39], v[216:219], v[204:207]\n\t"
            "v_mul_f32 v187, v237, v187\n\t"
            "v_cvt_pk_bf16_f32 v184, v184, v185\n\t"
            "v_mfma_f32_16x16x32_bf16 v[152:155], v[60:63], v[216:219], v[152:155]\n\t"
            "ds_read_b128 v[216:219], v232 offset:6144\n\t"
            "v_cvt_pk_bf16_f32 v185, v186, v187\n\t"
            "v_mfma_f32_16x16x32_bf16 v[180:183], v[168:171], v[196:199], 0\n\t"
            "ds_read_b64 v[186:187], v253 offset:352\n\t"
            "v_mul_f32 v228, v237, v228\n\t"
            "v_mfma_f32_16x16x32_bf16 v[180:183], v[156:159], v[200:203], v[180:183]\n\t"
            "ds_write_b64 v245, v[184:185] offset:6144\n\t"
            "s_waitcnt lgkmcnt(7)\n\t"
            "v_mfma_f32_16x16x32_bf16 v[148:151], v[88:91], v[220:223], v[148:151]\n\t"
            "ds_read_b64 v[184:185], v253 offset:320\n\t"
            "v_mul_f32 v229, v237, v229\n\t"
            "v_mfma_f32_16x16x32_bf16 v[208:211], v[112:115], v[220:223], v[208:211]\n\t"
            "v_mul_f32 v230, v237, v230\n\t"
            "v_mul_f32 v231, v237, v231\n\t"
            "v_mfma_f32_16x16x32_bf16 v[212:215], v[136:139], v[220:223], v[212:215]\n\t"
            "v_mul_f32 v180, v238, v180\n\t"
            "v_mul_f32 v181, v238, v181\n\t"
            "v_mfma_f32_16x16x32_bf16 v[188:191], v[176:179], v[188:191], 0\n\t"
            "v_mul_f32 v182, v238, v182\n\t"
            "v_mul_f32 v183, v238, v183\n\t"
            "v_mfma_f32_16x16x32_bf16 v[188:191], v[160:163], v[192:195], v[188:191]\n\t"
            "ds_read_b128 v[192:195], v232 offset:6400\n\t"
            "v_cvt_pk_bf16_f32 v180, v180, v181\n\t"
            "v_mfma_f32_16x16x32_bf16 v[144:147], v[16:19], v[220:223], v[144:147]\n\t"
            "v_cvt_pk_bf16_f32 v181, v182, v183\n\t"
            "v_cvt_pk_bf16_f32 v228, v228, v229\n\t"
            "v_mfma_f32_16x16x32_bf16 v[204:207], v[40:43], v[220:223], v[204:207]\n\t"
            "ds_read_b64 v[182:183], v253 offset:288\n\t"
            "v_cvt_pk_bf16_f32 v229, v230, v231\n\t"
            "v_mfma_f32_16x16x32_bf16 v[152:155], v[64:67], v[220:223], v[152:155]\n\t"
            "ds_read_b128 v[220:223], v233 offset:6144\n\t"
            "v_mul_f32 v188, v237, v188\n\t"
            "s_waitcnt lgkmcnt(10)\n\t"
            "v_mfma_f32_16x16x32_bf16 v[144:147], v[20:23], v[224:227], v[144:147]\n\t"
            "ds_write_b64 v245, v[180:181] offset:12288\n\t"
            "v_mfma_f32_16x16x32_bf16 v[204:207], v[44:47], v[224:227], v[204:207]\n\t"
            "ds_read_b64 v[180:181], v253 offset:256\n\t"
            "v_mul_f32 v189, v237, v189\n\t"
            "v_mfma_f32_16x16x32_bf16 v[152:155], v[68:71], v[224:227], v[152:155]\n\t"
            "ds_write_b64 v245, v[228:229] offset:6272\n\t"
            "v_mfma_f32_16x16x32_bf16 v[148:151], v[92:95], v[224:227], v[148:151]\n\t"
            "v_mul_f32 v190, v237, v190\n\t"
            "v_mul_f32 v191, v237, v191\n\t"
            "v_mfma_f32_16x16x32_bf16 v[208:211], v[116:119], v[224:227], v[208:211]\n\t"
            "ds_read_b32 v237, v251 offset:64\n\t"
            "v_cvt_pk_bf16_f32 v188, v188, v189\n\t"
            "v_mfma_f32_16x16x32_bf16 v[212:215], v[140:143], v[224:227], v[212:215]\n\t"
            "ds_read_b128 v[224:227], v232 offset:6272\n\t"
            "v_cvt_pk_bf16_f32 v189, v190, v191\n\t"
            "v_mfma_f32_16x16x32_bf16 v[228:231], v[172:175], v[196:199], 0\n\t"
            "s_waitcnt lgkmcnt(13)\n\t"
            "v_mul_f32 v152, v236, v152\n\t"
            "v_mul_f32 v153, v236, v153\n\t"
            "v_mfma_f32_16x16x32_bf16 v[228:231], v[164:167], v[200:203], v[228:231]\n\t"
            "ds_write_b64 v245, v[188:189] offset:6400\n\t"
            "v_mfma_f32_16x16x32_bf16 v[196:199], v[176:179], v[196:199], 0\n\t"
            "ds_read_b128 v[188:191], v233 offset:6272\n\t"
            "v_mul_f32 v154, v236, v154\n\t"
            "v_mfma_f32_16x16x32_bf16 v[196:199], v[160:163], v[200:203], v[196:199]\n\t"
            "s_waitcnt lgkmcnt(14)\n\t"
            "ds_read_b128 v[200:203], v233 offset:6400\n\t"
            "v_mul_f32 v155, v236, v155\n\t"
            "s_waitcnt lgkmcnt(6)\n\t"
            "v_mfma_f32_16x16x32_bf16 v[168:171], v[168:171], v[180:183], 0\n\t"
            "v_mul_f32 v228, v238, v228\n\t"
            "v_mul_f32 v229, v238, v229\n\t"
            "v_mfma_f32_16x16x32_bf16 v[168:171], v[156:159], v[184:187], v[168:171]\n\t"
            "v_mul_f32 v230, v238, v230\n\t"
            "v_mul_f32 v231, v238, v231\n\t"
            "v_mfma_f32_16x16x32_bf16 v[176:179], v[176:179], v[180:183], 0\n\t"
            "v_cvt_pk_bf16_f32 v228, v228, v229\n\t"
            "v_cvt_pk_bf16_f32 v229, v230, v231\n\t"
            "v_mfma_f32_16x16x32_bf16 v[176:179], v[160:163], v[184:187], v[176:179]\n\t"
            "ds_write_b64 v245, v[228:229] offset:12416\n\t"
            "v_mfma_f32_16x16x32_bf16 v[228:231], v[240:243], v[180:183], 0\n\t"
            "v_cvt_pk_bf16_f32 v152, v152, v153\n\t"
            "v_cvt_pk_bf16_f32 v153, v154, v155\n\t"
            "v_mfma_f32_16x16x32_bf16 v[228:231], v[240:243], v[184:187], v[228:231]\n\t"
            "v_mul_f32 v212, v236, v212\n\t"
            "v_mul_f32 v213, v236, v213\n\t"
            "v_mfma_f32_16x16x32_bf16 v[160:163], v[72:75], v[216:219], 0\n\t"
            "v_mul_f32 v214, v236, v214\n\t"
            "v_mul_f32 v215, v236, v215\n\t"
            "v_mfma_f32_16x16x32_bf16 v[160:163], v[76:79], v[220:223], v[160:163]\n\t"
            "v_cvt_pk_bf16_f32 v154, v212, v213\n\t"
            "v_cvt_pk_bf16_f32 v155, v214, v215\n\t"
            "s_waitcnt lgkmcnt(4)\n\t"
            "v_mfma_f32_16x16x32_bf16 v[160:163], v[80:83], v[224:227], v[160:163]\n\t"
            "ds_read_b128 v[212:215], v232 offset:12416\n\t"
            "v_mul_f32 v196, v238, v196\n\t"
            "s_waitcnt lgkmcnt(3)\n\t"
            "v_mfma_f32_16x16x32_bf16 v[160:163], v[84:87], v[188:191], v[160:163]\n\t"
            "v_mul_f32 v197, v238, v197\n\t"
            "v_mul_f32 v198, v238, v198\n\t"
            "v_mfma_f32_16x16x32_bf16 v[160:163], v[88:91], v[192:195], v[160:163]\n\t"
            "v_mul_f32 v199, v238, v199\n\t"
            "v_cvt_pk_bf16_f32 v196, v196, v197\n\t"
            "s_waitcnt lgkmcnt(2)\n\t"
            "v_mfma_f32_16x16x32_bf16 v[160:163], v[92:95], v[200:203], v[160:163]\n\t"
            "ds_read_b32 v238, v251 offset:128\n\t"
            "v_cvt_pk_bf16_f32 v197, v198, v199\n\t"
            "v_mfma_f32_16x16x32_bf16 v[172:175], v[172:175], v[180:183], 0\n\t"
            "v_rcp_f32 v229, v228\n\t"
            "v_mfma_f32_16x16x32_bf16 v[172:175], v[164:167], v[184:187], v[172:175]\n\t"
            "ds_write_b64 v245, v[196:197] offset:12544\n\t"
            "v_mul_f32 v168, v229, v168\n\t"
            "v_mfma_f32_16x16x16_bf16 v[180:183], v[152:153], v[154:155], 0\n\t"
            "v_mul_f32 v169, v229, v169\n\t"
            "v_mul_f32 v176, v229, v176\n\t"
            "v_mul_f32 v177, v229, v177\n\t"
            "v_mfma_f32_16x16x32_bf16 v[196:199], v[24:27], v[216:219], 0\n\t"
            "s_nop 0\n\t"
            "v_mul_f32 v172, v229, v172\n\t"
            "v_mul_f32 v173, v229, v173\n\t"
            "v_mfma_f32_16x16x32_bf16 v[196:199], v[28:31], v[220:223], v[196:199]\n\t"
            "v_mul_f32 v170, v229, v170\n\t"
            "v_mul_f32 v171, v229, v171\n\t"
            "v_mfma_f32_16x16x32_bf16 v[196:199], v[32:35], v[224:227], v[196:199]\n\t"
            "v_cvt_pk_bf16_f32 v168, v168, v169\n\t"
            "v_mul_f32 v174, v229, v174\n\t"
            "v_mfma_f32_16x16x32_bf16 v[196:199], v[36:39], v[188:191], v[196:199]\n\t"
            "v_mul_f32 v175, v229, v175\n\t"
            "v_cvt_pk_bf16_f32 v172, v172, v173\n\t"
            "v_mfma_f32_16x16x32_bf16 v[196:199], v[40:43], v[192:195], v[196:199]\n\t"
            "v_mul_f32 v178, v229, v178\n\t"
            "v_mul_f32 v179, v229, v179\n\t"
            "v_mfma_f32_16x16x32_bf16 v[196:199], v[44:47], v[200:203], v[196:199]\n\t"
            "v_cvt_pk_bf16_f32 v176, v176, v177\n\t"
            "v_xor_b32 v230, s73, v255\n\t"
            "v_mfma_f32_16x16x32_bf16 v[156:159], v[0:3], v[216:219], 0\n\t"
            "v_cvt_pk_bf16_f32 v169, v170, v171\n\t"
            "v_cvt_pk_bf16_f32 v173, v174, v175\n\t"
            "v_mfma_f32_16x16x32_bf16 v[156:159], v[4:7], v[220:223], v[156:159]\n\t"
            "v_cvt_pk_bf16_f32 v177, v178, v179\n\t"
            "v_mul_f32 v144, v236, v144\n\t"
            "v_mfma_f32_16x16x32_bf16 v[156:159], v[8:11], v[224:227], v[156:159]\n\t"
            "v_mul_f32 v145, v236, v145\n\t"
            "v_mul_f32 v146, v236, v146\n\t"
            "v_mfma_f32_16x16x32_bf16 v[156:159], v[12:15], v[188:191], v[156:159]\n\t"
            "v_mul_f32 v147, v236, v147\n\t"
            "v_cvt_pk_bf16_f32 v144, v144, v145\n\t"
            "v_mfma_f32_16x16x32_bf16 v[156:159], v[16:19], v[192:195], v[156:159]\n\t"
            "v_mul_f32 v148, v236, v148\n\t"
            "v_mul_f32 v149, v236, v149\n\t"
            "v_mfma_f32_16x16x32_bf16 v[156:159], v[20:23], v[200:203], v[156:159]\n\t"
            "s_mov_b64 exec, s[68:69]\n\t"
            "ds_write_b64 v230, v[168:169]\n\t"
            "ds_write_b64 v230, v[172:173] offset:128\n\t"
            "ds_write_b64 v230, v[176:177] offset:256\n\t"
            "s_mov_b64 exec, -1\n\t"
            "v_mfma_f32_16x16x32_bf16 v[164:167], v[48:51], v[216:219], 0\n\t"
            "v_cvt_pk_bf16_f32 v145, v146, v147\n\t"
            "v_mul_f32 v204, v236, v204\n\t"
            "v_mfma_f32_16x16x32_bf16 v[164:167], v[52:55], v[220:223], v[164:167]\n\t"
            "v_mul_f32 v205, v236, v205\n\t"
            "v_mul_f32 v206, v236, v206\n\t"
            "v_mfma_f32_16x16x32_bf16 v[164:167], v[56:59], v[224:227], v[164:167]\n\t"
            "v_mul_f32 v207, v236, v207\n\t"
            "v_mul_f32 v150, v236, v150\n\t"
            "v_mfma_f32_16x16x32_bf16 v[164:167], v[60:63], v[188:191], v[164:167]\n\t"
            "v_mul_f32 v151, v236, v151\n\t"
            "v_cvt_pk_bf16_f32 v148, v148, v149\n\t"
            "v_mfma_f32_16x16x32_bf16 v[164:167], v[64:67], v[192:195], v[164:167]\n\t"
            "v_cvt_pk_bf16_f32 v146, v204, v205\n\t"
            "v_cvt_pk_bf16_f32 v147, v206, v207\n\t"
            "v_mfma_f32_16x16x32_bf16 v[164:167], v[68:71], v[200:203], v[164:167]\n\t"
            "s_waitcnt lgkmcnt(0)\n\t"
            "s_barrier\n\t"
            "s_setprio 2\n\t"
            "ds_read_b128 v[204:207], v232 offset:12288\n\t"
            "v_cvt_pk_bf16_f32 v149, v150, v151\n\t"
            "v_mfma_f32_16x16x32_bf16 v[228:231], v[96:99], v[216:219], 0\n\t"
            "v_mul_f32 v208, v236, v208\n\t"
            "v_mul_f32 v209, v236, v209\n\t"
            "v_mfma_f32_16x16x32_bf16 v[228:231], v[100:103], v[220:223], v[228:231]\n\t"
            "v_mul_f32 v210, v236, v210\n\t"
            "v_mul_f32 v211, v236, v211\n\t"
            "v_mfma_f32_16x16x32_bf16 v[228:231], v[104:107], v[224:227], v[228:231]\n\t"
            "v_cvt_pk_bf16_f32 v150, v208, v209\n\t"
            "v_cvt_pk_bf16_f32 v151, v210, v211\n\t"
            "v_mfma_f32_16x16x32_bf16 v[228:231], v[108:111], v[188:191], v[228:231]\n\t"
            "ds_read_b128 v[208:211], v233 offset:12288\n\t"
            "v_mul_f32 v156, v237, v156\n\t"
            "v_mfma_f32_16x16x32_bf16 v[228:231], v[112:115], v[192:195], v[228:231]\n\t"
            "v_mul_f32 v157, v237, v157\n\t"
            "v_mul_f32 v158, v237, v158\n\t"
            "v_mfma_f32_16x16x32_bf16 v[228:231], v[116:119], v[200:203], v[228:231]\n\t"
            "v_mul_f32 v159, v237, v159\n\t"
            "v_cvt_pk_bf16_f32 v156, v156, v157\n\t"
            "v_mfma_f32_16x16x32_bf16 v[180:183], v[144:147], v[148:151], v[180:183]\n\t"
            "v_cvt_pk_bf16_f32 v157, v158, v159\n\t"
            "v_mul_f32 v196, v237, v196\n\t"
            "v_mul_f32 v197, v237, v197\n\t"
            "v_mul_f32 v198, v237, v198\n\t"
            "v_mul_f32 v199, v237, v199\n\t"
            "v_cvt_pk_bf16_f32 v158, v196, v197\n\t"
            "v_cvt_pk_bf16_f32 v159, v198, v199\n\t"
            "v_mfma_f32_16x16x32_bf16 v[196:199], v[120:123], v[216:219], 0\n\t"
            "ds_read_b128 v[216:219], v233 offset:12416\n\t"
            "v_mul_f32 v164, v237, v164\n\t"
            "v_mfma_f32_16x16x32_bf16 v[196:199], v[124:127], v[220:223], v[196:199]\n\t"
            "ds_read_b128 v[220:223], v232 offset:12544\n\t"
            "v_mul_f32 v165, v237, v165\n\t"
            "v_mfma_f32_16x16x32_bf16 v[196:199], v[128:131], v[224:227], v[196:199]\n\t"
            "ds_read_b128 v[224:227], v233 offset:12544\n\t"
            "v_mul_f32 v166, v237, v166\n\t"
            "v_mfma_f32_16x16x32_bf16 v[196:199], v[132:135], v[188:191], v[196:199]\n\t"
            "v_mul_f32 v167, v237, v167\n\t"
            "v_cvt_pk_bf16_f32 v164, v164, v165\n\t"
            "v_mfma_f32_16x16x32_bf16 v[196:199], v[136:139], v[192:195], v[196:199]\n\t"
            "v_cvt_pk_bf16_f32 v165, v166, v167\n\t"
            "v_mul_f32 v160, v237, v160\n\t"
            "v_mfma_f32_16x16x32_bf16 v[196:199], v[140:143], v[200:203], v[196:199]\n\t"
            "v_mul_f32 v161, v237, v161\n\t"
            "v_mul_f32 v162, v237, v162\n\t"
            "s_waitcnt lgkmcnt(4)\n\t"
            "v_mfma_f32_16x16x32_bf16 v[176:179], v[48:51], v[204:207], 0\n\t"
            "v_mul_f32 v163, v237, v163\n\t"
            "v_cvt_pk_bf16_f32 v160, v160, v161\n\t"
            "s_waitcnt lgkmcnt(3)\n\t"
            "v_mfma_f32_16x16x32_bf16 v[176:179], v[52:55], v[208:211], v[176:179]\n\t"
            "v_cvt_pk_bf16_f32 v161, v162, v163\n\t"
            "v_mul_f32 v228, v237, v228\n\t"
            "v_mfma_f32_16x16x32_bf16 v[176:179], v[56:59], v[212:215], v[176:179]\n\t"
            "v_mul_f32 v196, v237, v196\n\t"
            "v_mul_f32 v197, v237, v197\n\t"
            "s_waitcnt lgkmcnt(2)\n\t"
            "v_mfma_f32_16x16x32_bf16 v[176:179], v[60:63], v[216:219], v[176:179]\n\t"
            "v_mul_f32 v198, v237, v198\n\t"
            "v_mul_f32 v199, v237, v199\n\t"
            "s_waitcnt lgkmcnt(1)\n\t"
            "v_mfma_f32_16x16x32_bf16 v[176:179], v[64:67], v[220:223], v[176:179]\n\t"
            "v_cvt_pk_bf16_f32 v166, v196, v197\n\t"
            "v_cvt_pk_bf16_f32 v167, v198, v199\n\t"
            "s_waitcnt lgkmcnt(0)\n\t"
            "v_mfma_f32_16x16x32_bf16 v[176:179], v[68:71], v[224:227], v[176:179]\n\t"
            "v_mul_f32 v229, v237, v229\n\t"
            "v_mul_f32 v230, v237, v230\n\t"
            "v_mul_f32 v231, v237, v231\n\t"
            "v_cvt_pk_bf16_f32 v162, v228, v229\n\t"
            "v_mfma_f32_16x16x16_bf16 v[188:191], v[152:153], v[166:167], 0\n\t"
            "v_cvt_pk_bf16_f32 v163, v230, v231\n\t"
            "v_exp_f32 v180, v180\n\t"
            "v_mfma_f32_16x16x16_bf16 v[192:195], v[164:165], v[166:167], 0\n\t"
            "v_exp_f32 v181, v181\n\t"
            "v_exp_f32 v182, v182\n\t"
            "v_mfma_f32_16x16x16_bf16 v[184:187], v[164:165], v[154:155], 0\n\t"
            "v_exp_f32 v183, v183\n\t"
            "v_mul_f32 v176, v238, v176\n\t"
            "v_mfma_f32_16x16x32_bf16 v[168:171], v[0:3], v[204:207], 0\n\t"
            "v_mul_f32 v177, v238, v177\n\t"
            "v_mul_f32 v178, v238, v178\n\t"
            "v_mfma_f32_16x16x32_bf16 v[168:171], v[4:7], v[208:211], v[168:171]\n\t"
            "v_mul_f32 v179, v238, v179\n\t"
            "v_cvt_pk_bf16_f32 v176, v176, v177\n\t"
            "v_mfma_f32_16x16x32_bf16 v[168:171], v[8:11], v[212:215], v[168:171]\n\t"
            "v_cvt_pk_bf16_f32 v180, v180, v181\n\t"
            "v_cvt_pk_bf16_f32 v177, v178, v179\n\t"
            "v_mfma_f32_16x16x32_bf16 v[168:171], v[12:15], v[216:219], v[168:171]\n\t"
            "v_cvt_pk_bf16_f32 v181, v182, v183\n\t"
            "v_mfma_f32_16x16x32_bf16 v[168:171], v[16:19], v[220:223], v[168:171]\n\t"
            "v_mfma_f32_16x16x32_bf16 v[168:171], v[20:23], v[224:227], v[168:171]\n\t"
            "v_mfma_f32_16x16x32_bf16 v[188:191], v[144:147], v[160:163], v[188:191]\n\t"
            "v_mfma_f32_16x16x32_bf16 v[200:203], v[120:123], v[204:207], 0\n\t"
            "v_mfma_f32_16x16x32_bf16 v[200:203], v[124:127], v[208:211], v[200:203]\n\t"
            "v_mfma_f32_16x16x32_bf16 v[200:203], v[128:131], v[212:215], v[200:203]\n\t"
            "v_mfma_f32_16x16x32_bf16 v[200:203], v[132:135], v[216:219], v[200:203]\n\t"
            "v_mfma_f32_16x16x32_bf16 v[200:203], v[136:139], v[220:223], v[200:203]\n\t"
            "s_nop 1\n\t"
            "v_mul_f32 v168, v238, v168\n\t"
            "v_mfma_f32_16x16x32_bf16 v[200:203], v[140:143], v[224:227], v[200:203]\n\t"
            "v_exp_f32 v188, v188\n\t"
            "v_mfma_f32_16x16x32_bf16 v[192:195], v[156:159], v[160:163], v[192:195]\n\t"
            "s_setprio 0\n\t"
            "v_exp_f32 v189, v189\n\t"
            "v_mfma_f32_16x16x32_bf16 v[172:175], v[72:75], v[204:207], 0\n\t"
            "v_exp_f32 v190, v190\n\t"
            "v_mfma_f32_16x16x32_bf16 v[172:175], v[76:79], v[208:211], v[172:175]\n\t"
            "v_exp_f32 v191, v191\n\t"
            "v_mfma_f32_16x16x32_bf16 v[172:175], v[80:83], v[212:215], v[172:175]\n\t"
            "s_nop 0\n\t"
            "v_exp_f32 v192, v192\n\t"
            "v_mfma_f32_16x16x32_bf16 v[172:175], v[84:87], v[216:219], v[172:175]\n\t"
            "v_exp_f32 v193, v193\n\t"
            "v_mfma_f32_16x16x32_bf16 v[172:175], v[88:91], v[220:223], v[172:175]\n\t"
            "v_exp_f32 v194, v194\n\t"
            "v_mfma_f32_16x16x32_bf16 v[172:175], v[92:95], v[224:227], v[172:175]\n\t"
            "v_exp_f32 v195, v195\n\t"
            "v_mfma_f32_16x16x32_bf16 v[184:187], v[156:159], v[148:151], v[184:187]\n\t"
            "v_cvt_pk_bf16_f32 v188, v188, v189\n\t"
            "v_cvt_pk_bf16_f32 v189, v190, v191\n\t"
            "v_mfma_f32_16x16x32_bf16 v[196:199], v[24:27], v[204:207], 0\n\t"
            "v_cvt_pk_bf16_f32 v190, v192, v193\n\t"
            "v_cvt_pk_bf16_f32 v191, v194, v195\n\t"
            "v_mfma_f32_16x16x32_bf16 v[196:199], v[28:31], v[208:211], v[196:199]\n\t"
            "v_mul_f32 v169, v238, v169\n\t"
            "v_mul_f32 v170, v238, v170\n\t"
            "v_mfma_f32_16x16x32_bf16 v[196:199], v[32:35], v[212:215], v[196:199]\n\t"
            "v_exp_f32 v184, v184\n\t"
            "v_mfma_f32_16x16x32_bf16 v[196:199], v[36:39], v[216:219], v[196:199]\n\t"
            "v_exp_f32 v185, v185\n\t"
            "v_mfma_f32_16x16x32_bf16 v[196:199], v[40:43], v[220:223], v[196:199]\n\t"
            "v_exp_f32 v186, v186\n\t"
            "v_mfma_f32_16x16x32_bf16 v[196:199], v[44:47], v[224:227], v[196:199]\n\t"
            "v_exp_f32 v187, v187\n\t"
            "v_cvt_pk_bf16_f32 v182, v184, v185\n\t"
            "v_cvt_pk_bf16_f32 v183, v186, v187\n\t"
            "v_mfma_f32_16x16x16_bf16 v[184:187], v[176:177], v[154:155], 0\n\t"
            "v_mul_f32 v171, v238, v171\n\t"
            "v_cvt_pk_bf16_f32 v168, v168, v169\n\t"
            "v_cvt_pk_bf16_f32 v169, v170, v171\n\t"
            "v_mfma_f32_16x16x16_bf16 v[192:195], v[176:177], v[166:167], 0\n\t"
            "v_mul_f32 v196, v238, v196\n\t"
            "v_mul_f32 v197, v238, v197\n\t"
            "v_mul_f32 v198, v238, v198\n\t"
            "v_mfma_f32_16x16x32_bf16 v[228:231], v[96:99], v[204:207], 0\n\t"
            "ds_read_b64 v[204:205], v235 offset:136\n\t"
            "v_mul_f32 v199, v238, v199\n\t"
            "v_mfma_f32_16x16x32_bf16 v[228:231], v[100:103], v[208:211], v[228:231]\n\t"
            "ds_read_b64 v[206:207], v234 offset:128\n\t"
            "v_cvt_pk_bf16_f32 v170, v196, v197\n\t"
            "v_mfma_f32_16x16x32_bf16 v[228:231], v[104:107], v[212:215], v[228:231]\n\t"
            "ds_read_b128 v[208:211], v234\n\t"
            "v_cvt_pk_bf16_f32 v171, v198, v199\n\t"
            "v_mfma_f32_16x16x32_bf16 v[228:231], v[108:111], v[216:219], v[228:231]\n\t"
            "ds_read_b128 v[212:215], v235 offset:64\n\t"
            "v_mul_f32 v172, v238, v172\n\t"
            "v_mfma_f32_16x16x32_bf16 v[228:231], v[112:115], v[220:223], v[228:231]\n\t"
            "v_mul_f32 v173, v238, v173\n\t"
            "v_mul_f32 v200, v238, v200\n\t"
            "v_mfma_f32_16x16x32_bf16 v[228:231], v[116:119], v[224:227], v[228:231]\n\t"
            "v_mul_f32 v201, v238, v201\n\t"
            "v_mul_f32 v202, v238, v202\n\t"
            "v_mfma_f32_16x16x32_bf16 v[184:187], v[168:171], v[148:151], v[184:187]\n\t"
            "v_mul_f32 v203, v238, v203\n\t"
            "v_mul_f32 v174, v238, v174\n\t"
            "v_mfma_f32_16x16x32_bf16 v[192:195], v[168:171], v[160:163], v[192:195]\n\t"
            "v_mul_f32 v175, v238, v175\n\t"
            "v_cvt_pk_bf16_f32 v172, v172, v173\n\t"
            "v_cvt_pk_bf16_f32 v178, v200, v201\n\t"
            "v_cvt_pk_bf16_f32 v179, v202, v203\n\t"
            "v_cvt_pk_bf16_f32 v173, v174, v175\n\t"
            "v_mul_f32 v228, v238, v228\n\t"
            "v_mul_f32 v229, v238, v229\n\t"
            "s_waitcnt lgkmcnt(2)\n\t"
            "v_mfma_f32_16x16x32_bf16 v[216:219], v[204:207], v[152:155], 0\n\t"
            "v_mul_f32 v230, v238, v230\n\t"
            "v_mul_f32 v231, v238, v231\n\t"
            "s_waitcnt lgkmcnt(1)\n\t"
            "v_mfma_f32_16x16x32_bf16 v[216:219], v[208:211], v[148:151], v[216:219]\n\t"
            "v_cvt_pk_bf16_f32 v174, v228, v229\n\t"
            "v_cvt_pk_bf16_f32 v175, v230, v231\n\t"
            "s_waitcnt lgkmcnt(0)\n\t"
            "v_mfma_f32_16x16x32_bf16 v[216:219], v[212:215], v[144:147], v[216:219]\n\t"
            "v_exp_f32 v184, v184\n\t"
            "v_mfma_f32_16x16x32_bf16 v[220:223], v[204:207], v[164:167], 0\n\t"
            "v_exp_f32 v185, v185\n\t"
            "v_mfma_f32_16x16x32_bf16 v[220:223], v[208:211], v[160:163], v[220:223]\n\t"
            "v_exp_f32 v186, v186\n\t"
            "v_mfma_f32_16x16x32_bf16 v[220:223], v[212:215], v[156:159], v[220:223]\n\t"
            "v_exp_f32 v187, v187\n\t"
            "v_cvt_pk_bf16_f32 v184, v184, v185\n\t"
            "v_exp_f32 v216, v216\n\t"
            "v_mfma_f32_16x16x16_bf16 v[228:231], v[164:165], v[178:179], 0\n\t"
            "v_cvt_pk_bf16_f32 v185, v186, v187\n\t"
            "v_cvt_pk_bf16_f32 v216, v216, 0\n\t"
            "v_cndmask_b32_e64 v186, 0, v216, s[58:59]\n\t"
            "v_mfma_f32_16x16x16_bf16 v[200:203], v[176:177], v[178:179], 0\n\t"
            "v_mov_b32 v187, 0\n\t"
            "v_cndmask_b32_e64 v235, v217, v221, s[60:61]\n\t"
            "v_exp_f32 v192, v192\n\t"
            "v_mfma_f32_16x16x16_bf16 v[196:199], v[152:153], v[178:179], 0\n\t"
            "v_exp_f32 v193, v193\n\t"
            "v_exp_f32 v194, v194\n\t"
            "v_mfma_f32_16x16x32_bf16 v[224:227], v[204:207], v[176:179], 0\n\t"
            "v_exp_f32 v195, v195\n\t"
            "v_mfma_f32_16x16x32_bf16 v[224:227], v[208:211], v[172:175], v[224:227]\n\t"
            "v_exp_f32 v220, v220\n\t"
            "v_mfma_f32_16x16x32_bf16 v[224:227], v[212:215], v[168:171], v[224:227]\n\t"
            "v_cvt_pk_bf16_f32 v192, v192, v193\n\t"
            "v_cvt_pk_bf16_f32 v193, v194, v195\n\t"
            "v_mfma_f32_16x16x32_bf16 v[204:207], v[240:243], v[180:183], 0\n\t"
            "v_cvt_pk_bf16_f32 v220, v220, 0\n\t"
            "v_cndmask_b32_e64 v194, 0, v220, s[58:59]\n\t"
            "v_mfma_f32_16x16x32_bf16 v[204:207], v[240:243], v[184:187], v[204:207]\n\t"
            "v_mov_b32 v195, 0\n\t"
            "v_mfma_f32_16x16x32_bf16 v[200:203], v[168:171], v[172:175], v[200:203]\n\t"
            "v_exp_f32 v224, v224\n\t"
            "v_mfma_f32_16x16x32_bf16 v[196:199], v[144:147], v[172:175], v[196:199]\n\t"
            "v_cvt_pk_bf16_f32 v224, v224, 0\n\t"
            "v_cndmask_b32_e64 v235, v235, v225, s[62:63]\n\t"
            "v_mfma_f32_16x16x32_bf16 v[228:231], v[156:159], v[172:175], v[228:231]\n\t"
            "v_cndmask_b32_e64 v235, v235, v239, s[64:65]\n\t"
            "v_cmp_nlt_f32_e64 s[24:25], |v235|, s23\n\t"
            "v_exp_f32 v235, v235\n\t"
            "v_exp_f32 v200, v200\n\t"
            "v_exp_f32 v201, v201\n\t"
            "v_exp_f32 v196, v196\n\t"
            "v_exp_f32 v197, v197\n\t"
            "v_exp_f32 v198, v198\n\t"
            "v_exp_f32 v199, v199\n\t"
            "v_exp_f32 v228, v228\n\t"
            "v_exp_f32 v229, v229\n\t"
            "v_exp_f32 v230, v230\n\t"
            "v_exp_f32 v231, v231\n\t"
            "v_cvt_pk_bf16_f32 v196, v196, v197\n\t"
            "v_cvt_pk_bf16_f32 v197, v198, v199\n\t"
            "v_cvt_pk_bf16_f32 v198, v228, v229\n\t"
            "v_cvt_pk_bf16_f32 v199, v230, v231\n\t"
            "v_exp_f32 v202, v202\n\t"
            "v_exp_f32 v203, v203\n\t"
            "v_mfma_f32_16x16x32_bf16 v[212:215], v[240:243], v[196:199], 0\n\t"
            "v_cvt_pk_bf16_f32 v200, v200, v201\n\t"
            "v_cvt_pk_bf16_f32 v201, v202, v203\n\t"
            "v_cndmask_b32_e64 v202, 0, v224, s[58:59]\n\t"
            "v_mov_b32 v203, 0\n\t"
            "v_cvt_pk_bf16_f32 v235, v235, 0\n\t"
            "v_cndmask_b32_e64 v235, 0, v235, s[66:67]\n\t"
            "v_mfma_f32_16x16x32_bf16 v[212:215], v[240:243], v[200:203], v[212:215]\n\t"
            "ds_write_b16 v250, v235 offset:256\n\t"
            "v_mfma_f32_16x16x32_bf16 v[208:211], v[240:243], v[188:191], 0\n\t"
            "v_add_u32 v205, 0xe0800000, v204\n\t"
            "v_mfma_f32_16x16x32_bf16 v[208:211], v[240:243], v[192:195], v[208:211]\n\t"
            "v_rcp_f32 v236, v204\n\t"
            "s_nop 2\n\t"
            "v_add_u32 v213, 0xe0800000, v212\n\t"
            "v_rcp_f32 v238, v212\n\t"
            "s_nop 1\n\t"
            "v_add_u32 v209, 0xe0800000, v208\n\t"
            "v_rcp_f32 v237, v208\n\t"
            "v_or3_b32 v205, v205, v209, v213\n\t"
            "v_cmp_le_u32 vcc, 2.0, v205\n\t"
            ".Lwa_in%=:\n\t"
            "s_or_b64 vcc, vcc, s[24:25]\n\t"
            "s_cbranch_scc1 .Lwa_slow%=\n\t"
            ".Lwa_ok%=:\n\t"
            "s_bfe_u32 s49, s70, 0x30001\n\t"
            "s_lshl_b32 s49, s49, 4\n\t"
            "s_or_b32 s73, s77, s49\n\t"
            "s_add_u32 s70, s70, 1\n\t"
            "s_add_u32 s72, s72, 800\n\t"
            "s_add_u32 s76, s76, 800\n\t"
            "s_add_u32 s77, s77, 384\n\t"
            "s_xor_b32 s48, s48, s47\n\t"
            "s_sub_u32 s44, s44, 1\n\t"
            "s_cmp_gt_u32 s44, 2\n\t"
            "s_barrier\n\t"
            "s_setprio 0\n\t"
            "s_cbranch_scc1 .Lwa_loop%=\n\t"
            "s_cmp_eq_u32 s44, 0\n\t"
            "s_cbranch_scc1 .Lwa_done%=\n\t"
            "s_cmp_lg_u32 s44, 2\n\t"
            "s_cbranch_scc1 .Lwa_loop%=\n\t"
            "s_mov_b32 m0, s40\n\t"
            "v_lshlrev_b32 v234, 3, v248\n\t"
            "buffer_load_dword v234, s[36:39], s41 offen sc1 lds\n\t"
            "s_branch .Lwa_loop%=\n\t"
            ".Lwa_slow%=:\n\t"
            "s_waitcnt lgkmcnt(0)\n\t"
            "s_nop 7\n\t"
            "v_mfma_f32_16x16x16_bf16 v[180:183], v[152:153], v[154:155], 0\n\t"
            "v_add_u32 v234, s72, v252\n\t"
            "v_cndmask_b32_e64 v235, v246, v234, s[50:51]\n\t"
            "v_cndmask_b32_e64 v234, v246, v234, s[26:27]\n\t"
            "v_mfma_f32_16x16x16_bf16 v[228:231], v[164:165], v[154:155], 0\n\t"
            "ds_read_b64 v[216:217], v235 offset:136\n\t"
            "ds_read_b64 v[218:219], v234 offset:128\n\t"
            "v_mfma_f32_16x16x16_bf16 v[184:187], v[176:177], v[154:155], 0\n\t"
            "ds_read_b128 v[220:223], v234\n\t"
            "ds_read_b128 v[224:227], v235 offset:64\n\t"
            "v_mfma_f32_16x16x32_bf16 v[180:183], v[144:147], v[148:151], v[180:183]\n\t"
            "v_mfma_f32_16x16x32_bf16 v[228:231], v[156:159], v[148:151], v[228:231]\n\t"
            "s_nop 1\n\t"
            "v_mfma_f32_16x16x32_bf16 v[184:187], v[168:171], v[148:151], v[184:187]\n\t"
            "v_mfma_f32_16x16x16_bf16 v[188:191], v[152:153], v[166:167], 0\n\t"
            "v_mfma_f32_16x16x16_bf16 v[192:195], v[176:177], v[166:167], 0\n\t"
            "v_mfma_f32_16x16x16_bf16 v[196:199], v[152:153], v[178:179], 0\n\t"
            "s_waitcnt lgkmcnt(2)\n\t"
            "v_mfma_f32_16x16x32_bf16 v[204:207], v[216:219], v[152:155], 0\n\t"
            "s_waitcnt lgkmcnt(1)\n\t"
            "v_mfma_f32_16x16x32_bf16 v[204:207], v[220:223], v[148:151], v[204:207]\n\t"
            "s_waitcnt lgkmcnt(0)\n\t"
            "v_mfma_f32_16x16x32_bf16 v[204:207], v[224:227], v[144:147], v[204:207]\n\t"
            "v_mfma_f32_16x16x32_bf16 v[208:211], v[216:219], v[164:167], 0\n\t"
            "v_mfma_f32_16x16x32_bf16 v[208:211], v[220:223], v[160:163], v[208:211]\n\t"
            "v_mfma_f32_16x16x32_bf16 v[208:211], v[224:227], v[156:159], v[208:211]\n\t"
            "v_mfma_f32_16x16x32_bf16 v[188:191], v[144:147], v[160:163], v[188:191]\n\t"
            "v_mfma_f32_16x16x32_bf16 v[192:195], v[168:171], v[160:163], v[192:195]\n\t"
            "v_mfma_f32_16x16x32_bf16 v[212:215], v[216:219], v[176:179], 0\n\t"
            "s_nop 1\n\t"
            "v_max3_f32 v232, v204, v180, v181\n\t"
            "v_mfma_f32_16x16x32_bf16 v[212:215], v[220:223], v[172:175], v[212:215]\n\t"
            "v_max3_f32 v232, v232, v182, v183\n\t"
            "v_max3_f32 v232, v232, v228, v229\n\t"
            "v_mfma_f32_16x16x32_bf16 v[212:215], v[224:227], v[168:171], v[212:215]\n\t"
            "v_max3_f32 v232, v232, v230, v231\n\t"
            "v_max3_f32 v232, v232, v184, v185\n\t"
            "v_mfma_f32_16x16x32_bf16 v[196:199], v[144:147], v[172:175], v[196:199]\n\t"
            "v_max3_f32 v232, v232, v186, v187\n\t"
            "v_mov_b32 v233, v232\n\t"
            "v_cndmask_b32_e64 v235, v205, v209, s[60:61]\n\t"
            "v_mfma_f32_16x16x16_bf16 v[200:203], v[176:177], v[178:179], 0\n\t"
            "v_permlane32_swap_b32 v232, v233\n\t"
            "v_max_f32 v232, v232, v233\n\t"
            "v_mov_b32 v233, v232\n\t"
            "v_max3_f32 v234, v205, v209, v213\n\t"
            "v_max_f32 v234, v234, v239\n\t"
            "v_permlane16_swap_b32 v232, v233\n\t"
            "v_mfma_f32_16x16x32_bf16 v[200:203], v[168:171], v[172:175], v[200:203]\n\t"
            "v_max_f32 v232, v232, v233\n\t"
            "v_sub_f32 v180, v180, v232\n\t"
            "v_sub_f32 v181, v181, v232\n\t"
            "v_exp_f32 v180, v180\n\t"
            "v_exp_f32 v181, v181\n\t"
            "v_sub_f32 v182, v182, v232\n\t"
            "v_sub_f32 v183, v183, v232\n\t"
            "v_exp_f32 v182, v182\n\t"
            "v_exp_f32 v183, v183\n\t"
            "v_sub_f32 v228, v228, v232\n\t"
            "v_sub_f32 v229, v229, v232\n\t"
            "v_sub_f32 v230, v230, v232\n\t"
            "v_sub_f32 v231, v231, v232\n\t"
            "v_exp_f32 v228, v228\n\t"
            "v_exp_f32 v229, v229\n\t"
            "v_exp_f32 v230, v230\n\t"
            "v_exp_f32 v231, v231\n\t"
            "v_cvt_pk_bf16_f32 v180, v180, v181\n\t"
            "v_cvt_pk_bf16_f32 v181, v182, v183\n\t"
            "v_cvt_pk_bf16_f32 v182, v228, v229\n\t"
            "v_cvt_pk_bf16_f32 v183, v230, v231\n\t"
            "v_mfma_f32_16x16x16_bf16 v[228:231], v[164:165], v[166:167], 0\n\t"
            "v_sub_f32 v184, v184, v232\n\t"
            "v_sub_f32 v185, v185, v232\n\t"
            "v_sub_f32 v186, v186, v232\n\t"
            "v_sub_f32 v187, v187, v232\n\t"
            "v_sub_f32 v204, v204, v232\n\t"
            "v_max3_f32 v232, v208, v188, v189\n\t"
            "v_max3_f32 v232, v232, v190, v191\n\t"
            "v_mfma_f32_16x16x32_bf16 v[228:231], v[156:159], v[160:163], v[228:231]\n\t"
            "v_max_f32_dpp v234, v234, v234 row_ror:8 row_mask:0xf bank_mask:0xf\n\t"
            "v_cndmask_b32_e64 v235, v235, v213, s[62:63]\n\t"
            "v_cndmask_b32_e64 v235, v235, v239, s[64:65]\n\t"
            "v_max_f32_dpp v234, v234, v234 row_ror:4 row_mask:0xf bank_mask:0xf\n\t"
            "v_exp_f32 v184, v184\n\t"
            "v_exp_f32 v185, v185\n\t"
            "v_max_f32_dpp v234, v234, v234 row_ror:2 row_mask:0xf bank_mask:0xf\n\t"
            "v_exp_f32 v186, v186\n\t"
            "v_max3_f32 v232, v232, v228, v229\n\t"
            "v_max3_f32 v232, v232, v230, v231\n\t"
            "v_max3_f32 v232, v232, v192, v193\n\t"
            "v_max3_f32 v232, v232, v194, v195\n\t"
            "v_mov_b32 v233, v232\n\t"
            "v_max_f32_dpp v234, v234, v234 row_ror:1 row_mask:0xf bank_mask:0xf\n\t"
            "v_sub_f32 v235, v235, v234\n\t"
            "v_permlane32_swap_b32 v232, v233\n\t"
            "v_max_f32 v232, v232, v233\n\t"
            "v_mov_b32 v233, v232\n\t"
            "v_exp_f32 v235, v235\n\t"
            "v_exp_f32 v187, v187\n\t"
            "v_permlane16_swap_b32 v232, v233\n\t"
            "v_max_f32 v232, v232, v233\n\t"
            "v_sub_f32 v188, v188, v232\n\t"
            "v_sub_f32 v189, v189, v232\n\t"
            "v_exp_f32 v188, v188\n\t"
            "v_exp_f32 v189, v189\n\t"
            "v_sub_f32 v190, v190, v232\n\t"
            "v_sub_f32 v191, v191, v232\n\t"
            "v_exp_f32 v190, v190\n\t"
            "v_exp_f32 v191, v191\n\t"
            "v_sub_f32 v228, v228, v232\n\t"
            "v_sub_f32 v229, v229, v232\n\t"
            "v_sub_f32 v230, v230, v232\n\t"
            "v_sub_f32 v231, v231, v232\n\t"
            "v_exp_f32 v228, v228\n\t"
            "v_exp_f32 v229, v229\n\t"
            "v_exp_f32 v230, v230\n\t"
            "v_exp_f32 v231, v231\n\t"
            "v_cvt_pk_bf16_f32 v188, v188, v189\n\t"
            "v_cvt_pk_bf16_f32 v189, v190, v191\n\t"
            "v_cvt_pk_bf16_f32 v190, v228, v229\n\t"
            "v_cvt_pk_bf16_f32 v191, v230, v231\n\t"
            "v_mfma_f32_16x16x16_bf16 v[228:231], v[164:165], v[178:179], 0\n\t"
            "v_sub_f32 v192, v192, v232\n\t"
            "v_sub_f32 v193, v193, v232\n\t"
            "v_sub_f32 v194, v194, v232\n\t"
            "v_sub_f32 v195, v195, v232\n\t"
            "v_sub_f32 v208, v208, v232\n\t"
            "v_max3_f32 v232, v212, v196, v197\n\t"
            "v_max3_f32 v232, v232, v198, v199\n\t"
            "v_mfma_f32_16x16x32_bf16 v[228:231], v[156:159], v[172:175], v[228:231]\n\t"
            "v_exp_f32 v192, v192\n\t"
            "v_exp_f32 v193, v193\n\t"
            "v_exp_f32 v204, v204\n\t"
            "v_exp_f32 v194, v194\n\t"
            "v_exp_f32 v195, v195\n\t"
            "v_exp_f32 v208, v208\n\t"
            "v_cvt_pk_bf16_f32 v184, v184, v185\n\t"
            "v_cvt_pk_bf16_f32 v192, v192, v193\n\t"
            "v_max3_f32 v232, v232, v228, v229\n\t"
            "v_max3_f32 v232, v232, v230, v231\n\t"
            "v_max3_f32 v232, v232, v200, v201\n\t"
            "v_max3_f32 v232, v232, v202, v203\n\t"
            "v_mov_b32 v233, v232\n\t"
            "v_cvt_pk_bf16_f32 v235, v235, 0\n\t"
            "v_cvt_pk_bf16_f32 v185, v186, v187\n\t"
            "v_permlane32_swap_b32 v232, v233\n\t"
            "v_max_f32 v232, v232, v233\n\t"
            "v_mov_b32 v233, v232\n\t"
            "v_cvt_pk_bf16_f32 v204, v204, 0\n\t"
            "v_cvt_pk_bf16_f32 v193, v194, v195\n\t"
            "v_permlane16_swap_b32 v232, v233\n\t"
            "v_max_f32 v232, v232, v233\n\t"
            "v_sub_f32 v196, v196, v232\n\t"
            "v_sub_f32 v197, v197, v232\n\t"
            "v_exp_f32 v196, v196\n\t"
            "v_exp_f32 v197, v197\n\t"
            "v_sub_f32 v198, v198, v232\n\t"
            "v_sub_f32 v199, v199, v232\n\t"
            "v_exp_f32 v198, v198\n\t"
            "v_exp_f32 v199, v199\n\t"
            "v_sub_f32 v228, v228, v232\n\t"
            "v_sub_f32 v229, v229, v232\n\t"
            "v_sub_f32 v230, v230, v232\n\t"
            "v_sub_f32 v231, v231, v232\n\t"
            "v_sub_f32 v200, v200, v232\n\t"
            "v_sub_f32 v201, v201, v232\n\t"
            "v_exp_f32 v228, v228\n\t"
            "v_exp_f32 v229, v229\n\t"
            "v_exp_f32 v230, v230\n\t"
            "v_exp_f32 v231, v231\n\t"
            "v_exp_f32 v200, v200\n\t"
            "v_exp_f32 v201, v201\n\t"
            "v_sub_f32 v202, v202, v232\n\t"
            "v_sub_f32 v203, v203, v232\n\t"
            "v_sub_f32 v212, v212, v232\n\t"
            "v_cvt_pk_bf16_f32 v196, v196, v197\n\t"
            "v_exp_f32 v202, v202\n\t"
            "v_exp_f32 v203, v203\n\t"
            "v_exp_f32 v212, v212\n\t"
            "v_cvt_pk_bf16_f32 v197, v198, v199\n\t"
            "v_cvt_pk_bf16_f32 v198, v228, v229\n\t"
            "v_cvt_pk_bf16_f32 v199, v230, v231\n\t"
            "v_cvt_pk_bf16_f32 v200, v200, v201\n\t"
            "v_cvt_pk_bf16_f32 v208, v208, 0\n\t"
            "v_cvt_pk_bf16_f32 v201, v202, v203\n\t"
            "v_cvt_pk_bf16_f32 v212, v212, 0\n\t"
            "v_cndmask_b32_e64 v235, 0, v235, s[66:67]\n\t"
            "v_cndmask_b32_e64 v186, 0, v204, s[58:59]\n\t"
            "v_mfma_f32_16x16x32_bf16 v[204:207], v[240:243], v[180:183], 0\n\t"
            "v_cndmask_b32_e64 v194, 0, v208, s[58:59]\n\t"
            "v_cndmask_b32_e64 v202, 0, v212, s[58:59]\n\t"
            "v_mfma_f32_16x16x32_bf16 v[208:211], v[240:243], v[188:191], 0\n\t"
            "ds_write_b16 v250, v235 offset:256\n\t"
            "v_mfma_f32_16x16x32_bf16 v[212:215], v[240:243], v[196:199], 0\n\t"
            "v_mov_b32 v187, 0\n\t"
            "v_mov_b32 v195, 0\n\t"
            "v_mov_b32 v203, 0\n\t"
            "v_mfma_f32_16x16x32_bf16 v[204:207], v[240:243], v[184:187], v[204:207]\n\t"
            "v_mfma_f32_16x16x32_bf16 v[208:211], v[240:243], v[192:195], v[208:211]\n\t"
            "v_mfma_f32_16x16x32_bf16 v[212:215], v[240:243], v[200:203], v[212:215]\n\t"
            "s_nop 5\n\t"
            "v_rcp_f32 v236, v204\n\t"
            "v_rcp_f32 v237, v208\n\t"
            "v_rcp_f32 v238, v212\n\t"
            "s_waitcnt lgkmcnt(0)\n\t"
            "s_nop 7\n\t"
            "s_branch .Lwa_ok%=\n\t"
            ".Lwa_done%=:\n\t"
            "s_setprio 2\n\t"
            "s_waitcnt lgkmcnt(0)\n\t"
            "s_nop 7\n\t"
            "ds_read_b64 v[156:157], v247\n\t"
            "v_add_u32 v235, s45, v248\n\t"
            "v_xor_b32 v234, s73, v254\n\t"
            "v_mov_b32 v159, 0\n\t"
            "v_mov_b32 v163, 0\n\t"
            "v_mov_b32 v167, 0\n\t"
            "ds_read_b128 v[168:171], v235\n\t"
            "ds_read_u16 v158, v234\n\t"
            "ds_read_b128 v[172:175], v235 offset:1536\n\t"
            "ds_read_b128 v[176:179], v235 offset:3072\n\t"
            "ds_read_b64 v[160:161], v247 offset:3072\n\t"
            "ds_read_u16 v162, v234 offset:256\n\t"
            "ds_read_b64 v[164:165], v247 offset:1536\n\t"
            "ds_read_u16 v166, v234 offset:128\n\t"
            "s_waitcnt lgkmcnt(7)\n\t"
            "v_mfma_f32_16x16x32_bf16 v[228:231], v[168:171], v[180:183], 0\n\t"
            "s_waitcnt lgkmcnt(6)\n\t"
            "v_mfma_f32_16x16x32_bf16 v[228:231], v[156:159], v[184:187], v[228:231]\n\t"
            "s_nop 7\n\t"
            "v_mul_f32 v228, v236, v228\n\t"
            "v_mul_f32 v229, v236, v229\n\t"
            "v_mul_f32 v230, v236, v230\n\t"
            "v_mul_f32 v231, v236, v231\n\t"
            "v_cvt_pk_bf16_f32 v228, v228, v229\n\t"
            "v_cvt_pk_bf16_f32 v229, v230, v231\n\t"
            "ds_write_b64 v245, v[228:229]\n\t"
            "s_waitcnt lgkmcnt(6)\n\t"
            "v_mfma_f32_16x16x32_bf16 v[228:231], v[172:175], v[180:183], 0\n\t"
            "s_waitcnt lgkmcnt(5)\n\t"
            "v_mfma_f32_16x16x32_bf16 v[180:183], v[176:179], v[180:183], 0\n\t"
            "s_waitcnt lgkmcnt(3)\n\t"
            "v_mfma_f32_16x16x32_bf16 v[180:183], v[160:163], v[184:187], v[180:183]\n\t"
            "s_waitcnt lgkmcnt(1)\n\t"
            "v_mfma_f32_16x16x32_bf16 v[228:231], v[164:167], v[184:187], v[228:231]\n\t"
            "v_mfma_f32_16x16x32_bf16 v[184:187], v[168:171], v[188:191], 0\n\t"
            "v_mfma_f32_16x16x32_bf16 v[184:187], v[156:159], v[192:195], v[184:187]\n\t"
            "s_nop 3\n\t"
            "v_mul_f32 v180, v236, v180\n\t"
            "v_mul_f32 v181, v236, v181\n\t"
            "v_mul_f32 v182, v236, v182\n\t"
            "v_mul_f32 v183, v236, v183\n\t"
            "v_cvt_pk_bf16_f32 v180, v180, v181\n\t"
            "v_cvt_pk_bf16_f32 v181, v182, v183\n\t"
            "v_mul_f32 v228, v236, v228\n\t"
            "v_mul_f32 v229, v236, v229\n\t"
            "v_mul_f32 v230, v236, v230\n\t"
            "v_mul_f32 v231, v236, v231\n\t"
            "v_cvt_pk_bf16_f32 v228, v228, v229\n\t"
            "v_cvt_pk_bf16_f32 v229, v230, v231\n\t"
            "v_mul_f32 v184, v237, v184\n\t"
            "v_mul_f32 v185, v237, v185\n\t"
            "v_mul_f32 v186, v237, v186\n\t"
            "v_mul_f32 v187, v237, v187\n\t"
            "v_cvt_pk_bf16_f32 v184, v184, v185\n\t"
            "v_cvt_pk_bf16_f32 v185, v186, v187\n\t"
            "ds_read_b64 v[186:187], v253 offset:352\n\t"
            "ds_write_b64 v245, v[180:181] offset:256\n\t"
            "v_mfma_f32_16x16x32_bf16 v[180:183], v[168:171], v[196:199], 0\n\t"
            "ds_write_b64 v245, v[228:229] offset:128\n\t"
            "v_mfma_f32_16x16x32_bf16 v[180:183], v[156:159], v[200:203], v[180:183]\n\t"
            "ds_write_b64 v245, v[184:185] offset:6144\n\t"
            "v_mfma_f32_16x16x32_bf16 v[228:231], v[172:175], v[188:191], 0\n\t"
            "ds_read_b64 v[184:185], v253 offset:320\n\t"
            "v_mfma_f32_16x16x32_bf16 v[228:231], v[164:167], v[192:195], v[228:231]\n\t"
            "v_mfma_f32_16x16x32_bf16 v[188:191], v[176:179], v[188:191], 0\n\t"
            "v_mfma_f32_16x16x32_bf16 v[188:191], v[160:163], v[192:195], v[188:191]\n\t"
            "s_nop 1\n\t"
            "v_mul_f32 v180, v238, v180\n\t"
            "v_mul_f32 v181, v238, v181\n\t"
            "v_mul_f32 v182, v238, v182\n\t"
            "v_mul_f32 v183, v238, v183\n\t"
            "v_cvt_pk_bf16_f32 v180, v180, v181\n\t"
            "v_cvt_pk_bf16_f32 v181, v182, v183\n\t"
            "v_mul_f32 v228, v237, v228\n\t"
            "v_mul_f32 v229, v237, v229\n\t"
            "v_mul_f32 v230, v237, v230\n\t"
            "v_mul_f32 v231, v237, v231\n\t"
            "v_cvt_pk_bf16_f32 v228, v228, v229\n\t"
            "v_cvt_pk_bf16_f32 v229, v230, v231\n\t"
            "v_mul_f32 v188, v237, v188\n\t"
            "v_mul_f32 v189, v237, v189\n\t"
            "v_mul_f32 v190, v237, v190\n\t"
            "v_mul_f32 v191, v237, v191\n\t"
            "v_cvt_pk_bf16_f32 v188, v188, v189\n\t"
            "v_cvt_pk_bf16_f32 v189, v190, v191\n\t"
            "ds_read_b64 v[182:183], v253 offset:288\n\t"
            "ds_write_b64 v245, v[180:181] offset:12288\n\t"
            "ds_read_b64 v[180:181], v253 offset:256\n\t"
            "ds_write_b64 v245, v[228:229] offset:6272\n\t"
            "v_mfma_f32_16x16x32_bf16 v[228:231], v[172:175], v[196:199], 0\n\t"
            "ds_write_b64 v245, v[188:189] offset:6400\n\t"
            "v_mfma_f32_16x16x32_bf16 v[228:231], v[164:167], v[200:203], v[228:231]\n\t"
            "v_mfma_f32_16x16x32_bf16 v[196:199], v[176:179], v[196:199], 0\n\t"
            "v_mfma_f32_16x16x32_bf16 v[196:199], v[160:163], v[200:203], v[196:199]\n\t"
            "s_nop 5\n\t"
            "v_mul_f32 v228, v238, v228\n\t"
            "s_waitcnt lgkmcnt(2)\n\t"
            "v_mfma_f32_16x16x32_bf16 v[168:171], v[168:171], v[180:183], 0\n\t"
            "v_mfma_f32_16x16x32_bf16 v[172:175], v[172:175], v[180:183], 0\n\t"
            "v_mfma_f32_16x16x32_bf16 v[176:179], v[176:179], v[180:183], 0\n\t"
            "v_mul_f32 v229, v238, v229\n\t"
            "v_mfma_f32_16x16x32_bf16 v[168:171], v[156:159], v[184:187], v[168:171]\n\t"
            "v_mul_f32 v230, v238, v230\n\t"
            "v_mul_f32 v231, v238, v231\n\t"
            "v_mfma_f32_16x16x32_bf16 v[172:175], v[164:167], v[184:187], v[172:175]\n\t"
            "v_cvt_pk_bf16_f32 v228, v228, v229\n\t"
            "v_cvt_pk_bf16_f32 v229, v230, v231\n\t"
            "v_mfma_f32_16x16x32_bf16 v[176:179], v[160:163], v[184:187], v[176:179]\n\t"
            "ds_write_b64 v245, v[228:229] offset:12416\n\t"
            "v_mfma_f32_16x16x32_bf16 v[228:231], v[240:243], v[180:183], 0\n\t"
            "v_mul_f32 v196, v238, v196\n\t"
            "v_mul_f32 v197, v238, v197\n\t"
            "v_mfma_f32_16x16x32_bf16 v[228:231], v[240:243], v[184:187], v[228:231]\n\t"
            "v_mul_f32 v198, v238, v198\n\t"
            "v_mul_f32 v199, v238, v199\n\t"
            "v_cvt_pk_bf16_f32 v196, v196, v197\n\t"
            "v_cvt_pk_bf16_f32 v197, v198, v199\n\t"
            "ds_write_b64 v245, v[196:197] offset:12544\n\t"
            "s_nop 2\n\t"
            "v_rcp_f32 v229, v228\n\t"
            "s_nop 0\n\t"
            "v_mul_f32 v168, v229, v168\n\t"
            "v_mul_f32 v169, v229, v169\n\t"
            "v_mul_f32 v172, v229, v172\n\t"
            "v_mul_f32 v173, v229, v173\n\t"
            "v_mul_f32 v176, v229, v176\n\t"
            "v_mul_f32 v177, v229, v177\n\t"
            "v_mul_f32 v170, v229, v170\n\t"
            "v_mul_f32 v171, v229, v171\n\t"
            "v_cvt_pk_bf16_f32 v168, v168, v169\n\t"
            "v_mul_f32 v174, v229, v174\n\t"
            "v_mul_f32 v175, v229, v175\n\t"
            "v_cvt_pk_bf16_f32 v172, v172, v173\n\t"
            "v_mul_f32 v178, v229, v178\n\t"
            "v_mul_f32 v179, v229, v179\n\t"
            "v_cvt_pk_bf16_f32 v176, v176, v177\n\t"
            "v_xor_b32 v230, s73, v255\n\t"
            "v_cvt_pk_bf16_f32 v169, v170, v171\n\t"
            "v_cvt_pk_bf16_f32 v173, v174, v175\n\t"
            "v_cvt_pk_bf16_f32 v177, v178, v179\n\t"
            "s_mov_b64 exec, s[68:69]\n\t"
            "ds_write_b64 v230, v[168:169]\n\t"
            "ds_write_b64 v230, v[172:173] offset:128\n\t"
            "ds_write_b64 v230, v[176:177] offset:256\n\t"
            "s_mov_b64 exec, -1\n\t"
            "s_waitcnt lgkmcnt(0)\n\t"
            "v_mov_b32 v144, s42\n\t"
            "v_bfrev_b32 v145, -2\n\t"
            "ds_write_b32 v144, v145\n\t"
            "s_waitcnt lgkmcnt(0)\n\t"
            "s_nop 7\n\t"
            : "+{v[232:235]}"(pl), "+{v249}"(aRSw_),
              "+{s44}"(s44), "+{s48}"(s48), "+{s70}"(s70), "+{s72}"(s72), "+{s73}"(s73), "+{s76}"(s76), "+{s77}"(s77)
            : "{v244}"(pE_), "{v245}"(aO_), "{v246}"(aZ_), "{v247}"(aVB_), "{v248}"(pST_), "{v250}"(aPLw_), "{v251}"(aRSr_), "{v252}"(g16_), "{v253}"(aPLr_), "{v254}"(loVL_), "{v255}"(loOL_), "{v[240:243]}"(ones4), "{s47}"((ldsb + X_OFF) ^ (ldsb + X1_OFF)),
              "{s[58:59]}"(0xFFFFull), "{s[60:61]}"(0xFFFF0000ull), "{s[62:63]}"(0xFFFF00000000ull), "{s[64:65]}"(0xFFFF000000000000ull),
              "{s[66:67]}"(0x1FFFFFFFFFFFFull), "{s[68:69]}"(0x0001000100010001ull), "{s[26:27]}"(0x1111111111111111ull), "{s23}"(64.f), "{s57}"(RS_OFF + 768 + (4 + (w >> 1)) * 192 - RS_BASE), "{s45}"(ldsb + V_OFF + w * 4608),
              "{s[28:31]}"(wr), "{s33}"(wave * 36864), "{s46}"(ldsb + PG_OFF), "{s42}"(ldsb + PG_OFF + w * 4), "{s56}"(ldsb + O_OFF), "{s[36:39]}"(tr), "{s41}"(to), "{s40}"(ldsb + X_OFF + 18432), "{s34}"(SH_OFF + bx * 12288 + w * 3072), "{s35}"(ldsb + O_OFF + w * 3072),
              "{s[50:51]}"(0x2222222222222222ull), "{s[52:53]}"(0x4444444444444444ull), "{s[54:55]}"(0x8888888888888888ull)
            : "s24", "s25", "v0", "v1", "v2", "v3", "v4", "v5", "v6", "v7", "v8", "v9", "v10", "v11", "v12", "v13", "v14", "v15", "v16", "v17", "v18", "v19", "v20", "v21", "v22", "v23", "v24", "v25", "v26", "v27", "v28", "v29", "v30", "v31", "v32", "v33", "v34", "v35", "v36", "v37", "v38", "v39", "v40", "v41", "v42", "v43", "v44", "v45", "v46", "v47", "v48", "v49", "v50", "v51", "v52", "v53", "v54", "v55", "v56", "v57", "v58", "v59", "v60", "v61", "v62", "v63", "v64", "v65", "v66", "v67", "v68", "v69", "v70", "v71", "v72", "v73", "v74", "v75", "v76", "v77", "v78", "v79", "v80", "v81", "v82", "v83", "v84", "v85", "v86", "v87", "v88", "v89", "v90", "v91", "v92", "v93", "v94", "v95", "v96", "v97", "v98", "v99", "v100", "v101", "v102", "v103", "v104", "v105", "v106", "v107", "v108", "v109", "v110", "v111", "v112", "v113", "v114", "v115", "v116", "v117", "v118", "v119", "v120", "v121", "v122", "v123", "v124", "v125", "v126", "v127", "v128", "v129", "v130", "v131", "v132", "v133", "v134", "v135", "v136", "v137", "v138", "v139", "v140", "v141", "v142", "v143", "v144", "v145", "v146", "v147", "v148", "v149", "v150", "v151", "v152", "v153", "v154", "v155", "v156", "v157", "v158", "v159", "v160", "v161", "v162", "v163", "v164", "v165", "v166", "v167", "v168", "v169", "v170", "v171", "v172", "v173", "v174", "v175", "v176", "v177", "v178", "v179", "v180", "v181", "v182", "v183", "v184", "v185", "v186", "v187", "v188", "v189", "v190", "v191", "v192", "v193", "v194", "v195", "v196", "v197", "v198", "v199", "v200", "v201", "v202", "v203", "v204", "v205", "v206", "v207", "v208", "v209", "v210", "v211", "v212", "v213", "v214", "v215", "v216", "v217", "v218", "v219", "v220", "v221", "v222", "v223", "v224", "v225", "v226", "v227", "v228", "v229", "v230", "v231", "v236", "v237", "v238", "v239", "s49", "vcc", "scc", "m0", "memory");
        }
        asm volatile("wk_code_end:");

        if (q0 == 40 && wave == 0) {
#pragma unroll 1
            for (int it = 0; it < SPIN_MAX && __hip_atomic_load((gu32*)((char*)wf + CN_OFF), RLX_AGENT) != (unsigned)GRID; ++it) __builtin_amdgcn_s_sleep(8);
            __hip_atomic_store((gu32*)((char*)wf + EP_OFF), __hip_atomic_load((gu32*)((char*)wf + EP_OFF), RLX_AGENT) + 1u, RLX_AGENT);
        }
    } else {
        ROLE_SETUP
        {
            LANE_SETUP
            const bool isV = wave < 6;
            CONV_CONSTS(0)
            const unsigned aCA_ = ldsb + CT_OFF + lane * 16, rsb = ldsb + RS_BASE, pE = fE - X_OFF;
            const unsigned aRSw = (lane < 48 ? rsb : ldsb + RS_OFF + 2304 - 192) + lane * 4, aRSr = rsb + (lane & 48);
            unsigned pr[3];
#pragma unroll
            for (int k = 0; k < 3; ++k) {
                const int c8 = 12 * (w - 2) + 4 * k + g;
                const int ws = c8 / 6, r6 = c8 - 6 * ws;
                pr[k] = l15 * 384 + (r6 >> 1) * 128 + (((2 * ws + (r6 & 1)) ^ sw) << 4);
            }
            unsigned r242 = isV ? pE : ldsb + O_OFF + pE, r243 = isV ? ldsb + V_OFF + w * 9216 + lane * 16 : (ldsb + O_OFF + pE) ^ 64;
            u32x4 r244 = {isV ? ldsb + V_OFF + w * 9216 + 1024 + lane * 8 : pr[0], isV ? aRSr : pr[1], isV ? aRSw : pr[2], (unsigned)lane * 16};
            const int hd = isV ? 2 * w : 2 * w - 3;
            const unsigned vq = ldsb + OL_OFF + l15 * 384 + (((2 * hd + (g >> 1)) ^ sw) << 4) + 8 * (g & 1);
            u32x4 f1 = {16 + l15 < cnt ? vq + 6144 : ldsb + OL_OFF + 22 * 384 + g * 8, pE, ldsb + V_OFF + hd * 4608 + lane * 16, ldsb + V_OFF + hd * 4608 + 1024 + lane * 8};
            u32x4 f2 = {aRSw, aRSr, rsb + l15 * 4, 0u};
            const unsigned lB = ldsb + LB_OFF(l15 & 7) + (g >> 1) * 1536 + (g & 1) * 768 + 96 * w;
            const unsigned long long ob = (unsigned long long)out, xb = (unsigned long long)((const char*)x - XBIAS), wb = (unsigned long long)wf;
            const u32x4 orr = {(unsigned)ob, (unsigned)(ob >> 32) & 0xffffu, (unsigned)(IMG * IMG * CH * 2), 0x00020000u};
            const u32x4 xr = {(unsigned)xb, (unsigned)(xb >> 32) & 0xffffu, (unsigned)(IMG * IMG * CH * 2 + XBIAS), 0x00020000u};
            const u32x4 wr = {(unsigned)wb, (unsigned)(wb >> 32) & 0xffffu, (unsigned)WS_BYTES, 0x00020000u};
            unsigned s45 = OOB, s46 = OOB, s47 = (en0 & 0x7ffffffu) + 96 * w;
            unsigned s48 = ldsb + X1_OFF, s67 = ldsb + X_OFF, s76 = ldsb + WL_OFF + 4, s56 = en0 >> 30;
            unsigned s21 = SH_OFF + bx * 12288, s44 = ldsb + PG_OFF + w * 4, s70 = isV ? wave * 36864 : (wave - 2) * 36864 + 18432;
            unsigned long long xp = 0, sp = 0;
#pragma unroll
            for (int v = 0; v < 4; ++v) {
                const unsigned r = (CROW >> (8 * v)) & 0xFF;
                xp |= (unsigned long long)XPAT(r) << (16 * v);
                sp |= (unsigned long long)SPAT(r) << (16 * v);
            }
#define B_VIN "{v250}"(pXw_), "{v251}"(pSTw_), "{v252}"(aCA_), "{v253}"(aB_), "{v254}"(aA_), "{v255}"(idx_), "{v[238:241]}"(sel4)
#define B_SIN "{s[40:43]}"(orr), "{s[72:75]}"(xr), "{s39}"(0x401fffffu), "{s[28:31]}"(wr), "{s33}"(wave * 36864), "{s78}"(96 * w), \
              "{s27}"(96 * w - 384), "{s[50:51]}"(xp), "{s[52:53]}"(sp), "{s54}"(CLANE), "{s55}"(CROW & 0x0F0F0F0Fu), "{s[34:35]}"(0x0001000100010001ull), \
              "{s[60:61]}"(0x2222222222222222ull), "{s[62:63]}"(0x4444444444444444ull), "{s[64:65]}"(0x8888888888888888ull), \
              "{s77}"(w == 2 ? 768 : 6 * ROWBYTES + 768), "{s25}"(w == 2 ? 3 * ROWBYTES : OOB), "{s32}"(ldsb + X_OFF + 18432), \
              "{s24}"(ldsb + WL_OFF + 12 + 4 * (cnt - 5)), "{s66}"(ldsb + w * 8192), "{s71}"((ldsb + X_OFF) ^ (ldsb + X1_OFF)), \
              "{s23}"(ldsb + WL_OFF), "{s22}"(ldsb + O_OFF), \
              "{s20}"(6 * ROWBYTES + 2304 + 192 * (w & 1)), "{s19}"(2048 + (w & 1) * 1024)
            asm volatile("" :: B_VIN, "{v242}"(r242), "{v243}"(r243), "{v[244:247]}"(r244), "{v[228:231]}"(f1), "{v[232:235]}"(f2), "v"(lB), "v"(vq));
            asm volatile("" :: B_SIN);
            asm volatile("s_waitcnt vmcnt(9)" ::: "memory");
            unsigned lm = *(const unsigned*)(smem + TP_OFF + 4096 + w * 256 + ((l15 & 7) * 4 + g) * 4);
            lm = l15 < 8 ? lm : OOB + 47;
            const int row = lm & 63;
            u32x4 f0 = {XPAT(row) + (unsigned)(O_OFF - X_OFF), SPAT(row) + (unsigned)(STL_OFF - X_OFF - 18816), lB, vq};
            asm volatile(
            "s_setprio 3\n\t"
            "s_waitcnt lgkmcnt(0)\n\t"
            "s_barrier\n\t"
            "s_nop 7\n\t"
            "ds_read_b128 v[168:171], v254\n\t"
            "v_mov_b64 v[176:177], 0\n\t"
            "v_mov_b64 v[178:179], 0\n\t"
            "v_mov_b64 v[180:181], 0\n\t"
            "v_mov_b64 v[182:183], 0\n\t"
            "v_mov_b64 v[184:185], 0\n\t"
            "v_mov_b64 v[186:187], 0\n\t"
            "v_mov_b64 v[188:189], 0\n\t"
            "v_mov_b64 v[190:191], 0\n\t"
            "v_add_u32 v236, s67, v224\n\t"
            "v_xor_b32 v237, 16, v236\n\t"
            "v_add_u32 v248, s67, v225\n\t"
            "ds_read_b64 v[144:145], v226\n\t"
            "ds_read_b64 v[146:147], v226 offset:384\n\t"
            "ds_read_b64 v[148:149], v226 offset:3072\n\t"
            "ds_read_b64 v[150:151], v226 offset:3456\n\t"
            "ds_read_b128 v[172:175], v254 offset:64\n\t"
            "ds_read_b64 v[152:153], v226 offset:8\n\t"
            "ds_read_b64 v[154:155], v226 offset:392\n\t"
            "ds_read_b64 v[156:157], v226 offset:3080\n\t"
            "ds_read_b64 v[158:159], v226 offset:3464\n\t"
            "ds_read_b64 v[160:161], v226 offset:16\n\t"
            "ds_read_b64 v[162:163], v226 offset:400\n\t"
            "ds_read_b64 v[164:165], v226 offset:3088\n\t"
            "ds_read_b64 v[166:167], v226 offset:3472\n\t"
            "s_waitcnt lgkmcnt(9)\n\t"
            "v_smfmac_f32_16x16x64_bf16 v[176:179], v[168:171], v[144:151], v255\n\t"
            "ds_read_b128 v[168:171], v254 offset:128\n\t"
            "ds_read_b64 v[144:145], v226 offset:24\n\t"
            "ds_read_b64 v[146:147], v226 offset:408\n\t"
            "ds_read_b64 v[148:149], v226 offset:3096\n\t"
            "s_waitcnt lgkmcnt(8)\n\t"
            "v_smfmac_f32_16x16x64_bf16 v[180:183], v[172:175], v[152:159], v255\n\t"
            "ds_read_b64 v[150:151], v226 offset:3480\n\t"
            "ds_read_b128 v[172:175], v254 offset:192\n\t"
            "ds_read_b64 v[152:153], v226 offset:32\n\t"
            "v_cvt_pk_bf16_f32 v176, v176, v177\n\t"
            "v_cvt_pk_bf16_f32 v177, v178, v179\n\t"
            "ds_read_b64 v[154:155], v226 offset:416\n\t"
            "ds_read_b64 v[156:157], v226 offset:3104\n\t"
            "ds_read_b64 v[158:159], v226 offset:3488\n\t"
            "v_cvt_pk_bf16_f32 v178, v180, v181\n\t"
            "v_cvt_pk_bf16_f32 v179, v182, v183\n\t"
            "v_mov_b64 v[180:181], 0\n\t"
            "v_mov_b64 v[182:183], 0\n\t"
            "v_mfma_f32_4x4x4_16b_bf16 v[196:199], v[176:177], v[176:177], 0\n\t"
            "v_mfma_f32_4x4x4_16b_bf16 v[192:195], v[238:239], v[176:177], 0\n\t"
            "s_nop 0\n\t"
            "v_mfma_f32_4x4x4_16b_bf16 v[196:199], v[178:179], v[178:179], v[196:199]\n\t"
            "v_mfma_f32_4x4x4_16b_bf16 v[192:195], v[238:239], v[178:179], v[192:195]\n\t"
            "ds_write_b128 v236, v[176:179]\n\t"
            "s_waitcnt lgkmcnt(10)\n\t"
            "v_smfmac_f32_16x16x64_bf16 v[184:187], v[168:171], v[160:167], v255\n\t"
            "ds_read_b128 v[168:171], v254 offset:256\n\t"
            "s_waitcnt lgkmcnt(6)\n\t"
            "v_smfmac_f32_16x16x64_bf16 v[188:191], v[172:175], v[144:151], v255\n\t"
            "ds_read_b64 v[160:161], v226 offset:40\n\t"
            "v_mov_b64 v[176:177], 0\n\t"
            "v_mov_b64 v[178:179], 0\n\t"
            "ds_read_b64 v[162:163], v226 offset:424\n\t"
            "ds_read_b64 v[164:165], v226 offset:3112\n\t"
            "ds_read_b64 v[166:167], v226 offset:3496\n\t"
            "v_cvt_pk_bf16_f32 v184, v184, v185\n\t"
            "v_cvt_pk_bf16_f32 v185, v186, v187\n\t"
            "v_cvt_pk_bf16_f32 v186, v188, v189\n\t"
            "v_cvt_pk_bf16_f32 v187, v190, v191\n\t"
            "v_mov_b64 v[188:189], 0\n\t"
            "v_mov_b64 v[190:191], 0\n\t"
            "v_mfma_f32_4x4x4_16b_bf16 v[196:199], v[184:185], v[184:185], v[196:199]\n\t"
            "v_mfma_f32_4x4x4_16b_bf16 v[192:195], v[238:239], v[184:185], v[192:195]\n\t"
            "s_nop 0\n\t"
            "v_mfma_f32_4x4x4_16b_bf16 v[196:199], v[186:187], v[186:187], v[196:199]\n\t"
            "v_mfma_f32_4x4x4_16b_bf16 v[192:195], v[238:239], v[186:187], v[192:195]\n\t"
            "ds_read_b128 v[172:175], v254 offset:320\n\t"
            "ds_read_b64 v[144:145], v226 offset:48\n\t"
            "s_waitcnt lgkmcnt(6)\n\t"
            "v_smfmac_f32_16x16x64_bf16 v[176:179], v[168:171], v[152:159], v255\n\t"
            "ds_read_b64 v[146:147], v226 offset:432\n\t"
            "ds_read_b64 v[148:149], v226 offset:3120\n\t"
            "ds_read_b64 v[150:151], v226 offset:3504\n\t"
            "ds_read_b128 v[168:171], v254 offset:384\n\t"
            "ds_read_b64 v[152:153], v226 offset:56\n\t"
            "ds_read_b64 v[154:155], v226 offset:440\n\t"
            "ds_read_b64 v[156:157], v226 offset:3128\n\t"
            "ds_read_b64 v[158:159], v226 offset:3512\n\t"
            "v_cvt_pk_bf16_f32 v176, v176, v177\n\t"
            "v_cvt_pk_bf16_f32 v177, v178, v179\n\t"
            "ds_write_b128 v237, v[184:187]\n\t"
            "s_waitcnt lgkmcnt(10)\n\t"
            "v_smfmac_f32_16x16x64_bf16 v[180:183], v[172:175], v[160:167], v255\n\t"
            "ds_read_b128 v[172:175], v254 offset:448\n\t"
            "v_mov_b64 v[184:185], 0\n\t"
            "v_mov_b64 v[186:187], 0\n\t"
            "ds_read_b64 v[160:161], v226 offset:64\n\t"
            "ds_read_b64 v[162:163], v226 offset:448\n\t"
            "ds_read_b64 v[164:165], v226 offset:3136\n\t"
            "s_waitcnt lgkmcnt(9)\n\t"
            "v_smfmac_f32_16x16x64_bf16 v[184:187], v[168:171], v[144:151], v255\n\t"
            "ds_read_b64 v[166:167], v226 offset:3520\n\t"
            "v_cvt_pk_bf16_f32 v178, v180, v181\n\t"
            "v_cvt_pk_bf16_f32 v179, v182, v183\n\t"
            "v_mov_b64 v[180:181], 0\n\t"
            "v_mov_b64 v[182:183], 0\n\t"
            "v_mfma_f32_4x4x4_16b_bf16 v[196:199], v[176:177], v[176:177], v[196:199]\n\t"
            "v_mfma_f32_4x4x4_16b_bf16 v[192:195], v[238:239], v[176:177], v[192:195]\n\t"
            "s_nop 0\n\t"
            "v_mfma_f32_4x4x4_16b_bf16 v[196:199], v[178:179], v[178:179], v[196:199]\n\t"
            "v_mfma_f32_4x4x4_16b_bf16 v[192:195], v[238:239], v[178:179], v[192:195]\n\t"
            "ds_read_b128 v[168:171], v254 offset:512\n\t"
            "v_cvt_pk_bf16_f32 v184, v184, v185\n\t"
            "v_cvt_pk_bf16_f32 v185, v186, v187\n\t"
            "ds_read_b64 v[144:145], v226 offset:72\n\t"
            "s_waitcnt lgkmcnt(6)\n\t"
            "v_smfmac_f32_16x16x64_bf16 v[188:191], v[172:175], v[152:159], v255\n\t"
            "ds_read_b64 v[146:147], v226 offset:456\n\t"
            "ds_read_b64 v[148:149], v226 offset:3144\n\t"
            "ds_read_b64 v[150:151], v226 offset:3528\n\t"
            "ds_read_b128 v[172:175], v254 offset:576\n\t"
            "ds_read_b64 v[152:153], v226 offset:80\n\t"
            "ds_read_b64 v[154:155], v226 offset:464\n\t"
            "ds_read_b64 v[156:157], v226 offset:3152\n\t"
            "ds_read_b64 v[158:159], v226 offset:3536\n\t"
            "v_cvt_pk_bf16_f32 v186, v188, v189\n\t"
            "v_cvt_pk_bf16_f32 v187, v190, v191\n\t"
            "v_mov_b64 v[188:189], 0\n\t"
            "v_mov_b64 v[190:191], 0\n\t"
            "v_mfma_f32_4x4x4_16b_bf16 v[196:199], v[184:185], v[184:185], v[196:199]\n\t"
            "v_mfma_f32_4x4x4_16b_bf16 v[192:195], v[238:239], v[184:185], v[192:195]\n\t"
            "s_nop 0\n\t"
            "v_mfma_f32_4x4x4_16b_bf16 v[196:199], v[186:187], v[186:187], v[196:199]\n\t"
            "v_mfma_f32_4x4x4_16b_bf16 v[192:195], v[238:239], v[186:187], v[192:195]\n\t"
            "ds_write_b128 v236, v[176:179] offset:128\n\t"
            "v_mov_b64 v[176:177], 0\n\t"
            "v_mov_b64 v[178:179], 0\n\t"
            "s_waitcnt lgkmcnt(10)\n\t"
            "s_nop 0\n\t"
            "v_smfmac_f32_16x16x64_bf16 v[176:179], v[168:171], v[160:167], v255\n\t"
            "ds_read_b128 v[168:171], v254 offset:640\n\t"
            "s_waitcnt lgkmcnt(6)\n\t"
            "v_smfmac_f32_16x16x64_bf16 v[180:183], v[172:175], v[144:151], v255\n\t"
            "ds_read_b64 v[160:161], v226 offset:88\n\t"
            "ds_read_b64 v[162:163], v226 offset:472\n\t"
            "ds_read_b64 v[164:165], v226 offset:3160\n\t"
            "ds_read_b64 v[166:167], v226 offset:3544\n\t"
            "ds_read_b128 v[172:175], v254 offset:704\n\t"
            "ds_write_b128 v237, v[184:187] offset:128\n\t"
            "v_mov_b64 v[184:185], 0\n\t"
            "v_mov_b64 v[186:187], 0\n\t"
            "v_cvt_pk_bf16_f32 v176, v176, v177\n\t"
            "v_cvt_pk_bf16_f32 v177, v178, v179\n\t"
            "v_cvt_pk_bf16_f32 v178, v180, v181\n\t"
            "v_cvt_pk_bf16_f32 v179, v182, v183\n\t"
            "s_nop 1\n\t"
            "v_mfma_f32_4x4x4_16b_bf16 v[196:199], v[176:177], v[176:177], v[196:199]\n\t"
            "v_mfma_f32_4x4x4_16b_bf16 v[192:195], v[238:239], v[176:177], v[192:195]\n\t"
            "s_nop 0\n\t"
            "v_mfma_f32_4x4x4_16b_bf16 v[196:199], v[178:179], v[178:179], v[196:199]\n\t"
            "v_mfma_f32_4x4x4_16b_bf16 v[192:195], v[238:239], v[178:179], v[192:195]\n\t"
            "ds_write_b128 v236, v[176:179] offset:256\n\t"
            "s_waitcnt lgkmcnt(7)\n\t"
            "v_smfmac_f32_16x16x64_bf16 v[184:187], v[168:171], v[152:159], v255\n\t"
            "s_waitcnt lgkmcnt(2)\n\t"
            "v_smfmac_f32_16x16x64_bf16 v[188:191], v[172:175], v[160:167], v255\n\t"
            "s_nop 5\n\t"
            "v_cvt_pk_bf16_f32 v184, v184, v185\n\t"
            "v_cvt_pk_bf16_f32 v185, v186, v187\n\t"
            "v_cvt_pk_bf16_f32 v186, v188, v189\n\t"
            "v_cvt_pk_bf16_f32 v187, v190, v191\n\t"
            "s_nop 1\n\t"
            "v_mfma_f32_4x4x4_16b_bf16 v[196:199], v[184:185], v[184:185], v[196:199]\n\t"
            "v_mfma_f32_4x4x4_16b_bf16 v[192:195], v[238:239], v[184:185], v[192:195]\n\t"
            "s_nop 0\n\t"
            "v_mfma_f32_4x4x4_16b_bf16 v[196:199], v[186:187], v[186:187], v[196:199]\n\t"
            "v_mfma_f32_4x4x4_16b_bf16 v[192:195], v[238:239], v[186:187], v[192:195]\n\t"
            "ds_write_b128 v237, v[184:187] offset:256\n\t"
            "s_nop 6\n\t"
            "v_cndmask_b32_e64 v193, v196, v197, s[60:61]\n\t"
            "v_cndmask_b32_e64 v193, v193, v198, s[62:63]\n\t"
            "v_cndmask_b32_e64 v193, v193, v199, s[64:65]\n\t"
            "ds_write_b64 v248, v[192:193] offset:18816\n\t"
            "s_waitcnt lgkmcnt(0)\n\t"
            "s_nop 7\n\t"
            "v_mov_b32 v144, s44\n\t"
            "v_mov_b32 v145, 0x0fffffff\n\t"
            "ds_write_b32 v144, v145\n\t"
            "s_nop 7\n\t"
            "ds_read_b128 v[168:171], v254\n\t"
            "s_waitcnt vmcnt(0)\n\t"
            "ds_read_b64 v[144:145], v253\n\t"
            "v_mov_b64 v[176:177], 0\n\t"
            "v_mov_b64 v[178:179], 0\n\t"
            "v_mov_b64 v[180:181], 0\n\t"
            "v_mov_b64 v[182:183], 0\n\t"
            "v_mov_b32 v249, s76\n\t"
            "v_mov_b64 v[184:185], 0\n\t"
            "v_mov_b64 v[186:187], 0\n\t"
            "v_mov_b64 v[188:189], 0\n\t"
            "v_mov_b64 v[190:191], 0\n\t"
            "v_add_u32 v236, s67, v250\n\t"
            "v_add_u32 v248, s67, v251\n\t"
            "ds_read_b64 v[146:147], v253 offset:96\n\t"
            "ds_read_b64 v[148:149], v253 offset:1744\n\t"
            "ds_read_b64 v[150:151], v253 offset:1840\n\t"
            "ds_read_b128 v[172:175], v254 offset:64\n\t"
            "ds_read_b64 v[152:153], v253 offset:8\n\t"
            "ds_read_b64 v[154:155], v253 offset:104\n\t"
            "ds_read_b64 v[156:157], v253 offset:1752\n\t"
            "ds_read_b64 v[158:159], v253 offset:1848\n\t"
            "ds_read_b64 v[160:161], v253 offset:16\n\t"
            "ds_read_b64 v[162:163], v253 offset:112\n\t"
            "ds_read_b64 v[164:165], v253 offset:1760\n\t"
            "ds_read_b64 v[166:167], v253 offset:1856\n\t"
            "ds_read_b32 v249, v249\n\t"
            "s_waitcnt lgkmcnt(10)\n\t"
            "v_smfmac_f32_16x16x64_bf16 v[176:179], v[168:171], v[144:151], v255\n\t"
            "ds_read_b128 v[168:171], v254 offset:128\n\t"
            "ds_read_b64 v[144:145], v253 offset:24\n\t"
            "ds_read_b64 v[146:147], v253 offset:120\n\t"
            "ds_read_b64 v[148:149], v253 offset:1768\n\t"
            "s_waitcnt lgkmcnt(9)\n\t"
            "v_smfmac_f32_16x16x64_bf16 v[180:183], v[172:175], v[152:159], v255\n\t"
            "ds_read_b64 v[150:151], v253 offset:1864\n\t"
            "ds_read_b128 v[172:175], v254 offset:192\n\t"
            "ds_read_b64 v[152:153], v253 offset:32\n\t"
            "v_cvt_pk_bf16_f32 v176, v176, v177\n\t"
            "v_cvt_pk_bf16_f32 v177, v178, v179\n\t"
            "s_waitcnt lgkmcnt(7)\n\t"
            "v_readfirstlane_b32 s26, v249\n\t"
            "ds_read_b64 v[154:155], v253 offset:128\n\t"
            "s_and_b32 s68, s26, 0x7ffffff\n\t"
            "s_add_u32 s68, s68, s78\n\t"
            "s_and_b32 s69, s26, 0x38000000\n\t"
            "s_or_b32 s69, s69, s39\n\t"
            "s_bitcmp1_b32 s26, 27\n\t"
            "s_cselect_b32 s79, 2.0, s68\n\t"
            "ds_read_b64 v[156:157], v253 offset:1776\n\t"
            "s_and_b32 s57, s56, 3\n\t"
            "s_lshl_b32 s58, s57, 3\n\t"
            "s_lshr_b32 m0, s54, s58\n\t"
            "s_waitcnt lgkmcnt(8)\n\t"
            "v_smfmac_f32_16x16x64_bf16 v[184:187], v[168:171], v[160:167], v255\n\t"
            "s_lshl_b32 s58, s57, 4\n\t"
            "s_lshr_b64 s[36:37], s[50:51], s58\n\t"
            "s_and_b32 s57, s36, 0xffff\n\t"
            "s_add_u32 s57, s57, s67\n\t"
            "ds_read_b64 v[158:159], v253 offset:1872\n\t"
            "s_lshr_b64 s[36:37], s[52:53], s58\n\t"
            "s_and_b32 s58, s36, 0xffff\n\t"
            "s_add_u32 s58, s58, s67\n\t"
            "ds_read_b128 v[168:171], v254 offset:256\n\t"
            "s_and_b32 s59, s50, 0xffff\n\t"
            "s_add_u32 s59, s59, s67\n\t"
            "s_and_b32 s38, s52, 0xffff\n\t"
            "s_add_u32 s38, s38, s67\n\t"
            "s_waitcnt lgkmcnt(5)\n\t"
            "v_smfmac_f32_16x16x64_bf16 v[188:191], v[172:175], v[144:151], v255\n\t"
            "ds_read_b64 v[160:161], v253 offset:40\n\t"
            "v_cvt_pk_bf16_f32 v178, v180, v181\n\t"
            "v_cvt_pk_bf16_f32 v179, v182, v183\n\t"
            "v_writelane_b32 v236, s59, m0\n\t"
            "v_writelane_b32 v236, s57, 63\n\t"
            "v_mfma_f32_4x4x4_16b_bf16 v[196:199], v[176:177], v[176:177], 0\n\t"
            "v_mfma_f32_4x4x4_16b_bf16 v[192:195], v[238:239], v[176:177], 0\n\t"
            "s_nop 0\n\t"
            "v_mfma_f32_4x4x4_16b_bf16 v[196:199], v[178:179], v[178:179], v[196:199]\n\t"
            "v_mfma_f32_4x4x4_16b_bf16 v[192:195], v[238:239], v[178:179], v[192:195]\n\t"
            "ds_read_b64 v[162:163], v253 offset:136\n\t"
            "v_mov_b64 v[180:181], 0\n\t"
            "v_mov_b64 v[182:183], 0\n\t"
            "v_cvt_pk_bf16_f32 v184, v184, v185\n\t"
            "v_cvt_pk_bf16_f32 v185, v186, v187\n\t"
            "v_cvt_pk_bf16_f32 v186, v188, v189\n\t"
            "v_cvt_pk_bf16_f32 v187, v190, v191\n\t"
            "v_xor_b32 v237, 16, v236\n\t"
            "v_mov_b64 v[188:189], 0\n\t"
            "v_mfma_f32_4x4x4_16b_bf16 v[196:199], v[184:185], v[184:185], v[196:199]\n\t"
            "v_mfma_f32_4x4x4_16b_bf16 v[192:195], v[238:239], v[184:185], v[192:195]\n\t"
            "s_nop 0\n\t"
            "v_mfma_f32_4x4x4_16b_bf16 v[196:199], v[186:187], v[186:187], v[196:199]\n\t"
            "v_mfma_f32_4x4x4_16b_bf16 v[192:195], v[238:239], v[186:187], v[192:195]\n\t"
            "ds_read_b64 v[164:165], v253 offset:1784\n\t"
            "v_mov_b64 v[190:191], 0\n\t"
            "v_writelane_b32 v248, s38, m0\n\t"
            "v_writelane_b32 v248, s58, 63\n\t"
            "ds_read_b64 v[166:167], v253 offset:1880\n\t"
            "ds_read_b128 v[172:175], v254 offset:320\n\t"
            "ds_read_b64 v[144:145], v253 offset:48\n\t"
            "ds_read_b64 v[146:147], v253 offset:144\n\t"
            "ds_read_b64 v[148:149], v253 offset:1792\n\t"
            "ds_read_b64 v[150:151], v253 offset:1888\n\t"
            "ds_write_b128 v236, v[176:179]\n\t"
            "v_mov_b64 v[176:177], 0\n\t"
            "v_mov_b64 v[178:179], 0\n\t"
            "s_waitcnt lgkmcnt(10)\n\t"
            "s_nop 0\n\t"
            "v_smfmac_f32_16x16x64_bf16 v[176:179], v[168:171], v[152:159], v255\n\t"
            "ds_read_b128 v[168:171], v254 offset:384\n\t"
            "ds_read_b64 v[152:153], v253 offset:56\n\t"
            "ds_read_b64 v[154:155], v253 offset:152\n\t"
            "ds_read_b64 v[156:157], v253 offset:1800\n\t"
            "ds_read_b64 v[158:159], v253 offset:1896\n\t"
            "s_waitcnt lgkmcnt(10)\n\t"
            "v_smfmac_f32_16x16x64_bf16 v[180:183], v[172:175], v[160:167], v255\n\t"
            "ds_read_b128 v[172:175], v254 offset:448\n\t"
            "ds_read_b64 v[160:161], v253 offset:64\n\t"
            "v_cvt_pk_bf16_f32 v176, v176, v177\n\t"
            "v_cvt_pk_bf16_f32 v177, v178, v179\n\t"
            "ds_read_b64 v[162:163], v253 offset:160\n\t"
            "ds_read_b64 v[164:165], v253 offset:1808\n\t"
            "ds_read_b64 v[166:167], v253 offset:1904\n\t"
            "s_waitcnt lgkmcnt(14)\n\t"
            "ds_write_b128 v237, v[184:187]\n\t"
            "v_mov_b64 v[184:185], 0\n\t"
            "v_mov_b64 v[186:187], 0\n\t"
            "v_cvt_pk_bf16_f32 v178, v180, v181\n\t"
            "v_cvt_pk_bf16_f32 v179, v182, v183\n\t"
            "s_waitcnt lgkmcnt(10)\n\t"
            "v_smfmac_f32_16x16x64_bf16 v[184:187], v[168:171], v[144:151], v255\n\t"
            "ds_read_b128 v[168:171], v254 offset:512\n\t"
            "v_mov_b64 v[180:181], 0\n\t"
            "v_mov_b64 v[182:183], 0\n\t"
            "v_mfma_f32_4x4x4_16b_bf16 v[196:199], v[176:177], v[176:177], v[196:199]\n\t"
            "v_mfma_f32_4x4x4_16b_bf16 v[192:195], v[238:239], v[176:177], v[192:195]\n\t"
            "s_nop 0\n\t"
            "v_mfma_f32_4x4x4_16b_bf16 v[196:199], v[178:179], v[178:179], v[196:199]\n\t"
            "v_mfma_f32_4x4x4_16b_bf16 v[192:195], v[238:239], v[178:179], v[192:195]\n\t"
            "ds_read_b64 v[144:145], v253 offset:72\n\t"
            "s_waitcnt lgkmcnt(7)\n\t"
            "v_smfmac_f32_16x16x64_bf16 v[188:191], v[172:175], v[152:159], v255\n\t"
            "ds_read_b64 v[146:147], v253 offset:168\n\t"
            "v_cvt_pk_bf16_f32 v184, v184, v185\n\t"
            "v_cvt_pk_bf16_f32 v185, v186, v187\n\t"
            "ds_read_b64 v[148:149], v253 offset:1816\n\t"
            "ds_read_b64 v[150:151], v253 offset:1912\n\t"
            "ds_read_b128 v[172:175], v254 offset:576\n\t"
            "ds_read_b64 v[152:153], v253 offset:80\n\t"
            "ds_read_b64 v[154:155], v253 offset:176\n\t"
            "v_cvt_pk_bf16_f32 v186, v188, v189\n\t"
            "v_cvt_pk_bf16_f32 v187, v190, v191\n\t"
            "v_mov_b64 v[188:189], 0\n\t"
            "v_mov_b64 v[190:191], 0\n\t"
            "v_mfma_f32_4x4x4_16b_bf16 v[196:199], v[184:185], v[184:185], v[196:199]\n\t"
            "v_mfma_f32_4x4x4_16b_bf16 v[192:195], v[238:239], v[184:185], v[192:195]\n\t"
            "s_nop 0\n\t"
            "v_mfma_f32_4x4x4_16b_bf16 v[196:199], v[186:187], v[186:187], v[196:199]\n\t"
            "v_mfma_f32_4x4x4_16b_bf16 v[192:195], v[238:239], v[186:187], v[192:195]\n\t"
            "ds_read_b64 v[156:157], v253 offset:1824\n\t"
            "ds_read_b64 v[158:159], v253 offset:1920\n\t"
            "s_waitcnt lgkmcnt(14)\n\t"
            "ds_write_b128 v236, v[176:179] offset:128\n\t"
            "v_mov_b64 v[176:177], 0\n\t"
            "v_mov_b64 v[178:179], 0\n\t"
            "s_waitcnt lgkmcnt(10)\n\t"
            "s_nop 0\n\t"
            "v_smfmac_f32_16x16x64_bf16 v[176:179], v[168:171], v[160:167], v255\n\t"
            "ds_read_b128 v[168:171], v254 offset:640\n\t"
            "s_waitcnt lgkmcnt(6)\n\t"
            "v_smfmac_f32_16x16x64_bf16 v[180:183], v[172:175], v[144:151], v255\n\t"
            "ds_read_b64 v[160:161], v253 offset:88\n\t"
            "ds_read_b64 v[162:163], v253 offset:184\n\t"
            "ds_read_b64 v[164:165], v253 offset:1832\n\t"
            "ds_read_b64 v[166:167], v253 offset:1928\n\t"
            "ds_read_b128 v[172:175], v254 offset:704\n\t"
            "ds_write_b128 v237, v[184:187] offset:128\n\t"
            "v_mov_b64 v[184:185], 0\n\t"
            "v_mov_b64 v[186:187], 0\n\t"
            "v_cvt_pk_bf16_f32 v176, v176, v177\n\t"
            "v_cvt_pk_bf16_f32 v177, v178, v179\n\t"
            "v_cvt_pk_bf16_f32 v178, v180, v181\n\t"
            "v_cvt_pk_bf16_f32 v179, v182, v183\n\t"
            "s_nop 1\n\t"
            "v_mfma_f32_4x4x4_16b_bf16 v[196:199], v[176:177], v[176:177], v[196:199]\n\t"
            "v_mfma_f32_4x4x4_16b_bf16 v[192:195], v[238:239], v[176:177], v[192:195]\n\t"
            "s_nop 0\n\t"
            "v_mfma_f32_4x4x4_16b_bf16 v[196:199], v[178:179], v[178:179], v[196:199]\n\t"
            "v_mfma_f32_4x4x4_16b_bf16 v[192:195], v[238:239], v[178:179], v[192:195]\n\t"
            "ds_write_b128 v236, v[176:179] offset:256\n\t"
            "s_waitcnt lgkmcnt(7)\n\t"
            "v_smfmac_f32_16x16x64_bf16 v[184:187], v[168:171], v[152:159], v255\n\t"
            "s_waitcnt lgkmcnt(2)\n\t"
            "v_smfmac_f32_16x16x64_bf16 v[188:191], v[172:175], v[160:167], v255\n\t"
            "s_nop 5\n\t"
            "v_cvt_pk_bf16_f32 v184, v184, v185\n\t"
            "v_cvt_pk_bf16_f32 v185, v186, v187\n\t"
            "v_cvt_pk_bf16_f32 v186, v188, v189\n\t"
            "v_cvt_pk_bf16_f32 v187, v190, v191\n\t"
            "s_nop 1\n\t"
            "v_mfma_f32_4x4x4_16b_bf16 v[196:199], v[184:185], v[184:185], v[196:199]\n\t"
            "v_mfma_f32_4x4x4_16b_bf16 v[192:195], v[238:239], v[184:185], v[192:195]\n\t"
            "s_nop 0\n\t"
            "v_mfma_f32_4x4x4_16b_bf16 v[196:199], v[186:187], v[186:187], v[196:199]\n\t"
            "v_mfma_f32_4x4x4_16b_bf16 v[192:195], v[238:239], v[186:187], v[192:195]\n\t"
            "ds_write_b128 v237, v[184:187] offset:256\n\t"
            "s_nop 6\n\t"
            "v_cndmask_b32_e64 v193, v196, v197, s[60:61]\n\t"
            "v_cndmask_b32_e64 v193, v193, v198, s[62:63]\n\t"
            "v_cndmask_b32_e64 v193, v193, v199, s[64:65]\n\t"
            "ds_write_b64 v248, v[192:193] offset:18816\n\t"
            "s_waitcnt lgkmcnt(0)\n\t"
            "s_nop 7\n\t"
            "v_mov_b32 v144, s44\n\t"
            "v_mov_b32 v145, 0xbfffffff\n\t"
            "ds_write_b32 v144, v145\n\t"
            "s_setprio 1\n\t"
            "v_mov_b32 v144, s23\n\t"
            ".Lgo%=:\n\t"
            "ds_read_b32 v145, v144 offset:124\n\t"
            "s_waitcnt lgkmcnt(0)\n\t"
            "v_cmp_ne_u32 vcc, -1, v145\n\t"
            "s_nop 4\n\t"
            "s_cbranch_vccnz .Lgo%=\n\t"
            "s_nop 7\n\t"
            "ds_read_b32 v249, v252 offset:1024\n\t"
            "s_add_u32 s49, s70, 0\n\t"
            "buffer_load_dwordx4 v[0:3], v247, s[28:31], s49 offen sc1\n\t"
            "s_add_u32 s49, s70, 1024\n\t"
            "buffer_load_dwordx4 v[4:7], v247, s[28:31], s49 offen sc1\n\t"
            "s_add_u32 s49, s70, 2048\n\t"
            "buffer_load_dwordx4 v[8:11], v247, s[28:31], s49 offen sc1\n\t"
            "s_add_u32 s49, s70, 3072\n\t"
            "buffer_load_dwordx4 v[12:15], v247, s[28:31], s49 offen sc1\n\t"
            "s_add_u32 s49, s70, 4096\n\t"
            "buffer_load_dwordx4 v[16:19], v247, s[28:31], s49 offen sc1\n\t"
            "s_add_u32 s49, s70, 5120\n\t"
            "buffer_load_dwordx4 v[20:23], v247, s[28:31], s49 offen sc1\n\t"
            "s_add_u32 s49, s70, 6144\n\t"
            "buffer_load_dwordx4 v[24:27], v247, s[28:31], s49 offen sc1\n\t"
            "s_add_u32 s49, s70, 7168\n\t"
            "buffer_load_dwordx4 v[28:31], v247, s[28:31], s49 offen sc1\n\t"
            "s_add_u32 s49, s70, 8192\n\t"
            "buffer_load_dwordx4 v[32:35], v247, s[28:31], s49 offen sc1\n\t"
            "s_add_u32 s49, s70, 9216\n\t"
            "buffer_load_dwordx4 v[36:39], v247, s[28:31], s49 offen sc1\n\t"
            "s_add_u32 s49, s70, 10240\n\t"
            "buffer_load_dwordx4 v[40:43], v247, s[28:31], s49 offen sc1\n\t"
            "s_add_u32 s49, s70, 11264\n\t"
            "buffer_load_dwordx4 v[44:47], v247, s[28:31], s49 offen sc1\n\t"
            "s_add_u32 s49, s70, 12288\n\t"
            "buffer_load_dwordx4 v[48:51], v247, s[28:31], s49 offen sc1\n\t"
            "s_add_u32 s49, s70, 13312\n\t"
            "buffer_load_dwordx4 v[52:55], v247, s[28:31], s49 offen sc1\n\t"
            "s_add_u32 s49, s70, 14336\n\t"
            "buffer_load_dwordx4 v[56:59], v247, s[28:31], s49 offen sc1\n\t"
            "s_add_u32 s49, s70, 15360\n\t"
            "buffer_load_dwordx4 v[60:63], v247, s[28:31], s49 offen sc1\n\t"
            "s_add_u32 s49, s70, 16384\n\t"
            "buffer_load_dwordx4 v[64:67], v247, s[28:31], s49 offen sc1\n\t"
            "s_add_u32 s49, s70, 17408\n\t"
            "buffer_load_dwordx4 v[68:71], v247, s[28:31], s49 offen sc1\n\t"
            "s_waitcnt lgkmcnt(0)\n\t"
            "v_and_b32 v249, s69, v249\n\t"
            "s_mov_b32 exec_hi, 0x3fffff\n\t"
            "s_add_u32 m0, s66, 0\n\t"
            "s_add_u32 s49, s79, 0\n\t"
            "buffer_load_dwordx4 v249, s[72:75], s49 offen lds\n\t"
            "s_mov_b32 exec_hi, -1\n\t"
            "s_mov_b32 exec_hi, 0x3fffff\n\t"
            "s_add_u32 m0, s66, 872\n\t"
            "s_add_u32 s49, s68, 196224\n\t"
            "buffer_load_dwordx4 v249, s[72:75], s49 offen lds\n\t"
            "s_mov_b32 exec_hi, -1\n\t"
            "s_mov_b32 exec_hi, 0x3fffff\n\t"
            "s_add_u32 m0, s66, 1744\n\t"
            "s_add_u32 s49, s68, 392448\n\t"
            "buffer_load_dwordx4 v249, s[72:75], s49 offen lds\n\t"
            "s_mov_b32 exec_hi, -1\n\t"
            "s_mov_b32 exec_hi, 0x3fffff\n\t"
            "s_add_u32 m0, s66, 2616\n\t"
            "s_add_u32 s49, s68, 588672\n\t"
            "buffer_load_dwordx4 v249, s[72:75], s49 offen lds\n\t"
            "s_mov_b32 exec_hi, -1\n\t"
            "s_mov_b32 exec_hi, 0x3fffff\n\t"
            "s_add_u32 m0, s66, 3488\n\t"
            "s_add_u32 s49, s68, 784896\n\t"
            "buffer_load_dwordx4 v249, s[72:75], s49 offen lds\n\t"
            "s_mov_b32 exec_hi, -1\n\t"
            "s_mov_b32 exec_hi, 0x3fffff\n\t"
            "s_add_u32 m0, s66, 4360\n\t"
            "s_add_u32 s49, s68, 981120\n\t"
            "buffer_load_dwordx4 v249, s[72:75], s49 offen lds\n\t"
            "s_mov_b32 exec_hi, -1\n\t"
            "s_mov_b32 exec_hi, 0x3fffff\n\t"
            "s_add_u32 m0, s66, 5232\n\t"
            "s_add_u32 s49, s68, 1177344\n\t"
            "buffer_load_dwordx4 v249, s[72:75], s49 offen lds\n\t"
            "s_mov_b32 exec_hi, -1\n\t"
            "s_mov_b32 exec_hi, 0x3fffff\n\t"
            "s_add_u32 m0, s66, 6104\n\t"
            "s_add_u32 s49, s68, 1373568\n\t"
            "buffer_load_dwordx4 v249, s[72:75], s49 offen lds\n\t"
            "s_mov_b32 exec_hi, -1\n\t"
            "s_mov_b32 exec_hi, 0x3fffff\n\t"
            "s_add_u32 m0, s66, 6976\n\t"
            "s_add_u32 s49, s68, 1569792\n\t"
            "buffer_load_dwordx4 v249, s[72:75], s49 offen lds\n\t"
            "s_mov_b32 exec_hi, -1\n\t"
            "s_add_u32 s45, s46, s27\n\t"
            "s_mov_b32 s46, s47\n\t"
            "s_mov_b32 s47, s68\n\t"
            "s_xor_b32 s48, s48, s71\n\t"
            "s_xor_b32 s67, s67, s71\n\t"
            "s_add_u32 s76, s76, 4\n\t"
            "s_lshl_b32 s56, s56, 2\n\t"
            "s_lshr_b32 s49, s26, 30\n\t"
            "s_or_b32 s56, s56, s49\n\t"
            "s_cmp_ge_u32 s76, s24\n\t"
            "s_cselect_b64 vcc, -1, 0\n\t"
            "s_nop 7\n\t"
            "v_mov_b32 v144, s23\n\t"
            ".Lpg%=:\n\t"
            "ds_read_b128 v[148:151], v144 offset:96\n\t"
            "s_waitcnt lgkmcnt(0)\n\t"
            "v_and_b32 v148, v148, v149\n\t"
            "v_and_b32 v150, v150, v151\n\t"
            "v_and_b32 v148, v148, v150\n\t"
            "v_cmp_le_i32 vcc, 0, v148\n\t"
            "s_nop 4\n\t"
            "s_cbranch_vccnz .Lpg%=\n\t"
            "s_setprio 3\n\t"
            "s_nop 7\n\t"
            "v_add_u32 v196, s22, v229\n\t"
            "v_xor_b32 v197, 64, v196\n\t"
            "v_add_u32 v198, s22, v247\n\t"
            "v_mov_b32 v249, s76\n\t"
            "v_add_u32 v236, s67, v250\n\t"
            "v_add_u32 v248, s67, v251\n\t"
            "ds_read_b128 v[144:147], v196\n\t"
            "ds_read_b128 v[148:151], v197\n\t"
            "ds_read_b128 v[152:155], v196 offset:128\n\t"
            "ds_read_b128 v[156:159], v197 offset:128\n\t"
            "ds_read_b128 v[160:163], v196 offset:256\n\t"
            "ds_read_b128 v[164:167], v197 offset:256\n\t"
            "ds_read_b128 v[216:219], v198 offset:12288\n\t"
            "ds_read_b128 v[220:223], v198 offset:13312\n\t"
            "v_add_u32 v198, s48, v247\n\t"
            "ds_read_b128 v[168:171], v196 offset:6144\n\t"
            "ds_read_b128 v[172:175], v197 offset:6144\n\t"
            "ds_read_b128 v[176:179], v196 offset:6272\n\t"
            "ds_read_b128 v[180:183], v197 offset:6272\n\t"
            "ds_read_b128 v[184:187], v196 offset:6400\n\t"
            "v_add_u32 v196, s48, v229\n\t"
            "ds_read_b128 v[188:191], v197 offset:6400\n\t"
            "s_waitcnt vmcnt(9)\n\t"
            "s_waitcnt lgkmcnt(13)\n\t"
            "v_mfma_f32_16x16x32_bf16 v[200:203], v[0:3], v[144:147], 0\n\t"
            "ds_read_b32 v249, v249\n\t"
            "v_xor_b32 v197, 64, v196\n\t"
            "s_waitcnt lgkmcnt(13)\n\t"
            "v_mfma_f32_16x16x32_bf16 v[200:203], v[4:7], v[148:151], v[200:203]\n\t"
            "s_add_u32 s49, s33, 18432\n\t"
            "buffer_load_dwordx4 v[72:75], v247, s[28:31], s49 offen sc1\n\t"
            "s_waitcnt lgkmcnt(12)\n\t"
            "v_mfma_f32_16x16x32_bf16 v[200:203], v[8:11], v[152:155], v[200:203]\n\t"
            "s_waitcnt lgkmcnt(8)\n\t"
            "v_add_f32 v216, v216, v218\n\t"
            "s_waitcnt lgkmcnt(7)\n\t"
            "v_add_f32 v220, v220, v222\n\t"
            "v_mfma_f32_16x16x32_bf16 v[200:203], v[12:15], v[156:159], v[200:203]\n\t"
            "v_add_f32 v216, v216, v220\n\t"
            "v_add_f32 v217, v217, v219\n\t"
            "v_mfma_f32_16x16x32_bf16 v[200:203], v[16:19], v[160:163], v[200:203]\n\t"
            "v_add_f32 v221, v221, v223\n\t"
            "v_mul_f32 v216, 0x3baaaaab, v216\n\t"
            "v_mfma_f32_16x16x32_bf16 v[200:203], v[20:23], v[164:167], v[200:203]\n\t"
            "v_add_f32 v217, v217, v221\n\t"
            "v_mul_f32_e64 v216, v216, -v216\n\t"
            "v_mfma_f32_16x16x32_bf16 v[204:207], v[24:27], v[144:147], 0\n\t"
            "v_fmac_f32 v216, 0x3baaaaab, v217\n\t"
            "v_max_f32 v216, 0, v216\n\t"
            "v_mfma_f32_16x16x32_bf16 v[204:207], v[28:31], v[148:151], v[204:207]\n\t"
            "v_add_f32 v216, 0x3727c5ac, v216\n\t"
            "v_rsq_f32 v216, v216\n\t"
            "v_mfma_f32_16x16x32_bf16 v[204:207], v[32:35], v[152:155], v[204:207]\n\t"
            "ds_write_b32 v232, v216\n\t"
            "v_mfma_f32_16x16x32_bf16 v[204:207], v[36:39], v[156:159], v[204:207]\n\t"
            "ds_read_b32 v192, v234\n\t"
            "s_waitcnt lgkmcnt(2)\n\t"
            "v_readfirstlane_b32 s26, v249\n\t"
            "v_mfma_f32_16x16x32_bf16 v[204:207], v[40:43], v[160:163], v[204:207]\n\t"
            "ds_read_b32 v193, v234 offset:64\n\t"
            "v_mfma_f32_16x16x32_bf16 v[204:207], v[44:47], v[164:167], v[204:207]\n\t"
            "ds_read_b32 v249, v252 offset:1024\n\t"
            "v_mfma_f32_16x16x32_bf16 v[208:211], v[48:51], v[144:147], 0\n\t"
            "ds_read_b128 v[144:147], v196\n\t"
            "v_mfma_f32_16x16x32_bf16 v[208:211], v[52:55], v[148:151], v[208:211]\n\t"
            "ds_read_b128 v[148:151], v197\n\t"
            "v_mfma_f32_16x16x32_bf16 v[208:211], v[56:59], v[152:155], v[208:211]\n\t"
            "ds_read_b128 v[152:155], v196 offset:128\n\t"
            "v_mfma_f32_16x16x32_bf16 v[208:211], v[60:63], v[156:159], v[208:211]\n\t"
            "ds_read_b128 v[156:159], v197 offset:128\n\t"
            "v_mfma_f32_16x16x32_bf16 v[208:211], v[64:67], v[160:163], v[208:211]\n\t"
            "ds_read_b128 v[160:163], v196 offset:256\n\t"
            "v_mfma_f32_16x16x32_bf16 v[208:211], v[68:71], v[164:167], v[208:211]\n\t"
            "ds_read_b128 v[164:167], v197 offset:256\n\t"
            "s_waitcnt lgkmcnt(8)\n\t"
            "v_mul_f32 v200, v192, v200\n\t"
            "v_mfma_f32_16x16x32_bf16 v[212:215], v[0:3], v[168:171], 0\n\t"
            "v_mul_f32 v201, v192, v201\n\t"
            "v_mul_f32 v204, v192, v204\n\t"
            "v_mfma_f32_16x16x32_bf16 v[212:215], v[4:7], v[172:175], v[212:215]\n\t"
            "v_mul_f32 v205, v192, v205\n\t"
            "v_mul_f32 v202, v192, v202\n\t"
            "v_mfma_f32_16x16x32_bf16 v[212:215], v[8:11], v[176:179], v[212:215]\n\t"
            "v_mul_f32 v203, v192, v203\n\t"
            "v_cvt_pk_bf16_f32 v200, v200, v201\n\t"
            "v_mfma_f32_16x16x32_bf16 v[212:215], v[12:15], v[180:183], v[212:215]\n\t"
            "v_mul_f32 v206, v192, v206\n\t"
            "v_mul_f32 v207, v192, v207\n\t"
            "v_mfma_f32_16x16x32_bf16 v[212:215], v[16:19], v[184:187], v[212:215]\n\t"
            "v_cvt_pk_bf16_f32 v204, v204, v205\n\t"
            "v_cvt_pk_bf16_f32 v201, v202, v203\n\t"
            "v_mfma_f32_16x16x32_bf16 v[212:215], v[20:23], v[188:191], v[212:215]\n\t"
            "v_cvt_pk_bf16_f32 v205, v206, v207\n\t"
            "v_mul_f32 v208, v192, v208\n\t"
            "v_mfma_f32_16x16x32_bf16 v[216:219], v[24:27], v[168:171], 0\n\t"
            "ds_write_b64 v227, v[200:201]\n\t"
            "v_mfma_f32_16x16x32_bf16 v[216:219], v[28:31], v[172:175], v[216:219]\n\t"
            "ds_read_b128 v[200:203], v198 offset:18816\n\t"
            "v_mul_f32 v209, v192, v209\n\t"
            "v_mfma_f32_16x16x32_bf16 v[216:219], v[32:35], v[176:179], v[216:219]\n\t"
            "ds_write_b64 v227, v[204:205] offset:128\n\t"
            "v_mfma_f32_16x16x32_bf16 v[216:219], v[36:39], v[180:183], v[216:219]\n\t"
            "ds_read_b128 v[204:207], v198 offset:19840\n\t"
            "v_mul_f32 v210, v192, v210\n\t"
            "v_mfma_f32_16x16x32_bf16 v[216:219], v[40:43], v[184:187], v[216:219]\n\t"
            "s_add_u32 s49, s33, 19456\n\t"
            "buffer_load_dwordx4 v[76:79], v247, s[28:31], s49 offen sc1\n\t"
            "v_mfma_f32_16x16x32_bf16 v[216:219], v[44:47], v[188:191], v[216:219]\n\t"
            "s_add_u32 s49, s33, 20480\n\t"
            "buffer_load_dwordx4 v[80:83], v247, s[28:31], s49 offen sc1\n\t"
            "v_mfma_f32_16x16x32_bf16 v[220:223], v[48:51], v[168:171], 0\n\t"
            "ds_read_b128 v[168:171], v196 offset:6144\n\t"
            "s_waitcnt lgkmcnt(3)\n\t"
            "v_add_f32 v200, v200, v202\n\t"
            "v_mfma_f32_16x16x32_bf16 v[220:223], v[52:55], v[172:175], v[220:223]\n\t"
            "ds_read_b128 v[172:175], v197 offset:6144\n\t"
            "s_waitcnt lgkmcnt(2)\n\t"
            "v_add_f32 v204, v204, v206\n\t"
            "v_mfma_f32_16x16x32_bf16 v[220:223], v[56:59], v[176:179], v[220:223]\n\t"
            "ds_read_b128 v[176:179], v196 offset:6272\n\t"
            "v_add_f32 v200, v200, v204\n\t"
            "v_mfma_f32_16x16x32_bf16 v[220:223], v[60:63], v[180:183], v[220:223]\n\t"
            "ds_read_b128 v[180:183], v197 offset:6272\n\t"
            "v_add_f32 v201, v201, v203\n\t"
            "v_mfma_f32_16x16x32_bf16 v[220:223], v[64:67], v[184:187], v[220:223]\n\t"
            "ds_read_b128 v[184:187], v196 offset:6400\n\t"
            "v_add_f32 v205, v205, v207\n\t"
            "v_mfma_f32_16x16x32_bf16 v[220:223], v[68:71], v[188:191], v[220:223]\n\t"
            "ds_read_b128 v[188:191], v197 offset:6400\n\t"
            "v_mul_f32 v200, 0x3baaaaab, v200\n\t"
            "v_add_f32 v201, v201, v205\n\t"
            "v_mul_f32_e64 v200, v200, -v200\n\t"
            "v_fmac_f32 v200, 0x3baaaaab, v201\n\t"
            "v_max_f32 v200, 0, v200\n\t"
            "v_add_f32 v200, 0x3727c5ac, v200\n\t"
            "v_rsq_f32 v200, v200\n\t"
            "v_mul_f32 v211, v192, v211\n\t"
            "v_cvt_pk_bf16_f32 v208, v208, v209\n\t"
            "v_cvt_pk_bf16_f32 v209, v210, v211\n\t"
            "v_mul_f32 v212, v193, v212\n\t"
            "v_mul_f32 v213, v193, v213\n\t"
            "v_mul_f32 v214, v193, v214\n\t"
            "v_mul_f32 v215, v193, v215\n\t"
            "v_cvt_pk_bf16_f32 v212, v212, v213\n\t"
            "v_cvt_pk_bf16_f32 v213, v214, v215\n\t"
            "v_mul_f32 v216, v193, v216\n\t"
            "v_mul_f32 v217, v193, v217\n\t"
            "v_mul_f32 v218, v193, v218\n\t"
            "v_mul_f32 v219, v193, v219\n\t"
            "v_mul_f32 v220, v193, v220\n\t"
            "v_mul_f32 v221, v193, v221\n\t"
            "v_mul_f32 v222, v193, v222\n\t"
            "v_mul_f32 v223, v193, v223\n\t"
            "v_cvt_pk_bf16_f32 v216, v216, v217\n\t"
            "v_cvt_pk_bf16_f32 v217, v218, v219\n\t"
            "v_cvt_pk_bf16_f32 v220, v220, v221\n\t"
            "v_cvt_pk_bf16_f32 v221, v222, v223\n\t"
            "ds_write_b32 v232, v200\n\t"
            "v_mfma_f32_16x16x32_bf16 v[200:203], v[144:147], v[0:3], 0\n\t"
            "ds_read_b128 v[192:195], v233\n\t"
            "v_mfma_f32_16x16x32_bf16 v[200:203], v[148:151], v[4:7], v[200:203]\n\t"
            "ds_write_b64 v227, v[208:209] offset:256\n\t"
            "v_mfma_f32_16x16x32_bf16 v[200:203], v[152:155], v[8:11], v[200:203]\n\t"
            "s_add_u32 s49, s33, 21504\n\t"
            "buffer_load_dwordx4 v[84:87], v247, s[28:31], s49 offen sc1\n\t"
            "v_mfma_f32_16x16x32_bf16 v[200:203], v[156:159], v[12:15], v[200:203]\n\t"
            "s_add_u32 s49, s33, 22528\n\t"
            "buffer_load_dwordx4 v[88:91], v247, s[28:31], s49 offen sc1\n\t"
            "v_mfma_f32_16x16x32_bf16 v[200:203], v[160:163], v[16:19], v[200:203]\n\t"
            "ds_write_b64 v228, v[212:213]\n\t"
            "v_mfma_f32_16x16x32_bf16 v[200:203], v[164:167], v[20:23], v[200:203]\n\t"
            "s_add_u32 s49, s33, 23552\n\t"
            "buffer_load_dwordx4 v[92:95], v247, s[28:31], s49 offen sc1\n\t"
            "v_mfma_f32_16x16x32_bf16 v[204:207], v[144:147], v[24:27], 0\n\t"
            "s_add_u32 s49, s33, 24576\n\t"
            "buffer_load_dwordx4 v[96:99], v247, s[28:31], s49 offen sc1\n\t"
            "v_mfma_f32_16x16x32_bf16 v[204:207], v[148:151], v[28:31], v[204:207]\n\t"
            "s_add_u32 s49, s33, 25600\n\t"
            "buffer_load_dwordx4 v[100:103], v247, s[28:31], s49 offen sc1\n\t"
            "v_mfma_f32_16x16x32_bf16 v[204:207], v[152:155], v[32:35], v[204:207]\n\t"
            "ds_write_b64 v228, v[216:217] offset:128\n\t"
            "v_mfma_f32_16x16x32_bf16 v[204:207], v[156:159], v[36:39], v[204:207]\n\t"
            "s_add_u32 s49, s33, 26624\n\t"
            "buffer_load_dwordx4 v[104:107], v247, s[28:31], s49 offen sc1\n\t"
            "v_mfma_f32_16x16x32_bf16 v[204:207], v[160:163], v[40:43], v[204:207]\n\t"
            "s_waitcnt lgkmcnt(3)\n\t"
            "v_mul_f32 v200, v192, v200\n\t"
            "v_mul_f32 v201, v193, v201\n\t"
            "v_mfma_f32_16x16x32_bf16 v[204:207], v[164:167], v[44:47], v[204:207]\n\t"
            "v_mul_f32 v202, v194, v202\n\t"
            "v_mul_f32 v203, v195, v203\n\t"
            "v_mfma_f32_16x16x32_bf16 v[208:211], v[144:147], v[48:51], 0\n\t"
            "ds_read_b128 v[144:147], v196 offset:12288\n\t"
            "v_cvt_pk_bf16_f32 v200, v200, v201\n\t"
            "v_mfma_f32_16x16x32_bf16 v[208:211], v[148:151], v[52:55], v[208:211]\n\t"
            "ds_read_b128 v[148:151], v197 offset:12288\n\t"
            "v_cvt_pk_bf16_f32 v201, v202, v203\n\t"
            "v_mfma_f32_16x16x32_bf16 v[208:211], v[152:155], v[56:59], v[208:211]\n\t"
            "ds_read_b128 v[152:155], v196 offset:12416\n\t"
            "v_mul_f32 v204, v192, v204\n\t"
            "v_mfma_f32_16x16x32_bf16 v[208:211], v[156:159], v[60:63], v[208:211]\n\t"
            "ds_read_b128 v[156:159], v197 offset:12416\n\t"
            "v_mul_f32 v205, v193, v205\n\t"
            "v_mfma_f32_16x16x32_bf16 v[208:211], v[160:163], v[64:67], v[208:211]\n\t"
            "ds_read_b128 v[160:163], v196 offset:12544\n\t"
            "v_mul_f32 v206, v194, v206\n\t"
            "v_mfma_f32_16x16x32_bf16 v[208:211], v[164:167], v[68:71], v[208:211]\n\t"
            "ds_read_b128 v[164:167], v197 offset:12544\n\t"
            "v_mul_f32 v207, v195, v207\n\t"
            "v_mfma_f32_16x16x32_bf16 v[212:215], v[168:171], v[0:3], 0\n\t"
            "s_add_u32 s49, s33, 27648\n\t"
            "buffer_load_dwordx4 v[108:111], v247, s[28:31], s49 offen sc1\n\t"
            "v_mfma_f32_16x16x32_bf16 v[212:215], v[172:175], v[4:7], v[212:215]\n\t"
            "ds_write_b64 v228, v[220:221] offset:256\n\t"
            "v_mfma_f32_16x16x32_bf16 v[212:215], v[176:179], v[8:11], v[212:215]\n\t"
            "v_mul_f32 v208, v192, v208\n\t"
            "v_mul_f32 v209, v193, v209\n\t"
            "v_mfma_f32_16x16x32_bf16 v[212:215], v[180:183], v[12:15], v[212:215]\n\t"
            "v_mul_f32 v210, v194, v210\n\t"
            "v_mul_f32 v211, v195, v211\n\t"
            "v_mfma_f32_16x16x32_bf16 v[212:215], v[184:187], v[16:19], v[212:215]\n\t"
            "ds_read_b128 v[192:195], v233 offset:64\n\t"
            "v_cvt_pk_bf16_f32 v204, v204, v205\n\t"
            "v_mfma_f32_16x16x32_bf16 v[212:215], v[188:191], v[20:23], v[212:215]\n\t"
            "s_add_u32 s49, s33, 28672\n\t"
            "buffer_load_dwordx4 v[112:115], v247, s[28:31], s49 offen sc1\n\t"
            "v_mfma_f32_16x16x32_bf16 v[216:219], v[168:171], v[24:27], 0\n\t"
            "s_add_u32 s49, s33, 29696\n\t"
            "buffer_load_dwordx4 v[116:119], v247, s[28:31], s49 offen sc1\n\t"
            "v_mfma_f32_16x16x32_bf16 v[216:219], v[172:175], v[28:31], v[216:219]\n\t"
            "s_add_u32 s49, s33, 30720\n\t"
            "buffer_load_dwordx4 v[120:123], v247, s[28:31], s49 offen sc1\n\t"
            "v_mfma_f32_16x16x32_bf16 v[216:219], v[176:179], v[32:35], v[216:219]\n\t"
            "s_waitcnt lgkmcnt(0)\n\t"
            "v_mul_f32 v212, v192, v212\n\t"
            "v_mul_f32 v213, v193, v213\n\t"
            "v_mfma_f32_16x16x32_bf16 v[216:219], v[180:183], v[36:39], v[216:219]\n\t"
            "v_mul_f32 v214, v194, v214\n\t"
            "v_mul_f32 v215, v195, v215\n\t"
            "v_mfma_f32_16x16x32_bf16 v[216:219], v[184:187], v[40:43], v[216:219]\n\t"
            "v_cvt_pk_bf16_f32 v202, v212, v213\n\t"
            "v_cvt_pk_bf16_f32 v203, v214, v215\n\t"
            "v_mfma_f32_16x16x32_bf16 v[216:219], v[188:191], v[44:47], v[216:219]\n\t"
            "s_add_u32 s49, s33, 31744\n\t"
            "buffer_load_dwordx4 v[124:127], v247, s[28:31], s49 offen sc1\n\t"
            "v_mfma_f32_16x16x32_bf16 v[220:223], v[168:171], v[48:51], 0\n\t"
            "ds_read_b128 v[168:171], v254\n\t"
            "v_cvt_pk_bf16_f32 v205, v206, v207\n\t"
            "v_mfma_f32_16x16x32_bf16 v[220:223], v[172:175], v[52:55], v[220:223]\n\t"
            "ds_read_b128 v[172:175], v254 offset:64\n\t"
            "v_cvt_pk_bf16_f32 v208, v208, v209\n\t"
            "v_mfma_f32_16x16x32_bf16 v[220:223], v[176:179], v[56:59], v[220:223]\n\t"
            "s_add_u32 s49, s33, 32768\n\t"
            "buffer_load_dwordx4 v[128:131], v247, s[28:31], s49 offen sc1\n\t"
            "v_mfma_f32_16x16x32_bf16 v[220:223], v[180:183], v[60:63], v[220:223]\n\t"
            "v_mul_f32 v216, v192, v216\n\t"
            "v_mul_f32 v217, v193, v217\n\t"
            "v_mfma_f32_16x16x32_bf16 v[220:223], v[184:187], v[64:67], v[220:223]\n\t"
            "v_mul_f32 v218, v194, v218\n\t"
            "v_mul_f32 v219, v195, v219\n\t"
            "v_mfma_f32_16x16x32_bf16 v[220:223], v[188:191], v[68:71], v[220:223]\n\t"
            "v_cvt_pk_bf16_f32 v206, v216, v217\n\t"
            "v_cvt_pk_bf16_f32 v207, v218, v219\n\t"
            "v_mfma_f32_16x16x32_bf16 v[212:215], v[144:147], v[0:3], 0\n\t"
            "s_add_u32 s49, s33, 33792\n\t"
            "buffer_load_dwordx4 v[132:135], v247, s[28:31], s49 offen sc1\n\t"
            "v_mfma_f32_16x16x32_bf16 v[212:215], v[148:151], v[4:7], v[212:215]\n\t"
            "s_add_u32 s49, s33, 34816\n\t"
            "buffer_load_dwordx4 v[136:139], v247, s[28:31], s49 offen sc1\n\t"
            "v_mfma_f32_16x16x32_bf16 v[212:215], v[152:155], v[8:11], v[212:215]\n\t"
            "v_cvt_pk_bf16_f32 v209, v210, v211\n\t"
            "v_mul_f32 v220, v192, v220\n\t"
            "v_mfma_f32_16x16x32_bf16 v[212:215], v[156:159], v[12:15], v[212:215]\n\t"
            "v_mul_f32 v221, v193, v221\n\t"
            "v_mul_f32 v222, v194, v222\n\t"
            "v_mfma_f32_16x16x32_bf16 v[212:215], v[160:163], v[16:19], v[212:215]\n\t"
            "v_mul_f32 v223, v195, v223\n\t"
            "v_cvt_pk_bf16_f32 v210, v220, v221\n\t"
            "v_mfma_f32_16x16x32_bf16 v[212:215], v[164:167], v[20:23], v[212:215]\n\t"
            "ds_read_b128 v[192:195], v233 offset:128\n\t"
            "v_cvt_pk_bf16_f32 v211, v222, v223\n\t"
            "v_mfma_f32_16x16x32_bf16 v[216:219], v[144:147], v[24:27], 0\n\t"
            "s_add_u32 s49, s33, 35840\n\t"
            "buffer_load_dwordx4 v[140:143], v247, s[28:31], s49 offen sc1\n\t"
            "v_mfma_f32_16x16x32_bf16 v[216:219], v[148:151], v[28:31], v[216:219]\n\t"
            "s_and_b32 s68, s26, 0x7ffffff\n\t"
            "s_add_u32 s68, s68, s78\n\t"
            "s_and_b32 s69, s26, 0x38000000\n\t"
            "s_or_b32 s69, s69, s39\n\t"
            "s_bitcmp1_b32 s26, 27\n\t"
            "s_cselect_b32 s79, 2.0, s68\n\t"
            "v_mfma_f32_16x16x32_bf16 v[216:219], v[152:155], v[32:35], v[216:219]\n\t"
            "s_and_b32 s57, s56, 3\n\t"
            "s_lshl_b32 s58, s57, 3\n\t"
            "s_lshr_b32 m0, s54, s58\n\t"
            "v_mfma_f32_16x16x32_bf16 v[216:219], v[156:159], v[36:39], v[216:219]\n\t"
            "s_lshl_b32 s58, s57, 4\n\t"
            "s_lshr_b64 s[36:37], s[50:51], s58\n\t"
            "s_and_b32 s57, s36, 0xffff\n\t"
            "s_add_u32 s57, s57, s67\n\t"
            "v_mfma_f32_16x16x32_bf16 v[216:219], v[160:163], v[40:43], v[216:219]\n\t"
            "s_lshr_b64 s[36:37], s[52:53], s58\n\t"
            "s_and_b32 s58, s36, 0xffff\n\t"
            "s_add_u32 s58, s58, s67\n\t"
            "v_mfma_f32_16x16x32_bf16 v[216:219], v[164:167], v[44:47], v[216:219]\n\t"
            "s_and_b32 s59, s50, 0xffff\n\t"
            "s_add_u32 s59, s59, s67\n\t"
            "s_and_b32 s38, s52, 0xffff\n\t"
            "s_add_u32 s38, s38, s67\n\t"
            "v_mfma_f32_16x16x32_bf16 v[220:223], v[144:147], v[48:51], 0\n\t"
            "s_waitcnt vmcnt(18)\n\t"
            "ds_read_b64 v[144:145], v253\n\t"
            "v_mov_b64 v[176:177], 0\n\t"
            "v_mfma_f32_16x16x32_bf16 v[220:223], v[148:151], v[52:55], v[220:223]\n\t"
            "ds_read_b64 v[146:147], v253 offset:96\n\t"
            "v_mov_b64 v[178:179], 0\n\t"
            "v_mfma_f32_16x16x32_bf16 v[220:223], v[152:155], v[56:59], v[220:223]\n\t"
            "ds_read_b64 v[148:149], v253 offset:1744\n\t"
            "v_mov_b64 v[180:181], 0\n\t"
            "v_mfma_f32_16x16x32_bf16 v[220:223], v[156:159], v[60:63], v[220:223]\n\t"
            "ds_read_b64 v[150:151], v253 offset:1840\n\t"
            "v_mov_b64 v[182:183], 0\n\t"
            "v_mfma_f32_16x16x32_bf16 v[220:223], v[160:163], v[64:67], v[220:223]\n\t"
            "ds_read_b64 v[152:153], v253 offset:8\n\t"
            "v_mov_b64 v[184:185], 0\n\t"
            "v_mfma_f32_16x16x32_bf16 v[220:223], v[164:167], v[68:71], v[220:223]\n\t"
            "ds_read_b64 v[154:155], v253 offset:104\n\t"
            "v_mov_b64 v[186:187], 0\n\t"
            "v_mov_b64 v[188:189], 0\n\t"
            "v_mov_b64 v[190:191], 0\n\t"
            "s_waitcnt lgkmcnt(6)\n\t"
            "v_mul_f32 v212, v192, v212\n\t"
            "v_mul_f32 v213, v193, v213\n\t"
            "v_mul_f32 v214, v194, v214\n\t"
            "v_mul_f32 v215, v195, v215\n\t"
            "v_mul_f32 v216, v192, v216\n\t"
            "v_mul_f32 v217, v193, v217\n\t"
            "v_mul_f32 v218, v194, v218\n\t"
            "v_mul_f32 v219, v195, v219\n\t"
            "v_mul_f32 v220, v192, v220\n\t"
            "v_mul_f32 v221, v193, v221\n\t"
            "v_mul_f32 v222, v194, v222\n\t"
            "v_mul_f32 v223, v195, v223\n\t"
            "v_writelane_b32 v236, s59, m0\n\t"
            "v_writelane_b32 v236, s57, 63\n\t"
            "v_xor_b32 v237, 16, v236\n\t"
            "v_writelane_b32 v248, s38, m0\n\t"
            "v_and_b32 v249, s69, v249\n\t"
            "v_cvt_pk_bf16_f32 v212, v212, v213\n\t"
            "v_cvt_pk_bf16_f32 v216, v216, v217\n\t"
            "s_waitcnt lgkmcnt(2)\n\t"
            "v_smfmac_f32_16x16x64_bf16 v[176:179], v[168:171], v[144:151], v255\n\t"
            "ds_read_b64 v[156:157], v253 offset:1752\n\t"
            "v_cvt_pk_bf16_f32 v220, v220, v221\n\t"
            "v_cvt_pk_bf16_f32 v213, v214, v215\n\t"
            "v_cvt_pk_bf16_f32 v217, v218, v219\n\t"
            "v_cvt_pk_bf16_f32 v221, v222, v223\n\t"
            "v_writelane_b32 v248, s58, 63\n\t"
            "ds_read_b64 v[158:159], v253 offset:1848\n\t"
            "ds_read_b64 v[160:161], v253 offset:16\n\t"
            "v_cvt_pk_bf16_f32 v176, v176, v177\n\t"
            "v_cvt_pk_bf16_f32 v177, v178, v179\n\t"
            "ds_read_b64 v[162:163], v253 offset:112\n\t"
            "ds_read_b64 v[164:165], v253 offset:1760\n\t"
            "ds_read_b64 v[166:167], v253 offset:1856\n\t"
            "ds_read_b128 v[168:171], v254 offset:128\n\t"
            "ds_read_b64 v[144:145], v253 offset:24\n\t"
            "ds_read_b64 v[146:147], v253 offset:120\n\t"
            "ds_read_b64 v[148:149], v253 offset:1768\n\t"
            "ds_read_b64 v[150:151], v253 offset:1864\n\t"
            "ds_write_b128 v230, v[200:203]\n\t"
            "ds_write_b128 v230, v[204:207] offset:1536\n\t"
            "s_waitcnt lgkmcnt(11)\n\t"
            "v_smfmac_f32_16x16x64_bf16 v[180:183], v[172:175], v[152:159], v255\n\t"
            "ds_read_b128 v[172:175], v254 offset:192\n\t"
            "ds_read_b64 v[152:153], v253 offset:32\n\t"
            "ds_read_b64 v[154:155], v253 offset:128\n\t"
            "ds_read_b64 v[156:157], v253 offset:1776\n\t"
            "s_waitcnt lgkmcnt(10)\n\t"
            "v_smfmac_f32_16x16x64_bf16 v[184:187], v[168:171], v[160:167], v255\n\t"
            "ds_read_b64 v[158:159], v253 offset:1872\n\t"
            "ds_read_b128 v[168:171], v254 offset:256\n\t"
            "ds_read_b64 v[160:161], v253 offset:40\n\t"
            "v_cvt_pk_bf16_f32 v178, v180, v181\n\t"
            "v_cvt_pk_bf16_f32 v179, v182, v183\n\t"
            "v_mov_b64 v[180:181], 0\n\t"
            "v_mov_b64 v[182:183], 0\n\t"
            "s_nop 0\n\t"
            "v_cvt_pk_bf16_f32 v184, v184, v185\n\t"
            "v_cvt_pk_bf16_f32 v185, v186, v187\n\t"
            "ds_read_b64 v[162:163], v253 offset:136\n\t"
            "ds_read_b64 v[164:165], v253 offset:1784\n\t"
            "s_waitcnt lgkmcnt(14)\n\t"
            "ds_read_b64 v[166:167], v253 offset:1880\n\t"
            "s_waitcnt lgkmcnt(9)\n\t"
            "v_smfmac_f32_16x16x64_bf16 v[188:191], v[172:175], v[144:151], v255\n\t"
            "ds_read_b128 v[172:175], v254 offset:320\n\t"
            "ds_read_b64 v[144:145], v253 offset:48\n\t"
            "v_mfma_f32_4x4x4_16b_bf16 v[196:199], v[176:177], v[176:177], 0\n\t"
            "v_mfma_f32_4x4x4_16b_bf16 v[192:195], v[238:239], v[176:177], 0\n\t"
            "s_nop 0\n\t"
            "v_mfma_f32_4x4x4_16b_bf16 v[196:199], v[178:179], v[178:179], v[196:199]\n\t"
            "v_mfma_f32_4x4x4_16b_bf16 v[192:195], v[238:239], v[178:179], v[192:195]\n\t"
            "ds_read_b64 v[146:147], v253 offset:144\n\t"
            "v_cvt_pk_bf16_f32 v186, v188, v189\n\t"
            "v_cvt_pk_bf16_f32 v187, v190, v191\n\t"
            "v_mov_b64 v[188:189], 0\n\t"
            "v_mov_b64 v[190:191], 0\n\t"
            "ds_read_b64 v[148:149], v253 offset:1792\n\t"
            "ds_read_b64 v[150:151], v253 offset:1888\n\t"
            "ds_write_b128 v236, v[176:179]\n\t"
            "v_mov_b64 v[176:177], 0\n\t"
            "v_mov_b64 v[178:179], 0\n\t"
            "s_waitcnt lgkmcnt(10)\n\t"
            "s_nop 0\n\t"
            "v_smfmac_f32_16x16x64_bf16 v[176:179], v[168:171], v[152:159], v255\n\t"
            "ds_read_b128 v[168:171], v254 offset:384\n\t"
            "s_waitcnt lgkmcnt(6)\n\t"
            "v_smfmac_f32_16x16x64_bf16 v[180:183], v[172:175], v[160:167], v255\n\t"
            "ds_read_b64 v[152:153], v253 offset:56\n\t"
            "ds_read_b64 v[154:155], v253 offset:152\n\t"
            "v_mfma_f32_4x4x4_16b_bf16 v[196:199], v[184:185], v[184:185], v[196:199]\n\t"
            "v_mfma_f32_4x4x4_16b_bf16 v[192:195], v[238:239], v[184:185], v[192:195]\n\t"
            "s_nop 0\n\t"
            "v_mfma_f32_4x4x4_16b_bf16 v[196:199], v[186:187], v[186:187], v[196:199]\n\t"
            "v_mfma_f32_4x4x4_16b_bf16 v[192:195], v[238:239], v[186:187], v[192:195]\n\t"
            "ds_read_b64 v[156:157], v253 offset:1800\n\t"
            "v_cvt_pk_bf16_f32 v176, v176, v177\n\t"
            "v_cvt_pk_bf16_f32 v177, v178, v179\n\t"
            "v_cvt_pk_bf16_f32 v178, v180, v181\n\t"
            "v_cvt_pk_bf16_f32 v179, v182, v183\n\t"
            "v_mov_b64 v[180:181], 0\n\t"
            "v_mov_b64 v[182:183], 0\n\t"
            "ds_read_b64 v[158:159], v253 offset:1896\n\t"
            "ds_read_b128 v[172:175], v254 offset:448\n\t"
            "ds_read_b64 v[160:161], v253 offset:64\n\t"
            "ds_read_b64 v[162:163], v253 offset:160\n\t"
            "ds_read_b64 v[164:165], v253 offset:1808\n\t"
            "ds_read_b64 v[166:167], v253 offset:1904\n\t"
            "s_waitcnt lgkmcnt(14)\n\t"
            "ds_write_b128 v237, v[184:187]\n\t"
            "v_mov_b64 v[184:185], 0\n\t"
            "v_mov_b64 v[186:187], 0\n\t"
            "s_waitcnt lgkmcnt(10)\n\t"
            "s_nop 0\n\t"
            "v_smfmac_f32_16x16x64_bf16 v[184:187], v[168:171], v[144:151], v255\n\t"
            "ds_read_b128 v[168:171], v254 offset:512\n\t"
            "ds_read_b64 v[144:145], v253 offset:72\n\t"
            "ds_read_b64 v[146:147], v253 offset:168\n\t"
            "ds_read_b64 v[148:149], v253 offset:1816\n\t"
            "ds_read_b64 v[150:151], v253 offset:1912\n\t"
            "s_waitcnt lgkmcnt(10)\n\t"
            "v_smfmac_f32_16x16x64_bf16 v[188:191], v[172:175], v[152:159], v255\n\t"
            "ds_read_b128 v[172:175], v254 offset:576\n\t"
            "ds_read_b64 v[152:153], v253 offset:80\n\t"
            "v_mfma_f32_4x4x4_16b_bf16 v[196:199], v[176:177], v[176:177], v[196:199]\n\t"
            "v_mfma_f32_4x4x4_16b_bf16 v[192:195], v[238:239], v[176:177], v[192:195]\n\t"
            "s_nop 0\n\t"
            "v_mfma_f32_4x4x4_16b_bf16 v[196:199], v[178:179], v[178:179], v[196:199]\n\t"
            "v_mfma_f32_4x4x4_16b_bf16 v[192:195], v[238:239], v[178:179], v[192:195]\n\t"
            "ds_read_b64 v[154:155], v253 offset:176\n\t"
            "v_cvt_pk_bf16_f32 v184, v184, v185\n\t"
            "v_cvt_pk_bf16_f32 v185, v186, v187\n\t"
            "v_cvt_pk_bf16_f32 v186, v188, v189\n\t"
            "v_cvt_pk_bf16_f32 v187, v190, v191\n\t"
            "v_mov_b64 v[188:189], 0\n\t"
            "v_mov_b64 v[190:191], 0\n\t"
            "ds_read_b64 v[156:157], v253 offset:1824\n\t"
            "ds_read_b64 v[158:159], v253 offset:1920\n\t"
            "s_waitcnt lgkmcnt(14)\n\t"
            "ds_write_b128 v236, v[176:179] offset:128\n\t"
            "v_mov_b64 v[176:177], 0\n\t"
            "v_mov_b64 v[178:179], 0\n\t"
            "s_waitcnt lgkmcnt(10)\n\t"
            "s_nop 0\n\t"
            "v_smfmac_f32_16x16x64_bf16 v[176:179], v[168:171], v[160:167], v255\n\t"
            "ds_read_b128 v[168:171], v254 offset:640\n\t"
            "s_waitcnt lgkmcnt(6)\n\t"
            "v_smfmac_f32_16x16x64_bf16 v[180:183], v[172:175], v[144:151], v255\n\t"
            "ds_read_b64 v[160:161], v253 offset:88\n\t"
            "ds_read_b64 v[162:163], v253 offset:184\n\t"
            "v_mfma_f32_4x4x4_16b_bf16 v[196:199], v[184:185], v[184:185], v[196:199]\n\t"
            "v_mfma_f32_4x4x4_16b_bf16 v[192:195], v[238:239], v[184:185], v[192:195]\n\t"
            "s_nop 0\n\t"
            "v_mfma_f32_4x4x4_16b_bf16 v[196:199], v[186:187], v[186:187], v[196:199]\n\t"
            "v_mfma_f32_4x4x4_16b_bf16 v[192:195], v[238:239], v[186:187], v[192:195]\n\t"
            "ds_read_b64 v[164:165], v253 offset:1832\n\t"
            "v_cvt_pk_bf16_f32 v176, v176, v177\n\t"
            "v_cvt_pk_bf16_f32 v177, v178, v179\n\t"
            "v_cvt_pk_bf16_f32 v178, v180, v181\n\t"
            "v_cvt_pk_bf16_f32 v179, v182, v183\n\t"
            "ds_read_b64 v[166:167], v253 offset:1928\n\t"
            "ds_read_b128 v[172:175], v254 offset:704\n\t"
            "ds_write_b128 v237, v[184:187] offset:128\n\t"
            "v_mov_b64 v[184:185], 0\n\t"
            "v_mov_b64 v[186:187], 0\n\t"
            "s_waitcnt lgkmcnt(6)\n\t"
            "s_nop 0\n\t"
            "v_smfmac_f32_16x16x64_bf16 v[184:187], v[168:171], v[152:159], v255\n\t"
            "ds_write_b128 v230, v[208:211] offset:3072\n\t"
            "ds_write_b128 v236, v[176:179] offset:256\n\t"
            "ds_write_b64 v231, v[212:213]\n\t"
            "ds_write_b64 v231, v[216:217] offset:1536\n\t"
            "ds_write_b64 v231, v[220:221] offset:3072\n\t"
            "s_waitcnt lgkmcnt(6)\n\t"
            "v_smfmac_f32_16x16x64_bf16 v[188:191], v[172:175], v[160:167], v255\n\t"
            "s_mov_b32 exec_hi, 0x3fffff\n\t"
            "s_add_u32 m0, s66, 0\n\t"
            "s_add_u32 s49, s79, 0\n\t"
            "buffer_load_dwordx4 v249, s[72:75], s49 offen lds\n\t"
            "s_mov_b32 exec_hi, -1\n\t"
            "v_mfma_f32_4x4x4_16b_bf16 v[196:199], v[176:177], v[176:177], v[196:199]\n\t"
            "v_mfma_f32_4x4x4_16b_bf16 v[192:195], v[238:239], v[176:177], v[192:195]\n\t"
            "s_nop 0\n\t"
            "v_mfma_f32_4x4x4_16b_bf16 v[196:199], v[178:179], v[178:179], v[196:199]\n\t"
            "v_mfma_f32_4x4x4_16b_bf16 v[192:195], v[238:239], v[178:179], v[192:195]\n\t"
            "s_mov_b32 exec_hi, 0x3fffff\n\t"
            "s_add_u32 m0, s66, 872\n\t"
            "s_add_u32 s49, s68, 196224\n\t"
            "buffer_load_dwordx4 v249, s[72:75], s49 offen lds\n\t"
            "s_mov_b32 exec_hi, -1\n\t"
            "s_mov_b32 exec_hi, 0x3fffff\n\t"
            "s_add_u32 m0, s66, 1744\n\t"
            "s_add_u32 s49, s68, 392448\n\t"
            "buffer_load_dwordx4 v249, s[72:75], s49 offen lds\n\t"
            "s_mov_b32 exec_hi, -1\n\t"
            "s_mov_b32 exec_hi, 0x3fffff\n\t"
            "s_add_u32 m0, s66, 2616\n\t"
            "s_add_u32 s49, s68, 588672\n\t"
            "buffer_load_dwordx4 v249, s[72:75], s49 offen lds\n\t"
            "s_mov_b32 exec_hi, -1\n\t"
            "s_mov_b32 exec_hi, 0x3fffff\n\t"
            "s_add_u32 m0, s66, 3488\n\t"
            "s_add_u32 s49, s68, 784896\n\t"
            "buffer_load_dwordx4 v249, s[72:75], s49 offen lds\n\t"
            "s_mov_b32 exec_hi, -1\n\t"
            "s_mov_b32 exec_hi, 0x3fffff\n\t"
            "s_add_u32 m0, s66, 4360\n\t"
            "s_add_u32 s49, s68, 981120\n\t"
            "buffer_load_dwordx4 v249, s[72:75], s49 offen lds\n\t"
            "s_mov_b32 exec_hi, -1\n\t"
            "v_cvt_pk_bf16_f32 v184, v184, v185\n\t"
            "v_cvt_pk_bf16_f32 v185, v186, v187\n\t"
            "v_cvt_pk_bf16_f32 v186, v188, v189\n\t"
            "v_cvt_pk_bf16_f32 v187, v190, v191\n\t"
            "s_nop 1\n\t"
            "v_mfma_f32_4x4x4_16b_bf16 v[196:199], v[184:185], v[184:185], v[196:199]\n\t"
            "v_mfma_f32_4x4x4_16b_bf16 v[192:195], v[238:239], v[184:185], v[192:195]\n\t"
            "s_nop 0\n\t"
            "v_mfma_f32_4x4x4_16b_bf16 v[196:199], v[186:187], v[186:187], v[196:199]\n\t"
            "v_mfma_f32_4x4x4_16b_bf16 v[192:195], v[238:239], v[186:187], v[192:195]\n\t"
            "s_mov_b32 exec_hi, 0x3fffff\n\t"
            "s_add_u32 m0, s66, 5232\n\t"
            "s_add_u32 s49, s68, 1177344\n\t"
            "buffer_load_dwordx4 v249, s[72:75], s49 offen lds\n\t"
            "s_mov_b32 exec_hi, -1\n\t"
            "s_mov_b32 exec_hi, 0x3fffff\n\t"
            "s_add_u32 m0, s66, 6104\n\t"
            "s_add_u32 s49, s68, 1373568\n\t"
            "buffer_load_dwordx4 v249, s[72:75], s49 offen lds\n\t"
            "s_mov_b32 exec_hi, -1\n\t"
            "v_cndmask_b32_e64 v193, v196, v197, s[60:61]\n\t"
            "v_cndmask_b32_e64 v193, v193, v198, s[62:63]\n\t"
            "v_cndmask_b32_e64 v193, v193, v199, s[64:65]\n\t"
            "ds_write_b128 v237, v[184:187] offset:256\n\t"
            "ds_write_b64 v248, v[192:193] offset:18816\n\t"
            "s_mov_b32 exec_hi, 0x3fffff\n\t"
            "s_add_u32 m0, s66, 6976\n\t"
            "s_add_u32 s49, s68, 1569792\n\t"
            "buffer_load_dwordx4 v249, s[72:75], s49 offen lds\n\t"
            "s_mov_b32 exec_hi, -1\n\t"
            "s_waitcnt lgkmcnt(0)\n\t"
            "s_nop 7\n\t"
            "v_mov_b32 v144, s44\n\t"
            "v_mov_b32 v145, -1\n\t"
            "ds_write_b32 v144, v145\n\t"
            "s_cmp_ge_u32 s33, 221184\n\t"
            "s_cbranch_scc1 .Lrp%=\n\t"
            "s_barrier\n\t"
            "s_waitcnt vmcnt(9)\n\t"
            "s_nop 7\n\t"
            "v_add_u32 v232, s67, v242\n\t"
            "v_xor_b32 v233, 64, v232\n\t"
            "v_add_u32 v234, s67, v247\n\t"
            "ds_read_b128 v[144:147], v232\n\t"
            "ds_read_b128 v[148:151], v233\n\t"
            "ds_read_b128 v[152:155], v232 offset:128\n\t"
            "ds_read_b128 v[156:159], v233 offset:128\n\t"
            "ds_read_b128 v[160:163], v232 offset:256\n\t"
            "ds_read_b128 v[164:167], v233 offset:256\n\t"
            "ds_read_b128 v[176:179], v234 offset:18816\n\t"
            "ds_read_b128 v[180:183], v234 offset:19840\n\t"
            "s_waitcnt lgkmcnt(7)\n\t"
            "v_mfma_f32_16x16x32_bf16 v[200:203], v[144:147], v[0:3], 0\n\t"
            "s_waitcnt lgkmcnt(6)\n\t"
            "v_mfma_f32_16x16x32_bf16 v[200:203], v[148:151], v[4:7], v[200:203]\n\t"
            "s_waitcnt lgkmcnt(5)\n\t"
            "v_mfma_f32_16x16x32_bf16 v[200:203], v[152:155], v[8:11], v[200:203]\n\t"
            "s_waitcnt lgkmcnt(4)\n\t"
            "v_mfma_f32_16x16x32_bf16 v[200:203], v[156:159], v[12:15], v[200:203]\n\t"
            "s_waitcnt lgkmcnt(1)\n\t"
            "v_add_f32 v176, v176, v178\n\t"
            "s_waitcnt lgkmcnt(0)\n\t"
            "v_add_f32 v180, v180, v182\n\t"
            "v_mfma_f32_16x16x32_bf16 v[200:203], v[160:163], v[16:19], v[200:203]\n\t"
            "v_add_f32 v176, v176, v180\n\t"
            "v_add_f32 v177, v177, v179\n\t"
            "v_mfma_f32_16x16x32_bf16 v[200:203], v[164:167], v[20:23], v[200:203]\n\t"
            "v_add_f32 v181, v181, v183\n\t"
            "v_mul_f32 v176, 0x3baaaaab, v176\n\t"
            "v_mfma_f32_16x16x32_bf16 v[204:207], v[144:147], v[24:27], 0\n\t"
            "v_add_f32 v177, v177, v181\n\t"
            "v_mul_f32_e64 v176, v176, -v176\n\t"
            "v_mfma_f32_16x16x32_bf16 v[204:207], v[148:151], v[28:31], v[204:207]\n\t"
            "v_fmac_f32 v176, 0x3baaaaab, v177\n\t"
            "v_max_f32 v176, 0, v176\n\t"
            "v_mfma_f32_16x16x32_bf16 v[204:207], v[152:155], v[32:35], v[204:207]\n\t"
            "v_add_f32 v176, 0x3727c5ac, v176\n\t"
            "v_rsq_f32 v176, v176\n\t"
            "v_mfma_f32_16x16x32_bf16 v[204:207], v[156:159], v[36:39], v[204:207]\n\t"
            "ds_write_b32 v246, v176\n\t"
            "v_mfma_f32_16x16x32_bf16 v[204:207], v[160:163], v[40:43], v[204:207]\n\t"
            "ds_read_b128 v[228:231], v245\n\t"
            "v_mfma_f32_16x16x32_bf16 v[204:207], v[164:167], v[44:47], v[204:207]\n\t"
            "v_mfma_f32_16x16x32_bf16 v[208:211], v[144:147], v[48:51], 0\n\t"
            "v_mfma_f32_16x16x32_bf16 v[208:211], v[148:151], v[52:55], v[208:211]\n\t"
            "v_mfma_f32_16x16x32_bf16 v[208:211], v[152:155], v[56:59], v[208:211]\n\t"
            "v_mfma_f32_16x16x32_bf16 v[208:211], v[156:159], v[60:63], v[208:211]\n\t"
            "v_mfma_f32_16x16x32_bf16 v[208:211], v[160:163], v[64:67], v[208:211]\n\t"
            "v_mfma_f32_16x16x32_bf16 v[208:211], v[164:167], v[68:71], v[208:211]\n\t"
            "v_mfma_f32_16x16x32_bf16 v[212:215], v[144:147], v[72:75], 0\n\t"
            "s_waitcnt lgkmcnt(0)\n\t"
            "v_mul_f32 v200, v228, v200\n\t"
            "v_mul_f32 v201, v229, v201\n\t"
            "v_mfma_f32_16x16x32_bf16 v[212:215], v[148:151], v[76:79], v[212:215]\n\t"
            "v_mul_f32 v204, v228, v204\n\t"
            "v_mul_f32 v205, v229, v205\n\t"
            "v_mfma_f32_16x16x32_bf16 v[212:215], v[152:155], v[80:83], v[212:215]\n\t"
            "v_mul_f32 v208, v228, v208\n\t"
            "v_mul_f32 v209, v229, v209\n\t"
            "v_mfma_f32_16x16x32_bf16 v[212:215], v[156:159], v[84:87], v[212:215]\n\t"
            "v_mul_f32 v202, v230, v202\n\t"
            "v_mul_f32 v203, v231, v203\n\t"
            "v_mfma_f32_16x16x32_bf16 v[212:215], v[160:163], v[88:91], v[212:215]\n\t"
            "v_cvt_pk_bf16_f32 v200, v200, v201\n\t"
            "v_mul_f32 v206, v230, v206\n\t"
            "v_mfma_f32_16x16x32_bf16 v[212:215], v[164:167], v[92:95], v[212:215]\n\t"
            "v_mul_f32 v207, v231, v207\n\t"
            "v_cvt_pk_bf16_f32 v204, v204, v205\n\t"
            "v_mfma_f32_16x16x32_bf16 v[216:219], v[144:147], v[96:99], 0\n\t"
            "v_mul_f32 v210, v230, v210\n\t"
            "v_mul_f32 v211, v231, v211\n\t"
            "v_mfma_f32_16x16x32_bf16 v[216:219], v[148:151], v[100:103], v[216:219]\n\t"
            "v_cvt_pk_bf16_f32 v208, v208, v209\n\t"
            "v_cvt_pk_bf16_f32 v201, v202, v203\n\t"
            "v_mfma_f32_16x16x32_bf16 v[216:219], v[152:155], v[104:107], v[216:219]\n\t"
            "v_mul_f32 v212, v228, v212\n\t"
            "v_mul_f32 v213, v229, v213\n\t"
            "v_mfma_f32_16x16x32_bf16 v[216:219], v[156:159], v[108:111], v[216:219]\n\t"
            "v_mul_f32 v214, v230, v214\n\t"
            "v_mul_f32 v215, v231, v215\n\t"
            "v_mfma_f32_16x16x32_bf16 v[216:219], v[160:163], v[112:115], v[216:219]\n\t"
            "v_cvt_pk_bf16_f32 v212, v212, v213\n\t"
            "v_cvt_pk_bf16_f32 v205, v206, v207\n\t"
            "v_mfma_f32_16x16x32_bf16 v[216:219], v[164:167], v[116:119], v[216:219]\n\t"
            "v_cvt_pk_bf16_f32 v209, v210, v211\n\t"
            "v_cvt_pk_bf16_f32 v213, v214, v215\n\t"
            "v_mfma_f32_16x16x32_bf16 v[220:223], v[144:147], v[120:123], 0\n\t"
            "v_mfma_f32_16x16x32_bf16 v[220:223], v[148:151], v[124:127], v[220:223]\n\t"
            "v_mfma_f32_16x16x32_bf16 v[220:223], v[152:155], v[128:131], v[220:223]\n\t"
            "v_mfma_f32_16x16x32_bf16 v[220:223], v[156:159], v[132:135], v[220:223]\n\t"
            "s_nop 1\n\t"
            "v_mul_f32 v216, v228, v216\n\t"
            "v_mfma_f32_16x16x32_bf16 v[220:223], v[160:163], v[136:139], v[220:223]\n\t"
            "v_mul_f32 v217, v229, v217\n\t"
            "v_mul_f32 v218, v230, v218\n\t"
            "v_mfma_f32_16x16x32_bf16 v[220:223], v[164:167], v[140:143], v[220:223]\n\t"
            "v_mul_f32 v219, v231, v219\n\t"
            "v_cvt_pk_bf16_f32 v216, v216, v217\n\t"
            "v_cvt_pk_bf16_f32 v217, v218, v219\n\t"
            "s_nop 4\n\t"
            "v_mul_f32 v220, v228, v220\n\t"
            "v_mul_f32 v221, v229, v221\n\t"
            "v_mul_f32 v222, v230, v222\n\t"
            "v_mul_f32 v223, v231, v223\n\t"
            "v_cvt_pk_bf16_f32 v220, v220, v221\n\t"
            "v_cvt_pk_bf16_f32 v221, v222, v223\n\t"
            "s_nop 7\n\t"
            "s_add_u32 s45, s46, s27\n\t"
            "s_mov_b32 s46, s47\n\t"
            "s_mov_b32 s47, s68\n\t"
            "s_xor_b32 s48, s48, s71\n\t"
            "s_xor_b32 s67, s67, s71\n\t"
            "s_add_u32 s76, s76, 4\n\t"
            "s_lshl_b32 s56, s56, 2\n\t"
            "s_lshr_b32 s49, s26, 30\n\t"
            "s_or_b32 s56, s56, s49\n\t"
            "ds_read_b128 v[168:171], v254\n\t"
            "ds_read_b128 v[172:175], v254 offset:64\n\t"
            "s_waitcnt vmcnt(0)\n\t"
            "ds_read_b64 v[144:145], v253\n\t"
            "ds_read_b64 v[146:147], v253 offset:96\n\t"
            "ds_read_b64 v[148:149], v253 offset:1744\n\t"
            "ds_read_b64 v[150:151], v253 offset:1840\n\t"
            "ds_read_b64 v[152:153], v253 offset:8\n\t"
            "ds_read_b64 v[154:155], v253 offset:104\n\t"
            "ds_read_b64 v[156:157], v253 offset:1752\n\t"
            "ds_read_b64 v[158:159], v253 offset:1848\n\t"
            "ds_read_b64 v[160:161], v253 offset:16\n\t"
            "ds_read_b64 v[162:163], v253 offset:112\n\t"
            "ds_read_b64 v[164:165], v253 offset:1760\n\t"
            "ds_read_b64 v[166:167], v253 offset:1856\n\t"
            "s_waitcnt lgkmcnt(0)\n\t"
            "s_nop 7\n\t"
            ".p2align 6\n\t"
            ".Lwc_loop%=:\n\t"
            "s_setprio 3\n\t"
            "ds_read_b128 v[228:231], v245 offset:64\n\t"
            "v_mov_b64 v[176:177], 0\n\t"
            "v_mov_b64 v[178:179], 0\n\t"
            "v_mov_b32 v249, s76\n\t"
            "v_mov_b64 v[180:181], 0\n\t"
            "s_waitcnt lgkmcnt(1)\n\t"
            "v_smfmac_f32_16x16x64_bf16 v[176:179], v[168:171], v[144:151], v255\n\t"
            "s_waitcnt lgkmcnt(0)\n\t"
            "s_barrier\n\t"
            "ds_read_b32 v249, v249\n\t"
            "v_mov_b64 v[182:183], 0\n\t"
            "v_mov_b64 v[184:185], 0\n\t"
            "v_mov_b64 v[186:187], 0\n\t"
            "v_smfmac_f32_16x16x64_bf16 v[180:183], v[172:175], v[152:159], v255\n\t"
            "ds_read_b64 v[144:145], v253 offset:24\n\t"
            "v_mov_b64 v[188:189], 0\n\t"
            "v_mov_b64 v[190:191], 0\n\t"
            "v_add_u32 v236, s67, v250\n\t"
            "v_cvt_pk_bf16_f32 v176, v176, v177\n\t"
            "v_cvt_pk_bf16_f32 v177, v178, v179\n\t"
            "v_add_u32 v232, s48, v242\n\t"
            "v_xor_b32 v233, 64, v232\n\t"
            "v_cvt_pk_bf16_f32 v178, v180, v181\n\t"
            "v_cvt_pk_bf16_f32 v179, v182, v183\n\t"
            "v_mov_b64 v[180:181], 0\n\t"
            "v_mov_b64 v[182:183], 0\n\t"
            "v_add_u32 v248, s67, v251\n\t"
            "v_add_u32 v234, s67, v247\n\t"
            "ds_read_b128 v[168:171], v254 offset:128\n\t"
            "ds_read_b64 v[150:151], v253 offset:1864\n\t"
            "ds_read_b64 v[146:147], v253 offset:120\n\t"
            "ds_read_b128 v[172:175], v254 offset:192\n\t"
            "ds_read_b64 v[148:149], v253 offset:1768\n\t"
            "s_waitcnt lgkmcnt(6)\n\t"
            "v_readfirstlane_b32 s26, v249\n\t"
            "ds_read_b64 v[154:155], v253 offset:128\n\t"
            "s_and_b32 s68, s26, 0x7ffffff\n\t"
            "s_add_u32 s68, s68, s78\n\t"
            "s_and_b32 s69, s26, 0x38000000\n\t"
            "s_or_b32 s69, s69, s39\n\t"
            "s_bitcmp1_b32 s26, 27\n\t"
            "s_cselect_b32 s79, 2.0, s68\n\t"
            "ds_read_b64 v[156:157], v253 offset:1776\n\t"
            "s_and_b32 s57, s56, 3\n\t"
            "s_lshl_b32 s58, s57, 3\n\t"
            "s_lshr_b32 m0, s54, s58\n\t"
            "ds_read_b64 v[158:159], v253 offset:1872\n\t"
            "s_lshl_b32 s58, s57, 4\n\t"
            "s_lshr_b64 s[36:37], s[50:51], s58\n\t"
            "s_and_b32 s57, s36, 0xffff\n\t"
            "s_add_u32 s57, s57, s67\n\t"
            "ds_read_b64 v[152:153], v253 offset:32\n\t"
            "s_lshr_b64 s[36:37], s[52:53], s58\n\t"
            "s_and_b32 s58, s36, 0xffff\n\t"
            "s_add_u32 s58, s58, s67\n\t"
            "s_waitcnt lgkmcnt(8)\n\t"
            "v_smfmac_f32_16x16x64_bf16 v[184:187], v[168:171], v[160:167], v255\n\t"
            "s_and_b32 s59, s50, 0xffff\n\t"
            "s_add_u32 s59, s59, s67\n\t"
            "s_and_b32 s38, s52, 0xffff\n\t"
            "s_add_u32 s38, s38, s67\n\t"
            "ds_read_b128 v[168:171], v254 offset:256\n\t"
            "v_writelane_b32 v236, s59, m0\n\t"
            "s_waitcnt lgkmcnt(5)\n\t"
            "v_smfmac_f32_16x16x64_bf16 v[188:191], v[172:175], v[144:151], v255\n\t"
            "ds_read_b64 v[162:163], v253 offset:136\n\t"
            "v_writelane_b32 v236, s57, 63\n\t"
            "v_cvt_pk_bf16_f32 v184, v184, v185\n\t"
            "v_mfma_f32_4x4x4_16b_bf16 v[196:199], v[176:177], v[176:177], 0\n\t"
            "v_mfma_f32_4x4x4_16b_bf16 v[192:195], v[238:239], v[176:177], 0\n\t"
            "s_nop 0\n\t"
            "v_mfma_f32_4x4x4_16b_bf16 v[196:199], v[178:179], v[178:179], v[196:199]\n\t"
            "v_mfma_f32_4x4x4_16b_bf16 v[192:195], v[238:239], v[178:179], v[192:195]\n\t"
            "ds_read_b128 v[172:175], v254 offset:320\n\t"
            "v_cvt_pk_bf16_f32 v185, v186, v187\n\t"
            "v_cvt_pk_bf16_f32 v187, v190, v191\n\t"
            "v_cvt_pk_bf16_f32 v186, v188, v189\n\t"
            "v_xor_b32 v237, 16, v236\n\t"
            "v_mov_b64 v[190:191], 0\n\t"
            "v_mov_b64 v[188:189], 0\n\t"
            "v_writelane_b32 v248, s38, m0\n\t"
            "v_writelane_b32 v248, s58, 63\n\t"
            "ds_read_b64 v[160:161], v253 offset:40\n\t"
            "ds_read_b64 v[164:165], v253 offset:1784\n\t"
            "ds_read_b64 v[166:167], v253 offset:1880\n\t"
            "ds_read_b64 v[146:147], v253 offset:144\n\t"
            "ds_read_b64 v[148:149], v253 offset:1792\n\t"
            "ds_read_b64 v[150:151], v253 offset:1888\n\t"
            "ds_read_b64 v[144:145], v253 offset:48\n\t"
            "ds_read_b32 v249, v252 offset:1024\n\t"
            "s_waitcnt lgkmcnt(14)\n\t"
            "ds_write_b128 v236, v[176:179]\n\t"
            "v_mov_b64 v[178:179], 0\n\t"
            "v_mov_b64 v[176:177], 0\n\t"
            "s_waitcnt lgkmcnt(11)\n\t"
            "s_nop 0\n\t"
            "v_smfmac_f32_16x16x64_bf16 v[176:179], v[168:171], v[152:159], v255\n\t"
            "ds_read_b64 v[152:153], v253 offset:56\n\t"
            "ds_read_b64 v[156:157], v253 offset:1800\n\t"
            "ds_read_b128 v[168:171], v254 offset:384\n\t"
            "ds_read_b64 v[154:155], v253 offset:152\n\t"
            "s_waitcnt lgkmcnt(10)\n\t"
            "v_smfmac_f32_16x16x64_bf16 v[180:183], v[172:175], v[160:167], v255\n\t"
            "ds_read_b128 v[172:175], v254 offset:448\n\t"
            "ds_read_b64 v[158:159], v253 offset:1896\n\t"
            "v_mfma_f32_4x4x4_16b_bf16 v[196:199], v[184:185], v[184:185], v[196:199]\n\t"
            "v_mfma_f32_4x4x4_16b_bf16 v[192:195], v[238:239], v[184:185], v[192:195]\n\t"
            "s_nop 0\n\t"
            "v_mfma_f32_4x4x4_16b_bf16 v[196:199], v[186:187], v[186:187], v[196:199]\n\t"
            "v_mfma_f32_4x4x4_16b_bf16 v[192:195], v[238:239], v[186:187], v[192:195]\n\t"
            "ds_read_b64 v[160:161], v253 offset:64\n\t"
            "v_cvt_pk_bf16_f32 v176, v176, v177\n\t"
            "v_cvt_pk_bf16_f32 v177, v178, v179\n\t"
            "v_cvt_pk_bf16_f32 v178, v180, v181\n\t"
            "v_cvt_pk_bf16_f32 v179, v182, v183\n\t"
            "v_mov_b64 v[182:183], 0\n\t"
            "v_mov_b64 v[180:181], 0\n\t"
            "s_waitcnt lgkmcnt(8)\n\t"
            "v_and_b32 v249, s69, v249\n\t"
            "ds_read_b64 v[166:167], v253 offset:1904\n\t"
            "ds_read_b64 v[164:165], v253 offset:1808\n\t"
            "ds_read_b64 v[162:163], v253 offset:160\n\t"
            "ds_write_b128 v237, v[184:187]\n\t"
            "v_mov_b64 v[184:185], 0\n\t"
            "v_mov_b64 v[186:187], 0\n\t"
            "s_waitcnt lgkmcnt(8)\n\t"
            "s_nop 0\n\t"
            "v_smfmac_f32_16x16x64_bf16 v[184:187], v[168:171], v[144:151], v255\n\t"
            "ds_read_b64 v[148:149], v253 offset:1816\n\t"
            "s_waitcnt lgkmcnt(6)\n\t"
            "v_smfmac_f32_16x16x64_bf16 v[188:191], v[172:175], v[152:159], v255\n\t"
            "ds_read_b128 v[168:171], v254 offset:512\n\t"
            "ds_read_b64 v[146:147], v253 offset:168\n\t"
            "v_mfma_f32_4x4x4_16b_bf16 v[196:199], v[176:177], v[176:177], v[196:199]\n\t"
            "v_mfma_f32_4x4x4_16b_bf16 v[192:195], v[238:239], v[176:177], v[192:195]\n\t"
            "s_nop 0\n\t"
            "v_mfma_f32_4x4x4_16b_bf16 v[196:199], v[178:179], v[178:179], v[196:199]\n\t"
            "v_mfma_f32_4x4x4_16b_bf16 v[192:195], v[238:239], v[178:179], v[192:195]\n\t"
            "ds_read_b64 v[144:145], v253 offset:72\n\t"
            "v_cvt_pk_bf16_f32 v184, v184, v185\n\t"
            "v_cvt_pk_bf16_f32 v185, v186, v187\n\t"
            "v_cvt_pk_bf16_f32 v187, v190, v191\n\t"
            "v_cvt_pk_bf16_f32 v186, v188, v189\n\t"
            "v_mov_b64 v[188:189], 0\n\t"
            "v_mov_b64 v[190:191], 0\n\t"
            "ds_read_b64 v[150:151], v253 offset:1912\n\t"
            "ds_read_b128 v[172:175], v254 offset:576\n\t"
            "ds_read_b64 v[156:157], v253 offset:1824\n\t"
            "ds_read_b64 v[154:155], v253 offset:176\n\t"
            "ds_read_b64 v[158:159], v253 offset:1920\n\t"
            "ds_read_b64 v[152:153], v253 offset:80\n\t"
            "s_waitcnt lgkmcnt(14)\n\t"
            "ds_write_b128 v236, v[176:179] offset:128\n\t"
            "v_mov_b64 v[178:179], 0\n\t"
            "v_mov_b64 v[176:177], 0\n\t"
            "s_waitcnt lgkmcnt(9)\n\t"
            "s_nop 0\n\t"
            "v_smfmac_f32_16x16x64_bf16 v[176:179], v[168:171], v[160:167], v255\n\t"
            "ds_read_b128 v[168:171], v254 offset:640\n\t"
            "ds_read_b64 v[164:165], v253 offset:1832\n\t"
            "ds_read_b64 v[160:161], v253 offset:88\n\t"
            "ds_read_b64 v[162:163], v253 offset:184\n\t"
            "ds_read_b64 v[166:167], v253 offset:1928\n\t"
            "s_waitcnt lgkmcnt(10)\n\t"
            "v_smfmac_f32_16x16x64_bf16 v[180:183], v[172:175], v[144:151], v255\n\t"
            "ds_read_b128 v[172:175], v254 offset:704\n\t"
            "ds_read_b128 v[144:147], v232 offset:6144\n\t"
            "v_mfma_f32_4x4x4_16b_bf16 v[196:199], v[184:185], v[184:185], v[196:199]\n\t"
            "v_mfma_f32_4x4x4_16b_bf16 v[192:195], v[238:239], v[184:185], v[192:195]\n\t"
            "s_nop 0\n\t"
            "v_mfma_f32_4x4x4_16b_bf16 v[196:199], v[186:187], v[186:187], v[196:199]\n\t"
            "v_mfma_f32_4x4x4_16b_bf16 v[192:195], v[238:239], v[186:187], v[192:195]\n\t"
            "ds_read_b128 v[148:151], v233 offset:6144\n\t"
            "v_cvt_pk_bf16_f32 v176, v176, v177\n\t"
            "v_cvt_pk_bf16_f32 v177, v178, v179\n\t"
            "v_cvt_pk_bf16_f32 v178, v180, v181\n\t"
            "v_cvt_pk_bf16_f32 v179, v182, v183\n\t"
            "ds_read_b128 v[180:183], v233 offset:12288\n\t"
            "ds_write_b128 v237, v[184:187] offset:128\n\t"
            "v_mov_b64 v[184:185], 0\n\t"
            "v_mov_b64 v[186:187], 0\n\t"
            "s_waitcnt lgkmcnt(9)\n\t"
            "s_nop 0\n\t"
            "v_smfmac_f32_16x16x64_bf16 v[184:187], v[168:171], v[152:159], v255\n\t"
            "ds_read_b128 v[152:155], v232 offset:6272\n\t"
            "s_waitcnt lgkmcnt(5)\n\t"
            "v_smfmac_f32_16x16x64_bf16 v[188:191], v[172:175], v[160:167], v255\n\t"
            "ds_read_b128 v[156:159], v233 offset:6272\n\t"
            "ds_read_b128 v[164:167], v233 offset:6400\n\t"
            "v_mfma_f32_4x4x4_16b_bf16 v[196:199], v[176:177], v[176:177], v[196:199]\n\t"
            "v_mfma_f32_4x4x4_16b_bf16 v[192:195], v[238:239], v[176:177], v[192:195]\n\t"
            "s_nop 0\n\t"
            "v_mfma_f32_4x4x4_16b_bf16 v[196:199], v[178:179], v[178:179], v[196:199]\n\t"
            "v_mfma_f32_4x4x4_16b_bf16 v[192:195], v[238:239], v[178:179], v[192:195]\n\t"
            "ds_read_b128 v[160:163], v232 offset:6400\n\t"
            "v_cvt_pk_bf16_f32 v184, v184, v185\n\t"
            "v_cvt_pk_bf16_f32 v185, v186, v187\n\t"
            "v_cvt_pk_bf16_f32 v186, v188, v189\n\t"
            "v_cvt_pk_bf16_f32 v187, v190, v191\n\t"
            "s_nop 1\n\t"
            "v_mfma_f32_4x4x4_16b_bf16 v[196:199], v[184:185], v[184:185], v[196:199]\n\t"
            "v_mfma_f32_4x4x4_16b_bf16 v[192:195], v[238:239], v[184:185], v[192:195]\n\t"
            "s_nop 0\n\t"
            "v_mfma_f32_4x4x4_16b_bf16 v[196:199], v[186:187], v[186:187], v[196:199]\n\t"
            "v_mfma_f32_4x4x4_16b_bf16 v[192:195], v[238:239], v[186:187], v[192:195]\n\t"
            "ds_read_b128 v[188:191], v233 offset:12416\n\t"
            "s_waitcnt lgkmcnt(8)\n\t"
            "v_mfma_f32_16x16x32_bf16 v[224:227], v[144:147], v[0:3], 0\n\t"
            "s_setprio 3\n\t"
            "ds_write_b128 v236, v[176:179] offset:256\n\t"
            "s_waitcnt lgkmcnt(8)\n\t"
            "v_mfma_f32_16x16x32_bf16 v[224:227], v[148:151], v[4:7], v[224:227]\n\t"
            "ds_read_b128 v[176:179], v232 offset:12288\n\t"
            "s_waitcnt lgkmcnt(6)\n\t"
            "v_mfma_f32_16x16x32_bf16 v[224:227], v[152:155], v[8:11], v[224:227]\n\t"
            "ds_write_b128 v237, v[184:187] offset:256\n\t"
            "s_waitcnt lgkmcnt(6)\n\t"
            "v_mfma_f32_16x16x32_bf16 v[224:227], v[156:159], v[12:15], v[224:227]\n\t"
            "ds_read_b128 v[184:187], v232 offset:12416\n\t"
            "v_cndmask_b32_e64 v193, v196, v197, s[60:61]\n\t"
            "s_waitcnt lgkmcnt(5)\n\t"
            "v_mfma_f32_16x16x32_bf16 v[224:227], v[160:163], v[16:19], v[224:227]\n\t"
            "v_cndmask_b32_e64 v193, v193, v198, s[62:63]\n\t"
            "v_cndmask_b32_e64 v193, v193, v199, s[64:65]\n\t"
            "v_mfma_f32_16x16x32_bf16 v[224:227], v[164:167], v[20:23], v[224:227]\n\t"
            "ds_read_b128 v[196:199], v233 offset:12544\n\t"
            "v_mfma_f32_16x16x32_bf16 v[168:171], v[144:147], v[24:27], 0\n\t"
            "ds_write_b64 v248, v[192:193] offset:18816\n\t"
            "v_mfma_f32_16x16x32_bf16 v[168:171], v[148:151], v[28:31], v[168:171]\n\t"
            "ds_read_b128 v[192:195], v232 offset:12544\n\t"
            "v_add_u32 v232, s67, v242\n\t"
            "v_mfma_f32_16x16x32_bf16 v[168:171], v[152:155], v[32:35], v[168:171]\n\t"
            "s_nop 0\n\t"
            "v_mul_f32 v227, v231, v227\n\t"
            "v_mul_f32 v226, v230, v226\n\t"
            "v_mfma_f32_16x16x32_bf16 v[168:171], v[156:159], v[36:39], v[168:171]\n\t"
            "v_cvt_pk_bf16_f32 v203, v226, v227\n\t"
            "v_mul_f32 v224, v228, v224\n\t"
            "v_mfma_f32_16x16x32_bf16 v[168:171], v[160:163], v[40:43], v[168:171]\n\t"
            "v_mul_f32 v225, v229, v225\n\t"
            "v_cvt_pk_bf16_f32 v202, v224, v225\n\t"
            "v_mfma_f32_16x16x32_bf16 v[168:171], v[164:167], v[44:47], v[168:171]\n\t"
            "s_mov_b32 exec_hi, 0x3fffff\n\t"
            "s_add_u32 m0, s66, 0\n\t"
            "s_add_u32 s49, s79, 0\n\t"
            "buffer_load_dwordx4 v249, s[72:75], s49 offen lds\n\t"
            "s_mov_b32 exec_hi, -1\n\t"
            "v_mfma_f32_16x16x32_bf16 v[172:175], v[144:147], v[48:51], 0\n\t"
            "s_mov_b32 exec_hi, 0x3fffff\n\t"
            "s_add_u32 m0, s66, 872\n\t"
            "s_add_u32 s49, s68, 196224\n\t"
            "buffer_load_dwordx4 v249, s[72:75], s49 offen lds\n\t"
            "s_mov_b32 exec_hi, -1\n\t"
            "v_mfma_f32_16x16x32_bf16 v[172:175], v[148:151], v[52:55], v[172:175]\n\t"
            "v_mul_f32 v170, v230, v170\n\t"
            "v_mul_f32 v169, v229, v169\n\t"
            "v_mfma_f32_16x16x32_bf16 v[172:175], v[152:155], v[56:59], v[172:175]\n\t"
            "v_mul_f32 v168, v228, v168\n\t"
            "v_cvt_pk_bf16_f32 v206, v168, v169\n\t"
            "v_mfma_f32_16x16x32_bf16 v[172:175], v[156:159], v[60:63], v[172:175]\n\t"
            "v_mul_f32 v171, v231, v171\n\t"
            "v_cvt_pk_bf16_f32 v207, v170, v171\n\t"
            "v_mfma_f32_16x16x32_bf16 v[172:175], v[160:163], v[64:67], v[172:175]\n\t"
            "s_mov_b32 exec_hi, 0x3fffff\n\t"
            "s_add_u32 m0, s66, 1744\n\t"
            "s_add_u32 s49, s68, 392448\n\t"
            "buffer_load_dwordx4 v249, s[72:75], s49 offen lds\n\t"
            "s_mov_b32 exec_hi, -1\n\t"
            "v_mfma_f32_16x16x32_bf16 v[172:175], v[164:167], v[68:71], v[172:175]\n\t"
            "s_mov_b32 exec_hi, 0x3fffff\n\t"
            "s_add_u32 m0, s66, 2616\n\t"
            "s_add_u32 s49, s68, 588672\n\t"
            "buffer_load_dwordx4 v249, s[72:75], s49 offen lds\n\t"
            "s_mov_b32 exec_hi, -1\n\t"
            "v_mfma_f32_16x16x32_bf16 v[224:227], v[144:147], v[72:75], 0\n\t"
            "s_mov_b32 exec_hi, 0x3fffff\n\t"
            "s_add_u32 m0, s66, 3488\n\t"
            "s_add_u32 s49, s68, 784896\n\t"
            "buffer_load_dwordx4 v249, s[72:75], s49 offen lds\n\t"
            "s_mov_b32 exec_hi, -1\n\t"
            "v_mfma_f32_16x16x32_bf16 v[224:227], v[148:151], v[76:79], v[224:227]\n\t"
            "v_mul_f32 v174, v230, v174\n\t"
            "v_mul_f32 v173, v229, v173\n\t"
            "v_mfma_f32_16x16x32_bf16 v[224:227], v[152:155], v[80:83], v[224:227]\n\t"
            "v_mul_f32 v175, v231, v175\n\t"
            "v_cvt_pk_bf16_f32 v211, v174, v175\n\t"
            "v_mfma_f32_16x16x32_bf16 v[224:227], v[156:159], v[84:87], v[224:227]\n\t"
            "v_mul_f32 v172, v228, v172\n\t"
            "v_cvt_pk_bf16_f32 v210, v172, v173\n\t"
            "v_mfma_f32_16x16x32_bf16 v[224:227], v[160:163], v[88:91], v[224:227]\n\t"
            "s_mov_b32 exec_hi, 0x3fffff\n\t"
            "s_add_u32 m0, s66, 4360\n\t"
            "s_add_u32 s49, s68, 981120\n\t"
            "buffer_load_dwordx4 v249, s[72:75], s49 offen lds\n\t"
            "s_mov_b32 exec_hi, -1\n\t"
            "v_mfma_f32_16x16x32_bf16 v[224:227], v[164:167], v[92:95], v[224:227]\n\t"
            "s_mov_b32 exec_hi, 0x3fffff\n\t"
            "s_add_u32 m0, s66, 5232\n\t"
            "s_add_u32 s49, s68, 1177344\n\t"
            "buffer_load_dwordx4 v249, s[72:75], s49 offen lds\n\t"
            "s_mov_b32 exec_hi, -1\n\t"
            "v_mfma_f32_16x16x32_bf16 v[168:171], v[144:147], v[96:99], 0\n\t"
            "s_mov_b32 exec_hi, 0x3fffff\n\t"
            "s_add_u32 m0, s66, 6104\n\t"
            "s_add_u32 s49, s68, 1373568\n\t"
            "buffer_load_dwordx4 v249, s[72:75], s49 offen lds\n\t"
            "s_mov_b32 exec_hi, -1\n\t"
            "v_mfma_f32_16x16x32_bf16 v[168:171], v[148:151], v[100:103], v[168:171]\n\t"
            "v_mul_f32 v226, v230, v226\n\t"
            "v_mul_f32 v227, v231, v227\n\t"
            "v_mfma_f32_16x16x32_bf16 v[168:171], v[152:155], v[104:107], v[168:171]\n\t"
            "v_cvt_pk_bf16_f32 v215, v226, v227\n\t"
            "v_mul_f32 v224, v228, v224\n\t"
            "v_mfma_f32_16x16x32_bf16 v[168:171], v[156:159], v[108:111], v[168:171]\n\t"
            "v_mul_f32 v225, v229, v225\n\t"
            "v_cvt_pk_bf16_f32 v214, v224, v225\n\t"
            "v_mfma_f32_16x16x32_bf16 v[168:171], v[160:163], v[112:115], v[168:171]\n\t"
            "s_mov_b32 exec_hi, 0x3fffff\n\t"
            "s_add_u32 m0, s66, 6976\n\t"
            "s_add_u32 s49, s68, 1569792\n\t"
            "buffer_load_dwordx4 v249, s[72:75], s49 offen lds\n\t"
            "s_mov_b32 exec_hi, -1\n\t"
            "v_mfma_f32_16x16x32_bf16 v[168:171], v[164:167], v[116:119], v[168:171]\n\t"
            "s_add_u32 s45, s46, s27\n\t"
            "s_mov_b32 s46, s47\n\t"
            "s_mov_b32 s47, s68\n\t"
            "v_mfma_f32_16x16x32_bf16 v[172:175], v[144:147], v[120:123], 0\n\t"
            "s_xor_b32 s48, s48, s71\n\t"
            "s_xor_b32 s67, s67, s71\n\t"
            "s_add_u32 s76, s76, 4\n\t"
            "v_mfma_f32_16x16x32_bf16 v[172:175], v[148:151], v[124:127], v[172:175]\n\t"
            "s_lshl_b32 s56, s56, 2\n\t"
            "s_lshr_b32 s49, s26, 30\n\t"
            "s_or_b32 s56, s56, s49\n\t"
            "v_mfma_f32_16x16x32_bf16 v[172:175], v[152:155], v[128:131], v[172:175]\n\t"
            "v_mul_f32 v168, v228, v168\n\t"
            "v_mul_f32 v171, v231, v171\n\t"
            "v_mfma_f32_16x16x32_bf16 v[172:175], v[156:159], v[132:135], v[172:175]\n\t"
            "v_mul_f32 v169, v229, v169\n\t"
            "v_mul_f32 v170, v230, v170\n\t"
            "v_mfma_f32_16x16x32_bf16 v[172:175], v[160:163], v[136:139], v[172:175]\n\t"
            "v_cvt_pk_bf16_f32 v219, v170, v171\n\t"
            "v_cvt_pk_bf16_f32 v218, v168, v169\n\t"
            "v_mfma_f32_16x16x32_bf16 v[172:175], v[164:167], v[140:143], v[172:175]\n\t"
            "ds_write_b128 v243, v[200:203]\n\t"
            "s_waitcnt lgkmcnt(6)\n\t"
            "v_mfma_f32_16x16x32_bf16 v[224:227], v[176:179], v[0:3], 0\n\t"
            "ds_write_b128 v243, v[204:207] offset:1536\n\t"
            "v_mfma_f32_16x16x32_bf16 v[224:227], v[180:183], v[4:7], v[224:227]\n\t"
            "ds_write_b128 v243, v[216:219] offset:6144\n\t"
            "s_waitcnt lgkmcnt(6)\n\t"
            "v_mfma_f32_16x16x32_bf16 v[224:227], v[184:187], v[8:11], v[224:227]\n\t"
            "ds_write_b128 v243, v[212:215] offset:4608\n\t"
            "v_mfma_f32_16x16x32_bf16 v[224:227], v[188:191], v[12:15], v[224:227]\n\t"
            "v_mul_f32 v174, v230, v174\n\t"
            "v_mul_f32 v175, v231, v175\n\t"
            "s_waitcnt lgkmcnt(4)\n\t"
            "v_mfma_f32_16x16x32_bf16 v[224:227], v[192:195], v[16:19], v[224:227]\n\t"
            "v_mul_f32 v172, v228, v172\n\t"
            "v_mul_f32 v173, v229, v173\n\t"
            "v_mfma_f32_16x16x32_bf16 v[224:227], v[196:199], v[20:23], v[224:227]\n\t"
            "ds_read_b128 v[228:231], v245 offset:128\n\t"
            "v_cvt_pk_bf16_f32 v223, v174, v175\n\t"
            "v_mfma_f32_16x16x32_bf16 v[168:171], v[176:179], v[24:27], 0\n\t"
            "v_cvt_pk_bf16_f32 v222, v172, v173\n\t"
            "v_xor_b32 v233, 64, v232\n\t"
            "v_mfma_f32_16x16x32_bf16 v[168:171], v[180:183], v[28:31], v[168:171]\n\t"
            "ds_write_b128 v243, v[220:223] offset:7680\n\t"
            "v_mfma_f32_16x16x32_bf16 v[168:171], v[184:187], v[32:35], v[168:171]\n\t"
            "ds_write_b128 v243, v[208:211] offset:3072\n\t"
            "v_mfma_f32_16x16x32_bf16 v[168:171], v[188:191], v[36:39], v[168:171]\n\t"
            "v_mfma_f32_16x16x32_bf16 v[168:171], v[192:195], v[40:43], v[168:171]\n\t"
            "v_mfma_f32_16x16x32_bf16 v[168:171], v[196:199], v[44:47], v[168:171]\n\t"
            "v_mfma_f32_16x16x32_bf16 v[172:175], v[176:179], v[48:51], 0\n\t"
            "s_waitcnt lgkmcnt(2)\n\t"
            "v_mul_f32 v225, v229, v225\n\t"
            "v_mul_f32 v224, v228, v224\n\t"
            "v_mfma_f32_16x16x32_bf16 v[172:175], v[180:183], v[52:55], v[172:175]\n\t"
            "v_mul_f32 v227, v231, v227\n\t"
            "v_mul_f32 v226, v230, v226\n\t"
            "v_mfma_f32_16x16x32_bf16 v[172:175], v[184:187], v[56:59], v[172:175]\n\t"
            "v_cvt_pk_bf16_f32 v224, v224, v225\n\t"
            "v_cvt_pk_bf16_f32 v225, v226, v227\n\t"
            "v_mfma_f32_16x16x32_bf16 v[172:175], v[188:191], v[60:63], v[172:175]\n\t"
            "ds_write_b64 v244, v[224:225]\n\t"
            "v_mfma_f32_16x16x32_bf16 v[172:175], v[192:195], v[64:67], v[172:175]\n\t"
            "v_mul_f32 v171, v231, v171\n\t"
            "v_mul_f32 v168, v228, v168\n\t"
            "v_mfma_f32_16x16x32_bf16 v[172:175], v[196:199], v[68:71], v[172:175]\n\t"
            "v_mul_f32 v169, v229, v169\n\t"
            "v_cvt_pk_bf16_f32 v168, v168, v169\n\t"
            "v_mfma_f32_16x16x32_bf16 v[224:227], v[176:179], v[72:75], 0\n\t"
            "v_mul_f32 v170, v230, v170\n\t"
            "v_cvt_pk_bf16_f32 v169, v170, v171\n\t"
            "v_mfma_f32_16x16x32_bf16 v[224:227], v[180:183], v[76:79], v[224:227]\n\t"
            "ds_write_b64 v244, v[168:169] offset:1536\n\t"
            "v_mfma_f32_16x16x32_bf16 v[224:227], v[184:187], v[80:83], v[224:227]\n\t"
            "v_mul_f32 v175, v231, v175\n\t"
            "v_mul_f32 v173, v229, v173\n\t"
            "v_mfma_f32_16x16x32_bf16 v[224:227], v[188:191], v[84:87], v[224:227]\n\t"
            "v_mul_f32 v174, v230, v174\n\t"
            "v_mul_f32 v172, v228, v172\n\t"
            "v_mfma_f32_16x16x32_bf16 v[224:227], v[192:195], v[88:91], v[224:227]\n\t"
            "v_cvt_pk_bf16_f32 v172, v172, v173\n\t"
            "v_cvt_pk_bf16_f32 v173, v174, v175\n\t"
            "v_mfma_f32_16x16x32_bf16 v[224:227], v[196:199], v[92:95], v[224:227]\n\t"
            "ds_write_b64 v244, v[172:173] offset:3072\n\t"
            "v_mfma_f32_16x16x32_bf16 v[168:171], v[176:179], v[96:99], 0\n\t"
            "v_mfma_f32_16x16x32_bf16 v[168:171], v[180:183], v[100:103], v[168:171]\n\t"
            "v_mfma_f32_16x16x32_bf16 v[168:171], v[184:187], v[104:107], v[168:171]\n\t"
            "v_mfma_f32_16x16x32_bf16 v[168:171], v[188:191], v[108:111], v[168:171]\n\t"
            "v_mfma_f32_16x16x32_bf16 v[168:171], v[192:195], v[112:115], v[168:171]\n\t"
            "s_nop 1\n\t"
            "v_mul_f32 v225, v229, v225\n\t"
            "v_mfma_f32_16x16x32_bf16 v[168:171], v[196:199], v[116:119], v[168:171]\n\t"
            "v_mul_f32 v227, v231, v227\n\t"
            "v_mul_f32 v224, v228, v224\n\t"
            "v_mfma_f32_16x16x32_bf16 v[172:175], v[176:179], v[120:123], 0\n\t"
            "v_cvt_pk_bf16_f32 v224, v224, v225\n\t"
            "v_mul_f32 v226, v230, v226\n\t"
            "v_mfma_f32_16x16x32_bf16 v[172:175], v[180:183], v[124:127], v[172:175]\n\t"
            "v_cvt_pk_bf16_f32 v225, v226, v227\n\t"
            "v_mfma_f32_16x16x32_bf16 v[172:175], v[184:187], v[128:131], v[172:175]\n\t"
            "v_mul_f32 v171, v231, v171\n\t"
            "v_mul_f32 v170, v230, v170\n\t"
            "v_mfma_f32_16x16x32_bf16 v[172:175], v[188:191], v[132:135], v[172:175]\n\t"
            "v_mul_f32 v169, v229, v169\n\t"
            "v_mul_f32 v168, v228, v168\n\t"
            "v_mfma_f32_16x16x32_bf16 v[172:175], v[192:195], v[136:139], v[172:175]\n\t"
            "v_cvt_pk_bf16_f32 v168, v168, v169\n\t"
            "v_cvt_pk_bf16_f32 v169, v170, v171\n\t"
            "v_mfma_f32_16x16x32_bf16 v[172:175], v[196:199], v[140:143], v[172:175]\n\t"
            "ds_write_b64 v244, v[224:225] offset:4608\n\t"
            "ds_write_b64 v244, v[168:169] offset:6144\n\t"
            "ds_read_b128 v[168:171], v254\n\t"
            "s_nop 4\n\t"
            "v_mul_f32 v172, v228, v172\n\t"
            "v_mul_f32 v175, v231, v175\n\t"
            "v_mul_f32 v173, v229, v173\n\t"
            "v_mul_f32 v174, v230, v174\n\t"
            "v_cvt_pk_bf16_f32 v172, v172, v173\n\t"
            "v_cvt_pk_bf16_f32 v173, v174, v175\n\t"
            "ds_write_b64 v244, v[172:173] offset:7680\n\t"
            "ds_read_b128 v[172:175], v254 offset:64\n\t"
            "s_waitcnt lgkmcnt(1)\n\t"
            "s_cmp_lt_u32 s46, 0x7000000\n\t"
            "s_barrier\n\t"
            "s_cbranch_scc0 .Lvx%=\n\t"
            "ds_read_b128 v[144:147], v232\n\t"
            "ds_read_b128 v[148:151], v233\n\t"
            "ds_read_b128 v[156:159], v233 offset:128\n\t"
            "ds_read_b128 v[152:155], v232 offset:128\n\t"
            "ds_read_b128 v[160:163], v232 offset:256\n\t"
            "ds_read_b128 v[164:167], v233 offset:256\n\t"
            "ds_read_b128 v[176:179], v234 offset:18816\n\t"
            "ds_read_b128 v[180:183], v234 offset:19840\n\t"
            "s_waitcnt lgkmcnt(7)\n\t"
            "v_mfma_f32_16x16x32_bf16 v[200:203], v[144:147], v[0:3], 0\n\t"
            "s_waitcnt lgkmcnt(6)\n\t"
            "v_mfma_f32_16x16x32_bf16 v[200:203], v[148:151], v[4:7], v[200:203]\n\t"
            "s_waitcnt lgkmcnt(4)\n\t"
            "v_mfma_f32_16x16x32_bf16 v[200:203], v[152:155], v[8:11], v[200:203]\n\t"
            "v_mfma_f32_16x16x32_bf16 v[200:203], v[156:159], v[12:15], v[200:203]\n\t"
            "s_waitcnt lgkmcnt(1)\n\t"
            "v_add_f32 v177, v177, v179\n\t"
            "v_add_f32 v176, v176, v178\n\t"
            "v_mfma_f32_16x16x32_bf16 v[200:203], v[160:163], v[16:19], v[200:203]\n\t"
            "s_waitcnt lgkmcnt(0)\n\t"
            "v_add_f32 v180, v180, v182\n\t"
            "v_add_f32 v176, v176, v180\n\t"
            "v_mfma_f32_16x16x32_bf16 v[200:203], v[164:167], v[20:23], v[200:203]\n\t"
            "v_mul_f32 v176, 0x3baaaaab, v176\n\t"
            "v_mul_f32_e64 v176, v176, -v176\n\t"
            "v_mfma_f32_16x16x32_bf16 v[204:207], v[144:147], v[24:27], 0\n\t"
            "v_add_f32 v181, v181, v183\n\t"
            "v_add_f32 v177, v177, v181\n\t"
            "v_mfma_f32_16x16x32_bf16 v[204:207], v[148:151], v[28:31], v[204:207]\n\t"
            "v_fmac_f32 v176, 0x3baaaaab, v177\n\t"
            "v_max_f32 v176, 0, v176\n\t"
            "v_mfma_f32_16x16x32_bf16 v[204:207], v[152:155], v[32:35], v[204:207]\n\t"
            "v_add_f32 v176, 0x3727c5ac, v176\n\t"
            "v_rsq_f32 v176, v176\n\t"
            "v_mfma_f32_16x16x32_bf16 v[204:207], v[156:159], v[36:39], v[204:207]\n\t"
            "ds_write_b32 v246, v176\n\t"
            "v_mfma_f32_16x16x32_bf16 v[204:207], v[160:163], v[40:43], v[204:207]\n\t"
            "ds_read_b128 v[228:231], v245\n\t"
            "v_mfma_f32_16x16x32_bf16 v[204:207], v[164:167], v[44:47], v[204:207]\n\t"
            "v_mfma_f32_16x16x32_bf16 v[208:211], v[144:147], v[48:51], 0\n\t"
            "v_mfma_f32_16x16x32_bf16 v[208:211], v[148:151], v[52:55], v[208:211]\n\t"
            "v_mfma_f32_16x16x32_bf16 v[208:211], v[152:155], v[56:59], v[208:211]\n\t"
            "v_mfma_f32_16x16x32_bf16 v[208:211], v[156:159], v[60:63], v[208:211]\n\t"
            "v_mfma_f32_16x16x32_bf16 v[208:211], v[160:163], v[64:67], v[208:211]\n\t"
            "v_mfma_f32_16x16x32_bf16 v[208:211], v[164:167], v[68:71], v[208:211]\n\t"
            "v_mfma_f32_16x16x32_bf16 v[212:215], v[144:147], v[72:75], 0\n\t"
            "s_waitcnt lgkmcnt(0)\n\t"
            "v_mul_f32 v202, v230, v202\n\t"
            "v_mul_f32 v207, v231, v207\n\t"
            "v_mfma_f32_16x16x32_bf16 v[212:215], v[148:151], v[76:79], v[212:215]\n\t"
            "v_mul_f32 v206, v230, v206\n\t"
            "v_mul_f32 v205, v229, v205\n\t"
            "v_mfma_f32_16x16x32_bf16 v[212:215], v[152:155], v[80:83], v[212:215]\n\t"
            "v_mul_f32 v208, v228, v208\n\t"
            "v_mul_f32 v210, v230, v210\n\t"
            "v_mfma_f32_16x16x32_bf16 v[212:215], v[156:159], v[84:87], v[212:215]\n\t"
            "v_mul_f32 v211, v231, v211\n\t"
            "v_mul_f32 v201, v229, v201\n\t"
            "v_mfma_f32_16x16x32_bf16 v[212:215], v[160:163], v[88:91], v[212:215]\n\t"
            "v_mul_f32 v209, v229, v209\n\t"
            "v_cvt_pk_bf16_f32 v208, v208, v209\n\t"
            "v_mfma_f32_16x16x32_bf16 v[212:215], v[164:167], v[92:95], v[212:215]\n\t"
            "v_cvt_pk_bf16_f32 v209, v210, v211\n\t"
            "s_waitcnt vmcnt(0)\n\t"
            "v_mfma_f32_16x16x32_bf16 v[216:219], v[144:147], v[96:99], 0\n\t"
            "v_mul_f32 v200, v228, v200\n\t"
            "v_cvt_pk_bf16_f32 v200, v200, v201\n\t"
            "v_mfma_f32_16x16x32_bf16 v[216:219], v[148:151], v[100:103], v[216:219]\n\t"
            "v_mul_f32 v204, v228, v204\n\t"
            "v_mul_f32 v203, v231, v203\n\t"
            "v_mfma_f32_16x16x32_bf16 v[216:219], v[152:155], v[104:107], v[216:219]\n\t"
            "v_mul_f32 v215, v231, v215\n\t"
            "v_mul_f32 v214, v230, v214\n\t"
            "v_mfma_f32_16x16x32_bf16 v[216:219], v[156:159], v[108:111], v[216:219]\n\t"
            "v_mul_f32 v213, v229, v213\n\t"
            "v_cvt_pk_bf16_f32 v201, v202, v203\n\t"
            "v_mfma_f32_16x16x32_bf16 v[216:219], v[160:163], v[112:115], v[216:219]\n\t"
            "v_mul_f32 v212, v228, v212\n\t"
            "v_cvt_pk_bf16_f32 v212, v212, v213\n\t"
            "v_mfma_f32_16x16x32_bf16 v[216:219], v[164:167], v[116:119], v[216:219]\n\t"
            "v_cvt_pk_bf16_f32 v213, v214, v215\n\t"
            "v_cvt_pk_bf16_f32 v204, v204, v205\n\t"
            "v_mfma_f32_16x16x32_bf16 v[220:223], v[144:147], v[120:123], 0\n\t"
            "ds_read_b64 v[144:145], v253\n\t"
            "v_cvt_pk_bf16_f32 v205, v206, v207\n\t"
            "v_mfma_f32_16x16x32_bf16 v[220:223], v[148:151], v[124:127], v[220:223]\n\t"
            "ds_read_b64 v[148:149], v253 offset:1744\n\t"
            "v_mfma_f32_16x16x32_bf16 v[220:223], v[152:155], v[128:131], v[220:223]\n\t"
            "ds_read_b64 v[152:153], v253 offset:8\n\t"
            "v_mul_f32 v217, v229, v217\n\t"
            "v_mfma_f32_16x16x32_bf16 v[220:223], v[156:159], v[132:135], v[220:223]\n\t"
            "ds_read_b64 v[156:157], v253 offset:1752\n\t"
            "v_mul_f32 v218, v230, v218\n\t"
            "v_mfma_f32_16x16x32_bf16 v[220:223], v[160:163], v[136:139], v[220:223]\n\t"
            "ds_read_b64 v[160:161], v253 offset:16\n\t"
            "v_mul_f32 v216, v228, v216\n\t"
            "v_mfma_f32_16x16x32_bf16 v[220:223], v[164:167], v[140:143], v[220:223]\n\t"
            "ds_read_b64 v[154:155], v253 offset:104\n\t"
            "v_cvt_pk_bf16_f32 v216, v216, v217\n\t"
            "v_mul_f32 v219, v231, v219\n\t"
            "v_cvt_pk_bf16_f32 v217, v218, v219\n\t"
            "ds_read_b64 v[164:165], v253 offset:1760\n\t"
            "ds_read_b64 v[166:167], v253 offset:1856\n\t"
            "ds_read_b64 v[162:163], v253 offset:112\n\t"
            "ds_read_b64 v[158:159], v253 offset:1848\n\t"
            "v_mul_f32 v221, v229, v221\n\t"
            "v_mul_f32 v223, v231, v223\n\t"
            "v_mul_f32 v222, v230, v222\n\t"
            "v_mul_f32 v220, v228, v220\n\t"
            "v_cvt_pk_bf16_f32 v220, v220, v221\n\t"
            "v_cvt_pk_bf16_f32 v221, v222, v223\n\t"
            "ds_read_b64 v[146:147], v253 offset:96\n\t"
            "ds_read_b64 v[150:151], v253 offset:1840\n\t"
            "s_branch .Lwc_loop%=\n\t"
            ".Lvx%=:\n\t"
            "s_waitcnt lgkmcnt(0)\n\t"
            "s_nop 7\n\t"
            "s_setprio 3\n\t"
            "v_add_u32 v236, s19, v252\n\t"
            "ds_read_b128 v[244:247], v236\n\t"
            "s_add_u32 s70, s33, 73728\n\t"
            "s_waitcnt lgkmcnt(0)\n\t"
            "buffer_load_dwordx4 v[200:203], v244, s[28:31], s21 offen\n\t"
            "buffer_load_dwordx4 v[204:207], v245, s[28:31], s21 offen\n\t"
            "buffer_load_dwordx4 v[208:211], v246, s[28:31], s21 offen\n\t"
            "s_add_u32 s21, s21, 6144\n\t"
            "buffer_load_dwordx4 v[212:215], v244, s[28:31], s21 offen\n\t"
            "buffer_load_dwordx4 v[216:219], v245, s[28:31], s21 offen\n\t"
            "buffer_load_dwordx4 v[220:223], v246, s[28:31], s21 offen\n\t"
            "s_nop 7\n\t"
            "s_add_u32 s49, s70, 0\n\t"
            "buffer_load_dwordx4 v[0:3], v247, s[28:31], s49 offen sc1\n\t"
            "s_add_u32 s49, s70, 1024\n\t"
            "buffer_load_dwordx4 v[4:7], v247, s[28:31], s49 offen sc1\n\t"
            "s_add_u32 s49, s70, 2048\n\t"
            "buffer_load_dwordx4 v[8:11], v247, s[28:31], s49 offen sc1\n\t"
            "s_add_u32 s49, s70, 3072\n\t"
            "buffer_load_dwordx4 v[12:15], v247, s[28:31], s49 offen sc1\n\t"
            "s_add_u32 s49, s70, 4096\n\t"
            "buffer_load_dwordx4 v[16:19], v247, s[28:31], s49 offen sc1\n\t"
            "s_add_u32 s49, s70, 5120\n\t"
            "buffer_load_dwordx4 v[20:23], v247, s[28:31], s49 offen sc1\n\t"
            "s_add_u32 s49, s70, 6144\n\t"
            "buffer_load_dwordx4 v[24:27], v247, s[28:31], s49 offen sc1\n\t"
            "s_add_u32 s49, s70, 7168\n\t"
            "buffer_load_dwordx4 v[28:31], v247, s[28:31], s49 offen sc1\n\t"
            "s_add_u32 s49, s70, 8192\n\t"
            "buffer_load_dwordx4 v[32:35], v247, s[28:31], s49 offen sc1\n\t"
            "s_add_u32 s49, s70, 9216\n\t"
            "buffer_load_dwordx4 v[36:39], v247, s[28:31], s49 offen sc1\n\t"
            "s_add_u32 s49, s70, 10240\n\t"
            "buffer_load_dwordx4 v[40:43], v247, s[28:31], s49 offen sc1\n\t"
            "s_add_u32 s49, s70, 11264\n\t"
            "buffer_load_dwordx4 v[44:47], v247, s[28:31], s49 offen sc1\n\t"
            "s_add_u32 s49, s70, 12288\n\t"
            "buffer_load_dwordx4 v[48:51], v247, s[28:31], s49 offen sc1\n\t"
            "s_add_u32 s49, s70, 13312\n\t"
            "buffer_load_dwordx4 v[52:55], v247, s[28:31], s49 offen sc1\n\t"
            "s_add_u32 s49, s70, 14336\n\t"
            "buffer_load_dwordx4 v[56:59], v247, s[28:31], s49 offen sc1\n\t"
            "s_add_u32 s49, s70, 15360\n\t"
            "buffer_load_dwordx4 v[60:63], v247, s[28:31], s49 offen sc1\n\t"
            "s_add_u32 s49, s70, 16384\n\t"
            "buffer_load_dwordx4 v[64:67], v247, s[28:31], s49 offen sc1\n\t"
            "s_add_u32 s49, s70, 17408\n\t"
            "buffer_load_dwordx4 v[68:71], v247, s[28:31], s49 offen sc1\n\t"
            "s_add_u32 s49, s70, 18432\n\t"
            "buffer_load_dwordx4 v[72:75], v247, s[28:31], s49 offen sc1\n\t"
            "s_add_u32 s49, s70, 19456\n\t"
            "buffer_load_dwordx4 v[76:79], v247, s[28:31], s49 offen sc1\n\t"
            "s_add_u32 s49, s70, 20480\n\t"
            "buffer_load_dwordx4 v[80:83], v247, s[28:31], s49 offen sc1\n\t"
            "s_add_u32 s49, s70, 21504\n\t"
            "buffer_load_dwordx4 v[84:87], v247, s[28:31], s49 offen sc1\n\t"
            "s_add_u32 s49, s70, 22528\n\t"
            "buffer_load_dwordx4 v[88:91], v247, s[28:31], s49 offen sc1\n\t"
            "s_add_u32 s49, s70, 23552\n\t"
            "buffer_load_dwordx4 v[92:95], v247, s[28:31], s49 offen sc1\n\t"
            "s_waitcnt vmcnt(18)\n\t"
            "s_add_u32 s49, s70, 24576\n\t"
            "buffer_load_dwordx4 v[96:99], v247, s[28:31], s49 offen sc1\n\t"
            "s_add_u32 s49, s70, 25600\n\t"
            "buffer_load_dwordx4 v[100:103], v247, s[28:31], s49 offen sc1\n\t"
            "v_add_u32 v242, s22, v242\n\t"
            "v_xor_b32 v243, 64, v242\n\t"
            "v_lshlrev_b32 v192, 16, v200\n\t"
            "v_and_b32 v193, 0xffff0000, v200\n\t"
            "v_lshlrev_b32 v194, 16, v201\n\t"
            "v_and_b32 v195, 0xffff0000, v201\n\t"
            "v_lshlrev_b32 v196, 16, v202\n\t"
            "v_and_b32 v197, 0xffff0000, v202\n\t"
            "v_lshlrev_b32 v198, 16, v203\n\t"
            "v_and_b32 v199, 0xffff0000, v203\n\t"
            "v_bfe_u32 v248, v247, 2, 6\n\t"
            "v_add_u32 v248, s23, v248\n\t"
            "v_lshlrev_b32 v224, 16, v204\n\t"
            "v_and_b32 v225, 0xffff0000, v204\n\t"
            "v_lshlrev_b32 v226, 16, v205\n\t"
            "v_and_b32 v227, 0xffff0000, v205\n\t"
            "v_lshrrev_b32 v236, 4, v247\n\t"
            "v_and_b32 v236, 48, v236\n\t"
            "v_add_u32 v236, s20, v236\n\t"
            "v_bfe_u32 v249, v247, 2, 6\n\t"
            "v_lshlrev_b32 v228, 16, v206\n\t"
            "v_and_b32 v229, 0xffff0000, v206\n\t"
            "v_lshlrev_b32 v230, 16, v207\n\t"
            "v_and_b32 v231, 0xffff0000, v207\n\t"
            "v_add_u32 v249, s23, v249\n\t"
            "ds_read_b128 v[144:147], v242 offset:53760\n\t"
            "ds_read_b128 v[148:151], v243 offset:53760\n\t"
            "ds_read_b128 v[152:155], v242 offset:53888\n\t"
            "ds_read_b128 v[156:159], v243 offset:53888\n\t"
            "ds_read_b128 v[160:163], v242 offset:54016\n\t"
            "ds_read_b128 v[164:167], v243 offset:54016\n\t"
            "ds_read_b32 v248, v248\n\t"
            "ds_read_b32 v249, v249 offset:64\n\t"
            "s_add_u32 s49, s70, 26624\n\t"
            "buffer_load_dwordx4 v[104:107], v247, s[28:31], s49 offen sc1\n\t"
            "s_add_u32 s49, s70, 27648\n\t"
            "buffer_load_dwordx4 v[108:111], v247, s[28:31], s49 offen sc1\n\t"
            "s_waitcnt lgkmcnt(7)\n\t"
            "v_mfma_f32_16x16x32_bf16 v[192:195], v[0:3], v[144:147], v[192:195]\n\t"
            "s_add_u32 s49, s70, 28672\n\t"
            "buffer_load_dwordx4 v[112:115], v247, s[28:31], s49 offen sc1\n\t"
            "s_waitcnt lgkmcnt(6)\n\t"
            "v_mfma_f32_16x16x32_bf16 v[192:195], v[4:7], v[148:151], v[192:195]\n\t"
            "s_add_u32 s49, s70, 29696\n\t"
            "buffer_load_dwordx4 v[116:119], v247, s[28:31], s49 offen sc1\n\t"
            "s_waitcnt lgkmcnt(5)\n\t"
            "v_mfma_f32_16x16x32_bf16 v[192:195], v[8:11], v[152:155], v[192:195]\n\t"
            "s_waitcnt vmcnt(18)\n\t"
            "s_add_u32 s49, s70, 30720\n\t"
            "buffer_load_dwordx4 v[120:123], v247, s[28:31], s49 offen sc1\n\t"
            "s_waitcnt lgkmcnt(4)\n\t"
            "v_mfma_f32_16x16x32_bf16 v[192:195], v[12:15], v[156:159], v[192:195]\n\t"
            "s_add_u32 s49, s70, 31744\n\t"
            "buffer_load_dwordx4 v[124:127], v247, s[28:31], s49 offen sc1\n\t"
            "s_waitcnt lgkmcnt(3)\n\t"
            "v_mfma_f32_16x16x32_bf16 v[192:195], v[16:19], v[160:163], v[192:195]\n\t"
            "s_add_u32 s49, s70, 32768\n\t"
            "buffer_load_dwordx4 v[128:131], v247, s[28:31], s49 offen sc1\n\t"
            "s_waitcnt lgkmcnt(2)\n\t"
            "v_mfma_f32_16x16x32_bf16 v[192:195], v[20:23], v[164:167], v[192:195]\n\t"
            "s_add_u32 s49, s70, 33792\n\t"
            "buffer_load_dwordx4 v[132:135], v247, s[28:31], s49 offen sc1\n\t"
            "v_mfma_f32_16x16x32_bf16 v[196:199], v[24:27], v[144:147], v[196:199]\n\t"
            "s_add_u32 s49, s70, 34816\n\t"
            "buffer_load_dwordx4 v[136:139], v247, s[28:31], s49 offen sc1\n\t"
            "v_mfma_f32_16x16x32_bf16 v[196:199], v[28:31], v[148:151], v[196:199]\n\t"
            "s_add_u32 s49, s70, 35840\n\t"
            "buffer_load_dwordx4 v[140:143], v247, s[28:31], s49 offen sc1\n\t"
            "v_mfma_f32_16x16x32_bf16 v[196:199], v[32:35], v[152:155], v[196:199]\n\t"
            "s_waitcnt vmcnt(18)\n\t"
            "s_waitcnt lgkmcnt(1)\n\t"
            "v_ashrrev_i32 v237, 31, v248\n\t"
            "v_mfma_f32_16x16x32_bf16 v[196:199], v[36:39], v[156:159], v[196:199]\n\t"
            "v_and_b32 v237, 1177344, v237\n\t"
            "v_sub_u32 v236, v236, v237\n\t"
            "v_mfma_f32_16x16x32_bf16 v[196:199], v[40:43], v[160:163], v[196:199]\n\t"
            "v_bfe_i32 v237, v248, 30, 1\n\t"
            "v_and_b32 v237, 2304, v237\n\t"
            "v_mfma_f32_16x16x32_bf16 v[196:199], v[44:47], v[164:167], v[196:199]\n\t"
            "v_cvt_pk_bf16_f32 v192, v192, v193\n\t"
            "v_sub_u32 v236, v236, v237\n\t"
            "v_mfma_f32_16x16x32_bf16 v[224:227], v[48:51], v[144:147], v[224:227]\n\t"
            "v_and_b32 v248, 0x7ffffff, v248\n\t"
            "v_cvt_pk_bf16_f32 v193, v194, v195\n\t"
            "v_mfma_f32_16x16x32_bf16 v[224:227], v[52:55], v[148:151], v[224:227]\n\t"
            "v_add_u32 v248, v248, v236\n\t"
            "v_lshrrev_b32 v236, 4, v247\n\t"
            "v_mfma_f32_16x16x32_bf16 v[224:227], v[56:59], v[152:155], v[224:227]\n\t"
            "v_cvt_pk_bf16_f32 v194, v196, v197\n\t"
            "v_cvt_pk_bf16_f32 v195, v198, v199\n\t"
            "v_mfma_f32_16x16x32_bf16 v[224:227], v[60:63], v[156:159], v[224:227]\n\t"
            "buffer_store_dwordx4 v[192:195], v248, s[40:43], 0 offen sc1\n\t"
            "v_mfma_f32_16x16x32_bf16 v[224:227], v[64:67], v[160:163], v[224:227]\n\t"
            "s_waitcnt vmcnt(13)\n\t"
            "v_and_b32 v236, 48, v236\n\t"
            "s_waitcnt lgkmcnt(0)\n\t"
            "v_ashrrev_i32 v237, 31, v249\n\t"
            "v_mfma_f32_16x16x32_bf16 v[224:227], v[68:71], v[164:167], v[224:227]\n\t"
            "v_add_u32 v236, s20, v236\n\t"
            "v_and_b32 v237, 1177344, v237\n\t"
            "v_mfma_f32_16x16x32_bf16 v[228:231], v[72:75], v[144:147], v[228:231]\n\t"
            "v_sub_u32 v236, v236, v237\n\t"
            "v_bfe_i32 v237, v249, 30, 1\n\t"
            "v_mfma_f32_16x16x32_bf16 v[228:231], v[76:79], v[148:151], v[228:231]\n\t"
            "v_and_b32 v237, 2304, v237\n\t"
            "v_sub_u32 v236, v236, v237\n\t"
            "v_mfma_f32_16x16x32_bf16 v[228:231], v[80:83], v[152:155], v[228:231]\n\t"
            "v_cvt_pk_bf16_f32 v224, v224, v225\n\t"
            "v_cvt_pk_bf16_f32 v225, v226, v227\n\t"
            "v_mfma_f32_16x16x32_bf16 v[228:231], v[84:87], v[156:159], v[228:231]\n\t"
            "v_and_b32 v249, 0x7ffffff, v249\n\t"
            "v_add_u32 v249, v249, v236\n\t"
            "v_mfma_f32_16x16x32_bf16 v[228:231], v[88:91], v[160:163], v[228:231]\n\t"
            "v_mfma_f32_16x16x32_bf16 v[228:231], v[92:95], v[164:167], v[228:231]\n\t"
            "s_nop 7\n\t"
            "v_cvt_pk_bf16_f32 v226, v228, v229\n\t"
            "v_cvt_pk_bf16_f32 v227, v230, v231\n\t"
            "buffer_store_dwordx4 v[224:227], v248, s[40:43], 0 offen offset:64 sc1\n\t"
            "s_nop 7\n\t"
            "v_mov_b32 v168, s23\n\t"
            ".Ldv%=:\n\t"
            "ds_read_b128 v[172:175], v168 offset:96\n\t"
            "s_waitcnt lgkmcnt(0)\n\t"
            "v_or_b32 v172, v172, v173\n\t"
            "v_or3_b32 v172, v172, v174, v175\n\t"
            "v_cmp_gt_i32 vcc, 0, v172\n\t"
            "s_nop 4\n\t"
            "s_cbranch_vccnz .Ldv%=\n\t"
            "s_nop 7\n\t"
            "ds_read_b128 v[168:171], v242 offset:59904\n\t"
            "v_lshlrev_b32 v232, 16, v212\n\t"
            "v_and_b32 v233, 0xffff0000, v212\n\t"
            "v_lshlrev_b32 v234, 16, v213\n\t"
            "v_and_b32 v235, 0xffff0000, v213\n\t"
            "s_waitcnt vmcnt(32)\n\t"
            "v_lshlrev_b32 v192, 16, v214\n\t"
            "v_and_b32 v193, 0xffff0000, v214\n\t"
            "v_lshlrev_b32 v194, 16, v215\n\t"
            "v_and_b32 v195, 0xffff0000, v215\n\t"
            "v_lshlrev_b32 v196, 16, v216\n\t"
            "v_and_b32 v197, 0xffff0000, v216\n\t"
            "v_lshlrev_b32 v198, 16, v217\n\t"
            "v_and_b32 v199, 0xffff0000, v217\n\t"
            "v_lshlrev_b32 v224, 16, v218\n\t"
            "v_and_b32 v225, 0xffff0000, v218\n\t"
            "v_lshlrev_b32 v226, 16, v219\n\t"
            "v_and_b32 v227, 0xffff0000, v219\n\t"
            "v_lshlrev_b32 v228, 16, v208\n\t"
            "v_and_b32 v229, 0xffff0000, v208\n\t"
            "v_lshlrev_b32 v230, 16, v209\n\t"
            "v_and_b32 v231, 0xffff0000, v209\n\t"
            "ds_read_b128 v[172:175], v243 offset:59904\n\t"
            "ds_read_b128 v[176:179], v242 offset:60032\n\t"
            "ds_read_b128 v[180:183], v243 offset:60032\n\t"
            "ds_read_b128 v[184:187], v242 offset:60160\n\t"
            "s_waitcnt lgkmcnt(4)\n\t"
            "v_mfma_f32_16x16x32_bf16 v[232:235], v[0:3], v[168:171], v[232:235]\n\t"
            "ds_read_b128 v[188:191], v243 offset:60160\n\t"
            "s_waitcnt lgkmcnt(4)\n\t"
            "v_mfma_f32_16x16x32_bf16 v[232:235], v[4:7], v[172:175], v[232:235]\n\t"
            "s_waitcnt lgkmcnt(3)\n\t"
            "v_mfma_f32_16x16x32_bf16 v[232:235], v[8:11], v[176:179], v[232:235]\n\t"
            "s_waitcnt vmcnt(26)\n\t"
            "s_waitcnt lgkmcnt(2)\n\t"
            "v_mfma_f32_16x16x32_bf16 v[232:235], v[12:15], v[180:183], v[232:235]\n\t"
            "s_waitcnt lgkmcnt(1)\n\t"
            "v_mfma_f32_16x16x32_bf16 v[232:235], v[16:19], v[184:187], v[232:235]\n\t"
            "s_waitcnt lgkmcnt(0)\n\t"
            "v_mfma_f32_16x16x32_bf16 v[232:235], v[20:23], v[188:191], v[232:235]\n\t"
            "v_mfma_f32_16x16x32_bf16 v[192:195], v[24:27], v[168:171], v[192:195]\n\t"
            "v_mfma_f32_16x16x32_bf16 v[192:195], v[28:31], v[172:175], v[192:195]\n\t"
            "v_mfma_f32_16x16x32_bf16 v[192:195], v[32:35], v[176:179], v[192:195]\n\t"
            "s_waitcnt vmcnt(20)\n\t"
            "v_mfma_f32_16x16x32_bf16 v[192:195], v[36:39], v[180:183], v[192:195]\n\t"
            "v_mfma_f32_16x16x32_bf16 v[192:195], v[40:43], v[184:187], v[192:195]\n\t"
            "s_nop 1\n\t"
            "v_cvt_pk_bf16_f32 v232, v232, v233\n\t"
            "v_mfma_f32_16x16x32_bf16 v[192:195], v[44:47], v[188:191], v[192:195]\n\t"
            "v_cvt_pk_bf16_f32 v233, v234, v235\n\t"
            "v_mfma_f32_16x16x32_bf16 v[196:199], v[48:51], v[168:171], v[196:199]\n\t"
            "v_mfma_f32_16x16x32_bf16 v[196:199], v[52:55], v[172:175], v[196:199]\n\t"
            "v_mfma_f32_16x16x32_bf16 v[196:199], v[56:59], v[176:179], v[196:199]\n\t"
            "v_mfma_f32_16x16x32_bf16 v[196:199], v[60:63], v[180:183], v[196:199]\n\t"
            "v_mfma_f32_16x16x32_bf16 v[196:199], v[64:67], v[184:187], v[196:199]\n\t"
            "s_nop 1\n\t"
            "v_cvt_pk_bf16_f32 v234, v192, v193\n\t"
            "v_mfma_f32_16x16x32_bf16 v[196:199], v[68:71], v[188:191], v[196:199]\n\t"
            "v_cvt_pk_bf16_f32 v235, v194, v195\n\t"
            "v_lshlrev_b32 v192, 16, v220\n\t"
            "v_and_b32 v193, 0xffff0000, v220\n\t"
            "v_lshlrev_b32 v194, 16, v221\n\t"
            "v_and_b32 v195, 0xffff0000, v221\n\t"
            "buffer_store_dwordx4 v[232:235], v249, s[40:43], 0 offen sc1\n\t"
            "s_waitcnt vmcnt(15)\n\t"
            "v_mfma_f32_16x16x32_bf16 v[224:227], v[72:75], v[168:171], v[224:227]\n\t"
            "v_cvt_pk_bf16_f32 v196, v196, v197\n\t"
            "v_cvt_pk_bf16_f32 v197, v198, v199\n\t"
            "v_mfma_f32_16x16x32_bf16 v[224:227], v[76:79], v[172:175], v[224:227]\n\t"
            "v_lshlrev_b32 v232, 16, v210\n\t"
            "v_and_b32 v233, 0xffff0000, v210\n\t"
            "v_mfma_f32_16x16x32_bf16 v[224:227], v[80:83], v[176:179], v[224:227]\n\t"
            "s_waitcnt vmcnt(9)\n\t"
            "v_lshlrev_b32 v234, 16, v211\n\t"
            "v_mfma_f32_16x16x32_bf16 v[224:227], v[84:87], v[180:183], v[224:227]\n\t"
            "v_and_b32 v235, 0xffff0000, v211\n\t"
            "v_mfma_f32_16x16x32_bf16 v[224:227], v[88:91], v[184:187], v[224:227]\n\t"
            "v_mfma_f32_16x16x32_bf16 v[224:227], v[92:95], v[188:191], v[224:227]\n\t"
            "v_mfma_f32_16x16x32_bf16 v[228:231], v[96:99], v[144:147], v[228:231]\n\t"
            "v_mfma_f32_16x16x32_bf16 v[228:231], v[100:103], v[148:151], v[228:231]\n\t"
            "v_mfma_f32_16x16x32_bf16 v[228:231], v[104:107], v[152:155], v[228:231]\n\t"
            "v_mfma_f32_16x16x32_bf16 v[228:231], v[108:111], v[156:159], v[228:231]\n\t"
            "v_mfma_f32_16x16x32_bf16 v[228:231], v[112:115], v[160:163], v[228:231]\n\t"
            "v_mfma_f32_16x16x32_bf16 v[228:231], v[116:119], v[164:167], v[228:231]\n\t"
            "s_nop 1\n\t"
            "v_cvt_pk_bf16_f32 v198, v224, v225\n\t"
            "v_cvt_pk_bf16_f32 v199, v226, v227\n\t"
            "buffer_store_dwordx4 v[196:199], v249, s[40:43], 0 offen offset:64 sc1\n\t"
            "s_waitcnt vmcnt(4)\n\t"
            "v_mfma_f32_16x16x32_bf16 v[232:235], v[120:123], v[144:147], v[232:235]\n\t"
            "v_lshlrev_b32 v196, 16, v222\n\t"
            "v_and_b32 v197, 0xffff0000, v222\n\t"
            "v_mfma_f32_16x16x32_bf16 v[232:235], v[124:127], v[148:151], v[232:235]\n\t"
            "v_cvt_pk_bf16_f32 v228, v228, v229\n\t"
            "v_cvt_pk_bf16_f32 v229, v230, v231\n\t"
            "v_mfma_f32_16x16x32_bf16 v[232:235], v[128:131], v[152:155], v[232:235]\n\t"
            "s_waitcnt vmcnt(10)\n\t"
            "v_lshlrev_b32 v198, 16, v223\n\t"
            "v_mfma_f32_16x16x32_bf16 v[232:235], v[132:135], v[156:159], v[232:235]\n\t"
            "v_and_b32 v199, 0xffff0000, v223\n\t"
            "v_mfma_f32_16x16x32_bf16 v[232:235], v[136:139], v[160:163], v[232:235]\n\t"
            "v_mfma_f32_16x16x32_bf16 v[232:235], v[140:143], v[164:167], v[232:235]\n\t"
            "v_mfma_f32_16x16x32_bf16 v[192:195], v[96:99], v[168:171], v[192:195]\n\t"
            "v_mfma_f32_16x16x32_bf16 v[192:195], v[100:103], v[172:175], v[192:195]\n\t"
            "v_mfma_f32_16x16x32_bf16 v[192:195], v[104:107], v[176:179], v[192:195]\n\t"
            "v_mfma_f32_16x16x32_bf16 v[192:195], v[108:111], v[180:183], v[192:195]\n\t"
            "v_mfma_f32_16x16x32_bf16 v[192:195], v[112:115], v[184:187], v[192:195]\n\t"
            "v_mfma_f32_16x16x32_bf16 v[192:195], v[116:119], v[188:191], v[192:195]\n\t"
            "s_nop 1\n\t"
            "v_cvt_pk_bf16_f32 v230, v232, v233\n\t"
            "v_cvt_pk_bf16_f32 v231, v234, v235\n\t"
            "buffer_store_dwordx4 v[228:231], v248, s[40:43], 0 offen offset:128 sc1\n\t"
            "s_waitcnt vmcnt(5)\n\t"
            "v_mfma_f32_16x16x32_bf16 v[196:199], v[120:123], v[168:171], v[196:199]\n\t"
            "v_mfma_f32_16x16x32_bf16 v[196:199], v[124:127], v[172:175], v[196:199]\n\t"
            "v_cvt_pk_bf16_f32 v192, v192, v193\n\t"
            "v_mfma_f32_16x16x32_bf16 v[196:199], v[128:131], v[176:179], v[196:199]\n\t"
            "v_cvt_pk_bf16_f32 v193, v194, v195\n\t"
            "v_mfma_f32_16x16x32_bf16 v[196:199], v[132:135], v[180:183], v[196:199]\n\t"
            "v_mfma_f32_16x16x32_bf16 v[196:199], v[136:139], v[184:187], v[196:199]\n\t"
            "v_mfma_f32_16x16x32_bf16 v[196:199], v[140:143], v[188:191], v[196:199]\n\t"
            "s_nop 7\n\t"
            "v_cvt_pk_bf16_f32 v194, v196, v197\n\t"
            "v_cvt_pk_bf16_f32 v195, v198, v199\n\t"
            "buffer_store_dwordx4 v[192:195], v249, s[40:43], 0 offen offset:128 sc1\n\t"
            "s_nop 7\n\t"
            "s_branch .Lre%=\n\t"
            ".Lrp%=:\n\t"
            "v_add_u32 v236, s19, v252\n\t"
            "ds_write_b128 v236, v[244:247]\n\t"
            "v_add_u32 v236, s48, v244\n\t"
            "v_add_u32 v237, s48, v245\n\t"
            "v_add_u32 v248, s48, v246\n\t"
            "ds_read_b128 v[200:203], v236\n\t"
            "ds_read_b128 v[204:207], v237\n\t"
            "ds_read_b128 v[208:211], v248\n\t"
            "ds_read_b128 v[212:215], v236 offset:6144\n\t"
            "ds_read_b128 v[216:219], v237 offset:6144\n\t"
            "ds_read_b128 v[220:223], v248 offset:6144\n\t"
            "ds_read_b128 v[224:227], v236 offset:12288\n\t"
            "ds_read_b128 v[228:231], v237 offset:12288\n\t"
            "ds_read_b128 v[232:235], v248 offset:12288\n\t"
            "s_waitcnt lgkmcnt(0)\n\t"
            "s_barrier\n\t"
            "s_add_u32 s45, s46, s27\n\t"
            "s_mov_b32 s46, s47\n\t"
            "s_mov_b32 s47, s68\n\t"
            "s_xor_b32 s48, s48, s71\n\t"
            "s_xor_b32 s67, s67, s71\n\t"
            "s_add_u32 s76, s76, 4\n\t"
            "s_lshl_b32 s56, s56, 2\n\t"
            "s_lshr_b32 s49, s26, 30\n\t"
            "s_or_b32 s56, s56, s49\n\t"
            "s_cmp_ge_u32 s76, s24\n\t"
            "s_cselect_b64 vcc, -1, 0\n\t"
            "s_add_u32 s49, s33, 0\n\t"
            "buffer_load_dwordx4 v[0:3], v247, s[28:31], s49 offen sc1\n\t"
            "s_add_u32 s49, s33, 1024\n\t"
            "buffer_load_dwordx4 v[4:7], v247, s[28:31], s49 offen sc1\n\t"
            "s_add_u32 s49, s33, 2048\n\t"
            "buffer_load_dwordx4 v[8:11], v247, s[28:31], s49 offen sc1\n\t"
            "s_add_u32 s49, s33, 3072\n\t"
            "buffer_load_dwordx4 v[12:15], v247, s[28:31], s49 offen sc1\n\t"
            "s_add_u32 s49, s33, 4096\n\t"
            "buffer_load_dwordx4 v[16:19], v247, s[28:31], s49 offen sc1\n\t"
            "s_add_u32 s49, s33, 5120\n\t"
            "buffer_load_dwordx4 v[20:23], v247, s[28:31], s49 offen sc1\n\t"
            "s_add_u32 s49, s33, 6144\n\t"
            "buffer_load_dwordx4 v[24:27], v247, s[28:31], s49 offen sc1\n\t"
            "s_add_u32 s49, s33, 7168\n\t"
            "buffer_load_dwordx4 v[28:31], v247, s[28:31], s49 offen sc1\n\t"
            "s_add_u32 s49, s33, 8192\n\t"
            "buffer_load_dwordx4 v[32:35], v247, s[28:31], s49 offen sc1\n\t"
            "s_add_u32 s49, s33, 9216\n\t"
            "buffer_load_dwordx4 v[36:39], v247, s[28:31], s49 offen sc1\n\t"
            "s_add_u32 s49, s33, 10240\n\t"
            "buffer_load_dwordx4 v[40:43], v247, s[28:31], s49 offen sc1\n\t"
            "s_add_u32 s49, s33, 11264\n\t"
            "buffer_load_dwordx4 v[44:47], v247, s[28:31], s49 offen sc1\n\t"
            "s_add_u32 s49, s33, 12288\n\t"
            "buffer_load_dwordx4 v[48:51], v247, s[28:31], s49 offen sc1\n\t"
            "s_add_u32 s49, s33, 13312\n\t"
            "buffer_load_dwordx4 v[52:55], v247, s[28:31], s49 offen sc1\n\t"
            "s_add_u32 s49, s33, 14336\n\t"
            "buffer_load_dwordx4 v[56:59], v247, s[28:31], s49 offen sc1\n\t"
            "s_add_u32 s49, s33, 15360\n\t"
            "buffer_load_dwordx4 v[60:63], v247, s[28:31], s49 offen sc1\n\t"
            "s_add_u32 s49, s33, 16384\n\t"
            "buffer_load_dwordx4 v[64:67], v247, s[28:31], s49 offen sc1\n\t"
            "s_add_u32 s49, s33, 17408\n\t"
            "buffer_load_dwordx4 v[68:71], v247, s[28:31], s49 offen sc1\n\t"
            "ds_read_b128 v[168:171], v254\n\t"
            "ds_read_b128 v[172:175], v254 offset:64\n\t"
            "s_waitcnt vmcnt(0)\n\t"
            "ds_read_b64 v[144:145], v253\n\t"
            "ds_read_b64 v[146:147], v253 offset:96\n\t"
            "ds_read_b64 v[148:149], v253 offset:1744\n\t"
            "ds_read_b64 v[150:151], v253 offset:1840\n\t"
            "ds_read_b64 v[152:153], v253 offset:8\n\t"
            "ds_read_b64 v[154:155], v253 offset:104\n\t"
            "ds_read_b64 v[156:157], v253 offset:1752\n\t"
            "ds_read_b64 v[158:159], v253 offset:1848\n\t"
            "ds_read_b64 v[160:161], v253 offset:16\n\t"
            "ds_read_b64 v[162:163], v253 offset:112\n\t"
            "ds_read_b64 v[164:165], v253 offset:1760\n\t"
            "ds_read_b64 v[166:167], v253 offset:1856\n\t"
            "s_waitcnt lgkmcnt(0)\n\t"
            "s_nop 7\n\t"
            ".p2align 6\n\t"
            ".Lwb_loop%=:\n\t"
            "s_setprio 3\n\t"
            "v_mov_b64 v[176:177], 0\n\t"
            "v_mov_b64 v[178:179], 0\n\t"
            "v_mov_b64 v[180:181], 0\n\t"
            "v_mov_b64 v[182:183], 0\n\t"
            "s_waitcnt lgkmcnt(8)\n\t"
            "v_smfmac_f32_16x16x64_bf16 v[176:179], v[168:171], v[144:151], v255\n\t"
            "ds_read_b128 v[168:171], v254 offset:128\n\t"
            "s_waitcnt lgkmcnt(5)\n\t"
            "v_smfmac_f32_16x16x64_bf16 v[180:183], v[172:175], v[152:159], v255\n\t"
            "ds_read_b64 v[144:145], v253 offset:24\n\t"
            "v_mov_b32 v249, s76\n\t"
            "v_mov_b64 v[184:185], 0\n\t"
            "v_mov_b64 v[186:187], 0\n\t"
            "v_mov_b64 v[188:189], 0\n\t"
            "v_mov_b64 v[190:191], 0\n\t"
            "v_cvt_pk_bf16_f32 v176, v176, v177\n\t"
            "v_cvt_pk_bf16_f32 v177, v178, v179\n\t"
            "v_cvt_pk_bf16_f32 v178, v180, v181\n\t"
            "v_cvt_pk_bf16_f32 v179, v182, v183\n\t"
            "v_add_u32 v236, s67, v250\n\t"
            "v_mov_b64 v[180:181], 0\n\t"
            "v_mov_b64 v[182:183], 0\n\t"
            "v_add_u32 v248, s67, v251\n\t"
            "ds_read_b32 v249, v249\n\t"
            "ds_read_b64 v[146:147], v253 offset:120\n\t"
            "ds_read_b64 v[148:149], v253 offset:1768\n\t"
            "ds_read_b64 v[150:151], v253 offset:1864\n\t"
            "ds_read_b128 v[172:175], v254 offset:192\n\t"
            "ds_read_b64 v[152:153], v253 offset:32\n\t"
            "s_waitcnt lgkmcnt(7)\n\t"
            "v_smfmac_f32_16x16x64_bf16 v[184:187], v[168:171], v[160:167], v255\n\t"
            "s_barrier\n\t"
            "ds_read_b64 v[154:155], v253 offset:128\n\t"
            "ds_read_b64 v[156:157], v253 offset:1776\n\t"
            "ds_read_b64 v[158:159], v253 offset:1872\n\t"
            "ds_read_b128 v[168:171], v254 offset:256\n\t"
            "ds_read_b64 v[160:161], v253 offset:40\n\t"
            "ds_read_b64 v[162:163], v253 offset:136\n\t"
            "ds_read_b64 v[164:165], v253 offset:1784\n\t"
            "v_cvt_pk_bf16_f32 v184, v184, v185\n\t"
            "v_cvt_pk_bf16_f32 v185, v186, v187\n\t"
            "ds_read_b64 v[166:167], v253 offset:1880\n\t"
            "s_waitcnt lgkmcnt(13)\n\t"
            "v_readfirstlane_b32 s26, v249\n\t"
            "ds_read_b32 v249, v252 offset:1024\n\t"
            "s_and_b32 s68, s26, 0x7ffffff\n\t"
            "s_add_u32 s68, s68, s78\n\t"
            "s_and_b32 s69, s26, 0x38000000\n\t"
            "s_or_b32 s69, s69, s39\n\t"
            "s_bitcmp1_b32 s26, 27\n\t"
            "s_cselect_b32 s79, 2.0, s68\n\t"
            "s_waitcnt lgkmcnt(10)\n\t"
            "v_smfmac_f32_16x16x64_bf16 v[188:191], v[172:175], v[144:151], v255\n\t"
            "s_and_b32 s57, s56, 3\n\t"
            "s_lshl_b32 s58, s57, 3\n\t"
            "s_lshr_b32 m0, s54, s58\n\t"
            "ds_read_b128 v[172:175], v254 offset:320\n\t"
            "s_lshl_b32 s58, s57, 4\n\t"
            "s_lshr_b64 s[36:37], s[50:51], s58\n\t"
            "s_and_b32 s57, s36, 0xffff\n\t"
            "s_add_u32 s57, s57, s67\n\t"
            "v_mfma_f32_4x4x4_16b_bf16 v[196:199], v[176:177], v[176:177], 0\n\t"
            "v_mfma_f32_4x4x4_16b_bf16 v[192:195], v[238:239], v[176:177], 0\n\t"
            "s_nop 0\n\t"
            "v_mfma_f32_4x4x4_16b_bf16 v[196:199], v[178:179], v[178:179], v[196:199]\n\t"
            "v_mfma_f32_4x4x4_16b_bf16 v[192:195], v[238:239], v[178:179], v[192:195]\n\t"
            "ds_read_b64 v[144:145], v253 offset:48\n\t"
            "s_lshr_b64 s[36:37], s[52:53], s58\n\t"
            "s_and_b32 s58, s36, 0xffff\n\t"
            "s_add_u32 s58, s58, s67\n\t"
            "ds_read_b64 v[146:147], v253 offset:144\n\t"
            "s_and_b32 s59, s50, 0xffff\n\t"
            "s_add_u32 s59, s59, s67\n\t"
            "s_and_b32 s38, s52, 0xffff\n\t"
            "s_add_u32 s38, s38, s67\n\t"
            "ds_read_b64 v[148:149], v253 offset:1792\n\t"
            "v_writelane_b32 v236, s59, m0\n\t"
            "v_writelane_b32 v236, s57, 63\n\t"
            "v_cvt_pk_bf16_f32 v186, v188, v189\n\t"
            "v_cvt_pk_bf16_f32 v187, v190, v191\n\t"
            "v_xor_b32 v237, 16, v236\n\t"
            "v_mov_b64 v[188:189], 0\n\t"
            "v_mov_b64 v[190:191], 0\n\t"
            "v_writelane_b32 v248, s38, m0\n\t"
            "s_waitcnt lgkmcnt(4)\n\t"
            "v_and_b32 v249, s69, v249\n\t"
            "v_writelane_b32 v248, s58, 63\n\t"
            "ds_read_b64 v[150:151], v253 offset:1888\n\t"
            "ds_write_b128 v236, v[176:179]\n\t"
            "v_mov_b64 v[176:177], 0\n\t"
            "v_mov_b64 v[178:179], 0\n\t"
            "s_nop 1\n\t"
            "v_smfmac_f32_16x16x64_bf16 v[176:179], v[168:171], v[152:159], v255\n\t"
            "ds_read_b128 v[168:171], v254 offset:384\n\t"
            "s_waitcnt lgkmcnt(6)\n\t"
            "v_smfmac_f32_16x16x64_bf16 v[180:183], v[172:175], v[160:167], v255\n\t"
            "ds_read_b64 v[152:153], v253 offset:56\n\t"
            "ds_read_b64 v[154:155], v253 offset:152\n\t"
            "v_mfma_f32_4x4x4_16b_bf16 v[196:199], v[184:185], v[184:185], v[196:199]\n\t"
            "v_mfma_f32_4x4x4_16b_bf16 v[192:195], v[238:239], v[184:185], v[192:195]\n\t"
            "s_nop 0\n\t"
            "v_mfma_f32_4x4x4_16b_bf16 v[196:199], v[186:187], v[186:187], v[196:199]\n\t"
            "v_mfma_f32_4x4x4_16b_bf16 v[192:195], v[238:239], v[186:187], v[192:195]\n\t"
            "ds_read_b64 v[156:157], v253 offset:1800\n\t"
            "v_cvt_pk_bf16_f32 v176, v176, v177\n\t"
            "v_cvt_pk_bf16_f32 v177, v178, v179\n\t"
            "v_cvt_pk_bf16_f32 v178, v180, v181\n\t"
            "v_cvt_pk_bf16_f32 v179, v182, v183\n\t"
            "v_mov_b64 v[180:181], 0\n\t"
            "v_mov_b64 v[182:183], 0\n\t"
            "ds_read_b64 v[158:159], v253 offset:1896\n\t"
            "ds_read_b128 v[172:175], v254 offset:448\n\t"
            "ds_read_b64 v[160:161], v253 offset:64\n\t"
            "ds_read_b64 v[162:163], v253 offset:160\n\t"
            "ds_read_b64 v[164:165], v253 offset:1808\n\t"
            "ds_read_b64 v[166:167], v253 offset:1904\n\t"
            "s_waitcnt lgkmcnt(14)\n\t"
            "ds_write_b128 v237, v[184:187]\n\t"
            "v_mov_b64 v[184:185], 0\n\t"
            "v_mov_b64 v[186:187], 0\n\t"
            "s_waitcnt lgkmcnt(10)\n\t"
            "s_nop 0\n\t"
            "v_smfmac_f32_16x16x64_bf16 v[184:187], v[168:171], v[144:151], v255\n\t"
            "ds_read_b128 v[168:171], v254 offset:512\n\t"
            "ds_read_b64 v[144:145], v253 offset:72\n\t"
            "ds_read_b64 v[146:147], v253 offset:168\n\t"
            "ds_read_b64 v[148:149], v253 offset:1816\n\t"
            "ds_read_b64 v[150:151], v253 offset:1912\n\t"
            "s_waitcnt lgkmcnt(10)\n\t"
            "v_smfmac_f32_16x16x64_bf16 v[188:191], v[172:175], v[152:159], v255\n\t"
            "ds_read_b128 v[172:175], v254 offset:576\n\t"
            "ds_read_b64 v[152:153], v253 offset:80\n\t"
            "v_mfma_f32_4x4x4_16b_bf16 v[196:199], v[176:177], v[176:177], v[196:199]\n\t"
            "v_mfma_f32_4x4x4_16b_bf16 v[192:195], v[238:239], v[176:177], v[192:195]\n\t"
            "s_nop 0\n\t"
            "v_mfma_f32_4x4x4_16b_bf16 v[196:199], v[178:179], v[178:179], v[196:199]\n\t"
            "v_mfma_f32_4x4x4_16b_bf16 v[192:195], v[238:239], v[178:179], v[192:195]\n\t"
            "ds_read_b64 v[154:155], v253 offset:176\n\t"
            "v_cvt_pk_bf16_f32 v184, v184, v185\n\t"
            "v_cvt_pk_bf16_f32 v185, v186, v187\n\t"
            "v_cvt_pk_bf16_f32 v186, v188, v189\n\t"
            "v_cvt_pk_bf16_f32 v187, v190, v191\n\t"
            "v_mov_b64 v[188:189], 0\n\t"
            "v_mov_b64 v[190:191], 0\n\t"
            "ds_read_b64 v[156:157], v253 offset:1824\n\t"
            "ds_read_b64 v[158:159], v253 offset:1920\n\t"
            "s_waitcnt lgkmcnt(14)\n\t"
            "ds_write_b128 v236, v[176:179] offset:128\n\t"
            "v_mov_b64 v[176:177], 0\n\t"
            "v_mov_b64 v[178:179], 0\n\t"
            "s_waitcnt lgkmcnt(10)\n\t"
            "s_nop 0\n\t"
            "v_smfmac_f32_16x16x64_bf16 v[176:179], v[168:171], v[160:167], v255\n\t"
            "ds_read_b128 v[168:171], v254 offset:640\n\t"
            "s_waitcnt lgkmcnt(6)\n\t"
            "v_smfmac_f32_16x16x64_bf16 v[180:183], v[172:175], v[144:151], v255\n\t"
            "ds_read_b64 v[160:161], v253 offset:88\n\t"
            "ds_read_b64 v[162:163], v253 offset:184\n\t"
            "v_mfma_f32_4x4x4_16b_bf16 v[196:199], v[184:185], v[184:185], v[196:199]\n\t"
            "v_mfma_f32_4x4x4_16b_bf16 v[192:195], v[238:239], v[184:185], v[192:195]\n\t"
            "s_nop 0\n\t"
            "v_mfma_f32_4x4x4_16b_bf16 v[196:199], v[186:187], v[186:187], v[196:199]\n\t"
            "v_mfma_f32_4x4x4_16b_bf16 v[192:195], v[238:239], v[186:187], v[192:195]\n\t"
            "ds_read_b64 v[164:165], v253 offset:1832\n\t"
            "v_cvt_pk_bf16_f32 v176, v176, v177\n\t"
            "v_cvt_pk_bf16_f32 v177, v178, v179\n\t"
            "v_cvt_pk_bf16_f32 v178, v180, v181\n\t"
            "v_cvt_pk_bf16_f32 v179, v182, v183\n\t"
            "v_lshlrev_b32 v180, 16, v202\n\t"
            "v_and_b32 v181, 0xffff0000, v202\n\t"
            "v_lshlrev_b32 v182, 16, v203\n\t"
            "v_and_b32 v183, 0xffff0000, v203\n\t"
            "ds_read_b64 v[166:167], v253 offset:1928\n\t"
            "ds_read_b128 v[172:175], v254 offset:704\n\t"
            "ds_read_b128 v[144:147], v242\n\t"
            "ds_read_b128 v[148:151], v243\n\t"
            "ds_write_b128 v237, v[184:187] offset:128\n\t"
            "v_mov_b64 v[184:185], 0\n\t"
            "v_mov_b64 v[186:187], 0\n\t"
            "s_waitcnt lgkmcnt(8)\n\t"
            "s_nop 0\n\t"
            "v_smfmac_f32_16x16x64_bf16 v[184:187], v[168:171], v[152:159], v255\n\t"
            "ds_read_b128 v[152:155], v242 offset:128\n\t"
            "ds_read_b128 v[156:159], v243 offset:128\n\t"
            "ds_read_b128 v[168:171], v242 offset:6144\n\t"
            "ds_write_b128 v236, v[176:179] offset:256\n\t"
            "v_add_u32 v236, s48, v244\n\t"
            "s_waitcnt lgkmcnt(7)\n\t"
            "v_smfmac_f32_16x16x64_bf16 v[188:191], v[172:175], v[160:167], v255\n\t"
            "ds_read_b128 v[160:163], v242 offset:256\n\t"
            "ds_read_b128 v[164:167], v243 offset:256\n\t"
            "v_mfma_f32_4x4x4_16b_bf16 v[196:199], v[176:177], v[176:177], v[196:199]\n\t"
            "v_mfma_f32_4x4x4_16b_bf16 v[192:195], v[238:239], v[176:177], v[192:195]\n\t"
            "s_nop 0\n\t"
            "v_mfma_f32_4x4x4_16b_bf16 v[196:199], v[178:179], v[178:179], v[196:199]\n\t"
            "v_mfma_f32_4x4x4_16b_bf16 v[192:195], v[238:239], v[178:179], v[192:195]\n\t"
            "ds_read_b128 v[172:175], v243 offset:6144\n\t"
            "v_cvt_pk_bf16_f32 v184, v184, v185\n\t"
            "v_cvt_pk_bf16_f32 v185, v186, v187\n\t"
            "v_cvt_pk_bf16_f32 v186, v188, v189\n\t"
            "v_cvt_pk_bf16_f32 v187, v190, v191\n\t"
            "v_lshlrev_b32 v176, 16, v200\n\t"
            "v_and_b32 v177, 0xffff0000, v200\n\t"
            "v_mfma_f32_4x4x4_16b_bf16 v[196:199], v[184:185], v[184:185], v[196:199]\n\t"
            "v_mfma_f32_4x4x4_16b_bf16 v[192:195], v[238:239], v[184:185], v[192:195]\n\t"
            "s_nop 0\n\t"
            "v_mfma_f32_4x4x4_16b_bf16 v[196:199], v[186:187], v[186:187], v[196:199]\n\t"
            "v_mfma_f32_4x4x4_16b_bf16 v[192:195], v[238:239], v[186:187], v[192:195]\n\t"
            "s_mov_b32 exec_hi, 0x3fffff\n\t"
            "s_add_u32 m0, s66, 0\n\t"
            "s_add_u32 s49, s79, 0\n\t"
            "buffer_load_dwordx4 v249, s[72:75], s49 offen lds\n\t"
            "s_mov_b32 exec_hi, -1\n\t"
            "v_lshlrev_b32 v178, 16, v201\n\t"
            "v_and_b32 v179, 0xffff0000, v201\n\t"
            "v_lshlrev_b32 v188, 16, v206\n\t"
            "v_and_b32 v189, 0xffff0000, v206\n\t"
            "s_waitcnt lgkmcnt(9)\n\t"
            "v_mfma_f32_16x16x32_bf16 v[176:179], v[0:3], v[144:147], v[176:179]\n\t"
            "s_setprio 3\n\t"
            "ds_read_b128 v[200:203], v236\n\t"
            "v_lshlrev_b32 v190, 16, v207\n\t"
            "s_waitcnt lgkmcnt(9)\n\t"
            "v_mfma_f32_16x16x32_bf16 v[176:179], v[4:7], v[148:151], v[176:179]\n\t"
            "s_mov_b32 exec_hi, 0x3fffff\n\t"
            "s_add_u32 m0, s66, 872\n\t"
            "s_add_u32 s49, s68, 196224\n\t"
            "buffer_load_dwordx4 v249, s[72:75], s49 offen lds\n\t"
            "s_mov_b32 exec_hi, -1\n\t"
            "s_waitcnt lgkmcnt(7)\n\t"
            "v_mfma_f32_16x16x32_bf16 v[176:179], v[8:11], v[152:155], v[176:179]\n\t"
            "s_mov_b32 exec_hi, 0x3fffff\n\t"
            "s_add_u32 m0, s66, 1744\n\t"
            "s_add_u32 s49, s68, 392448\n\t"
            "buffer_load_dwordx4 v249, s[72:75], s49 offen lds\n\t"
            "s_mov_b32 exec_hi, -1\n\t"
            "s_waitcnt lgkmcnt(6)\n\t"
            "v_mfma_f32_16x16x32_bf16 v[176:179], v[12:15], v[156:159], v[176:179]\n\t"
            "s_mov_b32 exec_hi, 0x3fffff\n\t"
            "s_add_u32 m0, s66, 2616\n\t"
            "s_add_u32 s49, s68, 588672\n\t"
            "buffer_load_dwordx4 v249, s[72:75], s49 offen lds\n\t"
            "s_mov_b32 exec_hi, -1\n\t"
            "s_waitcnt lgkmcnt(3)\n\t"
            "v_mfma_f32_16x16x32_bf16 v[176:179], v[16:19], v[160:163], v[176:179]\n\t"
            "s_mov_b32 exec_hi, 0x3fffff\n\t"
            "s_add_u32 m0, s66, 3488\n\t"
            "s_add_u32 s49, s68, 784896\n\t"
            "buffer_load_dwordx4 v249, s[72:75], s49 offen lds\n\t"
            "s_mov_b32 exec_hi, -1\n\t"
            "s_waitcnt lgkmcnt(2)\n\t"
            "v_mfma_f32_16x16x32_bf16 v[176:179], v[20:23], v[164:167], v[176:179]\n\t"
            "ds_write_b128 v237, v[184:187] offset:256\n\t"
            "v_mfma_f32_16x16x32_bf16 v[180:183], v[24:27], v[144:147], v[180:183]\n\t"
            "v_lshlrev_b32 v184, 16, v204\n\t"
            "v_and_b32 v185, 0xffff0000, v204\n\t"
            "v_mfma_f32_16x16x32_bf16 v[180:183], v[28:31], v[148:151], v[180:183]\n\t"
            "v_lshlrev_b32 v186, 16, v205\n\t"
            "v_and_b32 v187, 0xffff0000, v205\n\t"
            "v_mfma_f32_16x16x32_bf16 v[180:183], v[32:35], v[152:155], v[180:183]\n\t"
            "s_mov_b32 exec_hi, 0x3fffff\n\t"
            "s_add_u32 m0, s66, 4360\n\t"
            "s_add_u32 s49, s68, 981120\n\t"
            "buffer_load_dwordx4 v249, s[72:75], s49 offen lds\n\t"
            "s_mov_b32 exec_hi, -1\n\t"
            "v_mfma_f32_16x16x32_bf16 v[180:183], v[36:39], v[156:159], v[180:183]\n\t"
            "s_mov_b32 exec_hi, 0x3fffff\n\t"
            "s_add_u32 m0, s66, 5232\n\t"
            "s_add_u32 s49, s68, 1177344\n\t"
            "buffer_load_dwordx4 v249, s[72:75], s49 offen lds\n\t"
            "s_mov_b32 exec_hi, -1\n\t"
            "v_mfma_f32_16x16x32_bf16 v[180:183], v[40:43], v[160:163], v[180:183]\n\t"
            "v_and_b32 v191, 0xffff0000, v207\n\t"
            "v_cndmask_b32_e64 v193, v196, v197, s[60:61]\n\t"
            "v_mfma_f32_16x16x32_bf16 v[180:183], v[44:47], v[164:167], v[180:183]\n\t"
            "v_cndmask_b32_e64 v193, v193, v198, s[62:63]\n\t"
            "v_cndmask_b32_e64 v193, v193, v199, s[64:65]\n\t"
            "v_mfma_f32_16x16x32_bf16 v[184:187], v[48:51], v[144:147], v[184:187]\n\t"
            "ds_read_b128 v[196:199], v252\n\t"
            "v_lshlrev_b32 v194, 16, v209\n\t"
            "v_mfma_f32_16x16x32_bf16 v[184:187], v[52:55], v[148:151], v[184:187]\n\t"
            "s_mov_b32 exec_hi, 0x3fffff\n\t"
            "s_add_u32 m0, s66, 6104\n\t"
            "s_add_u32 s49, s68, 1373568\n\t"
            "buffer_load_dwordx4 v249, s[72:75], s49 offen lds\n\t"
            "s_mov_b32 exec_hi, -1\n\t"
            "v_mfma_f32_16x16x32_bf16 v[184:187], v[56:59], v[152:155], v[184:187]\n\t"
            "s_mov_b32 exec_hi, 0x3fffff\n\t"
            "s_add_u32 m0, s66, 6976\n\t"
            "s_add_u32 s49, s68, 1569792\n\t"
            "buffer_load_dwordx4 v249, s[72:75], s49 offen lds\n\t"
            "s_mov_b32 exec_hi, -1\n\t"
            "v_mfma_f32_16x16x32_bf16 v[184:187], v[60:63], v[156:159], v[184:187]\n\t"
            "s_bfe_u32 s57, s56, 0x20004\n\t"
            "s_lshl_b32 s58, s57, 3\n\t"
            "s_lshr_b32 s58, s55, s58\n\t"
            "s_lshl_b64 s[36:37], s[34:35], s58\n\t"
            "v_mfma_f32_16x16x32_bf16 v[184:187], v[64:67], v[160:163], v[184:187]\n\t"
            "s_bitcmp1_b32 s57, 1\n\t"
            "s_cselect_b32 s58, 1177344, 0\n\t"
            "s_bitcmp1_b32 s57, 0\n\t"
            "s_cselect_b32 s57, 2304, 0\n\t"
            "s_add_u32 s57, s57, s58\n\t"
            "v_mfma_f32_16x16x32_bf16 v[184:187], v[68:71], v[164:167], v[184:187]\n\t"
            "ds_write_b64 v248, v[192:193] offset:18816\n\t"
            "v_mfma_f32_16x16x32_bf16 v[188:191], v[72:75], v[144:147], v[188:191]\n\t"
            "v_lshlrev_b32 v192, 16, v208\n\t"
            "v_and_b32 v193, 0xffff0000, v208\n\t"
            "v_mfma_f32_16x16x32_bf16 v[188:191], v[76:79], v[148:151], v[188:191]\n\t"
            "v_and_b32 v195, 0xffff0000, v209\n\t"
            "s_waitcnt lgkmcnt(1)\n\t"
            "s_mov_b64 exec, s[36:37]\n\t"
            "v_add_u32 v196, s57, v196\n\t"
            "s_mov_b64 exec, -1\n\t"
            "v_mfma_f32_16x16x32_bf16 v[188:191], v[80:83], v[152:155], v[188:191]\n\t"
            "v_cvt_pk_bf16_f32 v176, v176, v177\n\t"
            "v_cvt_pk_bf16_f32 v177, v178, v179\n\t"
            "v_mfma_f32_16x16x32_bf16 v[188:191], v[84:87], v[156:159], v[188:191]\n\t"
            "v_cvt_pk_bf16_f32 v178, v180, v181\n\t"
            "v_cvt_pk_bf16_f32 v179, v182, v183\n\t"
            "v_mfma_f32_16x16x32_bf16 v[188:191], v[88:91], v[160:163], v[188:191]\n\t"
            "s_cbranch_vccnz .Lst0_%=\n\t"
            "buffer_store_dwordx4 v[176:179], v196, s[40:43], s45 offen\n\t"
            ".Lsr0_%=:\n\t"
            "v_mfma_f32_16x16x32_bf16 v[188:191], v[92:95], v[164:167], v[188:191]\n\t"
            "s_nop 0\n\t"
            "v_lshlrev_b32 v176, 16, v210\n\t"
            "v_and_b32 v177, 0xffff0000, v210\n\t"
            "v_mfma_f32_16x16x32_bf16 v[192:195], v[96:99], v[144:147], v[192:195]\n\t"
            "v_lshlrev_b32 v178, 16, v211\n\t"
            "v_and_b32 v179, 0xffff0000, v211\n\t"
            "v_mfma_f32_16x16x32_bf16 v[192:195], v[100:103], v[148:151], v[192:195]\n\t"
            "v_lshlrev_b32 v180, 16, v212\n\t"
            "v_and_b32 v181, 0xffff0000, v212\n\t"
            "v_mfma_f32_16x16x32_bf16 v[192:195], v[104:107], v[152:155], v[192:195]\n\t"
            "v_lshlrev_b32 v182, 16, v213\n\t"
            "v_and_b32 v183, 0xffff0000, v213\n\t"
            "v_mfma_f32_16x16x32_bf16 v[192:195], v[108:111], v[156:159], v[192:195]\n\t"
            "v_cvt_pk_bf16_f32 v184, v184, v185\n\t"
            "v_cvt_pk_bf16_f32 v185, v186, v187\n\t"
            "v_mfma_f32_16x16x32_bf16 v[192:195], v[112:115], v[160:163], v[192:195]\n\t"
            "v_cvt_pk_bf16_f32 v186, v188, v189\n\t"
            "v_cvt_pk_bf16_f32 v187, v190, v191\n\t"
            "v_mfma_f32_16x16x32_bf16 v[192:195], v[116:119], v[164:167], v[192:195]\n\t"
            "s_cbranch_vccnz .Lst1_%=\n\t"
            "buffer_store_dwordx4 v[184:187], v196, s[40:43], s45 offen offset:64\n\t"
            ".Lsr1_%=:\n\t"
            "v_mfma_f32_16x16x32_bf16 v[176:179], v[120:123], v[144:147], v[176:179]\n\t"
            "ds_read_b128 v[144:147], v242 offset:6272\n\t"
            "v_lshlrev_b32 v184, 16, v214\n\t"
            "v_mfma_f32_16x16x32_bf16 v[176:179], v[124:127], v[148:151], v[176:179]\n\t"
            "ds_read_b128 v[148:151], v243 offset:6272\n\t"
            "v_and_b32 v185, 0xffff0000, v214\n\t"
            "v_mfma_f32_16x16x32_bf16 v[176:179], v[128:131], v[152:155], v[176:179]\n\t"
            "ds_read_b128 v[152:155], v242 offset:6400\n\t"
            "v_lshlrev_b32 v186, 16, v215\n\t"
            "v_mfma_f32_16x16x32_bf16 v[176:179], v[132:135], v[156:159], v[176:179]\n\t"
            "ds_read_b128 v[156:159], v243 offset:6400\n\t"
            "v_and_b32 v187, 0xffff0000, v215\n\t"
            "v_mfma_f32_16x16x32_bf16 v[176:179], v[136:139], v[160:163], v[176:179]\n\t"
            "ds_read_b128 v[160:163], v242 offset:12288\n\t"
            "v_lshlrev_b32 v188, 16, v216\n\t"
            "v_mfma_f32_16x16x32_bf16 v[176:179], v[140:143], v[164:167], v[176:179]\n\t"
            "ds_read_b128 v[164:167], v243 offset:12288\n\t"
            "v_and_b32 v189, 0xffff0000, v216\n\t"
            "v_mfma_f32_16x16x32_bf16 v[180:183], v[0:3], v[168:171], v[180:183]\n\t"
            "ds_read_b128 v[212:215], v236 offset:6144\n\t"
            "v_lshlrev_b32 v190, 16, v217\n\t"
            "v_mfma_f32_16x16x32_bf16 v[180:183], v[4:7], v[172:175], v[180:183]\n\t"
            "v_and_b32 v191, 0xffff0000, v217\n\t"
            "v_cvt_pk_bf16_f32 v192, v192, v193\n\t"
            "v_cvt_pk_bf16_f32 v193, v194, v195\n\t"
            "s_waitcnt lgkmcnt(6)\n\t"
            "v_mfma_f32_16x16x32_bf16 v[180:183], v[8:11], v[144:147], v[180:183]\n\t"
            "v_cvt_pk_bf16_f32 v194, v176, v177\n\t"
            "v_cvt_pk_bf16_f32 v195, v178, v179\n\t"
            "s_waitcnt lgkmcnt(5)\n\t"
            "v_mfma_f32_16x16x32_bf16 v[180:183], v[12:15], v[148:151], v[180:183]\n\t"
            "s_cbranch_vccnz .Lst2_%=\n\t"
            "buffer_store_dwordx4 v[192:195], v196, s[40:43], s45 offen offset:128\n\t"
            ".Lsr2_%=:\n\t"
            "s_waitcnt lgkmcnt(4)\n\t"
            "v_mfma_f32_16x16x32_bf16 v[180:183], v[16:19], v[152:155], v[180:183]\n\t"
            "v_lshlrev_b32 v192, 16, v218\n\t"
            "v_and_b32 v193, 0xffff0000, v218\n\t"
            "s_waitcnt lgkmcnt(3)\n\t"
            "v_mfma_f32_16x16x32_bf16 v[180:183], v[20:23], v[156:159], v[180:183]\n\t"
            "v_lshlrev_b32 v194, 16, v219\n\t"
            "v_and_b32 v195, 0xffff0000, v219\n\t"
            "v_mfma_f32_16x16x32_bf16 v[184:187], v[24:27], v[168:171], v[184:187]\n\t"
            "v_lshlrev_b32 v176, 16, v220\n\t"
            "v_and_b32 v177, 0xffff0000, v220\n\t"
            "v_mfma_f32_16x16x32_bf16 v[184:187], v[28:31], v[172:175], v[184:187]\n\t"
            "v_lshlrev_b32 v178, 16, v221\n\t"
            "v_and_b32 v179, 0xffff0000, v221\n\t"
            "v_mfma_f32_16x16x32_bf16 v[184:187], v[32:35], v[144:147], v[184:187]\n\t"
            "v_cvt_pk_bf16_f32 v180, v180, v181\n\t"
            "v_cvt_pk_bf16_f32 v181, v182, v183\n\t"
            "v_mfma_f32_16x16x32_bf16 v[184:187], v[36:39], v[148:151], v[184:187]\n\t"
            "v_mov_b32 v249, s76\n\t"
            "v_add_u32 v237, s48, v245\n\t"
            "v_mfma_f32_16x16x32_bf16 v[184:187], v[40:43], v[152:155], v[184:187]\n\t"
            "ds_read_b32 v249, v249 offset:4\n\t"
            "v_add_u32 v248, s48, v246\n\t"
            "v_mfma_f32_16x16x32_bf16 v[184:187], v[44:47], v[156:159], v[184:187]\n\t"
            "ds_read_b128 v[204:207], v237\n\t"
            "v_mfma_f32_16x16x32_bf16 v[188:191], v[48:51], v[168:171], v[188:191]\n\t"
            "ds_read_b128 v[208:211], v248\n\t"
            "v_mfma_f32_16x16x32_bf16 v[188:191], v[52:55], v[172:175], v[188:191]\n\t"
            "ds_read_b128 v[216:219], v237 offset:6144\n\t"
            "v_mfma_f32_16x16x32_bf16 v[188:191], v[56:59], v[144:147], v[188:191]\n\t"
            "s_nop 1\n\t"
            "v_cvt_pk_bf16_f32 v182, v184, v185\n\t"
            "v_mfma_f32_16x16x32_bf16 v[188:191], v[60:63], v[148:151], v[188:191]\n\t"
            "v_cvt_pk_bf16_f32 v183, v186, v187\n\t"
            "v_lshlrev_b32 v184, 16, v224\n\t"
            "v_mfma_f32_16x16x32_bf16 v[188:191], v[64:67], v[152:155], v[188:191]\n\t"
            "s_cbranch_vccnz .Lst3_%=\n\t"
            "buffer_store_dwordx4 v[180:183], v197, s[40:43], s45 offen\n\t"
            ".Lsr3_%=:\n\t"
            "v_mfma_f32_16x16x32_bf16 v[188:191], v[68:71], v[156:159], v[188:191]\n\t"
            "s_nop 0\n\t"
            "v_lshlrev_b32 v180, 16, v222\n\t"
            "v_and_b32 v181, 0xffff0000, v222\n\t"
            "v_mfma_f32_16x16x32_bf16 v[192:195], v[72:75], v[168:171], v[192:195]\n\t"
            "v_lshlrev_b32 v182, 16, v223\n\t"
            "v_and_b32 v183, 0xffff0000, v223\n\t"
            "v_mfma_f32_16x16x32_bf16 v[192:195], v[76:79], v[172:175], v[192:195]\n\t"
            "ds_read_b128 v[220:223], v248 offset:6144\n\t"
            "v_and_b32 v185, 0xffff0000, v224\n\t"
            "v_mfma_f32_16x16x32_bf16 v[192:195], v[80:83], v[144:147], v[192:195]\n\t"
            "v_lshlrev_b32 v186, 16, v225\n\t"
            "v_and_b32 v187, 0xffff0000, v225\n\t"
            "v_mfma_f32_16x16x32_bf16 v[192:195], v[84:87], v[148:151], v[192:195]\n\t"
            "v_cvt_pk_bf16_f32 v188, v188, v189\n\t"
            "v_cvt_pk_bf16_f32 v189, v190, v191\n\t"
            "v_mfma_f32_16x16x32_bf16 v[192:195], v[88:91], v[152:155], v[192:195]\n\t"
            "s_waitcnt lgkmcnt(4)\n\t"
            "v_readfirstlane_b32 s44, v249\n\t"
            "v_mfma_f32_16x16x32_bf16 v[192:195], v[92:95], v[156:159], v[192:195]\n\t"
            "s_and_b32 s70, s44, 0x7ffffff\n\t"
            "s_add_u32 s70, s70, s77\n\t"
            "s_and_b32 s44, s44, 0x08000000\n\t"
            "s_or_b32 s44, s44, s39\n\t"
            "v_mfma_f32_16x16x32_bf16 v[176:179], v[96:99], v[168:171], v[176:179]\n\t"
            "ds_read_b32 v249, v252 offset:1028\n\t"
            "v_mfma_f32_16x16x32_bf16 v[176:179], v[100:103], v[172:175], v[176:179]\n\t"
            "s_nop 0\n\t"
            "v_cvt_pk_bf16_f32 v190, v192, v193\n\t"
            "v_cvt_pk_bf16_f32 v191, v194, v195\n\t"
            "v_mfma_f32_16x16x32_bf16 v[176:179], v[104:107], v[144:147], v[176:179]\n\t"
            "s_cbranch_vccnz .Lst4_%=\n\t"
            "buffer_store_dwordx4 v[188:191], v197, s[40:43], s45 offen offset:64\n\t"
            ".Lsr4_%=:\n\t"
            "v_mfma_f32_16x16x32_bf16 v[176:179], v[108:111], v[148:151], v[176:179]\n\t"
            "s_nop 0\n\t"
            "v_lshlrev_b32 v188, 16, v226\n\t"
            "v_and_b32 v189, 0xffff0000, v226\n\t"
            "v_mfma_f32_16x16x32_bf16 v[176:179], v[112:115], v[152:155], v[176:179]\n\t"
            "v_lshlrev_b32 v190, 16, v227\n\t"
            "v_and_b32 v191, 0xffff0000, v227\n\t"
            "v_mfma_f32_16x16x32_bf16 v[176:179], v[116:119], v[156:159], v[176:179]\n\t"
            "ds_read_b128 v[224:227], v236 offset:12288\n\t"
            "v_lshlrev_b32 v192, 16, v228\n\t"
            "v_mfma_f32_16x16x32_bf16 v[180:183], v[120:123], v[168:171], v[180:183]\n\t"
            "ds_read_b128 v[168:171], v242 offset:12416\n\t"
            "v_and_b32 v193, 0xffff0000, v228\n\t"
            "v_mfma_f32_16x16x32_bf16 v[180:183], v[124:127], v[172:175], v[180:183]\n\t"
            "ds_read_b128 v[172:175], v243 offset:12416\n\t"
            "v_lshlrev_b32 v194, 16, v229\n\t"
            "v_mfma_f32_16x16x32_bf16 v[180:183], v[128:131], v[144:147], v[180:183]\n\t"
            "ds_read_b128 v[144:147], v242 offset:12544\n\t"
            "v_and_b32 v195, 0xffff0000, v229\n\t"
            "v_mfma_f32_16x16x32_bf16 v[180:183], v[132:135], v[148:151], v[180:183]\n\t"
            "ds_read_b128 v[148:151], v243 offset:12544\n\t"
            "v_cvt_pk_bf16_f32 v176, v176, v177\n\t"
            "v_mfma_f32_16x16x32_bf16 v[180:183], v[136:139], v[152:155], v[180:183]\n\t"
            "s_waitcnt lgkmcnt(0)\n\t"
            "s_barrier\n\t"
            "v_cvt_pk_bf16_f32 v177, v178, v179\n\t"
            "v_and_b32 v249, s44, v249\n\t"
            "v_mfma_f32_16x16x32_bf16 v[180:183], v[140:143], v[156:159], v[180:183]\n\t"
            "v_mfma_f32_16x16x32_bf16 v[184:187], v[0:3], v[160:163], v[184:187]\n\t"
            "v_mfma_f32_16x16x32_bf16 v[184:187], v[4:7], v[164:167], v[184:187]\n\t"
            "v_mfma_f32_16x16x32_bf16 v[184:187], v[8:11], v[168:171], v[184:187]\n\t"
            "v_mfma_f32_16x16x32_bf16 v[184:187], v[12:15], v[172:175], v[184:187]\n\t"
            "v_mfma_f32_16x16x32_bf16 v[184:187], v[16:19], v[144:147], v[184:187]\n\t"
            "v_mfma_f32_16x16x32_bf16 v[184:187], v[20:23], v[148:151], v[184:187]\n\t"
            "s_nop 1\n\t"
            "v_cvt_pk_bf16_f32 v178, v180, v181\n\t"
            "v_mfma_f32_16x16x32_bf16 v[188:191], v[24:27], v[160:163], v[188:191]\n\t"
            "v_cvt_pk_bf16_f32 v179, v182, v183\n\t"
            "v_lshlrev_b32 v180, 16, v232\n\t"
            "v_mfma_f32_16x16x32_bf16 v[188:191], v[28:31], v[164:167], v[188:191]\n\t"
            "s_cbranch_vccnz .Lst5_%=\n\t"
            "buffer_store_dwordx4 v[176:179], v197, s[40:43], s45 offen offset:128\n\t"
            ".Lsr5_%=:\n\t"
            "v_mfma_f32_16x16x32_bf16 v[188:191], v[32:35], v[168:171], v[188:191]\n\t"
            "s_nop 0\n\t"
            "v_lshlrev_b32 v176, 16, v230\n\t"
            "v_and_b32 v177, 0xffff0000, v230\n\t"
            "v_mfma_f32_16x16x32_bf16 v[188:191], v[36:39], v[172:175], v[188:191]\n\t"
            "v_lshlrev_b32 v178, 16, v231\n\t"
            "v_and_b32 v179, 0xffff0000, v231\n\t"
            "v_mfma_f32_16x16x32_bf16 v[188:191], v[40:43], v[144:147], v[188:191]\n\t"
            "ds_read_b128 v[228:231], v237 offset:12288\n\t"
            "v_and_b32 v181, 0xffff0000, v232\n\t"
            "v_mfma_f32_16x16x32_bf16 v[188:191], v[44:47], v[148:151], v[188:191]\n\t"
            "v_lshlrev_b32 v182, 16, v233\n\t"
            "v_and_b32 v183, 0xffff0000, v233\n\t"
            "v_mfma_f32_16x16x32_bf16 v[192:195], v[48:51], v[160:163], v[192:195]\n\t"
            "v_cvt_pk_bf16_f32 v184, v184, v185\n\t"
            "v_cvt_pk_bf16_f32 v185, v186, v187\n\t"
            "v_mfma_f32_16x16x32_bf16 v[192:195], v[52:55], v[164:167], v[192:195]\n\t"
            "s_nop 1\n\t"
            "v_cvt_pk_bf16_f32 v186, v188, v189\n\t"
            "v_mfma_f32_16x16x32_bf16 v[192:195], v[56:59], v[168:171], v[192:195]\n\t"
            "v_cvt_pk_bf16_f32 v187, v190, v191\n\t"
            "v_mfma_f32_16x16x32_bf16 v[192:195], v[60:63], v[172:175], v[192:195]\n\t"
            "s_cbranch_vccnz .Lst6_%=\n\t"
            "buffer_store_dwordx4 v[184:187], v198, s[40:43], s45 offen\n\t"
            ".Lsr6_%=:\n\t"
            "v_mfma_f32_16x16x32_bf16 v[192:195], v[64:67], v[144:147], v[192:195]\n\t"
            "s_nop 0\n\t"
            "v_lshlrev_b32 v184, 16, v234\n\t"
            "v_and_b32 v185, 0xffff0000, v234\n\t"
            "v_mfma_f32_16x16x32_bf16 v[192:195], v[68:71], v[148:151], v[192:195]\n\t"
            "v_lshlrev_b32 v186, 16, v235\n\t"
            "v_and_b32 v187, 0xffff0000, v235\n\t"
            "v_mfma_f32_16x16x32_bf16 v[176:179], v[72:75], v[160:163], v[176:179]\n\t"
            "ds_read_b128 v[232:235], v248 offset:12288\n\t"
            "v_mfma_f32_16x16x32_bf16 v[176:179], v[76:79], v[164:167], v[176:179]\n\t"
            "v_mfma_f32_16x16x32_bf16 v[176:179], v[80:83], v[168:171], v[176:179]\n\t"
            "s_nop 1\n\t"
            "v_cvt_pk_bf16_f32 v192, v192, v193\n\t"
            "v_mfma_f32_16x16x32_bf16 v[176:179], v[84:87], v[172:175], v[176:179]\n\t"
            "v_cvt_pk_bf16_f32 v193, v194, v195\n\t"
            "v_mfma_f32_16x16x32_bf16 v[176:179], v[88:91], v[144:147], v[176:179]\n\t"
            "v_mfma_f32_16x16x32_bf16 v[176:179], v[92:95], v[148:151], v[176:179]\n\t"
            "v_mfma_f32_16x16x32_bf16 v[180:183], v[96:99], v[160:163], v[180:183]\n\t"
            "v_mfma_f32_16x16x32_bf16 v[180:183], v[100:103], v[164:167], v[180:183]\n\t"
            "v_mfma_f32_16x16x32_bf16 v[180:183], v[104:107], v[168:171], v[180:183]\n\t"
            "v_mfma_f32_16x16x32_bf16 v[180:183], v[108:111], v[172:175], v[180:183]\n\t"
            "v_mfma_f32_16x16x32_bf16 v[180:183], v[112:115], v[144:147], v[180:183]\n\t"
            "v_mfma_f32_16x16x32_bf16 v[180:183], v[116:119], v[148:151], v[180:183]\n\t"
            "s_nop 1\n\t"
            "v_cvt_pk_bf16_f32 v194, v176, v177\n\t"
            "v_mfma_f32_16x16x32_bf16 v[184:187], v[120:123], v[160:163], v[184:187]\n\t"
            "v_cvt_pk_bf16_f32 v195, v178, v179\n\t"
            "v_mfma_f32_16x16x32_bf16 v[184:187], v[124:127], v[164:167], v[184:187]\n\t"
            "s_cbranch_vccnz .Lst7_%=\n\t"
            "buffer_store_dwordx4 v[192:195], v198, s[40:43], s45 offen offset:64\n\t"
            ".Lsr7_%=:\n\t"
            "v_mfma_f32_16x16x32_bf16 v[184:187], v[128:131], v[168:171], v[184:187]\n\t"
            "s_waitcnt vmcnt(8)\n\t"
            "ds_read_b128 v[168:171], v254\n\t"
            "v_cvt_pk_bf16_f32 v180, v180, v181\n\t"
            "v_mfma_f32_16x16x32_bf16 v[184:187], v[132:135], v[172:175], v[184:187]\n\t"
            "ds_read_b128 v[172:175], v254 offset:64\n\t"
            "v_cvt_pk_bf16_f32 v181, v182, v183\n\t"
            "v_mfma_f32_16x16x32_bf16 v[184:187], v[136:139], v[144:147], v[184:187]\n\t"
            "ds_read_b64 v[144:145], v253\n\t"
            "v_mfma_f32_16x16x32_bf16 v[184:187], v[140:143], v[148:151], v[184:187]\n\t"
            "ds_read_b64 v[146:147], v253 offset:96\n\t"
            "ds_read_b64 v[148:149], v253 offset:1744\n\t"
            "ds_read_b64 v[150:151], v253 offset:1840\n\t"
            "ds_read_b64 v[152:153], v253 offset:8\n\t"
            "ds_read_b64 v[154:155], v253 offset:104\n\t"
            "ds_read_b64 v[156:157], v253 offset:1752\n\t"
            "ds_read_b64 v[158:159], v253 offset:1848\n\t"
            "ds_read_b64 v[160:161], v253 offset:16\n\t"
            "v_cvt_pk_bf16_f32 v182, v184, v185\n\t"
            "v_cvt_pk_bf16_f32 v183, v186, v187\n\t"
            "ds_read_b64 v[162:163], v253 offset:112\n\t"
            "ds_read_b64 v[164:165], v253 offset:1760\n\t"
            "s_waitcnt lgkmcnt(14)\n\t"
            "ds_read_b64 v[166:167], v253 offset:1856\n\t"
            "s_cbranch_vccnz .Lst8_%=\n\t"
            "buffer_store_dwordx4 v[180:183], v198, s[40:43], s45 offen offset:128\n\t"
            ".Lsr8_%=:\n\t"
            "s_mov_b32 m0, s32\n\t"
            "s_add_u32 s49, s70, s25\n\t"
            "buffer_load_dword v249, s[72:75], s70 offen lds\n\t"
            "buffer_load_dword v249, s[72:75], s49 offen lds\n\t"
            "s_add_u32 s45, s46, s27\n\t"
            "s_mov_b32 s46, s47\n\t"
            "s_mov_b32 s47, s68\n\t"
            "s_xor_b32 s48, s48, s71\n\t"
            "s_xor_b32 s67, s67, s71\n\t"
            "s_add_u32 s76, s76, 4\n\t"
            "s_lshl_b32 s56, s56, 2\n\t"
            "s_lshr_b32 s49, s26, 30\n\t"
            "s_or_b32 s56, s56, s49\n\t"
            "s_cmp_ge_u32 s76, s24\n\t"
            "s_cselect_b64 vcc, -1, 0\n\t"
            "s_cmp_lt_u32 s46, 0x7000000\n\t"
            "s_cbranch_scc1 .Lwb_loop%=\n\t"
            "s_branch .Lsx%=\n\t"
            ".Lst0_%=:\n\t"
            "buffer_store_dwordx4 v[176:179], v196, s[40:43], s45 offen sc1\n\t"
            "s_branch .Lsr0_%=\n\t"
            ".Lst1_%=:\n\t"
            "buffer_store_dwordx4 v[184:187], v196, s[40:43], s45 offen offset:64 sc1\n\t"
            "s_branch .Lsr1_%=\n\t"
            ".Lst2_%=:\n\t"
            "buffer_store_dwordx4 v[192:195], v196, s[40:43], s45 offen offset:128 sc1\n\t"
            "s_branch .Lsr2_%=\n\t"
            ".Lst3_%=:\n\t"
            "buffer_store_dwordx4 v[180:183], v197, s[40:43], s45 offen sc1\n\t"
            "s_branch .Lsr3_%=\n\t"
            ".Lst4_%=:\n\t"
            "buffer_store_dwordx4 v[188:191], v197, s[40:43], s45 offen offset:64 sc1\n\t"
            "s_branch .Lsr4_%=\n\t"
            ".Lst5_%=:\n\t"
            "buffer_store_dwordx4 v[176:179], v197, s[40:43], s45 offen offset:128 sc1\n\t"
            "s_branch .Lsr5_%=\n\t"
            ".Lst6_%=:\n\t"
            "buffer_store_dwordx4 v[184:187], v198, s[40:43], s45 offen sc1\n\t"
            "s_branch .Lsr6_%=\n\t"
            ".Lst7_%=:\n\t"
            "buffer_store_dwordx4 v[192:195], v198, s[40:43], s45 offen offset:64 sc1\n\t"
            "s_branch .Lsr7_%=\n\t"
            ".Lst8_%=:\n\t"
            "buffer_store_dwordx4 v[180:183], v198, s[40:43], s45 offen offset:128 sc1\n\t"
            "s_branch .Lsr8_%=\n\t"
            ".Lsx%=:\n\t"
            "s_waitcnt lgkmcnt(0)\n\t"
            "s_nop 7\n\t"
            "s_setprio 3\n\t"
            "s_nop 7\n\t"
            "ds_read_b128 v[196:199], v252\n\t"
            "s_bfe_u32 s57, s56, 0x20004\n\t"
            "s_lshl_b32 s58, s57, 3\n\t"
            "s_lshr_b32 s58, s55, s58\n\t"
            "s_lshl_b64 s[36:37], s[34:35], s58\n\t"
            "s_bitcmp1_b32 s57, 1\n\t"
            "s_cselect_b32 s58, 1177344, 0\n\t"
            "s_bitcmp1_b32 s57, 0\n\t"
            "s_cselect_b32 s57, 2304, 0\n\t"
            "s_add_u32 s57, s57, s58\n\t"
            "s_waitcnt lgkmcnt(0)\n\t"
            "s_mov_b64 exec, s[36:37]\n\t"
            "v_add_u32 v196, s57, v196\n\t"
            "s_mov_b64 exec, -1\n\t"
            "s_nop 7\n\t"
            "v_mov_b32 v144, s23\n\t"
            ".Ldp%=:\n\t"
            "ds_read_b128 v[148:151], v144 offset:96\n\t"
            "s_waitcnt lgkmcnt(0)\n\t"
            "v_or_b32 v148, v148, v149\n\t"
            "v_or3_b32 v148, v148, v150, v151\n\t"
            "v_cmp_gt_i32 vcc, 0, v148\n\t"
            "s_nop 4\n\t"
            "s_cbranch_vccnz .Ldp%=\n\t"
            "s_nop 7\n\t"
            "ds_read_b128 v[184:187], v242\n\t"
            "v_lshlrev_b32 v152, 16, v200\n\t"
            "v_and_b32 v153, 0xffff0000, v200\n\t"
            "v_lshlrev_b32 v154, 16, v201\n\t"
            "v_and_b32 v155, 0xffff0000, v201\n\t"
            "v_lshlrev_b32 v156, 16, v202\n\t"
            "v_and_b32 v157, 0xffff0000, v202\n\t"
            "v_lshlrev_b32 v158, 16, v203\n\t"
            "v_and_b32 v159, 0xffff0000, v203\n\t"
            "v_lshlrev_b32 v160, 16, v204\n\t"
            "v_and_b32 v161, 0xffff0000, v204\n\t"
            "v_lshlrev_b32 v162, 16, v205\n\t"
            "v_and_b32 v163, 0xffff0000, v205\n\t"
            "v_lshlrev_b32 v164, 16, v206\n\t"
            "v_and_b32 v165, 0xffff0000, v206\n\t"
            "v_lshlrev_b32 v166, 16, v207\n\t"
            "v_and_b32 v167, 0xffff0000, v207\n\t"
            "v_lshlrev_b32 v176, 16, v208\n\t"
            "v_and_b32 v177, 0xffff0000, v208\n\t"
            "v_lshlrev_b32 v178, 16, v209\n\t"
            "v_and_b32 v179, 0xffff0000, v209\n\t"
            "v_lshlrev_b32 v180, 16, v210\n\t"
            "v_and_b32 v181, 0xffff0000, v210\n\t"
            "v_lshlrev_b32 v182, 16, v211\n\t"
            "v_and_b32 v183, 0xffff0000, v211\n\t"
            "v_lshlrev_b32 v144, 16, v212\n\t"
            "v_and_b32 v145, 0xffff0000, v212\n\t"
            "v_lshlrev_b32 v146, 16, v213\n\t"
            "v_and_b32 v147, 0xffff0000, v213\n\t"
            "v_lshlrev_b32 v148, 16, v214\n\t"
            "v_and_b32 v149, 0xffff0000, v214\n\t"
            "s_waitcnt lgkmcnt(0)\n\t"
            "v_mfma_f32_16x16x32_bf16 v[152:155], v[0:3], v[184:187], v[152:155]\n\t"
            "ds_read_b128 v[188:191], v243\n\t"
            "v_lshlrev_b32 v150, 16, v215\n\t"
            "v_mfma_f32_16x16x32_bf16 v[156:159], v[24:27], v[184:187], v[156:159]\n\t"
            "ds_read_b128 v[192:195], v242 offset:128\n\t"
            "v_and_b32 v151, 0xffff0000, v215\n\t"
            "v_mfma_f32_16x16x32_bf16 v[160:163], v[48:51], v[184:187], v[160:163]\n\t"
            "v_lshlrev_b32 v168, 16, v216\n\t"
            "v_and_b32 v169, 0xffff0000, v216\n\t"
            "v_mfma_f32_16x16x32_bf16 v[164:167], v[72:75], v[184:187], v[164:167]\n\t"
            "v_lshlrev_b32 v170, 16, v217\n\t"
            "v_and_b32 v171, 0xffff0000, v217\n\t"
            "v_mfma_f32_16x16x32_bf16 v[176:179], v[96:99], v[184:187], v[176:179]\n\t"
            "v_lshlrev_b32 v172, 16, v218\n\t"
            "v_and_b32 v173, 0xffff0000, v218\n\t"
            "v_mfma_f32_16x16x32_bf16 v[180:183], v[120:123], v[184:187], v[180:183]\n\t"
            "ds_read_b128 v[184:187], v243 offset:128\n\t"
            "v_lshlrev_b32 v174, 16, v219\n\t"
            "v_and_b32 v175, 0xffff0000, v219\n\t"
            "s_waitcnt lgkmcnt(2)\n\t"
            "v_mfma_f32_16x16x32_bf16 v[152:155], v[4:7], v[188:191], v[152:155]\n\t"
            "v_mfma_f32_16x16x32_bf16 v[156:159], v[28:31], v[188:191], v[156:159]\n\t"
            "v_mfma_f32_16x16x32_bf16 v[160:163], v[52:55], v[188:191], v[160:163]\n\t"
            "v_mfma_f32_16x16x32_bf16 v[164:167], v[76:79], v[188:191], v[164:167]\n\t"
            "v_mfma_f32_16x16x32_bf16 v[176:179], v[100:103], v[188:191], v[176:179]\n\t"
            "v_mfma_f32_16x16x32_bf16 v[180:183], v[124:127], v[188:191], v[180:183]\n\t"
            "ds_read_b128 v[188:191], v242 offset:256\n\t"
            "s_waitcnt lgkmcnt(2)\n\t"
            "v_mfma_f32_16x16x32_bf16 v[152:155], v[8:11], v[192:195], v[152:155]\n\t"
            "v_mfma_f32_16x16x32_bf16 v[156:159], v[32:35], v[192:195], v[156:159]\n\t"
            "v_mfma_f32_16x16x32_bf16 v[160:163], v[56:59], v[192:195], v[160:163]\n\t"
            "v_mfma_f32_16x16x32_bf16 v[164:167], v[80:83], v[192:195], v[164:167]\n\t"
            "v_mfma_f32_16x16x32_bf16 v[176:179], v[104:107], v[192:195], v[176:179]\n\t"
            "v_mfma_f32_16x16x32_bf16 v[180:183], v[128:131], v[192:195], v[180:183]\n\t"
            "ds_read_b128 v[192:195], v243 offset:256\n\t"
            "s_waitcnt lgkmcnt(2)\n\t"
            "v_mfma_f32_16x16x32_bf16 v[152:155], v[12:15], v[184:187], v[152:155]\n\t"
            "v_mfma_f32_16x16x32_bf16 v[156:159], v[36:39], v[184:187], v[156:159]\n\t"
            "v_mfma_f32_16x16x32_bf16 v[160:163], v[60:63], v[184:187], v[160:163]\n\t"
            "v_mfma_f32_16x16x32_bf16 v[164:167], v[84:87], v[184:187], v[164:167]\n\t"
            "v_mfma_f32_16x16x32_bf16 v[176:179], v[108:111], v[184:187], v[176:179]\n\t"
            "v_mfma_f32_16x16x32_bf16 v[180:183], v[132:135], v[184:187], v[180:183]\n\t"
            "ds_read_b128 v[184:187], v242 offset:6144\n\t"
            "s_waitcnt lgkmcnt(2)\n\t"
            "v_mfma_f32_16x16x32_bf16 v[152:155], v[16:19], v[188:191], v[152:155]\n\t"
            "v_mfma_f32_16x16x32_bf16 v[156:159], v[40:43], v[188:191], v[156:159]\n\t"
            "v_mfma_f32_16x16x32_bf16 v[160:163], v[64:67], v[188:191], v[160:163]\n\t"
            "v_mfma_f32_16x16x32_bf16 v[164:167], v[88:91], v[188:191], v[164:167]\n\t"
            "v_mfma_f32_16x16x32_bf16 v[176:179], v[112:115], v[188:191], v[176:179]\n\t"
            "v_mfma_f32_16x16x32_bf16 v[180:183], v[136:139], v[188:191], v[180:183]\n\t"
            "ds_read_b128 v[188:191], v243 offset:6144\n\t"
            "s_waitcnt lgkmcnt(2)\n\t"
            "v_mfma_f32_16x16x32_bf16 v[152:155], v[20:23], v[192:195], v[152:155]\n\t"
            "v_mfma_f32_16x16x32_bf16 v[156:159], v[44:47], v[192:195], v[156:159]\n\t"
            "v_mfma_f32_16x16x32_bf16 v[160:163], v[68:71], v[192:195], v[160:163]\n\t"
            "v_mfma_f32_16x16x32_bf16 v[164:167], v[92:95], v[192:195], v[164:167]\n\t"
            "v_mfma_f32_16x16x32_bf16 v[176:179], v[116:119], v[192:195], v[176:179]\n\t"
            "v_mfma_f32_16x16x32_bf16 v[180:183], v[140:143], v[192:195], v[180:183]\n\t"
            "ds_read_b128 v[192:195], v242 offset:6272\n\t"
            "s_waitcnt lgkmcnt(2)\n\t"
            "v_mfma_f32_16x16x32_bf16 v[144:147], v[0:3], v[184:187], v[144:147]\n\t"
            "v_cvt_pk_bf16_f32 v152, v152, v153\n\t"
            "v_cvt_pk_bf16_f32 v153, v154, v155\n\t"
            "v_mfma_f32_16x16x32_bf16 v[148:151], v[24:27], v[184:187], v[148:151]\n\t"
            "v_cvt_pk_bf16_f32 v160, v160, v161\n\t"
            "v_cvt_pk_bf16_f32 v161, v162, v163\n\t"
            "v_mfma_f32_16x16x32_bf16 v[168:171], v[48:51], v[184:187], v[168:171]\n\t"
            "v_cvt_pk_bf16_f32 v154, v156, v157\n\t"
            "v_cvt_pk_bf16_f32 v155, v158, v159\n\t"
            "v_mfma_f32_16x16x32_bf16 v[172:175], v[72:75], v[184:187], v[172:175]\n\t"
            "v_cvt_pk_bf16_f32 v162, v164, v165\n\t"
            "v_cvt_pk_bf16_f32 v163, v166, v167\n\t"
            "s_waitcnt lgkmcnt(1)\n\t"
            "v_mfma_f32_16x16x32_bf16 v[144:147], v[4:7], v[188:191], v[144:147]\n\t"
            "v_lshlrev_b32 v156, 16, v220\n\t"
            "v_and_b32 v157, 0xffff0000, v220\n\t"
            "v_mfma_f32_16x16x32_bf16 v[148:151], v[28:31], v[188:191], v[148:151]\n\t"
            "v_lshlrev_b32 v158, 16, v221\n\t"
            "v_and_b32 v159, 0xffff0000, v221\n\t"
            "v_mfma_f32_16x16x32_bf16 v[168:171], v[52:55], v[188:191], v[168:171]\n\t"
            "v_lshlrev_b32 v164, 16, v222\n\t"
            "v_and_b32 v165, 0xffff0000, v222\n\t"
            "v_mfma_f32_16x16x32_bf16 v[156:159], v[96:99], v[184:187], v[156:159]\n\t"
            "v_lshlrev_b32 v166, 16, v223\n\t"
            "v_and_b32 v167, 0xffff0000, v223\n\t"
            "v_mfma_f32_16x16x32_bf16 v[172:175], v[76:79], v[188:191], v[172:175]\n\t"
            "buffer_store_dwordx4 v[152:155], v196, s[40:43], s45 offen sc1\n\t"
            "v_mfma_f32_16x16x32_bf16 v[164:167], v[120:123], v[184:187], v[164:167]\n\t"
            "ds_read_b128 v[184:187], v243 offset:6272\n\t"
            "v_cvt_pk_bf16_f32 v176, v176, v177\n\t"
            "v_mfma_f32_16x16x32_bf16 v[156:159], v[100:103], v[188:191], v[156:159]\n\t"
            "buffer_store_dwordx4 v[160:163], v196, s[40:43], s45 offen offset:64 sc1\n\t"
            "v_mfma_f32_16x16x32_bf16 v[164:167], v[124:127], v[188:191], v[164:167]\n\t"
            "ds_read_b128 v[188:191], v242 offset:6400\n\t"
            "v_cvt_pk_bf16_f32 v177, v178, v179\n\t"
            "s_waitcnt lgkmcnt(2)\n\t"
            "v_mfma_f32_16x16x32_bf16 v[144:147], v[8:11], v[192:195], v[144:147]\n\t"
            "v_cvt_pk_bf16_f32 v178, v180, v181\n\t"
            "v_cvt_pk_bf16_f32 v179, v182, v183\n\t"
            "v_mfma_f32_16x16x32_bf16 v[148:151], v[32:35], v[192:195], v[148:151]\n\t"
            "buffer_store_dwordx4 v[176:179], v196, s[40:43], s45 offen offset:128 sc1\n\t"
            "v_mfma_f32_16x16x32_bf16 v[168:171], v[56:59], v[192:195], v[168:171]\n\t"
            "v_lshlrev_b32 v152, 16, v224\n\t"
            "v_and_b32 v153, 0xffff0000, v224\n\t"
            "v_mfma_f32_16x16x32_bf16 v[172:175], v[80:83], v[192:195], v[172:175]\n\t"
            "v_lshlrev_b32 v154, 16, v225\n\t"
            "v_and_b32 v155, 0xffff0000, v225\n\t"
            "v_mfma_f32_16x16x32_bf16 v[156:159], v[104:107], v[192:195], v[156:159]\n\t"
            "v_lshlrev_b32 v180, 16, v226\n\t"
            "v_and_b32 v181, 0xffff0000, v226\n\t"
            "v_mfma_f32_16x16x32_bf16 v[164:167], v[128:131], v[192:195], v[164:167]\n\t"
            "ds_read_b128 v[192:195], v243 offset:6400\n\t"
            "v_lshlrev_b32 v182, 16, v227\n\t"
            "s_waitcnt lgkmcnt(2)\n\t"
            "v_mfma_f32_16x16x32_bf16 v[144:147], v[12:15], v[184:187], v[144:147]\n\t"
            "v_and_b32 v183, 0xffff0000, v227\n\t"
            "v_lshlrev_b32 v160, 16, v228\n\t"
            "v_mfma_f32_16x16x32_bf16 v[148:151], v[36:39], v[184:187], v[148:151]\n\t"
            "v_and_b32 v161, 0xffff0000, v228\n\t"
            "v_lshlrev_b32 v162, 16, v229\n\t"
            "s_waitcnt lgkmcnt(1)\n\t"
            "v_mfma_f32_16x16x32_bf16 v[144:147], v[16:19], v[188:191], v[144:147]\n\t"
            "v_and_b32 v163, 0xffff0000, v229\n\t"
            "v_lshlrev_b32 v176, 16, v230\n\t"
            "v_mfma_f32_16x16x32_bf16 v[168:171], v[60:63], v[184:187], v[168:171]\n\t"
            "v_and_b32 v177, 0xffff0000, v230\n\t"
            "v_lshlrev_b32 v178, 16, v231\n\t"
            "v_mfma_f32_16x16x32_bf16 v[148:151], v[40:43], v[188:191], v[148:151]\n\t"
            "v_and_b32 v179, 0xffff0000, v231\n\t"
            "v_mfma_f32_16x16x32_bf16 v[172:175], v[84:87], v[184:187], v[172:175]\n\t"
            "v_mfma_f32_16x16x32_bf16 v[168:171], v[64:67], v[188:191], v[168:171]\n\t"
            "v_mfma_f32_16x16x32_bf16 v[156:159], v[108:111], v[184:187], v[156:159]\n\t"
            "s_waitcnt lgkmcnt(0)\n\t"
            "v_mfma_f32_16x16x32_bf16 v[144:147], v[20:23], v[192:195], v[144:147]\n\t"
            "v_mfma_f32_16x16x32_bf16 v[148:151], v[44:47], v[192:195], v[148:151]\n\t"
            "v_mfma_f32_16x16x32_bf16 v[164:167], v[132:135], v[184:187], v[164:167]\n\t"
            "ds_read_b128 v[184:187], v242 offset:12288\n\t"
            "v_mfma_f32_16x16x32_bf16 v[172:175], v[88:91], v[188:191], v[172:175]\n\t"
            "v_mfma_f32_16x16x32_bf16 v[168:171], v[68:71], v[192:195], v[168:171]\n\t"
            "v_mfma_f32_16x16x32_bf16 v[156:159], v[112:115], v[188:191], v[156:159]\n\t"
            "s_nop 1\n\t"
            "v_cvt_pk_bf16_f32 v144, v144, v145\n\t"
            "v_mfma_f32_16x16x32_bf16 v[164:167], v[136:139], v[188:191], v[164:167]\n\t"
            "ds_read_b128 v[188:191], v243 offset:12288\n\t"
            "v_cvt_pk_bf16_f32 v145, v146, v147\n\t"
            "v_mfma_f32_16x16x32_bf16 v[172:175], v[92:95], v[192:195], v[172:175]\n\t"
            "v_cvt_pk_bf16_f32 v146, v148, v149\n\t"
            "v_cvt_pk_bf16_f32 v147, v150, v151\n\t"
            "v_mfma_f32_16x16x32_bf16 v[156:159], v[116:119], v[192:195], v[156:159]\n\t"
            "buffer_store_dwordx4 v[144:147], v197, s[40:43], s45 offen sc1\n\t"
            "v_mfma_f32_16x16x32_bf16 v[164:167], v[140:143], v[192:195], v[164:167]\n\t"
            "ds_read_b128 v[192:195], v242 offset:12416\n\t"
            "v_cvt_pk_bf16_f32 v168, v168, v169\n\t"
            "s_waitcnt lgkmcnt(2)\n\t"
            "v_mfma_f32_16x16x32_bf16 v[152:155], v[0:3], v[184:187], v[152:155]\n\t"
            "v_cvt_pk_bf16_f32 v169, v170, v171\n\t"
            "v_cvt_pk_bf16_f32 v170, v172, v173\n\t"
            "v_mfma_f32_16x16x32_bf16 v[180:183], v[24:27], v[184:187], v[180:183]\n\t"
            "v_cvt_pk_bf16_f32 v171, v174, v175\n\t"
            "v_cvt_pk_bf16_f32 v156, v156, v157\n\t"
            "v_mfma_f32_16x16x32_bf16 v[160:163], v[48:51], v[184:187], v[160:163]\n\t"
            "buffer_store_dwordx4 v[168:171], v197, s[40:43], s45 offen offset:64 sc1\n\t"
            "v_mfma_f32_16x16x32_bf16 v[176:179], v[72:75], v[184:187], v[176:179]\n\t"
            "v_cvt_pk_bf16_f32 v157, v158, v159\n\t"
            "v_cvt_pk_bf16_f32 v158, v164, v165\n\t"
            "s_waitcnt lgkmcnt(1)\n\t"
            "v_mfma_f32_16x16x32_bf16 v[152:155], v[4:7], v[188:191], v[152:155]\n\t"
            "v_cvt_pk_bf16_f32 v159, v166, v167\n\t"
            "v_lshlrev_b32 v164, 16, v232\n\t"
            "v_mfma_f32_16x16x32_bf16 v[180:183], v[28:31], v[188:191], v[180:183]\n\t"
            "buffer_store_dwordx4 v[156:159], v197, s[40:43], s45 offen offset:128 sc1\n\t"
            "v_mfma_f32_16x16x32_bf16 v[160:163], v[52:55], v[188:191], v[160:163]\n\t"
            "v_and_b32 v165, 0xffff0000, v232\n\t"
            "v_lshlrev_b32 v166, 16, v233\n\t"
            "v_mfma_f32_16x16x32_bf16 v[176:179], v[76:79], v[188:191], v[176:179]\n\t"
            "v_and_b32 v167, 0xffff0000, v233\n\t"
            "v_lshlrev_b32 v156, 16, v234\n\t"
            "s_waitcnt lgkmcnt(0)\n\t"
            "v_mfma_f32_16x16x32_bf16 v[152:155], v[8:11], v[192:195], v[152:155]\n\t"
            "v_and_b32 v157, 0xffff0000, v234\n\t"
            "v_lshlrev_b32 v158, 16, v235\n\t"
            "v_mfma_f32_16x16x32_bf16 v[164:167], v[96:99], v[184:187], v[164:167]\n\t"
            "v_and_b32 v159, 0xffff0000, v235\n\t"
            "v_mfma_f32_16x16x32_bf16 v[164:167], v[100:103], v[188:191], v[164:167]\n\t"
            "s_nop 0\n\t"
            "v_mfma_f32_16x16x32_bf16 v[156:159], v[120:123], v[184:187], v[156:159]\n\t"
            "ds_read_b128 v[184:187], v243 offset:12416\n\t"
            "v_mfma_f32_16x16x32_bf16 v[156:159], v[124:127], v[188:191], v[156:159]\n\t"
            "ds_read_b128 v[188:191], v242 offset:12544\n\t"
            "v_mfma_f32_16x16x32_bf16 v[180:183], v[32:35], v[192:195], v[180:183]\n\t"
            "v_mfma_f32_16x16x32_bf16 v[160:163], v[56:59], v[192:195], v[160:163]\n\t"
            "v_mfma_f32_16x16x32_bf16 v[176:179], v[80:83], v[192:195], v[176:179]\n\t"
            "v_mfma_f32_16x16x32_bf16 v[164:167], v[104:107], v[192:195], v[164:167]\n\t"
            "v_mfma_f32_16x16x32_bf16 v[156:159], v[128:131], v[192:195], v[156:159]\n\t"
            "ds_read_b128 v[192:195], v243 offset:12544\n\t"
            "s_waitcnt lgkmcnt(2)\n\t"
            "v_mfma_f32_16x16x32_bf16 v[152:155], v[12:15], v[184:187], v[152:155]\n\t"
            "v_mfma_f32_16x16x32_bf16 v[180:183], v[36:39], v[184:187], v[180:183]\n\t"
            "s_waitcnt lgkmcnt(1)\n\t"
            "v_mfma_f32_16x16x32_bf16 v[152:155], v[16:19], v[188:191], v[152:155]\n\t"
            "v_mfma_f32_16x16x32_bf16 v[160:163], v[60:63], v[184:187], v[160:163]\n\t"
            "v_mfma_f32_16x16x32_bf16 v[180:183], v[40:43], v[188:191], v[180:183]\n\t"
            "v_mfma_f32_16x16x32_bf16 v[176:179], v[84:87], v[184:187], v[176:179]\n\t"
            "s_waitcnt lgkmcnt(0)\n\t"
            "v_mfma_f32_16x16x32_bf16 v[152:155], v[20:23], v[192:195], v[152:155]\n\t"
            "v_mfma_f32_16x16x32_bf16 v[160:163], v[64:67], v[188:191], v[160:163]\n\t"
            "v_mfma_f32_16x16x32_bf16 v[180:183], v[44:47], v[192:195], v[180:183]\n\t"
            "v_mfma_f32_16x16x32_bf16 v[164:167], v[108:111], v[184:187], v[164:167]\n\t"
            "v_mfma_f32_16x16x32_bf16 v[176:179], v[88:91], v[188:191], v[176:179]\n\t"
            "v_mfma_f32_16x16x32_bf16 v[156:159], v[132:135], v[184:187], v[156:159]\n\t"
            "v_mfma_f32_16x16x32_bf16 v[160:163], v[68:71], v[192:195], v[160:163]\n\t"
            "s_nop 1\n\t"
            "v_cvt_pk_bf16_f32 v152, v152, v153\n\t"
            "v_mfma_f32_16x16x32_bf16 v[164:167], v[112:115], v[188:191], v[164:167]\n\t"
            "v_cvt_pk_bf16_f32 v153, v154, v155\n\t"
            "v_mfma_f32_16x16x32_bf16 v[176:179], v[92:95], v[192:195], v[176:179]\n\t"
            "v_cvt_pk_bf16_f32 v154, v180, v181\n\t"
            "v_cvt_pk_bf16_f32 v155, v182, v183\n\t"
            "v_mfma_f32_16x16x32_bf16 v[156:159], v[136:139], v[188:191], v[156:159]\n\t"
            "buffer_store_dwordx4 v[152:155], v198, s[40:43], s45 offen sc1\n\t"
            "v_mfma_f32_16x16x32_bf16 v[164:167], v[116:119], v[192:195], v[164:167]\n\t"
            "v_cvt_pk_bf16_f32 v160, v160, v161\n\t"
            "v_cvt_pk_bf16_f32 v161, v162, v163\n\t"
            "v_mfma_f32_16x16x32_bf16 v[156:159], v[140:143], v[192:195], v[156:159]\n\t"
            "v_cvt_pk_bf16_f32 v162, v176, v177\n\t"
            "v_cvt_pk_bf16_f32 v163, v178, v179\n\t"
            "buffer_store_dwordx4 v[160:163], v198, s[40:43], s45 offen offset:64 sc1\n\t"
            "s_nop 1\n\t"
            "v_cvt_pk_bf16_f32 v164, v164, v165\n\t"
            "v_cvt_pk_bf16_f32 v165, v166, v167\n\t"
            "s_nop 0\n\t"
            "v_cvt_pk_bf16_f32 v166, v156, v157\n\t"
            "v_cvt_pk_bf16_f32 v167, v158, v159\n\t"
            "buffer_store_dwordx4 v[164:167], v198, s[40:43], s45 offen offset:128 sc1\n\t"
            "s_nop 7\n\t"
            ".Lre%=:\n\t"
            : "+{v[224:227]}"(f0), "+{v[228:231]}"(f1), "+{v[232:235]}"(f2), "+{v242}"(r242), "+{v243}"(r243), "+{v[244:247]}"(r244),
              "+{s45}"(s45), "+{s46}"(s46), "+{s47}"(s47), "+{s48}"(s48), "+{s67}"(s67), "+{s76}"(s76), "+{s56}"(s56), "+{s44}"(s44), "+{s70}"(s70), "+{s21}"(s21)
            : B_VIN, B_SIN
            : "v0", "v1", "v2", "v3", "v4", "v5", "v6", "v7", "v8", "v9", "v10", "v11", "v12", "v13", "v14", "v15", "v16", "v17", "v18", "v19", "v20", "v21", "v22", "v23", "v24", "v25", "v26", "v27", "v28", "v29", "v30", "v31", "v32", "v33", "v34", "v35", "v36", "v37", "v38", "v39", "v40", "v41", "v42", "v43", "v44", "v45", "v46", "v47", "v48", "v49", "v50", "v51", "v52", "v53", "v54", "v55", "v56", "v57", "v58", "v59", "v60", "v61", "v62", "v63", "v64", "v65", "v66", "v67", "v68", "v69", "v70", "v71", "v72", "v73", "v74", "v75", "v76", "v77", "v78", "v79", "v80", "v81", "v82", "v83", "v84", "v85", "v86", "v87", "v88", "v89", "v90", "v91", "v92", "v93", "v94", "v95", "v96", "v97", "v98", "v99", "v100", "v101", "v102", "v103", "v104", "v105", "v106", "v107", "v108", "v109", "v110", "v111", "v112", "v113", "v114", "v115", "v116", "v117", "v118", "v119", "v120", "v121", "v122", "v123", "v124", "v125", "v126", "v127", "v128", "v129", "v130", "v131", "v132", "v133", "v134", "v135", "v136", "v137", "v138", "v139", "v140", "v141", "v142", "v143", "v144", "v145", "v146", "v147", "v148", "v149", "v150", "v151", "v152", "v153", "v154", "v155", "v156", "v157", "v158", "v159", "v160", "v161", "v162", "v163", "v164", "v165", "v166", "v167", "v168", "v169", "v170", "v171", "v172", "v173", "v174", "v175", "v176", "v177", "v178", "v179", "v180", "v181", "v182", "v183", "v184", "v185", "v186", "v187", "v188", "v189", "v190", "v191", "v192", "v193", "v194", "v195", "v196", "v197", "v198", "v199", "v200", "v201", "v202", "v203", "v204", "v205", "v206", "v207", "v208", "v209", "v210", "v211", "v212", "v213", "v214", "v215", "v216", "v217", "v218", "v219", "v220", "v221", "v222", "v223", "v236", "v237", "v248", "v249", "s26", "s68", "s69", "s79", "s36", "s37", "s38", "s57", "s58", "s59", "s49", "vcc", "scc", "m0", "memory");
        }
        asm volatile("wk_code_end1:");
        asm volatile("wk_code_end2:");

    }
}

extern "C" void kernel_launch(void* const* d_in, const int* in_sizes, int n_in,
                              void* d_out, int out_size, void* d_ws, size_t ws_size,
                              hipStream_t stream) {
    const unsigned short* x = (const unsigned short*)d_in[0];
    const unsigned short* dw = (const unsigned short*)d_in[1];
    const unsigned short* wqkv = (const unsigned short*)d_in[2];
    const unsigned short* wo = (const unsigned short*)d_in[3];
    unsigned short* out = (unsigned short*)d_out;
    u32x4* wf = (u32x4*)d_ws;
    win_attn_kernel<<<GRID, 512, 0, stream>>>(x, dw, wqkv, wo, wf, out);
}
```

```cpp
#include <hip/hip_runtime.h>
#include <hip/hip_bf16.h>
#include <stdint.h>


typedef unsigned u32x4 __attribute__((ext_vector_type(4)));

constexpr int IMG = 511;
constexpr int CH = 192;
constexpr int NWX = 73;
constexpr int NWIN = NWX * NWX;
constexpr int GRID = 256;
constexpr int NSHORT = 21 * GRID - NWIN;
constexpr int ROWBYTES = IMG * CH * 2;
constexpr int XBIAS = (IMG + 1) * CH * 2;
constexpr unsigned OOB = 0x40000000u;

constexpr int X_OFF = 32768;
constexpr int X1_OFF = 53632;
constexpr int O_OFF = 74496;
constexpr int V_OFF = 92928;
constexpr int KQL_OFF = 111360;
constexpr int OL_OFF = 128256;
constexpr int AT_OFF = 137472;
constexpr int CT_OFF = AT_OFF + 19584;
constexpr int RS_OFF = CT_OFF + 4096;
constexpr int WL_OFF = RS_OFF + 2304 + 64;
constexpr int LDS_BYTES = WL_OFF + 128;
#define RS_BASE (RS_OFF + (wave < 4 ? wave * 384 : 768 + wave * 192))
constexpr int TP_OFF = X1_OFF + 8192;
constexpr int STL_OFF = O_OFF + 12288;
#define LB_OFF(b) ((b) < 7 ? V_OFF + (b) * 6160 : X1_OFF + ((b) - 7) * 6160)
#define LPOS(wc) (((wc) & 1) ? 0 : 6)
constexpr unsigned CROW = 0x060a0230u;
constexpr unsigned CLANE = 0x00132C3Fu;

constexpr unsigned long long TOKMAP[6] = {0x20001c190f2a2202ull, 0x052517121f061a13ull, 0x181e08151d0b260cull, 0x1b2b2903102d0901ull, 0x04162e0e2824142cull, 0x110a072127232f0dull};
constexpr unsigned long long ROWMAP[8] = {0x08321b240f1c0006ull, 0x23310e2a09170d14ull, 0x210c2f030a273418ull, 0x1d2b11221f043739ull, 0x3234363a103e192dull, 0x291a20022c3f1605ull, 0x303a3c2628122e15ull, 0x39253d1e01070b13ull};

constexpr int WF_BYTES = 8 * 36 * 1024;
constexpr int EP_OFF = WF_BYTES;
constexpr int CN_OFF = EP_OFF + 256, FL_OFF = EP_OFF + 512;
constexpr int SH_OFF = EP_OFF + 1024;
constexpr int WS_BYTES = SH_OFF + GRID * 12288;
constexpr int GO_OFF = WL_OFF + 124;
constexpr int PG_OFF = WL_OFF + 96;
constexpr int SPIN_MAX = 1 << 20;

#define BAR() asm volatile("s_waitcnt lgkmcnt(0)\n\ts_barrier" ::: "memory")
#define OPQ(x) asm volatile("" : "+v"(x))
__host__ __device__ __forceinline__ constexpr unsigned byte_of(unsigned long long a, unsigned long long b, int i) {
    return (unsigned)(((i & 8) ? b : a) >> (8 * (i & 7))) & 0xFFu;
}
__host__ __device__ __forceinline__ constexpr void seq_to_win(int seq, int& wy, int& wx) {
    const bool last = seq >= 5110;
    const int s = last ? 35 : seq / 146, r = seq - 146 * s;
    const int kx = last ? r / 6 : r >> 2, pos = r - (last ? 6 : 4) * kx;
    const bool low = last && pos >= (kx == 36 ? 2 : 4);
    wy = low ? 72 : 2 * s + (pos & 1);
    wx = 2 * kx + (low ? (kx == 36 ? 0 : pos - 4) : pos >> 1);
}
__host__ __device__ __forceinline__ constexpr int win_to_seq(int wy, int wx) {
    const int kx = wx >> 1;
    return wy == 72 ? 5110 + 6 * kx + (kx == 36 ? 2 : 4 + (wx & 1)) : 146 * (wy >> 1) + (wy >= 70 ? 6 : 4) * kx + 2 * (wx & 1) + (wy & 1);
}
__host__ __device__ __forceinline__ constexpr int block_of(int wy, int wx) { return wy >= 70 ? 1295 + (wx & ~1) + (wy == 72) : 37 * (wy >> 1) + (wx >> 1); }
__host__ __device__ __forceinline__ constexpr void block_pos(int b, int& ky, int& kx) {
    if (b >= 1295) { kx = (b - 1295) >> 1; ky = 70 + 2 * ((b - 1295) & 1); }
    else { const int s = b / 37; kx = b - 37 * s; ky = 2 * s; }
}
__host__ __device__ __forceinline__ constexpr unsigned wl_entry(int seq) {
    int wy = 0, wx = 0;
    seq_to_win(seq, wy, wx);
    return (unsigned)((wy * 7 * IMG + wx * 7) * (CH * 2)) | (wy == 0 ? 0x08000000u : 0u) | (wx == 0 ? 0x10000000u : 0u) |
           (wx == NWX - 1 ? 0x20000000u : 0u) | ((unsigned)(wx & 1) << 30) | ((unsigned)(wy & 1) << 31);
}
__host__ __device__ __forceinline__ constexpr unsigned dma_off(int lane) {
    const int col = (lane * 43) >> 8;
    return (unsigned)(col * (CH * 2 - 96) + 16 * lane) | (col == 0 ? 0x10000000u : 0u) | (col == 8 ? 0x20000000u : 0u);
}

__host__ __device__ __forceinline__ constexpr unsigned pf_off(int lane) {
    const int l = lane < 63 ? lane : 62, r = l / 21;
    return (unsigned)(r * ROWBYTES + (l - 21 * r) * 128) | (r == 0 ? 0x08000000u : 0u);
}
constexpr int RECN = 80;
struct Tables { unsigned rec[GRID][RECN]; unsigned img[768]; };
constexpr Tables make_tables() {
    Tables t = {};
    for (int bx = 0; bx < GRID; ++bx) {
        unsigned* r = t.rec[bx];
        const int q0 = (bx & 7) * 32 + (bx >> 3), cnt = q0 < NSHORT ? 20 : 21, start = NWIN - cnt - (q0 < NSHORT ? 20 * q0 : 21 * q0 - NSHORT);
        for (int i = 0; i < 32; ++i) r[i] = i < cnt ? wl_entry(start + i) : 0x7ffffffu;
        int wy = 0, wx = 0;
        seq_to_win(start, wy, wx);
        const int key0 = block_of(wy, wx);
        seq_to_win(start + cnt - 1, wy, wx);
        const int nblk = block_of(wy, wx) - key0 + 1;
        for (int b = 0; b < 8; ++b) {
            int ky = 0, kx = 0;
            block_pos(key0 + b, ky, kx);
            r[64 + b] = b < nblk ? (unsigned)(((7 * ky + 5) * IMG + 14 * kx + 5) * (CH * 2) + XBIAS) | (kx == 36 ? 1u : 0u) : OOB;
            for (int c = 0; c < 4; ++c) {
                const int y = ky + (c >> 1), x = 2 * kx + (c & 1), i = win_to_seq(y, x) - start;
                const bool ok = b < nblk && y < NWX && x < NWX && i >= 0 && i < cnt;
                r[32 + 4 * b + c] = ok ? (unsigned)(((7 * y + LPOS(y)) * IMG + 7 * x + LPOS(x)) * (CH * 2) + i) : OOB + 47;
            }
        }
        r[72] = q0; r[73] = cnt;
    }
    for (int lane = 0; lane < 64; ++lane) {
        for (int e = 0; e < 3; ++e) {
            const int tk = byte_of(TOKMAP[2 * e], TOKMAP[2 * e + 1], lane & 15);
            t.img[192 + 4 * lane + e] = (unsigned)((tk / 7) * ROWBYTES + (tk % 7) * (CH * 2)) + 16 * (lane >> 4);
        }
        t.img[448 + 4 * lane] = dma_off(lane);
        t.img[449 + 4 * lane] = pf_off(lane);
    }
    return t;
}
static __constant__ const Tables TAB = make_tables();
typedef __attribute__((address_space(1))) unsigned gu32;
typedef __attribute__((address_space(1))) unsigned long long gu64;
#define RLX_AGENT __ATOMIC_RELAXED, __HIP_MEMORY_SCOPE_AGENT
#define TAG_OF(e) (((unsigned long long)~((e) + 1u) << 32) | ((e) + 1u))

__global__ __launch_bounds__(512, 2) void win_attn_kernel(const unsigned short* __restrict__ x, const unsigned short* __restrict__ dw,
                                                          const unsigned short* __restrict__ wqkv, const unsigned short* __restrict__ wo,
                                                          u32x4* wf, unsigned short* __restrict__ out) {
    __shared__ __attribute__((aligned(16))) char smem[LDS_BYTES];

    asm volatile("wk_code_begin:");
    const int tid = threadIdx.x;
    const int wave = __builtin_amdgcn_readfirstlane(tid >> 6);
    const int w = wave & 3;
    const unsigned ldsb = (unsigned)(size_t)smem;
#define ROLE_SETUP                                                                                 \
    int bx = blockIdx.x;                                                                           \
    asm volatile("" : "+s"(bx));                                                                   \
    bx = __builtin_amdgcn_readfirstlane(bx);                                                       \
    const unsigned* rec = TAB.rec[bx];                                                             \
    const unsigned en0 = rec[0];                                                                   \
    const int q0 = rec[72], cnt = rec[73];
    asm volatile(
        "s_load_dwordx8 s[24:31], %[ka], 0x0\n\t"
        "s_load_dwordx2 s[32:33], %[ka], 0x20\n\t"
        "s_mul_i32 s36, %[bx], %[recb]\n\t"
        "s_xor_b32 s37, %[wave], 4\n\t"
        "s_lshl2_add_u32 s38, s37, s36\n\t"
        "s_load_dword s38, %[tab], s38 offset:0x100\n\t"
        "s_load_dword s39, %[tab], s36\n\t"
        "v_mbcnt_lo_u32_b32 v1, -1, 0\n\t"
        "v_mbcnt_hi_u32_b32 v1, -1, v1\n\t"
        "s_and_b32 s44, %[bx], 7\n\t"
        "s_lshl_b32 s44, s44, 5\n\t"
        "s_lshr_b32 s45, %[bx], 3\n\t"
        "s_add_u32 s44, s44, s45\n\t"
        "v_lshlrev_b32 v2, 4, v1\n\t"
        "s_mov_b32 s43, 0x20000\n\t"
        "s_cmp_ge_u32 s44, 36\n\t"
        "s_cbranch_scc1 .Lnp%=\n\t"
        "s_mul_i32 s45, s44, 6\n\t"
        "s_lshl1_add_u32 s46, s44, 210\n\t"
        "s_cmp_ge_u32 %[wave], 6\n\t"
        "s_cselect_b64 vcc, -1, 0\n\t"
        "s_cselect_b32 s45, s46, s45\n\t"
        "s_movk_i32 s52, 1152\n\t"
        "s_cselect_b32 s52, 384, s52\n\t"
        "s_cselect_b32 s53, 0, 0xbbaaaaab\n\t"
        "s_add_u32 s45, s45, %[wave]\n\t"
        "s_mul_i32 s46, s45, 171\n\t"
        "s_lshr_b32 s46, s46, 10\n\t"
        "s_mul_i32 s47, s46, 6\n\t"
        "s_sub_u32 s47, s45, s47\n\t"
        "s_mul_i32 s48, s46, 43\n\t"
        "s_lshr_b32 s48, s48, 8\n\t"
        "s_mul_i32 s49, s48, 6\n\t"
        "s_sub_u32 s49, s46, s49\n\t"
        "s_mul_i32 s50, s48, 48\n\t"
        "s_cmp_lt_u32 s49, 3\n\t"
        "s_cselect_b32 s34, 1.0, 0x3e553b94\n\t"
        "s_movk_i32 s51, -48\n\t"
        "s_cselect_b32 s51, 192, s51\n\t"
        "s_cmp_lt_u32 s48, 4\n\t"
        "s_cselect_b32 s51, s51, s50\n\t"
        "s_cselect_b32 s34, s34, 1.0\n\t"
        "s_add_u32 s51, s51, s50\n\t"
        "s_lshl4_add_u32 s51, s49, s51\n\t"
        "s_and_b32 s49, s49, 1\n\t"
        "s_mul_i32 s49, s49, 12\n\t"
        "s_add_u32 s49, s49, 576\n\t"
        "s_cmp_ge_u32 %[wave], 6\n\t"
        "s_cselect_b32 s49, s49, 0\n\t"
        "s_sub_u32 s51, s51, s49\n\t"
        "s_and_b32 s49, s47, 1\n\t"
        "s_mul_i32 s49, s49, 88\n\t"
        "s_lshl3_add_u32 s47, s47, s49\n\t"
        "v_lshrrev_b32 v3, 5, v1\n\t"
        "v_mul_u32_u24 v3, 48, v3\n\t"
        "v_lshrrev_b32 v4, 1, v1\n\t"
        "v_and_or_b32 v3, v4, 8, v3\n\t"
        "v_add_u32 v3, s47, v3\n\t"
        "v_mul_u32_u24 v3, s52, v3\n\t"
        "v_and_b32 v4, 15, v1\n\t"
        "v_and_b32 v5, 12, v1\n\t"
        "v_cndmask_b32 v5, 0, v5, vcc\n\t"
        "v_add3_u32 v5, v4, v5, s51\n\t"
        "v_lshl_add_u32 v3, v5, 1, v3\n\t"
        "v_mov_b64 v[8:9], 0\n\t"
        "v_mov_b64 v[10:11], 0\n\t"
        "v_mov_b64 v[12:13], 0\n\t"
        "v_mov_b64 v[14:15], 0\n\t"
        "s_mul_i32 s42, s52, 192\n\t"
        "s_cmp_ge_u32 %[wave], 6\n\t"
        "s_waitcnt lgkmcnt(0)\n\t"
        "s_cselect_b64 s[40:41], s[30:31], s[28:29]\n\t"
        "s_and_b32 s41, s41, 0xffff\n\t"
        "buffer_load_short_d16_hi v8, v3, s[40:43], 0 offen\n\t"
        "buffer_load_short_d16_hi v9, v3, s[40:43], s52 offen\n\t"
        "s_lshl_b32 s46, s52, 1\n\t"
        "buffer_load_short_d16_hi v10, v3, s[40:43], s46 offen\n\t"
        "s_add_u32 s46, s46, s52\n\t"
        "buffer_load_short_d16_hi v11, v3, s[40:43], s46 offen\n\t"
        "s_add_u32 s46, s46, s52\n\t"
        "buffer_load_short_d16_hi v12, v3, s[40:43], s46 offen\n\t"
        "s_add_u32 s46, s46, s52\n\t"
        "buffer_load_short_d16_hi v13, v3, s[40:43], s46 offen\n\t"
        "s_add_u32 s46, s46, s52\n\t"
        "buffer_load_short_d16_hi v14, v3, s[40:43], s46 offen\n\t"
        "s_add_u32 s46, s46, s52\n\t"
        "buffer_load_short_d16_hi v15, v3, s[40:43], s46 offen\n\t"
        "v_mov_b32 v6, %[ep]\n\t"
        "global_load_dword v7, v6, s[32:33] sc1\n\t"
        "v_mov_b32 v5, %[red]\n\t"
        "v_mad_u32_u24 v4, v4, %[c144], v5\n\t"
        "v_lshrrev_b32 v5, 2, v1\n\t"
        "v_and_b32 v5, 12, v5\n\t"
        "s_lshl_b32 s46, %[wave], 4\n\t"
        "v_add3_u32 v5, v4, v5, s46\n\t"
        "s_mov_b32 s40, s32\n\t"
        "s_and_b32 s41, s33, 0xffff\n\t"
        "s_mov_b32 s42, %[wsb]\n\t"
        "s_lshl_b32 s45, s45, 10\n\t"
        "s_waitcnt vmcnt(1)\n\t"
        "v_add_f32 v16, v8, v10\n\t"
        "v_add_f32 v17, v9, v11\n\t"
        "v_add_f32 v18, v12, v14\n\t"
        "v_add_f32 v19, v13, v15\n\t"
        "v_add_f32 v16, v16, v18\n\t"
        "v_add_f32 v17, v17, v19\n\t"
        "v_add_f32 v16, v16, v17\n\t"
        "ds_write_b32 v5, v16\n\t"
        "s_waitcnt lgkmcnt(0)\n\t"
        "s_barrier\n\t"
        "ds_read_b128 v[16:19], v4\n\t"
        "ds_read_b128 v[20:23], v4 offset:16\n\t"
        "ds_read_b128 v[24:27], v4 offset:32\n\t"
        "ds_read_b128 v[28:31], v4 offset:48\n\t"
        "ds_read_b128 v[32:35], v4 offset:64\n\t"
        "ds_read_b128 v[36:39], v4 offset:80\n\t"
        "s_waitcnt lgkmcnt(4)\n\t"
        "v_add_f32 v16, v16, v18\n\t"
        "v_add_f32 v17, v17, v19\n\t"
        "v_add_f32 v20, v20, v22\n\t"
        "v_add_f32 v21, v21, v23\n\t"
        "s_waitcnt lgkmcnt(2)\n\t"
        "v_add_f32 v24, v24, v26\n\t"
        "v_add_f32 v25, v25, v27\n\t"
        "v_add_f32 v28, v28, v30\n\t"
        "v_add_f32 v29, v29, v31\n\t"
        "v_add_f32 v16, v16, v20\n\t"
        "v_add_f32 v17, v17, v21\n\t"
        "s_waitcnt lgkmcnt(0)\n\t"
        "v_add_f32 v32, v32, v34\n\t"
        "v_add_f32 v33, v33, v35\n\t"
        "v_add_f32 v36, v36, v38\n\t"
        "v_add_f32 v37, v37, v39\n\t"
        "v_add_f32 v24, v24, v28\n\t"
        "v_add_f32 v25, v25, v29\n\t"
        "v_add_f32 v32, v32, v36\n\t"
        "v_add_f32 v33, v33, v37\n\t"
        "v_add_f32 v16, v16, v24\n\t"
        "v_add_f32 v17, v17, v25\n\t"
        "v_add_f32 v16, v16, v32\n\t"
        "v_add_f32 v17, v17, v33\n\t"
        "v_add_f32 v16, v16, v17\n\t"
        "v_mul_f32 v16, s53, v16\n\t"
        "v_mul_f32 v16, s34, v16\n\t"
        "v_fma_f32 v8, v8, s34, v16\n\t"
        "v_fma_f32 v9, v9, s34, v16\n\t"
        "v_fma_f32 v10, v10, s34, v16\n\t"
        "v_fma_f32 v11, v11, s34, v16\n\t"
        "v_fma_f32 v12, v12, s34, v16\n\t"
        "v_fma_f32 v13, v13, s34, v16\n\t"
        "v_fma_f32 v14, v14, s34, v16\n\t"
        "v_fma_f32 v15, v15, s34, v16\n\t"
        "v_cvt_pk_bf16_f32 v8, v8, v9\n\t"
        "v_cvt_pk_bf16_f32 v9, v10, v11\n\t"
        "v_cvt_pk_bf16_f32 v10, v12, v13\n\t"
        "v_cvt_pk_bf16_f32 v11, v14, v15\n\t"
        "buffer_store_dwordx4 v[8:11], v2, s[40:43], s45 offen sc1\n\t"
        "s_or_b32 s46, s44, s37\n\t"
        "s_cmp_lg_u32 s46, 0\n\t"
        "s_cbranch_scc1 .Lnc%=\n\t"
        "v_mov_b32 v3, 0\n\t"
        "global_store_dword v6, v3, s[32:33] offset:256 sc1\n\t"
        ".Lnc%=:\n\t"
        "v_lshl_add_u32 v6, s44, 3, v6\n\t"
        "s_waitcnt vmcnt(0)\n\t"
        "s_barrier\n\t"
        "s_cmp_lg_u32 %[wave], 0\n\t"
        "s_cbranch_scc1 .Lnp%=\n\t"
        "v_add_u32 v4, 1, v7\n\t"
        "v_not_b32 v5, v4\n\t"
        "s_mov_b64 exec, 1\n\t"
        "global_store_dwordx2 v6, v[4:5], s[32:33] offset:512 sc1\n\t"
        "s_mov_b64 exec, -1\n\t"
        ".Lnp%=:\n\t"
        "s_getpc_b64 s[40:41]\n\t"
        ".Lwk_here%=:\n\t"
        "s_sub_u32 s40, s40, .Lwk_here%=-wk_code_begin\n\t"
        "s_subb_u32 s41, s41, 0\n\t"
        "s_and_b32 s41, s41, 0xffff\n\t"
        "s_mov_b32 s42, wk_code_end-wk_code_begin\n\t"
        "s_max_u32 s42, s42, wk_code_end1-wk_code_begin\n\t"
        "s_max_u32 s42, s42, wk_code_end2-wk_code_begin\n\t"
        "s_add_u32 m0, %[lds], %[sink]\n\t"
        "v_lshlrev_b32 v3, 7, v1\n\t"
        "s_lshl_b32 s45, %[wave], 13\n\t"
        "buffer_load_dword v3, s[40:43], s45 offen lds\n\t"
        "s_mov_b64 s[40:41], %[tab]\n\t"
        "s_and_b32 s41, s41, 0xffff\n\t"
        "s_mov_b32 s42, %[tabb]\n\t"
        "s_lshl_b32 s46, %[wave], 10\n\t"
        "s_add_u32 s47, s46, %[imgs]\n\t"
        "s_add_u32 s48, s46, %[imgd]\n\t"
        "s_lshl_b32 s49, %[wave], 8\n\t"
        "s_add_u32 s49, s49, %[mapd]\n\t"
        "s_add_u32 s50, s36, 128\n\t"
        "s_mov_b64 vcc, -1\n\t"
        "s_cmp_eq_u32 %[wave], 0\n\t"
        "s_cselect_b32 s47, s36, s47\n\t"
        "s_cselect_b32 s48, %[wld], s48\n\t"
        "s_cselect_b64 vcc, 0xff, vcc\n\t"
        "s_cmp_ge_u32 %[wave], 4\n\t"
        "s_cselect_b32 s47, s50, s47\n\t"
        "s_cselect_b32 s48, s49, s48\n\t"
        "s_cselect_b64 exec, 0xff, vcc\n\t"
        "s_add_u32 m0, %[lds], s48\n\t"
        "s_and_b32 s49, %[wave], 3\n\t"
        "buffer_load_dwordx4 v2, s[40:43], s47 offen lds\n\t"
        "s_mov_b64 exec, -1\n\t"
        "v_mul_u32_u24 v3, 43, v1\n\t"
        "v_lshrrev_b32 v3, 8, v3\n\t"
        "v_mad_u32_u24 v4, v3, %[c288], v2\n\t"
        "s_mul_i32 s49, s49, 96\n\t"
        "s_mul_i32 s50, s37, 6160\n\t"
        "s_add_u32 s50, s50, %[lb0]\n\t"
        "s_cmp_eq_u32 s37, 7\n\t"
        "s_cselect_b32 s50, %[lb7], s50\n\t"
        "s_add_u32 s46, s46, %[tp]\n\t"
        "s_waitcnt lgkmcnt(0)\n\t"
        "s_cmp_ge_u32 %[wave], 4\n\t"
        "s_cbranch_scc1 .Lib%=\n\t"
        "s_mov_b32 s40, s26\n\t"
        "s_and_b32 s41, s27, 0xffff\n\t"
        "s_movk_i32 s42, 3456\n\t"
        "s_add_u32 m0, %[lds], s46\n\t"
        "s_nop 0\n\t"
        "buffer_load_dwordx4 v4, s[40:43], s49 offen lds\n\t"
        ".Lib%=:\n\t"
        "s_sub_u32 s40, s24, %[xb]\n\t"
        "s_subb_u32 s41, s25, 0\n\t"
        "s_and_b32 s41, s41, 0xffff\n\t"
        "s_mov_b32 s42, %[xsz]\n\t"
        "s_cmp_eq_u32 s38, 2.0\n\t"
        "s_cbranch_scc1 .Lic%=\n\t"
        "s_mov_b32 exec_hi, 0xffff\n\t"
        "s_add_u32 m0, %[lds], s50\n\t"
        "s_mov_b32 s46, 4\n\t"
        "s_bitcmp1_b32 s38, 0\n\t"
        "s_cselect_b32 s47, 2.0, s38\n\t"
        "s_bitset0_b32 s38, 0\n\t"
        ".Lid%=:\n\t"
        "buffer_load_dwordx4 v2, s[40:43], s38 offen lds\n\t"
        "buffer_load_dwordx4 v2, s[40:43], s47 offen offset:768 lds\n\t"
        "s_add_u32 m0, m0, 1536\n\t"
        "s_add_u32 s38, s38, %[rb]\n\t"
        "s_add_u32 s47, s47, %[rb]\n\t"
        "s_sub_u32 s46, s46, 1\n\t"
        "s_cmp_lg_u32 s46, 0\n\t"
        "s_cbranch_scc1 .Lid%=\n\t"
        ".Lic%=:\n\t"
        "s_cmp_lt_u32 %[wave], 4\n\t"
        "s_cbranch_scc1 .Lie%=\n\t"
        "s_mov_b32 exec_hi, 0x3fffff\n\t"
        "s_and_b32 s46, s39, 0x10000000\n\t"
        "s_and_b32 s47, s39, 0x20000000\n\t"
        "v_cmp_eq_u32 vcc, 0, v3\n\t"
        "v_mov_b32 v5, s46\n\t"
        "v_mov_b32 v6, s47\n\t"
        "v_cndmask_b32 v5, 0, v5, vcc\n\t"
        "v_cmp_eq_u32 vcc, 8, v3\n\t"
        "s_and_b32 s46, s39, 0x7ffffff\n\t"
        "s_add_u32 s46, s46, s49\n\t"
        "v_cndmask_b32 v6, 0, v6, vcc\n\t"
        "v_or3_b32 v4, v4, v5, v6\n\t"
        "s_bitcmp1_b32 s39, 27\n\t"
        "s_cselect_b32 s47, 2.0, s46\n\t"
        "s_lshl_b32 s45, s37, 13\n\t"
        "s_add_u32 m0, %[lds], s45\n\t"
        "s_mov_b32 s48, 8\n\t"
        "buffer_load_dwordx4 v4, s[40:43], s47 offen lds\n\t"
        ".Lia%=:\n\t"
        "s_add_u32 m0, m0, 872\n\t"
        "s_add_u32 s46, s46, %[rb]\n\t"
        "s_sub_u32 s48, s48, 1\n\t"
        "buffer_load_dwordx4 v4, s[40:43], s46 offen lds\n\t"
        "s_cmp_lg_u32 s48, 0\n\t"
        "s_cbranch_scc1 .Lia%=\n\t"
        ".Lie%=:\n\t"
        "s_mov_b64 exec, -1\n\t"
        :: [ka] "s"(__builtin_amdgcn_kernarg_segment_ptr()), [bx] "s"(blockIdx.x), [wave] "s"(wave), [tab] "s"(&TAB), [lds] "s"(ldsb),
           [red] "s"(ldsb + O_OFF), [c144] "s"(144), [c288] "s"(288), [recb] "n"(RECN * 4), [ep] "n"(EP_OFF), [wsb] "n"(WS_BYTES), [sink] "n"(X_OFF + 18432),
           [tabb] "n"(sizeof(Tables)), [imgs] "n"(GRID * RECN * 4 - 1024), [imgd] "n"(AT_OFF + 18816 - 1024), [mapd] "n"(TP_OFF + 3072), [wld] "n"(WL_OFF),
           [lb0] "n"(LB_OFF(0)), [lb7] "n"(LB_OFF(7)), [tp] "n"(TP_OFF), [xb] "n"(XBIAS), [xsz] "n"(IMG * IMG * CH * 2 + XBIAS), [rb] "n"(ROWBYTES)
        : "v1", "v2", "v3", "v4", "v5", "v6", "v7", "v8", "v9", "v10", "v11", "v12", "v13", "v14", "v15", "v16", "v17", "v18", "v19", "v20", "v21", "v22", "v23", "v24", "v25", "v26", "v27", "v28", "v29", "v30", "v31", "v32", "v33", "v34", "v35", "v36", "v37", "v38", "v39", "s24", "s25", "s26", "s27", "s28", "s29", "s30", "s31", "s32", "s33", "s34", "s36", "s37", "s38", "s39", "s40", "s41", "s42", "s43", "s44", "s45", "s46", "s47", "s48", "s49", "s50", "s51", "s52", "s53", "m0", "vcc", "scc", "memory");
#define WAIT_GO                                                                                    \
        {                                                                                          \
            unsigned go_;                                                                          \
            asm volatile(".Lgo%=:\n\tds_read_b32 %0, %1\n\ts_waitcnt lgkmcnt(0)\n\tv_cmp_ne_u32 vcc, -1, %0\n\ts_nop 4\n\ts_cbranch_vccnz .Lgo%=" \
                         : "=&v"(go_) : "v"(ldsb + GO_OFF) : "vcc", "memory");                     \
        }
#define LANE_SETUP                                                                                 \
    int lane;                                                                                      \
    asm volatile("v_mbcnt_lo_u32_b32 %0, -1, 0\n\tv_mbcnt_hi_u32_b32 %0, -1, %0" : "=v"(lane));    \
    __builtin_assume(lane >= 0 && lane < 64);                                                      \
    const int g = lane >> 4, l15 = lane & 15, sw = l15 >> 1;                                       \
    int fE = X_OFF + l15 * 384 + ((g ^ sw) << 4);                                                  \
    OPQ(fE);
#define XPAT(r) (((r) < 48 ? (r) : 48) * 384 + (((2 * w) ^ (((r) >> 1) & 7)) << 4))
#define SPAT(r) ((w >> 1) * 1024 + (w & 1) * 8 + (r) * 16)
#define CONV_CONSTS(var) \
            const unsigned long long rlo = g == 0 ? ROWMAP[0] : (g == 1 ? ROWMAP[2] : (g == 2 ? ROWMAP[4] : ROWMAP[6])); \
            const unsigned long long rhi = g == 0 ? ROWMAP[1] : (g == 1 ? ROWMAP[3] : (g == 2 ? ROWMAP[5] : ROWMAP[7])); \
            int xrow = byte_of(rlo, rhi, l15); \
            if (lane == (int)((CLANE >> (8 * (var))) & 0xFF)) xrow = 48; \
            if (lane == 63) xrow = (CROW >> (8 * (var))) & 0xFF; \
            const unsigned pXw_ = XPAT(xrow); \
            const unsigned pSTw_ = (w >> 1) * 1024 + (w & 1) * 8 + xrow * 16; \
            const int oy = (0x5320 >> (4 * (l15 >> 2))) & 15, ox = (0x5320 >> (4 * (l15 & 3))) & 15; \
            const unsigned aB_ = ldsb + w * 8192 + (oy + (g >> 1)) * 872 + (ox + 2 * (g & 1)) * 96; \
            const int dyi = g - (l15 >> 3); \
            const unsigned aA_ = ldsb + AT_OFF + (l15 & 3) * 16 + \
                                 ((dyi < 0 || dyi > 2) ? 18816 : (dyi == 0 ? 0 : (dyi == 1 ? 6208 : 12544)) + ((l15 >> 2) & 1) * 3072 + w * 768); \
            const unsigned idx_ = (l15 & 3) * 0x5555u; \
            const unsigned se = (2 || l15 == 4 * g) ? 0x3F803F80u : 0u; \
            const u32x4 sel4 = {se, se, se, se};

    if (wave < 4) {
        ROLE_SETUP
        LANE_SETUP
        asm volatile("s_waitcnt vmcnt(0)" ::: "memory");
        if (lane < 48) {
            unsigned tp[9];
#pragma unroll
            for (int k = 0; k < 9; ++k) tp[k] = *(const unsigned short*)(smem + TP_OFF + w * 1024 + k * 96 + lane * 2);
            tp[4] |= 0x3F800000u;
#pragma unroll
            for (int dy = 0; dy < 3; ++dy) {
                char* p = smem + AT_OFF + (dy == 0 ? 0 : (dy == 1 ? 6208 : 12544)) + w * 768 + lane * 16;
                *(u32x4*)p = (u32x4){tp[3 * dy], tp[3 * dy + 1], tp[3 * dy + 2], 0u};
                *(u32x4*)(p + 3072) = (u32x4){0u, tp[3 * dy], tp[3 * dy + 1], tp[3 * dy + 2]};
            }
        }
        BAR();
        {
            const int dW = X_OFF + l15 * 384 + (((2 * w + (g >> 1)) ^ sw) << 4) + 8 * (g & 1);
            const unsigned rsb = ldsb + RS_BASE;
            const unsigned pE_ = fE - X_OFF, aO_ = ldsb + dW + (O_OFF - X_OFF), aZ_ = ldsb + AT_OFF + 18816, aVB_ = ldsb + V_OFF + w * 4608 + 1024 + lane * 8;
            unsigned aRSw_ = rsb + lane * 4;
            const unsigned pST_ = lane * 16, aPLw_ = rsb + lane * 2, aRSr_ = rsb + l15 * 4;
            const unsigned g16_ = g * 16, aPLr_ = (l15 & 3) ? aZ_ : rsb + g * 8;
            const unsigned loVL_ = ((2 * w + (l15 >> 3)) << 4) + (l15 & 7) * 2, loOL_ = ((2 * w + (g >> 1)) << 4) + 8 * (g & 1);
            const unsigned on = (l15 & 3) ? 0u : 0x3F803F80u;
            const u32x4 ones4 = {on, on, on, on};
            const unsigned snk = ldsb + O_OFF + 15360 + w * 192, kr = ldsb + KQL_OFF + l15 * 800;
            const bool ok1 = 16 + l15 < cnt, dg = g == (l15 >> 2);
            u32x4 pl = {kr + w * 192 + g * 16, (ok1 ? kr + 12800 + w * 192 : snk) + g * 16, dg ? kr + 768 + w * 4 : snk, dg && ok1 ? kr + 12800 + 768 + w * 4 : snk};
            unsigned pcl, pch, len;
            asm volatile("s_getpc_b64 s[36:37]\n.Lwk_h2%=:\n\ts_sub_u32 s36, s36, .Lwk_h2%=-wk_code_begin\n\ts_subb_u32 s37, s37, 0\n\t"
                         "s_mov_b32 %2, wk_code_end-wk_code_begin\n\ts_max_u32 %2, %2, wk_code_end1-wk_code_begin" : "={s36}"(pcl), "={s37}"(pch), "=s"(len) :: "scc");
            pcl = __builtin_amdgcn_readfirstlane(pcl); pch = __builtin_amdgcn_readfirstlane(pch); len = __builtin_amdgcn_readfirstlane(len);
            const unsigned long long tb = w == 0 ? ((unsigned long long)pch << 32) | pcl : (unsigned long long)wf;
            const u32x4 tr = {(unsigned)tb, (unsigned)(tb >> 32) & 0xffffu, w == 0 ? len : (w == 1 ? (unsigned)WF_BYTES : SH_OFF + bx * 12288 + 12288), 0x00020000u};
            const unsigned to = w == 0 ? ((bx >> 3) % 6) * 8192 : (w == 1 ? 6 * 36864 + ((bx >> 3) % 9) * 8192 : SH_OFF + bx * 12288 + (w - 2) * 8192);
            asm volatile("" :: "{v244}"(pE_), "{v245}"(aO_), "{v246}"(aZ_), "{v247}"(aVB_), "{v248}"(pST_), "{v249}"(aRSw_), "{v250}"(aPLw_), "{v251}"(aRSr_), "{v252}"(g16_), "{v253}"(aPLr_), "{v254}"(loVL_), "{v255}"(loOL_), "{v[240:243]}"(ones4), "{v[232:235]}"(pl), "{s[36:39]}"(tr), "{s41}"(to));
            if (w == 0) {
                gu64* fp = (gu64*)((char*)wf + (lane < 36 ? FL_OFF + lane * 8 : EP_OFF));
#pragma unroll 1
                for (int it = 0; it < SPIN_MAX; ++it) {
                    const unsigned long long f = __hip_atomic_load(fp, RLX_AGENT);
                    const unsigned ep = __builtin_amdgcn_readlane((unsigned)f, 63);
                    if ((__builtin_amdgcn_ballot_w64(f != TAG_OF(ep)) & 0xfffffffffull) == 0ull) break;
                    __builtin_amdgcn_s_sleep(1);
                }
                __builtin_amdgcn_fence(__ATOMIC_ACQUIRE, "wavefront");
                asm volatile("ds_write_b32 %0, %1" :: "v"(ldsb + GO_OFF), "v"(~0u) : "memory");
            } else WAIT_GO
            __builtin_amdgcn_s_setprio(2);
            unsigned s44 = cnt, s48 = ldsb + X_OFF, s70 = 0;
            unsigned s72 = ldsb + KQL_OFF + w * 192, s73 = ldsb + OL_OFF, s77 = ldsb + OL_OFF, s76 = ldsb + KQL_OFF + 768 + w * 4;
            const unsigned long long wb = (unsigned long long)wf;
            const u32x4 wr = {(unsigned)wb, (unsigned)(wb >> 32) & 0xffffu, (unsigned)WS_BYTES, 0x00020000u};
            asm volatile(
            "s_setprio 2\n\t"
            "s_add_u32 s49, s33, 0\n\t"
            "buffer_load_dwordx4 v[0:3], v248, s[28:31], s49 offen sc1\n\t"
            "s_add_u32 s49, s33, 1024\n\t"
            "buffer_load_dwordx4 v[4:7], v248, s[28:31], s49 offen sc1\n\t"
            "s_add_u32 s49, s33, 2048\n\t"
            "buffer_load_dwordx4 v[8:11], v248, s[28:31], s49 offen sc1\n\t"
            "s_add_u32 s49, s33, 3072\n\t"
            "buffer_load_dwordx4 v[12:15], v248, s[28:31], s49 offen sc1\n\t"
            "s_add_u32 s49, s33, 4096\n\t"
            "buffer_load_dwordx4 v[16:19], v248, s[28:31], s49 offen sc1\n\t"
            "s_add_u32 s49, s33, 5120\n\t"
            "buffer_load_dwordx4 v[20:23], v248, s[28:31], s49 offen sc1\n\t"
            "s_add_u32 s49, s33, 6144\n\t"
            "buffer_load_dwordx4 v[24:27], v248, s[28:31], s49 offen sc1\n\t"
            "s_add_u32 s49, s33, 7168\n\t"
            "buffer_load_dwordx4 v[28:31], v248, s[28:31], s49 offen sc1\n\t"
            "s_add_u32 s49, s33, 8192\n\t"
            "buffer_load_dwordx4 v[32:35], v248, s[28:31], s49 offen sc1\n\t"
            "s_add_u32 s49, s33, 9216\n\t"
            "buffer_load_dwordx4 v[36:39], v248, s[28:31], s49 offen sc1\n\t"
            "s_add_u32 s49, s33, 10240\n\t"
            "buffer_load_dwordx4 v[40:43], v248, s[28:31], s49 offen sc1\n\t"
            "s_add_u32 s49, s33, 11264\n\t"
            "buffer_load_dwordx4 v[44:47], v248, s[28:31], s49 offen sc1\n\t"
            "s_add_u32 s49, s33, 12288\n\t"
            "buffer_load_dwordx4 v[48:51], v248, s[28:31], s49 offen sc1\n\t"
            "s_add_u32 s49, s33, 13312\n\t"
            "buffer_load_dwordx4 v[52:55], v248, s[28:31], s49 offen sc1\n\t"
            "s_add_u32 s49, s33, 14336\n\t"
            "buffer_load_dwordx4 v[56:59], v248, s[28:31], s49 offen sc1\n\t"
            "s_add_u32 s49, s33, 15360\n\t"
            "buffer_load_dwordx4 v[60:63], v248, s[28:31], s49 offen sc1\n\t"
            "v_mov_b32 v224, s46\n\t"
            ".Ls1%=:\n\t"
            "ds_read_b128 v[220:223], v224\n\t"
            "s_waitcnt lgkmcnt(0)\n\t"
            "v_and_b32 v220, v220, v221\n\t"
            "v_and_b32 v222, v222, v223\n\t"
            "v_and_b32 v220, v220, v222\n\t"
            "s_nop 0\n\t"
            "v_readfirstlane_b32 s49, v220\n\t"
            "s_nop 3\n\t"
            "s_bitcmp1_b32 s49, 27\n\t"
            "s_cbranch_scc0 .Ls1%=\n\t"
            "s_nop 7\n\t"
            "v_add_u32 v237, s35, v248\n\t"
            "v_add_u32 v238, s56, v244\n\t"
            "v_xor_b32 v239, 64, v238\n\t"
            "v_add_u32 v236, s56, v248\n\t"
            "ds_read_b128 v[200:203], v237\n\t"
            "ds_read_b128 v[204:207], v237 offset:1024\n\t"
            "ds_read_b128 v[144:147], v238\n\t"
            "ds_read_b128 v[148:151], v239\n\t"
            "ds_read_b128 v[152:155], v238 offset:128\n\t"
            "ds_read_b128 v[156:159], v239 offset:128\n\t"
            "ds_read_b128 v[160:163], v238 offset:256\n\t"
            "ds_read_b128 v[164:167], v239 offset:256\n\t"
            "ds_read_b128 v[168:171], v238 offset:6144\n\t"
            "ds_read_b128 v[172:175], v239 offset:6144\n\t"
            "ds_read_b128 v[176:179], v238 offset:6272\n\t"
            "ds_read_b128 v[180:183], v239 offset:6272\n\t"
            "ds_read_b128 v[184:187], v238 offset:6400\n\t"
            "ds_read_b128 v[188:191], v239 offset:6400\n\t"
            "ds_read_b128 v[208:211], v237 offset:2048\n\t"
            "s_waitcnt lgkmcnt(14)\n\t"
            "ds_read_b128 v[224:227], v236 offset:12288\n\t"
            "s_waitcnt lgkmcnt(14)\n\t"
            "ds_read_b128 v[228:231], v236 offset:13312\n\t"
            "s_add_u32 s49, s33, 16384\n\t"
            "buffer_load_dwordx4 v[64:67], v248, s[28:31], s49 offen sc1\n\t"
            "s_add_u32 s49, s33, 17408\n\t"
            "buffer_load_dwordx4 v[68:71], v248, s[28:31], s49 offen sc1\n\t"
            "s_waitcnt vmcnt(12)\n\t"
            "s_waitcnt lgkmcnt(14)\n\t"
            "v_mfma_f32_16x16x32_bf16 v[192:195], v[0:3], v[144:147], 0\n\t"
            "buffer_store_dwordx4 v[200:203], v248, s[28:31], s34 offen\n\t"
            "s_waitcnt lgkmcnt(8)\n\t"
            "v_mfma_f32_16x16x32_bf16 v[212:215], v[0:3], v[168:171], 0\n\t"
            "buffer_store_dwordx4 v[204:207], v248, s[28:31], s34 offen offset:1024\n\t"
            "v_mfma_f32_16x16x32_bf16 v[192:195], v[4:7], v[148:151], v[192:195]\n\t"
            "s_waitcnt lgkmcnt(2)\n\t"
            "buffer_store_dwordx4 v[208:211], v248, s[28:31], s34 offen offset:2048\n\t"
            "v_mfma_f32_16x16x32_bf16 v[212:215], v[4:7], v[172:175], v[212:215]\n\t"
            "s_add_u32 s49, s33, 18432\n\t"
            "buffer_load_dwordx4 v[72:75], v248, s[28:31], s49 offen sc1\n\t"
            "v_mfma_f32_16x16x32_bf16 v[192:195], v[8:11], v[152:155], v[192:195]\n\t"
            "s_add_u32 s49, s33, 19456\n\t"
            "buffer_load_dwordx4 v[76:79], v248, s[28:31], s49 offen sc1\n\t"
            "v_mfma_f32_16x16x32_bf16 v[212:215], v[8:11], v[176:179], v[212:215]\n\t"
            "s_add_u32 s49, s33, 20480\n\t"
            "buffer_load_dwordx4 v[80:83], v248, s[28:31], s49 offen sc1\n\t"
            "v_mfma_f32_16x16x32_bf16 v[192:195], v[12:15], v[156:159], v[192:195]\n\t"
            "s_add_u32 s49, s33, 21504\n\t"
            "buffer_load_dwordx4 v[84:87], v248, s[28:31], s49 offen sc1\n\t"
            "v_mfma_f32_16x16x32_bf16 v[212:215], v[12:15], v[180:183], v[212:215]\n\t"
            "s_add_u32 s49, s33, 22528\n\t"
            "buffer_load_dwordx4 v[88:91], v248, s[28:31], s49 offen sc1\n\t"
            "v_mfma_f32_16x16x32_bf16 v[192:195], v[16:19], v[160:163], v[192:195]\n\t"
            "s_add_u32 s49, s33, 23552\n\t"
            "buffer_load_dwordx4 v[92:95], v248, s[28:31], s49 offen sc1\n\t"
            "v_mfma_f32_16x16x32_bf16 v[212:215], v[16:19], v[184:187], v[212:215]\n\t"
            "s_waitcnt lgkmcnt(1)\n\t"
            "v_add_f32 v224, v224, v226\n\t"
            "s_waitcnt lgkmcnt(0)\n\t"
            "v_add_f32 v228, v228, v230\n\t"
            "v_mfma_f32_16x16x32_bf16 v[192:195], v[20:23], v[164:167], v[192:195]\n\t"
            "v_add_f32 v224, v224, v228\n\t"
            "v_add_f32 v225, v225, v227\n\t"
            "v_mfma_f32_16x16x32_bf16 v[212:215], v[20:23], v[188:191], v[212:215]\n\t"
            "s_waitcnt vmcnt(15)\n\t"
            "s_add_u32 s49, s33, 24576\n\t"
            "buffer_load_dwordx4 v[96:99], v248, s[28:31], s49 offen sc1\n\t"
            "v_mfma_f32_16x16x32_bf16 v[196:199], v[24:27], v[144:147], 0\n\t"
            "s_add_u32 s49, s33, 25600\n\t"
            "buffer_load_dwordx4 v[100:103], v248, s[28:31], s49 offen sc1\n\t"
            "v_mfma_f32_16x16x32_bf16 v[216:219], v[24:27], v[168:171], 0\n\t"
            "s_add_u32 s49, s33, 26624\n\t"
            "buffer_load_dwordx4 v[104:107], v248, s[28:31], s49 offen sc1\n\t"
            "v_mfma_f32_16x16x32_bf16 v[196:199], v[28:31], v[148:151], v[196:199]\n\t"
            "s_add_u32 s49, s33, 27648\n\t"
            "buffer_load_dwordx4 v[108:111], v248, s[28:31], s49 offen sc1\n\t"
            "v_mfma_f32_16x16x32_bf16 v[216:219], v[28:31], v[172:175], v[216:219]\n\t"
            "s_add_u32 s49, s33, 28672\n\t"
            "buffer_load_dwordx4 v[112:115], v248, s[28:31], s49 offen sc1\n\t"
            "v_mfma_f32_16x16x32_bf16 v[196:199], v[32:35], v[152:155], v[196:199]\n\t"
            "s_add_u32 s49, s33, 29696\n\t"
            "buffer_load_dwordx4 v[116:119], v248, s[28:31], s49 offen sc1\n\t"
            "v_mfma_f32_16x16x32_bf16 v[216:219], v[32:35], v[176:179], v[216:219]\n\t"
            "v_add_f32 v229, v229, v231\n\t"
            "v_mul_f32 v224, 0x3baaaaab, v224\n\t"
            "v_mfma_f32_16x16x32_bf16 v[196:199], v[36:39], v[156:159], v[196:199]\n\t"
            "v_add_f32 v225, v225, v229\n\t"
            "v_mul_f32_e64 v224, v224, -v224\n\t"
            "v_mfma_f32_16x16x32_bf16 v[216:219], v[36:39], v[180:183], v[216:219]\n\t"
            "v_fmac_f32 v224, 0x3baaaaab, v225\n\t"
            "v_max_f32 v224, 0, v224\n\t"
            "v_mfma_f32_16x16x32_bf16 v[196:199], v[40:43], v[160:163], v[196:199]\n\t"
            "v_add_f32 v224, 0x3727c5ac, v224\n\t"
            "v_rsq_f32 v224, v224\n\t"
            "v_mfma_f32_16x16x32_bf16 v[216:219], v[40:43], v[184:187], v[216:219]\n\t"
            "ds_write_b32 v249, v224\n\t"
            "v_mfma_f32_16x16x32_bf16 v[196:199], v[44:47], v[164:167], v[196:199]\n\t"
            "ds_read_b32 v236, v251\n\t"
            "v_mfma_f32_16x16x32_bf16 v[216:219], v[44:47], v[188:191], v[216:219]\n\t"
            "ds_read_b32 v237, v251 offset:64\n\t"
            "s_waitcnt vmcnt(15)\n\t"
            "v_mfma_f32_16x16x32_bf16 v[200:203], v[48:51], v[144:147], 0\n\t"
            "s_add_u32 s49, s33, 30720\n\t"
            "buffer_load_dwordx4 v[120:123], v248, s[28:31], s49 offen sc1\n\t"
            "v_mfma_f32_16x16x32_bf16 v[220:223], v[48:51], v[168:171], 0\n\t"
            "s_add_u32 s49, s33, 31744\n\t"
            "buffer_load_dwordx4 v[124:127], v248, s[28:31], s49 offen sc1\n\t"
            "v_mfma_f32_16x16x32_bf16 v[200:203], v[52:55], v[148:151], v[200:203]\n\t"
            "s_add_u32 s49, s33, 32768\n\t"
            "buffer_load_dwordx4 v[128:131], v248, s[28:31], s49 offen sc1\n\t"
            "v_mfma_f32_16x16x32_bf16 v[220:223], v[52:55], v[172:175], v[220:223]\n\t"
            "s_add_u32 s49, s33, 33792\n\t"
            "buffer_load_dwordx4 v[132:135], v248, s[28:31], s49 offen sc1\n\t"
            "v_mfma_f32_16x16x32_bf16 v[200:203], v[56:59], v[152:155], v[200:203]\n\t"
            "s_add_u32 s49, s33, 34816\n\t"
            "buffer_load_dwordx4 v[136:139], v248, s[28:31], s49 offen sc1\n\t"
            "v_mfma_f32_16x16x32_bf16 v[220:223], v[56:59], v[176:179], v[220:223]\n\t"
            "s_add_u32 s49, s33, 35840\n\t"
            "buffer_load_dwordx4 v[140:143], v248, s[28:31], s49 offen sc1\n\t"
            "s_setprio 0\n\t"
            "v_mfma_f32_16x16x32_bf16 v[200:203], v[60:63], v[156:159], v[200:203]\n\t"
            "s_waitcnt lgkmcnt(1)\n\t"
            "v_mul_f32 v192, v236, v192\n\t"
            "v_mul_f32 v193, v236, v193\n\t"
            "v_mfma_f32_16x16x32_bf16 v[220:223], v[60:63], v[180:183], v[220:223]\n\t"
            "s_waitcnt lgkmcnt(0)\n\t"
            "v_mul_f32 v212, v237, v212\n\t"
            "v_mul_f32 v213, v237, v213\n\t"
            "v_mfma_f32_16x16x32_bf16 v[200:203], v[64:67], v[160:163], v[200:203]\n\t"
            "v_mul_f32 v194, v236, v194\n\t"
            "v_mul_f32 v195, v236, v195\n\t"
            "v_mfma_f32_16x16x32_bf16 v[220:223], v[64:67], v[184:187], v[220:223]\n\t"
            "v_cvt_pk_bf16_f32 v192, v192, v193\n\t"
            "v_mul_f32 v214, v237, v214\n\t"
            "v_mfma_f32_16x16x32_bf16 v[200:203], v[68:71], v[164:167], v[200:203]\n\t"
            "v_mul_f32 v215, v237, v215\n\t"
            "v_cvt_pk_bf16_f32 v212, v212, v213\n\t"
            "v_mfma_f32_16x16x32_bf16 v[220:223], v[68:71], v[188:191], v[220:223]\n\t"
            "s_waitcnt vmcnt(12)\n\t"
            "v_cvt_pk_bf16_f32 v193, v194, v195\n\t"
            "v_mfma_f32_16x16x32_bf16 v[204:207], v[72:75], v[144:147], 0\n\t"
            "v_cvt_pk_bf16_f32 v213, v214, v215\n\t"
            "v_mul_f32 v196, v236, v196\n\t"
            "v_mfma_f32_16x16x32_bf16 v[224:227], v[72:75], v[168:171], 0\n\t"
            "v_mul_f32 v197, v236, v197\n\t"
            "v_mul_f32 v198, v236, v198\n\t"
            "v_mfma_f32_16x16x32_bf16 v[204:207], v[76:79], v[148:151], v[204:207]\n\t"
            "v_mul_f32 v199, v236, v199\n\t"
            "v_mul_f32 v216, v237, v216\n\t"
            "v_mfma_f32_16x16x32_bf16 v[224:227], v[76:79], v[172:175], v[224:227]\n\t"
            "v_mul_f32 v217, v237, v217\n\t"
            "v_mul_f32 v218, v237, v218\n\t"
            "v_mfma_f32_16x16x32_bf16 v[204:207], v[80:83], v[152:155], v[204:207]\n\t"
            "v_mul_f32 v219, v237, v219\n\t"
            "v_cvt_pk_bf16_f32 v194, v196, v197\n\t"
            "v_mfma_f32_16x16x32_bf16 v[224:227], v[80:83], v[176:179], v[224:227]\n\t"
            "v_cvt_pk_bf16_f32 v195, v198, v199\n\t"
            "v_cvt_pk_bf16_f32 v214, v216, v217\n\t"
            "v_mfma_f32_16x16x32_bf16 v[204:207], v[84:87], v[156:159], v[204:207]\n\t"
            "v_cvt_pk_bf16_f32 v215, v218, v219\n\t"
            "v_mul_f32 v200, v236, v200\n\t"
            "v_mfma_f32_16x16x32_bf16 v[224:227], v[84:87], v[180:183], v[224:227]\n\t"
            "v_mul_f32 v201, v236, v201\n\t"
            "v_mul_f32 v220, v237, v220\n\t"
            "v_mfma_f32_16x16x32_bf16 v[204:207], v[88:91], v[160:163], v[204:207]\n\t"
            "v_mul_f32 v221, v237, v221\n\t"
            "v_mul_f32 v202, v236, v202\n\t"
            "v_mfma_f32_16x16x32_bf16 v[224:227], v[88:91], v[184:187], v[224:227]\n\t"
            "v_mul_f32 v203, v236, v203\n\t"
            "v_cvt_pk_bf16_f32 v200, v200, v201\n\t"
            "v_mfma_f32_16x16x32_bf16 v[204:207], v[92:95], v[164:167], v[204:207]\n\t"
            "v_mul_f32 v222, v237, v222\n\t"
            "v_mul_f32 v223, v237, v223\n\t"
            "v_mfma_f32_16x16x32_bf16 v[224:227], v[92:95], v[188:191], v[224:227]\n\t"
            "s_waitcnt vmcnt(6)\n\t"
            "v_cvt_pk_bf16_f32 v220, v220, v221\n\t"
            "v_mfma_f32_16x16x32_bf16 v[196:199], v[96:99], v[144:147], 0\n\t"
            "v_cvt_pk_bf16_f32 v201, v202, v203\n\t"
            "v_cvt_pk_bf16_f32 v221, v222, v223\n\t"
            "v_mfma_f32_16x16x32_bf16 v[216:219], v[96:99], v[168:171], 0\n\t"
            "v_mul_f32 v204, v236, v204\n\t"
            "v_mul_f32 v205, v236, v205\n\t"
            "v_mfma_f32_16x16x32_bf16 v[196:199], v[100:103], v[148:151], v[196:199]\n\t"
            "v_mul_f32 v224, v237, v224\n\t"
            "v_mul_f32 v225, v237, v225\n\t"
            "v_mfma_f32_16x16x32_bf16 v[216:219], v[100:103], v[172:175], v[216:219]\n\t"
            "v_mul_f32 v206, v236, v206\n\t"
            "v_mul_f32 v207, v236, v207\n\t"
            "v_mfma_f32_16x16x32_bf16 v[196:199], v[104:107], v[152:155], v[196:199]\n\t"
            "v_cvt_pk_bf16_f32 v204, v204, v205\n\t"
            "v_mul_f32 v226, v237, v226\n\t"
            "v_mfma_f32_16x16x32_bf16 v[216:219], v[104:107], v[176:179], v[216:219]\n\t"
            "v_mul_f32 v227, v237, v227\n\t"
            "v_cvt_pk_bf16_f32 v224, v224, v225\n\t"
            "v_mfma_f32_16x16x32_bf16 v[196:199], v[108:111], v[156:159], v[196:199]\n\t"
            "v_cvt_pk_bf16_f32 v205, v206, v207\n\t"
            "v_cvt_pk_bf16_f32 v225, v226, v227\n\t"
            "v_mfma_f32_16x16x32_bf16 v[216:219], v[108:111], v[180:183], v[216:219]\n\t"
            "ds_write_b128 v232, v[192:195]\n\t"
            "v_mfma_f32_16x16x32_bf16 v[196:199], v[112:115], v[160:163], v[196:199]\n\t"
            "ds_write_b128 v233, v[212:215]\n\t"
            "v_mfma_f32_16x16x32_bf16 v[216:219], v[112:115], v[184:187], v[216:219]\n\t"
            "v_mfma_f32_16x16x32_bf16 v[196:199], v[116:119], v[164:167], v[196:199]\n\t"
            "v_mfma_f32_16x16x32_bf16 v[216:219], v[116:119], v[188:191], v[216:219]\n\t"
            "s_waitcnt vmcnt(0)\n\t"
            "v_mfma_f32_16x16x32_bf16 v[208:211], v[120:123], v[144:147], 0\n\t"
            "v_mfma_f32_16x16x32_bf16 v[228:231], v[120:123], v[168:171], 0\n\t"
            "v_mfma_f32_16x16x32_bf16 v[208:211], v[124:127], v[148:151], v[208:211]\n\t"
            "v_mfma_f32_16x16x32_bf16 v[228:231], v[124:127], v[172:175], v[228:231]\n\t"
            "s_nop 1\n\t"
            "v_mul_f32 v196, v236, v196\n\t"
            "v_mfma_f32_16x16x32_bf16 v[208:211], v[128:131], v[152:155], v[208:211]\n\t"
            "v_mul_f32 v197, v236, v197\n\t"
            "v_mul_f32 v198, v236, v198\n\t"
            "v_mfma_f32_16x16x32_bf16 v[228:231], v[128:131], v[176:179], v[228:231]\n\t"
            "v_mul_f32 v199, v236, v199\n\t"
            "v_mul_f32 v216, v237, v216\n\t"
            "v_mfma_f32_16x16x32_bf16 v[208:211], v[132:135], v[156:159], v[208:211]\n\t"
            "v_mul_f32 v217, v237, v217\n\t"
            "v_mul_f32 v218, v237, v218\n\t"
            "v_mfma_f32_16x16x32_bf16 v[228:231], v[132:135], v[180:183], v[228:231]\n\t"
            "v_mul_f32 v219, v237, v219\n\t"
            "v_cvt_pk_bf16_f32 v206, v196, v197\n\t"
            "v_mfma_f32_16x16x32_bf16 v[208:211], v[136:139], v[160:163], v[208:211]\n\t"
            "v_cvt_pk_bf16_f32 v207, v198, v199\n\t"
            "v_cvt_pk_bf16_f32 v226, v216, v217\n\t"
            "v_mfma_f32_16x16x32_bf16 v[228:231], v[136:139], v[184:187], v[228:231]\n\t"
            "v_cvt_pk_bf16_f32 v227, v218, v219\n\t"
            "v_mfma_f32_16x16x32_bf16 v[208:211], v[140:143], v[164:167], v[208:211]\n\t"
            "ds_write_b128 v232, v[204:207] offset:64\n\t"
            "v_mfma_f32_16x16x32_bf16 v[228:231], v[140:143], v[188:191], v[228:231]\n\t"
            "ds_write_b128 v233, v[224:227] offset:64\n\t"
            "s_nop 4\n\t"
            "v_mul_f32 v208, v236, v208\n\t"
            "v_mul_f32 v209, v236, v209\n\t"
            "v_mul_f32 v210, v236, v210\n\t"
            "v_mul_f32 v211, v236, v211\n\t"
            "v_cvt_pk_bf16_f32 v202, v208, v209\n\t"
            "v_cvt_pk_bf16_f32 v203, v210, v211\n\t"
            "v_mul_f32 v228, v237, v228\n\t"
            "v_mul_f32 v229, v237, v229\n\t"
            "v_mfma_f32_16x16x16_bf16 v[196:199], v[200:201], v[202:203], 0\n\t"
            "v_mul_f32 v230, v237, v230\n\t"
            "v_mul_f32 v231, v237, v231\n\t"
            "v_cvt_pk_bf16_f32 v222, v228, v229\n\t"
            "v_cvt_pk_bf16_f32 v223, v230, v231\n\t"
            "s_nop 1\n\t"
            "v_mfma_f32_16x16x16_bf16 v[216:219], v[220:221], v[222:223], 0\n\t"
            "ds_write_b128 v232, v[200:203] offset:128\n\t"
            "v_mfma_f32_16x16x32_bf16 v[196:199], v[192:195], v[204:207], v[196:199]\n\t"
            "ds_write_b128 v233, v[220:223] offset:128\n\t"
            "s_nop 2\n\t"
            "v_mfma_f32_16x16x32_bf16 v[216:219], v[212:215], v[224:227], v[216:219]\n\t"
            "s_nop 2\n\t"
            "v_cndmask_b32_e64 v196, v196, v197, s[50:51]\n\t"
            "v_cndmask_b32_e64 v196, v196, v198, s[52:53]\n\t"
            "v_cndmask_b32_e64 v196, v196, v199, s[54:55]\n\t"
            "ds_write_b32 v234, v196\n\t"
            "s_nop 0\n\t"
            "v_cndmask_b32_e64 v216, v216, v217, s[50:51]\n\t"
            "v_cndmask_b32_e64 v216, v216, v218, s[52:53]\n\t"
            "v_cndmask_b32_e64 v216, v216, v219, s[54:55]\n\t"
            "ds_write_b32 v235, v216\n\t"
            "s_waitcnt lgkmcnt(0)\n\t"
            "s_nop 7\n\t"
            "s_cmp_lg_u32 s33, 0\n\t"
            "s_cbranch_scc1 .Lar%=\n\t"
            "s_mov_b64 exec, 1\n\t"
            "v_mov_b32 v216, 1\n\t"
            "s_mov_b32 s49, 295168\n\t"
            "buffer_atomic_add v216, off, s[28:31], s49\n\t"
            "s_mov_b64 exec, -1\n\t"
            ".Lar%=:\n\t"
            "v_mov_b32 v224, s46\n\t"
            ".Ls2%=:\n\t"
            "ds_read_b128 v[220:223], v224\n\t"
            "s_waitcnt lgkmcnt(0)\n\t"
            "v_and_b32 v220, v220, v221\n\t"
            "v_and_b32 v222, v222, v223\n\t"
            "v_and_b32 v220, v220, v222\n\t"
            "s_nop 0\n\t"
            "v_readfirstlane_b32 s49, v220\n\t"
            "s_nop 3\n\t"
            "s_bitcmp1_b32 s49, 31\n\t"
            "s_cbranch_scc0 .Ls2%=\n\t"
            "s_waitcnt lgkmcnt(0)\n\t"
            "s_nop 7\n\t"
            "s_setprio 0\n\t"
            "v_add_u32 v232, s48, v244\n\t"
            "v_xor_b32 v233, 64, v232\n\t"
            "v_add_u32 v208, s48, v248\n\t"
            "v_add_u32 v234, s72, v252\n\t"
            "v_cndmask_b32_e64 v235, v246, v234, s[50:51]\n\t"
            "v_cndmask_b32_e64 v234, v246, v234, s[26:27]\n\t"
            "v_mov_b32 v239, s76\n\t"
            "ds_read_b128 v[216:219], v232\n\t"
            "ds_read_b128 v[220:223], v233\n\t"
            "ds_read_b128 v[224:227], v232 offset:128\n\t"
            "ds_read_b128 v[204:207], v208 offset:18816\n\t"
            "ds_read_b128 v[208:211], v208 offset:19840\n\t"
            "ds_read_b128 v[188:191], v233 offset:6272\n\t"
            "ds_read_b128 v[192:195], v232 offset:6400\n\t"
            "ds_read_b128 v[200:203], v233 offset:6400\n\t"
            "ds_read_b32 v239, v239\n\t"
            "s_waitcnt lgkmcnt(8)\n\t"
            "v_mfma_f32_16x16x32_bf16 v[144:147], v[0:3], v[216:219], 0\n\t"
            "s_waitcnt lgkmcnt(7)\n\t"
            "v_mfma_f32_16x16x32_bf16 v[144:147], v[4:7], v[220:223], v[144:147]\n\t"
            "s_waitcnt lgkmcnt(5)\n\t"
            "v_add_f32 v204, v204, v206\n\t"
            "v_add_f32 v205, v205, v207\n\t"
            "v_mfma_f32_16x16x32_bf16 v[144:147], v[8:11], v[224:227], v[144:147]\n\t"
            "s_waitcnt lgkmcnt(4)\n\t"
            "v_add_f32 v208, v208, v210\n\t"
            "v_add_f32 v209, v209, v211\n\t"
            "v_mfma_f32_16x16x32_bf16 v[212:215], v[120:123], v[216:219], 0\n\t"
            "v_add_f32 v204, v204, v208\n\t"
            "v_add_f32 v205, v205, v209\n\t"
            "v_mfma_f32_16x16x32_bf16 v[212:215], v[124:127], v[220:223], v[212:215]\n\t"
            "v_mul_f32 v204, 0x3baaaaab, v204\n\t"
            "v_mul_f32_e64 v204, v204, -v204\n\t"
            "v_mfma_f32_16x16x32_bf16 v[212:215], v[128:131], v[224:227], v[212:215]\n\t"
            "v_fmac_f32 v204, 0x3baaaaab, v205\n\t"
            "v_max_f32 v204, 0, v204\n\t"
            "v_mfma_f32_16x16x32_bf16 v[152:155], v[48:51], v[216:219], 0\n\t"
            "v_add_f32 v204, 0x3727c5ac, v204\n\t"
            "v_rsq_f32 v204, v204\n\t"
            "v_mfma_f32_16x16x32_bf16 v[152:155], v[52:55], v[220:223], v[152:155]\n\t"
            "ds_write_b32 v249, v204\n\t"
            "v_mfma_f32_16x16x32_bf16 v[152:155], v[56:59], v[224:227], v[152:155]\n\t"
            "ds_read_b32 v236, v251\n\t"
            "v_mfma_f32_16x16x32_bf16 v[208:211], v[96:99], v[216:219], 0\n\t"
            "ds_read_b32 v237, v251 offset:64\n\t"
            "v_mfma_f32_16x16x32_bf16 v[208:211], v[100:103], v[220:223], v[208:211]\n\t"
            "ds_read_b32 v238, v251 offset:128\n\t"
            "v_mfma_f32_16x16x32_bf16 v[208:211], v[104:107], v[224:227], v[208:211]\n\t"
            "v_mfma_f32_16x16x32_bf16 v[148:151], v[72:75], v[216:219], 0\n\t"
            "v_mfma_f32_16x16x32_bf16 v[148:151], v[76:79], v[220:223], v[148:151]\n\t"
            "v_mfma_f32_16x16x32_bf16 v[148:151], v[80:83], v[224:227], v[148:151]\n\t"
            "v_mfma_f32_16x16x32_bf16 v[204:207], v[24:27], v[216:219], 0\n\t"
            "ds_read_b128 v[216:219], v233 offset:128\n\t"
            "v_mfma_f32_16x16x32_bf16 v[204:207], v[28:31], v[220:223], v[204:207]\n\t"
            "ds_read_b128 v[220:223], v232 offset:256\n\t"
            "v_mfma_f32_16x16x32_bf16 v[204:207], v[32:35], v[224:227], v[204:207]\n\t"
            "ds_read_b128 v[224:227], v233 offset:256\n\t"
            "s_waitcnt lgkmcnt(2)\n\t"
            "v_mfma_f32_16x16x32_bf16 v[144:147], v[12:15], v[216:219], v[144:147]\n\t"
            "s_waitcnt lgkmcnt(1)\n\t"
            "v_mfma_f32_16x16x32_bf16 v[144:147], v[16:19], v[220:223], v[144:147]\n\t"
            "s_waitcnt lgkmcnt(0)\n\t"
            "v_mfma_f32_16x16x32_bf16 v[144:147], v[20:23], v[224:227], v[144:147]\n\t"
            "v_mfma_f32_16x16x32_bf16 v[148:151], v[84:87], v[216:219], v[148:151]\n\t"
            "v_mfma_f32_16x16x32_bf16 v[148:151], v[88:91], v[220:223], v[148:151]\n\t"
            "v_mfma_f32_16x16x32_bf16 v[148:151], v[92:95], v[224:227], v[148:151]\n\t"
            "v_mfma_f32_16x16x32_bf16 v[152:155], v[60:63], v[216:219], v[152:155]\n\t"
            "v_mfma_f32_16x16x32_bf16 v[152:155], v[64:67], v[220:223], v[152:155]\n\t"
            "v_mfma_f32_16x16x32_bf16 v[152:155], v[68:71], v[224:227], v[152:155]\n\t"
            "s_nop 1\n\t"
            "v_mul_f32 v144, v236, v144\n\t"
            "v_mfma_f32_16x16x32_bf16 v[204:207], v[36:39], v[216:219], v[204:207]\n\t"
            "v_mul_f32 v145, v236, v145\n\t"
            "v_mul_f32 v146, v236, v146\n\t"
            "v_mfma_f32_16x16x32_bf16 v[204:207], v[40:43], v[220:223], v[204:207]\n\t"
            "v_mul_f32 v147, v236, v147\n\t"
            "v_cvt_pk_bf16_f32 v144, v144, v145\n\t"
            "v_mfma_f32_16x16x32_bf16 v[204:207], v[44:47], v[224:227], v[204:207]\n\t"
            "v_mul_f32 v152, v236, v152\n\t"
            "v_mul_f32 v153, v236, v153\n\t"
            "v_mfma_f32_16x16x32_bf16 v[208:211], v[108:111], v[216:219], v[208:211]\n\t"
            "v_mul_f32 v154, v236, v154\n\t"
            "v_mul_f32 v155, v236, v155\n\t"
            "v_mfma_f32_16x16x32_bf16 v[208:211], v[112:115], v[220:223], v[208:211]\n\t"
            "v_cvt_pk_bf16_f32 v152, v152, v153\n\t"
            "v_cvt_pk_bf16_f32 v153, v154, v155\n\t"
            "v_mfma_f32_16x16x32_bf16 v[208:211], v[116:119], v[224:227], v[208:211]\n\t"
            "v_mul_f32 v148, v236, v148\n\t"
            "v_mul_f32 v149, v236, v149\n\t"
            "v_mfma_f32_16x16x32_bf16 v[212:215], v[132:135], v[216:219], v[212:215]\n\t"
            "ds_read_b128 v[216:219], v232 offset:6144\n\t"
            "v_cvt_pk_bf16_f32 v145, v146, v147\n\t"
            "v_mfma_f32_16x16x32_bf16 v[212:215], v[136:139], v[220:223], v[212:215]\n\t"
            "ds_read_b128 v[220:223], v233 offset:6144\n\t"
            "v_mul_f32 v204, v236, v204\n\t"
            "v_mfma_f32_16x16x32_bf16 v[212:215], v[140:143], v[224:227], v[212:215]\n\t"
            "ds_read_b128 v[224:227], v232 offset:6272\n\t"
            "v_mul_f32 v205, v236, v205\n\t"
            "v_mul_f32 v206, v236, v206\n\t"
            "v_mul_f32 v207, v236, v207\n\t"
            "v_mul_f32 v150, v236, v150\n\t"
            "v_mul_f32 v151, v236, v151\n\t"
            "v_cvt_pk_bf16_f32 v148, v148, v149\n\t"
            "v_cvt_pk_bf16_f32 v146, v204, v205\n\t"
            "v_mul_f32 v212, v236, v212\n\t"
            "v_mul_f32 v213, v236, v213\n\t"
            "v_mul_f32 v214, v236, v214\n\t"
            "v_mul_f32 v215, v236, v215\n\t"
            "v_cvt_pk_bf16_f32 v154, v212, v213\n\t"
            "v_cvt_pk_bf16_f32 v155, v214, v215\n\t"
            "v_cvt_pk_bf16_f32 v147, v206, v207\n\t"
            "v_cvt_pk_bf16_f32 v149, v150, v151\n\t"
            "v_mul_f32 v208, v236, v208\n\t"
            "v_mul_f32 v209, v236, v209\n\t"
            "v_mul_f32 v210, v236, v210\n\t"
            "v_mul_f32 v211, v236, v211\n\t"
            "v_cvt_pk_bf16_f32 v150, v208, v209\n\t"
            "v_cvt_pk_bf16_f32 v151, v210, v211\n\t"
            "s_waitcnt lgkmcnt(2)\n\t"
            "v_mfma_f32_16x16x32_bf16 v[160:163], v[72:75], v[216:219], 0\n\t"
            "ds_read_b128 v[204:207], v232 offset:12288\n\t"
            "s_waitcnt lgkmcnt(2)\n\t"
            "v_mfma_f32_16x16x32_bf16 v[160:163], v[76:79], v[220:223], v[160:163]\n\t"
            "ds_read_b128 v[208:211], v233 offset:12288\n\t"
            "s_waitcnt lgkmcnt(2)\n\t"
            "v_mfma_f32_16x16x32_bf16 v[160:163], v[80:83], v[224:227], v[160:163]\n\t"
            "ds_read_b128 v[212:215], v232 offset:12416\n\t"
            "v_mfma_f32_16x16x32_bf16 v[160:163], v[84:87], v[188:191], v[160:163]\n\t"
            "v_mfma_f32_16x16x32_bf16 v[160:163], v[88:91], v[192:195], v[160:163]\n\t"
            "v_mfma_f32_16x16x32_bf16 v[160:163], v[92:95], v[200:203], v[160:163]\n\t"
            "v_mfma_f32_16x16x32_bf16 v[196:199], v[24:27], v[216:219], 0\n\t"
            "v_mfma_f32_16x16x32_bf16 v[196:199], v[28:31], v[220:223], v[196:199]\n\t"
            "v_mfma_f32_16x16x32_bf16 v[196:199], v[32:35], v[224:227], v[196:199]\n\t"
            "v_mfma_f32_16x16x32_bf16 v[196:199], v[36:39], v[188:191], v[196:199]\n\t"
            "v_mfma_f32_16x16x32_bf16 v[196:199], v[40:43], v[192:195], v[196:199]\n\t"
            "v_mfma_f32_16x16x32_bf16 v[196:199], v[44:47], v[200:203], v[196:199]\n\t"
            "s_nop 1\n\t"
            "v_mul_f32 v160, v237, v160\n\t"
            "v_mfma_f32_16x16x32_bf16 v[156:159], v[0:3], v[216:219], 0\n\t"
            "v_mul_f32 v161, v237, v161\n\t"
            "v_mul_f32 v162, v237, v162\n\t"
            "v_mfma_f32_16x16x32_bf16 v[156:159], v[4:7], v[220:223], v[156:159]\n\t"
            "v_mul_f32 v163, v237, v163\n\t"
            "v_cvt_pk_bf16_f32 v160, v160, v161\n\t"
            "v_mfma_f32_16x16x32_bf16 v[156:159], v[8:11], v[224:227], v[156:159]\n\t"
            "v_mul_f32 v196, v237, v196\n\t"
            "v_mul_f32 v197, v237, v197\n\t"
            "v_mfma_f32_16x16x32_bf16 v[156:159], v[12:15], v[188:191], v[156:159]\n\t"
            "v_mul_f32 v198, v237, v198\n\t"
            "v_mul_f32 v199, v237, v199\n\t"
            "v_mfma_f32_16x16x32_bf16 v[156:159], v[16:19], v[192:195], v[156:159]\n\t"
            "v_cvt_pk_bf16_f32 v161, v162, v163\n\t"
            "v_mfma_f32_16x16x32_bf16 v[156:159], v[20:23], v[200:203], v[156:159]\n\t"
            "v_mfma_f32_16x16x16_bf16 v[180:183], v[152:153], v[154:155], 0\n\t"
            "v_mfma_f32_16x16x32_bf16 v[164:167], v[48:51], v[216:219], 0\n\t"
            "v_mfma_f32_16x16x32_bf16 v[164:167], v[52:55], v[220:223], v[164:167]\n\t"
            "v_mfma_f32_16x16x32_bf16 v[164:167], v[56:59], v[224:227], v[164:167]\n\t"
            "v_mfma_f32_16x16x32_bf16 v[164:167], v[60:63], v[188:191], v[164:167]\n\t"
            "v_mfma_f32_16x16x32_bf16 v[164:167], v[64:67], v[192:195], v[164:167]\n\t"
            "s_nop 1\n\t"
            "v_mul_f32 v156, v237, v156\n\t"
            "v_mfma_f32_16x16x32_bf16 v[164:167], v[68:71], v[200:203], v[164:167]\n\t"
            "v_mul_f32 v157, v237, v157\n\t"
            "v_mul_f32 v158, v237, v158\n\t"
            "v_mfma_f32_16x16x32_bf16 v[228:231], v[96:99], v[216:219], 0\n\t"
            "v_mul_f32 v159, v237, v159\n\t"
            "v_cvt_pk_bf16_f32 v156, v156, v157\n\t"
            "v_mfma_f32_16x16x32_bf16 v[228:231], v[100:103], v[220:223], v[228:231]\n\t"
            "v_cvt_pk_bf16_f32 v157, v158, v159\n\t"
            "v_cvt_pk_bf16_f32 v158, v196, v197\n\t"
            "v_mfma_f32_16x16x32_bf16 v[228:231], v[104:107], v[224:227], v[228:231]\n\t"
            "v_cvt_pk_bf16_f32 v159, v198, v199\n\t"
            "v_mul_f32 v164, v237, v164\n\t"
            "v_mfma_f32_16x16x32_bf16 v[228:231], v[108:111], v[188:191], v[228:231]\n\t"
            "v_mul_f32 v165, v237, v165\n\t"
            "v_mul_f32 v166, v237, v166\n\t"
            "v_mfma_f32_16x16x32_bf16 v[228:231], v[112:115], v[192:195], v[228:231]\n\t"
            "v_mul_f32 v167, v237, v167\n\t"
            "v_cvt_pk_bf16_f32 v164, v164, v165\n\t"
            "v_mfma_f32_16x16x32_bf16 v[228:231], v[116:119], v[200:203], v[228:231]\n\t"
            "v_cvt_pk_bf16_f32 v165, v166, v167\n\t"
            "v_mfma_f32_16x16x32_bf16 v[180:183], v[144:147], v[148:151], v[180:183]\n\t"
            "v_mfma_f32_16x16x32_bf16 v[196:199], v[120:123], v[216:219], 0\n\t"
            "ds_read_b128 v[216:219], v233 offset:12416\n\t"
            "v_mfma_f32_16x16x32_bf16 v[196:199], v[124:127], v[220:223], v[196:199]\n\t"
            "ds_read_b128 v[220:223], v232 offset:12544\n\t"
            "v_mfma_f32_16x16x32_bf16 v[196:199], v[128:131], v[224:227], v[196:199]\n\t"
            "ds_read_b128 v[224:227], v233 offset:12544\n\t"
            "v_mul_f32 v228, v237, v228\n\t"
            "v_mfma_f32_16x16x32_bf16 v[196:199], v[132:135], v[188:191], v[196:199]\n\t"
            "v_mul_f32 v229, v237, v229\n\t"
            "v_mul_f32 v230, v237, v230\n\t"
            "v_mfma_f32_16x16x32_bf16 v[196:199], v[136:139], v[192:195], v[196:199]\n\t"
            "v_mul_f32 v231, v237, v231\n\t"
            "v_cvt_pk_bf16_f32 v162, v228, v229\n\t"
            "v_mfma_f32_16x16x32_bf16 v[196:199], v[140:143], v[200:203], v[196:199]\n\t"
            "v_cvt_pk_bf16_f32 v163, v230, v231\n\t"
            "v_exp_f32 v180, v180\n\t"
            "s_waitcnt lgkmcnt(5)\n\t"
            "v_mfma_f32_16x16x32_bf16 v[176:179], v[48:51], v[204:207], 0\n\t"
            "v_exp_f32 v181, v181\n\t"
            "s_waitcnt lgkmcnt(4)\n\t"
            "v_mfma_f32_16x16x32_bf16 v[176:179], v[52:55], v[208:211], v[176:179]\n\t"
            "v_exp_f32 v182, v182\n\t"
            "s_waitcnt lgkmcnt(3)\n\t"
            "v_mfma_f32_16x16x32_bf16 v[176:179], v[56:59], v[212:215], v[176:179]\n\t"
            "v_mul_f32 v196, v237, v196\n\t"
            "v_mul_f32 v197, v237, v197\n\t"
            "s_waitcnt lgkmcnt(2)\n\t"
            "v_mfma_f32_16x16x32_bf16 v[176:179], v[60:63], v[216:219], v[176:179]\n\t"
            "v_mul_f32 v198, v237, v198\n\t"
            "v_mul_f32 v199, v237, v199\n\t"
            "s_waitcnt lgkmcnt(1)\n\t"
            "v_mfma_f32_16x16x32_bf16 v[176:179], v[64:67], v[220:223], v[176:179]\n\t"
            "v_cvt_pk_bf16_f32 v166, v196, v197\n\t"
            "v_cvt_pk_bf16_f32 v167, v198, v199\n\t"
            "s_waitcnt lgkmcnt(0)\n\t"
            "v_mfma_f32_16x16x32_bf16 v[176:179], v[68:71], v[224:227], v[176:179]\n\t"
            "v_exp_f32 v183, v183\n\t"
            "v_cvt_pk_bf16_f32 v180, v180, v181\n\t"
            "v_cvt_pk_bf16_f32 v181, v182, v183\n\t"
            "v_mfma_f32_16x16x16_bf16 v[188:191], v[152:153], v[166:167], 0\n\t"
            "v_mfma_f32_16x16x16_bf16 v[192:195], v[164:165], v[166:167], 0\n\t"
            "v_mfma_f32_16x16x16_bf16 v[184:187], v[164:165], v[154:155], 0\n\t"
            "s_nop 1\n\t"
            "v_mul_f32 v176, v238, v176\n\t"
            "v_mfma_f32_16x16x32_bf16 v[168:171], v[0:3], v[204:207], 0\n\t"
            "v_mul_f32 v177, v238, v177\n\t"
            "v_mul_f32 v178, v238, v178\n\t"
            "v_mfma_f32_16x16x32_bf16 v[168:171], v[4:7], v[208:211], v[168:171]\n\t"
            "v_mul_f32 v179, v238, v179\n\t"
            "v_cvt_pk_bf16_f32 v176, v176, v177\n\t"
            "v_mfma_f32_16x16x32_bf16 v[168:171], v[8:11], v[212:215], v[168:171]\n\t"
            "v_cvt_pk_bf16_f32 v177, v178, v179\n\t"
            "v_mfma_f32_16x16x32_bf16 v[168:171], v[12:15], v[216:219], v[168:171]\n\t"
            "v_mfma_f32_16x16x32_bf16 v[168:171], v[16:19], v[220:223], v[168:171]\n\t"
            "v_mfma_f32_16x16x32_bf16 v[168:171], v[20:23], v[224:227], v[168:171]\n\t"
            "v_mfma_f32_16x16x32_bf16 v[188:191], v[144:147], v[160:163], v[188:191]\n\t"
            "v_mfma_f32_16x16x32_bf16 v[200:203], v[120:123], v[204:207], 0\n\t"
            "v_mfma_f32_16x16x32_bf16 v[200:203], v[124:127], v[208:211], v[200:203]\n\t"
            "v_mfma_f32_16x16x32_bf16 v[200:203], v[128:131], v[212:215], v[200:203]\n\t"
            "v_mfma_f32_16x16x32_bf16 v[200:203], v[132:135], v[216:219], v[200:203]\n\t"
            "v_mfma_f32_16x16x32_bf16 v[200:203], v[136:139], v[220:223], v[200:203]\n\t"
            "s_nop 1\n\t"
            "v_mul_f32 v168, v238, v168\n\t"
            "v_mfma_f32_16x16x32_bf16 v[200:203], v[140:143], v[224:227], v[200:203]\n\t"
            "v_exp_f32 v188, v188\n\t"
            "v_mfma_f32_16x16x32_bf16 v[192:195], v[156:159], v[160:163], v[192:195]\n\t"
            "v_exp_f32 v189, v189\n\t"
            "v_mfma_f32_16x16x32_bf16 v[172:175], v[72:75], v[204:207], 0\n\t"
            "v_exp_f32 v190, v190\n\t"
            "v_mfma_f32_16x16x32_bf16 v[172:175], v[76:79], v[208:211], v[172:175]\n\t"
            "v_exp_f32 v191, v191\n\t"
            "v_mfma_f32_16x16x32_bf16 v[172:175], v[80:83], v[212:215], v[172:175]\n\t"
            "v_cvt_pk_bf16_f32 v188, v188, v189\n\t"
            "v_cvt_pk_bf16_f32 v189, v190, v191\n\t"
            "v_mfma_f32_16x16x32_bf16 v[172:175], v[84:87], v[216:219], v[172:175]\n\t"
            "v_exp_f32 v192, v192\n\t"
            "v_mfma_f32_16x16x32_bf16 v[172:175], v[88:91], v[220:223], v[172:175]\n\t"
            "v_exp_f32 v193, v193\n\t"
            "v_mfma_f32_16x16x32_bf16 v[172:175], v[92:95], v[224:227], v[172:175]\n\t"
            "v_exp_f32 v194, v194\n\t"
            "v_mfma_f32_16x16x32_bf16 v[184:187], v[156:159], v[148:151], v[184:187]\n\t"
            "v_exp_f32 v195, v195\n\t"
            "v_mfma_f32_16x16x32_bf16 v[196:199], v[24:27], v[204:207], 0\n\t"
            "v_cvt_pk_bf16_f32 v190, v192, v193\n\t"
            "v_cvt_pk_bf16_f32 v191, v194, v195\n\t"
            "v_mfma_f32_16x16x32_bf16 v[196:199], v[28:31], v[208:211], v[196:199]\n\t"
            "v_mul_f32 v169, v238, v169\n\t"
            "v_mul_f32 v170, v238, v170\n\t"
            "v_mfma_f32_16x16x32_bf16 v[196:199], v[32:35], v[212:215], v[196:199]\n\t"
            "v_exp_f32 v184, v184\n\t"
            "v_mfma_f32_16x16x32_bf16 v[196:199], v[36:39], v[216:219], v[196:199]\n\t"
            "v_exp_f32 v185, v185\n\t"
            "v_mfma_f32_16x16x32_bf16 v[196:199], v[40:43], v[220:223], v[196:199]\n\t"
            "v_exp_f32 v186, v186\n\t"
            "v_mfma_f32_16x16x32_bf16 v[196:199], v[44:47], v[224:227], v[196:199]\n\t"
            "v_exp_f32 v187, v187\n\t"
            "v_cvt_pk_bf16_f32 v182, v184, v185\n\t"
            "v_cvt_pk_bf16_f32 v183, v186, v187\n\t"
            "v_mfma_f32_16x16x16_bf16 v[184:187], v[176:177], v[154:155], 0\n\t"
            "v_mul_f32 v171, v238, v171\n\t"
            "v_cvt_pk_bf16_f32 v168, v168, v169\n\t"
            "v_cvt_pk_bf16_f32 v169, v170, v171\n\t"
            "v_mfma_f32_16x16x16_bf16 v[192:195], v[176:177], v[166:167], 0\n\t"
            "v_mul_f32 v196, v238, v196\n\t"
            "v_mul_f32 v197, v238, v197\n\t"
            "v_mul_f32 v198, v238, v198\n\t"
            "v_mfma_f32_16x16x32_bf16 v[228:231], v[96:99], v[204:207], 0\n\t"
            "ds_read_b64 v[204:205], v235 offset:136\n\t"
            "v_mul_f32 v199, v238, v199\n\t"
            "v_mfma_f32_16x16x32_bf16 v[228:231], v[100:103], v[208:211], v[228:231]\n\t"
            "ds_read_b64 v[206:207], v234 offset:128\n\t"
            "v_cvt_pk_bf16_f32 v170, v196, v197\n\t"
            "v_mfma_f32_16x16x32_bf16 v[228:231], v[104:107], v[212:215], v[228:231]\n\t"
            "ds_read_b128 v[208:211], v234\n\t"
            "v_cvt_pk_bf16_f32 v171, v198, v199\n\t"
            "v_mfma_f32_16x16x32_bf16 v[228:231], v[108:111], v[216:219], v[228:231]\n\t"
            "ds_read_b128 v[212:215], v235 offset:64\n\t"
            "v_mul_f32 v172, v238, v172\n\t"
            "v_mfma_f32_16x16x32_bf16 v[228:231], v[112:115], v[220:223], v[228:231]\n\t"
            "v_mul_f32 v173, v238, v173\n\t"
            "v_mul_f32 v200, v238, v200\n\t"
            "v_mfma_f32_16x16x32_bf16 v[228:231], v[116:119], v[224:227], v[228:231]\n\t"
            "v_mul_f32 v201, v238, v201\n\t"
            "v_mul_f32 v202, v238, v202\n\t"
            "v_mfma_f32_16x16x32_bf16 v[184:187], v[168:171], v[148:151], v[184:187]\n\t"
            "v_mul_f32 v203, v238, v203\n\t"
            "v_mul_f32 v174, v238, v174\n\t"
            "v_mfma_f32_16x16x32_bf16 v[192:195], v[168:171], v[160:163], v[192:195]\n\t"
            "v_mul_f32 v175, v238, v175\n\t"
            "v_cvt_pk_bf16_f32 v172, v172, v173\n\t"
            "v_cvt_pk_bf16_f32 v178, v200, v201\n\t"
            "v_cvt_pk_bf16_f32 v179, v202, v203\n\t"
            "v_cvt_pk_bf16_f32 v173, v174, v175\n\t"
            "v_mul_f32 v228, v238, v228\n\t"
            "v_mul_f32 v229, v238, v229\n\t"
            "s_waitcnt lgkmcnt(2)\n\t"
            "v_mfma_f32_16x16x32_bf16 v[220:223], v[204:207], v[164:167], 0\n\t"
            "v_mul_f32 v230, v238, v230\n\t"
            "v_mul_f32 v231, v238, v231\n\t"
            "s_waitcnt lgkmcnt(1)\n\t"
            "v_mfma_f32_16x16x32_bf16 v[220:223], v[208:211], v[160:163], v[220:223]\n\t"
            "v_cvt_pk_bf16_f32 v174, v228, v229\n\t"
            "v_cvt_pk_bf16_f32 v175, v230, v231\n\t"
            "s_waitcnt lgkmcnt(0)\n\t"
            "v_mfma_f32_16x16x32_bf16 v[220:223], v[212:215], v[156:159], v[220:223]\n\t"
            "v_exp_f32 v184, v184\n\t"
            "v_mfma_f32_16x16x32_bf16 v[216:219], v[204:207], v[152:155], 0\n\t"
            "v_exp_f32 v185, v185\n\t"
            "v_mfma_f32_16x16x32_bf16 v[216:219], v[208:211], v[148:151], v[216:219]\n\t"
            "v_exp_f32 v186, v186\n\t"
            "v_mfma_f32_16x16x32_bf16 v[216:219], v[212:215], v[144:147], v[216:219]\n\t"
            "v_exp_f32 v187, v187\n\t"
            "v_cvt_pk_bf16_f32 v184, v184, v185\n\t"
            "v_cvt_pk_bf16_f32 v185, v186, v187\n\t"
            "v_mfma_f32_16x16x16_bf16 v[228:231], v[164:165], v[178:179], 0\n\t"
            "v_mov_b32 v187, 0\n\t"
            "v_exp_f32 v192, v192\n\t"
            "v_mfma_f32_16x16x16_bf16 v[200:203], v[176:177], v[178:179], 0\n\t"
            "v_exp_f32 v193, v193\n\t"
            "v_exp_f32 v216, v216\n\t"
            "v_mfma_f32_16x16x16_bf16 v[196:199], v[152:153], v[178:179], 0\n\t"
            "v_cvt_pk_bf16_f32 v216, v216, 0\n\t"
            "v_cndmask_b32_e64 v186, 0, v216, s[58:59]\n\t"
            "v_cndmask_b32_e64 v235, v217, v221, s[60:61]\n\t"
            "v_mfma_f32_16x16x32_bf16 v[224:227], v[204:207], v[176:179], 0\n\t"
            "v_exp_f32 v194, v194\n\t"
            "v_mfma_f32_16x16x32_bf16 v[224:227], v[208:211], v[172:175], v[224:227]\n\t"
            "v_exp_f32 v195, v195\n\t"
            "v_mfma_f32_16x16x32_bf16 v[224:227], v[212:215], v[168:171], v[224:227]\n\t"
            "v_exp_f32 v220, v220\n\t"
            "v_mfma_f32_16x16x32_bf16 v[204:207], v[240:243], v[180:183], 0\n\t"
            "v_cvt_pk_bf16_f32 v192, v192, v193\n\t"
            "v_cvt_pk_bf16_f32 v193, v194, v195\n\t"
            "v_mfma_f32_16x16x32_bf16 v[204:207], v[240:243], v[184:187], v[204:207]\n\t"
            "v_cvt_pk_bf16_f32 v220, v220, 0\n\t"
            "v_cndmask_b32_e64 v194, 0, v220, s[58:59]\n\t"
            "v_mfma_f32_16x16x32_bf16 v[200:203], v[168:171], v[172:175], v[200:203]\n\t"
            "v_exp_f32 v224, v224\n\t"
            "v_mfma_f32_16x16x32_bf16 v[196:199], v[144:147], v[172:175], v[196:199]\n\t"
            "v_cvt_pk_bf16_f32 v224, v224, 0\n\t"
            "v_cndmask_b32_e64 v235, v235, v225, s[62:63]\n\t"
            "v_mfma_f32_16x16x32_bf16 v[228:231], v[156:159], v[172:175], v[228:231]\n\t"
            "v_cndmask_b32_e64 v235, v235, v239, s[64:65]\n\t"
            "v_cmp_nlt_f32_e64 s[24:25], |v235|, s23\n\t"
            "v_exp_f32 v235, v235\n\t"
            "v_exp_f32 v200, v200\n\t"
            "v_exp_f32 v201, v201\n\t"
            "v_exp_f32 v196, v196\n\t"
            "v_exp_f32 v197, v197\n\t"
            "v_exp_f32 v198, v198\n\t"
            "v_exp_f32 v199, v199\n\t"
            "v_exp_f32 v228, v228\n\t"
            "v_exp_f32 v229, v229\n\t"
            "v_exp_f32 v230, v230\n\t"
            "v_exp_f32 v231, v231\n\t"
            "v_cvt_pk_bf16_f32 v196, v196, v197\n\t"
            "v_cvt_pk_bf16_f32 v197, v198, v199\n\t"
            "v_cvt_pk_bf16_f32 v198, v228, v229\n\t"
            "v_cvt_pk_bf16_f32 v199, v230, v231\n\t"
            "v_exp_f32 v202, v202\n\t"
            "v_exp_f32 v203, v203\n\t"
            "v_mfma_f32_16x16x32_bf16 v[212:215], v[240:243], v[196:199], 0\n\t"
            "v_cvt_pk_bf16_f32 v200, v200, v201\n\t"
            "v_cvt_pk_bf16_f32 v201, v202, v203\n\t"
            "v_cndmask_b32_e64 v202, 0, v224, s[58:59]\n\t"
            "v_mov_b32 v203, 0\n\t"
            "v_cvt_pk_bf16_f32 v235, v235, 0\n\t"
            "v_cndmask_b32_e64 v235, 0, v235, s[66:67]\n\t"
            "v_mfma_f32_16x16x32_bf16 v[212:215], v[240:243], v[200:203], v[212:215]\n\t"
            "ds_write_b16 v250, v235 offset:256\n\t"
            "v_mfma_f32_16x16x32_bf16 v[208:211], v[240:243], v[188:191], 0\n\t"
            "v_mov_b32 v195, 0\n\t"
            "v_add_u32 v205, 0xe0800000, v204\n\t"
            "v_rcp_f32 v236, v204\n\t"
            "v_mfma_f32_16x16x32_bf16 v[208:211], v[240:243], v[192:195], v[208:211]\n\t"
            "s_nop 1\n\t"
            "v_add_u32 v213, 0xe0800000, v212\n\t"
            "v_rcp_f32 v238, v212\n\t"
            "s_nop 3\n\t"
            "v_add_u32 v209, 0xe0800000, v208\n\t"
            "v_rcp_f32 v237, v208\n\t"
            "v_or3_b32 v205, v205, v209, v213\n\t"
            "v_cmp_le_u32 vcc, 2.0, v205\n\t"
            "s_waitcnt lgkmcnt(0)\n\t"
            "s_nop 7\n\t"
            "s_bfe_u32 s49, s70, 0x30001\n\t"
            "s_lshl_b32 s49, s49, 4\n\t"
            "s_or_b32 s73, s77, s49\n\t"
            "s_add_u32 s70, s70, 1\n\t"
            "s_add_u32 s76, s76, 800\n\t"
            "s_add_u32 s77, s77, 384\n\t"
            "s_xor_b32 s48, s48, s47\n\t"
            "s_sub_u32 s44, s44, 1\n\t"
            "s_branch .Lwa_in%=\n\t"
            ".p2align 6\n\t"
            ".Lwa_loop%=:\n\t"
            "ds_read_b64 v[156:157], v247\n\t"
            "v_add_u32 v235, s45, v248\n\t"
            "v_xor_b32 v234, s73, v254\n\t"
            "v_add_u32 v232, s48, v244\n\t"
            "v_xor_b32 v233, 64, v232\n\t"
            "v_mov_b32 v159, 0\n\t"
            "v_add_u32 v208, s48, v248\n\t"
            "v_mov_b32 v167, 0\n\t"
            "v_mov_b32 v163, 0\n\t"
            "v_mov_b32 v239, s76\n\t"
            "ds_read_b128 v[168:171], v235\n\t"
            "ds_read_u16 v158, v234\n\t"
            "ds_read_b128 v[216:219], v232\n\t"
            "ds_read_b128 v[220:223], v233\n\t"
            "ds_read_b128 v[224:227], v232 offset:128\n\t"
            "ds_read_b128 v[172:175], v235 offset:1536\n\t"
            "ds_read_b64 v[164:165], v247 offset:1536\n\t"
            "ds_read_u16 v166, v234 offset:128\n\t"
            "ds_read_b128 v[176:179], v235 offset:3072\n\t"
            "ds_read_b64 v[160:161], v247 offset:3072\n\t"
            "ds_read_u16 v162, v234 offset:256\n\t"
            "v_add_u32 v234, s72, v252\n\t"
            "v_cndmask_b32_e64 v235, v246, v234, s[50:51]\n\t"
            "v_cndmask_b32_e64 v234, v246, v234, s[26:27]\n\t"
            "ds_read_b128 v[204:207], v208 offset:18816\n\t"
            "ds_read_b128 v[208:211], v208 offset:19840\n\t"
            "s_waitcnt lgkmcnt(14)\n\t"
            "ds_read_b32 v239, v239\n\t"
            "s_waitcnt lgkmcnt(13)\n\t"
            "v_mfma_f32_16x16x32_bf16 v[228:231], v[168:171], v[180:183], 0\n\t"
            "s_waitcnt lgkmcnt(12)\n\t"
            "v_mfma_f32_16x16x32_bf16 v[228:231], v[156:159], v[184:187], v[228:231]\n\t"
            "s_waitcnt lgkmcnt(11)\n\t"
            "v_mfma_f32_16x16x32_bf16 v[144:147], v[0:3], v[216:219], 0\n\t"
            "s_waitcnt lgkmcnt(10)\n\t"
            "v_mfma_f32_16x16x32_bf16 v[144:147], v[4:7], v[220:223], v[144:147]\n\t"
            "s_waitcnt lgkmcnt(9)\n\t"
            "v_mfma_f32_16x16x32_bf16 v[144:147], v[8:11], v[224:227], v[144:147]\n\t"
            "v_mfma_f32_16x16x32_bf16 v[212:215], v[120:123], v[216:219], 0\n\t"
            "v_mfma_f32_16x16x32_bf16 v[212:215], v[124:127], v[220:223], v[212:215]\n\t"
            "s_waitcnt lgkmcnt(2)\n\t"
            "v_add_f32 v204, v204, v206\n\t"
            "v_add_f32 v205, v205, v207\n\t"
            "v_mfma_f32_16x16x32_bf16 v[212:215], v[128:131], v[224:227], v[212:215]\n\t"
            "v_mul_f32 v228, v236, v228\n\t"
            "v_mul_f32 v229, v236, v229\n\t"
            "v_mfma_f32_16x16x32_bf16 v[152:155], v[48:51], v[216:219], 0\n\t"
            "v_mul_f32 v230, v236, v230\n\t"
            "v_mul_f32 v231, v236, v231\n\t"
            "v_mfma_f32_16x16x32_bf16 v[152:155], v[52:55], v[220:223], v[152:155]\n\t"
            "v_cvt_pk_bf16_f32 v228, v228, v229\n\t"
            "v_cvt_pk_bf16_f32 v229, v230, v231\n\t"
            "v_mfma_f32_16x16x32_bf16 v[152:155], v[56:59], v[224:227], v[152:155]\n\t"
            "ds_write_b64 v245, v[228:229]\n\t"
            "v_mfma_f32_16x16x32_bf16 v[228:231], v[172:175], v[180:183], 0\n\t"
            "s_waitcnt lgkmcnt(2)\n\t"
            "v_add_f32 v208, v208, v210\n\t"
            "v_add_f32 v209, v209, v211\n\t"
            "v_mfma_f32_16x16x32_bf16 v[228:231], v[164:167], v[184:187], v[228:231]\n\t"
            "v_add_f32 v204, v204, v208\n\t"
            "v_add_f32 v205, v205, v209\n\t"
            "v_mfma_f32_16x16x32_bf16 v[180:183], v[176:179], v[180:183], 0\n\t"
            "v_mul_f32 v204, 0x3baaaaab, v204\n\t"
            "v_mul_f32_e64 v204, v204, -v204\n\t"
            "v_mfma_f32_16x16x32_bf16 v[180:183], v[160:163], v[184:187], v[180:183]\n\t"
            "v_fmac_f32 v204, 0x3baaaaab, v205\n\t"
            "v_max_f32 v204, 0, v204\n\t"
            "v_mfma_f32_16x16x32_bf16 v[208:211], v[96:99], v[216:219], 0\n\t"
            "v_add_f32 v204, 0x3727c5ac, v204\n\t"
            "v_mul_f32 v228, v236, v228\n\t"
            "v_mfma_f32_16x16x32_bf16 v[208:211], v[100:103], v[220:223], v[208:211]\n\t"
            "v_rsq_f32 v204, v204\n\t"
            "v_mfma_f32_16x16x32_bf16 v[208:211], v[104:107], v[224:227], v[208:211]\n\t"
            "ds_write_b32 v249, v204\n\t"
            "v_mfma_f32_16x16x32_bf16 v[184:187], v[168:171], v[188:191], 0\n\t"
            "v_mul_f32 v229, v236, v229\n\t"
            "v_mul_f32 v230, v236, v230\n\t"
            "v_mfma_f32_16x16x32_bf16 v[184:187], v[156:159], v[192:195], v[184:187]\n\t"
            "v_mul_f32 v231, v236, v231\n\t"
            "v_cvt_pk_bf16_f32 v228, v228, v229\n\t"
            "v_mfma_f32_16x16x32_bf16 v[148:151], v[72:75], v[216:219], 0\n\t"
            "v_cvt_pk_bf16_f32 v229, v230, v231\n\t"
            "v_mul_f32 v180, v236, v180\n\t"
            "v_mfma_f32_16x16x32_bf16 v[148:151], v[76:79], v[220:223], v[148:151]\n\t"
            "v_mul_f32 v181, v236, v181\n\t"
            "v_mul_f32 v182, v236, v182\n\t"
            "v_mfma_f32_16x16x32_bf16 v[148:151], v[80:83], v[224:227], v[148:151]\n\t"
            "ds_write_b64 v245, v[228:229] offset:128\n\t"
            "v_mfma_f32_16x16x32_bf16 v[204:207], v[24:27], v[216:219], 0\n\t"
            "ds_read_b128 v[216:219], v233 offset:128\n\t"
            "v_mul_f32 v183, v236, v183\n\t"
            "v_mfma_f32_16x16x32_bf16 v[204:207], v[28:31], v[220:223], v[204:207]\n\t"
            "ds_read_b128 v[220:223], v232 offset:256\n\t"
            "v_cvt_pk_bf16_f32 v180, v180, v181\n\t"
            "v_mfma_f32_16x16x32_bf16 v[204:207], v[32:35], v[224:227], v[204:207]\n\t"
            "ds_read_b128 v[224:227], v233 offset:256\n\t"
            "v_cvt_pk_bf16_f32 v181, v182, v183\n\t"
            "v_mfma_f32_16x16x32_bf16 v[228:231], v[172:175], v[188:191], 0\n\t"
            "ds_read_b32 v236, v251\n\t"
            "v_mul_f32 v184, v237, v184\n\t"
            "v_mfma_f32_16x16x32_bf16 v[228:231], v[164:167], v[192:195], v[228:231]\n\t"
            "ds_write_b64 v245, v[180:181] offset:256\n\t"
            "v_mfma_f32_16x16x32_bf16 v[188:191], v[176:179], v[188:191], 0\n\t"
            "v_mul_f32 v185, v237, v185\n\t"
            "v_mul_f32 v186, v237, v186\n\t"
            "v_mfma_f32_16x16x32_bf16 v[188:191], v[160:163], v[192:195], v[188:191]\n\t"
            "ds_read_b128 v[192:195], v232 offset:6400\n\t"
            "v_mul_f32 v187, v237, v187\n\t"
            "v_mfma_f32_16x16x32_bf16 v[180:183], v[168:171], v[196:199], 0\n\t"
            "v_mul_f32 v228, v237, v228\n\t"
            "v_mul_f32 v229, v237, v229\n\t"
            "v_mfma_f32_16x16x32_bf16 v[180:183], v[156:159], v[200:203], v[180:183]\n\t"
            "s_waitcnt lgkmcnt(5)\n\t"
            "v_mul_f32 v230, v237, v230\n\t"
            "v_mul_f32 v231, v237, v231\n\t"
            "v_mfma_f32_16x16x32_bf16 v[144:147], v[12:15], v[216:219], v[144:147]\n\t"
            "s_waitcnt lgkmcnt(4)\n\t"
            "v_cvt_pk_bf16_f32 v228, v228, v229\n\t"
            "v_cvt_pk_bf16_f32 v229, v230, v231\n\t"
            "v_mfma_f32_16x16x32_bf16 v[144:147], v[16:19], v[220:223], v[144:147]\n\t"
            "s_waitcnt lgkmcnt(3)\n\t"
            "v_cvt_pk_bf16_f32 v184, v184, v185\n\t"
            "v_cvt_pk_bf16_f32 v185, v186, v187\n\t"
            "v_mfma_f32_16x16x32_bf16 v[144:147], v[20:23], v[224:227], v[144:147]\n\t"
            "ds_read_b64 v[186:187], v253 offset:352\n\t"
            "v_mul_f32 v180, v238, v180\n\t"
            "v_mfma_f32_16x16x32_bf16 v[148:151], v[84:87], v[216:219], v[148:151]\n\t"
            "v_mul_f32 v181, v238, v181\n\t"
            "v_mul_f32 v182, v238, v182\n\t"
            "v_mfma_f32_16x16x32_bf16 v[148:151], v[88:91], v[220:223], v[148:151]\n\t"
            "v_mul_f32 v183, v238, v183\n\t"
            "v_cvt_pk_bf16_f32 v180, v180, v181\n\t"
            "v_mfma_f32_16x16x32_bf16 v[148:151], v[92:95], v[224:227], v[148:151]\n\t"
            "ds_write_b64 v245, v[228:229] offset:6272\n\t"
            "v_mfma_f32_16x16x32_bf16 v[152:155], v[60:63], v[216:219], v[152:155]\n\t"
            "v_cvt_pk_bf16_f32 v181, v182, v183\n\t"
            "v_mul_f32 v188, v237, v188\n\t"
            "v_mfma_f32_16x16x32_bf16 v[152:155], v[64:67], v[220:223], v[152:155]\n\t"
            "ds_read_b64 v[182:183], v253 offset:288\n\t"
            "v_mul_f32 v189, v237, v189\n\t"
            "v_mfma_f32_16x16x32_bf16 v[152:155], v[68:71], v[224:227], v[152:155]\n\t"
            "ds_write_b64 v245, v[180:181] offset:12288\n\t"
            "v_mfma_f32_16x16x32_bf16 v[228:231], v[172:175], v[196:199], 0\n\t"
            "ds_read_b64 v[180:181], v253 offset:256\n\t"
            "v_mul_f32 v190, v237, v190\n\t"
            "v_mfma_f32_16x16x32_bf16 v[228:231], v[164:167], v[200:203], v[228:231]\n\t"
            "ds_write_b64 v245, v[184:185] offset:6144\n\t"
            "v_mfma_f32_16x16x32_bf16 v[196:199], v[176:179], v[196:199], 0\n\t"
            "ds_read_b64 v[184:185], v253 offset:320\n\t"
            "v_mul_f32 v191, v237, v191\n\t"
            "v_mfma_f32_16x16x32_bf16 v[196:199], v[160:163], v[200:203], v[196:199]\n\t"
            "ds_read_b128 v[200:203], v233 offset:6400\n\t"
            "v_cvt_pk_bf16_f32 v188, v188, v189\n\t"
            "v_mfma_f32_16x16x32_bf16 v[204:207], v[36:39], v[216:219], v[204:207]\n\t"
            "ds_read_b32 v237, v251 offset:64\n\t"
            "v_cvt_pk_bf16_f32 v189, v190, v191\n\t"
            "v_mfma_f32_16x16x32_bf16 v[204:207], v[40:43], v[220:223], v[204:207]\n\t"
            "v_mul_f32 v228, v238, v228\n\t"
            "v_mul_f32 v229, v238, v229\n\t"
            "v_mfma_f32_16x16x32_bf16 v[204:207], v[44:47], v[224:227], v[204:207]\n\t"
            "ds_write_b64 v245, v[188:189] offset:6400\n\t"
            "v_mfma_f32_16x16x32_bf16 v[208:211], v[108:111], v[216:219], v[208:211]\n\t"
            "ds_read_b128 v[188:191], v233 offset:6272\n\t"
            "v_mul_f32 v230, v238, v230\n\t"
            "v_mfma_f32_16x16x32_bf16 v[208:211], v[112:115], v[220:223], v[208:211]\n\t"
            "v_mul_f32 v231, v238, v231\n\t"
            "v_cvt_pk_bf16_f32 v228, v228, v229\n\t"
            "v_mfma_f32_16x16x32_bf16 v[208:211], v[116:119], v[224:227], v[208:211]\n\t"
            "v_cvt_pk_bf16_f32 v229, v230, v231\n\t"
            "v_mul_f32 v196, v238, v196\n\t"
            "v_mfma_f32_16x16x32_bf16 v[212:215], v[132:135], v[216:219], v[212:215]\n\t"
            "ds_read_b128 v[216:219], v232 offset:6144\n\t"
            "v_mul_f32 v197, v238, v197\n\t"
            "v_mfma_f32_16x16x32_bf16 v[212:215], v[136:139], v[220:223], v[212:215]\n\t"
            "s_waitcnt lgkmcnt(14)\n\t"
            "ds_read_b128 v[220:223], v233 offset:6144\n\t"
            "v_mul_f32 v198, v238, v198\n\t"
            "v_mfma_f32_16x16x32_bf16 v[212:215], v[140:143], v[224:227], v[212:215]\n\t"
            "s_waitcnt lgkmcnt(14)\n\t"
            "ds_read_b128 v[224:227], v232 offset:6272\n\t"
            "v_mul_f32 v199, v238, v199\n\t"
            "s_waitcnt lgkmcnt(9)\n\t"
            "v_mfma_f32_16x16x32_bf16 v[168:171], v[168:171], v[180:183], 0\n\t"
            "s_waitcnt lgkmcnt(7)\n\t"
            "ds_read_b32 v238, v251 offset:128\n\t"
            "v_cvt_pk_bf16_f32 v196, v196, v197\n\t"
            "v_mfma_f32_16x16x32_bf16 v[168:171], v[156:159], v[184:187], v[168:171]\n\t"
            "ds_write_b64 v245, v[228:229] offset:12416\n\t"
            "v_mfma_f32_16x16x32_bf16 v[176:179], v[176:179], v[180:183], 0\n\t"
            "v_cvt_pk_bf16_f32 v197, v198, v199\n\t"
            "v_mul_f32 v152, v236, v152\n\t"
            "v_mfma_f32_16x16x32_bf16 v[176:179], v[160:163], v[184:187], v[176:179]\n\t"
            "ds_write_b64 v245, v[196:197] offset:12544\n\t"
            "v_mfma_f32_16x16x32_bf16 v[172:175], v[172:175], v[180:183], 0\n\t"
            "v_mul_f32 v153, v236, v153\n\t"
            "v_mul_f32 v154, v236, v154\n\t"
            "v_mfma_f32_16x16x32_bf16 v[172:175], v[164:167], v[184:187], v[172:175]\n\t"
            "s_waitcnt lgkmcnt(5)\n\t"
            "v_mul_f32 v155, v236, v155\n\t"
            "v_cvt_pk_bf16_f32 v152, v152, v153\n\t"
            "v_mfma_f32_16x16x32_bf16 v[160:163], v[72:75], v[216:219], 0\n\t"
            "s_waitcnt lgkmcnt(4)\n\t"
            "v_cvt_pk_bf16_f32 v153, v154, v155\n\t"
            "v_mul_f32 v212, v236, v212\n\t"
            "v_mfma_f32_16x16x32_bf16 v[160:163], v[76:79], v[220:223], v[160:163]\n\t"
            "s_waitcnt lgkmcnt(3)\n\t"
            "v_mul_f32 v213, v236, v213\n\t"
            "v_mul_f32 v214, v236, v214\n\t"
            "v_mfma_f32_16x16x32_bf16 v[160:163], v[80:83], v[224:227], v[160:163]\n\t"
            "v_mul_f32 v215, v236, v215\n\t"
            "v_cvt_pk_bf16_f32 v154, v212, v213\n\t"
            "v_mfma_f32_16x16x32_bf16 v[160:163], v[84:87], v[188:191], v[160:163]\n\t"
            "v_cvt_pk_bf16_f32 v155, v214, v215\n\t"
            "v_mul_f32 v144, v236, v144\n\t"
            "v_mfma_f32_16x16x32_bf16 v[160:163], v[88:91], v[192:195], v[160:163]\n\t"
            "ds_read_b128 v[212:215], v232 offset:12416\n\t"
            "v_mul_f32 v145, v236, v145\n\t"
            "v_mfma_f32_16x16x32_bf16 v[160:163], v[92:95], v[200:203], v[160:163]\n\t"
            "v_mul_f32 v146, v236, v146\n\t"
            "v_mul_f32 v147, v236, v147\n\t"
            "v_mfma_f32_16x16x32_bf16 v[228:231], v[240:243], v[180:183], 0\n\t"
            "v_cvt_pk_bf16_f32 v144, v144, v145\n\t"
            "v_mul_f32 v148, v236, v148\n\t"
            "v_mfma_f32_16x16x32_bf16 v[228:231], v[240:243], v[184:187], v[228:231]\n\t"
            "v_mul_f32 v149, v236, v149\n\t"
            "v_cvt_pk_bf16_f32 v145, v146, v147\n\t"
            "v_mfma_f32_16x16x32_bf16 v[196:199], v[24:27], v[216:219], 0\n\t"
            "v_mul_f32 v204, v236, v204\n\t"
            "v_mul_f32 v205, v236, v205\n\t"
            "v_mfma_f32_16x16x32_bf16 v[196:199], v[28:31], v[220:223], v[196:199]\n\t"
            "v_mul_f32 v206, v236, v206\n\t"
            "v_mul_f32 v207, v236, v207\n\t"
            "v_mfma_f32_16x16x32_bf16 v[196:199], v[32:35], v[224:227], v[196:199]\n\t"
            "v_rcp_f32 v229, v228\n\t"
            "v_mfma_f32_16x16x32_bf16 v[196:199], v[36:39], v[188:191], v[196:199]\n\t"
            "v_mul_f32 v168, v229, v168\n\t"
            "v_mul_f32 v169, v229, v169\n\t"
            "v_mfma_f32_16x16x32_bf16 v[196:199], v[40:43], v[192:195], v[196:199]\n\t"
            "v_mul_f32 v172, v229, v172\n\t"
            "v_mul_f32 v173, v229, v173\n\t"
            "v_mfma_f32_16x16x32_bf16 v[196:199], v[44:47], v[200:203], v[196:199]\n\t"
            "v_mul_f32 v176, v229, v176\n\t"
            "v_mul_f32 v177, v229, v177\n\t"
            "v_mfma_f32_16x16x32_bf16 v[156:159], v[0:3], v[216:219], 0\n\t"
            "v_mul_f32 v170, v229, v170\n\t"
            "v_mul_f32 v171, v229, v171\n\t"
            "v_mfma_f32_16x16x32_bf16 v[156:159], v[4:7], v[220:223], v[156:159]\n\t"
            "v_cvt_pk_bf16_f32 v168, v168, v169\n\t"
            "v_mul_f32 v174, v229, v174\n\t"
            "v_mfma_f32_16x16x32_bf16 v[156:159], v[8:11], v[224:227], v[156:159]\n\t"
            "v_mul_f32 v175, v229, v175\n\t"
            "v_cvt_pk_bf16_f32 v172, v172, v173\n\t"
            "v_mfma_f32_16x16x32_bf16 v[156:159], v[12:15], v[188:191], v[156:159]\n\t"
            "v_mul_f32 v178, v229, v178\n\t"
            "v_mul_f32 v179, v229, v179\n\t"
            "v_mfma_f32_16x16x32_bf16 v[156:159], v[16:19], v[192:195], v[156:159]\n\t"
            "v_cvt_pk_bf16_f32 v176, v176, v177\n\t"
            "v_xor_b32 v230, s73, v255\n\t"
            "v_mfma_f32_16x16x32_bf16 v[156:159], v[20:23], v[200:203], v[156:159]\n\t"
            "v_cvt_pk_bf16_f32 v169, v170, v171\n\t"
            "v_cvt_pk_bf16_f32 v173, v174, v175\n\t"
            "v_cvt_pk_bf16_f32 v177, v178, v179\n\t"
            "v_mul_f32 v150, v236, v150\n\t"
            "v_mfma_f32_16x16x16_bf16 v[180:183], v[152:153], v[154:155], 0\n\t"
            "s_mov_b64 exec, s[68:69]\n\t"
            "ds_write_b64 v230, v[168:169]\n\t"
            "ds_write_b64 v230, v[172:173] offset:128\n\t"
            "ds_write_b64 v230, v[176:177] offset:256\n\t"
            "s_mov_b64 exec, -1\n\t"
            "v_mfma_f32_16x16x32_bf16 v[164:167], v[48:51], v[216:219], 0\n\t"
            "v_mul_f32 v151, v236, v151\n\t"
            "v_cvt_pk_bf16_f32 v148, v148, v149\n\t"
            "v_mfma_f32_16x16x32_bf16 v[164:167], v[52:55], v[220:223], v[164:167]\n\t"
            "v_cvt_pk_bf16_f32 v146, v204, v205\n\t"
            "v_cvt_pk_bf16_f32 v147, v206, v207\n\t"
            "v_mfma_f32_16x16x32_bf16 v[164:167], v[56:59], v[224:227], v[164:167]\n\t"
            "ds_read_b128 v[204:207], v232 offset:12288\n\t"
            "v_cvt_pk_bf16_f32 v149, v150, v151\n\t"
            "v_mfma_f32_16x16x32_bf16 v[164:167], v[60:63], v[188:191], v[164:167]\n\t"
            "v_mul_f32 v208, v236, v208\n\t"
            "v_mul_f32 v209, v236, v209\n\t"
            "v_mfma_f32_16x16x32_bf16 v[164:167], v[64:67], v[192:195], v[164:167]\n\t"
            "v_mul_f32 v210, v236, v210\n\t"
            "v_mul_f32 v211, v236, v211\n\t"
            "v_mfma_f32_16x16x32_bf16 v[164:167], v[68:71], v[200:203], v[164:167]\n\t"
            "v_cvt_pk_bf16_f32 v150, v208, v209\n\t"
            "v_cvt_pk_bf16_f32 v151, v210, v211\n\t"
            "v_mfma_f32_16x16x32_bf16 v[228:231], v[96:99], v[216:219], 0\n\t"
            "s_waitcnt lgkmcnt(1)\n\t"
            "s_barrier\n\t"
            "s_setprio 2\n\t"
            "ds_read_b128 v[208:211], v233 offset:12288\n\t"
            "v_mul_f32 v156, v237, v156\n\t"
            "v_mfma_f32_16x16x32_bf16 v[228:231], v[100:103], v[220:223], v[228:231]\n\t"
            "v_mul_f32 v157, v237, v157\n\t"
            "v_mul_f32 v158, v237, v158\n\t"
            "v_mfma_f32_16x16x32_bf16 v[228:231], v[104:107], v[224:227], v[228:231]\n\t"
            "v_mul_f32 v159, v237, v159\n\t"
            "v_cvt_pk_bf16_f32 v156, v156, v157\n\t"
            "v_mfma_f32_16x16x32_bf16 v[228:231], v[108:111], v[188:191], v[228:231]\n\t"
            "v_cvt_pk_bf16_f32 v157, v158, v159\n\t"
            "v_mul_f32 v196, v237, v196\n\t"
            "v_mfma_f32_16x16x32_bf16 v[228:231], v[112:115], v[192:195], v[228:231]\n\t"
            "v_mul_f32 v197, v237, v197\n\t"
            "v_mul_f32 v198, v237, v198\n\t"
            "v_mfma_f32_16x16x32_bf16 v[228:231], v[116:119], v[200:203], v[228:231]\n\t"
            "v_mul_f32 v199, v237, v199\n\t"
            "v_cvt_pk_bf16_f32 v158, v196, v197\n\t"
            "v_mfma_f32_16x16x32_bf16 v[180:183], v[144:147], v[148:151], v[180:183]\n\t"
            "v_cvt_pk_bf16_f32 v159, v198, v199\n\t"
            "v_mul_f32 v164, v237, v164\n\t"
            "v_mfma_f32_16x16x32_bf16 v[196:199], v[120:123], v[216:219], 0\n\t"
            "ds_read_b128 v[216:219], v233 offset:12416\n\t"
            "v_mul_f32 v165, v237, v165\n\t"
            "v_mfma_f32_16x16x32_bf16 v[196:199], v[124:127], v[220:223], v[196:199]\n\t"
            "ds_read_b128 v[220:223], v232 offset:12544\n\t"
            "v_mul_f32 v166, v237, v166\n\t"
            "v_mfma_f32_16x16x32_bf16 v[196:199], v[128:131], v[224:227], v[196:199]\n\t"
            "ds_read_b128 v[224:227], v233 offset:12544\n\t"
            "v_mul_f32 v167, v237, v167\n\t"
            "v_mfma_f32_16x16x32_bf16 v[196:199], v[132:135], v[188:191], v[196:199]\n\t"
            "v_cvt_pk_bf16_f32 v164, v164, v165\n\t"
            "v_cvt_pk_bf16_f32 v165, v166, v167\n\t"
            "v_mfma_f32_16x16x32_bf16 v[196:199], v[136:139], v[192:195], v[196:199]\n\t"
            "v_mul_f32 v160, v237, v160\n\t"
            "v_mul_f32 v161, v237, v161\n\t"
            "v_mfma_f32_16x16x32_bf16 v[196:199], v[140:143], v[200:203], v[196:199]\n\t"
            "s_waitcnt lgkmcnt(4)\n\t"
            "v_mul_f32 v162, v237, v162\n\t"
            "v_mul_f32 v163, v237, v163\n\t"
            "v_mfma_f32_16x16x32_bf16 v[176:179], v[48:51], v[204:207], 0\n\t"
            "s_waitcnt lgkmcnt(3)\n\t"
            "v_cvt_pk_bf16_f32 v160, v160, v161\n\t"
            "v_cvt_pk_bf16_f32 v161, v162, v163\n\t"
            "v_mfma_f32_16x16x32_bf16 v[176:179], v[52:55], v[208:211], v[176:179]\n\t"
            "v_mul_f32 v228, v237, v228\n\t"
            "v_mul_f32 v229, v237, v229\n\t"
            "v_mfma_f32_16x16x32_bf16 v[176:179], v[56:59], v[212:215], v[176:179]\n\t"
            "s_waitcnt lgkmcnt(2)\n\t"
            "v_mul_f32 v196, v237, v196\n\t"
            "v_mul_f32 v197, v237, v197\n\t"
            "v_mfma_f32_16x16x32_bf16 v[176:179], v[60:63], v[216:219], v[176:179]\n\t"
            "s_waitcnt lgkmcnt(1)\n\t"
            "v_mul_f32 v198, v237, v198\n\t"
            "v_mul_f32 v199, v237, v199\n\t"
            "v_mfma_f32_16x16x32_bf16 v[176:179], v[64:67], v[220:223], v[176:179]\n\t"
            "s_waitcnt lgkmcnt(0)\n\t"
            "v_cvt_pk_bf16_f32 v166, v196, v197\n\t"
            "v_cvt_pk_bf16_f32 v167, v198, v199\n\t"
            "v_mfma_f32_16x16x32_bf16 v[176:179], v[68:71], v[224:227], v[176:179]\n\t"
            "v_mul_f32 v230, v237, v230\n\t"
            "v_mul_f32 v231, v237, v231\n\t"
            "v_cvt_pk_bf16_f32 v162, v228, v229\n\t"
            "v_cvt_pk_bf16_f32 v163, v230, v231\n\t"
            "v_mfma_f32_16x16x16_bf16 v[188:191], v[152:153], v[166:167], 0\n\t"
            "v_exp_f32 v180, v180\n\t"
            "v_exp_f32 v181, v181\n\t"
            "v_mfma_f32_16x16x16_bf16 v[192:195], v[164:165], v[166:167], 0\n\t"
            "v_exp_f32 v182, v182\n\t"
            "v_exp_f32 v183, v183\n\t"
            "v_mfma_f32_16x16x16_bf16 v[184:187], v[164:165], v[154:155], 0\n\t"
            "v_mul_f32 v176, v238, v176\n\t"
            "v_mul_f32 v177, v238, v177\n\t"
            "v_mul_f32 v178, v238, v178\n\t"
            "v_mfma_f32_16x16x32_bf16 v[168:171], v[0:3], v[204:207], 0\n\t"
            "s_bfe_u32 s49, s70, 0x30001\n\t"
            "v_mul_f32 v179, v238, v179\n\t"
            "v_cvt_pk_bf16_f32 v176, v176, v177\n\t"
            "v_mfma_f32_16x16x32_bf16 v[168:171], v[4:7], v[208:211], v[168:171]\n\t"
            "s_lshl_b32 s49, s49, 4\n\t"
            "v_cvt_pk_bf16_f32 v180, v180, v181\n\t"
            "v_cvt_pk_bf16_f32 v177, v178, v179\n\t"
            "v_mfma_f32_16x16x32_bf16 v[168:171], v[8:11], v[212:215], v[168:171]\n\t"
            "s_or_b32 s73, s77, s49\n\t"
            "v_cvt_pk_bf16_f32 v181, v182, v183\n\t"
            "v_mfma_f32_16x16x32_bf16 v[168:171], v[12:15], v[216:219], v[168:171]\n\t"
            "v_mfma_f32_16x16x32_bf16 v[168:171], v[16:19], v[220:223], v[168:171]\n\t"
            "v_mfma_f32_16x16x32_bf16 v[168:171], v[20:23], v[224:227], v[168:171]\n\t"
            "v_mfma_f32_16x16x32_bf16 v[188:191], v[144:147], v[160:163], v[188:191]\n\t"
            "v_mfma_f32_16x16x32_bf16 v[200:203], v[120:123], v[204:207], 0\n\t"
            "v_mfma_f32_16x16x32_bf16 v[200:203], v[124:127], v[208:211], v[200:203]\n\t"
            "v_mfma_f32_16x16x32_bf16 v[200:203], v[128:131], v[212:215], v[200:203]\n\t"
            "v_mfma_f32_16x16x32_bf16 v[200:203], v[132:135], v[216:219], v[200:203]\n\t"
            "v_mfma_f32_16x16x32_bf16 v[200:203], v[136:139], v[220:223], v[200:203]\n\t"
            "s_add_u32 s70, s70, 1\n\t"
            "s_nop 0\n\t"
            "v_mul_f32 v168, v238, v168\n\t"
            "v_mfma_f32_16x16x32_bf16 v[200:203], v[140:143], v[224:227], v[200:203]\n\t"
            "s_add_u32 s76, s76, 800\n\t"
            "v_exp_f32 v188, v188\n\t"
            "v_mfma_f32_16x16x32_bf16 v[192:195], v[156:159], v[160:163], v[192:195]\n\t"
            "s_setprio 0\n\t"
            "v_exp_f32 v189, v189\n\t"
            "v_mfma_f32_16x16x32_bf16 v[172:175], v[72:75], v[204:207], 0\n\t"
            "s_add_u32 s77, s77, 384\n\t"
            "v_exp_f32 v190, v190\n\t"
            "v_mfma_f32_16x16x32_bf16 v[172:175], v[76:79], v[208:211], v[172:175]\n\t"
            "s_xor_b32 s48, s48, s47\n\t"
            "v_exp_f32 v191, v191\n\t"
            "v_mfma_f32_16x16x32_bf16 v[172:175], v[80:83], v[212:215], v[172:175]\n\t"
            "s_sub_u32 s44, s44, 1\n\t"
            "v_exp_f32 v192, v192\n\t"
            "v_mfma_f32_16x16x32_bf16 v[172:175], v[84:87], v[216:219], v[172:175]\n\t"
            "v_exp_f32 v193, v193\n\t"
            "v_mfma_f32_16x16x32_bf16 v[172:175], v[88:91], v[220:223], v[172:175]\n\t"
            "v_exp_f32 v194, v194\n\t"
            "v_mfma_f32_16x16x32_bf16 v[172:175], v[92:95], v[224:227], v[172:175]\n\t"
            "v_exp_f32 v195, v195\n\t"
            "v_mfma_f32_16x16x32_bf16 v[184:187], v[156:159], v[148:151], v[184:187]\n\t"
            "v_cvt_pk_bf16_f32 v188, v188, v189\n\t"
            "v_cvt_pk_bf16_f32 v189, v190, v191\n\t"
            "v_mfma_f32_16x16x32_bf16 v[196:199], v[24:27], v[204:207], 0\n\t"
            "v_cvt_pk_bf16_f32 v190, v192, v193\n\t"
            "v_cvt_pk_bf16_f32 v191, v194, v195\n\t"
            "v_mfma_f32_16x16x32_bf16 v[196:199], v[28:31], v[208:211], v[196:199]\n\t"
            "v_mul_f32 v169, v238, v169\n\t"
            "v_mul_f32 v170, v238, v170\n\t"
            "v_mfma_f32_16x16x32_bf16 v[196:199], v[32:35], v[212:215], v[196:199]\n\t"
            "v_exp_f32 v184, v184\n\t"
            "v_mfma_f32_16x16x32_bf16 v[196:199], v[36:39], v[216:219], v[196:199]\n\t"
            "v_exp_f32 v185, v185\n\t"
            "v_mfma_f32_16x16x32_bf16 v[196:199], v[40:43], v[220:223], v[196:199]\n\t"
            "v_exp_f32 v186, v186\n\t"
            "v_mfma_f32_16x16x32_bf16 v[196:199], v[44:47], v[224:227], v[196:199]\n\t"
            "v_exp_f32 v187, v187\n\t"
            "v_cvt_pk_bf16_f32 v182, v184, v185\n\t"
            "v_cvt_pk_bf16_f32 v183, v186, v187\n\t"
            "v_mfma_f32_16x16x16_bf16 v[184:187], v[176:177], v[154:155], 0\n\t"
            "v_mul_f32 v171, v238, v171\n\t"
            "v_cvt_pk_bf16_f32 v168, v168, v169\n\t"
            "v_cvt_pk_bf16_f32 v169, v170, v171\n\t"
            "v_mfma_f32_16x16x16_bf16 v[192:195], v[176:177], v[166:167], 0\n\t"
            "v_mul_f32 v196, v238, v196\n\t"
            "v_mul_f32 v197, v238, v197\n\t"
            "v_mul_f32 v198, v238, v198\n\t"
            "v_mfma_f32_16x16x32_bf16 v[228:231], v[96:99], v[204:207], 0\n\t"
            "ds_read_b64 v[204:205], v235 offset:136\n\t"
            "v_mul_f32 v199, v238, v199\n\t"
            "v_mfma_f32_16x16x32_bf16 v[228:231], v[100:103], v[208:211], v[228:231]\n\t"
            "ds_read_b64 v[206:207], v234 offset:128\n\t"
            "v_cvt_pk_bf16_f32 v170, v196, v197\n\t"
            "v_mfma_f32_16x16x32_bf16 v[228:231], v[104:107], v[212:215], v[228:231]\n\t"
            "ds_read_b128 v[208:211], v234\n\t"
            "v_cvt_pk_bf16_f32 v171, v198, v199\n\t"
            "v_mfma_f32_16x16x32_bf16 v[228:231], v[108:111], v[216:219], v[228:231]\n\t"
            "ds_read_b128 v[212:215], v235 offset:64\n\t"
            "v_mul_f32 v172, v238, v172\n\t"
            "v_mfma_f32_16x16x32_bf16 v[228:231], v[112:115], v[220:223], v[228:231]\n\t"
            "v_mul_f32 v173, v238, v173\n\t"
            "v_mul_f32 v200, v238, v200\n\t"
            "v_mfma_f32_16x16x32_bf16 v[228:231], v[116:119], v[224:227], v[228:231]\n\t"
            "v_mul_f32 v201, v238, v201\n\t"
            "v_mul_f32 v202, v238, v202\n\t"
            "v_mfma_f32_16x16x32_bf16 v[184:187], v[168:171], v[148:151], v[184:187]\n\t"
            "v_mul_f32 v203, v238, v203\n\t"
            "v_mul_f32 v174, v238, v174\n\t"
            "v_mfma_f32_16x16x32_bf16 v[192:195], v[168:171], v[160:163], v[192:195]\n\t"
            "v_mul_f32 v175, v238, v175\n\t"
            "v_cvt_pk_bf16_f32 v172, v172, v173\n\t"
            "v_cvt_pk_bf16_f32 v178, v200, v201\n\t"
            "v_cvt_pk_bf16_f32 v179, v202, v203\n\t"
            "v_cvt_pk_bf16_f32 v173, v174, v175\n\t"
            "v_mul_f32 v228, v238, v228\n\t"
            "v_mul_f32 v229, v238, v229\n\t"
            "s_waitcnt lgkmcnt(2)\n\t"
            "v_mfma_f32_16x16x32_bf16 v[220:223], v[204:207], v[164:167], 0\n\t"
            "s_waitcnt lgkmcnt(1)\n\t"
            "v_mul_f32 v230, v238, v230\n\t"
            "v_mul_f32 v231, v238, v231\n\t"
            "v_mfma_f32_16x16x32_bf16 v[220:223], v[208:211], v[160:163], v[220:223]\n\t"
            "s_waitcnt lgkmcnt(0)\n\t"
            "v_cvt_pk_bf16_f32 v174, v228, v229\n\t"
            "v_cvt_pk_bf16_f32 v175, v230, v231\n\t"
            "v_mfma_f32_16x16x32_bf16 v[220:223], v[212:215], v[156:159], v[220:223]\n\t"
            "v_exp_f32 v184, v184\n\t"
            "v_mfma_f32_16x16x32_bf16 v[216:219], v[204:207], v[152:155], 0\n\t"
            "v_exp_f32 v185, v185\n\t"
            "v_mfma_f32_16x16x32_bf16 v[216:219], v[208:211], v[148:151], v[216:219]\n\t"
            "v_exp_f32 v186, v186\n\t"
            "v_mfma_f32_16x16x32_bf16 v[216:219], v[212:215], v[144:147], v[216:219]\n\t"
            "v_exp_f32 v187, v187\n\t"
            "v_cvt_pk_bf16_f32 v184, v184, v185\n\t"
            "v_cvt_pk_bf16_f32 v185, v186, v187\n\t"
            "v_mfma_f32_16x16x16_bf16 v[228:231], v[164:165], v[178:179], 0\n\t"
            "v_mov_b32 v187, 0\n\t"
            "v_exp_f32 v192, v192\n\t"
            "v_mfma_f32_16x16x16_bf16 v[200:203], v[176:177], v[178:179], 0\n\t"
            "v_exp_f32 v193, v193\n\t"
            "v_exp_f32 v216, v216\n\t"
            "v_mfma_f32_16x16x16_bf16 v[196:199], v[152:153], v[178:179], 0\n\t"
            "v_cvt_pk_bf16_f32 v216, v216, 0\n\t"
            "v_cndmask_b32_e64 v186, 0, v216, s[58:59]\n\t"
            "v_cndmask_b32_e64 v235, v217, v221, s[60:61]\n\t"
            "v_mfma_f32_16x16x32_bf16 v[224:227], v[204:207], v[176:179], 0\n\t"
            "v_exp_f32 v194, v194\n\t"
            "v_mfma_f32_16x16x32_bf16 v[224:227], v[208:211], v[172:175], v[224:227]\n\t"
            "v_exp_f32 v195, v195\n\t"
            "v_mfma_f32_16x16x32_bf16 v[224:227], v[212:215], v[168:171], v[224:227]\n\t"
            "v_exp_f32 v220, v220\n\t"
            "v_mfma_f32_16x16x32_bf16 v[204:207], v[240:243], v[180:183], 0\n\t"
            "v_cvt_pk_bf16_f32 v192, v192, v193\n\t"
            "v_cvt_pk_bf16_f32 v193, v194, v195\n\t"
            "v_mfma_f32_16x16x32_bf16 v[204:207], v[240:243], v[184:187], v[204:207]\n\t"
            "v_cvt_pk_bf16_f32 v220, v220, 0\n\t"
            "v_cndmask_b32_e64 v194, 0, v220, s[58:59]\n\t"
            "v_mfma_f32_16x16x32_bf16 v[200:203], v[168:171], v[172:175], v[200:203]\n\t"
            "v_exp_f32 v224, v224\n\t"
            "v_mfma_f32_16x16x32_bf16 v[196:199], v[144:147], v[172:175], v[196:199]\n\t"
            "v_cvt_pk_bf16_f32 v224, v224, 0\n\t"
            "v_cndmask_b32_e64 v235, v235, v225, s[62:63]\n\t"
            "v_mfma_f32_16x16x32_bf16 v[228:231], v[156:159], v[172:175], v[228:231]\n\t"
            "v_cndmask_b32_e64 v235, v235, v239, s[64:65]\n\t"
            "v_cmp_nlt_f32_e64 s[24:25], |v235|, s23\n\t"
            "v_exp_f32 v235, v235\n\t"
            "v_exp_f32 v200, v200\n\t"
            "v_exp_f32 v201, v201\n\t"
            "v_exp_f32 v196, v196\n\t"
            "v_exp_f32 v197, v197\n\t"
            "v_exp_f32 v198, v198\n\t"
            "v_exp_f32 v199, v199\n\t"
            "v_exp_f32 v228, v228\n\t"
            "v_exp_f32 v229, v229\n\t"
            "v_exp_f32 v230, v230\n\t"
            "v_exp_f32 v231, v231\n\t"
            "v_cvt_pk_bf16_f32 v196, v196, v197\n\t"
            "v_cvt_pk_bf16_f32 v197, v198, v199\n\t"
            "v_cvt_pk_bf16_f32 v198, v228, v229\n\t"
            "v_cvt_pk_bf16_f32 v199, v230, v231\n\t"
            "v_exp_f32 v202, v202\n\t"
            "v_exp_f32 v203, v203\n\t"
            "v_mfma_f32_16x16x32_bf16 v[212:215], v[240:243], v[196:199], 0\n\t"
            "v_cvt_pk_bf16_f32 v200, v200, v201\n\t"
            "v_cvt_pk_bf16_f32 v201, v202, v203\n\t"
            "v_cndmask_b32_e64 v202, 0, v224, s[58:59]\n\t"
            "v_mov_b32 v203, 0\n\t"
            "v_cvt_pk_bf16_f32 v235, v235, 0\n\t"
            "v_cndmask_b32_e64 v235, 0, v235, s[66:67]\n\t"
            "v_mfma_f32_16x16x32_bf16 v[212:215], v[240:243], v[200:203], v[212:215]\n\t"
            "ds_write_b16 v250, v235 offset:256\n\t"
            "v_mfma_f32_16x16x32_bf16 v[208:211], v[240:243], v[188:191], 0\n\t"
            "v_mov_b32 v195, 0\n\t"
            "v_add_u32 v205, 0xe0800000, v204\n\t"
            "v_rcp_f32 v236, v204\n\t"
            "v_mfma_f32_16x16x32_bf16 v[208:211], v[240:243], v[192:195], v[208:211]\n\t"
            "s_nop 1\n\t"
            "v_add_u32 v213, 0xe0800000, v212\n\t"
            "v_rcp_f32 v238, v212\n\t"
            "s_nop 3\n\t"
            "v_add_u32 v209, 0xe0800000, v208\n\t"
            "v_rcp_f32 v237, v208\n\t"
            "v_or3_b32 v205, v205, v209, v213\n\t"
            "v_cmp_le_u32 vcc, 2.0, v205\n\t"
            ".Lwa_in%=:\n\t"
            "s_or_b64 vcc, vcc, s[24:25]\n\t"
            "s_cbranch_scc1 .Lwa_slow%=\n\t"
            ".Lwa_ok%=:\n\t"
            "s_add_u32 s72, s72, 800\n\t"
            "s_cmp_gt_u32 s44, 2\n\t"
            "s_barrier\n\t"
            "s_setprio 0\n\t"
            "s_cbranch_scc1 .Lwa_loop%=\n\t"
            "s_cmp_eq_u32 s44, 0\n\t"
            "s_cbranch_scc1 .Lwa_done%=\n\t"
            "s_cmp_lg_u32 s44, 2\n\t"
            "s_cbranch_scc1 .Lwa_loop%=\n\t"
            "s_mov_b32 m0, s40\n\t"
            "v_lshlrev_b32 v234, 3, v248\n\t"
            "buffer_load_dword v234, s[36:39], s41 offen sc1 lds\n\t"
            "s_branch .Lwa_loop%=\n\t"
            ".Lwa_slow%=:\n\t"
            "s_waitcnt lgkmcnt(0)\n\t"
            "s_nop 7\n\t"
            "v_mfma_f32_16x16x16_bf16 v[180:183], v[152:153], v[154:155], 0\n\t"
            "v_add_u32 v234, s72, v252\n\t"
            "v_cndmask_b32_e64 v235, v246, v234, s[50:51]\n\t"
            "v_cndmask_b32_e64 v234, v246, v234, s[26:27]\n\t"
            "v_mfma_f32_16x16x16_bf16 v[228:231], v[164:165], v[154:155], 0\n\t"
            "ds_read_b64 v[216:217], v235 offset:136\n\t"
            "ds_read_b64 v[218:219], v234 offset:128\n\t"
            "v_mfma_f32_16x16x16_bf16 v[184:187], v[176:177], v[154:155], 0\n\t"
            "ds_read_b128 v[220:223], v234\n\t"
            "ds_read_b128 v[224:227], v235 offset:64\n\t"
            "v_mfma_f32_16x16x32_bf16 v[180:183], v[144:147], v[148:151], v[180:183]\n\t"
            "v_mfma_f32_16x16x32_bf16 v[228:231], v[156:159], v[148:151], v[228:231]\n\t"
            "s_nop 1\n\t"
            "v_mfma_f32_16x16x32_bf16 v[184:187], v[168:171], v[148:151], v[184:187]\n\t"
            "v_mfma_f32_16x16x16_bf16 v[188:191], v[152:153], v[166:167], 0\n\t"
            "v_mfma_f32_16x16x16_bf16 v[192:195], v[176:177], v[166:167], 0\n\t"
            "v_mfma_f32_16x16x16_bf16 v[196:199], v[152:153], v[178:179], 0\n\t"
            "s_waitcnt lgkmcnt(2)\n\t"
            "v_mfma_f32_16x16x32_bf16 v[204:207], v[216:219], v[152:155], 0\n\t"
            "s_waitcnt lgkmcnt(1)\n\t"
            "v_mfma_f32_16x16x32_bf16 v[204:207], v[220:223], v[148:151], v[204:207]\n\t"
            "s_waitcnt lgkmcnt(0)\n\t"
            "v_mfma_f32_16x16x32_bf16 v[204:207], v[224:227], v[144:147], v[204:207]\n\t"
            "v_mfma_f32_16x16x32_bf16 v[208:211], v[216:219], v[164:167], 0\n\t"
            "v_mfma_f32_16x16x32_bf16 v[208:211], v[220:223], v[160:163], v[208:211]\n\t"
            "v_mfma_f32_16x16x32_bf16 v[208:211], v[224:227], v[156:159], v[208:211]\n\t"
            "v_mfma_f32_16x16x32_bf16 v[188:191], v[144:147], v[160:163], v[188:191]\n\t"
            "v_mfma_f32_16x16x32_bf16 v[192:195], v[168:171], v[160:163], v[192:195]\n\t"
            "v_mfma_f32_16x16x32_bf16 v[212:215], v[216:219], v[176:179], 0\n\t"
            "s_nop 1\n\t"
            "v_max3_f32 v232, v204, v180, v181\n\t"
            "v_mfma_f32_16x16x32_bf16 v[212:215], v[220:223], v[172:175], v[212:215]\n\t"
            "v_max3_f32 v232, v232, v182, v183\n\t"
            "v_max3_f32 v232, v232, v228, v229\n\t"
            "v_mfma_f32_16x16x32_bf16 v[212:215], v[224:227], v[168:171], v[212:215]\n\t"
            "v_max3_f32 v232, v232, v230, v231\n\t"
            "v_max3_f32 v232, v232, v184, v185\n\t"
            "v_mfma_f32_16x16x32_bf16 v[196:199], v[144:147], v[172:175], v[196:199]\n\t"
            "v_max3_f32 v232, v232, v186, v187\n\t"
            "v_mov_b32 v233, v232\n\t"
            "v_cndmask_b32_e64 v235, v205, v209, s[60:61]\n\t"
            "v_mfma_f32_16x16x16_bf16 v[200:203], v[176:177], v[178:179], 0\n\t"
            "v_permlane32_swap_b32 v232, v233\n\t"
            "v_max_f32 v232, v232, v233\n\t"
            "v_mov_b32 v233, v232\n\t"
            "v_max3_f32 v234, v205, v209, v213\n\t"
            "v_max_f32 v234, v234, v239\n\t"
            "v_permlane16_swap_b32 v232, v233\n\t"
            "v_mfma_f32_16x16x32_bf16 v[200:203], v[168:171], v[172:175], v[200:203]\n\t"
            "v_max_f32 v232, v232, v233\n\t"
            "v_sub_f32 v180, v180, v232\n\t"
            "v_sub_f32 v181, v181, v232\n\t"
            "v_exp_f32 v180, v180\n\t"
            "v_exp_f32 v181, v181\n\t"
            "v_sub_f32 v182, v182, v232\n\t"
            "v_sub_f32 v183, v183, v232\n\t"
            "v_exp_f32 v182, v182\n\t"
            "v_exp_f32 v183, v183\n\t"
            "v_sub_f32 v228, v228, v232\n\t"
            "v_sub_f32 v229, v229, v232\n\t"
            "v_sub_f32 v230, v230, v232\n\t"
            "v_sub_f32 v231, v231, v232\n\t"
            "v_exp_f32 v228, v228\n\t"
            "v_exp_f32 v229, v229\n\t"
            "v_exp_f32 v230, v230\n\t"
            "v_exp_f32 v231, v231\n\t"
            "v_cvt_pk_bf16_f32 v180, v180, v181\n\t"
            "v_cvt_pk_bf16_f32 v181, v182, v183\n\t"
            "v_cvt_pk_bf16_f32 v182, v228, v229\n\t"
            "v_cvt_pk_bf16_f32 v183, v230, v231\n\t"
            "v_mfma_f32_16x16x16_bf16 v[228:231], v[164:165], v[166:167], 0\n\t"
            "v_sub_f32 v184, v184, v232\n\t"
            "v_sub_f32 v185, v185, v232\n\t"
            "v_sub_f32 v186, v186, v232\n\t"
            "v_sub_f32 v187, v187, v232\n\t"
            "v_sub_f32 v204, v204, v232\n\t"
            "v_max3_f32 v232, v208, v188, v189\n\t"
            "v_max3_f32 v232, v232, v190, v191\n\t"
            "v_mfma_f32_16x16x32_bf16 v[228:231], v[156:159], v[160:163], v[228:231]\n\t"
            "v_max_f32_dpp v234, v234, v234 row_ror:8 row_mask:0xf bank_mask:0xf\n\t"
            "v_cndmask_b32_e64 v235, v235, v213, s[62:63]\n\t"
            "v_cndmask_b32_e64 v235, v235, v239, s[64:65]\n\t"
            "v_max_f32_dpp v234, v234, v234 row_ror:4 row_mask:0xf bank_mask:0xf\n\t"
            "v_exp_f32 v184, v184\n\t"
            "v_exp_f32 v185, v185\n\t"
            "v_max_f32_dpp v234, v234, v234 row_ror:2 row_mask:0xf bank_mask:0xf\n\t"
            "v_exp_f32 v186, v186\n\t"
            "v_max3_f32 v232, v232, v228, v229\n\t"
            "v_max3_f32 v232, v232, v230, v231\n\t"
            "v_max3_f32 v232, v232, v192, v193\n\t"
            "v_max3_f32 v232, v232, v194, v195\n\t"
            "v_mov_b32 v233, v232\n\t"
            "v_max_f32_dpp v234, v234, v234 row_ror:1 row_mask:0xf bank_mask:0xf\n\t"
            "v_sub_f32 v235, v235, v234\n\t"
            "v_permlane32_swap_b32 v232, v233\n\t"
            "v_max_f32 v232, v232, v233\n\t"
            "v_mov_b32 v233, v232\n\t"
            "v_exp_f32 v235, v235\n\t"
            "v_exp_f32 v187, v187\n\t"
            "v_permlane16_swap_b32 v232, v233\n\t"
            "v_max_f32 v232, v232, v233\n\t"
            "v_sub_f32 v188, v188, v232\n\t"
            "v_sub_f32 v189, v189, v232\n\t"
            "v_exp_f32 v188, v188\n\t"
            "v_exp_f32 v189, v189\n\t"
            "v_sub_f32 v190, v190, v232\n\t"
            "v_sub_f32 v191, v191, v232\n\t"
            "v_exp_f32 v190, v190\n\t"
            "v_exp_f32 v191, v191\n\t"
            "v_sub_f32 v228, v228, v232\n\t"
            "v_sub_f32 v229, v229, v232\n\t"
            "v_sub_f32 v230, v230, v232\n\t"
            "v_sub_f32 v231, v231, v232\n\t"
            "v_exp_f32 v228, v228\n\t"
            "v_exp_f32 v229, v229\n\t"
            "v_exp_f32 v230, v230\n\t"
            "v_exp_f32 v231, v231\n\t"
            "v_cvt_pk_bf16_f32 v188, v188, v189\n\t"
            "v_cvt_pk_bf16_f32 v189, v190, v191\n\t"
            "v_cvt_pk_bf16_f32 v190, v228, v229\n\t"
            "v_cvt_pk_bf16_f32 v191, v230, v231\n\t"
            "v_mfma_f32_16x16x16_bf16 v[228:231], v[164:165], v[178:179], 0\n\t"
            "v_sub_f32 v192, v192, v232\n\t"
            "v_sub_f32 v193, v193, v232\n\t"
            "v_sub_f32 v194, v194, v232\n\t"
            "v_sub_f32 v195, v195, v232\n\t"
            "v_sub_f32 v208, v208, v232\n\t"
            "v_max3_f32 v232, v212, v196, v197\n\t"
            "v_max3_f32 v232, v232, v198, v199\n\t"
            "v_mfma_f32_16x16x32_bf16 v[228:231], v[156:159], v[172:175], v[228:231]\n\t"
            "v_exp_f32 v192, v192\n\t"
            "v_exp_f32 v193, v193\n\t"
            "v_exp_f32 v204, v204\n\t"
            "v_exp_f32 v194, v194\n\t"
            "v_exp_f32 v195, v195\n\t"
            "v_exp_f32 v208, v208\n\t"
            "v_cvt_pk_bf16_f32 v184, v184, v185\n\t"
            "v_cvt_pk_bf16_f32 v192, v192, v193\n\t"
            "v_max3_f32 v232, v232, v228, v229\n\t"
            "v_max3_f32 v232, v232, v230, v231\n\t"
            "v_max3_f32 v232, v232, v200, v201\n\t"
            "v_max3_f32 v232, v232, v202, v203\n\t"
            "v_mov_b32 v233, v232\n\t"
            "v_cvt_pk_bf16_f32 v235, v235, 0\n\t"
            "v_cvt_pk_bf16_f32 v185, v186, v187\n\t"
            "v_permlane32_swap_b32 v232, v233\n\t"
            "v_max_f32 v232, v232, v233\n\t"
            "v_mov_b32 v233, v232\n\t"
            "v_cvt_pk_bf16_f32 v204, v204, 0\n\t"
            "v_cvt_pk_bf16_f32 v193, v194, v195\n\t"
            "v_permlane16_swap_b32 v232, v233\n\t"
            "v_max_f32 v232, v232, v233\n\t"
            "v_sub_f32 v196, v196, v232\n\t"
            "v_sub_f32 v197, v197, v232\n\t"
            "v_exp_f32 v196, v196\n\t"
            "v_exp_f32 v197, v197\n\t"
            "v_sub_f32 v198, v198, v232\n\t"
            "v_sub_f32 v199, v199, v232\n\t"
            "v_exp_f32 v198, v198\n\t"
            "v_exp_f32 v199, v199\n\t"
            "v_sub_f32 v228, v228, v232\n\t"
            "v_sub_f32 v229, v229, v232\n\t"
            "v_sub_f32 v230, v230, v232\n\t"
            "v_sub_f32 v231, v231, v232\n\t"
            "v_sub_f32 v200, v200, v232\n\t"
            "v_sub_f32 v201, v201, v232\n\t"
            "v_exp_f32 v228, v228\n\t"
            "v_exp_f32 v229, v229\n\t"
            "v_exp_f32 v230, v230\n\t"
            "v_exp_f32 v231, v231\n\t"
            "v_exp_f32 v200, v200\n\t"
            "v_exp_f32 v201, v201\n\t"
            "v_sub_f32 v202, v202, v232\n\t"
            "v_sub_f32 v203, v203, v232\n\t"
            "v_sub_f32 v212, v212, v232\n\t"
            "v_cvt_pk_bf16_f32 v196, v196, v197\n\t"
            "v_exp_f32 v202, v202\n\t"
            "v_exp_f32 v203, v203\n\t"
            "v_exp_f32 v212, v212\n\t"
            "v_cvt_pk_bf16_f32 v197, v198, v199\n\t"
            "v_cvt_pk_bf16_f32 v198, v228, v229\n\t"
            "v_cvt_pk_bf16_f32 v199, v230, v231\n\t"
            "v_cvt_pk_bf16_f32 v200, v200, v201\n\t"
            "v_cvt_pk_bf16_f32 v208, v208, 0\n\t"
            "v_cvt_pk_bf16_f32 v201, v202, v203\n\t"
            "v_cvt_pk_bf16_f32 v212, v212, 0\n\t"
            "v_cndmask_b32_e64 v235, 0, v235, s[66:67]\n\t"
            "v_cndmask_b32_e64 v186, 0, v204, s[58:59]\n\t"
            "v_mfma_f32_16x16x32_bf16 v[204:207], v[240:243], v[180:183], 0\n\t"
            "v_cndmask_b32_e64 v194, 0, v208, s[58:59]\n\t"
            "v_cndmask_b32_e64 v202, 0, v212, s[58:59]\n\t"
            "v_mfma_f32_16x16x32_bf16 v[208:211], v[240:243], v[188:191], 0\n\t"
            "ds_write_b16 v250, v235 offset:256\n\t"
            "v_mfma_f32_16x16x32_bf16 v[212:215], v[240:243], v[196:199], 0\n\t"
            "v_mov_b32 v187, 0\n\t"
            "v_mov_b32 v195, 0\n\t"
            "v_mov_b32 v203, 0\n\t"
            "v_mfma_f32_16x16x32_bf16 v[204:207], v[240:243], v[184:187], v[204:207]\n\t"
            "v_mfma_f32_16x16x32_bf16 v[208:211], v[240:243], v[192:195], v[208:211]\n\t"
            "v_mfma_f32_16x16x32_bf16 v[212:215], v[240:243], v[200:203], v[212:215]\n\t"
            "s_nop 5\n\t"
            "v_rcp_f32 v236, v204\n\t"
            "v_rcp_f32 v237, v208\n\t"
            "v_rcp_f32 v238, v212\n\t"
            "s_waitcnt lgkmcnt(0)\n\t"
            "s_nop 7\n\t"
            "s_branch .Lwa_ok%=\n\t"
            ".Lwa_done%=:\n\t"
            "s_setprio 2\n\t"
            "s_waitcnt lgkmcnt(0)\n\t"
            "s_nop 7\n\t"
            "ds_read_b64 v[156:157], v247\n\t"
            "v_add_u32 v235, s45, v248\n\t"
            "v_xor_b32 v234, s73, v254\n\t"
            "v_mov_b32 v159, 0\n\t"
            "v_mov_b32 v163, 0\n\t"
            "v_mov_b32 v167, 0\n\t"
            "ds_read_b128 v[168:171], v235\n\t"
            "ds_read_u16 v158, v234\n\t"
            "ds_read_b128 v[172:175], v235 offset:1536\n\t"
            "ds_read_b128 v[176:179], v235 offset:3072\n\t"
            "ds_read_b64 v[160:161], v247 offset:3072\n\t"
            "ds_read_u16 v162, v234 offset:256\n\t"
            "ds_read_b64 v[164:165], v247 offset:1536\n\t"
            "ds_read_u16 v166, v234 offset:128\n\t"
            "s_waitcnt lgkmcnt(7)\n\t"
            "v_mfma_f32_16x16x32_bf16 v[228:231], v[168:171], v[180:183], 0\n\t"
            "s_waitcnt lgkmcnt(6)\n\t"
            "v_mfma_f32_16x16x32_bf16 v[228:231], v[156:159], v[184:187], v[228:231]\n\t"
            "s_nop 7\n\t"
            "v_mul_f32 v228, v236, v228\n\t"
            "v_mul_f32 v229, v236, v229\n\t"
            "v_mul_f32 v230, v236, v230\n\t"
            "v_mul_f32 v231, v236, v231\n\t"
            "v_cvt_pk_bf16_f32 v228, v228, v229\n\t"
            "v_cvt_pk_bf16_f32 v229, v230, v231\n\t"
            "ds_write_b64 v245, v[228:229]\n\t"
            "s_waitcnt lgkmcnt(6)\n\t"
            "v_mfma_f32_16x16x32_bf16 v[228:231], v[172:175], v[180:183], 0\n\t"
            "s_waitcnt lgkmcnt(5)\n\t"
            "v_mfma_f32_16x16x32_bf16 v[180:183], v[176:179], v[180:183], 0\n\t"
            "s_waitcnt lgkmcnt(3)\n\t"
            "v_mfma_f32_16x16x32_bf16 v[180:183], v[160:163], v[184:187], v[180:183]\n\t"
            "s_waitcnt lgkmcnt(1)\n\t"
            "v_mfma_f32_16x16x32_bf16 v[228:231], v[164:167], v[184:187], v[228:231]\n\t"
            "v_mfma_f32_16x16x32_bf16 v[184:187], v[168:171], v[188:191], 0\n\t"
            "v_mfma_f32_16x16x32_bf16 v[184:187], v[156:159], v[192:195], v[184:187]\n\t"
            "s_nop 3\n\t"
            "v_mul_f32 v180, v236, v180\n\t"
            "v_mul_f32 v181, v236, v181\n\t"
            "v_mul_f32 v182, v236, v182\n\t"
            "v_mul_f32 v183, v236, v183\n\t"
            "v_cvt_pk_bf16_f32 v180, v180, v181\n\t"
            "v_cvt_pk_bf16_f32 v181, v182, v183\n\t"
            "v_mul_f32 v228, v236, v228\n\t"
            "v_mul_f32 v229, v236, v229\n\t"
            "v_mul_f32 v230, v236, v230\n\t"
            "v_mul_f32 v231, v236, v231\n\t"
            "v_cvt_pk_bf16_f32 v228, v228, v229\n\t"
            "v_cvt_pk_bf16_f32 v229, v230, v231\n\t"
            "v_mul_f32 v184, v237, v184\n\t"
            "v_mul_f32 v185, v237, v185\n\t"
            "v_mul_f32 v186, v237, v186\n\t"
            "v_mul_f32 v187, v237, v187\n\t"
            "v_cvt_pk_bf16_f32 v184, v184, v185\n\t"
            "v_cvt_pk_bf16_f32 v185, v186, v187\n\t"
            "ds_read_b64 v[186:187], v253 offset:352\n\t"
            "ds_write_b64 v245, v[180:181] offset:256\n\t"
            "v_mfma_f32_16x16x32_bf16 v[180:183], v[168:171], v[196:199], 0\n\t"
            "ds_write_b64 v245, v[228:229] offset:128\n\t"
            "v_mfma_f32_16x16x32_bf16 v[180:183], v[156:159], v[200:203], v[180:183]\n\t"
            "ds_write_b64 v245, v[184:185] offset:6144\n\t"
            "v_mfma_f32_16x16x32_bf16 v[228:231], v[172:175], v[188:191], 0\n\t"
            "ds_read_b64 v[184:185], v253 offset:320\n\t"
            "v_mfma_f32_16x16x32_bf16 v[228:231], v[164:167], v[192:195], v[228:231]\n\t"
            "v_mfma_f32_16x16x32_bf16 v[188:191], v[176:179], v[188:191], 0\n\t"
            "v_mfma_f32_16x16x32_bf16 v[188:191], v[160:163], v[192:195], v[188:191]\n\t"
            "s_nop 1\n\t"
            "v_mul_f32 v180, v238, v180\n\t"
            "v_mul_f32 v181, v238, v181\n\t"
            "v_mul_f32 v182, v238, v182\n\t"
            "v_mul_f32 v183, v238, v183\n\t"
            "v_cvt_pk_bf16_f32 v180, v180, v181\n\t"
            "v_cvt_pk_bf16_f32 v181, v182, v183\n\t"
            "v_mul_f32 v228, v237, v228\n\t"
            "v_mul_f32 v229, v237, v229\n\t"
            "v_mul_f32 v230, v237, v230\n\t"
            "v_mul_f32 v231, v237, v231\n\t"
            "v_cvt_pk_bf16_f32 v228, v228, v229\n\t"
            "v_cvt_pk_bf16_f32 v229, v230, v231\n\t"
            "v_mul_f32 v188, v237, v188\n\t"
            "v_mul_f32 v189, v237, v189\n\t"
            "v_mul_f32 v190, v237, v190\n\t"
            "v_mul_f32 v191, v237, v191\n\t"
            "v_cvt_pk_bf16_f32 v188, v188, v189\n\t"
            "v_cvt_pk_bf16_f32 v189, v190, v191\n\t"
            "ds_read_b64 v[182:183], v253 offset:288\n\t"
            "ds_write_b64 v245, v[180:181] offset:12288\n\t"
            "ds_read_b64 v[180:181], v253 offset:256\n\t"
            "ds_write_b64 v245, v[228:229] offset:6272\n\t"
            "v_mfma_f32_16x16x32_bf16 v[228:231], v[172:175], v[196:199], 0\n\t"
            "ds_write_b64 v245, v[188:189] offset:6400\n\t"
            "v_mfma_f32_16x16x32_bf16 v[228:231], v[164:167], v[200:203], v[228:231]\n\t"
            "v_mfma_f32_16x16x32_bf16 v[196:199], v[176:179], v[196:199], 0\n\t"
            "v_mfma_f32_16x16x32_bf16 v[196:199], v[160:163], v[200:203], v[196:199]\n\t"
            "s_nop 5\n\t"
            "v_mul_f32 v228, v238, v228\n\t"
            "s_waitcnt lgkmcnt(2)\n\t"
            "v_mfma_f32_16x16x32_bf16 v[168:171], v[168:171], v[180:183], 0\n\t"
            "v_mfma_f32_16x16x32_bf16 v[172:175], v[172:175], v[180:183], 0\n\t"
            "v_mfma_f32_16x16x32_bf16 v[176:179], v[176:179], v[180:183], 0\n\t"
            "v_mul_f32 v229, v238, v229\n\t"
            "v_mfma_f32_16x16x32_bf16 v[168:171], v[156:159], v[184:187], v[168:171]\n\t"
            "v_mul_f32 v230, v238, v230\n\t"
            "v_mul_f32 v231, v238, v231\n\t"
            "v_mfma_f32_16x16x32_bf16 v[172:175], v[164:167], v[184:187], v[172:175]\n\t"
            "v_cvt_pk_bf16_f32 v228, v228, v229\n\t"
            "v_cvt_pk_bf16_f32 v229, v230, v231\n\t"
            "v_mfma_f32_16x16x32_bf16 v[176:179], v[160:163], v[184:187], v[176:179]\n\t"
            "ds_write_b64 v245, v[228:229] offset:12416\n\t"
            "v_mfma_f32_16x16x32_bf16 v[228:231], v[240:243], v[180:183], 0\n\t"
            "v_mul_f32 v196, v238, v196\n\t"
            "v_mul_f32 v197, v238, v197\n\t"
            "v_mfma_f32_16x16x32_bf16 v[228:231], v[240:243], v[184:187], v[228:231]\n\t"
            "v_mul_f32 v198, v238, v198\n\t"
            "v_mul_f32 v199, v238, v199\n\t"
            "v_cvt_pk_bf16_f32 v196, v196, v197\n\t"
            "v_cvt_pk_bf16_f32 v197, v198, v199\n\t"
            "ds_write_b64 v245, v[196:197] offset:12544\n\t"
            "s_nop 2\n\t"
            "v_rcp_f32 v229, v228\n\t"
            "s_nop 0\n\t"
            "v_mul_f32 v168, v229, v168\n\t"
            "v_mul_f32 v169, v229, v169\n\t"
            "v_mul_f32 v172, v229, v172\n\t"
            "v_mul_f32 v173, v229, v173\n\t"
            "v_mul_f32 v176, v229, v176\n\t"
            "v_mul_f32 v177, v229, v177\n\t"
            "v_mul_f32 v170, v229, v170\n\t"
            "v_mul_f32 v171, v229, v171\n\t"
            "v_cvt_pk_bf16_f32 v168, v168, v169\n\t"
            "v_mul_f32 v174, v229, v174\n\t"
            "v_mul_f32 v175, v229, v175\n\t"
            "v_cvt_pk_bf16_f32 v172, v172, v173\n\t"
            "v_mul_f32 v178, v229, v178\n\t"
            "v_mul_f32 v179, v229, v179\n\t"
            "v_cvt_pk_bf16_f32 v176, v176, v177\n\t"
            "v_xor_b32 v230, s73, v255\n\t"
            "v_cvt_pk_bf16_f32 v169, v170, v171\n\t"
            "v_cvt_pk_bf16_f32 v173, v174, v175\n\t"
            "v_cvt_pk_bf16_f32 v177, v178, v179\n\t"
            "s_mov_b64 exec, s[68:69]\n\t"
            "ds_write_b64 v230, v[168:169]\n\t"
            "ds_write_b64 v230, v[172:173] offset:128\n\t"
            "ds_write_b64 v230, v[176:177] offset:256\n\t"
            "s_mov_b64 exec, -1\n\t"
            "s_waitcnt lgkmcnt(0)\n\t"
            "v_mov_b32 v144, s42\n\t"
            "v_bfrev_b32 v145, -2\n\t"
            "ds_write_b32 v144, v145\n\t"
            "s_waitcnt lgkmcnt(0)\n\t"
            "s_nop 7\n\t"
            : "+{v[232:235]}"(pl), "+{v249}"(aRSw_),
              "+{s44}"(s44), "+{s48}"(s48), "+{s70}"(s70), "+{s72}"(s72), "+{s73}"(s73), "+{s76}"(s76), "+{s77}"(s77)
            : "{v244}"(pE_), "{v245}"(aO_), "{v246}"(aZ_), "{v247}"(aVB_), "{v248}"(pST_), "{v250}"(aPLw_), "{v251}"(aRSr_), "{v252}"(g16_), "{v253}"(aPLr_), "{v254}"(loVL_), "{v255}"(loOL_), "{v[240:243]}"(ones4), "{s47}"((ldsb + X_OFF) ^ (ldsb + X1_OFF)),
              "{s[58:59]}"(0xFFFFull), "{s[60:61]}"(0xFFFF0000ull), "{s[62:63]}"(0xFFFF00000000ull), "{s[64:65]}"(0xFFFF000000000000ull),
              "{s[66:67]}"(0x1FFFFFFFFFFFFull), "{s[68:69]}"(0x0001000100010001ull), "{s[26:27]}"(0x1111111111111111ull), "{s23}"(64.f), "{s57}"(RS_OFF + 768 + (4 + (w >> 1)) * 192 - RS_BASE), "{s45}"(ldsb + V_OFF + w * 4608),
              "{s[28:31]}"(wr), "{s33}"(wave * 36864), "{s46}"(ldsb + PG_OFF), "{s42}"(ldsb + PG_OFF + w * 4), "{s56}"(ldsb + O_OFF), "{s[36:39]}"(tr), "{s41}"(to), "{s40}"(ldsb + X_OFF + 18432), "{s34}"(SH_OFF + bx * 12288 + w * 3072), "{s35}"(ldsb + O_OFF + w * 3072),
              "{s[50:51]}"(0x2222222222222222ull), "{s[52:53]}"(0x4444444444444444ull), "{s[54:55]}"(0x8888888888888888ull)
            : "s24", "s25", "v0", "v1", "v2", "v3", "v4", "v5", "v6", "v7", "v8", "v9", "v10", "v11", "v12", "v13", "v14", "v15", "v16", "v17", "v18", "v19", "v20", "v21", "v22", "v23", "v24", "v25", "v26", "v27", "v28", "v29", "v30", "v31", "v32", "v33", "v34", "v35", "v36", "v37", "v38", "v39", "v40", "v41", "v42", "v43", "v44", "v45", "v46", "v47", "v48", "v49", "v50", "v51", "v52", "v53", "v54", "v55", "v56", "v57", "v58", "v59", "v60", "v61", "v62", "v63", "v64", "v65", "v66", "v67", "v68", "v69", "v70", "v71", "v72", "v73", "v74", "v75", "v76", "v77", "v78", "v79", "v80", "v81", "v82", "v83", "v84", "v85", "v86", "v87", "v88", "v89", "v90", "v91", "v92", "v93", "v94", "v95", "v96", "v97", "v98", "v99", "v100", "v101", "v102", "v103", "v104", "v105", "v106", "v107", "v108", "v109", "v110", "v111", "v112", "v113", "v114", "v115", "v116", "v117", "v118", "v119", "v120", "v121", "v122", "v123", "v124", "v125", "v126", "v127", "v128", "v129", "v130", "v131", "v132", "v133", "v134", "v135", "v136", "v137", "v138", "v139", "v140", "v141", "v142", "v143", "v144", "v145", "v146", "v147", "v148", "v149", "v150", "v151", "v152", "v153", "v154", "v155", "v156", "v157", "v158", "v159", "v160", "v161", "v162", "v163", "v164", "v165", "v166", "v167", "v168", "v169", "v170", "v171", "v172", "v173", "v174", "v175", "v176", "v177", "v178", "v179", "v180", "v181", "v182", "v183", "v184", "v185", "v186", "v187", "v188", "v189", "v190", "v191", "v192", "v193", "v194", "v195", "v196", "v197", "v198", "v199", "v200", "v201", "v202", "v203", "v204", "v205", "v206", "v207", "v208", "v209", "v210", "v211", "v212", "v213", "v214", "v215", "v216", "v217", "v218", "v219", "v220", "v221", "v222", "v223", "v224", "v225", "v226", "v227", "v228", "v229", "v230", "v231", "v236", "v237", "v238", "v239", "s49", "vcc", "scc", "m0", "memory");
        }
        asm volatile("wk_code_end:");

        if (q0 == 40 && wave == 0) {
#pragma unroll 1
            for (int it = 0; it < SPIN_MAX && __hip_atomic_load((gu32*)((char*)wf + CN_OFF), RLX_AGENT) != (unsigned)GRID; ++it) __builtin_amdgcn_s_sleep(8);
            __hip_atomic_store((gu32*)((char*)wf + EP_OFF), __hip_atomic_load((gu32*)((char*)wf + EP_OFF), RLX_AGENT) + 1u, RLX_AGENT);
        }
    } else {
        ROLE_SETUP
        {
            LANE_SETUP
            const bool isV = wave < 6;
            CONV_CONSTS(0)
            const unsigned aCA_ = ldsb + CT_OFF + lane * 16, rsb = ldsb + RS_BASE, pE = fE - X_OFF;
            const unsigned aRSw = (lane < 48 ? rsb : ldsb + RS_OFF + 2304 - 192) + lane * 4, aRSr = rsb + (lane & 48);
            unsigned pr[3];
#pragma unroll
            for (int k = 0; k < 3; ++k) {
                const int c8 = 12 * (w - 2) + 4 * k + g;
                const int ws = c8 / 6, r6 = c8 - 6 * ws;
                pr[k] = l15 * 384 + (r6 >> 1) * 128 + (((2 * ws + (r6 & 1)) ^ sw) << 4);
            }
            unsigned r242 = isV ? pE : ldsb + O_OFF + pE, r243 = isV ? ldsb + V_OFF + w * 9216 + lane * 16 : (ldsb + O_OFF + pE) ^ 64;
            u32x4 r244 = {isV ? ldsb + V_OFF + w * 9216 + 1024 + lane * 8 : pr[0], isV ? aRSr : pr[1], isV ? aRSw : pr[2], (unsigned)lane * 16};
            const int hd = isV ? 2 * w : 2 * w - 3;
            const unsigned vq = ldsb + OL_OFF + l15 * 384 + (((2 * hd + (g >> 1)) ^ sw) << 4) + 8 * (g & 1);
            u32x4 f1 = {16 + l15 < cnt ? vq + 6144 : ldsb + OL_OFF + 22 * 384 + g * 8, pE, ldsb + V_OFF + hd * 4608 + lane * 16, ldsb + V_OFF + hd * 4608 + 1024 + lane * 8};
            u32x4 f2 = {aRSw, aRSr, rsb + l15 * 4, 0u};
            const unsigned lB = ldsb + LB_OFF(l15 & 7) + (g >> 1) * 1536 + (g & 1) * 768 + 96 * w;
            const unsigned long long ob = (unsigned long long)out, xb = (unsigned long long)((const char*)x - XBIAS), wb = (unsigned long long)wf;
            const u32x4 orr = {(unsigned)ob, (unsigned)(ob >> 32) & 0xffffu, (unsigned)(IMG * IMG * CH * 2), 0x00020000u};
            const u32x4 xr = {(unsigned)xb, (unsigned)(xb >> 32) & 0xffffu, (unsigned)(IMG * IMG * CH * 2 + XBIAS), 0x00020000u};
            const u32x4 wr = {(unsigned)wb, (unsigned)(wb >> 32) & 0xffffu, (unsigned)WS_BYTES, 0x00020000u};
            unsigned s45 = OOB, s46 = OOB, s47 = (en0 & 0x7ffffffu) + 96 * w;
            unsigned s48 = ldsb + X1_OFF, s67 = ldsb + X_OFF, s76 = ldsb + WL_OFF + 4, s56 = en0 >> 30;
            unsigned s21 = SH_OFF + bx * 12288, s44 = ldsb + PG_OFF + w * 4, s70 = isV ? wave * 36864 : (wave - 2) * 36864 + 18432;
            unsigned long long xp = 0, sp = 0;
#pragma unroll
            for (int v = 0; v < 4; ++v) {
                const unsigned r = (CROW >> (8 * v)) & 0xFF;
                xp |= (unsigned long long)XPAT(r) << (16 * v);
                sp |= (unsigned long long)SPAT(r) << (16 * v);
            }
#define B_VIN "{v250}"(pXw_), "{v251}"(pSTw_), "{v252}"(aCA_), "{v253}"(aB_), "{v254}"(aA_), "{v255}"(idx_), "{v[238:241]}"(sel4)
#define B_SIN "{s[40:43]}"(orr), "{s[72:75]}"(xr), "{s39}"(0x401fffffu), "{s[28:31]}"(wr), "{s33}"(wave * 36864), "{s78}"(96 * w), \
              "{s27}"(96 * w - 384), "{s[50:51]}"(xp), "{s[52:53]}"(sp), "{s54}"(CLANE), "{s55}"(CROW & 0x0F0F0F0Fu), "{s[34:35]}"(0x0001000100010001ull), \
              "{s[60:61]}"(0x2222222222222222ull), "{s[62:63]}"(0x4444444444444444ull), "{s[64:65]}"(0x8888888888888888ull), \
              "{s77}"(w == 2 ? 768 : 6 * ROWBYTES + 768), "{s25}"(w == 2 ? 3 * ROWBYTES : OOB), "{s32}"(ldsb + X_OFF + 18432), \
              "{s24}"(ldsb + WL_OFF + 12 + 4 * (cnt - 0)), "{s66}"(ldsb + w * 8192), "{s71}"((ldsb + X_OFF) ^ (ldsb + X1_OFF)), \
              "{s23}"(ldsb + WL_OFF), "{s22}"(ldsb + O_OFF), \
              "{s20}"(6 * ROWBYTES + 2304 + 192 * (w & 1)), "{s19}"(2048 + (w & 1) * 1024)
            asm volatile("" :: B_VIN, "{v242}"(r242), "{v243}"(r243), "{v[244:247]}"(r244), "{v[228:231]}"(f1), "{v[232:235]}"(f2), "v"(lB), "v"(vq));
            asm volatile("" :: B_SIN);
            asm volatile("s_waitcnt vmcnt(9)" ::: "memory");
            unsigned lm = *(const unsigned*)(smem + TP_OFF + 4096 + w * 256 + ((l15 & 7) * 4 + g) * 4);
            lm = l15 < 8 ? lm : OOB + 47;
            const int row = lm & 63;
            u32x4 f0 = {XPAT(row) + (unsigned)(O_OFF - X_OFF), SPAT(row) + (unsigned)(STL_OFF - X_OFF - 18816), lB, vq};
            asm volatile(
            "s_setprio 3\n\t"
            "s_waitcnt lgkmcnt(0)\n\t"
            "s_barrier\n\t"
            "s_nop 7\n\t"
            "ds_read_b128 v[168:171], v254\n\t"
            "v_mov_b64 v[176:177], 0\n\t"
            "v_mov_b64 v[178:179], 0\n\t"
            "v_mov_b64 v[180:181], 0\n\t"
            "v_mov_b64 v[182:183], 0\n\t"
            "v_mov_b64 v[184:185], 0\n\t"
            "v_mov_b64 v[186:187], 0\n\t"
            "v_mov_b64 v[188:189], 0\n\t"
            "v_mov_b64 v[190:191], 0\n\t"
            "v_add_u32 v236, s67, v224\n\t"
            "v_xor_b32 v237, 16, v236\n\t"
            "v_add_u32 v248, s67, v225\n\t"
            "ds_read_b64 v[144:145], v226\n\t"
            "ds_read_b64 v[146:147], v226 offset:384\n\t"
            "ds_read_b64 v[148:149], v226 offset:3072\n\t"
            "ds_read_b64 v[150:151], v226 offset:3456\n\t"
            "ds_read_b128 v[172:175], v254 offset:64\n\t"
            "ds_read_b64 v[152:153], v226 offset:8\n\t"
            "ds_read_b64 v[154:155], v226 offset:392\n\t"
            "ds_read_b64 v[156:157], v226 offset:3080\n\t"
            "ds_read_b64 v[158:159], v226 offset:3464\n\t"
            "ds_read_b64 v[160:161], v226 offset:16\n\t"
            "ds_read_b64 v[162:163], v226 offset:400\n\t"
            "ds_read_b64 v[164:165], v226 offset:3088\n\t"
            "ds_read_b64 v[166:167], v226 offset:3472\n\t"
            "s_waitcnt lgkmcnt(9)\n\t"
            "v_smfmac_f32_16x16x64_bf16 v[176:179], v[168:171], v[144:151], v255\n\t"
            "ds_read_b128 v[168:171], v254 offset:128\n\t"
            "ds_read_b64 v[144:145], v226 offset:24\n\t"
            "ds_read_b64 v[146:147], v226 offset:408\n\t"
            "ds_read_b64 v[148:149], v226 offset:3096\n\t"
            "s_waitcnt lgkmcnt(8)\n\t"
            "v_smfmac_f32_16x16x64_bf16 v[180:183], v[172:175], v[152:159], v255\n\t"
            "ds_read_b64 v[150:151], v226 offset:3480\n\t"
            "ds_read_b128 v[172:175], v254 offset:192\n\t"
            "ds_read_b64 v[152:153], v226 offset:32\n\t"
            "v_cvt_pk_bf16_f32 v176, v176, v177\n\t"
            "v_cvt_pk_bf16_f32 v177, v178, v179\n\t"
            "ds_read_b64 v[154:155], v226 offset:416\n\t"
            "ds_read_b64 v[156:157], v226 offset:3104\n\t"
            "ds_read_b64 v[158:159], v226 offset:3488\n\t"
            "v_cvt_pk_bf16_f32 v178, v180, v181\n\t"
            "v_cvt_pk_bf16_f32 v179, v182, v183\n\t"
            "v_mov_b64 v[180:181], 0\n\t"
            "v_mov_b64 v[182:183], 0\n\t"
            "v_mfma_f32_4x4x4_16b_bf16 v[196:199], v[176:177], v[176:177], 0\n\t"
            "v_mfma_f32_4x4x4_16b_bf16 v[192:195], v[238:239], v[176:177], 0\n\t"
            "s_nop 0\n\t"
            "v_mfma_f32_4x4x4_16b_bf16 v[196:199], v[178:179], v[178:179], v[196:199]\n\t"
            "v_mfma_f32_4x4x4_16b_bf16 v[192:195], v[238:239], v[178:179], v[192:195]\n\t"
            "ds_write_b128 v236, v[176:179]\n\t"
            "s_waitcnt lgkmcnt(10)\n\t"
            "v_smfmac_f32_16x16x64_bf16 v[184:187], v[168:171], v[160:167], v255\n\t"
            "ds_read_b128 v[168:171], v254 offset:256\n\t"
            "s_waitcnt lgkmcnt(6)\n\t"
            "v_smfmac_f32_16x16x64_bf16 v[188:191], v[172:175], v[144:151], v255\n\t"
            "ds_read_b64 v[160:161], v226 offset:40\n\t"
            "v_mov_b64 v[176:177], 0\n\t"
            "v_mov_b64 v[178:179], 0\n\t"
            "ds_read_b64 v[162:163], v226 offset:424\n\t"
            "ds_read_b64 v[164:165], v226 offset:3112\n\t"
            "ds_read_b64 v[166:167], v226 offset:3496\n\t"
            "v_cvt_pk_bf16_f32 v184, v184, v185\n\t"
            "v_cvt_pk_bf16_f32 v185, v186, v187\n\t"
            "v_cvt_pk_bf16_f32 v186, v188, v189\n\t"
            "v_cvt_pk_bf16_f32 v187, v190, v191\n\t"
            "v_mov_b64 v[188:189], 0\n\t"
            "v_mov_b64 v[190:191], 0\n\t"
            "v_mfma_f32_4x4x4_16b_bf16 v[196:199], v[184:185], v[184:185], v[196:199]\n\t"
            "v_mfma_f32_4x4x4_16b_bf16 v[192:195], v[238:239], v[184:185], v[192:195]\n\t"
            "s_nop 0\n\t"
            "v_mfma_f32_4x4x4_16b_bf16 v[196:199], v[186:187], v[186:187], v[196:199]\n\t"
            "v_mfma_f32_4x4x4_16b_bf16 v[192:195], v[238:239], v[186:187], v[192:195]\n\t"
            "ds_read_b128 v[172:175], v254 offset:320\n\t"
            "ds_read_b64 v[144:145], v226 offset:48\n\t"
            "s_waitcnt lgkmcnt(6)\n\t"
            "v_smfmac_f32_16x16x64_bf16 v[176:179], v[168:171], v[152:159], v255\n\t"
            "ds_read_b64 v[146:147], v226 offset:432\n\t"
            "ds_read_b64 v[148:149], v226 offset:3120\n\t"
            "ds_read_b64 v[150:151], v226 offset:3504\n\t"
            "ds_read_b128 v[168:171], v254 offset:384\n\t"
            "ds_read_b64 v[152:153], v226 offset:56\n\t"
            "ds_read_b64 v[154:155], v226 offset:440\n\t"
            "ds_read_b64 v[156:157], v226 offset:3128\n\t"
            "ds_read_b64 v[158:159], v226 offset:3512\n\t"
            "v_cvt_pk_bf16_f32 v176, v176, v177\n\t"
            "v_cvt_pk_bf16_f32 v177, v178, v179\n\t"
            "ds_write_b128 v237, v[184:187]\n\t"
            "s_waitcnt lgkmcnt(10)\n\t"
            "v_smfmac_f32_16x16x64_bf16 v[180:183], v[172:175], v[160:167], v255\n\t"
            "ds_read_b128 v[172:175], v254 offset:448\n\t"
            "v_mov_b64 v[184:185], 0\n\t"
            "v_mov_b64 v[186:187], 0\n\t"
            "ds_read_b64 v[160:161], v226 offset:64\n\t"
            "ds_read_b64 v[162:163], v226 offset:448\n\t"
            "ds_read_b64 v[164:165], v226 offset:3136\n\t"
            "s_waitcnt lgkmcnt(9)\n\t"
            "v_smfmac_f32_16x16x64_bf16 v[184:187], v[168:171], v[144:151], v255\n\t"
            "ds_read_b64 v[166:167], v226 offset:3520\n\t"
            "v_cvt_pk_bf16_f32 v178, v180, v181\n\t"
            "v_cvt_pk_bf16_f32 v179, v182, v183\n\t"
            "v_mov_b64 v[180:181], 0\n\t"
            "v_mov_b64 v[182:183], 0\n\t"
            "v_mfma_f32_4x4x4_16b_bf16 v[196:199], v[176:177], v[176:177], v[196:199]\n\t"
            "v_mfma_f32_4x4x4_16b_bf16 v[192:195], v[238:239], v[176:177], v[192:195]\n\t"
            "s_nop 0\n\t"
            "v_mfma_f32_4x4x4_16b_bf16 v[196:199], v[178:179], v[178:179], v[196:199]\n\t"
            "v_mfma_f32_4x4x4_16b_bf16 v[192:195], v[238:239], v[178:179], v[192:195]\n\t"
            "ds_read_b128 v[168:171], v254 offset:512\n\t"
            "v_cvt_pk_bf16_f32 v184, v184, v185\n\t"
            "v_cvt_pk_bf16_f32 v185, v186, v187\n\t"
            "ds_read_b64 v[144:145], v226 offset:72\n\t"
            "s_waitcnt lgkmcnt(6)\n\t"
            "v_smfmac_f32_16x16x64_bf16 v[188:191], v[172:175], v[152:159], v255\n\t"
            "ds_read_b64 v[146:147], v226 offset:456\n\t"
            "ds_read_b64 v[148:149], v226 offset:3144\n\t"
            "ds_read_b64 v[150:151], v226 offset:3528\n\t"
            "ds_read_b128 v[172:175], v254 offset:576\n\t"
            "ds_read_b64 v[152:153], v226 offset:80\n\t"
            "ds_read_b64 v[154:155], v226 offset:464\n\t"
            "ds_read_b64 v[156:157], v226 offset:3152\n\t"
            "ds_read_b64 v[158:159], v226 offset:3536\n\t"
            "v_cvt_pk_bf16_f32 v186, v188, v189\n\t"
            "v_cvt_pk_bf16_f32 v187, v190, v191\n\t"
            "v_mov_b64 v[188:189], 0\n\t"
            "v_mov_b64 v[190:191], 0\n\t"
            "v_mfma_f32_4x4x4_16b_bf16 v[196:199], v[184:185], v[184:185], v[196:199]\n\t"
            "v_mfma_f32_4x4x4_16b_bf16 v[192:195], v[238:239], v[184:185], v[192:195]\n\t"
            "s_nop 0\n\t"
            "v_mfma_f32_4x4x4_16b_bf16 v[196:199], v[186:187], v[186:187], v[196:199]\n\t"
            "v_mfma_f32_4x4x4_16b_bf16 v[192:195], v[238:239], v[186:187], v[192:195]\n\t"
            "ds_write_b128 v236, v[176:179] offset:128\n\t"
            "v_mov_b64 v[176:177], 0\n\t"
            "v_mov_b64 v[178:179], 0\n\t"
            "s_waitcnt lgkmcnt(10)\n\t"
            "s_nop 0\n\t"
            "v_smfmac_f32_16x16x64_bf16 v[176:179], v[168:171], v[160:167], v255\n\t"
            "ds_read_b128 v[168:171], v254 offset:640\n\t"
            "s_waitcnt lgkmcnt(6)\n\t"
            "v_smfmac_f32_16x16x64_bf16 v[180:183], v[172:175], v[144:151], v255\n\t"
            "ds_read_b64 v[160:161], v226 offset:88\n\t"
            "ds_read_b64 v[162:163], v226 offset:472\n\t"
            "ds_read_b64 v[164:165], v226 offset:3160\n\t"
            "ds_read_b64 v[166:167], v226 offset:3544\n\t"
            "ds_read_b128 v[172:175], v254 offset:704\n\t"
            "ds_write_b128 v237, v[184:187] offset:128\n\t"
            "v_mov_b64 v[184:185], 0\n\t"
            "v_mov_b64 v[186:187], 0\n\t"
            "v_cvt_pk_bf16_f32 v176, v176, v177\n\t"
            "v_cvt_pk_bf16_f32 v177, v178, v179\n\t"
            "v_cvt_pk_bf16_f32 v178, v180, v181\n\t"
            "v_cvt_pk_bf16_f32 v179, v182, v183\n\t"
            "s_nop 1\n\t"
            "v_mfma_f32_4x4x4_16b_bf16 v[196:199], v[176:177], v[176:177], v[196:199]\n\t"
            "v_mfma_f32_4x4x4_16b_bf16 v[192:195], v[238:239], v[176:177], v[192:195]\n\t"
            "s_nop 0\n\t"
            "v_mfma_f32_4x4x4_16b_bf16 v[196:199], v[178:179], v[178:179], v[196:199]\n\t"
            "v_mfma_f32_4x4x4_16b_bf16 v[192:195], v[238:239], v[178:179], v[192:195]\n\t"
            "ds_write_b128 v236, v[176:179] offset:256\n\t"
            "s_waitcnt lgkmcnt(7)\n\t"
            "v_smfmac_f32_16x16x64_bf16 v[184:187], v[168:171], v[152:159], v255\n\t"
            "s_waitcnt lgkmcnt(2)\n\t"
            "v_smfmac_f32_16x16x64_bf16 v[188:191], v[172:175], v[160:167], v255\n\t"
            "s_nop 5\n\t"
            "v_cvt_pk_bf16_f32 v184, v184, v185\n\t"
            "v_cvt_pk_bf16_f32 v185, v186, v187\n\t"
            "v_cvt_pk_bf16_f32 v186, v188, v189\n\t"
            "v_cvt_pk_bf16_f32 v187, v190, v191\n\t"
            "s_nop 1\n\t"
            "v_mfma_f32_4x4x4_16b_bf16 v[196:199], v[184:185], v[184:185], v[196:199]\n\t"
            "v_mfma_f32_4x4x4_16b_bf16 v[192:195], v[238:239], v[184:185], v[192:195]\n\t"
            "s_nop 0\n\t"
            "v_mfma_f32_4x4x4_16b_bf16 v[196:199], v[186:187], v[186:187], v[196:199]\n\t"
            "v_mfma_f32_4x4x4_16b_bf16 v[192:195], v[238:239], v[186:187], v[192:195]\n\t"
            "ds_write_b128 v237, v[184:187] offset:256\n\t"
            "s_nop 6\n\t"
            "v_cndmask_b32_e64 v193, v196, v197, s[60:61]\n\t"
            "v_cndmask_b32_e64 v193, v193, v198, s[62:63]\n\t"
            "v_cndmask_b32_e64 v193, v193, v199, s[64:65]\n\t"
            "ds_write_b64 v248, v[192:193] offset:18816\n\t"
            "s_waitcnt lgkmcnt(0)\n\t"
            "s_nop 7\n\t"
            "v_mov_b32 v144, s44\n\t"
            "v_mov_b32 v145, 0x0fffffff\n\t"
            "ds_write_b32 v144, v145\n\t"
            "s_nop 7\n\t"
            "ds_read_b128 v[168:171], v254\n\t"
            "s_waitcnt vmcnt(0)\n\t"
            "ds_read_b64 v[144:145], v253\n\t"
            "v_mov_b64 v[176:177], 0\n\t"
            "v_mov_b64 v[178:179], 0\n\t"
            "v_mov_b64 v[180:181], 0\n\t"
            "v_mov_b64 v[182:183], 0\n\t"
            "v_mov_b32 v249, s76\n\t"
            "v_mov_b64 v[184:185], 0\n\t"
            "v_mov_b64 v[186:187], 0\n\t"
            "v_mov_b64 v[188:189], 0\n\t"
            "v_mov_b64 v[190:191], 0\n\t"
            "v_add_u32 v236, s67, v250\n\t"
            "v_add_u32 v248, s67, v251\n\t"
            "ds_read_b64 v[146:147], v253 offset:96\n\t"
            "ds_read_b64 v[148:149], v253 offset:1744\n\t"
            "ds_read_b64 v[150:151], v253 offset:1840\n\t"
            "ds_read_b128 v[172:175], v254 offset:64\n\t"
            "ds_read_b64 v[152:153], v253 offset:8\n\t"
            "ds_read_b64 v[154:155], v253 offset:104\n\t"
            "ds_read_b64 v[156:157], v253 offset:1752\n\t"
            "ds_read_b64 v[158:159], v253 offset:1848\n\t"
            "ds_read_b64 v[160:161], v253 offset:16\n\t"
            "ds_read_b64 v[162:163], v253 offset:112\n\t"
            "ds_read_b64 v[164:165], v253 offset:1760\n\t"
            "ds_read_b64 v[166:167], v253 offset:1856\n\t"
            "ds_read_b32 v249, v249\n\t"
            "s_waitcnt lgkmcnt(10)\n\t"
            "v_smfmac_f32_16x16x64_bf16 v[176:179], v[168:171], v[144:151], v255\n\t"
            "ds_read_b128 v[168:171], v254 offset:128\n\t"
            "ds_read_b64 v[144:145], v253 offset:24\n\t"
            "ds_read_b64 v[146:147], v253 offset:120\n\t"
            "ds_read_b64 v[148:149], v253 offset:1768\n\t"
            "s_waitcnt lgkmcnt(9)\n\t"
            "v_smfmac_f32_16x16x64_bf16 v[180:183], v[172:175], v[152:159], v255\n\t"
            "ds_read_b64 v[150:151], v253 offset:1864\n\t"
            "ds_read_b128 v[172:175], v254 offset:192\n\t"
            "ds_read_b64 v[152:153], v253 offset:32\n\t"
            "v_cvt_pk_bf16_f32 v176, v176, v177\n\t"
            "v_cvt_pk_bf16_f32 v177, v178, v179\n\t"
            "s_waitcnt lgkmcnt(7)\n\t"
            "v_readfirstlane_b32 s26, v249\n\t"
            "ds_read_b64 v[154:155], v253 offset:128\n\t"
            "s_and_b32 s68, s26, 0x7ffffff\n\t"
            "s_add_u32 s68, s68, s78\n\t"
            "s_and_b32 s69, s26, 0x38000000\n\t"
            "s_or_b32 s69, s69, s39\n\t"
            "s_bitcmp1_b32 s26, 27\n\t"
            "s_cselect_b32 s79, 2.0, s68\n\t"
            "ds_read_b64 v[156:157], v253 offset:1776\n\t"
            "s_and_b32 s57, s56, 3\n\t"
            "s_lshl_b32 s58, s57, 3\n\t"
            "s_lshr_b32 m0, s54, s58\n\t"
            "s_waitcnt lgkmcnt(8)\n\t"
            "v_smfmac_f32_16x16x64_bf16 v[184:187], v[168:171], v[160:167], v255\n\t"
            "s_lshl_b32 s58, s57, 4\n\t"
            "s_lshr_b64 s[36:37], s[50:51], s58\n\t"
            "s_and_b32 s57, s36, 0xffff\n\t"
            "s_add_u32 s57, s57, s67\n\t"
            "ds_read_b64 v[158:159], v253 offset:1872\n\t"
            "s_lshr_b64 s[36:37], s[52:53], s58\n\t"
            "s_and_b32 s58, s36, 0xffff\n\t"
            "s_add_u32 s58, s58, s67\n\t"
            "ds_read_b128 v[168:171], v254 offset:256\n\t"
            "s_and_b32 s59, s50, 0xffff\n\t"
            "s_add_u32 s59, s59, s67\n\t"
            "s_and_b32 s38, s52, 0xffff\n\t"
            "s_add_u32 s38, s38, s67\n\t"
            "s_waitcnt lgkmcnt(5)\n\t"
            "v_smfmac_f32_16x16x64_bf16 v[188:191], v[172:175], v[144:151], v255\n\t"
            "ds_read_b64 v[160:161], v253 offset:40\n\t"
            "v_cvt_pk_bf16_f32 v178, v180, v181\n\t"
            "v_cvt_pk_bf16_f32 v179, v182, v183\n\t"
            "v_writelane_b32 v236, s59, m0\n\t"
            "v_writelane_b32 v236, s57, 63\n\t"
            "v_mfma_f32_4x4x4_16b_bf16 v[196:199], v[176:177], v[176:177], 0\n\t"
            "v_mfma_f32_4x4x4_16b_bf16 v[192:195], v[238:239], v[176:177], 0\n\t"
            "s_nop 0\n\t"
            "v_mfma_f32_4x4x4_16b_bf16 v[196:199], v[178:179], v[178:179], v[196:199]\n\t"
            "v_mfma_f32_4x4x4_16b_bf16 v[192:195], v[238:239], v[178:179], v[192:195]\n\t"
            "ds_read_b64 v[162:163], v253 offset:136\n\t"
            "v_mov_b64 v[180:181], 0\n\t"
            "v_mov_b64 v[182:183], 0\n\t"
            "v_cvt_pk_bf16_f32 v184, v184, v185\n\t"
            "v_cvt_pk_bf16_f32 v185, v186, v187\n\t"
            "v_cvt_pk_bf16_f32 v186, v188, v189\n\t"
            "v_cvt_pk_bf16_f32 v187, v190, v191\n\t"
            "v_xor_b32 v237, 16, v236\n\t"
            "v_mov_b64 v[188:189], 0\n\t"
            "v_mfma_f32_4x4x4_16b_bf16 v[196:199], v[184:185], v[184:185], v[196:199]\n\t"
            "v_mfma_f32_4x4x4_16b_bf16 v[192:195], v[238:239], v[184:185], v[192:195]\n\t"
            "s_nop 0\n\t"
            "v_mfma_f32_4x4x4_16b_bf16 v[196:199], v[186:187], v[186:187], v[196:199]\n\t"
            "v_mfma_f32_4x4x4_16b_bf16 v[192:195], v[238:239], v[186:187], v[192:195]\n\t"
            "ds_read_b64 v[164:165], v253 offset:1784\n\t"
            "v_mov_b64 v[190:191], 0\n\t"
            "v_writelane_b32 v248, s38, m0\n\t"
            "v_writelane_b32 v248, s58, 63\n\t"
            "ds_read_b64 v[166:167], v253 offset:1880\n\t"
            "ds_read_b128 v[172:175], v254 offset:320\n\t"
            "ds_read_b64 v[144:145], v253 offset:48\n\t"
            "ds_read_b64 v[146:147], v253 offset:144\n\t"
            "ds_read_b64 v[148:149], v253 offset:1792\n\t"
            "ds_read_b64 v[150:151], v253 offset:1888\n\t"
            "ds_write_b128 v236, v[176:179]\n\t"
            "v_mov_b64 v[176:177], 0\n\t"
            "v_mov_b64 v[178:179], 0\n\t"
            "s_waitcnt lgkmcnt(10)\n\t"
            "s_nop 0\n\t"
            "v_smfmac_f32_16x16x64_bf16 v[176:179], v[168:171], v[152:159], v255\n\t"
            "ds_read_b128 v[168:171], v254 offset:384\n\t"
            "ds_read_b64 v[152:153], v253 offset:56\n\t"
            "ds_read_b64 v[154:155], v253 offset:152\n\t"
            "ds_read_b64 v[156:157], v253 offset:1800\n\t"
            "ds_read_b64 v[158:159], v253 offset:1896\n\t"
            "s_waitcnt lgkmcnt(10)\n\t"
            "v_smfmac_f32_16x16x64_bf16 v[180:183], v[172:175], v[160:167], v255\n\t"
            "ds_read_b128 v[172:175], v254 offset:448\n\t"
            "ds_read_b64 v[160:161], v253 offset:64\n\t"
            "v_cvt_pk_bf16_f32 v176, v176, v177\n\t"
            "v_cvt_pk_bf16_f32 v177, v178, v179\n\t"
            "ds_read_b64 v[162:163], v253 offset:160\n\t"
            "ds_read_b64 v[164:165], v253 offset:1808\n\t"
            "ds_read_b64 v[166:167], v253 offset:1904\n\t"
            "s_waitcnt lgkmcnt(14)\n\t"
            "ds_write_b128 v237, v[184:187]\n\t"
            "v_mov_b64 v[184:185], 0\n\t"
            "v_mov_b64 v[186:187], 0\n\t"
            "v_cvt_pk_bf16_f32 v178, v180, v181\n\t"
            "v_cvt_pk_bf16_f32 v179, v182, v183\n\t"
            "s_waitcnt lgkmcnt(10)\n\t"
            "v_smfmac_f32_16x16x64_bf16 v[184:187], v[168:171], v[144:151], v255\n\t"
            "ds_read_b128 v[168:171], v254 offset:512\n\t"
            "v_mov_b64 v[180:181], 0\n\t"
            "v_mov_b64 v[182:183], 0\n\t"
            "v_mfma_f32_4x4x4_16b_bf16 v[196:199], v[176:177], v[176:177], v[196:199]\n\t"
            "v_mfma_f32_4x4x4_16b_bf16 v[192:195], v[238:239], v[176:177], v[192:195]\n\t"
            "s_nop 0\n\t"
            "v_mfma_f32_4x4x4_16b_bf16 v[196:199], v[178:179], v[178:179], v[196:199]\n\t"
            "v_mfma_f32_4x4x4_16b_bf16 v[192:195], v[238:239], v[178:179], v[192:195]\n\t"
            "ds_read_b64 v[144:145], v253 offset:72\n\t"
            "s_waitcnt lgkmcnt(7)\n\t"
            "v_smfmac_f32_16x16x64_bf16 v[188:191], v[172:175], v[152:159], v255\n\t"
            "ds_read_b64 v[146:147], v253 offset:168\n\t"
            "v_cvt_pk_bf16_f32 v184, v184, v185\n\t"
            "v_cvt_pk_bf16_f32 v185, v186, v187\n\t"
            "ds_read_b64 v[148:149], v253 offset:1816\n\t"
            "ds_read_b64 v[150:151], v253 offset:1912\n\t"
            "ds_read_b128 v[172:175], v254 offset:576\n\t"
            "ds_read_b64 v[152:153], v253 offset:80\n\t"
            "ds_read_b64 v[154:155], v253 offset:176\n\t"
            "v_cvt_pk_bf16_f32 v186, v188, v189\n\t"
            "v_cvt_pk_bf16_f32 v187, v190, v191\n\t"
            "v_mov_b64 v[188:189], 0\n\t"
            "v_mov_b64 v[190:191], 0\n\t"
            "v_mfma_f32_4x4x4_16b_bf16 v[196:199], v[184:185], v[184:185], v[196:199]\n\t"
            "v_mfma_f32_4x4x4_16b_bf16 v[192:195], v[238:239], v[184:185], v[192:195]\n\t"
            "s_nop 0\n\t"
            "v_mfma_f32_4x4x4_16b_bf16 v[196:199], v[186:187], v[186:187], v[196:199]\n\t"
            "v_mfma_f32_4x4x4_16b_bf16 v[192:195], v[238:239], v[186:187], v[192:195]\n\t"
            "ds_read_b64 v[156:157], v253 offset:1824\n\t"
            "ds_read_b64 v[158:159], v253 offset:1920\n\t"
            "s_waitcnt lgkmcnt(14)\n\t"
            "ds_write_b128 v236, v[176:179] offset:128\n\t"
            "v_mov_b64 v[176:177], 0\n\t"
            "v_mov_b64 v[178:179], 0\n\t"
            "s_waitcnt lgkmcnt(10)\n\t"
            "s_nop 0\n\t"
            "v_smfmac_f32_16x16x64_bf16 v[176:179], v[168:171], v[160:167], v255\n\t"
            "ds_read_b128 v[168:171], v254 offset:640\n\t"
            "s_waitcnt lgkmcnt(6)\n\t"
            "v_smfmac_f32_16x16x64_bf16 v[180:183], v[172:175], v[144:151], v255\n\t"
            "ds_read_b64 v[160:161], v253 offset:88\n\t"
            "ds_read_b64 v[162:163], v253 offset:184\n\t"
            "ds_read_b64 v[164:165], v253 offset:1832\n\t"
            "ds_read_b64 v[166:167], v253 offset:1928\n\t"
            "ds_read_b128 v[172:175], v254 offset:704\n\t"
            "ds_write_b128 v237, v[184:187] offset:128\n\t"
            "v_mov_b64 v[184:185], 0\n\t"
            "v_mov_b64 v[186:187], 0\n\t"
            "v_cvt_pk_bf16_f32 v176, v176, v177\n\t"
            "v_cvt_pk_bf16_f32 v177, v178, v179\n\t"
            "v_cvt_pk_bf16_f32 v178, v180, v181\n\t"
            "v_cvt_pk_bf16_f32 v179, v182, v183\n\t"
            "s_nop 1\n\t"
            "v_mfma_f32_4x4x4_16b_bf16 v[196:199], v[176:177], v[176:177], v[196:199]\n\t"
            "v_mfma_f32_4x4x4_16b_bf16 v[192:195], v[238:239], v[176:177], v[192:195]\n\t"
            "s_nop 0\n\t"
            "v_mfma_f32_4x4x4_16b_bf16 v[196:199], v[178:179], v[178:179], v[196:199]\n\t"
            "v_mfma_f32_4x4x4_16b_bf16 v[192:195], v[238:239], v[178:179], v[192:195]\n\t"
            "ds_write_b128 v236, v[176:179] offset:256\n\t"
            "s_waitcnt lgkmcnt(7)\n\t"
            "v_smfmac_f32_16x16x64_bf16 v[184:187], v[168:171], v[152:159], v255\n\t"
            "s_waitcnt lgkmcnt(2)\n\t"
            "v_smfmac_f32_16x16x64_bf16 v[188:191], v[172:175], v[160:167], v255\n\t"
            "s_nop 5\n\t"
            "v_cvt_pk_bf16_f32 v184, v184, v185\n\t"
            "v_cvt_pk_bf16_f32 v185, v186, v187\n\t"
            "v_cvt_pk_bf16_f32 v186, v188, v189\n\t"
            "v_cvt_pk_bf16_f32 v187, v190, v191\n\t"
            "s_nop 1\n\t"
            "v_mfma_f32_4x4x4_16b_bf16 v[196:199], v[184:185], v[184:185], v[196:199]\n\t"
            "v_mfma_f32_4x4x4_16b_bf16 v[192:195], v[238:239], v[184:185], v[192:195]\n\t"
            "s_nop 0\n\t"
            "v_mfma_f32_4x4x4_16b_bf16 v[196:199], v[186:187], v[186:187], v[196:199]\n\t"
            "v_mfma_f32_4x4x4_16b_bf16 v[192:195], v[238:239], v[186:187], v[192:195]\n\t"
            "ds_write_b128 v237, v[184:187] offset:256\n\t"
            "s_nop 6\n\t"
            "v_cndmask_b32_e64 v193, v196, v197, s[60:61]\n\t"
            "v_cndmask_b32_e64 v193, v193, v198, s[62:63]\n\t"
            "v_cndmask_b32_e64 v193, v193, v199, s[64:65]\n\t"
            "ds_write_b64 v248, v[192:193] offset:18816\n\t"
            "s_waitcnt lgkmcnt(0)\n\t"
            "s_nop 7\n\t"
            "v_mov_b32 v144, s44\n\t"
            "v_mov_b32 v145, 0xbfffffff\n\t"
            "ds_write_b32 v144, v145\n\t"
            "s_setprio 1\n\t"
            "v_mov_b32 v144, s23\n\t"
            ".Lgo%=:\n\t"
            "ds_read_b32 v145, v144 offset:124\n\t"
            "s_waitcnt lgkmcnt(0)\n\t"
            "v_cmp_ne_u32 vcc, -1, v145\n\t"
            "s_nop 4\n\t"
            "s_cbranch_vccnz .Lgo%=\n\t"
            "s_nop 7\n\t"
            "ds_read_b32 v249, v252 offset:1024\n\t"
            "s_add_u32 s49, s70, 0\n\t"
            "buffer_load_dwordx4 v[0:3], v247, s[28:31], s49 offen sc1\n\t"
            "s_add_u32 s49, s70, 1024\n\t"
            "buffer_load_dwordx4 v[4:7], v247, s[28:31], s49 offen sc1\n\t"
            "s_add_u32 s49, s70, 2048\n\t"
            "buffer_load_dwordx4 v[8:11], v247, s[28:31], s49 offen sc1\n\t"
            "s_add_u32 s49, s70, 3072\n\t"
            "buffer_load_dwordx4 v[12:15], v247, s[28:31], s49 offen sc1\n\t"
            "s_add_u32 s49, s70, 4096\n\t"
            "buffer_load_dwordx4 v[16:19], v247, s[28:31], s49 offen sc1\n\t"
            "s_add_u32 s49, s70, 5120\n\t"
            "buffer_load_dwordx4 v[20:23], v247, s[28:31], s49 offen sc1\n\t"
            "s_add_u32 s49, s70, 6144\n\t"
            "buffer_load_dwordx4 v[24:27], v247, s[28:31], s49 offen sc1\n\t"
            "s_add_u32 s49, s70, 7168\n\t"
            "buffer_load_dwordx4 v[28:31], v247, s[28:31], s49 offen sc1\n\t"
            "s_add_u32 s49, s70, 8192\n\t"
            "buffer_load_dwordx4 v[32:35], v247, s[28:31], s49 offen sc1\n\t"
            "s_add_u32 s49, s70, 9216\n\t"
            "buffer_load_dwordx4 v[36:39], v247, s[28:31], s49 offen sc1\n\t"
            "s_add_u32 s49, s70, 10240\n\t"
            "buffer_load_dwordx4 v[40:43], v247, s[28:31], s49 offen sc1\n\t"
            "s_add_u32 s49, s70, 11264\n\t"
            "buffer_load_dwordx4 v[44:47], v247, s[28:31], s49 offen sc1\n\t"
            "s_add_u32 s49, s70, 12288\n\t"
            "buffer_load_dwordx4 v[48:51], v247, s[28:31], s49 offen sc1\n\t"
            "s_add_u32 s49, s70, 13312\n\t"
            "buffer_load_dwordx4 v[52:55], v247, s[28:31], s49 offen sc1\n\t"
            "s_add_u32 s49, s70, 14336\n\t"
            "buffer_load_dwordx4 v[56:59], v247, s[28:31], s49 offen sc1\n\t"
            "s_add_u32 s49, s70, 15360\n\t"
            "buffer_load_dwordx4 v[60:63], v247, s[28:31], s49 offen sc1\n\t"
            "s_add_u32 s49, s70, 16384\n\t"
            "buffer_load_dwordx4 v[64:67], v247, s[28:31], s49 offen sc1\n\t"
            "s_add_u32 s49, s70, 17408\n\t"
            "buffer_load_dwordx4 v[68:71], v247, s[28:31], s49 offen sc1\n\t"
            "s_waitcnt lgkmcnt(0)\n\t"
            "v_and_b32 v249, s69, v249\n\t"
            "s_mov_b32 exec_hi, 0x3fffff\n\t"
            "s_add_u32 m0, s66, 0\n\t"
            "s_add_u32 s49, s79, 0\n\t"
            "buffer_load_dwordx4 v249, s[72:75], s49 offen lds\n\t"
            "s_mov_b32 exec_hi, -1\n\t"
            "s_mov_b32 exec_hi, 0x3fffff\n\t"
            "s_add_u32 m0, s66, 872\n\t"
            "s_add_u32 s49, s68, 196224\n\t"
            "buffer_load_dwordx4 v249, s[72:75], s49 offen lds\n\t"
            "s_mov_b32 exec_hi, -1\n\t"
            "s_mov_b32 exec_hi, 0x3fffff\n\t"
            "s_add_u32 m0, s66, 1744\n\t"
            "s_add_u32 s49, s68, 392448\n\t"
            "buffer_load_dwordx4 v249, s[72:75], s49 offen lds\n\t"
            "s_mov_b32 exec_hi, -1\n\t"
            "s_mov_b32 exec_hi, 0x3fffff\n\t"
            "s_add_u32 m0, s66, 2616\n\t"
            "s_add_u32 s49, s68, 588672\n\t"
            "buffer_load_dwordx4 v249, s[72:75], s49 offen lds\n\t"
            "s_mov_b32 exec_hi, -1\n\t"
            "s_mov_b32 exec_hi, 0x3fffff\n\t"
            "s_add_u32 m0, s66, 3488\n\t"
            "s_add_u32 s49, s68, 784896\n\t"
            "buffer_load_dwordx4 v249, s[72:75], s49 offen lds\n\t"
            "s_mov_b32 exec_hi, -1\n\t"
            "s_mov_b32 exec_hi, 0x3fffff\n\t"
            "s_add_u32 m0, s66, 4360\n\t"
            "s_add_u32 s49, s68, 981120\n\t"
            "buffer_load_dwordx4 v249, s[72:75], s49 offen lds\n\t"
            "s_mov_b32 exec_hi, -1\n\t"
            "s_mov_b32 exec_hi, 0x3fffff\n\t"
            "s_add_u32 m0, s66, 5232\n\t"
            "s_add_u32 s49, s68, 1177344\n\t"
            "buffer_load_dwordx4 v249, s[72:75], s49 offen lds\n\t"
            "s_mov_b32 exec_hi, -1\n\t"
            "s_mov_b32 exec_hi, 0x3fffff\n\t"
            "s_add_u32 m0, s66, 6104\n\t"
            "s_add_u32 s49, s68, 1373568\n\t"
            "buffer_load_dwordx4 v249, s[72:75], s49 offen lds\n\t"
            "s_mov_b32 exec_hi, -1\n\t"
            "s_mov_b32 exec_hi, 0x3fffff\n\t"
            "s_add_u32 m0, s66, 6976\n\t"
            "s_add_u32 s49, s68, 1569792\n\t"
            "buffer_load_dwordx4 v249, s[72:75], s49 offen lds\n\t"
            "s_mov_b32 exec_hi, -1\n\t"
            "s_add_u32 s45, s46, s27\n\t"
            "s_mov_b32 s46, s47\n\t"
            "s_mov_b32 s47, s68\n\t"
            "s_xor_b32 s48, s48, s71\n\t"
            "s_xor_b32 s67, s67, s71\n\t"
            "s_add_u32 s76, s76, 4\n\t"
            "s_lshl_b32 s56, s56, 2\n\t"
            "s_lshr_b32 s49, s26, 30\n\t"
            "s_or_b32 s56, s56, s49\n\t"
            "s_nop 7\n\t"
            "v_mov_b32 v144, s23\n\t"
            ".Lpg%=:\n\t"
            "ds_read_b128 v[148:151], v144 offset:96\n\t"
            "s_waitcnt lgkmcnt(0)\n\t"
            "v_and_b32 v148, v148, v149\n\t"
            "v_and_b32 v150, v150, v151\n\t"
            "v_and_b32 v148, v148, v150\n\t"
            "v_cmp_le_i32 vcc, 0, v148\n\t"
            "s_nop 4\n\t"
            "s_cbranch_vccnz .Lpg%=\n\t"
            "s_setprio 3\n\t"
            "s_nop 7\n\t"
            "v_add_u32 v196, s22, v229\n\t"
            "v_xor_b32 v197, 64, v196\n\t"
            "v_add_u32 v198, s22, v247\n\t"
            "v_mov_b32 v249, s76\n\t"
            "v_add_u32 v236, s67, v250\n\t"
            "v_add_u32 v248, s67, v251\n\t"
            "ds_read_b128 v[144:147], v196\n\t"
            "ds_read_b128 v[148:151], v197\n\t"
            "ds_read_b128 v[152:155], v196 offset:128\n\t"
            "ds_read_b128 v[156:159], v197 offset:128\n\t"
            "ds_read_b128 v[160:163], v196 offset:256\n\t"
            "ds_read_b128 v[164:167], v197 offset:256\n\t"
            "ds_read_b128 v[216:219], v198 offset:12288\n\t"
            "ds_read_b128 v[220:223], v198 offset:13312\n\t"
            "v_add_u32 v198, s48, v247\n\t"
            "ds_read_b128 v[168:171], v196 offset:6144\n\t"
            "ds_read_b128 v[172:175], v197 offset:6144\n\t"
            "ds_read_b128 v[176:179], v196 offset:6272\n\t"
            "ds_read_b128 v[180:183], v197 offset:6272\n\t"
            "ds_read_b128 v[184:187], v196 offset:6400\n\t"
            "v_add_u32 v196, s48, v229\n\t"
            "ds_read_b128 v[188:191], v197 offset:6400\n\t"
            "s_waitcnt vmcnt(9)\n\t"
            "s_waitcnt lgkmcnt(13)\n\t"
            "v_mfma_f32_16x16x32_bf16 v[200:203], v[0:3], v[144:147], 0\n\t"
            "ds_read_b32 v249, v249\n\t"
            "v_xor_b32 v197, 64, v196\n\t"
            "s_waitcnt lgkmcnt(13)\n\t"
            "v_mfma_f32_16x16x32_bf16 v[200:203], v[4:7], v[148:151], v[200:203]\n\t"
            "s_add_u32 s49, s33, 18432\n\t"
            "buffer_load_dwordx4 v[72:75], v247, s[28:31], s49 offen sc1\n\t"
            "s_waitcnt lgkmcnt(12)\n\t"
            "v_mfma_f32_16x16x32_bf16 v[200:203], v[8:11], v[152:155], v[200:203]\n\t"
            "s_waitcnt lgkmcnt(8)\n\t"
            "v_add_f32 v216, v216, v218\n\t"
            "s_waitcnt lgkmcnt(7)\n\t"
            "v_add_f32 v220, v220, v222\n\t"
            "v_mfma_f32_16x16x32_bf16 v[200:203], v[12:15], v[156:159], v[200:203]\n\t"
            "v_add_f32 v216, v216, v220\n\t"
            "v_add_f32 v217, v217, v219\n\t"
            "v_mfma_f32_16x16x32_bf16 v[200:203], v[16:19], v[160:163], v[200:203]\n\t"
            "v_add_f32 v221, v221, v223\n\t"
            "v_mul_f32 v216, 0x3baaaaab, v216\n\t"
            "v_mfma_f32_16x16x32_bf16 v[200:203], v[20:23], v[164:167], v[200:203]\n\t"
            "v_add_f32 v217, v217, v221\n\t"
            "v_mul_f32_e64 v216, v216, -v216\n\t"
            "v_mfma_f32_16x16x32_bf16 v[204:207], v[24:27], v[144:147], 0\n\t"
            "v_fmac_f32 v216, 0x3baaaaab, v217\n\t"
            "v_max_f32 v216, 0, v216\n\t"
            "v_mfma_f32_16x16x32_bf16 v[204:207], v[28:31], v[148:151], v[204:207]\n\t"
            "v_add_f32 v216, 0x3727c5ac, v216\n\t"
            "v_rsq_f32 v216, v216\n\t"
            "v_mfma_f32_16x16x32_bf16 v[204:207], v[32:35], v[152:155], v[204:207]\n\t"
            "ds_write_b32 v232, v216\n\t"
            "v_mfma_f32_16x16x32_bf16 v[204:207], v[36:39], v[156:159], v[204:207]\n\t"
            "ds_read_b32 v192, v234\n\t"
            "s_waitcnt lgkmcnt(2)\n\t"
            "v_readfirstlane_b32 s26, v249\n\t"
            "v_mfma_f32_16x16x32_bf16 v[204:207], v[40:43], v[160:163], v[204:207]\n\t"
            "ds_read_b32 v193, v234 offset:64\n\t"
            "v_mfma_f32_16x16x32_bf16 v[204:207], v[44:47], v[164:167], v[204:207]\n\t"
            "ds_read_b32 v249, v252 offset:1024\n\t"
            "v_mfma_f32_16x16x32_bf16 v[208:211], v[48:51], v[144:147], 0\n\t"
            "ds_read_b128 v[144:147], v196\n\t"
            "v_mfma_f32_16x16x32_bf16 v[208:211], v[52:55], v[148:151], v[208:211]\n\t"
            "ds_read_b128 v[148:151], v197\n\t"
            "v_mfma_f32_16x16x32_bf16 v[208:211], v[56:59], v[152:155], v[208:211]\n\t"
            "ds_read_b128 v[152:155], v196 offset:128\n\t"
            "v_mfma_f32_16x16x32_bf16 v[208:211], v[60:63], v[156:159], v[208:211]\n\t"
            "ds_read_b128 v[156:159], v197 offset:128\n\t"
            "v_mfma_f32_16x16x32_bf16 v[208:211], v[64:67], v[160:163], v[208:211]\n\t"
            "ds_read_b128 v[160:163], v196 offset:256\n\t"
            "v_mfma_f32_16x16x32_bf16 v[208:211], v[68:71], v[164:167], v[208:211]\n\t"
            "ds_read_b128 v[164:167], v197 offset:256\n\t"
            "s_waitcnt lgkmcnt(8)\n\t"
            "v_mul_f32 v200, v192, v200\n\t"
            "v_mfma_f32_16x16x32_bf16 v[212:215], v[0:3], v[168:171], 0\n\t"
            "v_mul_f32 v201, v192, v201\n\t"
            "v_mul_f32 v204, v192, v204\n\t"
            "v_mfma_f32_16x16x32_bf16 v[212:215], v[4:7], v[172:175], v[212:215]\n\t"
            "v_mul_f32 v205, v192, v205\n\t"
            "v_mul_f32 v202, v192, v202\n\t"
            "v_mfma_f32_16x16x32_bf16 v[212:215], v[8:11], v[176:179], v[212:215]\n\t"
            "v_mul_f32 v203, v192, v203\n\t"
            "v_cvt_pk_bf16_f32 v200, v200, v201\n\t"
            "v_mfma_f32_16x16x32_bf16 v[212:215], v[12:15], v[180:183], v[212:215]\n\t"
            "v_mul_f32 v206, v192, v206\n\t"
            "v_mul_f32 v207, v192, v207\n\t"
            "v_mfma_f32_16x16x32_bf16 v[212:215], v[16:19], v[184:187], v[212:215]\n\t"
            "v_cvt_pk_bf16_f32 v204, v204, v205\n\t"
            "v_cvt_pk_bf16_f32 v201, v202, v203\n\t"
            "v_mfma_f32_16x16x32_bf16 v[212:215], v[20:23], v[188:191], v[212:215]\n\t"
            "v_cvt_pk_bf16_f32 v205, v206, v207\n\t"
            "v_mul_f32 v208, v192, v208\n\t"
            "v_mfma_f32_16x16x32_bf16 v[216:219], v[24:27], v[168:171], 0\n\t"
            "ds_write_b64 v227, v[200:201]\n\t"
            "v_mfma_f32_16x16x32_bf16 v[216:219], v[28:31], v[172:175], v[216:219]\n\t"
            "ds_read_b128 v[200:203], v198 offset:18816\n\t"
            "v_mul_f32 v209, v192, v209\n\t"
            "v_mfma_f32_16x16x32_bf16 v[216:219], v[32:35], v[176:179], v[216:219]\n\t"
            "ds_write_b64 v227, v[204:205] offset:128\n\t"
            "v_mfma_f32_16x16x32_bf16 v[216:219], v[36:39], v[180:183], v[216:219]\n\t"
            "ds_read_b128 v[204:207], v198 offset:19840\n\t"
            "v_mul_f32 v210, v192, v210\n\t"
            "v_mfma_f32_16x16x32_bf16 v[216:219], v[40:43], v[184:187], v[216:219]\n\t"
            "s_add_u32 s49, s33, 19456\n\t"
            "buffer_load_dwordx4 v[76:79], v247, s[28:31], s49 offen sc1\n\t"
            "v_mfma_f32_16x16x32_bf16 v[216:219], v[44:47], v[188:191], v[216:219]\n\t"
            "s_add_u32 s49, s33, 20480\n\t"
            "buffer_load_dwordx4 v[80:83], v247, s[28:31], s49 offen sc1\n\t"
            "v_mfma_f32_16x16x32_bf16 v[220:223], v[48:51], v[168:171], 0\n\t"
            "ds_read_b128 v[168:171], v196 offset:6144\n\t"
            "s_waitcnt lgkmcnt(3)\n\t"
            "v_add_f32 v200, v200, v202\n\t"
            "v_mfma_f32_16x16x32_bf16 v[220:223], v[52:55], v[172:175], v[220:223]\n\t"
            "ds_read_b128 v[172:175], v197 offset:6144\n\t"
            "s_waitcnt lgkmcnt(2)\n\t"
            "v_add_f32 v204, v204, v206\n\t"
            "v_mfma_f32_16x16x32_bf16 v[220:223], v[56:59], v[176:179], v[220:223]\n\t"
            "ds_read_b128 v[176:179], v196 offset:6272\n\t"
            "v_add_f32 v200, v200, v204\n\t"
            "v_mfma_f32_16x16x32_bf16 v[220:223], v[60:63], v[180:183], v[220:223]\n\t"
            "ds_read_b128 v[180:183], v197 offset:6272\n\t"
            "v_add_f32 v201, v201, v203\n\t"
            "v_mfma_f32_16x16x32_bf16 v[220:223], v[64:67], v[184:187], v[220:223]\n\t"
            "ds_read_b128 v[184:187], v196 offset:6400\n\t"
            "v_add_f32 v205, v205, v207\n\t"
            "v_mfma_f32_16x16x32_bf16 v[220:223], v[68:71], v[188:191], v[220:223]\n\t"
            "ds_read_b128 v[188:191], v197 offset:6400\n\t"
            "v_mul_f32 v200, 0x3baaaaab, v200\n\t"
            "v_add_f32 v201, v201, v205\n\t"
            "v_mul_f32_e64 v200, v200, -v200\n\t"
            "v_fmac_f32 v200, 0x3baaaaab, v201\n\t"
            "v_max_f32 v200, 0, v200\n\t"
            "v_add_f32 v200, 0x3727c5ac, v200\n\t"
            "v_rsq_f32 v200, v200\n\t"
            "v_mul_f32 v211, v192, v211\n\t"
            "v_cvt_pk_bf16_f32 v208, v208, v209\n\t"
            "v_cvt_pk_bf16_f32 v209, v210, v211\n\t"
            "v_mul_f32 v212, v193, v212\n\t"
            "v_mul_f32 v213, v193, v213\n\t"
            "v_mul_f32 v214, v193, v214\n\t"
            "v_mul_f32 v215, v193, v215\n\t"
            "v_cvt_pk_bf16_f32 v212, v212, v213\n\t"
            "v_cvt_pk_bf16_f32 v213, v214, v215\n\t"
            "v_mul_f32 v216, v193, v216\n\t"
            "v_mul_f32 v217, v193, v217\n\t"
            "v_mul_f32 v218, v193, v218\n\t"
            "v_mul_f32 v219, v193, v219\n\t"
            "v_mul_f32 v220, v193, v220\n\t"
            "v_mul_f32 v221, v193, v221\n\t"
            "v_mul_f32 v222, v193, v222\n\t"
            "v_mul_f32 v223, v193, v223\n\t"
            "v_cvt_pk_bf16_f32 v216, v216, v217\n\t"
            "v_cvt_pk_bf16_f32 v217, v218, v219\n\t"
            "v_cvt_pk_bf16_f32 v220, v220, v221\n\t"
            "v_cvt_pk_bf16_f32 v221, v222, v223\n\t"
            "ds_write_b32 v232, v200\n\t"
            "v_mfma_f32_16x16x32_bf16 v[200:203], v[144:147], v[0:3], 0\n\t"
            "ds_read_b128 v[192:195], v233\n\t"
            "v_mfma_f32_16x16x32_bf16 v[200:203], v[148:151], v[4:7], v[200:203]\n\t"
            "ds_write_b64 v227, v[208:209] offset:256\n\t"
            "v_mfma_f32_16x16x32_bf16 v[200:203], v[152:155], v[8:11], v[200:203]\n\t"
            "s_add_u32 s49, s33, 21504\n\t"
            "buffer_load_dwordx4 v[84:87], v247, s[28:31], s49 offen sc1\n\t"
            "v_mfma_f32_16x16x32_bf16 v[200:203], v[156:159], v[12:15], v[200:203]\n\t"
            "s_add_u32 s49, s33, 22528\n\t"
            "buffer_load_dwordx4 v[88:91], v247, s[28:31], s49 offen sc1\n\t"
            "v_mfma_f32_16x16x32_bf16 v[200:203], v[160:163], v[16:19], v[200:203]\n\t"
            "ds_write_b64 v228, v[212:213]\n\t"
            "v_mfma_f32_16x16x32_bf16 v[200:203], v[164:167], v[20:23], v[200:203]\n\t"
            "s_add_u32 s49, s33, 23552\n\t"
            "buffer_load_dwordx4 v[92:95], v247, s[28:31], s49 offen sc1\n\t"
            "v_mfma_f32_16x16x32_bf16 v[204:207], v[144:147], v[24:27], 0\n\t"
            "s_add_u32 s49, s33, 24576\n\t"
            "buffer_load_dwordx4 v[96:99], v247, s[28:31], s49 offen sc1\n\t"
            "v_mfma_f32_16x16x32_bf16 v[204:207], v[148:151], v[28:31], v[204:207]\n\t"
            "s_add_u32 s49, s33, 25600\n\t"
            "buffer_load_dwordx4 v[100:103], v247, s[28:31], s49 offen sc1\n\t"
            "v_mfma_f32_16x16x32_bf16 v[204:207], v[152:155], v[32:35], v[204:207]\n\t"
            "ds_write_b64 v228, v[216:217] offset:128\n\t"
            "v_mfma_f32_16x16x32_bf16 v[204:207], v[156:159], v[36:39], v[204:207]\n\t"
            "s_add_u32 s49, s33, 26624\n\t"
            "buffer_load_dwordx4 v[104:107], v247, s[28:31], s49 offen sc1\n\t"
            "v_mfma_f32_16x16x32_bf16 v[204:207], v[160:163], v[40:43], v[204:207]\n\t"
            "s_waitcnt lgkmcnt(3)\n\t"
            "v_mul_f32 v200, v192, v200\n\t"
            "v_mul_f32 v201, v193, v201\n\t"
            "v_mfma_f32_16x16x32_bf16 v[204:207], v[164:167], v[44:47], v[204:207]\n\t"
            "v_mul_f32 v202, v194, v202\n\t"
            "v_mul_f32 v203, v195, v203\n\t"
            "v_mfma_f32_16x16x32_bf16 v[208:211], v[144:147], v[48:51], 0\n\t"
            "ds_read_b128 v[144:147], v196 offset:12288\n\t"
            "v_cvt_pk_bf16_f32 v200, v200, v201\n\t"
            "v_mfma_f32_16x16x32_bf16 v[208:211], v[148:151], v[52:55], v[208:211]\n\t"
            "ds_read_b128 v[148:151], v197 offset:12288\n\t"
            "v_cvt_pk_bf16_f32 v201, v202, v203\n\t"
            "v_mfma_f32_16x16x32_bf16 v[208:211], v[152:155], v[56:59], v[208:211]\n\t"
            "ds_read_b128 v[152:155], v196 offset:12416\n\t"
            "v_mul_f32 v204, v192, v204\n\t"
            "v_mfma_f32_16x16x32_bf16 v[208:211], v[156:159], v[60:63], v[208:211]\n\t"
            "ds_read_b128 v[156:159], v197 offset:12416\n\t"
            "v_mul_f32 v205, v193, v205\n\t"
            "v_mfma_f32_16x16x32_bf16 v[208:211], v[160:163], v[64:67], v[208:211]\n\t"
            "ds_read_b128 v[160:163], v196 offset:12544\n\t"
            "v_mul_f32 v206, v194, v206\n\t"
            "v_mfma_f32_16x16x32_bf16 v[208:211], v[164:167], v[68:71], v[208:211]\n\t"
            "ds_read_b128 v[164:167], v197 offset:12544\n\t"
            "v_mul_f32 v207, v195, v207\n\t"
            "v_mfma_f32_16x16x32_bf16 v[212:215], v[168:171], v[0:3], 0\n\t"
            "s_add_u32 s49, s33, 27648\n\t"
            "buffer_load_dwordx4 v[108:111], v247, s[28:31], s49 offen sc1\n\t"
            "v_mfma_f32_16x16x32_bf16 v[212:215], v[172:175], v[4:7], v[212:215]\n\t"
            "ds_write_b64 v228, v[220:221] offset:256\n\t"
            "v_mfma_f32_16x16x32_bf16 v[212:215], v[176:179], v[8:11], v[212:215]\n\t"
            "v_mul_f32 v208, v192, v208\n\t"
            "v_mul_f32 v209, v193, v209\n\t"
            "v_mfma_f32_16x16x32_bf16 v[212:215], v[180:183], v[12:15], v[212:215]\n\t"
            "v_mul_f32 v210, v194, v210\n\t"
            "v_mul_f32 v211, v195, v211\n\t"
            "v_mfma_f32_16x16x32_bf16 v[212:215], v[184:187], v[16:19], v[212:215]\n\t"
            "ds_read_b128 v[192:195], v233 offset:64\n\t"
            "v_cvt_pk_bf16_f32 v204, v204, v205\n\t"
            "v_mfma_f32_16x16x32_bf16 v[212:215], v[188:191], v[20:23], v[212:215]\n\t"
            "s_add_u32 s49, s33, 28672\n\t"
            "buffer_load_dwordx4 v[112:115], v247, s[28:31], s49 offen sc1\n\t"
            "v_mfma_f32_16x16x32_bf16 v[216:219], v[168:171], v[24:27], 0\n\t"
            "s_add_u32 s49, s33, 29696\n\t"
            "buffer_load_dwordx4 v[116:119], v247, s[28:31], s49 offen sc1\n\t"
            "v_mfma_f32_16x16x32_bf16 v[216:219], v[172:175], v[28:31], v[216:219]\n\t"
            "s_add_u32 s49, s33, 30720\n\t"
            "buffer_load_dwordx4 v[120:123], v247, s[28:31], s49 offen sc1\n\t"
            "v_mfma_f32_16x16x32_bf16 v[216:219], v[176:179], v[32:35], v[216:219]\n\t"
            "s_waitcnt lgkmcnt(0)\n\t"
            "v_mul_f32 v212, v192, v212\n\t"
            "v_mul_f32 v213, v193, v213\n\t"
            "v_mfma_f32_16x16x32_bf16 v[216:219], v[180:183], v[36:39], v[216:219]\n\t"
            "v_mul_f32 v214, v194, v214\n\t"
            "v_mul_f32 v215, v195, v215\n\t"
            "v_mfma_f32_16x16x32_bf16 v[216:219], v[184:187], v[40:43], v[216:219]\n\t"
            "v_cvt_pk_bf16_f32 v202, v212, v213\n\t"
            "v_cvt_pk_bf16_f32 v203, v214, v215\n\t"
            "v_mfma_f32_16x16x32_bf16 v[216:219], v[188:191], v[44:47], v[216:219]\n\t"
            "s_add_u32 s49, s33, 31744\n\t"
            "buffer_load_dwordx4 v[124:127], v247, s[28:31], s49 offen sc1\n\t"
            "v_mfma_f32_16x16x32_bf16 v[220:223], v[168:171], v[48:51], 0\n\t"
            "ds_read_b128 v[168:171], v254\n\t"
            "v_cvt_pk_bf16_f32 v205, v206, v207\n\t"
            "v_mfma_f32_16x16x32_bf16 v[220:223], v[172:175], v[52:55], v[220:223]\n\t"
            "ds_read_b128 v[172:175], v254 offset:64\n\t"
            "v_cvt_pk_bf16_f32 v208, v208, v209\n\t"
            "v_mfma_f32_16x16x32_bf16 v[220:223], v[176:179], v[56:59], v[220:223]\n\t"
            "s_add_u32 s49, s33, 32768\n\t"
            "buffer_load_dwordx4 v[128:131], v247, s[28:31], s49 offen sc1\n\t"
            "v_mfma_f32_16x16x32_bf16 v[220:223], v[180:183], v[60:63], v[220:223]\n\t"
            "v_mul_f32 v216, v192, v216\n\t"
            "v_mul_f32 v217, v193, v217\n\t"
            "v_mfma_f32_16x16x32_bf16 v[220:223], v[184:187], v[64:67], v[220:223]\n\t"
            "v_mul_f32 v218, v194, v218\n\t"
            "v_mul_f32 v219, v195, v219\n\t"
            "v_mfma_f32_16x16x32_bf16 v[220:223], v[188:191], v[68:71], v[220:223]\n\t"
            "v_cvt_pk_bf16_f32 v206, v216, v217\n\t"
            "v_cvt_pk_bf16_f32 v207, v218, v219\n\t"
            "v_mfma_f32_16x16x32_bf16 v[212:215], v[144:147], v[0:3], 0\n\t"
            "s_add_u32 s49, s33, 33792\n\t"
            "buffer_load_dwordx4 v[132:135], v247, s[28:31], s49 offen sc1\n\t"
            "v_mfma_f32_16x16x32_bf16 v[212:215], v[148:151], v[4:7], v[212:215]\n\t"
            "s_add_u32 s49, s33, 34816\n\t"
            "buffer_load_dwordx4 v[136:139], v247, s[28:31], s49 offen sc1\n\t"
            "v_mfma_f32_16x16x32_bf16 v[212:215], v[152:155], v[8:11], v[212:215]\n\t"
            "v_cvt_pk_bf16_f32 v209, v210, v211\n\t"
            "v_mul_f32 v220, v192, v220\n\t"
            "v_mfma_f32_16x16x32_bf16 v[212:215], v[156:159], v[12:15], v[212:215]\n\t"
            "v_mul_f32 v221, v193, v221\n\t"
            "v_mul_f32 v222, v194, v222\n\t"
            "v_mfma_f32_16x16x32_bf16 v[212:215], v[160:163], v[16:19], v[212:215]\n\t"
            "v_mul_f32 v223, v195, v223\n\t"
            "v_cvt_pk_bf16_f32 v210, v220, v221\n\t"
            "v_mfma_f32_16x16x32_bf16 v[212:215], v[164:167], v[20:23], v[212:215]\n\t"
            "ds_read_b128 v[192:195], v233 offset:128\n\t"
            "v_cvt_pk_bf16_f32 v211, v222, v223\n\t"
            "v_mfma_f32_16x16x32_bf16 v[216:219], v[144:147], v[24:27], 0\n\t"
            "s_add_u32 s49, s33, 35840\n\t"
            "buffer_load_dwordx4 v[140:143], v247, s[28:31], s49 offen sc1\n\t"
            "v_mfma_f32_16x16x32_bf16 v[216:219], v[148:151], v[28:31], v[216:219]\n\t"
            "s_and_b32 s68, s26, 0x7ffffff\n\t"
            "s_add_u32 s68, s68, s78\n\t"
            "s_and_b32 s69, s26, 0x38000000\n\t"
            "s_or_b32 s69, s69, s39\n\t"
            "s_bitcmp1_b32 s26, 27\n\t"
            "s_cselect_b32 s79, 2.0, s68\n\t"
            "v_mfma_f32_16x16x32_bf16 v[216:219], v[152:155], v[32:35], v[216:219]\n\t"
            "s_and_b32 s57, s56, 3\n\t"
            "s_lshl_b32 s58, s57, 3\n\t"
            "s_lshr_b32 m0, s54, s58\n\t"
            "v_mfma_f32_16x16x32_bf16 v[216:219], v[156:159], v[36:39], v[216:219]\n\t"
            "s_lshl_b32 s58, s57, 4\n\t"
            "s_lshr_b64 s[36:37], s[50:51], s58\n\t"
            "s_and_b32 s57, s36, 0xffff\n\t"
            "s_add_u32 s57, s57, s67\n\t"
            "v_mfma_f32_16x16x32_bf16 v[216:219], v[160:163], v[40:43], v[216:219]\n\t"
            "s_lshr_b64 s[36:37], s[52:53], s58\n\t"
            "s_and_b32 s58, s36, 0xffff\n\t"
            "s_add_u32 s58, s58, s67\n\t"
            "v_mfma_f32_16x16x32_bf16 v[216:219], v[164:167], v[44:47], v[216:219]\n\t"
            "s_and_b32 s59, s50, 0xffff\n\t"
            "s_add_u32 s59, s59, s67\n\t"
            "s_and_b32 s38, s52, 0xffff\n\t"
            "s_add_u32 s38, s38, s67\n\t"
            "v_mfma_f32_16x16x32_bf16 v[220:223], v[144:147], v[48:51], 0\n\t"
            "s_waitcnt vmcnt(18)\n\t"
            "ds_read_b64 v[144:145], v253\n\t"
            "v_mov_b64 v[176:177], 0\n\t"
            "v_mfma_f32_16x16x32_bf16 v[220:223], v[148:151], v[52:55], v[220:223]\n\t"
            "ds_read_b64 v[146:147], v253 offset:96\n\t"
            "v_mov_b64 v[178:179], 0\n\t"
            "v_mfma_f32_16x16x32_bf16 v[220:223], v[152:155], v[56:59], v[220:223]\n\t"
            "ds_read_b64 v[148:149], v253 offset:1744\n\t"
            "v_mov_b64 v[180:181], 0\n\t"
            "v_mfma_f32_16x16x32_bf16 v[220:223], v[156:159], v[60:63], v[220:223]\n\t"
            "ds_read_b64 v[150:151], v253 offset:1840\n\t"
            "v_mov_b64 v[182:183], 0\n\t"
            "v_mfma_f32_16x16x32_bf16 v[220:223], v[160:163], v[64:67], v[220:223]\n\t"
            "ds_read_b64 v[152:153], v253 offset:8\n\t"
            "v_mov_b64 v[184:185], 0\n\t"
            "v_mfma_f32_16x16x32_bf16 v[220:223], v[164:167], v[68:71], v[220:223]\n\t"
            "ds_read_b64 v[154:155], v253 offset:104\n\t"
            "v_mov_b64 v[186:187], 0\n\t"
            "v_mov_b64 v[188:189], 0\n\t"
            "v_mov_b64 v[190:191], 0\n\t"
            "s_waitcnt lgkmcnt(6)\n\t"
            "v_mul_f32 v212, v192, v212\n\t"
            "v_mul_f32 v213, v193, v213\n\t"
            "v_mul_f32 v214, v194, v214\n\t"
            "v_mul_f32 v215, v195, v215\n\t"
            "v_mul_f32 v216, v192, v216\n\t"
            "v_mul_f32 v217, v193, v217\n\t"
            "v_mul_f32 v218, v194, v218\n\t"
            "v_mul_f32 v219, v195, v219\n\t"
            "v_mul_f32 v220, v192, v220\n\t"
            "v_mul_f32 v221, v193, v221\n\t"
            "v_mul_f32 v222, v194, v222\n\t"
            "v_mul_f32 v223, v195, v223\n\t"
            "v_writelane_b32 v236, s59, m0\n\t"
            "v_writelane_b32 v236, s57, 63\n\t"
            "v_xor_b32 v237, 16, v236\n\t"
            "v_writelane_b32 v248, s38, m0\n\t"
            "v_and_b32 v249, s69, v249\n\t"
            "v_cvt_pk_bf16_f32 v212, v212, v213\n\t"
            "v_cvt_pk_bf16_f32 v216, v216, v217\n\t"
            "s_waitcnt lgkmcnt(2)\n\t"
            "v_smfmac_f32_16x16x64_bf16 v[176:179], v[168:171], v[144:151], v255\n\t"
            "ds_read_b64 v[156:157], v253 offset:1752\n\t"
            "v_cvt_pk_bf16_f32 v220, v220, v221\n\t"
            "v_cvt_pk_bf16_f32 v213, v214, v215\n\t"
            "v_cvt_pk_bf16_f32 v217, v218, v219\n\t"
            "v_cvt_pk_bf16_f32 v221, v222, v223\n\t"
            "v_writelane_b32 v248, s58, 63\n\t"
            "ds_read_b64 v[158:159], v253 offset:1848\n\t"
            "ds_read_b64 v[160:161], v253 offset:16\n\t"
            "v_cvt_pk_bf16_f32 v176, v176, v177\n\t"
            "v_cvt_pk_bf16_f32 v177, v178, v179\n\t"
            "ds_read_b64 v[162:163], v253 offset:112\n\t"
            "ds_read_b64 v[164:165], v253 offset:1760\n\t"
            "ds_read_b64 v[166:167], v253 offset:1856\n\t"
            "ds_read_b128 v[168:171], v254 offset:128\n\t"
            "ds_read_b64 v[144:145], v253 offset:24\n\t"
            "ds_read_b64 v[146:147], v253 offset:120\n\t"
            "ds_read_b64 v[148:149], v253 offset:1768\n\t"
            "ds_read_b64 v[150:151], v253 offset:1864\n\t"
            "ds_write_b128 v230, v[200:203]\n\t"
            "ds_write_b128 v230, v[204:207] offset:1536\n\t"
            "s_waitcnt lgkmcnt(11)\n\t"
            "v_smfmac_f32_16x16x64_bf16 v[180:183], v[172:175], v[152:159], v255\n\t"
            "ds_read_b128 v[172:175], v254 offset:192\n\t"
            "ds_read_b64 v[152:153], v253 offset:32\n\t"
            "ds_read_b64 v[154:155], v253 offset:128\n\t"
            "ds_read_b64 v[156:157], v253 offset:1776\n\t"
            "s_waitcnt lgkmcnt(10)\n\t"
            "v_smfmac_f32_16x16x64_bf16 v[184:187], v[168:171], v[160:167], v255\n\t"
            "ds_read_b64 v[158:159], v253 offset:1872\n\t"
            "ds_read_b128 v[168:171], v254 offset:256\n\t"
            "ds_read_b64 v[160:161], v253 offset:40\n\t"
            "v_cvt_pk_bf16_f32 v178, v180, v181\n\t"
            "v_cvt_pk_bf16_f32 v179, v182, v183\n\t"
            "v_mov_b64 v[180:181], 0\n\t"
            "v_mov_b64 v[182:183], 0\n\t"
            "s_nop 0\n\t"
            "v_cvt_pk_bf16_f32 v184, v184, v185\n\t"
            "v_cvt_pk_bf16_f32 v185, v186, v187\n\t"
            "ds_read_b64 v[162:163], v253 offset:136\n\t"
            "ds_read_b64 v[164:165], v253 offset:1784\n\t"
            "s_waitcnt lgkmcnt(14)\n\t"
            "ds_read_b64 v[166:167], v253 offset:1880\n\t"
            "s_waitcnt lgkmcnt(9)\n\t"
            "v_smfmac_f32_16x16x64_bf16 v[188:191], v[172:175], v[144:151], v255\n\t"
            "ds_read_b128 v[172:175], v254 offset:320\n\t"
            "ds_read_b64 v[144:145], v253 offset:48\n\t"
            "v_mfma_f32_4x4x4_16b_bf16 v[196:199], v[176:177], v[176:177], 0\n\t"
            "v_mfma_f32_4x4x4_16b_bf16 v[192:195], v[238:239], v[176:177], 0\n\t"
            "s_nop 0\n\t"
            "v_mfma_f32_4x4x4_16b_bf16 v[196:199], v[178:179], v[178:179], v[196:199]\n\t"
            "v_mfma_f32_4x4x4_16b_bf16 v[192:195], v[238:239], v[178:179], v[192:195]\n\t"
            "ds_read_b64 v[146:147], v253 offset:144\n\t"
            "v_cvt_pk_bf16_f32 v186, v188, v189\n\t"
            "v_cvt_pk_bf16_f32 v187, v190, v191\n\t"
            "v_mov_b64 v[188:189], 0\n\t"
            "v_mov_b64 v[190:191], 0\n\t"
            "ds_read_b64 v[148:149], v253 offset:1792\n\t"
            "ds_read_b64 v[150:151], v253 offset:1888\n\t"
            "ds_write_b128 v236, v[176:179]\n\t"
            "v_mov_b64 v[176:177], 0\n\t"
            "v_mov_b64 v[178:179], 0\n\t"
            "s_waitcnt lgkmcnt(10)\n\t"
            "s_nop 0\n\t"
            "v_smfmac_f32_16x16x64_bf16 v[176:179], v[168:171], v[152:159], v255\n\t"
            "ds_read_b128 v[168:171], v254 offset:384\n\t"
            "s_waitcnt lgkmcnt(6)\n\t"
            "v_smfmac_f32_16x16x64_bf16 v[180:183], v[172:175], v[160:167], v255\n\t"
            "ds_read_b64 v[152:153], v253 offset:56\n\t"
            "ds_read_b64 v[154:155], v253 offset:152\n\t"
            "v_mfma_f32_4x4x4_16b_bf16 v[196:199], v[184:185], v[184:185], v[196:199]\n\t"
            "v_mfma_f32_4x4x4_16b_bf16 v[192:195], v[238:239], v[184:185], v[192:195]\n\t"
            "s_nop 0\n\t"
            "v_mfma_f32_4x4x4_16b_bf16 v[196:199], v[186:187], v[186:187], v[196:199]\n\t"
            "v_mfma_f32_4x4x4_16b_bf16 v[192:195], v[238:239], v[186:187], v[192:195]\n\t"
            "ds_read_b64 v[156:157], v253 offset:1800\n\t"
            "v_cvt_pk_bf16_f32 v176, v176, v177\n\t"
            "v_cvt_pk_bf16_f32 v177, v178, v179\n\t"
            "v_cvt_pk_bf16_f32 v178, v180, v181\n\t"
            "v_cvt_pk_bf16_f32 v179, v182, v183\n\t"
            "v_mov_b64 v[180:181], 0\n\t"
            "v_mov_b64 v[182:183], 0\n\t"
            "ds_read_b64 v[158:159], v253 offset:1896\n\t"
            "ds_read_b128 v[172:175], v254 offset:448\n\t"
            "ds_read_b64 v[160:161], v253 offset:64\n\t"
            "ds_read_b64 v[162:163], v253 offset:160\n\t"
            "ds_read_b64 v[164:165], v253 offset:1808\n\t"
            "ds_read_b64 v[166:167], v253 offset:1904\n\t"
            "s_waitcnt lgkmcnt(14)\n\t"
            "ds_write_b128 v237, v[184:187]\n\t"
            "v_mov_b64 v[184:185], 0\n\t"
            "v_mov_b64 v[186:187], 0\n\t"
            "s_waitcnt lgkmcnt(10)\n\t"
            "s_nop 0\n\t"
            "v_smfmac_f32_16x16x64_bf16 v[184:187], v[168:171], v[144:151], v255\n\t"
            "ds_read_b128 v[168:171], v254 offset:512\n\t"
            "ds_read_b64 v[144:145], v253 offset:72\n\t"
            "ds_read_b64 v[146:147], v253 offset:168\n\t"
            "ds_read_b64 v[148:149], v253 offset:1816\n\t"
            "ds_read_b64 v[150:151], v253 offset:1912\n\t"
            "s_waitcnt lgkmcnt(10)\n\t"
            "v_smfmac_f32_16x16x64_bf16 v[188:191], v[172:175], v[152:159], v255\n\t"
            "ds_read_b128 v[172:175], v254 offset:576\n\t"
            "ds_read_b64 v[152:153], v253 offset:80\n\t"
            "v_mfma_f32_4x4x4_16b_bf16 v[196:199], v[176:177], v[176:177], v[196:199]\n\t"
            "v_mfma_f32_4x4x4_16b_bf16 v[192:195], v[238:239], v[176:177], v[192:195]\n\t"
            "s_nop 0\n\t"
            "v_mfma_f32_4x4x4_16b_bf16 v[196:199], v[178:179], v[178:179], v[196:199]\n\t"
            "v_mfma_f32_4x4x4_16b_bf16 v[192:195], v[238:239], v[178:179], v[192:195]\n\t"
            "ds_read_b64 v[154:155], v253 offset:176\n\t"
            "v_cvt_pk_bf16_f32 v184, v184, v185\n\t"
            "v_cvt_pk_bf16_f32 v185, v186, v187\n\t"
            "v_cvt_pk_bf16_f32 v186, v188, v189\n\t"
            "v_cvt_pk_bf16_f32 v187, v190, v191\n\t"
            "v_mov_b64 v[188:189], 0\n\t"
            "v_mov_b64 v[190:191], 0\n\t"
            "ds_read_b64 v[156:157], v253 offset:1824\n\t"
            "ds_read_b64 v[158:159], v253 offset:1920\n\t"
            "s_waitcnt lgkmcnt(14)\n\t"
            "ds_write_b128 v236, v[176:179] offset:128\n\t"
            "v_mov_b64 v[176:177], 0\n\t"
            "v_mov_b64 v[178:179], 0\n\t"
            "s_waitcnt lgkmcnt(10)\n\t"
            "s_nop 0\n\t"
            "v_smfmac_f32_16x16x64_bf16 v[176:179], v[168:171], v[160:167], v255\n\t"
            "ds_read_b128 v[168:171], v254 offset:640\n\t"
            "s_waitcnt lgkmcnt(6)\n\t"
            "v_smfmac_f32_16x16x64_bf16 v[180:183], v[172:175], v[144:151], v255\n\t"
            "ds_read_b64 v[160:161], v253 offset:88\n\t"
            "ds_read_b64 v[162:163], v253 offset:184\n\t"
            "v_mfma_f32_4x4x4_16b_bf16 v[196:199], v[184:185], v[184:185], v[196:199]\n\t"
            "v_mfma_f32_4x4x4_16b_bf16 v[192:195], v[238:239], v[184:185], v[192:195]\n\t"
            "s_nop 0\n\t"
            "v_mfma_f32_4x4x4_16b_bf16 v[196:199], v[186:187], v[186:187], v[196:199]\n\t"
            "v_mfma_f32_4x4x4_16b_bf16 v[192:195], v[238:239], v[186:187], v[192:195]\n\t"
            "ds_read_b64 v[164:165], v253 offset:1832\n\t"
            "v_cvt_pk_bf16_f32 v176, v176, v177\n\t"
            "v_cvt_pk_bf16_f32 v177, v178, v179\n\t"
            "v_cvt_pk_bf16_f32 v178, v180, v181\n\t"
            "v_cvt_pk_bf16_f32 v179, v182, v183\n\t"
            "ds_read_b64 v[166:167], v253 offset:1928\n\t"
            "ds_read_b128 v[172:175], v254 offset:704\n\t"
            "ds_write_b128 v237, v[184:187] offset:128\n\t"
            "v_mov_b64 v[184:185], 0\n\t"
            "v_mov_b64 v[186:187], 0\n\t"
            "s_waitcnt lgkmcnt(6)\n\t"
            "s_nop 0\n\t"
            "v_smfmac_f32_16x16x64_bf16 v[184:187], v[168:171], v[152:159], v255\n\t"
            "ds_write_b128 v230, v[208:211] offset:3072\n\t"
            "ds_write_b128 v236, v[176:179] offset:256\n\t"
            "ds_write_b64 v231, v[212:213]\n\t"
            "ds_write_b64 v231, v[216:217] offset:1536\n\t"
            "ds_write_b64 v231, v[220:221] offset:3072\n\t"
            "s_waitcnt lgkmcnt(6)\n\t"
            "v_smfmac_f32_16x16x64_bf16 v[188:191], v[172:175], v[160:167], v255\n\t"
            "s_mov_b32 exec_hi, 0x3fffff\n\t"
            "s_add_u32 m0, s66, 0\n\t"
            "s_add_u32 s49, s79, 0\n\t"
            "buffer_load_dwordx4 v249, s[72:75], s49 offen lds\n\t"
            "s_mov_b32 exec_hi, -1\n\t"
            "v_mfma_f32_4x4x4_16b_bf16 v[196:199], v[176:177], v[176:177], v[196:199]\n\t"
            "v_mfma_f32_4x4x4_16b_bf16 v[192:195], v[238:239], v[176:177], v[192:195]\n\t"
            "s_nop 0\n\t"
            "v_mfma_f32_4x4x4_16b_bf16 v[196:199], v[178:179], v[178:179], v[196:199]\n\t"
            "v_mfma_f32_4x4x4_16b_bf16 v[192:195], v[238:239], v[178:179], v[192:195]\n\t"
            "s_mov_b32 exec_hi, 0x3fffff\n\t"
            "s_add_u32 m0, s66, 872\n\t"
            "s_add_u32 s49, s68, 196224\n\t"
            "buffer_load_dwordx4 v249, s[72:75], s49 offen lds\n\t"
            "s_mov_b32 exec_hi, -1\n\t"
            "s_mov_b32 exec_hi, 0x3fffff\n\t"
            "s_add_u32 m0, s66, 1744\n\t"
            "s_add_u32 s49, s68, 392448\n\t"
            "buffer_load_dwordx4 v249, s[72:75], s49 offen lds\n\t"
            "s_mov_b32 exec_hi, -1\n\t"
            "s_mov_b32 exec_hi, 0x3fffff\n\t"
            "s_add_u32 m0, s66, 2616\n\t"
            "s_add_u32 s49, s68, 588672\n\t"
            "buffer_load_dwordx4 v249, s[72:75], s49 offen lds\n\t"
            "s_mov_b32 exec_hi, -1\n\t"
            "s_mov_b32 exec_hi, 0x3fffff\n\t"
            "s_add_u32 m0, s66, 3488\n\t"
            "s_add_u32 s49, s68, 784896\n\t"
            "buffer_load_dwordx4 v249, s[72:75], s49 offen lds\n\t"
            "s_mov_b32 exec_hi, -1\n\t"
            "s_mov_b32 exec_hi, 0x3fffff\n\t"
            "s_add_u32 m0, s66, 4360\n\t"
            "s_add_u32 s49, s68, 981120\n\t"
            "buffer_load_dwordx4 v249, s[72:75], s49 offen lds\n\t"
            "s_mov_b32 exec_hi, -1\n\t"
            "v_cvt_pk_bf16_f32 v184, v184, v185\n\t"
            "v_cvt_pk_bf16_f32 v185, v186, v187\n\t"
            "v_cvt_pk_bf16_f32 v186, v188, v189\n\t"
            "v_cvt_pk_bf16_f32 v187, v190, v191\n\t"
            "s_nop 1\n\t"
            "v_mfma_f32_4x4x4_16b_bf16 v[196:199], v[184:185], v[184:185], v[196:199]\n\t"
            "v_mfma_f32_4x4x4_16b_bf16 v[192:195], v[238:239], v[184:185], v[192:195]\n\t"
            "s_nop 0\n\t"
            "v_mfma_f32_4x4x4_16b_bf16 v[196:199], v[186:187], v[186:187], v[196:199]\n\t"
            "v_mfma_f32_4x4x4_16b_bf16 v[192:195], v[238:239], v[186:187], v[192:195]\n\t"
            "s_mov_b32 exec_hi, 0x3fffff\n\t"
            "s_add_u32 m0, s66, 5232\n\t"
            "s_add_u32 s49, s68, 1177344\n\t"
            "buffer_load_dwordx4 v249, s[72:75], s49 offen lds\n\t"
            "s_mov_b32 exec_hi, -1\n\t"
            "s_mov_b32 exec_hi, 0x3fffff\n\t"
            "s_add_u32 m0, s66, 6104\n\t"
            "s_add_u32 s49, s68, 1373568\n\t"
            "buffer_load_dwordx4 v249, s[72:75], s49 offen lds\n\t"
            "s_mov_b32 exec_hi, -1\n\t"
            "v_cndmask_b32_e64 v193, v196, v197, s[60:61]\n\t"
            "v_cndmask_b32_e64 v193, v193, v198, s[62:63]\n\t"
            "v_cndmask_b32_e64 v193, v193, v199, s[64:65]\n\t"
            "ds_write_b128 v237, v[184:187] offset:256\n\t"
            "ds_write_b64 v248, v[192:193] offset:18816\n\t"
            "s_mov_b32 exec_hi, 0x3fffff\n\t"
            "s_add_u32 m0, s66, 6976\n\t"
            "s_add_u32 s49, s68, 1569792\n\t"
            "buffer_load_dwordx4 v249, s[72:75], s49 offen lds\n\t"
            "s_mov_b32 exec_hi, -1\n\t"
            "s_waitcnt lgkmcnt(0)\n\t"
            "s_nop 7\n\t"
            "v_mov_b32 v144, s44\n\t"
            "v_mov_b32 v145, -1\n\t"
            "ds_write_b32 v144, v145\n\t"
            "s_cmp_ge_u32 s33, 221184\n\t"
            "s_cbranch_scc1 .Lrp%=\n\t"
            "s_barrier\n\t"
            "s_waitcnt vmcnt(9)\n\t"
            "s_nop 7\n\t"
            "v_add_u32 v232, s67, v242\n\t"
            "v_xor_b32 v233, 64, v232\n\t"
            "v_add_u32 v234, s67, v247\n\t"
            "ds_read_b128 v[144:147], v232\n\t"
            "ds_read_b128 v[148:151], v233\n\t"
            "ds_read_b128 v[152:155], v232 offset:128\n\t"
            "ds_read_b128 v[156:159], v233 offset:128\n\t"
            "ds_read_b128 v[160:163], v232 offset:256\n\t"
            "ds_read_b128 v[164:167], v233 offset:256\n\t"
            "ds_read_b128 v[176:179], v234 offset:18816\n\t"
            "ds_read_b128 v[180:183], v234 offset:19840\n\t"
            "s_waitcnt lgkmcnt(7)\n\t"
            "v_mfma_f32_16x16x32_bf16 v[200:203], v[144:147], v[0:3], 0\n\t"
            "s_waitcnt lgkmcnt(6)\n\t"
            "v_mfma_f32_16x16x32_bf16 v[200:203], v[148:151], v[4:7], v[200:203]\n\t"
            "s_waitcnt lgkmcnt(5)\n\t"
            "v_mfma_f32_16x16x32_bf16 v[200:203], v[152:155], v[8:11], v[200:203]\n\t"
            "s_waitcnt lgkmcnt(4)\n\t"
            "v_mfma_f32_16x16x32_bf16 v[200:203], v[156:159], v[12:15], v[200:203]\n\t"
            "s_waitcnt lgkmcnt(1)\n\t"
            "v_add_f32 v176, v176, v178\n\t"
            "s_waitcnt lgkmcnt(0)\n\t"
            "v_add_f32 v180, v180, v182\n\t"
            "v_mfma_f32_16x16x32_bf16 v[200:203], v[160:163], v[16:19], v[200:203]\n\t"
            "v_add_f32 v176, v176, v180\n\t"
            "v_add_f32 v177, v177, v179\n\t"
            "v_mfma_f32_16x16x32_bf16 v[200:203], v[164:167], v[20:23], v[200:203]\n\t"
            "v_add_f32 v181, v181, v183\n\t"
            "v_mul_f32 v176, 0x3baaaaab, v176\n\t"
            "v_mfma_f32_16x16x32_bf16 v[204:207], v[144:147], v[24:27], 0\n\t"
            "v_add_f32 v177, v177, v181\n\t"
            "v_mul_f32_e64 v176, v176, -v176\n\t"
            "v_mfma_f32_16x16x32_bf16 v[204:207], v[148:151], v[28:31], v[204:207]\n\t"
            "v_fmac_f32 v176, 0x3baaaaab, v177\n\t"
            "v_max_f32 v176, 0, v176\n\t"
            "v_mfma_f32_16x16x32_bf16 v[204:207], v[152:155], v[32:35], v[204:207]\n\t"
            "v_add_f32 v176, 0x3727c5ac, v176\n\t"
            "v_rsq_f32 v176, v176\n\t"
            "v_mfma_f32_16x16x32_bf16 v[204:207], v[156:159], v[36:39], v[204:207]\n\t"
            "ds_write_b32 v246, v176\n\t"
            "v_mfma_f32_16x16x32_bf16 v[204:207], v[160:163], v[40:43], v[204:207]\n\t"
            "ds_read_b128 v[228:231], v245\n\t"
            "v_mfma_f32_16x16x32_bf16 v[204:207], v[164:167], v[44:47], v[204:207]\n\t"
            "v_mfma_f32_16x16x32_bf16 v[208:211], v[144:147], v[48:51], 0\n\t"
            "v_mfma_f32_16x16x32_bf16 v[208:211], v[148:151], v[52:55], v[208:211]\n\t"
            "v_mfma_f32_16x16x32_bf16 v[208:211], v[152:155], v[56:59], v[208:211]\n\t"
            "v_mfma_f32_16x16x32_bf16 v[208:211], v[156:159], v[60:63], v[208:211]\n\t"
            "v_mfma_f32_16x16x32_bf16 v[208:211], v[160:163], v[64:67], v[208:211]\n\t"
            "v_mfma_f32_16x16x32_bf16 v[208:211], v[164:167], v[68:71], v[208:211]\n\t"
            "v_mfma_f32_16x16x32_bf16 v[212:215], v[144:147], v[72:75], 0\n\t"
            "s_waitcnt lgkmcnt(0)\n\t"
            "v_mul_f32 v200, v228, v200\n\t"
            "v_mul_f32 v201, v229, v201\n\t"
            "v_mfma_f32_16x16x32_bf16 v[212:215], v[148:151], v[76:79], v[212:215]\n\t"
            "v_mul_f32 v204, v228, v204\n\t"
            "v_mul_f32 v205, v229, v205\n\t"
            "v_mfma_f32_16x16x32_bf16 v[212:215], v[152:155], v[80:83], v[212:215]\n\t"
            "v_mul_f32 v208, v228, v208\n\t"
            "v_mul_f32 v209, v229, v209\n\t"
            "v_mfma_f32_16x16x32_bf16 v[212:215], v[156:159], v[84:87], v[212:215]\n\t"
            "v_mul_f32 v202, v230, v202\n\t"
            "v_mul_f32 v203, v231, v203\n\t"
            "v_mfma_f32_16x16x32_bf16 v[212:215], v[160:163], v[88:91], v[212:215]\n\t"
            "v_cvt_pk_bf16_f32 v200, v200, v201\n\t"
            "v_mul_f32 v206, v230, v206\n\t"
            "v_mfma_f32_16x16x32_bf16 v[212:215], v[164:167], v[92:95], v[212:215]\n\t"
            "v_mul_f32 v207, v231, v207\n\t"
            "v_cvt_pk_bf16_f32 v204, v204, v205\n\t"
            "v_mfma_f32_16x16x32_bf16 v[216:219], v[144:147], v[96:99], 0\n\t"
            "v_mul_f32 v210, v230, v210\n\t"
            "v_mul_f32 v211, v231, v211\n\t"
            "v_mfma_f32_16x16x32_bf16 v[216:219], v[148:151], v[100:103], v[216:219]\n\t"
            "v_cvt_pk_bf16_f32 v208, v208, v209\n\t"
            "v_cvt_pk_bf16_f32 v201, v202, v203\n\t"
            "v_mfma_f32_16x16x32_bf16 v[216:219], v[152:155], v[104:107], v[216:219]\n\t"
            "v_mul_f32 v212, v228, v212\n\t"
            "v_mul_f32 v213, v229, v213\n\t"
            "v_mfma_f32_16x16x32_bf16 v[216:219], v[156:159], v[108:111], v[216:219]\n\t"
            "v_mul_f32 v214, v230, v214\n\t"
            "v_mul_f32 v215, v231, v215\n\t"
            "v_mfma_f32_16x16x32_bf16 v[216:219], v[160:163], v[112:115], v[216:219]\n\t"
            "v_cvt_pk_bf16_f32 v212, v212, v213\n\t"
            "v_cvt_pk_bf16_f32 v205, v206, v207\n\t"
            "v_mfma_f32_16x16x32_bf16 v[216:219], v[164:167], v[116:119], v[216:219]\n\t"
            "v_cvt_pk_bf16_f32 v209, v210, v211\n\t"
            "v_cvt_pk_bf16_f32 v213, v214, v215\n\t"
            "v_mfma_f32_16x16x32_bf16 v[220:223], v[144:147], v[120:123], 0\n\t"
            "v_mfma_f32_16x16x32_bf16 v[220:223], v[148:151], v[124:127], v[220:223]\n\t"
            "v_mfma_f32_16x16x32_bf16 v[220:223], v[152:155], v[128:131], v[220:223]\n\t"
            "v_mfma_f32_16x16x32_bf16 v[220:223], v[156:159], v[132:135], v[220:223]\n\t"
            "s_nop 1\n\t"
            "v_mul_f32 v216, v228, v216\n\t"
            "v_mfma_f32_16x16x32_bf16 v[220:223], v[160:163], v[136:139], v[220:223]\n\t"
            "v_mul_f32 v217, v229, v217\n\t"
            "v_mul_f32 v218, v230, v218\n\t"
            "v_mfma_f32_16x16x32_bf16 v[220:223], v[164:167], v[140:143], v[220:223]\n\t"
            "v_mul_f32 v219, v231, v219\n\t"
            "v_cvt_pk_bf16_f32 v216, v216, v217\n\t"
            "v_cvt_pk_bf16_f32 v217, v218, v219\n\t"
            "s_nop 4\n\t"
            "v_mul_f32 v220, v228, v220\n\t"
            "v_mul_f32 v221, v229, v221\n\t"
            "v_mul_f32 v222, v230, v222\n\t"
            "v_mul_f32 v223, v231, v223\n\t"
            "v_cvt_pk_bf16_f32 v220, v220, v221\n\t"
            "v_cvt_pk_bf16_f32 v221, v222, v223\n\t"
            "s_nop 7\n\t"
            "s_add_u32 s45, s46, s27\n\t"
            "s_mov_b32 s46, s47\n\t"
            "s_mov_b32 s47, s68\n\t"
            "s_xor_b32 s48, s48, s71\n\t"
            "s_xor_b32 s67, s67, s71\n\t"
            "s_add_u32 s76, s76, 4\n\t"
            "s_lshl_b32 s56, s56, 2\n\t"
            "s_lshr_b32 s49, s26, 30\n\t"
            "s_or_b32 s56, s56, s49\n\t"
            "ds_read_b128 v[168:171], v254\n\t"
            "ds_read_b128 v[172:175], v254 offset:64\n\t"
            "s_waitcnt vmcnt(0)\n\t"
            "ds_read_b64 v[144:145], v253\n\t"
            "ds_read_b64 v[146:147], v253 offset:96\n\t"
            "ds_read_b64 v[148:149], v253 offset:1744\n\t"
            "ds_read_b64 v[150:151], v253 offset:1840\n\t"
            "ds_read_b64 v[152:153], v253 offset:8\n\t"
            "ds_read_b64 v[154:155], v253 offset:104\n\t"
            "ds_read_b64 v[156:157], v253 offset:1752\n\t"
            "ds_read_b64 v[158:159], v253 offset:1848\n\t"
            "ds_read_b64 v[160:161], v253 offset:16\n\t"
            "ds_read_b64 v[162:163], v253 offset:112\n\t"
            "ds_read_b64 v[164:165], v253 offset:1760\n\t"
            "ds_read_b64 v[166:167], v253 offset:1856\n\t"
            "s_waitcnt lgkmcnt(0)\n\t"
            "s_nop 7\n\t"
            ".p2align 6\n\t"
            ".Lwc_loop%=:\n\t"
            "s_setprio 3\n\t"
            "ds_read_b128 v[228:231], v245 offset:64\n\t"
            "v_mov_b64 v[180:181], 0\n\t"
            "v_mov_b64 v[182:183], 0\n\t"
            "v_mov_b64 v[176:177], 0\n\t"
            "v_mov_b64 v[178:179], 0\n\t"
            "v_mov_b32 v249, s76\n\t"
            "v_mov_b64 v[184:185], 0\n\t"
            "s_waitcnt lgkmcnt(2)\n\t"
            "v_smfmac_f32_16x16x64_bf16 v[176:179], v[168:171], v[144:151], v255\n\t"
            "v_smfmac_f32_16x16x64_bf16 v[180:183], v[172:175], v[152:159], v255\n\t"
            "ds_read_b64 v[146:147], v253 offset:120\n\t"
            "v_mov_b64 v[188:189], 0\n\t"
            "v_mov_b64 v[186:187], 0\n\t"
            "v_mov_b64 v[190:191], 0\n\t"
            "v_add_u32 v236, s67, v250\n\t"
            "v_add_u32 v232, s48, v242\n\t"
            "v_xor_b32 v233, 64, v232\n\t"
            "v_add_u32 v248, s67, v251\n\t"
            "v_add_u32 v234, s67, v247\n\t"
            "v_cvt_pk_bf16_f32 v176, v176, v177\n\t"
            "v_cvt_pk_bf16_f32 v177, v178, v179\n\t"
            "v_cvt_pk_bf16_f32 v179, v182, v183\n\t"
            "v_cvt_pk_bf16_f32 v178, v180, v181\n\t"
            "v_mov_b64 v[180:181], 0\n\t"
            "v_mov_b64 v[182:183], 0\n\t"
            "ds_read_b64 v[144:145], v253 offset:24\n\t"
            "ds_read_b64 v[148:149], v253 offset:1768\n\t"
            "ds_read_b32 v249, v249\n\t"
            "ds_read_b128 v[172:175], v254 offset:192\n\t"
            "ds_read_b64 v[150:151], v253 offset:1864\n\t"
            "ds_read_b128 v[168:171], v254 offset:128\n\t"
            "ds_read_b64 v[154:155], v253 offset:128\n\t"
            "ds_read_b64 v[152:153], v253 offset:32\n\t"
            "ds_read_b64 v[156:157], v253 offset:1776\n\t"
            "ds_read_b64 v[158:159], v253 offset:1872\n\t"
            "s_waitcnt lgkmcnt(11)\n\t"
            "s_barrier\n\t"
            "s_waitcnt lgkmcnt(7)\n\t"
            "v_readfirstlane_b32 s26, v249\n\t"
            "ds_read_b32 v249, v252 offset:1024\n\t"
            "s_and_b32 s68, s26, 0x7ffffff\n\t"
            "s_add_u32 s68, s68, s78\n\t"
            "s_and_b32 s69, s26, 0x38000000\n\t"
            "s_or_b32 s69, s69, s39\n\t"
            "s_bitcmp1_b32 s26, 27\n\t"
            "s_cselect_b32 s79, 2.0, s68\n\t"
            "s_waitcnt lgkmcnt(5)\n\t"
            "v_smfmac_f32_16x16x64_bf16 v[184:187], v[168:171], v[160:167], v255\n\t"
            "v_smfmac_f32_16x16x64_bf16 v[188:191], v[172:175], v[144:151], v255\n\t"
            "s_and_b32 s57, s56, 3\n\t"
            "s_lshl_b32 s58, s57, 3\n\t"
            "s_lshr_b32 m0, s54, s58\n\t"
            "ds_read_b128 v[168:171], v254 offset:256\n\t"
            "s_lshl_b32 s58, s57, 4\n\t"
            "s_lshr_b64 s[36:37], s[50:51], s58\n\t"
            "s_and_b32 s57, s36, 0xffff\n\t"
            "s_add_u32 s57, s57, s67\n\t"
            "v_mfma_f32_4x4x4_16b_bf16 v[196:199], v[176:177], v[176:177], 0\n\t"
            "v_mfma_f32_4x4x4_16b_bf16 v[192:195], v[238:239], v[176:177], 0\n\t"
            "s_nop 0\n\t"
            "v_mfma_f32_4x4x4_16b_bf16 v[196:199], v[178:179], v[178:179], v[196:199]\n\t"
            "v_mfma_f32_4x4x4_16b_bf16 v[192:195], v[238:239], v[178:179], v[192:195]\n\t"
            "ds_read_b128 v[172:175], v254 offset:320\n\t"
            "s_lshr_b64 s[36:37], s[52:53], s58\n\t"
            "s_and_b32 s58, s36, 0xffff\n\t"
            "s_add_u32 s58, s58, s67\n\t"
            "ds_read_b64 v[164:165], v253 offset:1784\n\t"
            "s_and_b32 s59, s50, 0xffff\n\t"
            "s_add_u32 s59, s59, s67\n\t"
            "s_and_b32 s38, s52, 0xffff\n\t"
            "s_add_u32 s38, s38, s67\n\t"
            "ds_read_b64 v[166:167], v253 offset:1880\n\t"
            "v_writelane_b32 v236, s59, m0\n\t"
            "v_writelane_b32 v236, s57, 63\n\t"
            "v_cvt_pk_bf16_f32 v184, v184, v185\n\t"
            "v_cvt_pk_bf16_f32 v185, v186, v187\n\t"
            "v_cvt_pk_bf16_f32 v186, v188, v189\n\t"
            "v_cvt_pk_bf16_f32 v187, v190, v191\n\t"
            "v_xor_b32 v237, 16, v236\n\t"
            "v_mov_b64 v[188:189], 0\n\t"
            "v_mov_b64 v[190:191], 0\n\t"
            "v_writelane_b32 v248, s38, m0\n\t"
            "v_writelane_b32 v248, s58, 63\n\t"
            "s_waitcnt lgkmcnt(4)\n\t"
            "v_and_b32 v249, s69, v249\n\t"
            "ds_read_b64 v[162:163], v253 offset:136\n\t"
            "ds_read_b64 v[160:161], v253 offset:40\n\t"
            "ds_read_b64 v[150:151], v253 offset:1888\n\t"
            "ds_read_b64 v[144:145], v253 offset:48\n\t"
            "ds_read_b64 v[148:149], v253 offset:1792\n\t"
            "ds_read_b64 v[146:147], v253 offset:144\n\t"
            "ds_write_b128 v236, v[176:179]\n\t"
            "v_mov_b64 v[178:179], 0\n\t"
            "v_mov_b64 v[176:177], 0\n\t"
            "ds_write_b128 v237, v[184:187]\n\t"
            "s_waitcnt lgkmcnt(6)\n\t"
            "v_smfmac_f32_16x16x64_bf16 v[176:179], v[168:171], v[152:159], v255\n\t"
            "v_smfmac_f32_16x16x64_bf16 v[180:183], v[172:175], v[160:167], v255\n\t"
            "ds_read_b128 v[168:171], v254 offset:384\n\t"
            "ds_read_b64 v[152:153], v253 offset:56\n\t"
            "v_mfma_f32_4x4x4_16b_bf16 v[196:199], v[184:185], v[184:185], v[196:199]\n\t"
            "v_mfma_f32_4x4x4_16b_bf16 v[192:195], v[238:239], v[184:185], v[192:195]\n\t"
            "s_nop 0\n\t"
            "v_mfma_f32_4x4x4_16b_bf16 v[196:199], v[186:187], v[186:187], v[196:199]\n\t"
            "v_mfma_f32_4x4x4_16b_bf16 v[192:195], v[238:239], v[186:187], v[192:195]\n\t"
            "ds_read_b64 v[158:159], v253 offset:1896\n\t"
            "v_mov_b64 v[186:187], 0\n\t"
            "v_mov_b64 v[184:185], 0\n\t"
            "v_cvt_pk_bf16_f32 v176, v176, v177\n\t"
            "v_cvt_pk_bf16_f32 v177, v178, v179\n\t"
            "v_cvt_pk_bf16_f32 v178, v180, v181\n\t"
            "v_cvt_pk_bf16_f32 v179, v182, v183\n\t"
            "v_mov_b64 v[180:181], 0\n\t"
            "v_mov_b64 v[182:183], 0\n\t"
            "ds_read_b64 v[156:157], v253 offset:1800\n\t"
            "ds_read_b128 v[172:175], v254 offset:448\n\t"
            "ds_read_b64 v[154:155], v253 offset:152\n\t"
            "ds_read_b64 v[166:167], v253 offset:1904\n\t"
            "ds_read_b64 v[160:161], v253 offset:64\n\t"
            "ds_read_b64 v[164:165], v253 offset:1808\n\t"
            "s_waitcnt lgkmcnt(14)\n\t"
            "ds_read_b64 v[162:163], v253 offset:160\n\t"
            "s_waitcnt lgkmcnt(14)\n\t"
            "ds_write_b128 v236, v[176:179] offset:128\n\t"
            "s_waitcnt lgkmcnt(5)\n\t"
            "v_smfmac_f32_16x16x64_bf16 v[184:187], v[168:171], v[144:151], v255\n\t"
            "v_smfmac_f32_16x16x64_bf16 v[188:191], v[172:175], v[152:159], v255\n\t"
            "ds_read_b128 v[172:175], v254 offset:576\n\t"
            "ds_read_b64 v[146:147], v253 offset:168\n\t"
            "v_mfma_f32_4x4x4_16b_bf16 v[196:199], v[176:177], v[176:177], v[196:199]\n\t"
            "v_mfma_f32_4x4x4_16b_bf16 v[192:195], v[238:239], v[176:177], v[192:195]\n\t"
            "s_nop 0\n\t"
            "v_mfma_f32_4x4x4_16b_bf16 v[196:199], v[178:179], v[178:179], v[196:199]\n\t"
            "v_mfma_f32_4x4x4_16b_bf16 v[192:195], v[238:239], v[178:179], v[192:195]\n\t"
            "ds_read_b64 v[148:149], v253 offset:1816\n\t"
            "v_mov_b64 v[176:177], 0\n\t"
            "v_mov_b64 v[178:179], 0\n\t"
            "v_cvt_pk_bf16_f32 v184, v184, v185\n\t"
            "v_cvt_pk_bf16_f32 v185, v186, v187\n\t"
            "v_cvt_pk_bf16_f32 v187, v190, v191\n\t"
            "v_cvt_pk_bf16_f32 v186, v188, v189\n\t"
            "v_mov_b64 v[190:191], 0\n\t"
            "v_mov_b64 v[188:189], 0\n\t"
            "ds_read_b64 v[144:145], v253 offset:72\n\t"
            "ds_read_b64 v[150:151], v253 offset:1912\n\t"
            "ds_read_b128 v[168:171], v254 offset:512\n\t"
            "ds_read_b64 v[158:159], v253 offset:1920\n\t"
            "ds_read_b64 v[156:157], v253 offset:1824\n\t"
            "ds_read_b64 v[154:155], v253 offset:176\n\t"
            "ds_read_b64 v[152:153], v253 offset:80\n\t"
            "s_waitcnt lgkmcnt(14)\n\t"
            "ds_write_b128 v237, v[184:187] offset:128\n\t"
            "s_waitcnt lgkmcnt(5)\n\t"
            "v_smfmac_f32_16x16x64_bf16 v[176:179], v[168:171], v[160:167], v255\n\t"
            "v_smfmac_f32_16x16x64_bf16 v[180:183], v[172:175], v[144:151], v255\n\t"
            "ds_read_b64 v[166:167], v253 offset:1928\n\t"
            "ds_read_b128 v[168:171], v254 offset:640\n\t"
            "v_mfma_f32_4x4x4_16b_bf16 v[196:199], v[184:185], v[184:185], v[196:199]\n\t"
            "v_mfma_f32_4x4x4_16b_bf16 v[192:195], v[238:239], v[184:185], v[192:195]\n\t"
            "s_nop 0\n\t"
            "v_mfma_f32_4x4x4_16b_bf16 v[196:199], v[186:187], v[186:187], v[196:199]\n\t"
            "v_mfma_f32_4x4x4_16b_bf16 v[192:195], v[238:239], v[186:187], v[192:195]\n\t"
            "ds_read_b64 v[160:161], v253 offset:88\n\t"
            "v_mov_b64 v[184:185], 0\n\t"
            "v_mov_b64 v[186:187], 0\n\t"
            "v_cvt_pk_bf16_f32 v176, v176, v177\n\t"
            "v_cvt_pk_bf16_f32 v177, v178, v179\n\t"
            "v_cvt_pk_bf16_f32 v179, v182, v183\n\t"
            "v_cvt_pk_bf16_f32 v178, v180, v181\n\t"
            "ds_read_b128 v[172:175], v254 offset:704\n\t"
            "ds_read_b64 v[162:163], v253 offset:184\n\t"
            "ds_read_b64 v[164:165], v253 offset:1832\n\t"
            "ds_read_b128 v[144:147], v232 offset:6144\n\t"
            "ds_read_b128 v[148:151], v233 offset:6144\n\t"
            "ds_read_b128 v[180:183], v233 offset:12288\n\t"
            "ds_write_b128 v236, v[176:179] offset:256\n\t"
            "s_waitcnt lgkmcnt(4)\n\t"
            "v_smfmac_f32_16x16x64_bf16 v[184:187], v[168:171], v[152:159], v255\n\t"
            "v_smfmac_f32_16x16x64_bf16 v[188:191], v[172:175], v[160:167], v255\n\t"
            "ds_read_b128 v[152:155], v232 offset:6272\n\t"
            "ds_read_b128 v[156:159], v233 offset:6272\n\t"
            "v_mfma_f32_4x4x4_16b_bf16 v[196:199], v[176:177], v[176:177], v[196:199]\n\t"
            "v_mfma_f32_4x4x4_16b_bf16 v[192:195], v[238:239], v[176:177], v[192:195]\n\t"
            "s_nop 0\n\t"
            "v_mfma_f32_4x4x4_16b_bf16 v[196:199], v[178:179], v[178:179], v[196:199]\n\t"
            "v_mfma_f32_4x4x4_16b_bf16 v[192:195], v[238:239], v[178:179], v[192:195]\n\t"
            "ds_read_b128 v[160:163], v232 offset:6400\n\t"
            "v_cvt_pk_bf16_f32 v184, v184, v185\n\t"
            "v_cvt_pk_bf16_f32 v185, v186, v187\n\t"
            "v_cvt_pk_bf16_f32 v186, v188, v189\n\t"
            "v_cvt_pk_bf16_f32 v187, v190, v191\n\t"
            "s_nop 1\n\t"
            "v_mfma_f32_4x4x4_16b_bf16 v[196:199], v[184:185], v[184:185], v[196:199]\n\t"
            "v_mfma_f32_4x4x4_16b_bf16 v[192:195], v[238:239], v[184:185], v[192:195]\n\t"
            "s_nop 0\n\t"
            "v_mfma_f32_4x4x4_16b_bf16 v[196:199], v[186:187], v[186:187], v[196:199]\n\t"
            "v_mfma_f32_4x4x4_16b_bf16 v[192:195], v[238:239], v[186:187], v[192:195]\n\t"
            "ds_read_b128 v[164:167], v233 offset:6400\n\t"
            "s_waitcnt lgkmcnt(7)\n\t"
            "v_mfma_f32_16x16x32_bf16 v[224:227], v[144:147], v[0:3], 0\n\t"
            "s_setprio 3\n\t"
            "ds_read_b128 v[176:179], v232 offset:12288\n\t"
            "s_waitcnt lgkmcnt(7)\n\t"
            "v_mfma_f32_16x16x32_bf16 v[224:227], v[148:151], v[4:7], v[224:227]\n\t"
            "s_waitcnt lgkmcnt(4)\n\t"
            "ds_read_b128 v[188:191], v233 offset:12416\n\t"
            "v_mfma_f32_16x16x32_bf16 v[224:227], v[152:155], v[8:11], v[224:227]\n\t"
            "s_waitcnt lgkmcnt(4)\n\t"
            "v_cndmask_b32_e64 v193, v196, v197, s[60:61]\n\t"
            "v_cndmask_b32_e64 v193, v193, v198, s[62:63]\n\t"
            "v_mfma_f32_16x16x32_bf16 v[224:227], v[156:159], v[12:15], v[224:227]\n\t"
            "ds_write_b128 v237, v[184:187] offset:256\n\t"
            "s_mov_b32 exec_hi, 0x3fffff\n\t"
            "s_add_u32 m0, s66, 0\n\t"
            "s_add_u32 s49, s79, 0\n\t"
            "buffer_load_dwordx4 v249, s[72:75], s49 offen lds\n\t"
            "s_mov_b32 exec_hi, -1\n\t"
            "s_mov_b32 exec_hi, 0x3fffff\n\t"
            "s_add_u32 m0, s66, 872\n\t"
            "s_add_u32 s49, s68, 196224\n\t"
            "buffer_load_dwordx4 v249, s[72:75], s49 offen lds\n\t"
            "s_mov_b32 exec_hi, -1\n\t"
            "s_waitcnt lgkmcnt(4)\n\t"
            "v_mfma_f32_16x16x32_bf16 v[224:227], v[160:163], v[16:19], v[224:227]\n\t"
            "s_waitcnt lgkmcnt(3)\n\t"
            "ds_read_b128 v[184:187], v232 offset:12416\n\t"
            "v_cndmask_b32_e64 v193, v193, v199, s[64:65]\n\t"
            "v_mfma_f32_16x16x32_bf16 v[224:227], v[164:167], v[20:23], v[224:227]\n\t"
            "ds_write_b64 v248, v[192:193] offset:18816\n\t"
            "s_mov_b32 exec_hi, 0x3fffff\n\t"
            "s_add_u32 m0, s66, 1744\n\t"
            "s_add_u32 s49, s68, 392448\n\t"
            "buffer_load_dwordx4 v249, s[72:75], s49 offen lds\n\t"
            "s_mov_b32 exec_hi, -1\n\t"
            "s_mov_b32 exec_hi, 0x3fffff\n\t"
            "s_add_u32 m0, s66, 2616\n\t"
            "s_add_u32 s49, s68, 588672\n\t"
            "buffer_load_dwordx4 v249, s[72:75], s49 offen lds\n\t"
            "s_mov_b32 exec_hi, -1\n\t"
            "v_mfma_f32_16x16x32_bf16 v[168:171], v[144:147], v[24:27], 0\n\t"
            "ds_read_b128 v[192:195], v232 offset:12544\n\t"
            "v_mul_f32 v226, v230, v226\n\t"
            "v_mfma_f32_16x16x32_bf16 v[168:171], v[148:151], v[28:31], v[168:171]\n\t"
            "ds_read_b128 v[196:199], v233 offset:12544\n\t"
            "v_mul_f32 v224, v228, v224\n\t"
            "v_mfma_f32_16x16x32_bf16 v[168:171], v[152:155], v[32:35], v[168:171]\n\t"
            "v_mul_f32 v225, v229, v225\n\t"
            "v_cvt_pk_bf16_f32 v202, v224, v225\n\t"
            "v_mfma_f32_16x16x32_bf16 v[168:171], v[156:159], v[36:39], v[168:171]\n\t"
            "v_mul_f32 v227, v231, v227\n\t"
            "v_cvt_pk_bf16_f32 v203, v226, v227\n\t"
            "v_mfma_f32_16x16x32_bf16 v[168:171], v[160:163], v[40:43], v[168:171]\n\t"
            "v_add_u32 v232, s67, v242\n\t"
            "v_xor_b32 v233, 64, v232\n\t"
            "v_mfma_f32_16x16x32_bf16 v[168:171], v[164:167], v[44:47], v[168:171]\n\t"
            "s_mov_b32 exec_hi, 0x3fffff\n\t"
            "s_add_u32 m0, s66, 3488\n\t"
            "s_add_u32 s49, s68, 784896\n\t"
            "buffer_load_dwordx4 v249, s[72:75], s49 offen lds\n\t"
            "s_mov_b32 exec_hi, -1\n\t"
            "s_mov_b32 exec_hi, 0x3fffff\n\t"
            "s_add_u32 m0, s66, 4360\n\t"
            "s_add_u32 s49, s68, 981120\n\t"
            "buffer_load_dwordx4 v249, s[72:75], s49 offen lds\n\t"
            "s_mov_b32 exec_hi, -1\n\t"
            "ds_write_b128 v243, v[200:203]\n\t"
            "v_mfma_f32_16x16x32_bf16 v[172:175], v[144:147], v[48:51], 0\n\t"
            "v_mul_f32 v170, v230, v170\n\t"
            "v_mul_f32 v168, v228, v168\n\t"
            "v_mfma_f32_16x16x32_bf16 v[172:175], v[148:151], v[52:55], v[172:175]\n\t"
            "v_mul_f32 v171, v231, v171\n\t"
            "v_cvt_pk_bf16_f32 v207, v170, v171\n\t"
            "v_mfma_f32_16x16x32_bf16 v[172:175], v[152:155], v[56:59], v[172:175]\n\t"
            "v_mul_f32 v169, v229, v169\n\t"
            "v_cvt_pk_bf16_f32 v206, v168, v169\n\t"
            "v_mfma_f32_16x16x32_bf16 v[172:175], v[156:159], v[60:63], v[172:175]\n\t"
            "v_mfma_f32_16x16x32_bf16 v[172:175], v[160:163], v[64:67], v[172:175]\n\t"
            "v_mfma_f32_16x16x32_bf16 v[172:175], v[164:167], v[68:71], v[172:175]\n\t"
            "s_mov_b32 exec_hi, 0x3fffff\n\t"
            "s_add_u32 m0, s66, 5232\n\t"
            "s_add_u32 s49, s68, 1177344\n\t"
            "buffer_load_dwordx4 v249, s[72:75], s49 offen lds\n\t"
            "s_mov_b32 exec_hi, -1\n\t"
            "s_mov_b32 exec_hi, 0x3fffff\n\t"
            "s_add_u32 m0, s66, 6104\n\t"
            "s_add_u32 s49, s68, 1373568\n\t"
            "buffer_load_dwordx4 v249, s[72:75], s49 offen lds\n\t"
            "s_mov_b32 exec_hi, -1\n\t"
            "ds_write_b128 v243, v[204:207] offset:1536\n\t"
            "v_mfma_f32_16x16x32_bf16 v[224:227], v[144:147], v[72:75], 0\n\t"
            "v_mul_f32 v174, v230, v174\n\t"
            "v_mul_f32 v175, v231, v175\n\t"
            "v_mfma_f32_16x16x32_bf16 v[224:227], v[148:151], v[76:79], v[224:227]\n\t"
            "v_mul_f32 v172, v228, v172\n\t"
            "v_mul_f32 v173, v229, v173\n\t"
            "v_mfma_f32_16x16x32_bf16 v[224:227], v[152:155], v[80:83], v[224:227]\n\t"
            "v_cvt_pk_bf16_f32 v210, v172, v173\n\t"
            "v_cvt_pk_bf16_f32 v211, v174, v175\n\t"
            "v_mfma_f32_16x16x32_bf16 v[224:227], v[156:159], v[84:87], v[224:227]\n\t"
            "v_mfma_f32_16x16x32_bf16 v[224:227], v[160:163], v[88:91], v[224:227]\n\t"
            "v_mfma_f32_16x16x32_bf16 v[224:227], v[164:167], v[92:95], v[224:227]\n\t"
            "s_mov_b32 exec_hi, 0x3fffff\n\t"
            "s_add_u32 m0, s66, 6976\n\t"
            "s_add_u32 s49, s68, 1569792\n\t"
            "buffer_load_dwordx4 v249, s[72:75], s49 offen lds\n\t"
            "s_mov_b32 exec_hi, -1\n\t"
            "ds_write_b128 v243, v[208:211] offset:3072\n\t"
            "v_mfma_f32_16x16x32_bf16 v[168:171], v[144:147], v[96:99], 0\n\t"
            "s_add_u32 s45, s46, s27\n\t"
            "s_mov_b32 s46, s47\n\t"
            "s_mov_b32 s47, s68\n\t"
            "v_mfma_f32_16x16x32_bf16 v[168:171], v[148:151], v[100:103], v[168:171]\n\t"
            "s_xor_b32 s48, s48, s71\n\t"
            "s_xor_b32 s67, s67, s71\n\t"
            "s_add_u32 s76, s76, 4\n\t"
            "v_mfma_f32_16x16x32_bf16 v[168:171], v[152:155], v[104:107], v[168:171]\n\t"
            "s_lshl_b32 s56, s56, 2\n\t"
            "s_lshr_b32 s49, s26, 30\n\t"
            "s_or_b32 s56, s56, s49\n\t"
            "v_mfma_f32_16x16x32_bf16 v[168:171], v[156:159], v[108:111], v[168:171]\n\t"
            "v_mul_f32 v226, v230, v226\n\t"
            "v_mul_f32 v227, v231, v227\n\t"
            "v_mfma_f32_16x16x32_bf16 v[168:171], v[160:163], v[112:115], v[168:171]\n\t"
            "v_mul_f32 v225, v229, v225\n\t"
            "v_mul_f32 v224, v228, v224\n\t"
            "v_mfma_f32_16x16x32_bf16 v[168:171], v[164:167], v[116:119], v[168:171]\n\t"
            "v_cvt_pk_bf16_f32 v214, v224, v225\n\t"
            "v_cvt_pk_bf16_f32 v215, v226, v227\n\t"
            "ds_write_b128 v243, v[212:215] offset:4608\n\t"
            "v_mfma_f32_16x16x32_bf16 v[172:175], v[144:147], v[120:123], 0\n\t"
            "v_mfma_f32_16x16x32_bf16 v[172:175], v[148:151], v[124:127], v[172:175]\n\t"
            "v_mfma_f32_16x16x32_bf16 v[172:175], v[152:155], v[128:131], v[172:175]\n\t"
            "s_nop 1\n\t"
            "v_mul_f32 v168, v228, v168\n\t"
            "v_mfma_f32_16x16x32_bf16 v[172:175], v[156:159], v[132:135], v[172:175]\n\t"
            "v_mul_f32 v170, v230, v170\n\t"
            "v_mul_f32 v169, v229, v169\n\t"
            "v_mfma_f32_16x16x32_bf16 v[172:175], v[160:163], v[136:139], v[172:175]\n\t"
            "v_mul_f32 v171, v231, v171\n\t"
            "v_cvt_pk_bf16_f32 v219, v170, v171\n\t"
            "v_mfma_f32_16x16x32_bf16 v[172:175], v[164:167], v[140:143], v[172:175]\n\t"
            "v_cvt_pk_bf16_f32 v218, v168, v169\n\t"
            "ds_write_b128 v243, v[216:219] offset:6144\n\t"
            "s_waitcnt lgkmcnt(11)\n\t"
            "v_mfma_f32_16x16x32_bf16 v[224:227], v[176:179], v[0:3], 0\n\t"
            "v_mfma_f32_16x16x32_bf16 v[224:227], v[180:183], v[4:7], v[224:227]\n\t"
            "s_waitcnt lgkmcnt(8)\n\t"
            "v_mfma_f32_16x16x32_bf16 v[224:227], v[184:187], v[8:11], v[224:227]\n\t"
            "v_mfma_f32_16x16x32_bf16 v[224:227], v[188:191], v[12:15], v[224:227]\n\t"
            "s_waitcnt lgkmcnt(6)\n\t"
            "v_mul_f32 v174, v230, v174\n\t"
            "v_mfma_f32_16x16x32_bf16 v[224:227], v[192:195], v[16:19], v[224:227]\n\t"
            "s_waitcnt lgkmcnt(5)\n\t"
            "v_mul_f32 v175, v231, v175\n\t"
            "v_mul_f32 v172, v228, v172\n\t"
            "v_mfma_f32_16x16x32_bf16 v[224:227], v[196:199], v[20:23], v[224:227]\n\t"
            "v_mul_f32 v173, v229, v173\n\t"
            "v_cvt_pk_bf16_f32 v223, v174, v175\n\t"
            "v_mfma_f32_16x16x32_bf16 v[168:171], v[176:179], v[24:27], 0\n\t"
            "ds_read_b128 v[228:231], v245 offset:128\n\t"
            "v_cvt_pk_bf16_f32 v222, v172, v173\n\t"
            "v_mfma_f32_16x16x32_bf16 v[168:171], v[180:183], v[28:31], v[168:171]\n\t"
            "v_mfma_f32_16x16x32_bf16 v[168:171], v[184:187], v[32:35], v[168:171]\n\t"
            "v_mfma_f32_16x16x32_bf16 v[168:171], v[188:191], v[36:39], v[168:171]\n\t"
            "v_mfma_f32_16x16x32_bf16 v[168:171], v[192:195], v[40:43], v[168:171]\n\t"
            "v_mfma_f32_16x16x32_bf16 v[168:171], v[196:199], v[44:47], v[168:171]\n\t"
            "ds_write_b128 v243, v[220:223] offset:7680\n\t"
            "v_mfma_f32_16x16x32_bf16 v[172:175], v[176:179], v[48:51], 0\n\t"
            "v_mfma_f32_16x16x32_bf16 v[172:175], v[180:183], v[52:55], v[172:175]\n\t"
            "s_waitcnt lgkmcnt(1)\n\t"
            "v_mul_f32 v224, v228, v224\n\t"
            "v_mfma_f32_16x16x32_bf16 v[172:175], v[184:187], v[56:59], v[172:175]\n\t"
            "v_mul_f32 v226, v230, v226\n\t"
            "v_mul_f32 v225, v229, v225\n\t"
            "v_mfma_f32_16x16x32_bf16 v[172:175], v[188:191], v[60:63], v[172:175]\n\t"
            "v_mul_f32 v227, v231, v227\n\t"
            "v_cvt_pk_bf16_f32 v224, v224, v225\n\t"
            "v_mfma_f32_16x16x32_bf16 v[172:175], v[192:195], v[64:67], v[172:175]\n\t"
            "v_cvt_pk_bf16_f32 v225, v226, v227\n\t"
            "v_mul_f32 v169, v229, v169\n\t"
            "v_mfma_f32_16x16x32_bf16 v[172:175], v[196:199], v[68:71], v[172:175]\n\t"
            "ds_write_b64 v244, v[224:225]\n\t"
            "v_mfma_f32_16x16x32_bf16 v[224:227], v[176:179], v[72:75], 0\n\t"
            "v_mul_f32 v171, v231, v171\n\t"
            "v_mul_f32 v168, v228, v168\n\t"
            "v_mfma_f32_16x16x32_bf16 v[224:227], v[180:183], v[76:79], v[224:227]\n\t"
            "v_cvt_pk_bf16_f32 v168, v168, v169\n\t"
            "v_mul_f32 v170, v230, v170\n\t"
            "v_mfma_f32_16x16x32_bf16 v[224:227], v[184:187], v[80:83], v[224:227]\n\t"
            "v_cvt_pk_bf16_f32 v169, v170, v171\n\t"
            "v_mul_f32 v173, v229, v173\n\t"
            "v_mfma_f32_16x16x32_bf16 v[224:227], v[188:191], v[84:87], v[224:227]\n\t"
            "v_mul_f32 v175, v231, v175\n\t"
            "v_mul_f32 v174, v230, v174\n\t"
            "v_mfma_f32_16x16x32_bf16 v[224:227], v[192:195], v[88:91], v[224:227]\n\t"
            "v_mul_f32 v172, v228, v172\n\t"
            "v_cvt_pk_bf16_f32 v172, v172, v173\n\t"
            "v_mfma_f32_16x16x32_bf16 v[224:227], v[196:199], v[92:95], v[224:227]\n\t"
            "ds_write_b64 v244, v[168:169] offset:1536\n\t"
            "v_mfma_f32_16x16x32_bf16 v[168:171], v[176:179], v[96:99], 0\n\t"
            "v_cvt_pk_bf16_f32 v173, v174, v175\n\t"
            "v_mfma_f32_16x16x32_bf16 v[168:171], v[180:183], v[100:103], v[168:171]\n\t"
            "v_mfma_f32_16x16x32_bf16 v[168:171], v[184:187], v[104:107], v[168:171]\n\t"
            "v_mfma_f32_16x16x32_bf16 v[168:171], v[188:191], v[108:111], v[168:171]\n\t"
            "s_nop 1\n\t"
            "v_mul_f32 v225, v229, v225\n\t"
            "v_mfma_f32_16x16x32_bf16 v[168:171], v[192:195], v[112:115], v[168:171]\n\t"
            "v_mul_f32 v226, v230, v226\n\t"
            "v_mul_f32 v224, v228, v224\n\t"
            "v_mfma_f32_16x16x32_bf16 v[168:171], v[196:199], v[116:119], v[168:171]\n\t"
            "ds_write_b64 v244, v[172:173] offset:3072\n\t"
            "v_mfma_f32_16x16x32_bf16 v[172:175], v[176:179], v[120:123], 0\n\t"
            "v_mul_f32 v227, v231, v227\n\t"
            "v_cvt_pk_bf16_f32 v224, v224, v225\n\t"
            "v_mfma_f32_16x16x32_bf16 v[172:175], v[180:183], v[124:127], v[172:175]\n\t"
            "v_cvt_pk_bf16_f32 v225, v226, v227\n\t"
            "v_mfma_f32_16x16x32_bf16 v[172:175], v[184:187], v[128:131], v[172:175]\n\t"
            "s_nop 0\n\t"
            "v_mul_f32 v168, v228, v168\n\t"
            "v_mul_f32 v169, v229, v169\n\t"
            "v_mfma_f32_16x16x32_bf16 v[172:175], v[188:191], v[132:135], v[172:175]\n\t"
            "v_mul_f32 v171, v231, v171\n\t"
            "v_cvt_pk_bf16_f32 v168, v168, v169\n\t"
            "v_mfma_f32_16x16x32_bf16 v[172:175], v[192:195], v[136:139], v[172:175]\n\t"
            "v_mul_f32 v170, v230, v170\n\t"
            "v_cvt_pk_bf16_f32 v169, v170, v171\n\t"
            "v_mfma_f32_16x16x32_bf16 v[172:175], v[196:199], v[140:143], v[172:175]\n\t"
            "ds_write_b64 v244, v[224:225] offset:4608\n\t"
            "ds_write_b64 v244, v[168:169] offset:6144\n\t"
            "ds_read_b128 v[168:171], v254\n\t"
            "s_nop 4\n\t"
            "v_mul_f32 v172, v228, v172\n\t"
            "v_mul_f32 v173, v229, v173\n\t"
            "v_cvt_pk_bf16_f32 v172, v172, v173\n\t"
            "v_mul_f32 v174, v230, v174\n\t"
            "v_mul_f32 v175, v231, v175\n\t"
            "v_cvt_pk_bf16_f32 v173, v174, v175\n\t"
            "ds_write_b64 v244, v[172:173] offset:7680\n\t"
            "ds_read_b128 v[172:175], v254 offset:64\n\t"
            "s_waitcnt lgkmcnt(1)\n\t"
            "s_cmp_lt_u32 s46, 0x7000000\n\t"
            "s_barrier\n\t"
            "s_cbranch_scc0 .Lvx%=\n\t"
            "ds_read_b128 v[144:147], v232\n\t"
            "ds_read_b128 v[148:151], v233\n\t"
            "ds_read_b128 v[152:155], v232 offset:128\n\t"
            "ds_read_b128 v[156:159], v233 offset:128\n\t"
            "ds_read_b128 v[160:163], v232 offset:256\n\t"
            "ds_read_b128 v[164:167], v233 offset:256\n\t"
            "ds_read_b128 v[180:183], v234 offset:19840\n\t"
            "ds_read_b128 v[176:179], v234 offset:18816\n\t"
            "s_waitcnt lgkmcnt(7)\n\t"
            "v_mfma_f32_16x16x32_bf16 v[200:203], v[144:147], v[0:3], 0\n\t"
            "s_waitcnt lgkmcnt(6)\n\t"
            "v_mfma_f32_16x16x32_bf16 v[200:203], v[148:151], v[4:7], v[200:203]\n\t"
            "s_waitcnt lgkmcnt(5)\n\t"
            "v_mfma_f32_16x16x32_bf16 v[200:203], v[152:155], v[8:11], v[200:203]\n\t"
            "s_waitcnt lgkmcnt(4)\n\t"
            "v_mfma_f32_16x16x32_bf16 v[200:203], v[156:159], v[12:15], v[200:203]\n\t"
            "s_waitcnt lgkmcnt(1)\n\t"
            "v_add_f32 v181, v181, v183\n\t"
            "s_waitcnt lgkmcnt(0)\n\t"
            "v_add_f32 v176, v176, v178\n\t"
            "v_mfma_f32_16x16x32_bf16 v[200:203], v[160:163], v[16:19], v[200:203]\n\t"
            "v_add_f32 v177, v177, v179\n\t"
            "v_add_f32 v180, v180, v182\n\t"
            "v_mfma_f32_16x16x32_bf16 v[200:203], v[164:167], v[20:23], v[200:203]\n\t"
            "v_add_f32 v176, v176, v180\n\t"
            "v_mul_f32 v176, 0x3baaaaab, v176\n\t"
            "v_mfma_f32_16x16x32_bf16 v[204:207], v[144:147], v[24:27], 0\n\t"
            "v_mul_f32_e64 v176, v176, -v176\n\t"
            "v_add_f32 v177, v177, v181\n\t"
            "v_mfma_f32_16x16x32_bf16 v[204:207], v[148:151], v[28:31], v[204:207]\n\t"
            "v_fmac_f32 v176, 0x3baaaaab, v177\n\t"
            "v_max_f32 v176, 0, v176\n\t"
            "v_mfma_f32_16x16x32_bf16 v[204:207], v[152:155], v[32:35], v[204:207]\n\t"
            "v_add_f32 v176, 0x3727c5ac, v176\n\t"
            "v_mfma_f32_16x16x32_bf16 v[204:207], v[156:159], v[36:39], v[204:207]\n\t"
            "v_rsq_f32 v176, v176\n\t"
            "v_mfma_f32_16x16x32_bf16 v[204:207], v[160:163], v[40:43], v[204:207]\n\t"
            "ds_write_b32 v246, v176\n\t"
            "v_mfma_f32_16x16x32_bf16 v[204:207], v[164:167], v[44:47], v[204:207]\n\t"
            "ds_read_b128 v[228:231], v245\n\t"
            "v_mfma_f32_16x16x32_bf16 v[208:211], v[144:147], v[48:51], 0\n\t"
            "v_mfma_f32_16x16x32_bf16 v[208:211], v[148:151], v[52:55], v[208:211]\n\t"
            "v_mfma_f32_16x16x32_bf16 v[208:211], v[152:155], v[56:59], v[208:211]\n\t"
            "v_mfma_f32_16x16x32_bf16 v[208:211], v[156:159], v[60:63], v[208:211]\n\t"
            "v_mfma_f32_16x16x32_bf16 v[208:211], v[160:163], v[64:67], v[208:211]\n\t"
            "v_mfma_f32_16x16x32_bf16 v[208:211], v[164:167], v[68:71], v[208:211]\n\t"
            "v_mfma_f32_16x16x32_bf16 v[212:215], v[144:147], v[72:75], 0\n\t"
            "v_mfma_f32_16x16x32_bf16 v[212:215], v[148:151], v[76:79], v[212:215]\n\t"
            "s_waitcnt lgkmcnt(0)\n\t"
            "v_mul_f32 v201, v229, v201\n\t"
            "v_mul_f32 v205, v229, v205\n\t"
            "v_mfma_f32_16x16x32_bf16 v[212:215], v[152:155], v[80:83], v[212:215]\n\t"
            "v_mul_f32 v206, v230, v206\n\t"
            "v_mul_f32 v203, v231, v203\n\t"
            "v_mfma_f32_16x16x32_bf16 v[212:215], v[156:159], v[84:87], v[212:215]\n\t"
            "v_mul_f32 v211, v231, v211\n\t"
            "v_mul_f32 v208, v228, v208\n\t"
            "v_mfma_f32_16x16x32_bf16 v[212:215], v[160:163], v[88:91], v[212:215]\n\t"
            "v_mul_f32 v209, v229, v209\n\t"
            "v_cvt_pk_bf16_f32 v208, v208, v209\n\t"
            "v_mfma_f32_16x16x32_bf16 v[212:215], v[164:167], v[92:95], v[212:215]\n\t"
            "s_waitcnt vmcnt(0)\n\t"
            "v_mul_f32 v204, v228, v204\n\t"
            "v_mfma_f32_16x16x32_bf16 v[216:219], v[144:147], v[96:99], 0\n\t"
            "v_mul_f32 v207, v231, v207\n\t"
            "v_mul_f32 v200, v228, v200\n\t"
            "v_mfma_f32_16x16x32_bf16 v[216:219], v[148:151], v[100:103], v[216:219]\n\t"
            "v_cvt_pk_bf16_f32 v200, v200, v201\n\t"
            "v_cvt_pk_bf16_f32 v204, v204, v205\n\t"
            "v_mfma_f32_16x16x32_bf16 v[216:219], v[152:155], v[104:107], v[216:219]\n\t"
            "v_cvt_pk_bf16_f32 v205, v206, v207\n\t"
            "v_mul_f32 v214, v230, v214\n\t"
            "v_mfma_f32_16x16x32_bf16 v[216:219], v[156:159], v[108:111], v[216:219]\n\t"
            "v_mul_f32 v213, v229, v213\n\t"
            "v_mul_f32 v212, v228, v212\n\t"
            "v_mfma_f32_16x16x32_bf16 v[216:219], v[160:163], v[112:115], v[216:219]\n\t"
            "v_mul_f32 v215, v231, v215\n\t"
            "v_mul_f32 v202, v230, v202\n\t"
            "v_mfma_f32_16x16x32_bf16 v[216:219], v[164:167], v[116:119], v[216:219]\n\t"
            "v_cvt_pk_bf16_f32 v201, v202, v203\n\t"
            "v_mul_f32 v210, v230, v210\n\t"
            "v_mfma_f32_16x16x32_bf16 v[220:223], v[144:147], v[120:123], 0\n\t"
            "ds_read_b64 v[144:145], v253\n\t"
            "v_cvt_pk_bf16_f32 v209, v210, v211\n\t"
            "v_mfma_f32_16x16x32_bf16 v[220:223], v[148:151], v[124:127], v[220:223]\n\t"
            "ds_read_b64 v[150:151], v253 offset:1840\n\t"
            "v_cvt_pk_bf16_f32 v212, v212, v213\n\t"
            "v_mfma_f32_16x16x32_bf16 v[220:223], v[152:155], v[128:131], v[220:223]\n\t"
            "ds_read_b64 v[152:153], v253 offset:8\n\t"
            "v_mul_f32 v216, v228, v216\n\t"
            "v_mfma_f32_16x16x32_bf16 v[220:223], v[156:159], v[132:135], v[220:223]\n\t"
            "ds_read_b64 v[148:149], v253 offset:1744\n\t"
            "v_mul_f32 v217, v229, v217\n\t"
            "v_mfma_f32_16x16x32_bf16 v[220:223], v[160:163], v[136:139], v[220:223]\n\t"
            "ds_read_b64 v[146:147], v253 offset:96\n\t"
            "v_cvt_pk_bf16_f32 v216, v216, v217\n\t"
            "v_mfma_f32_16x16x32_bf16 v[220:223], v[164:167], v[140:143], v[220:223]\n\t"
            "ds_read_b64 v[164:165], v253 offset:1760\n\t"
            "v_cvt_pk_bf16_f32 v213, v214, v215\n\t"
            "v_mul_f32 v219, v231, v219\n\t"
            "v_mul_f32 v218, v230, v218\n\t"
            "v_cvt_pk_bf16_f32 v217, v218, v219\n\t"
            "ds_read_b64 v[154:155], v253 offset:104\n\t"
            "ds_read_b64 v[162:163], v253 offset:112\n\t"
            "ds_read_b64 v[158:159], v253 offset:1848\n\t"
            "v_mul_f32 v220, v228, v220\n\t"
            "v_mul_f32 v223, v231, v223\n\t"
            "v_mul_f32 v222, v230, v222\n\t"
            "v_mul_f32 v221, v229, v221\n\t"
            "v_cvt_pk_bf16_f32 v220, v220, v221\n\t"
            "v_cvt_pk_bf16_f32 v221, v222, v223\n\t"
            "ds_read_b64 v[160:161], v253 offset:16\n\t"
            "ds_read_b64 v[156:157], v253 offset:1752\n\t"
            "ds_read_b64 v[166:167], v253 offset:1856\n\t"
            "s_branch .Lwc_loop%=\n\t"
            ".Lvx%=:\n\t"
            "s_waitcnt lgkmcnt(0)\n\t"
            "s_nop 7\n\t"
            "s_setprio 3\n\t"
            "v_add_u32 v236, s19, v252\n\t"
            "ds_read_b128 v[244:247], v236\n\t"
            "s_add_u32 s70, s33, 73728\n\t"
            "s_waitcnt lgkmcnt(0)\n\t"
            "buffer_load_dwordx4 v[200:203], v244, s[28:31], s21 offen\n\t"
            "buffer_load_dwordx4 v[204:207], v245, s[28:31], s21 offen\n\t"
            "buffer_load_dwordx4 v[208:211], v246, s[28:31], s21 offen\n\t"
            "s_add_u32 s21, s21, 6144\n\t"
            "buffer_load_dwordx4 v[212:215], v244, s[28:31], s21 offen\n\t"
            "buffer_load_dwordx4 v[216:219], v245, s[28:31], s21 offen\n\t"
            "buffer_load_dwordx4 v[220:223], v246, s[28:31], s21 offen\n\t"
            "s_nop 7\n\t"
            "s_add_u32 s49, s70, 0\n\t"
            "buffer_load_dwordx4 v[0:3], v247, s[28:31], s49 offen sc1\n\t"
            "s_add_u32 s49, s70, 1024\n\t"
            "buffer_load_dwordx4 v[4:7], v247, s[28:31], s49 offen sc1\n\t"
            "s_add_u32 s49, s70, 2048\n\t"
            "buffer_load_dwordx4 v[8:11], v247, s[28:31], s49 offen sc1\n\t"
            "s_add_u32 s49, s70, 3072\n\t"
            "buffer_load_dwordx4 v[12:15], v247, s[28:31], s49 offen sc1\n\t"
            "s_add_u32 s49, s70, 4096\n\t"
            "buffer_load_dwordx4 v[16:19], v247, s[28:31], s49 offen sc1\n\t"
            "s_add_u32 s49, s70, 5120\n\t"
            "buffer_load_dwordx4 v[20:23], v247, s[28:31], s49 offen sc1\n\t"
            "s_add_u32 s49, s70, 6144\n\t"
            "buffer_load_dwordx4 v[24:27], v247, s[28:31], s49 offen sc1\n\t"
            "s_add_u32 s49, s70, 7168\n\t"
            "buffer_load_dwordx4 v[28:31], v247, s[28:31], s49 offen sc1\n\t"
            "s_add_u32 s49, s70, 8192\n\t"
            "buffer_load_dwordx4 v[32:35], v247, s[28:31], s49 offen sc1\n\t"
            "s_add_u32 s49, s70, 9216\n\t"
            "buffer_load_dwordx4 v[36:39], v247, s[28:31], s49 offen sc1\n\t"
            "s_add_u32 s49, s70, 10240\n\t"
            "buffer_load_dwordx4 v[40:43], v247, s[28:31], s49 offen sc1\n\t"
            "s_add_u32 s49, s70, 11264\n\t"
            "buffer_load_dwordx4 v[44:47], v247, s[28:31], s49 offen sc1\n\t"
            "s_add_u32 s49, s70, 12288\n\t"
            "buffer_load_dwordx4 v[48:51], v247, s[28:31], s49 offen sc1\n\t"
            "s_add_u32 s49, s70, 13312\n\t"
            "buffer_load_dwordx4 v[52:55], v247, s[28:31], s49 offen sc1\n\t"
            "s_add_u32 s49, s70, 14336\n\t"
            "buffer_load_dwordx4 v[56:59], v247, s[28:31], s49 offen sc1\n\t"
            "s_add_u32 s49, s70, 15360\n\t"
            "buffer_load_dwordx4 v[60:63], v247, s[28:31], s49 offen sc1\n\t"
            "s_add_u32 s49, s70, 16384\n\t"
            "buffer_load_dwordx4 v[64:67], v247, s[28:31], s49 offen sc1\n\t"
            "s_add_u32 s49, s70, 17408\n\t"
            "buffer_load_dwordx4 v[68:71], v247, s[28:31], s49 offen sc1\n\t"
            "s_add_u32 s49, s70, 18432\n\t"
            "buffer_load_dwordx4 v[72:75], v247, s[28:31], s49 offen sc1\n\t"
            "s_add_u32 s49, s70, 19456\n\t"
            "buffer_load_dwordx4 v[76:79], v247, s[28:31], s49 offen sc1\n\t"
            "s_add_u32 s49, s70, 20480\n\t"
            "buffer_load_dwordx4 v[80:83], v247, s[28:31], s49 offen sc1\n\t"
            "s_add_u32 s49, s70, 21504\n\t"
            "buffer_load_dwordx4 v[84:87], v247, s[28:31], s49 offen sc1\n\t"
            "s_add_u32 s49, s70, 22528\n\t"
            "buffer_load_dwordx4 v[88:91], v247, s[28:31], s49 offen sc1\n\t"
            "s_add_u32 s49, s70, 23552\n\t"
            "buffer_load_dwordx4 v[92:95], v247, s[28:31], s49 offen sc1\n\t"
            "s_waitcnt vmcnt(18)\n\t"
            "s_add_u32 s49, s70, 24576\n\t"
            "buffer_load_dwordx4 v[96:99], v247, s[28:31], s49 offen sc1\n\t"
            "s_add_u32 s49, s70, 25600\n\t"
            "buffer_load_dwordx4 v[100:103], v247, s[28:31], s49 offen sc1\n\t"
            "v_add_u32 v242, s22, v242\n\t"
            "v_xor_b32 v243, 64, v242\n\t"
            "v_lshlrev_b32 v192, 16, v200\n\t"
            "v_and_b32 v193, 0xffff0000, v200\n\t"
            "v_lshlrev_b32 v194, 16, v201\n\t"
            "v_and_b32 v195, 0xffff0000, v201\n\t"
            "v_lshlrev_b32 v196, 16, v202\n\t"
            "v_and_b32 v197, 0xffff0000, v202\n\t"
            "v_lshlrev_b32 v198, 16, v203\n\t"
            "v_and_b32 v199, 0xffff0000, v203\n\t"
            "v_bfe_u32 v248, v247, 2, 6\n\t"
            "v_add_u32 v248, s23, v248\n\t"
            "v_lshlrev_b32 v224, 16, v204\n\t"
            "v_and_b32 v225, 0xffff0000, v204\n\t"
            "v_lshlrev_b32 v226, 16, v205\n\t"
            "v_and_b32 v227, 0xffff0000, v205\n\t"
            "v_lshrrev_b32 v236, 4, v247\n\t"
            "v_and_b32 v236, 48, v236\n\t"
            "v_add_u32 v236, s20, v236\n\t"
            "v_bfe_u32 v249, v247, 2, 6\n\t"
            "v_lshlrev_b32 v228, 16, v206\n\t"
            "v_and_b32 v229, 0xffff0000, v206\n\t"
            "v_lshlrev_b32 v230, 16, v207\n\t"
            "v_and_b32 v231, 0xffff0000, v207\n\t"
            "v_add_u32 v249, s23, v249\n\t"
            "ds_read_b128 v[144:147], v242 offset:53760\n\t"
            "ds_read_b128 v[148:151], v243 offset:53760\n\t"
            "ds_read_b128 v[152:155], v242 offset:53888\n\t"
            "ds_read_b128 v[156:159], v243 offset:53888\n\t"
            "ds_read_b128 v[160:163], v242 offset:54016\n\t"
            "ds_read_b128 v[164:167], v243 offset:54016\n\t"
            "ds_read_b32 v248, v248\n\t"
            "ds_read_b32 v249, v249 offset:64\n\t"
            "s_add_u32 s49, s70, 26624\n\t"
            "buffer_load_dwordx4 v[104:107], v247, s[28:31], s49 offen sc1\n\t"
            "s_add_u32 s49, s70, 27648\n\t"
            "buffer_load_dwordx4 v[108:111], v247, s[28:31], s49 offen sc1\n\t"
            "s_waitcnt lgkmcnt(7)\n\t"
            "v_mfma_f32_16x16x32_bf16 v[192:195], v[0:3], v[144:147], v[192:195]\n\t"
            "s_add_u32 s49, s70, 28672\n\t"
            "buffer_load_dwordx4 v[112:115], v247, s[28:31], s49 offen sc1\n\t"
            "s_waitcnt lgkmcnt(6)\n\t"
            "v_mfma_f32_16x16x32_bf16 v[192:195], v[4:7], v[148:151], v[192:195]\n\t"
            "s_add_u32 s49, s70, 29696\n\t"
            "buffer_load_dwordx4 v[116:119], v247, s[28:31], s49 offen sc1\n\t"
            "s_waitcnt lgkmcnt(5)\n\t"
            "v_mfma_f32_16x16x32_bf16 v[192:195], v[8:11], v[152:155], v[192:195]\n\t"
            "s_waitcnt vmcnt(18)\n\t"
            "s_add_u32 s49, s70, 30720\n\t"
            "buffer_load_dwordx4 v[120:123], v247, s[28:31], s49 offen sc1\n\t"
            "s_waitcnt lgkmcnt(4)\n\t"
            "v_mfma_f32_16x16x32_bf16 v[192:195], v[12:15], v[156:159], v[192:195]\n\t"
            "s_add_u32 s49, s70, 31744\n\t"
            "buffer_load_dwordx4 v[124:127], v247, s[28:31], s49 offen sc1\n\t"
            "s_waitcnt lgkmcnt(3)\n\t"
            "v_mfma_f32_16x16x32_bf16 v[192:195], v[16:19], v[160:163], v[192:195]\n\t"
            "s_add_u32 s49, s70, 32768\n\t"
            "buffer_load_dwordx4 v[128:131], v247, s[28:31], s49 offen sc1\n\t"
            "s_waitcnt lgkmcnt(2)\n\t"
            "v_mfma_f32_16x16x32_bf16 v[192:195], v[20:23], v[164:167], v[192:195]\n\t"
            "s_add_u32 s49, s70, 33792\n\t"
            "buffer_load_dwordx4 v[132:135], v247, s[28:31], s49 offen sc1\n\t"
            "v_mfma_f32_16x16x32_bf16 v[196:199], v[24:27], v[144:147], v[196:199]\n\t"
            "s_add_u32 s49, s70, 34816\n\t"
            "buffer_load_dwordx4 v[136:139], v247, s[28:31], s49 offen sc1\n\t"
            "v_mfma_f32_16x16x32_bf16 v[196:199], v[28:31], v[148:151], v[196:199]\n\t"
            "s_add_u32 s49, s70, 35840\n\t"
            "buffer_load_dwordx4 v[140:143], v247, s[28:31], s49 offen sc1\n\t"
            "v_mfma_f32_16x16x32_bf16 v[196:199], v[32:35], v[152:155], v[196:199]\n\t"
            "s_waitcnt vmcnt(18)\n\t"
            "s_waitcnt lgkmcnt(1)\n\t"
            "v_ashrrev_i32 v237, 31, v248\n\t"
            "v_mfma_f32_16x16x32_bf16 v[196:199], v[36:39], v[156:159], v[196:199]\n\t"
            "v_and_b32 v237, 1177344, v237\n\t"
            "v_sub_u32 v236, v236, v237\n\t"
            "v_mfma_f32_16x16x32_bf16 v[196:199], v[40:43], v[160:163], v[196:199]\n\t"
            "v_bfe_i32 v237, v248, 30, 1\n\t"
            "v_and_b32 v237, 2304, v237\n\t"
            "v_mfma_f32_16x16x32_bf16 v[196:199], v[44:47], v[164:167], v[196:199]\n\t"
            "v_cvt_pk_bf16_f32 v192, v192, v193\n\t"
            "v_sub_u32 v236, v236, v237\n\t"
            "v_mfma_f32_16x16x32_bf16 v[224:227], v[48:51], v[144:147], v[224:227]\n\t"
            "v_and_b32 v248, 0x7ffffff, v248\n\t"
            "v_cvt_pk_bf16_f32 v193, v194, v195\n\t"
            "v_mfma_f32_16x16x32_bf16 v[224:227], v[52:55], v[148:151], v[224:227]\n\t"
            "v_add_u32 v248, v248, v236\n\t"
            "v_lshrrev_b32 v236, 4, v247\n\t"
            "v_mfma_f32_16x16x32_bf16 v[224:227], v[56:59], v[152:155], v[224:227]\n\t"
            "v_cvt_pk_bf16_f32 v194, v196, v197\n\t"
            "v_cvt_pk_bf16_f32 v195, v198, v199\n\t"
            "v_mfma_f32_16x16x32_bf16 v[224:227], v[60:63], v[156:159], v[224:227]\n\t"
            "buffer_store_dwordx4 v[192:195], v248, s[40:43], 0 offen sc0 sc1\n\t"
            "v_mfma_f32_16x16x32_bf16 v[224:227], v[64:67], v[160:163], v[224:227]\n\t"
            "s_waitcnt vmcnt(13)\n\t"
            "v_and_b32 v236, 48, v236\n\t"
            "s_waitcnt lgkmcnt(0)\n\t"
            "v_ashrrev_i32 v237, 31, v249\n\t"
            "v_mfma_f32_16x16x32_bf16 v[224:227], v[68:71], v[164:167], v[224:227]\n\t"
            "v_add_u32 v236, s20, v236\n\t"
            "v_and_b32 v237, 1177344, v237\n\t"
            "v_mfma_f32_16x16x32_bf16 v[228:231], v[72:75], v[144:147], v[228:231]\n\t"
            "v_sub_u32 v236, v236, v237\n\t"
            "v_bfe_i32 v237, v249, 30, 1\n\t"
            "v_mfma_f32_16x16x32_bf16 v[228:231], v[76:79], v[148:151], v[228:231]\n\t"
            "v_and_b32 v237, 2304, v237\n\t"
            "v_sub_u32 v236, v236, v237\n\t"
            "v_mfma_f32_16x16x32_bf16 v[228:231], v[80:83], v[152:155], v[228:231]\n\t"
            "v_cvt_pk_bf16_f32 v224, v224, v225\n\t"
            "v_cvt_pk_bf16_f32 v225, v226, v227\n\t"
            "v_mfma_f32_16x16x32_bf16 v[228:231], v[84:87], v[156:159], v[228:231]\n\t"
            "v_and_b32 v249, 0x7ffffff, v249\n\t"
            "v_add_u32 v249, v249, v236\n\t"
            "v_mfma_f32_16x16x32_bf16 v[228:231], v[88:91], v[160:163], v[228:231]\n\t"
            "v_mfma_f32_16x16x32_bf16 v[228:231], v[92:95], v[164:167], v[228:231]\n\t"
            "s_nop 7\n\t"
            "v_cvt_pk_bf16_f32 v226, v228, v229\n\t"
            "v_cvt_pk_bf16_f32 v227, v230, v231\n\t"
            "buffer_store_dwordx4 v[224:227], v248, s[40:43], 0 offen offset:64 sc0 sc1\n\t"
            "s_nop 7\n\t"
            "v_mov_b32 v168, s23\n\t"
            ".Ldv%=:\n\t"
            "ds_read_b128 v[172:175], v168 offset:96\n\t"
            "s_waitcnt lgkmcnt(0)\n\t"
            "v_or_b32 v172, v172, v173\n\t"
            "v_or3_b32 v172, v172, v174, v175\n\t"
            "v_cmp_gt_i32 vcc, 0, v172\n\t"
            "s_nop 4\n\t"
            "s_cbranch_vccnz .Ldv%=\n\t"
            "s_nop 7\n\t"
            "ds_read_b128 v[168:171], v242 offset:59904\n\t"
            "v_lshlrev_b32 v232, 16, v212\n\t"
            "v_and_b32 v233, 0xffff0000, v212\n\t"
            "v_lshlrev_b32 v234, 16, v213\n\t"
            "v_and_b32 v235, 0xffff0000, v213\n\t"
            "s_waitcnt vmcnt(32)\n\t"
            "v_lshlrev_b32 v192, 16, v214\n\t"
            "v_and_b32 v193, 0xffff0000, v214\n\t"
            "v_lshlrev_b32 v194, 16, v215\n\t"
            "v_and_b32 v195, 0xffff0000, v215\n\t"
            "v_lshlrev_b32 v196, 16, v216\n\t"
            "v_and_b32 v197, 0xffff0000, v216\n\t"
            "v_lshlrev_b32 v198, 16, v217\n\t"
            "v_and_b32 v199, 0xffff0000, v217\n\t"
            "v_lshlrev_b32 v224, 16, v218\n\t"
            "v_and_b32 v225, 0xffff0000, v218\n\t"
            "v_lshlrev_b32 v226, 16, v219\n\t"
            "v_and_b32 v227, 0xffff0000, v219\n\t"
            "v_lshlrev_b32 v228, 16, v208\n\t"
            "v_and_b32 v229, 0xffff0000, v208\n\t"
            "v_lshlrev_b32 v230, 16, v209\n\t"
            "v_and_b32 v231, 0xffff0000, v209\n\t"
            "ds_read_b128 v[172:175], v243 offset:59904\n\t"
            "ds_read_b128 v[176:179], v242 offset:60032\n\t"
            "ds_read_b128 v[180:183], v243 offset:60032\n\t"
            "ds_read_b128 v[184:187], v242 offset:60160\n\t"
            "s_waitcnt lgkmcnt(4)\n\t"
            "v_mfma_f32_16x16x32_bf16 v[232:235], v[0:3], v[168:171], v[232:235]\n\t"
            "ds_read_b128 v[188:191], v243 offset:60160\n\t"
            "s_waitcnt lgkmcnt(4)\n\t"
            "v_mfma_f32_16x16x32_bf16 v[232:235], v[4:7], v[172:175], v[232:235]\n\t"
            "s_waitcnt lgkmcnt(3)\n\t"
            "v_mfma_f32_16x16x32_bf16 v[232:235], v[8:11], v[176:179], v[232:235]\n\t"
            "s_waitcnt vmcnt(26)\n\t"
            "s_waitcnt lgkmcnt(2)\n\t"
            "v_mfma_f32_16x16x32_bf16 v[232:235], v[12:15], v[180:183], v[232:235]\n\t"
            "s_waitcnt lgkmcnt(1)\n\t"
            "v_mfma_f32_16x16x32_bf16 v[232:235], v[16:19], v[184:187], v[232:235]\n\t"
            "s_waitcnt lgkmcnt(0)\n\t"
            "v_mfma_f32_16x16x32_bf16 v[232:235], v[20:23], v[188:191], v[232:235]\n\t"
            "v_mfma_f32_16x16x32_bf16 v[192:195], v[24:27], v[168:171], v[192:195]\n\t"
            "v_mfma_f32_16x16x32_bf16 v[192:195], v[28:31], v[172:175], v[192:195]\n\t"
            "v_mfma_f32_16x16x32_bf16 v[192:195], v[32:35], v[176:179], v[192:195]\n\t"
            "s_waitcnt vmcnt(20)\n\t"
            "v_mfma_f32_16x16x32_bf16 v[192:195], v[36:39], v[180:183], v[192:195]\n\t"
            "v_mfma_f32_16x16x32_bf16 v[192:195], v[40:43], v[184:187], v[192:195]\n\t"
            "s_nop 1\n\t"
            "v_cvt_pk_bf16_f32 v232, v232, v233\n\t"
            "v_mfma_f32_16x16x32_bf16 v[192:195], v[44:47], v[188:191], v[192:195]\n\t"
            "v_cvt_pk_bf16_f32 v233, v234, v235\n\t"
            "v_mfma_f32_16x16x32_bf16 v[196:199], v[48:51], v[168:171], v[196:199]\n\t"
            "v_mfma_f32_16x16x32_bf16 v[196:199], v[52:55], v[172:175], v[196:199]\n\t"
            "v_mfma_f32_16x16x32_bf16 v[196:199], v[56:59], v[176:179], v[196:199]\n\t"
            "v_mfma_f32_16x16x32_bf16 v[196:199], v[60:63], v[180:183], v[196:199]\n\t"
            "v_mfma_f32_16x16x32_bf16 v[196:199], v[64:67], v[184:187], v[196:199]\n\t"
            "s_nop 1\n\t"
            "v_cvt_pk_bf16_f32 v234, v192, v193\n\t"
            "v_mfma_f32_16x16x32_bf16 v[196:199], v[68:71], v[188:191], v[196:199]\n\t"
            "v_cvt_pk_bf16_f32 v235, v194, v195\n\t"
            "v_lshlrev_b32 v192, 16, v220\n\t"
            "v_and_b32 v193, 0xffff0000, v220\n\t"
            "v_lshlrev_b32 v194, 16, v221\n\t"
            "v_and_b32 v195, 0xffff0000, v221\n\t"
            "buffer_store_dwordx4 v[232:235], v249, s[40:43], 0 offen sc0 sc1\n\t"
            "s_waitcnt vmcnt(15)\n\t"
            "v_mfma_f32_16x16x32_bf16 v[224:227], v[72:75], v[168:171], v[224:227]\n\t"
            "v_cvt_pk_bf16_f32 v196, v196, v197\n\t"
            "v_cvt_pk_bf16_f32 v197, v198, v199\n\t"
            "v_mfma_f32_16x16x32_bf16 v[224:227], v[76:79], v[172:175], v[224:227]\n\t"
            "v_lshlrev_b32 v232, 16, v210\n\t"
            "v_and_b32 v233, 0xffff0000, v210\n\t"
            "v_mfma_f32_16x16x32_bf16 v[224:227], v[80:83], v[176:179], v[224:227]\n\t"
            "s_waitcnt vmcnt(9)\n\t"
            "v_lshlrev_b32 v234, 16, v211\n\t"
            "v_mfma_f32_16x16x32_bf16 v[224:227], v[84:87], v[180:183], v[224:227]\n\t"
            "v_and_b32 v235, 0xffff0000, v211\n\t"
            "v_mfma_f32_16x16x32_bf16 v[224:227], v[88:91], v[184:187], v[224:227]\n\t"
            "v_mfma_f32_16x16x32_bf16 v[224:227], v[92:95], v[188:191], v[224:227]\n\t"
            "v_mfma_f32_16x16x32_bf16 v[228:231], v[96:99], v[144:147], v[228:231]\n\t"
            "v_mfma_f32_16x16x32_bf16 v[228:231], v[100:103], v[148:151], v[228:231]\n\t"
            "v_mfma_f32_16x16x32_bf16 v[228:231], v[104:107], v[152:155], v[228:231]\n\t"
            "v_mfma_f32_16x16x32_bf16 v[228:231], v[108:111], v[156:159], v[228:231]\n\t"
            "v_mfma_f32_16x16x32_bf16 v[228:231], v[112:115], v[160:163], v[228:231]\n\t"
            "v_mfma_f32_16x16x32_bf16 v[228:231], v[116:119], v[164:167], v[228:231]\n\t"
            "s_nop 1\n\t"
            "v_cvt_pk_bf16_f32 v198, v224, v225\n\t"
            "v_cvt_pk_bf16_f32 v199, v226, v227\n\t"
            "buffer_store_dwordx4 v[196:199], v249, s[40:43], 0 offen offset:64 sc0 sc1\n\t"
            "s_waitcnt vmcnt(4)\n\t"
            "v_mfma_f32_16x16x32_bf16 v[232:235], v[120:123], v[144:147], v[232:235]\n\t"
            "v_lshlrev_b32 v196, 16, v222\n\t"
            "v_and_b32 v197, 0xffff0000, v222\n\t"
            "v_mfma_f32_16x16x32_bf16 v[232:235], v[124:127], v[148:151], v[232:235]\n\t"
            "v_cvt_pk_bf16_f32 v228, v228, v229\n\t"
            "v_cvt_pk_bf16_f32 v229, v230, v231\n\t"
            "v_mfma_f32_16x16x32_bf16 v[232:235], v[128:131], v[152:155], v[232:235]\n\t"
            "s_waitcnt vmcnt(10)\n\t"
            "v_lshlrev_b32 v198, 16, v223\n\t"
            "v_mfma_f32_16x16x32_bf16 v[232:235], v[132:135], v[156:159], v[232:235]\n\t"
            "v_and_b32 v199, 0xffff0000, v223\n\t"
            "v_mfma_f32_16x16x32_bf16 v[232:235], v[136:139], v[160:163], v[232:235]\n\t"
            "v_mfma_f32_16x16x32_bf16 v[232:235], v[140:143], v[164:167], v[232:235]\n\t"
            "v_mfma_f32_16x16x32_bf16 v[192:195], v[96:99], v[168:171], v[192:195]\n\t"
            "v_mfma_f32_16x16x32_bf16 v[192:195], v[100:103], v[172:175], v[192:195]\n\t"
            "v_mfma_f32_16x16x32_bf16 v[192:195], v[104:107], v[176:179], v[192:195]\n\t"
            "v_mfma_f32_16x16x32_bf16 v[192:195], v[108:111], v[180:183], v[192:195]\n\t"
            "v_mfma_f32_16x16x32_bf16 v[192:195], v[112:115], v[184:187], v[192:195]\n\t"
            "v_mfma_f32_16x16x32_bf16 v[192:195], v[116:119], v[188:191], v[192:195]\n\t"
            "s_nop 1\n\t"
            "v_cvt_pk_bf16_f32 v230, v232, v233\n\t"
            "v_cvt_pk_bf16_f32 v231, v234, v235\n\t"
            "buffer_store_dwordx4 v[228:231], v248, s[40:43], 0 offen offset:128 sc0 sc1\n\t"
            "s_waitcnt vmcnt(5)\n\t"
            "v_mfma_f32_16x16x32_bf16 v[196:199], v[120:123], v[168:171], v[196:199]\n\t"
            "v_mfma_f32_16x16x32_bf16 v[196:199], v[124:127], v[172:175], v[196:199]\n\t"
            "v_cvt_pk_bf16_f32 v192, v192, v193\n\t"
            "v_mfma_f32_16x16x32_bf16 v[196:199], v[128:131], v[176:179], v[196:199]\n\t"
            "v_cvt_pk_bf16_f32 v193, v194, v195\n\t"
            "v_mfma_f32_16x16x32_bf16 v[196:199], v[132:135], v[180:183], v[196:199]\n\t"
            "v_mfma_f32_16x16x32_bf16 v[196:199], v[136:139], v[184:187], v[196:199]\n\t"
            "v_mfma_f32_16x16x32_bf16 v[196:199], v[140:143], v[188:191], v[196:199]\n\t"
            "s_nop 7\n\t"
            "v_cvt_pk_bf16_f32 v194, v196, v197\n\t"
            "v_cvt_pk_bf16_f32 v195, v198, v199\n\t"
            "buffer_store_dwordx4 v[192:195], v249, s[40:43], 0 offen offset:128 sc0 sc1\n\t"
            "s_nop 7\n\t"
            "s_branch .Lre%=\n\t"
            ".Lrp%=:\n\t"
            "v_add_u32 v236, s19, v252\n\t"
            "ds_write_b128 v236, v[244:247]\n\t"
            "v_add_u32 v236, s48, v244\n\t"
            "v_add_u32 v237, s48, v245\n\t"
            "v_add_u32 v248, s48, v246\n\t"
            "ds_read_b128 v[200:203], v236\n\t"
            "ds_read_b128 v[204:207], v237\n\t"
            "ds_read_b128 v[208:211], v248\n\t"
            "ds_read_b128 v[212:215], v236 offset:6144\n\t"
            "ds_read_b128 v[216:219], v237 offset:6144\n\t"
            "ds_read_b128 v[220:223], v248 offset:6144\n\t"
            "ds_read_b128 v[224:227], v236 offset:12288\n\t"
            "ds_read_b128 v[228:231], v237 offset:12288\n\t"
            "ds_read_b128 v[232:235], v248 offset:12288\n\t"
            "s_waitcnt lgkmcnt(0)\n\t"
            "s_barrier\n\t"
            "s_add_u32 s45, s46, s27\n\t"
            "s_mov_b32 s46, s47\n\t"
            "s_mov_b32 s47, s68\n\t"
            "s_xor_b32 s48, s48, s71\n\t"
            "s_xor_b32 s67, s67, s71\n\t"
            "s_add_u32 s76, s76, 4\n\t"
            "s_lshl_b32 s56, s56, 2\n\t"
            "s_lshr_b32 s49, s26, 30\n\t"
            "s_or_b32 s56, s56, s49\n\t"
            "s_add_u32 s49, s33, 0\n\t"
            "buffer_load_dwordx4 v[0:3], v247, s[28:31], s49 offen sc1\n\t"
            "s_add_u32 s49, s33, 1024\n\t"
            "buffer_load_dwordx4 v[4:7], v247, s[28:31], s49 offen sc1\n\t"
            "s_add_u32 s49, s33, 2048\n\t"
            "buffer_load_dwordx4 v[8:11], v247, s[28:31], s49 offen sc1\n\t"
            "s_add_u32 s49, s33, 3072\n\t"
            "buffer_load_dwordx4 v[12:15], v247, s[28:31], s49 offen sc1\n\t"
            "s_add_u32 s49, s33, 4096\n\t"
            "buffer_load_dwordx4 v[16:19], v247, s[28:31], s49 offen sc1\n\t"
            "s_add_u32 s49, s33, 5120\n\t"
            "buffer_load_dwordx4 v[20:23], v247, s[28:31], s49 offen sc1\n\t"
            "s_add_u32 s49, s33, 6144\n\t"
            "buffer_load_dwordx4 v[24:27], v247, s[28:31], s49 offen sc1\n\t"
            "s_add_u32 s49, s33, 7168\n\t"
            "buffer_load_dwordx4 v[28:31], v247, s[28:31], s49 offen sc1\n\t"
            "s_add_u32 s49, s33, 8192\n\t"
            "buffer_load_dwordx4 v[32:35], v247, s[28:31], s49 offen sc1\n\t"
            "s_add_u32 s49, s33, 9216\n\t"
            "buffer_load_dwordx4 v[36:39], v247, s[28:31], s49 offen sc1\n\t"
            "s_add_u32 s49, s33, 10240\n\t"
            "buffer_load_dwordx4 v[40:43], v247, s[28:31], s49 offen sc1\n\t"
            "s_add_u32 s49, s33, 11264\n\t"
            "buffer_load_dwordx4 v[44:47], v247, s[28:31], s49 offen sc1\n\t"
            "s_add_u32 s49, s33, 12288\n\t"
            "buffer_load_dwordx4 v[48:51], v247, s[28:31], s49 offen sc1\n\t"
            "s_add_u32 s49, s33, 13312\n\t"
            "buffer_load_dwordx4 v[52:55], v247, s[28:31], s49 offen sc1\n\t"
            "s_add_u32 s49, s33, 14336\n\t"
            "buffer_load_dwordx4 v[56:59], v247, s[28:31], s49 offen sc1\n\t"
            "s_add_u32 s49, s33, 15360\n\t"
            "buffer_load_dwordx4 v[60:63], v247, s[28:31], s49 offen sc1\n\t"
            "s_add_u32 s49, s33, 16384\n\t"
            "buffer_load_dwordx4 v[64:67], v247, s[28:31], s49 offen sc1\n\t"
            "s_add_u32 s49, s33, 17408\n\t"
            "buffer_load_dwordx4 v[68:71], v247, s[28:31], s49 offen sc1\n\t"
            "ds_read_b128 v[168:171], v254\n\t"
            "ds_read_b128 v[172:175], v254 offset:64\n\t"
            "s_waitcnt vmcnt(0)\n\t"
            "ds_read_b64 v[144:145], v253\n\t"
            "ds_read_b64 v[146:147], v253 offset:96\n\t"
            "ds_read_b64 v[148:149], v253 offset:1744\n\t"
            "ds_read_b64 v[150:151], v253 offset:1840\n\t"
            "ds_read_b64 v[152:153], v253 offset:8\n\t"
            "ds_read_b64 v[154:155], v253 offset:104\n\t"
            "ds_read_b64 v[156:157], v253 offset:1752\n\t"
            "ds_read_b64 v[158:159], v253 offset:1848\n\t"
            "ds_read_b64 v[160:161], v253 offset:16\n\t"
            "ds_read_b64 v[162:163], v253 offset:112\n\t"
            "ds_read_b64 v[164:165], v253 offset:1760\n\t"
            "ds_read_b64 v[166:167], v253 offset:1856\n\t"
            "s_waitcnt lgkmcnt(0)\n\t"
            "s_nop 7\n\t"
            ".p2align 6\n\t"
            ".Lwb_loop%=:\n\t"
            "s_setprio 3\n\t"
            "v_mov_b64 v[176:177], 0\n\t"
            "v_mov_b64 v[178:179], 0\n\t"
            "v_mov_b64 v[180:181], 0\n\t"
            "v_mov_b64 v[182:183], 0\n\t"
            "v_mov_b32 v249, s76\n\t"
            "v_mov_b64 v[190:191], 0\n\t"
            "s_waitcnt lgkmcnt(0)\n\t"
            "v_smfmac_f32_16x16x64_bf16 v[176:179], v[168:171], v[144:151], v255\n\t"
            "v_smfmac_f32_16x16x64_bf16 v[180:183], v[172:175], v[152:159], v255\n\t"
            "ds_read_b128 v[172:175], v254 offset:192\n\t"
            "v_mov_b64 v[184:185], 0\n\t"
            "v_mov_b64 v[186:187], 0\n\t"
            "v_mov_b64 v[188:189], 0\n\t"
            "v_add_u32 v236, s67, v250\n\t"
            "v_add_u32 v248, s67, v251\n\t"
            "ds_read_b64 v[146:147], v253 offset:120\n\t"
            "ds_read_b32 v249, v249\n\t"
            "v_cvt_pk_bf16_f32 v176, v176, v177\n\t"
            "v_cvt_pk_bf16_f32 v177, v178, v179\n\t"
            "v_cvt_pk_bf16_f32 v179, v182, v183\n\t"
            "v_cvt_pk_bf16_f32 v178, v180, v181\n\t"
            "v_mov_b64 v[180:181], 0\n\t"
            "s_barrier\n\t"
            "v_mov_b64 v[182:183], 0\n\t"
            "ds_read_b64 v[150:151], v253 offset:1864\n\t"
            "ds_read_b64 v[148:149], v253 offset:1768\n\t"
            "ds_read_b128 v[168:171], v254 offset:128\n\t"
            "ds_read_b64 v[144:145], v253 offset:24\n\t"
            "ds_read_b64 v[156:157], v253 offset:1776\n\t"
            "ds_read_b64 v[152:153], v253 offset:32\n\t"
            "ds_read_b64 v[158:159], v253 offset:1872\n\t"
            "ds_read_b64 v[154:155], v253 offset:128\n\t"
            "s_waitcnt lgkmcnt(8)\n\t"
            "v_readfirstlane_b32 s26, v249\n\t"
            "ds_read_b32 v249, v252 offset:1024\n\t"
            "s_and_b32 s68, s26, 0x7ffffff\n\t"
            "s_add_u32 s68, s68, s78\n\t"
            "s_and_b32 s69, s26, 0x38000000\n\t"
            "s_or_b32 s69, s69, s39\n\t"
            "s_bitcmp1_b32 s26, 27\n\t"
            "s_cselect_b32 s79, 2.0, s68\n\t"
            "s_and_b32 s57, s56, 3\n\t"
            "s_lshl_b32 s58, s57, 3\n\t"
            "s_lshr_b32 m0, s54, s58\n\t"
            "s_lshl_b32 s58, s57, 4\n\t"
            "s_lshr_b64 s[36:37], s[50:51], s58\n\t"
            "s_and_b32 s57, s36, 0xffff\n\t"
            "s_add_u32 s57, s57, s67\n\t"
            "s_waitcnt lgkmcnt(5)\n\t"
            "v_smfmac_f32_16x16x64_bf16 v[184:187], v[168:171], v[160:167], v255\n\t"
            "v_smfmac_f32_16x16x64_bf16 v[188:191], v[172:175], v[144:151], v255\n\t"
            "s_lshr_b64 s[36:37], s[52:53], s58\n\t"
            "s_and_b32 s58, s36, 0xffff\n\t"
            "s_add_u32 s58, s58, s67\n\t"
            "ds_read_b64 v[162:163], v253 offset:136\n\t"
            "s_and_b32 s59, s50, 0xffff\n\t"
            "s_add_u32 s59, s59, s67\n\t"
            "s_and_b32 s38, s52, 0xffff\n\t"
            "s_add_u32 s38, s38, s67\n\t"
            "v_mfma_f32_4x4x4_16b_bf16 v[196:199], v[176:177], v[176:177], 0\n\t"
            "v_mfma_f32_4x4x4_16b_bf16 v[192:195], v[238:239], v[176:177], 0\n\t"
            "s_nop 0\n\t"
            "v_mfma_f32_4x4x4_16b_bf16 v[196:199], v[178:179], v[178:179], v[196:199]\n\t"
            "v_mfma_f32_4x4x4_16b_bf16 v[192:195], v[238:239], v[178:179], v[192:195]\n\t"
            "ds_read_b64 v[164:165], v253 offset:1784\n\t"
            "v_writelane_b32 v236, s59, m0\n\t"
            "v_writelane_b32 v236, s57, 63\n\t"
            "v_cvt_pk_bf16_f32 v184, v184, v185\n\t"
            "v_cvt_pk_bf16_f32 v185, v186, v187\n\t"
            "v_cvt_pk_bf16_f32 v186, v188, v189\n\t"
            "v_cvt_pk_bf16_f32 v187, v190, v191\n\t"
            "v_xor_b32 v237, 16, v236\n\t"
            "v_mov_b64 v[188:189], 0\n\t"
            "v_mov_b64 v[190:191], 0\n\t"
            "s_waitcnt lgkmcnt(2)\n\t"
            "v_and_b32 v249, s69, v249\n\t"
            "v_writelane_b32 v248, s38, m0\n\t"
            "v_writelane_b32 v248, s58, 63\n\t"
            "ds_read_b128 v[168:171], v254 offset:256\n\t"
            "ds_read_b128 v[172:175], v254 offset:320\n\t"
            "ds_read_b64 v[160:161], v253 offset:40\n\t"
            "ds_read_b64 v[166:167], v253 offset:1880\n\t"
            "ds_read_b64 v[148:149], v253 offset:1792\n\t"
            "ds_read_b64 v[144:145], v253 offset:48\n\t"
            "ds_read_b64 v[150:151], v253 offset:1888\n\t"
            "ds_read_b64 v[146:147], v253 offset:144\n\t"
            "ds_write_b128 v236, v[176:179]\n\t"
            "v_mov_b64 v[178:179], 0\n\t"
            "v_mov_b64 v[176:177], 0\n\t"
            "ds_write_b128 v237, v[184:187]\n\t"
            "s_waitcnt lgkmcnt(6)\n\t"
            "v_smfmac_f32_16x16x64_bf16 v[176:179], v[168:171], v[152:159], v255\n\t"
            "v_smfmac_f32_16x16x64_bf16 v[180:183], v[172:175], v[160:167], v255\n\t"
            "ds_read_b64 v[158:159], v253 offset:1896\n\t"
            "ds_read_b64 v[154:155], v253 offset:152\n\t"
            "v_mfma_f32_4x4x4_16b_bf16 v[196:199], v[184:185], v[184:185], v[196:199]\n\t"
            "v_mfma_f32_4x4x4_16b_bf16 v[192:195], v[238:239], v[184:185], v[192:195]\n\t"
            "s_nop 0\n\t"
            "v_mfma_f32_4x4x4_16b_bf16 v[196:199], v[186:187], v[186:187], v[196:199]\n\t"
            "v_mfma_f32_4x4x4_16b_bf16 v[192:195], v[238:239], v[186:187], v[192:195]\n\t"
            "ds_read_b64 v[152:153], v253 offset:56\n\t"
            "v_mov_b64 v[186:187], 0\n\t"
            "v_mov_b64 v[184:185], 0\n\t"
            "v_cvt_pk_bf16_f32 v176, v176, v177\n\t"
            "v_cvt_pk_bf16_f32 v177, v178, v179\n\t"
            "v_cvt_pk_bf16_f32 v178, v180, v181\n\t"
            "v_cvt_pk_bf16_f32 v179, v182, v183\n\t"
            "v_mov_b64 v[180:181], 0\n\t"
            "v_mov_b64 v[182:183], 0\n\t"
            "ds_read_b128 v[172:175], v254 offset:448\n\t"
            "ds_read_b128 v[168:171], v254 offset:384\n\t"
            "ds_read_b64 v[156:157], v253 offset:1800\n\t"
            "ds_read_b64 v[160:161], v253 offset:64\n\t"
            "ds_read_b64 v[162:163], v253 offset:160\n\t"
            "ds_read_b64 v[166:167], v253 offset:1904\n\t"
            "s_waitcnt lgkmcnt(14)\n\t"
            "ds_read_b64 v[164:165], v253 offset:1808\n\t"
            "s_waitcnt lgkmcnt(14)\n\t"
            "ds_write_b128 v236, v[176:179] offset:128\n\t"
            "s_waitcnt lgkmcnt(5)\n\t"
            "v_smfmac_f32_16x16x64_bf16 v[184:187], v[168:171], v[144:151], v255\n\t"
            "v_smfmac_f32_16x16x64_bf16 v[188:191], v[172:175], v[152:159], v255\n\t"
            "ds_read_b64 v[144:145], v253 offset:72\n\t"
            "ds_read_b64 v[148:149], v253 offset:1816\n\t"
            "v_mfma_f32_4x4x4_16b_bf16 v[196:199], v[176:177], v[176:177], v[196:199]\n\t"
            "v_mfma_f32_4x4x4_16b_bf16 v[192:195], v[238:239], v[176:177], v[192:195]\n\t"
            "s_nop 0\n\t"
            "v_mfma_f32_4x4x4_16b_bf16 v[196:199], v[178:179], v[178:179], v[196:199]\n\t"
            "v_mfma_f32_4x4x4_16b_bf16 v[192:195], v[238:239], v[178:179], v[192:195]\n\t"
            "ds_read_b128 v[168:171], v254 offset:512\n\t"
            "v_mov_b64 v[176:177], 0\n\t"
            "v_mov_b64 v[178:179], 0\n\t"
            "v_cvt_pk_bf16_f32 v184, v184, v185\n\t"
            "v_cvt_pk_bf16_f32 v185, v186, v187\n\t"
            "v_cvt_pk_bf16_f32 v186, v188, v189\n\t"
            "v_cvt_pk_bf16_f32 v187, v190, v191\n\t"
            "v_mov_b64 v[190:191], 0\n\t"
            "v_mov_b64 v[188:189], 0\n\t"
            "ds_read_b128 v[172:175], v254 offset:576\n\t"
            "ds_read_b64 v[146:147], v253 offset:168\n\t"
            "ds_read_b64 v[150:151], v253 offset:1912\n\t"
            "ds_read_b64 v[152:153], v253 offset:80\n\t"
            "ds_read_b64 v[158:159], v253 offset:1920\n\t"
            "ds_read_b64 v[156:157], v253 offset:1824\n\t"
            "ds_read_b64 v[154:155], v253 offset:176\n\t"
            "s_waitcnt lgkmcnt(14)\n\t"
            "ds_write_b128 v237, v[184:187] offset:128\n\t"
            "s_waitcnt lgkmcnt(5)\n\t"
            "v_smfmac_f32_16x16x64_bf16 v[176:179], v[168:171], v[160:167], v255\n\t"
            "v_smfmac_f32_16x16x64_bf16 v[180:183], v[172:175], v[144:151], v255\n\t"
            "ds_read_b64 v[160:161], v253 offset:88\n\t"
            "ds_read_b64 v[164:165], v253 offset:1832\n\t"
            "v_mfma_f32_4x4x4_16b_bf16 v[196:199], v[184:185], v[184:185], v[196:199]\n\t"
            "v_mfma_f32_4x4x4_16b_bf16 v[192:195], v[238:239], v[184:185], v[192:195]\n\t"
            "s_nop 0\n\t"
            "v_mfma_f32_4x4x4_16b_bf16 v[196:199], v[186:187], v[186:187], v[196:199]\n\t"
            "v_mfma_f32_4x4x4_16b_bf16 v[192:195], v[238:239], v[186:187], v[192:195]\n\t"
            "ds_read_b128 v[172:175], v254 offset:704\n\t"
            "v_mov_b64 v[184:185], 0\n\t"
            "v_mov_b64 v[186:187], 0\n\t"
            "v_cvt_pk_bf16_f32 v176, v176, v177\n\t"
            "v_cvt_pk_bf16_f32 v177, v178, v179\n\t"
            "v_cvt_pk_bf16_f32 v179, v182, v183\n\t"
            "v_cvt_pk_bf16_f32 v178, v180, v181\n\t"
            "v_and_b32 v183, 0xffff0000, v203\n\t"
            "v_and_b32 v181, 0xffff0000, v202\n\t"
            "v_lshlrev_b32 v182, 16, v203\n\t"
            "v_lshlrev_b32 v180, 16, v202\n\t"
            "ds_read_b128 v[168:171], v254 offset:640\n\t"
            "ds_read_b64 v[162:163], v253 offset:184\n\t"
            "ds_read_b64 v[166:167], v253 offset:1928\n\t"
            "ds_read_b128 v[144:147], v242\n\t"
            "ds_read_b128 v[148:151], v243\n\t"
            "ds_write_b128 v236, v[176:179] offset:256\n\t"
            "v_add_u32 v236, s48, v244\n\t"
            "s_waitcnt lgkmcnt(3)\n\t"
            "v_smfmac_f32_16x16x64_bf16 v[184:187], v[168:171], v[152:159], v255\n\t"
            "v_smfmac_f32_16x16x64_bf16 v[188:191], v[172:175], v[160:167], v255\n\t"
            "ds_read_b128 v[152:155], v242 offset:128\n\t"
            "ds_read_b128 v[160:163], v242 offset:256\n\t"
            "v_mfma_f32_4x4x4_16b_bf16 v[196:199], v[176:177], v[176:177], v[196:199]\n\t"
            "v_mfma_f32_4x4x4_16b_bf16 v[192:195], v[238:239], v[176:177], v[192:195]\n\t"
            "s_nop 0\n\t"
            "v_mfma_f32_4x4x4_16b_bf16 v[196:199], v[178:179], v[178:179], v[196:199]\n\t"
            "v_mfma_f32_4x4x4_16b_bf16 v[192:195], v[238:239], v[178:179], v[192:195]\n\t"
            "ds_read_b128 v[156:159], v243 offset:128\n\t"
            "v_cvt_pk_bf16_f32 v184, v184, v185\n\t"
            "v_cvt_pk_bf16_f32 v185, v186, v187\n\t"
            "v_cvt_pk_bf16_f32 v186, v188, v189\n\t"
            "v_lshlrev_b32 v176, 16, v200\n\t"
            "v_cvt_pk_bf16_f32 v187, v190, v191\n\t"
            "v_lshlrev_b32 v178, 16, v201\n\t"
            "v_and_b32 v179, 0xffff0000, v201\n\t"
            "v_mfma_f32_4x4x4_16b_bf16 v[196:199], v[184:185], v[184:185], v[196:199]\n\t"
            "v_mfma_f32_4x4x4_16b_bf16 v[192:195], v[238:239], v[184:185], v[192:195]\n\t"
            "s_nop 0\n\t"
            "v_mfma_f32_4x4x4_16b_bf16 v[196:199], v[186:187], v[186:187], v[196:199]\n\t"
            "v_mfma_f32_4x4x4_16b_bf16 v[192:195], v[238:239], v[186:187], v[192:195]\n\t"
            "ds_read_b128 v[164:167], v243 offset:256\n\t"
            "v_and_b32 v177, 0xffff0000, v200\n\t"
            "v_lshlrev_b32 v190, 16, v207\n\t"
            "v_and_b32 v191, 0xffff0000, v207\n\t"
            "s_waitcnt lgkmcnt(6)\n\t"
            "v_mfma_f32_16x16x32_bf16 v[176:179], v[0:3], v[144:147], v[176:179]\n\t"
            "s_setprio 3\n\t"
            "ds_read_b128 v[168:171], v242 offset:6144\n\t"
            "v_and_b32 v189, 0xffff0000, v206\n\t"
            "s_waitcnt lgkmcnt(6)\n\t"
            "v_mfma_f32_16x16x32_bf16 v[176:179], v[4:7], v[148:151], v[176:179]\n\t"
            "s_waitcnt lgkmcnt(4)\n\t"
            "ds_read_b128 v[172:175], v243 offset:6144\n\t"
            "v_cndmask_b32_e64 v193, v196, v197, s[60:61]\n\t"
            "v_mfma_f32_16x16x32_bf16 v[176:179], v[8:11], v[152:155], v[176:179]\n\t"
            "s_mov_b32 exec_hi, 0x3fffff\n\t"
            "s_add_u32 m0, s66, 0\n\t"
            "s_add_u32 s49, s79, 0\n\t"
            "buffer_load_dwordx4 v249, s[72:75], s49 offen lds\n\t"
            "s_mov_b32 exec_hi, -1\n\t"
            "s_mov_b32 exec_hi, 0x3fffff\n\t"
            "s_add_u32 m0, s66, 872\n\t"
            "s_add_u32 s49, s68, 196224\n\t"
            "buffer_load_dwordx4 v249, s[72:75], s49 offen lds\n\t"
            "s_mov_b32 exec_hi, -1\n\t"
            "ds_write_b128 v237, v[184:187] offset:256\n\t"
            "s_waitcnt lgkmcnt(4)\n\t"
            "v_mfma_f32_16x16x32_bf16 v[176:179], v[12:15], v[156:159], v[176:179]\n\t"
            "ds_read_b128 v[200:203], v236\n\t"
            "v_and_b32 v185, 0xffff0000, v204\n\t"
            "v_mfma_f32_16x16x32_bf16 v[176:179], v[16:19], v[160:163], v[176:179]\n\t"
            "s_waitcnt lgkmcnt(4)\n\t"
            "v_lshlrev_b32 v184, 16, v204\n\t"
            "v_lshlrev_b32 v186, 16, v205\n\t"
            "v_mfma_f32_16x16x32_bf16 v[176:179], v[20:23], v[164:167], v[176:179]\n\t"
            "s_mov_b32 exec_hi, 0x3fffff\n\t"
            "s_add_u32 m0, s66, 1744\n\t"
            "s_add_u32 s49, s68, 392448\n\t"
            "buffer_load_dwordx4 v249, s[72:75], s49 offen lds\n\t"
            "s_mov_b32 exec_hi, -1\n\t"
            "s_mov_b32 exec_hi, 0x3fffff\n\t"
            "s_add_u32 m0, s66, 2616\n\t"
            "s_add_u32 s49, s68, 588672\n\t"
            "buffer_load_dwordx4 v249, s[72:75], s49 offen lds\n\t"
            "s_mov_b32 exec_hi, -1\n\t"
            "v_mfma_f32_16x16x32_bf16 v[180:183], v[24:27], v[144:147], v[180:183]\n\t"
            "v_and_b32 v187, 0xffff0000, v205\n\t"
            "v_lshlrev_b32 v188, 16, v206\n\t"
            "v_mfma_f32_16x16x32_bf16 v[180:183], v[28:31], v[148:151], v[180:183]\n\t"
            "v_cndmask_b32_e64 v193, v193, v198, s[62:63]\n\t"
            "v_cndmask_b32_e64 v193, v193, v199, s[64:65]\n\t"
            "v_mfma_f32_16x16x32_bf16 v[180:183], v[32:35], v[152:155], v[180:183]\n\t"
            "ds_read_b128 v[196:199], v252\n\t"
            "v_lshlrev_b32 v194, 16, v209\n\t"
            "v_mfma_f32_16x16x32_bf16 v[180:183], v[36:39], v[156:159], v[180:183]\n\t"
            "v_and_b32 v195, 0xffff0000, v209\n\t"
            "v_cvt_pk_bf16_f32 v176, v176, v177\n\t"
            "v_mfma_f32_16x16x32_bf16 v[180:183], v[40:43], v[160:163], v[180:183]\n\t"
            "v_cvt_pk_bf16_f32 v177, v178, v179\n\t"
            "v_add_u32 v237, s48, v245\n\t"
            "v_mfma_f32_16x16x32_bf16 v[180:183], v[44:47], v[164:167], v[180:183]\n\t"
            "s_mov_b32 exec_hi, 0x3fffff\n\t"
            "s_add_u32 m0, s66, 3488\n\t"
            "s_add_u32 s49, s68, 784896\n\t"
            "buffer_load_dwordx4 v249, s[72:75], s49 offen lds\n\t"
            "s_mov_b32 exec_hi, -1\n\t"
            "s_mov_b32 exec_hi, 0x3fffff\n\t"
            "s_add_u32 m0, s66, 4360\n\t"
            "s_add_u32 s49, s68, 981120\n\t"
            "buffer_load_dwordx4 v249, s[72:75], s49 offen lds\n\t"
            "s_mov_b32 exec_hi, -1\n\t"
            "ds_write_b64 v248, v[192:193] offset:18816\n\t"
            "v_mfma_f32_16x16x32_bf16 v[184:187], v[48:51], v[144:147], v[184:187]\n\t"
            "ds_read_b128 v[204:207], v237\n\t"
            "v_lshlrev_b32 v192, 16, v208\n\t"
            "v_mfma_f32_16x16x32_bf16 v[184:187], v[52:55], v[148:151], v[184:187]\n\t"
            "v_and_b32 v193, 0xffff0000, v208\n\t"
            "v_cvt_pk_bf16_f32 v179, v182, v183\n\t"
            "v_mfma_f32_16x16x32_bf16 v[184:187], v[56:59], v[152:155], v[184:187]\n\t"
            "v_cvt_pk_bf16_f32 v178, v180, v181\n\t"
            "v_and_b32 v183, 0xffff0000, v213\n\t"
            "v_mfma_f32_16x16x32_bf16 v[184:187], v[60:63], v[156:159], v[184:187]\n\t"
            "v_and_b32 v181, 0xffff0000, v212\n\t"
            "v_lshlrev_b32 v182, 16, v213\n\t"
            "v_mfma_f32_16x16x32_bf16 v[184:187], v[64:67], v[160:163], v[184:187]\n\t"
            "v_lshlrev_b32 v180, 16, v212\n\t"
            "v_add_u32 v248, s48, v246\n\t"
            "v_mfma_f32_16x16x32_bf16 v[184:187], v[68:71], v[164:167], v[184:187]\n\t"
            "s_mov_b32 exec_hi, 0x3fffff\n\t"
            "s_add_u32 m0, s66, 5232\n\t"
            "s_add_u32 s49, s68, 1177344\n\t"
            "buffer_load_dwordx4 v249, s[72:75], s49 offen lds\n\t"
            "s_mov_b32 exec_hi, -1\n\t"
            "s_mov_b32 exec_hi, 0x3fffff\n\t"
            "s_add_u32 m0, s66, 6104\n\t"
            "s_add_u32 s49, s68, 1373568\n\t"
            "buffer_load_dwordx4 v249, s[72:75], s49 offen lds\n\t"
            "s_mov_b32 exec_hi, -1\n\t"
            "v_mfma_f32_16x16x32_bf16 v[188:191], v[72:75], v[144:147], v[188:191]\n\t"
            "v_cvt_pk_bf16_f32 v184, v184, v185\n\t"
            "v_cvt_pk_bf16_f32 v185, v186, v187\n\t"
            "v_mfma_f32_16x16x32_bf16 v[188:191], v[76:79], v[148:151], v[188:191]\n\t"
            "v_mfma_f32_16x16x32_bf16 v[188:191], v[80:83], v[152:155], v[188:191]\n\t"
            "v_mfma_f32_16x16x32_bf16 v[188:191], v[84:87], v[156:159], v[188:191]\n\t"
            "v_mfma_f32_16x16x32_bf16 v[188:191], v[88:91], v[160:163], v[188:191]\n\t"
            "v_mfma_f32_16x16x32_bf16 v[188:191], v[92:95], v[164:167], v[188:191]\n\t"
            "s_mov_b32 exec_hi, 0x3fffff\n\t"
            "s_add_u32 m0, s66, 6976\n\t"
            "s_add_u32 s49, s68, 1569792\n\t"
            "buffer_load_dwordx4 v249, s[72:75], s49 offen lds\n\t"
            "s_mov_b32 exec_hi, -1\n\t"
            "v_mfma_f32_16x16x32_bf16 v[192:195], v[96:99], v[144:147], v[192:195]\n\t"
            "v_mov_b32 v249, s76\n\t"
            "v_mfma_f32_16x16x32_bf16 v[192:195], v[100:103], v[148:151], v[192:195]\n\t"
            "ds_read_b32 v249, v249 offset:4\n\t"
            "v_cvt_pk_bf16_f32 v186, v188, v189\n\t"
            "v_mfma_f32_16x16x32_bf16 v[192:195], v[104:107], v[152:155], v[192:195]\n\t"
            "v_cvt_pk_bf16_f32 v187, v190, v191\n\t"
            "v_and_b32 v191, 0xffff0000, v217\n\t"
            "v_mfma_f32_16x16x32_bf16 v[192:195], v[108:111], v[156:159], v[192:195]\n\t"
            "v_and_b32 v189, 0xffff0000, v216\n\t"
            "v_lshlrev_b32 v190, 16, v217\n\t"
            "v_mfma_f32_16x16x32_bf16 v[192:195], v[112:115], v[160:163], v[192:195]\n\t"
            "v_lshlrev_b32 v188, 16, v216\n\t"
            "v_mfma_f32_16x16x32_bf16 v[192:195], v[116:119], v[164:167], v[192:195]\n\t"
            "s_bfe_u32 s57, s56, 0x20004\n\t"
            "s_lshl_b32 s58, s57, 3\n\t"
            "s_lshr_b32 s58, s55, s58\n\t"
            "s_lshl_b64 s[36:37], s[34:35], s58\n\t"
            "s_bitcmp1_b32 s57, 1\n\t"
            "s_cselect_b32 s58, 1177344, 0\n\t"
            "s_bitcmp1_b32 s57, 0\n\t"
            "s_cselect_b32 s57, 2304, 0\n\t"
            "s_add_u32 s57, s57, s58\n\t"
            "s_waitcnt lgkmcnt(3)\n\t"
            "s_mov_b64 exec, s[36:37]\n\t"
            "v_add_u32 v196, s57, v196\n\t"
            "s_mov_b64 exec, -1\n\t"
            "buffer_store_dwordx4 v[176:179], v196, s[40:43], s45 offen sc0 sc1\n\t"
            "buffer_store_dwordx4 v[184:187], v196, s[40:43], s45 offen offset:64 sc0 sc1\n\t"
            "v_cvt_pk_bf16_f32 v192, v192, v193\n\t"
            "v_and_b32 v179, 0xffff0000, v211\n\t"
            "v_lshlrev_b32 v176, 16, v210\n\t"
            "v_lshlrev_b32 v178, 16, v211\n\t"
            "v_and_b32 v177, 0xffff0000, v210\n\t"
            "v_and_b32 v185, 0xffff0000, v214\n\t"
            "v_and_b32 v187, 0xffff0000, v215\n\t"
            "v_mfma_f32_16x16x32_bf16 v[176:179], v[120:123], v[144:147], v[176:179]\n\t"
            "ds_read_b128 v[144:147], v242 offset:6272\n\t"
            "v_lshlrev_b32 v184, 16, v214\n\t"
            "v_mfma_f32_16x16x32_bf16 v[176:179], v[124:127], v[148:151], v[176:179]\n\t"
            "ds_read_b128 v[148:151], v243 offset:6272\n\t"
            "v_lshlrev_b32 v186, 16, v215\n\t"
            "v_mfma_f32_16x16x32_bf16 v[176:179], v[128:131], v[152:155], v[176:179]\n\t"
            "ds_read_b128 v[152:155], v242 offset:6400\n\t"
            "v_cvt_pk_bf16_f32 v193, v194, v195\n\t"
            "v_mfma_f32_16x16x32_bf16 v[176:179], v[132:135], v[156:159], v[176:179]\n\t"
            "s_waitcnt lgkmcnt(3)\n\t"
            "ds_read_b128 v[156:159], v243 offset:6400\n\t"
            "v_readfirstlane_b32 s44, v249\n\t"
            "v_mfma_f32_16x16x32_bf16 v[176:179], v[136:139], v[160:163], v[176:179]\n\t"
            "ds_read_b128 v[160:163], v242 offset:12288\n\t"
            "v_mfma_f32_16x16x32_bf16 v[176:179], v[140:143], v[164:167], v[176:179]\n\t"
            "s_and_b32 s70, s44, 0x7ffffff\n\t"
            "s_add_u32 s70, s70, s77\n\t"
            "s_and_b32 s44, s44, 0x08000000\n\t"
            "s_or_b32 s44, s44, s39\n\t"
            "v_mfma_f32_16x16x32_bf16 v[180:183], v[0:3], v[168:171], v[180:183]\n\t"
            "ds_read_b128 v[164:167], v243 offset:12288\n\t"
            "v_mfma_f32_16x16x32_bf16 v[180:183], v[4:7], v[172:175], v[180:183]\n\t"
            "s_waitcnt lgkmcnt(5)\n\t"
            "ds_read_b32 v249, v252 offset:1028\n\t"
            "v_cvt_pk_bf16_f32 v195, v178, v179\n\t"
            "v_mfma_f32_16x16x32_bf16 v[180:183], v[8:11], v[144:147], v[180:183]\n\t"
            "s_waitcnt lgkmcnt(5)\n\t"
            "ds_read_b128 v[208:211], v248\n\t"
            "v_cvt_pk_bf16_f32 v194, v176, v177\n\t"
            "v_mfma_f32_16x16x32_bf16 v[180:183], v[12:15], v[148:151], v[180:183]\n\t"
            "s_waitcnt lgkmcnt(5)\n\t"
            "ds_read_b128 v[212:215], v236 offset:6144\n\t"
            "v_lshlrev_b32 v176, 16, v220\n\t"
            "v_mfma_f32_16x16x32_bf16 v[180:183], v[16:19], v[152:155], v[180:183]\n\t"
            "s_waitcnt lgkmcnt(5)\n\t"
            "v_lshlrev_b32 v178, 16, v221\n\t"
            "v_and_b32 v179, 0xffff0000, v221\n\t"
            "v_mfma_f32_16x16x32_bf16 v[180:183], v[20:23], v[156:159], v[180:183]\n\t"
            "buffer_store_dwordx4 v[192:195], v196, s[40:43], s45 offen offset:128 sc0 sc1\n\t"
            "v_mfma_f32_16x16x32_bf16 v[184:187], v[24:27], v[168:171], v[184:187]\n\t"
            "s_nop 0\n\t"
            "v_and_b32 v195, 0xffff0000, v219\n\t"
            "v_and_b32 v193, 0xffff0000, v218\n\t"
            "v_mfma_f32_16x16x32_bf16 v[184:187], v[28:31], v[172:175], v[184:187]\n\t"
            "v_lshlrev_b32 v192, 16, v218\n\t"
            "v_lshlrev_b32 v194, 16, v219\n\t"
            "v_mfma_f32_16x16x32_bf16 v[184:187], v[32:35], v[144:147], v[184:187]\n\t"
            "ds_read_b128 v[216:219], v237 offset:6144\n\t"
            "v_and_b32 v177, 0xffff0000, v220\n\t"
            "v_mfma_f32_16x16x32_bf16 v[184:187], v[36:39], v[148:151], v[184:187]\n\t"
            "v_cvt_pk_bf16_f32 v180, v180, v181\n\t"
            "v_cvt_pk_bf16_f32 v181, v182, v183\n\t"
            "v_mfma_f32_16x16x32_bf16 v[184:187], v[40:43], v[152:155], v[184:187]\n\t"
            "s_waitcnt lgkmcnt(3)\n\t"
            "v_and_b32 v249, s44, v249\n\t"
            "v_mfma_f32_16x16x32_bf16 v[184:187], v[44:47], v[156:159], v[184:187]\n\t"
            "v_mfma_f32_16x16x32_bf16 v[188:191], v[48:51], v[168:171], v[188:191]\n\t"
            "v_mfma_f32_16x16x32_bf16 v[188:191], v[52:55], v[172:175], v[188:191]\n\t"
            "v_mfma_f32_16x16x32_bf16 v[188:191], v[56:59], v[144:147], v[188:191]\n\t"
            "v_mfma_f32_16x16x32_bf16 v[188:191], v[60:63], v[148:151], v[188:191]\n\t"
            "v_mfma_f32_16x16x32_bf16 v[188:191], v[64:67], v[152:155], v[188:191]\n\t"
            "v_mfma_f32_16x16x32_bf16 v[188:191], v[68:71], v[156:159], v[188:191]\n\t"
            "s_nop 1\n\t"
            "v_cvt_pk_bf16_f32 v183, v186, v187\n\t"
            "v_mfma_f32_16x16x32_bf16 v[192:195], v[72:75], v[168:171], v[192:195]\n\t"
            "v_cvt_pk_bf16_f32 v182, v184, v185\n\t"
            "v_and_b32 v185, 0xffff0000, v224\n\t"
            "v_mfma_f32_16x16x32_bf16 v[192:195], v[76:79], v[172:175], v[192:195]\n\t"
            "v_lshlrev_b32 v186, 16, v225\n\t"
            "v_lshlrev_b32 v184, 16, v224\n\t"
            "v_mfma_f32_16x16x32_bf16 v[192:195], v[80:83], v[144:147], v[192:195]\n\t"
            "v_and_b32 v187, 0xffff0000, v225\n\t"
            "v_cvt_pk_bf16_f32 v188, v188, v189\n\t"
            "v_mfma_f32_16x16x32_bf16 v[192:195], v[84:87], v[148:151], v[192:195]\n\t"
            "v_cvt_pk_bf16_f32 v189, v190, v191\n\t"
            "v_mfma_f32_16x16x32_bf16 v[192:195], v[88:91], v[152:155], v[192:195]\n\t"
            "v_mfma_f32_16x16x32_bf16 v[192:195], v[92:95], v[156:159], v[192:195]\n\t"
            "buffer_store_dwordx4 v[180:183], v197, s[40:43], s45 offen sc0 sc1\n\t"
            "v_mfma_f32_16x16x32_bf16 v[176:179], v[96:99], v[168:171], v[176:179]\n\t"
            "s_nop 0\n\t"
            "v_lshlrev_b32 v182, 16, v223\n\t"
            "v_and_b32 v181, 0xffff0000, v222\n\t"
            "v_mfma_f32_16x16x32_bf16 v[176:179], v[100:103], v[172:175], v[176:179]\n\t"
            "v_lshlrev_b32 v180, 16, v222\n\t"
            "v_and_b32 v183, 0xffff0000, v223\n\t"
            "v_mfma_f32_16x16x32_bf16 v[176:179], v[104:107], v[144:147], v[176:179]\n\t"
            "ds_read_b128 v[220:223], v248 offset:6144\n\t"
            "v_cvt_pk_bf16_f32 v190, v192, v193\n\t"
            "v_mfma_f32_16x16x32_bf16 v[176:179], v[108:111], v[148:151], v[176:179]\n\t"
            "v_cvt_pk_bf16_f32 v191, v194, v195\n\t"
            "v_lshlrev_b32 v192, 16, v228\n\t"
            "v_mfma_f32_16x16x32_bf16 v[176:179], v[112:115], v[152:155], v[176:179]\n\t"
            "v_and_b32 v195, 0xffff0000, v229\n\t"
            "v_and_b32 v193, 0xffff0000, v228\n\t"
            "v_mfma_f32_16x16x32_bf16 v[176:179], v[116:119], v[156:159], v[176:179]\n\t"
            "buffer_store_dwordx4 v[188:191], v197, s[40:43], s45 offen offset:64 sc0 sc1\n\t"
            "v_mfma_f32_16x16x32_bf16 v[180:183], v[120:123], v[168:171], v[180:183]\n\t"
            "ds_read_b128 v[168:171], v242 offset:12416\n\t"
            "v_lshlrev_b32 v188, 16, v226\n\t"
            "v_mfma_f32_16x16x32_bf16 v[180:183], v[124:127], v[172:175], v[180:183]\n\t"
            "ds_read_b128 v[172:175], v243 offset:12416\n\t"
            "v_and_b32 v191, 0xffff0000, v227\n\t"
            "v_mfma_f32_16x16x32_bf16 v[180:183], v[128:131], v[144:147], v[180:183]\n\t"
            "ds_read_b128 v[144:147], v242 offset:12544\n\t"
            "v_and_b32 v189, 0xffff0000, v226\n\t"
            "v_mfma_f32_16x16x32_bf16 v[180:183], v[132:135], v[148:151], v[180:183]\n\t"
            "ds_read_b128 v[148:151], v243 offset:12544\n\t"
            "v_lshlrev_b32 v190, 16, v227\n\t"
            "v_mfma_f32_16x16x32_bf16 v[180:183], v[136:139], v[152:155], v[180:183]\n\t"
            "s_waitcnt lgkmcnt(0)\n\t"
            "s_barrier\n\t"
            "ds_read_b128 v[224:227], v236 offset:12288\n\t"
            "v_lshlrev_b32 v194, 16, v229\n\t"
            "v_mfma_f32_16x16x32_bf16 v[180:183], v[140:143], v[156:159], v[180:183]\n\t"
            "v_cvt_pk_bf16_f32 v176, v176, v177\n\t"
            "v_cvt_pk_bf16_f32 v177, v178, v179\n\t"
            "v_mfma_f32_16x16x32_bf16 v[184:187], v[0:3], v[160:163], v[184:187]\n\t"
            "v_mfma_f32_16x16x32_bf16 v[184:187], v[4:7], v[164:167], v[184:187]\n\t"
            "v_mfma_f32_16x16x32_bf16 v[184:187], v[8:11], v[168:171], v[184:187]\n\t"
            "v_mfma_f32_16x16x32_bf16 v[184:187], v[12:15], v[172:175], v[184:187]\n\t"
            "s_nop 1\n\t"
            "v_cvt_pk_bf16_f32 v179, v182, v183\n\t"
            "v_mfma_f32_16x16x32_bf16 v[184:187], v[16:19], v[144:147], v[184:187]\n\t"
            "v_cvt_pk_bf16_f32 v178, v180, v181\n\t"
            "v_and_b32 v181, 0xffff0000, v232\n\t"
            "v_mfma_f32_16x16x32_bf16 v[184:187], v[20:23], v[148:151], v[184:187]\n\t"
            "buffer_store_dwordx4 v[176:179], v197, s[40:43], s45 offen offset:128 sc0 sc1\n\t"
            "v_mfma_f32_16x16x32_bf16 v[188:191], v[24:27], v[160:163], v[188:191]\n\t"
            "s_nop 0\n\t"
            "v_lshlrev_b32 v176, 16, v230\n\t"
            "v_and_b32 v179, 0xffff0000, v231\n\t"
            "v_mfma_f32_16x16x32_bf16 v[188:191], v[28:31], v[164:167], v[188:191]\n\t"
            "v_lshlrev_b32 v178, 16, v231\n\t"
            "v_and_b32 v177, 0xffff0000, v230\n\t"
            "v_mfma_f32_16x16x32_bf16 v[188:191], v[32:35], v[168:171], v[188:191]\n\t"
            "ds_read_b128 v[228:231], v237 offset:12288\n\t"
            "v_and_b32 v183, 0xffff0000, v233\n\t"
            "v_mfma_f32_16x16x32_bf16 v[188:191], v[36:39], v[172:175], v[188:191]\n\t"
            "v_lshlrev_b32 v182, 16, v233\n\t"
            "v_lshlrev_b32 v180, 16, v232\n\t"
            "v_mfma_f32_16x16x32_bf16 v[188:191], v[40:43], v[144:147], v[188:191]\n\t"
            "v_cvt_pk_bf16_f32 v184, v184, v185\n\t"
            "v_cvt_pk_bf16_f32 v185, v186, v187\n\t"
            "v_mfma_f32_16x16x32_bf16 v[188:191], v[44:47], v[148:151], v[188:191]\n\t"
            "v_mfma_f32_16x16x32_bf16 v[192:195], v[48:51], v[160:163], v[192:195]\n\t"
            "v_mfma_f32_16x16x32_bf16 v[192:195], v[52:55], v[164:167], v[192:195]\n\t"
            "v_mfma_f32_16x16x32_bf16 v[192:195], v[56:59], v[168:171], v[192:195]\n\t"
            "v_mfma_f32_16x16x32_bf16 v[192:195], v[60:63], v[172:175], v[192:195]\n\t"
            "v_mfma_f32_16x16x32_bf16 v[192:195], v[64:67], v[144:147], v[192:195]\n\t"
            "v_mfma_f32_16x16x32_bf16 v[192:195], v[68:71], v[148:151], v[192:195]\n\t"
            "s_nop 1\n\t"
            "v_cvt_pk_bf16_f32 v187, v190, v191\n\t"
            "v_mfma_f32_16x16x32_bf16 v[176:179], v[72:75], v[160:163], v[176:179]\n\t"
            "v_cvt_pk_bf16_f32 v186, v188, v189\n\t"
            "v_mfma_f32_16x16x32_bf16 v[176:179], v[76:79], v[164:167], v[176:179]\n\t"
            "v_mfma_f32_16x16x32_bf16 v[176:179], v[80:83], v[168:171], v[176:179]\n\t"
            "v_mfma_f32_16x16x32_bf16 v[176:179], v[84:87], v[172:175], v[176:179]\n\t"
            "v_cvt_pk_bf16_f32 v192, v192, v193\n\t"
            "v_mfma_f32_16x16x32_bf16 v[176:179], v[88:91], v[144:147], v[176:179]\n\t"
            "v_cvt_pk_bf16_f32 v193, v194, v195\n\t"
            "v_mfma_f32_16x16x32_bf16 v[176:179], v[92:95], v[148:151], v[176:179]\n\t"
            "buffer_store_dwordx4 v[184:187], v198, s[40:43], s45 offen sc0 sc1\n\t"
            "v_mfma_f32_16x16x32_bf16 v[180:183], v[96:99], v[160:163], v[180:183]\n\t"
            "s_waitcnt vmcnt(7)\n\t"
            "ds_read_b64 v[152:153], v253 offset:8\n\t"
            "v_lshlrev_b32 v184, 16, v234\n\t"
            "v_mfma_f32_16x16x32_bf16 v[180:183], v[100:103], v[164:167], v[180:183]\n\t"
            "ds_read_b64 v[156:157], v253 offset:1752\n\t"
            "v_lshlrev_b32 v186, 16, v235\n\t"
            "v_mfma_f32_16x16x32_bf16 v[180:183], v[104:107], v[168:171], v[180:183]\n\t"
            "ds_read_b64 v[158:159], v253 offset:1848\n\t"
            "v_and_b32 v187, 0xffff0000, v235\n\t"
            "v_mfma_f32_16x16x32_bf16 v[180:183], v[108:111], v[172:175], v[180:183]\n\t"
            "ds_read_b64 v[154:155], v253 offset:104\n\t"
            "v_and_b32 v185, 0xffff0000, v234\n\t"
            "v_mfma_f32_16x16x32_bf16 v[180:183], v[112:115], v[144:147], v[180:183]\n\t"
            "ds_read_b128 v[232:235], v248 offset:12288\n\t"
            "v_cvt_pk_bf16_f32 v195, v178, v179\n\t"
            "v_mfma_f32_16x16x32_bf16 v[180:183], v[116:119], v[148:151], v[180:183]\n\t"
            "v_cvt_pk_bf16_f32 v194, v176, v177\n\t"
            "buffer_store_dwordx4 v[192:195], v198, s[40:43], s45 offen offset:64 sc0 sc1\n\t"
            "v_mfma_f32_16x16x32_bf16 v[184:187], v[120:123], v[160:163], v[184:187]\n\t"
            "ds_read_b64 v[162:163], v253 offset:112\n\t"
            "v_mfma_f32_16x16x32_bf16 v[184:187], v[124:127], v[164:167], v[184:187]\n\t"
            "ds_read_b64 v[160:161], v253 offset:16\n\t"
            "v_mfma_f32_16x16x32_bf16 v[184:187], v[128:131], v[168:171], v[184:187]\n\t"
            "ds_read_b64 v[166:167], v253 offset:1856\n\t"
            "v_cvt_pk_bf16_f32 v180, v180, v181\n\t"
            "v_mfma_f32_16x16x32_bf16 v[184:187], v[132:135], v[172:175], v[184:187]\n\t"
            "ds_read_b128 v[172:175], v254 offset:64\n\t"
            "v_cvt_pk_bf16_f32 v181, v182, v183\n\t"
            "v_mfma_f32_16x16x32_bf16 v[184:187], v[136:139], v[144:147], v[184:187]\n\t"
            "ds_read_b64 v[164:165], v253 offset:1760\n\t"
            "v_mfma_f32_16x16x32_bf16 v[184:187], v[140:143], v[148:151], v[184:187]\n\t"
            "s_waitcnt lgkmcnt(11)\n\t"
            "ds_read_b64 v[144:145], v253\n\t"
            "ds_read_b64 v[146:147], v253 offset:96\n\t"
            "ds_read_b64 v[148:149], v253 offset:1744\n\t"
            "ds_read_b128 v[168:171], v254\n\t"
            "s_waitcnt lgkmcnt(14)\n\t"
            "ds_read_b64 v[150:151], v253 offset:1840\n\t"
            "s_nop 0\n\t"
            "v_cvt_pk_bf16_f32 v183, v186, v187\n\t"
            "v_cvt_pk_bf16_f32 v182, v184, v185\n\t"
            "buffer_store_dwordx4 v[180:183], v198, s[40:43], s45 offen offset:128 sc0 sc1\n\t"
            "s_mov_b32 m0, s32\n\t"
            "s_add_u32 s49, s70, s25\n\t"
            "buffer_load_dword v249, s[72:75], s70 offen lds\n\t"
            "buffer_load_dword v249, s[72:75], s49 offen lds\n\t"
            "s_add_u32 s45, s46, s27\n\t"
            "s_mov_b32 s46, s47\n\t"
            "s_mov_b32 s47, s68\n\t"
            "s_xor_b32 s48, s48, s71\n\t"
            "s_xor_b32 s67, s67, s71\n\t"
            "s_add_u32 s76, s76, 4\n\t"
            "s_lshl_b32 s56, s56, 2\n\t"
            "s_lshr_b32 s49, s26, 30\n\t"
            "s_or_b32 s56, s56, s49\n\t"
            "s_cmp_lt_u32 s46, 0x7000000\n\t"
            "s_cbranch_scc1 .Lwb_loop%=\n\t"
            "s_waitcnt lgkmcnt(0)\n\t"
            "s_nop 7\n\t"
            "s_setprio 3\n\t"
            "s_nop 7\n\t"
            "ds_read_b128 v[196:199], v252\n\t"
            "s_bfe_u32 s57, s56, 0x20004\n\t"
            "s_lshl_b32 s58, s57, 3\n\t"
            "s_lshr_b32 s58, s55, s58\n\t"
            "s_lshl_b64 s[36:37], s[34:35], s58\n\t"
            "s_bitcmp1_b32 s57, 1\n\t"
            "s_cselect_b32 s58, 1177344, 0\n\t"
            "s_bitcmp1_b32 s57, 0\n\t"
            "s_cselect_b32 s57, 2304, 0\n\t"
            "s_add_u32 s57, s57, s58\n\t"
            "s_waitcnt lgkmcnt(0)\n\t"
            "s_mov_b64 exec, s[36:37]\n\t"
            "v_add_u32 v196, s57, v196\n\t"
            "s_mov_b64 exec, -1\n\t"
            "s_nop 7\n\t"
            "v_mov_b32 v144, s23\n\t"
            ".Ldp%=:\n\t"
            "ds_read_b128 v[148:151], v144 offset:96\n\t"
            "s_waitcnt lgkmcnt(0)\n\t"
            "v_or_b32 v148, v148, v149\n\t"
            "v_or3_b32 v148, v148, v150, v151\n\t"
            "v_cmp_gt_i32 vcc, 0, v148\n\t"
            "s_nop 4\n\t"
            "s_cbranch_vccnz .Ldp%=\n\t"
            "s_nop 7\n\t"
            "ds_read_b128 v[184:187], v242\n\t"
            "v_lshlrev_b32 v152, 16, v200\n\t"
            "v_and_b32 v153, 0xffff0000, v200\n\t"
            "v_lshlrev_b32 v154, 16, v201\n\t"
            "v_and_b32 v155, 0xffff0000, v201\n\t"
            "v_lshlrev_b32 v156, 16, v202\n\t"
            "v_and_b32 v157, 0xffff0000, v202\n\t"
            "v_lshlrev_b32 v158, 16, v203\n\t"
            "v_and_b32 v159, 0xffff0000, v203\n\t"
            "v_lshlrev_b32 v160, 16, v204\n\t"
            "v_and_b32 v161, 0xffff0000, v204\n\t"
            "v_lshlrev_b32 v162, 16, v205\n\t"
            "v_and_b32 v163, 0xffff0000, v205\n\t"
            "v_lshlrev_b32 v164, 16, v206\n\t"
            "v_and_b32 v165, 0xffff0000, v206\n\t"
            "v_lshlrev_b32 v166, 16, v207\n\t"
            "v_and_b32 v167, 0xffff0000, v207\n\t"
            "v_lshlrev_b32 v176, 16, v208\n\t"
            "v_and_b32 v177, 0xffff0000, v208\n\t"
            "v_lshlrev_b32 v178, 16, v209\n\t"
            "v_and_b32 v179, 0xffff0000, v209\n\t"
            "v_lshlrev_b32 v180, 16, v210\n\t"
            "v_and_b32 v181, 0xffff0000, v210\n\t"
            "v_lshlrev_b32 v182, 16, v211\n\t"
            "v_and_b32 v183, 0xffff0000, v211\n\t"
            "v_lshlrev_b32 v144, 16, v212\n\t"
            "v_and_b32 v145, 0xffff0000, v212\n\t"
            "v_lshlrev_b32 v146, 16, v213\n\t"
            "v_and_b32 v147, 0xffff0000, v213\n\t"
            "v_lshlrev_b32 v148, 16, v214\n\t"
            "v_and_b32 v149, 0xffff0000, v214\n\t"
            "s_waitcnt lgkmcnt(0)\n\t"
            "v_mfma_f32_16x16x32_bf16 v[152:155], v[0:3], v[184:187], v[152:155]\n\t"
            "ds_read_b128 v[188:191], v243\n\t"
            "v_lshlrev_b32 v150, 16, v215\n\t"
            "v_mfma_f32_16x16x32_bf16 v[156:159], v[24:27], v[184:187], v[156:159]\n\t"
            "ds_read_b128 v[192:195], v242 offset:128\n\t"
            "v_and_b32 v151, 0xffff0000, v215\n\t"
            "v_mfma_f32_16x16x32_bf16 v[160:163], v[48:51], v[184:187], v[160:163]\n\t"
            "v_lshlrev_b32 v168, 16, v216\n\t"
            "v_and_b32 v169, 0xffff0000, v216\n\t"
            "v_mfma_f32_16x16x32_bf16 v[164:167], v[72:75], v[184:187], v[164:167]\n\t"
            "v_lshlrev_b32 v170, 16, v217\n\t"
            "v_and_b32 v171, 0xffff0000, v217\n\t"
            "v_mfma_f32_16x16x32_bf16 v[176:179], v[96:99], v[184:187], v[176:179]\n\t"
            "v_lshlrev_b32 v172, 16, v218\n\t"
            "v_and_b32 v173, 0xffff0000, v218\n\t"
            "v_mfma_f32_16x16x32_bf16 v[180:183], v[120:123], v[184:187], v[180:183]\n\t"
            "ds_read_b128 v[184:187], v243 offset:128\n\t"
            "v_lshlrev_b32 v174, 16, v219\n\t"
            "v_and_b32 v175, 0xffff0000, v219\n\t"
            "s_waitcnt lgkmcnt(2)\n\t"
            "v_mfma_f32_16x16x32_bf16 v[152:155], v[4:7], v[188:191], v[152:155]\n\t"
            "v_mfma_f32_16x16x32_bf16 v[156:159], v[28:31], v[188:191], v[156:159]\n\t"
            "v_mfma_f32_16x16x32_bf16 v[160:163], v[52:55], v[188:191], v[160:163]\n\t"
            "v_mfma_f32_16x16x32_bf16 v[164:167], v[76:79], v[188:191], v[164:167]\n\t"
            "v_mfma_f32_16x16x32_bf16 v[176:179], v[100:103], v[188:191], v[176:179]\n\t"
            "v_mfma_f32_16x16x32_bf16 v[180:183], v[124:127], v[188:191], v[180:183]\n\t"
            "ds_read_b128 v[188:191], v242 offset:256\n\t"
            "s_waitcnt lgkmcnt(2)\n\t"
            "v_mfma_f32_16x16x32_bf16 v[152:155], v[8:11], v[192:195], v[152:155]\n\t"
            "v_mfma_f32_16x16x32_bf16 v[156:159], v[32:35], v[192:195], v[156:159]\n\t"
            "v_mfma_f32_16x16x32_bf16 v[160:163], v[56:59], v[192:195], v[160:163]\n\t"
            "v_mfma_f32_16x16x32_bf16 v[164:167], v[80:83], v[192:195], v[164:167]\n\t"
            "v_mfma_f32_16x16x32_bf16 v[176:179], v[104:107], v[192:195], v[176:179]\n\t"
            "v_mfma_f32_16x16x32_bf16 v[180:183], v[128:131], v[192:195], v[180:183]\n\t"
            "ds_read_b128 v[192:195], v243 offset:256\n\t"
            "s_waitcnt lgkmcnt(2)\n\t"
            "v_mfma_f32_16x16x32_bf16 v[152:155], v[12:15], v[184:187], v[152:155]\n\t"
            "v_mfma_f32_16x16x32_bf16 v[156:159], v[36:39], v[184:187], v[156:159]\n\t"
            "v_mfma_f32_16x16x32_bf16 v[160:163], v[60:63], v[184:187], v[160:163]\n\t"
            "v_mfma_f32_16x16x32_bf16 v[164:167], v[84:87], v[184:187], v[164:167]\n\t"
            "v_mfma_f32_16x16x32_bf16 v[176:179], v[108:111], v[184:187], v[176:179]\n\t"
            "v_mfma_f32_16x16x32_bf16 v[180:183], v[132:135], v[184:187], v[180:183]\n\t"
            "ds_read_b128 v[184:187], v242 offset:6144\n\t"
            "s_waitcnt lgkmcnt(2)\n\t"
            "v_mfma_f32_16x16x32_bf16 v[152:155], v[16:19], v[188:191], v[152:155]\n\t"
            "v_mfma_f32_16x16x32_bf16 v[156:159], v[40:43], v[188:191], v[156:159]\n\t"
            "v_mfma_f32_16x16x32_bf16 v[160:163], v[64:67], v[188:191], v[160:163]\n\t"
            "v_mfma_f32_16x16x32_bf16 v[164:167], v[88:91], v[188:191], v[164:167]\n\t"
            "v_mfma_f32_16x16x32_bf16 v[176:179], v[112:115], v[188:191], v[176:179]\n\t"
            "v_mfma_f32_16x16x32_bf16 v[180:183], v[136:139], v[188:191], v[180:183]\n\t"
            "ds_read_b128 v[188:191], v243 offset:6144\n\t"
            "s_waitcnt lgkmcnt(2)\n\t"
            "v_mfma_f32_16x16x32_bf16 v[152:155], v[20:23], v[192:195], v[152:155]\n\t"
            "v_mfma_f32_16x16x32_bf16 v[156:159], v[44:47], v[192:195], v[156:159]\n\t"
            "v_mfma_f32_16x16x32_bf16 v[160:163], v[68:71], v[192:195], v[160:163]\n\t"
            "v_mfma_f32_16x16x32_bf16 v[164:167], v[92:95], v[192:195], v[164:167]\n\t"
            "v_mfma_f32_16x16x32_bf16 v[176:179], v[116:119], v[192:195], v[176:179]\n\t"
            "v_mfma_f32_16x16x32_bf16 v[180:183], v[140:143], v[192:195], v[180:183]\n\t"
            "ds_read_b128 v[192:195], v242 offset:6272\n\t"
            "s_waitcnt lgkmcnt(2)\n\t"
            "v_mfma_f32_16x16x32_bf16 v[144:147], v[0:3], v[184:187], v[144:147]\n\t"
            "v_cvt_pk_bf16_f32 v152, v152, v153\n\t"
            "v_cvt_pk_bf16_f32 v153, v154, v155\n\t"
            "v_mfma_f32_16x16x32_bf16 v[148:151], v[24:27], v[184:187], v[148:151]\n\t"
            "v_cvt_pk_bf16_f32 v160, v160, v161\n\t"
            "v_cvt_pk_bf16_f32 v161, v162, v163\n\t"
            "v_mfma_f32_16x16x32_bf16 v[168:171], v[48:51], v[184:187], v[168:171]\n\t"
            "v_cvt_pk_bf16_f32 v154, v156, v157\n\t"
            "v_cvt_pk_bf16_f32 v155, v158, v159\n\t"
            "v_mfma_f32_16x16x32_bf16 v[172:175], v[72:75], v[184:187], v[172:175]\n\t"
            "v_cvt_pk_bf16_f32 v162, v164, v165\n\t"
            "v_cvt_pk_bf16_f32 v163, v166, v167\n\t"
            "s_waitcnt lgkmcnt(1)\n\t"
            "v_mfma_f32_16x16x32_bf16 v[144:147], v[4:7], v[188:191], v[144:147]\n\t"
            "v_lshlrev_b32 v156, 16, v220\n\t"
            "v_and_b32 v157, 0xffff0000, v220\n\t"
            "v_mfma_f32_16x16x32_bf16 v[148:151], v[28:31], v[188:191], v[148:151]\n\t"
            "v_lshlrev_b32 v158, 16, v221\n\t"
            "v_and_b32 v159, 0xffff0000, v221\n\t"
            "v_mfma_f32_16x16x32_bf16 v[168:171], v[52:55], v[188:191], v[168:171]\n\t"
            "v_lshlrev_b32 v164, 16, v222\n\t"
            "v_and_b32 v165, 0xffff0000, v222\n\t"
            "v_mfma_f32_16x16x32_bf16 v[156:159], v[96:99], v[184:187], v[156:159]\n\t"
            "v_lshlrev_b32 v166, 16, v223\n\t"
            "v_and_b32 v167, 0xffff0000, v223\n\t"
            "v_mfma_f32_16x16x32_bf16 v[172:175], v[76:79], v[188:191], v[172:175]\n\t"
            "buffer_store_dwordx4 v[152:155], v196, s[40:43], s45 offen sc0 sc1\n\t"
            "v_mfma_f32_16x16x32_bf16 v[164:167], v[120:123], v[184:187], v[164:167]\n\t"
            "ds_read_b128 v[184:187], v243 offset:6272\n\t"
            "v_cvt_pk_bf16_f32 v176, v176, v177\n\t"
            "v_mfma_f32_16x16x32_bf16 v[156:159], v[100:103], v[188:191], v[156:159]\n\t"
            "buffer_store_dwordx4 v[160:163], v196, s[40:43], s45 offen offset:64 sc0 sc1\n\t"
            "v_mfma_f32_16x16x32_bf16 v[164:167], v[124:127], v[188:191], v[164:167]\n\t"
            "ds_read_b128 v[188:191], v242 offset:6400\n\t"
            "v_cvt_pk_bf16_f32 v177, v178, v179\n\t"
            "s_waitcnt lgkmcnt(2)\n\t"
            "v_mfma_f32_16x16x32_bf16 v[144:147], v[8:11], v[192:195], v[144:147]\n\t"
            "v_cvt_pk_bf16_f32 v178, v180, v181\n\t"
            "v_cvt_pk_bf16_f32 v179, v182, v183\n\t"
            "v_mfma_f32_16x16x32_bf16 v[148:151], v[32:35], v[192:195], v[148:151]\n\t"
            "buffer_store_dwordx4 v[176:179], v196, s[40:43], s45 offen offset:128 sc0 sc1\n\t"
            "v_mfma_f32_16x16x32_bf16 v[168:171], v[56:59], v[192:195], v[168:171]\n\t"
            "v_lshlrev_b32 v152, 16, v224\n\t"
            "v_and_b32 v153, 0xffff0000, v224\n\t"
            "v_mfma_f32_16x16x32_bf16 v[172:175], v[80:83], v[192:195], v[172:175]\n\t"
            "v_lshlrev_b32 v154, 16, v225\n\t"
            "v_and_b32 v155, 0xffff0000, v225\n\t"
            "v_mfma_f32_16x16x32_bf16 v[156:159], v[104:107], v[192:195], v[156:159]\n\t"
            "v_lshlrev_b32 v180, 16, v226\n\t"
            "v_and_b32 v181, 0xffff0000, v226\n\t"
            "v_mfma_f32_16x16x32_bf16 v[164:167], v[128:131], v[192:195], v[164:167]\n\t"
            "ds_read_b128 v[192:195], v243 offset:6400\n\t"
            "v_lshlrev_b32 v182, 16, v227\n\t"
            "s_waitcnt lgkmcnt(2)\n\t"
            "v_mfma_f32_16x16x32_bf16 v[144:147], v[12:15], v[184:187], v[144:147]\n\t"
            "v_and_b32 v183, 0xffff0000, v227\n\t"
            "v_lshlrev_b32 v160, 16, v228\n\t"
            "v_mfma_f32_16x16x32_bf16 v[148:151], v[36:39], v[184:187], v[148:151]\n\t"
            "v_and_b32 v161, 0xffff0000, v228\n\t"
            "v_lshlrev_b32 v162, 16, v229\n\t"
            "s_waitcnt lgkmcnt(1)\n\t"
            "v_mfma_f32_16x16x32_bf16 v[144:147], v[16:19], v[188:191], v[144:147]\n\t"
            "v_and_b32 v163, 0xffff0000, v229\n\t"
            "v_lshlrev_b32 v176, 16, v230\n\t"
            "v_mfma_f32_16x16x32_bf16 v[168:171], v[60:63], v[184:187], v[168:171]\n\t"
            "v_and_b32 v177, 0xffff0000, v230\n\t"
            "v_lshlrev_b32 v178, 16, v231\n\t"
            "v_mfma_f32_16x16x32_bf16 v[148:151], v[40:43], v[188:191], v[148:151]\n\t"
            "v_and_b32 v179, 0xffff0000, v231\n\t"
            "v_mfma_f32_16x16x32_bf16 v[172:175], v[84:87], v[184:187], v[172:175]\n\t"
            "v_mfma_f32_16x16x32_bf16 v[168:171], v[64:67], v[188:191], v[168:171]\n\t"
            "v_mfma_f32_16x16x32_bf16 v[156:159], v[108:111], v[184:187], v[156:159]\n\t"
            "s_waitcnt lgkmcnt(0)\n\t"
            "v_mfma_f32_16x16x32_bf16 v[144:147], v[20:23], v[192:195], v[144:147]\n\t"
            "v_mfma_f32_16x16x32_bf16 v[148:151], v[44:47], v[192:195], v[148:151]\n\t"
            "v_mfma_f32_16x16x32_bf16 v[164:167], v[132:135], v[184:187], v[164:167]\n\t"
            "ds_read_b128 v[184:187], v242 offset:12288\n\t"
            "v_mfma_f32_16x16x32_bf16 v[172:175], v[88:91], v[188:191], v[172:175]\n\t"
            "v_mfma_f32_16x16x32_bf16 v[168:171], v[68:71], v[192:195], v[168:171]\n\t"
            "v_mfma_f32_16x16x32_bf16 v[156:159], v[112:115], v[188:191], v[156:159]\n\t"
            "s_nop 1\n\t"
            "v_cvt_pk_bf16_f32 v144, v144, v145\n\t"
            "v_mfma_f32_16x16x32_bf16 v[164:167], v[136:139], v[188:191], v[164:167]\n\t"
            "ds_read_b128 v[188:191], v243 offset:12288\n\t"
            "v_cvt_pk_bf16_f32 v145, v146, v147\n\t"
            "v_mfma_f32_16x16x32_bf16 v[172:175], v[92:95], v[192:195], v[172:175]\n\t"
            "v_cvt_pk_bf16_f32 v146, v148, v149\n\t"
            "v_cvt_pk_bf16_f32 v147, v150, v151\n\t"
            "v_mfma_f32_16x16x32_bf16 v[156:159], v[116:119], v[192:195], v[156:159]\n\t"
            "buffer_store_dwordx4 v[144:147], v197, s[40:43], s45 offen sc0 sc1\n\t"
            "v_mfma_f32_16x16x32_bf16 v[164:167], v[140:143], v[192:195], v[164:167]\n\t"
            "ds_read_b128 v[192:195], v242 offset:12416\n\t"
            "v_cvt_pk_bf16_f32 v168, v168, v169\n\t"
            "s_waitcnt lgkmcnt(2)\n\t"
            "v_mfma_f32_16x16x32_bf16 v[152:155], v[0:3], v[184:187], v[152:155]\n\t"
            "v_cvt_pk_bf16_f32 v169, v170, v171\n\t"
            "v_cvt_pk_bf16_f32 v170, v172, v173\n\t"
            "v_mfma_f32_16x16x32_bf16 v[180:183], v[24:27], v[184:187], v[180:183]\n\t"
            "v_cvt_pk_bf16_f32 v171, v174, v175\n\t"
            "v_cvt_pk_bf16_f32 v156, v156, v157\n\t"
            "v_mfma_f32_16x16x32_bf16 v[160:163], v[48:51], v[184:187], v[160:163]\n\t"
            "buffer_store_dwordx4 v[168:171], v197, s[40:43], s45 offen offset:64 sc0 sc1\n\t"
            "v_mfma_f32_16x16x32_bf16 v[176:179], v[72:75], v[184:187], v[176:179]\n\t"
            "v_cvt_pk_bf16_f32 v157, v158, v159\n\t"
            "v_cvt_pk_bf16_f32 v158, v164, v165\n\t"
            "s_waitcnt lgkmcnt(1)\n\t"
            "v_mfma_f32_16x16x32_bf16 v[152:155], v[4:7], v[188:191], v[152:155]\n\t"
            "v_cvt_pk_bf16_f32 v159, v166, v167\n\t"
            "v_lshlrev_b32 v164, 16, v232\n\t"
            "v_mfma_f32_16x16x32_bf16 v[180:183], v[28:31], v[188:191], v[180:183]\n\t"
            "buffer_store_dwordx4 v[156:159], v197, s[40:43], s45 offen offset:128 sc0 sc1\n\t"
            "v_mfma_f32_16x16x32_bf16 v[160:163], v[52:55], v[188:191], v[160:163]\n\t"
            "v_and_b32 v165, 0xffff0000, v232\n\t"
            "v_lshlrev_b32 v166, 16, v233\n\t"
            "v_mfma_f32_16x16x32_bf16 v[176:179], v[76:79], v[188:191], v[176:179]\n\t"
            "v_and_b32 v167, 0xffff0000, v233\n\t"
            "v_lshlrev_b32 v156, 16, v234\n\t"
            "s_waitcnt lgkmcnt(0)\n\t"
            "v_mfma_f32_16x16x32_bf16 v[152:155], v[8:11], v[192:195], v[152:155]\n\t"
            "v_and_b32 v157, 0xffff0000, v234\n\t"
            "v_lshlrev_b32 v158, 16, v235\n\t"
            "v_mfma_f32_16x16x32_bf16 v[164:167], v[96:99], v[184:187], v[164:167]\n\t"
            "v_and_b32 v159, 0xffff0000, v235\n\t"
            "v_mfma_f32_16x16x32_bf16 v[164:167], v[100:103], v[188:191], v[164:167]\n\t"
            "s_nop 0\n\t"
            "v_mfma_f32_16x16x32_bf16 v[156:159], v[120:123], v[184:187], v[156:159]\n\t"
            "ds_read_b128 v[184:187], v243 offset:12416\n\t"
            "v_mfma_f32_16x16x32_bf16 v[156:159], v[124:127], v[188:191], v[156:159]\n\t"
            "ds_read_b128 v[188:191], v242 offset:12544\n\t"
            "v_mfma_f32_16x16x32_bf16 v[180:183], v[32:35], v[192:195], v[180:183]\n\t"
            "v_mfma_f32_16x16x32_bf16 v[160:163], v[56:59], v[192:195], v[160:163]\n\t"
            "v_mfma_f32_16x16x32_bf16 v[176:179], v[80:83], v[192:195], v[176:179]\n\t"
            "v_mfma_f32_16x16x32_bf16 v[164:167], v[104:107], v[192:195], v[164:167]\n\t"
            "v_mfma_f32_16x16x32_bf16 v[156:159], v[128:131], v[192:195], v[156:159]\n\t"
            "ds_read_b128 v[192:195], v243 offset:12544\n\t"
            "s_waitcnt lgkmcnt(2)\n\t"
            "v_mfma_f32_16x16x32_bf16 v[152:155], v[12:15], v[184:187], v[152:155]\n\t"
            "v_mfma_f32_16x16x32_bf16 v[180:183], v[36:39], v[184:187], v[180:183]\n\t"
            "s_waitcnt lgkmcnt(1)\n\t"
            "v_mfma_f32_16x16x32_bf16 v[152:155], v[16:19], v[188:191], v[152:155]\n\t"
            "v_mfma_f32_16x16x32_bf16 v[160:163], v[60:63], v[184:187], v[160:163]\n\t"
            "v_mfma_f32_16x16x32_bf16 v[180:183], v[40:43], v[188:191], v[180:183]\n\t"
            "v_mfma_f32_16x16x32_bf16 v[176:179], v[84:87], v[184:187], v[176:179]\n\t"
            "s_waitcnt lgkmcnt(0)\n\t"
            "v_mfma_f32_16x16x32_bf16 v[152:155], v[20:23], v[192:195], v[152:155]\n\t"
            "v_mfma_f32_16x16x32_bf16 v[160:163], v[64:67], v[188:191], v[160:163]\n\t"
            "v_mfma_f32_16x16x32_bf16 v[180:183], v[44:47], v[192:195], v[180:183]\n\t"
            "v_mfma_f32_16x16x32_bf16 v[164:167], v[108:111], v[184:187], v[164:167]\n\t"
            "v_mfma_f32_16x16x32_bf16 v[176:179], v[88:91], v[188:191], v[176:179]\n\t"
            "v_mfma_f32_16x16x32_bf16 v[156:159], v[132:135], v[184:187], v[156:159]\n\t"
            "v_mfma_f32_16x16x32_bf16 v[160:163], v[68:71], v[192:195], v[160:163]\n\t"
            "s_nop 1\n\t"
            "v_cvt_pk_bf16_f32 v152, v152, v153\n\t"
            "v_mfma_f32_16x16x32_bf16 v[164:167], v[112:115], v[188:191], v[164:167]\n\t"
            "v_cvt_pk_bf16_f32 v153, v154, v155\n\t"
            "v_mfma_f32_16x16x32_bf16 v[176:179], v[92:95], v[192:195], v[176:179]\n\t"
            "v_cvt_pk_bf16_f32 v154, v180, v181\n\t"
            "v_cvt_pk_bf16_f32 v155, v182, v183\n\t"
            "v_mfma_f32_16x16x32_bf16 v[156:159], v[136:139], v[188:191], v[156:159]\n\t"
            "buffer_store_dwordx4 v[152:155], v198, s[40:43], s45 offen sc0 sc1\n\t"
            "v_mfma_f32_16x16x32_bf16 v[164:167], v[116:119], v[192:195], v[164:167]\n\t"
            "v_cvt_pk_bf16_f32 v160, v160, v161\n\t"
            "v_cvt_pk_bf16_f32 v161, v162, v163\n\t"
            "v_mfma_f32_16x16x32_bf16 v[156:159], v[140:143], v[192:195], v[156:159]\n\t"
            "v_cvt_pk_bf16_f32 v162, v176, v177\n\t"
            "v_cvt_pk_bf16_f32 v163, v178, v179\n\t"
            "buffer_store_dwordx4 v[160:163], v198, s[40:43], s45 offen offset:64 sc0 sc1\n\t"
            "s_nop 1\n\t"
            "v_cvt_pk_bf16_f32 v164, v164, v165\n\t"
            "v_cvt_pk_bf16_f32 v165, v166, v167\n\t"
            "s_nop 0\n\t"
            "v_cvt_pk_bf16_f32 v166, v156, v157\n\t"
            "v_cvt_pk_bf16_f32 v167, v158, v159\n\t"
            "buffer_store_dwordx4 v[164:167], v198, s[40:43], s45 offen offset:128 sc0 sc1\n\t"
            "s_nop 7\n\t"
            ".Lre%=:\n\t"
            : "+{v[224:227]}"(f0), "+{v[228:231]}"(f1), "+{v[232:235]}"(f2), "+{v242}"(r242), "+{v243}"(r243), "+{v[244:247]}"(r244),
              "+{s45}"(s45), "+{s46}"(s46), "+{s47}"(s47), "+{s48}"(s48), "+{s67}"(s67), "+{s76}"(s76), "+{s56}"(s56), "+{s44}"(s44), "+{s70}"(s70), "+{s21}"(s21)
            : B_VIN, B_SIN
            : "v0", "v1", "v2", "v3", "v4", "v5", "v6", "v7", "v8", "v9", "v10", "v11", "v12", "v13", "v14", "v15", "v16", "v17", "v18", "v19", "v20", "v21", "v22", "v23", "v24", "v25", "v26", "v27", "v28", "v29", "v30", "v31", "v32", "v33", "v34", "v35", "v36", "v37", "v38", "v39", "v40", "v41", "v42", "v43", "v44", "v45", "v46", "v47", "v48", "v49", "v50", "v51", "v52", "v53", "v54", "v55", "v56", "v57", "v58", "v59", "v60", "v61", "v62", "v63", "v64", "v65", "v66", "v67", "v68", "v69", "v70", "v71", "v72", "v73", "v74", "v75", "v76", "v77", "v78", "v79", "v80", "v81", "v82", "v83", "v84", "v85", "v86", "v87", "v88", "v89", "v90", "v91", "v92", "v93", "v94", "v95", "v96", "v97", "v98", "v99", "v100", "v101", "v102", "v103", "v104", "v105", "v106", "v107", "v108", "v109", "v110", "v111", "v112", "v113", "v114", "v115", "v116", "v117", "v118", "v119", "v120", "v121", "v122", "v123", "v124", "v125", "v126", "v127", "v128", "v129", "v130", "v131", "v132", "v133", "v134", "v135", "v136", "v137", "v138", "v139", "v140", "v141", "v142", "v143", "v144", "v145", "v146", "v147", "v148", "v149", "v150", "v151", "v152", "v153", "v154", "v155", "v156", "v157", "v158", "v159", "v160", "v161", "v162", "v163", "v164", "v165", "v166", "v167", "v168", "v169", "v170", "v171", "v172", "v173", "v174", "v175", "v176", "v177", "v178", "v179", "v180", "v181", "v182", "v183", "v184", "v185", "v186", "v187", "v188", "v189", "v190", "v191", "v192", "v193", "v194", "v195", "v196", "v197", "v198", "v199", "v200", "v201", "v202", "v203", "v204", "v205", "v206", "v207", "v208", "v209", "v210", "v211", "v212", "v213", "v214", "v215", "v216", "v217", "v218", "v219", "v220", "v221", "v222", "v223", "v236", "v237", "v248", "v249", "s26", "s68", "s69", "s79", "s36", "s37", "s38", "s57", "s58", "s59", "s49", "vcc", "scc", "m0", "memory");
        }
        asm volatile("wk_code_end1:");
        asm volatile("wk_code_end2:");

    }
}

extern "C" void kernel_launch(void* const* d_in, const int* in_sizes, int n_in,
                              void* d_out, int out_size, void* d_ws, size_t ws_size,
                              hipStream_t stream) {
    const unsigned short* x = (const unsigned short*)d_in[0];
    const unsigned short* dw = (const unsigned short*)d_in[1];
    const unsigned short* wqkv = (const unsigned short*)d_in[2];
    const unsigned short* wo = (const unsigned short*)d_in[3];
    unsigned short* out = (unsigned short*)d_out;
    u32x4* wf = (u32x4*)d_ws;
    win_attn_kernel<<<GRID, 512, 0, stream>>>(x, dw, wqkv, wo, wf, out);
}
```

```cpp
#include <hip/hip_runtime.h>
#include <hip/hip_bf16.h>
#include <stdint.h>


typedef unsigned u32x4 __attribute__((ext_vector_type(4)));

constexpr int IMG = 511;
constexpr int CH = 192;
constexpr int NWX = 73;
constexpr int NWIN = NWX * NWX;
constexpr int GRID = 256;
constexpr int NSHORT = 21 * GRID - NWIN;
constexpr int ROWBYTES = IMG * CH * 2;
constexpr int XBIAS = (IMG + 1) * CH * 2;
constexpr unsigned OOB = 0x40000000u;

constexpr int X_OFF = 32768;
constexpr int X1_OFF = 53632;
constexpr int O_OFF = 74496;
constexpr int V_OFF = 92928;
constexpr int KQL_OFF = 111360;
constexpr int OL_OFF = 128256;
constexpr int AT_OFF = 137472;
constexpr int CT_OFF = AT_OFF + 19584;
constexpr int RS_OFF = CT_OFF + 4096;
constexpr int WL_OFF = RS_OFF + 2304 + 64;
constexpr int LDS_BYTES = WL_OFF + 128;
#define RS_BASE (RS_OFF + (wave < 4 ? wave * 384 : 768 + wave * 192))
constexpr int TP_OFF = X1_OFF + 8192;
constexpr int STL_OFF = O_OFF + 12288;
#define LB_OFF(b) ((b) < 7 ? V_OFF + (b) * 6160 : X1_OFF + ((b) - 7) * 6160)
#define LPOS(wc) (((wc) & 1) ? 0 : 6)
constexpr unsigned CROW = 0x060a0230u;
constexpr unsigned CLANE = 0x00132C3Fu;

constexpr unsigned long long TOKMAP[6] = {0x20001c190f2a2202ull, 0x052517121f061a13ull, 0x181e08151d0b260cull, 0x1b2b2903102d0901ull, 0x04162e0e2824142cull, 0x110a072127232f0dull};
constexpr unsigned long long ROWMAP[8] = {0x08321b240f1c0006ull, 0x23310e2a09170d14ull, 0x210c2f030a273418ull, 0x1d2b11221f043739ull, 0x3234363a103e192dull, 0x291a20022c3f1605ull, 0x303a3c2628122e15ull, 0x39253d1e01070b13ull};

constexpr int WF_BYTES = 8 * 36 * 1024;
constexpr int EP_OFF = WF_BYTES;
constexpr int CN_OFF = EP_OFF + 256, FL_OFF = EP_OFF + 512;
constexpr int SH_OFF = EP_OFF + 1024;
constexpr int WS_BYTES = SH_OFF + GRID * 12288;
constexpr int GO_OFF = WL_OFF + 124;
constexpr int PG_OFF = WL_OFF + 96;
constexpr int SPIN_MAX = 1 << 20;

#define BAR() asm volatile("s_waitcnt lgkmcnt(0)\n\ts_barrier" ::: "memory")
#define OPQ(x) asm volatile("" : "+v"(x))
__host__ __device__ __forceinline__ constexpr unsigned byte_of(unsigned long long a, unsigned long long b, int i) {
    return (unsigned)(((i & 8) ? b : a) >> (8 * (i & 7))) & 0xFFu;
}
__host__ __device__ __forceinline__ constexpr void seq_to_win(int seq, int& wy, int& wx) {
    const bool last = seq >= 5110;
    const int s = last ? 35 : seq / 146, r = seq - 146 * s;
    const int kx = last ? r / 6 : r >> 2, pos = r - (last ? 6 : 4) * kx;
    const bool low = last && pos >= (kx == 36 ? 2 : 4);
    wy = low ? 72 : 2 * s + (pos & 1);
    wx = 2 * kx + (low ? (kx == 36 ? 0 : pos - 4) : pos >> 1);
}
__host__ __device__ __forceinline__ constexpr int win_to_seq(int wy, int wx) {
    const int kx = wx >> 1;
    return wy == 72 ? 5110 + 6 * kx + (kx == 36 ? 2 : 4 + (wx & 1)) : 146 * (wy >> 1) + (wy >= 70 ? 6 : 4) * kx + 2 * (wx & 1) + (wy & 1);
}
__host__ __device__ __forceinline__ constexpr int block_of(int wy, int wx) { return wy >= 70 ? 1295 + (wx & ~1) + (wy == 72) : 37 * (wy >> 1) + (wx >> 1); }
__host__ __device__ __forceinline__ constexpr void block_pos(int b, int& ky, int& kx) {
    if (b >= 1295) { kx = (b - 1295) >> 1; ky = 70 + 2 * ((b - 1295) & 1); }
    else { const int s = b / 37; kx = b - 37 * s; ky = 2 * s; }
}
__host__ __device__ __forceinline__ constexpr unsigned wl_entry(int seq) {
    int wy = 0, wx = 0;
    seq_to_win(seq, wy, wx);
    return (unsigned)((wy * 7 * IMG + wx * 7) * (CH * 2)) | (wy == 0 ? 0x08000000u : 0u) | (wx == 0 ? 0x10000000u : 0u) |
           (wx == NWX - 1 ? 0x20000000u : 0u) | ((unsigned)(wx & 1) << 30) | ((unsigned)(wy & 1) << 31);
}
__host__ __device__ __forceinline__ constexpr unsigned dma_off(int lane) {
    const int col = (lane * 43) >> 8;
    return (unsigned)(col * (CH * 2 - 96) + 16 * lane) | (col == 0 ? 0x10000000u : 0u) | (col == 8 ? 0x20000000u : 0u);
}

__host__ __device__ __forceinline__ constexpr unsigned pf_off(int lane) {
    const int l = lane < 63 ? lane : 62, r = l / 21;
    return (unsigned)(r * ROWBYTES + (l - 21 * r) * 128) | (r == 0 ? 0x08000000u : 0u);
}
constexpr int RECN = 80;
struct Tables { unsigned rec[GRID][RECN]; unsigned img[768]; };
constexpr Tables make_tables() {
    Tables t = {};
    for (int bx = 0; bx < GRID; ++bx) {
        unsigned* r = t.rec[bx];
        const int q0 = (bx & 7) * 32 + (bx >> 3), cnt = q0 < NSHORT ? 20 : 21, start = NWIN - cnt - (q0 < NSHORT ? 20 * q0 : 21 * q0 - NSHORT);
        for (int i = 0; i < 32; ++i) r[i] = i < cnt ? wl_entry(start + i) : 0x7ffffffu;
        int wy = 0, wx = 0;
        seq_to_win(start, wy, wx);
        const int key0 = block_of(wy, wx);
        seq_to_win(start + cnt - 1, wy, wx);
        const int nblk = block_of(wy, wx) - key0 + 1;
        for (int b = 0; b < 8; ++b) {
            int ky = 0, kx = 0;
            block_pos(key0 + b, ky, kx);
            r[64 + b] = b < nblk ? (unsigned)(((7 * ky + 5) * IMG + 14 * kx + 5) * (CH * 2) + XBIAS) | (kx == 36 ? 1u : 0u) : OOB;
            for (int c = 0; c < 4; ++c) {
                const int y = ky + (c >> 1), x = 2 * kx + (c & 1), i = win_to_seq(y, x) - start;
                const bool ok = b < nblk && y < NWX && x < NWX && i >= 0 && i < cnt;
                r[32 + 4 * b + c] = ok ? (unsigned)(((7 * y + LPOS(y)) * IMG + 7 * x + LPOS(x)) * (CH * 2) + i) : OOB + 47;
            }
        }
        r[72] = q0; r[73] = cnt;
    }
    for (int lane = 0; lane < 64; ++lane) {
        for (int e = 0; e < 3; ++e) {
            const int tk = byte_of(TOKMAP[2 * e], TOKMAP[2 * e + 1], lane & 15);
            t.img[192 + 4 * lane + e] = (unsigned)((tk / 7) * ROWBYTES + (tk % 7) * (CH * 2)) + 16 * (lane >> 4);
        }
        t.img[448 + 4 * lane] = dma_off(lane);
        t.img[449 + 4 * lane] = pf_off(lane);
    }
    return t;
}
static __constant__ const Tables TAB = make_tables();
typedef __attribute__((address_space(1))) unsigned gu32;
typedef __attribute__((address_space(1))) unsigned long long gu64;
#define RLX_AGENT __ATOMIC_RELAXED, __HIP_MEMORY_SCOPE_AGENT
#define TAG_OF(e) (((unsigned long long)~((e) + 1u) << 32) | ((e) + 1u))

__global__ __launch_bounds__(512, 2) void win_attn_kernel(const unsigned short* __restrict__ x, const unsigned short* __restrict__ dw,
                                                          const unsigned short* __restrict__ wqkv, const unsigned short* __restrict__ wo,
                                                          u32x4* wf, unsigned short* __restrict__ out) {
    __shared__ __attribute__((aligned(16))) char smem[LDS_BYTES];

    asm volatile("wk_code_begin:");
    const int tid = threadIdx.x;
    const int wave = __builtin_amdgcn_readfirstlane(tid >> 6);
    const int w = wave & 3;
    const unsigned ldsb = (unsigned)(size_t)smem;
#define ROLE_SETUP                                                                                 \
    int bx = blockIdx.x;                                                                           \
    asm volatile("" : "+s"(bx));                                                                   \
    bx = __builtin_amdgcn_readfirstlane(bx);                                                       \
    const unsigned* rec = TAB.rec[bx];                                                             \
    const unsigned en0 = rec[0];                                                                   \
    const int q0 = rec[72], cnt = rec[73];
    asm volatile(
        "s_load_dwordx8 s[24:31], %[ka], 0x0\n\t"
        "s_load_dwordx2 s[32:33], %[ka], 0x20\n\t"
        "s_mul_i32 s36, %[bx], %[recb]\n\t"
        "s_xor_b32 s37, %[wave], 4\n\t"
        "s_lshl2_add_u32 s38, s37, s36\n\t"
        "s_load_dword s38, %[tab], s38 offset:0x100\n\t"
        "s_load_dword s39, %[tab], s36\n\t"
        "v_mbcnt_lo_u32_b32 v1, -1, 0\n\t"
        "v_mbcnt_hi_u32_b32 v1, -1, v1\n\t"
        "s_and_b32 s44, %[bx], 7\n\t"
        "s_lshl_b32 s44, s44, 5\n\t"
        "s_lshr_b32 s45, %[bx], 3\n\t"
        "s_add_u32 s44, s44, s45\n\t"
        "v_lshlrev_b32 v2, 4, v1\n\t"
        "s_mov_b32 s43, 0x20000\n\t"
        "s_cmp_ge_u32 s44, 36\n\t"
        "s_cbranch_scc1 .Lnp%=\n\t"
        "s_mul_i32 s45, s44, 6\n\t"
        "s_lshl1_add_u32 s46, s44, 210\n\t"
        "s_cmp_ge_u32 %[wave], 6\n\t"
        "s_cselect_b64 vcc, -1, 0\n\t"
        "s_cselect_b32 s45, s46, s45\n\t"
        "s_movk_i32 s52, 1152\n\t"
        "s_cselect_b32 s52, 384, s52\n\t"
        "s_cselect_b32 s53, 0, 0xbbaaaaab\n\t"
        "s_add_u32 s45, s45, %[wave]\n\t"
        "s_mul_i32 s46, s45, 171\n\t"
        "s_lshr_b32 s46, s46, 10\n\t"
        "s_mul_i32 s47, s46, 6\n\t"
        "s_sub_u32 s47, s45, s47\n\t"
        "s_mul_i32 s48, s46, 43\n\t"
        "s_lshr_b32 s48, s48, 8\n\t"
        "s_mul_i32 s49, s48, 6\n\t"
        "s_sub_u32 s49, s46, s49\n\t"
        "s_mul_i32 s50, s48, 48\n\t"
        "s_cmp_lt_u32 s49, 3\n\t"
        "s_cselect_b32 s34, 1.0, 0x3e553b94\n\t"
        "s_movk_i32 s51, -48\n\t"
        "s_cselect_b32 s51, 192, s51\n\t"
        "s_cmp_lt_u32 s48, 4\n\t"
        "s_cselect_b32 s51, s51, s50\n\t"
        "s_cselect_b32 s34, s34, 1.0\n\t"
        "s_add_u32 s51, s51, s50\n\t"
        "s_lshl4_add_u32 s51, s49, s51\n\t"
        "s_and_b32 s49, s49, 1\n\t"
        "s_mul_i32 s49, s49, 12\n\t"
        "s_add_u32 s49, s49, 576\n\t"
        "s_cmp_ge_u32 %[wave], 6\n\t"
        "s_cselect_b32 s49, s49, 0\n\t"
        "s_sub_u32 s51, s51, s49\n\t"
        "s_and_b32 s49, s47, 1\n\t"
        "s_mul_i32 s49, s49, 88\n\t"
        "s_lshl3_add_u32 s47, s47, s49\n\t"
        "v_lshrrev_b32 v3, 5, v1\n\t"
        "v_mul_u32_u24 v3, 48, v3\n\t"
        "v_lshrrev_b32 v4, 1, v1\n\t"
        "v_and_or_b32 v3, v4, 8, v3\n\t"
        "v_add_u32 v3, s47, v3\n\t"
        "v_mul_u32_u24 v3, s52, v3\n\t"
        "v_and_b32 v4, 15, v1\n\t"
        "v_and_b32 v5, 12, v1\n\t"
        "v_cndmask_b32 v5, 0, v5, vcc\n\t"
        "v_add3_u32 v5, v4, v5, s51\n\t"
        "v_lshl_add_u32 v3, v5, 1, v3\n\t"
        "v_mov_b64 v[8:9], 0\n\t"
        "v_mov_b64 v[10:11], 0\n\t"
        "v_mov_b64 v[12:13], 0\n\t"
        "v_mov_b64 v[14:15], 0\n\t"
        "s_mul_i32 s42, s52, 192\n\t"
        "s_cmp_ge_u32 %[wave], 6\n\t"
        "s_waitcnt lgkmcnt(0)\n\t"
        "s_cselect_b64 s[40:41], s[30:31], s[28:29]\n\t"
        "s_and_b32 s41, s41, 0xffff\n\t"
        "buffer_load_short_d16_hi v8, v3, s[40:43], 0 offen\n\t"
        "buffer_load_short_d16_hi v9, v3, s[40:43], s52 offen\n\t"
        "s_lshl_b32 s46, s52, 1\n\t"
        "buffer_load_short_d16_hi v10, v3, s[40:43], s46 offen\n\t"
        "s_add_u32 s46, s46, s52\n\t"
        "buffer_load_short_d16_hi v11, v3, s[40:43], s46 offen\n\t"
        "s_add_u32 s46, s46, s52\n\t"
        "buffer_load_short_d16_hi v12, v3, s[40:43], s46 offen\n\t"
        "s_add_u32 s46, s46, s52\n\t"
        "buffer_load_short_d16_hi v13, v3, s[40:43], s46 offen\n\t"
        "s_add_u32 s46, s46, s52\n\t"
        "buffer_load_short_d16_hi v14, v3, s[40:43], s46 offen\n\t"
        "s_add_u32 s46, s46, s52\n\t"
        "buffer_load_short_d16_hi v15, v3, s[40:43], s46 offen\n\t"
        "v_mov_b32 v6, %[ep]\n\t"
        "global_load_dword v7, v6, s[32:33] sc1\n\t"
        "v_mov_b32 v5, %[red]\n\t"
        "v_mad_u32_u24 v4, v4, %[c144], v5\n\t"
        "v_lshrrev_b32 v5, 2, v1\n\t"
        "v_and_b32 v5, 12, v5\n\t"
        "s_lshl_b32 s46, %[wave], 4\n\t"
        "v_add3_u32 v5, v4, v5, s46\n\t"
        "s_mov_b32 s40, s32\n\t"
        "s_and_b32 s41, s33, 0xffff\n\t"
        "s_mov_b32 s42, %[wsb]\n\t"
        "s_lshl_b32 s45, s45, 10\n\t"
        "s_waitcnt vmcnt(1)\n\t"
        "v_add_f32 v16, v8, v10\n\t"
        "v_add_f32 v17, v9, v11\n\t"
        "v_add_f32 v18, v12, v14\n\t"
        "v_add_f32 v19, v13, v15\n\t"
        "v_add_f32 v16, v16, v18\n\t"
        "v_add_f32 v17, v17, v19\n\t"
        "v_add_f32 v16, v16, v17\n\t"
        "ds_write_b32 v5, v16\n\t"
        "s_waitcnt lgkmcnt(0)\n\t"
        "s_barrier\n\t"
        "ds_read_b128 v[16:19], v4\n\t"
        "ds_read_b128 v[20:23], v4 offset:16\n\t"
        "ds_read_b128 v[24:27], v4 offset:32\n\t"
        "ds_read_b128 v[28:31], v4 offset:48\n\t"
        "ds_read_b128 v[32:35], v4 offset:64\n\t"
        "ds_read_b128 v[36:39], v4 offset:80\n\t"
        "s_waitcnt lgkmcnt(4)\n\t"
        "v_add_f32 v16, v16, v18\n\t"
        "v_add_f32 v17, v17, v19\n\t"
        "v_add_f32 v20, v20, v22\n\t"
        "v_add_f32 v21, v21, v23\n\t"
        "s_waitcnt lgkmcnt(2)\n\t"
        "v_add_f32 v24, v24, v26\n\t"
        "v_add_f32 v25, v25, v27\n\t"
        "v_add_f32 v28, v28, v30\n\t"
        "v_add_f32 v29, v29, v31\n\t"
        "v_add_f32 v16, v16, v20\n\t"
        "v_add_f32 v17, v17, v21\n\t"
        "s_waitcnt lgkmcnt(0)\n\t"
        "v_add_f32 v32, v32, v34\n\t"
        "v_add_f32 v33, v33, v35\n\t"
        "v_add_f32 v36, v36, v38\n\t"
        "v_add_f32 v37, v37, v39\n\t"
        "v_add_f32 v24, v24, v28\n\t"
        "v_add_f32 v25, v25, v29\n\t"
        "v_add_f32 v32, v32, v36\n\t"
        "v_add_f32 v33, v33, v37\n\t"
        "v_add_f32 v16, v16, v24\n\t"
        "v_add_f32 v17, v17, v25\n\t"
        "v_add_f32 v16, v16, v32\n\t"
        "v_add_f32 v17, v17, v33\n\t"
        "v_add_f32 v16, v16, v17\n\t"
        "v_mul_f32 v16, s53, v16\n\t"
        "v_mul_f32 v16, s34, v16\n\t"
        "v_fma_f32 v8, v8, s34, v16\n\t"
        "v_fma_f32 v9, v9, s34, v16\n\t"
        "v_fma_f32 v10, v10, s34, v16\n\t"
        "v_fma_f32 v11, v11, s34, v16\n\t"
        "v_fma_f32 v12, v12, s34, v16\n\t"
        "v_fma_f32 v13, v13, s34, v16\n\t"
        "v_fma_f32 v14, v14, s34, v16\n\t"
        "v_fma_f32 v15, v15, s34, v16\n\t"
        "v_cvt_pk_bf16_f32 v8, v8, v9\n\t"
        "v_cvt_pk_bf16_f32 v9, v10, v11\n\t"
        "v_cvt_pk_bf16_f32 v10, v12, v13\n\t"
        "v_cvt_pk_bf16_f32 v11, v14, v15\n\t"
        "buffer_store_dwordx4 v[8:11], v2, s[40:43], s45 offen sc1\n\t"
        "s_or_b32 s46, s44, s37\n\t"
        "s_cmp_lg_u32 s46, 0\n\t"
        "s_cbranch_scc1 .Lnc%=\n\t"
        "v_mov_b32 v3, 0\n\t"
        "global_store_dword v6, v3, s[32:33] offset:256 sc1\n\t"
        ".Lnc%=:\n\t"
        "v_lshl_add_u32 v6, s44, 3, v6\n\t"
        "s_waitcnt vmcnt(0)\n\t"
        "s_barrier\n\t"
        "s_cmp_lg_u32 %[wave], 0\n\t"
        "s_cbranch_scc1 .Lnp%=\n\t"
        "v_add_u32 v4, 1, v7\n\t"
        "v_not_b32 v5, v4\n\t"
        "s_mov_b64 exec, 1\n\t"
        "global_store_dwordx2 v6, v[4:5], s[32:33] offset:512 sc1\n\t"
        "s_mov_b64 exec, -1\n\t"
        ".Lnp%=:\n\t"
        "s_getpc_b64 s[40:41]\n\t"
        ".Lwk_here%=:\n\t"
        "s_sub_u32 s40, s40, .Lwk_here%=-wk_code_begin\n\t"
        "s_subb_u32 s41, s41, 0\n\t"
        "s_and_b32 s41, s41, 0xffff\n\t"
        "s_mov_b32 s42, wk_code_end-wk_code_begin\n\t"
        "s_max_u32 s42, s42, wk_code_end1-wk_code_begin\n\t"
        "s_max_u32 s42, s42, wk_code_end2-wk_code_begin\n\t"
        "s_add_u32 m0, %[lds], %[sink]\n\t"
        "v_lshlrev_b32 v3, 7, v1\n\t"
        "s_lshl_b32 s45, %[wave], 13\n\t"
        "buffer_load_dword v3, s[40:43], s45 offen lds\n\t"
        "s_mov_b64 s[40:41], %[tab]\n\t"
        "s_and_b32 s41, s41, 0xffff\n\t"
        "s_mov_b32 s42, %[tabb]\n\t"
        "s_lshl_b32 s46, %[wave], 10\n\t"
        "s_add_u32 s47, s46, %[imgs]\n\t"
        "s_add_u32 s48, s46, %[imgd]\n\t"
        "s_lshl_b32 s49, %[wave], 8\n\t"
        "s_add_u32 s49, s49, %[mapd]\n\t"
        "s_add_u32 s50, s36, 128\n\t"
        "s_mov_b64 vcc, -1\n\t"
        "s_cmp_eq_u32 %[wave], 0\n\t"
        "s_cselect_b32 s47, s36, s47\n\t"
        "s_cselect_b32 s48, %[wld], s48\n\t"
        "s_cselect_b64 vcc, 0xff, vcc\n\t"
        "s_cmp_ge_u32 %[wave], 4\n\t"
        "s_cselect_b32 s47, s50, s47\n\t"
        "s_cselect_b32 s48, s49, s48\n\t"
        "s_cselect_b64 exec, 0xff, vcc\n\t"
        "s_add_u32 m0, %[lds], s48\n\t"
        "s_and_b32 s49, %[wave], 3\n\t"
        "buffer_load_dwordx4 v2, s[40:43], s47 offen lds\n\t"
        "s_mov_b64 exec, -1\n\t"
        "v_mul_u32_u24 v3, 43, v1\n\t"
        "v_lshrrev_b32 v3, 8, v3\n\t"
        "v_mad_u32_u24 v4, v3, %[c288], v2\n\t"
        "s_mul_i32 s49, s49, 96\n\t"
        "s_mul_i32 s50, s37, 6160\n\t"
        "s_add_u32 s50, s50, %[lb0]\n\t"
        "s_cmp_eq_u32 s37, 7\n\t"
        "s_cselect_b32 s50, %[lb7], s50\n\t"
        "s_add_u32 s46, s46, %[tp]\n\t"
        "s_waitcnt lgkmcnt(0)\n\t"
        "s_cmp_ge_u32 %[wave], 4\n\t"
        "s_cbranch_scc1 .Lib%=\n\t"
        "s_mov_b32 s40, s26\n\t"
        "s_and_b32 s41, s27, 0xffff\n\t"
        "s_movk_i32 s42, 3456\n\t"
        "s_add_u32 m0, %[lds], s46\n\t"
        "s_nop 0\n\t"
        "buffer_load_dwordx4 v4, s[40:43], s49 offen lds\n\t"
        ".Lib%=:\n\t"
        "s_sub_u32 s40, s24, %[xb]\n\t"
        "s_subb_u32 s41, s25, 0\n\t"
        "s_and_b32 s41, s41, 0xffff\n\t"
        "s_mov_b32 s42, %[xsz]\n\t"
        "s_cmp_eq_u32 s38, 2.0\n\t"
        "s_cbranch_scc1 .Lic%=\n\t"
        "s_mov_b32 exec_hi, 0xffff\n\t"
        "s_add_u32 m0, %[lds], s50\n\t"
        "s_mov_b32 s46, 4\n\t"
        "s_bitcmp1_b32 s38, 0\n\t"
        "s_cselect_b32 s47, 2.0, s38\n\t"
        "s_bitset0_b32 s38, 0\n\t"
        ".Lid%=:\n\t"
        "buffer_load_dwordx4 v2, s[40:43], s38 offen lds\n\t"
        "buffer_load_dwordx4 v2, s[40:43], s47 offen offset:768 lds\n\t"
        "s_add_u32 m0, m0, 1536\n\t"
        "s_add_u32 s38, s38, %[rb]\n\t"
        "s_add_u32 s47, s47, %[rb]\n\t"
        "s_sub_u32 s46, s46, 1\n\t"
        "s_cmp_lg_u32 s46, 0\n\t"
        "s_cbranch_scc1 .Lid%=\n\t"
        ".Lic%=:\n\t"
        "s_cmp_lt_u32 %[wave], 4\n\t"
        "s_cbranch_scc1 .Lie%=\n\t"
        "s_mov_b32 exec_hi, 0x3fffff\n\t"
        "s_and_b32 s46, s39, 0x10000000\n\t"
        "s_and_b32 s47, s39, 0x20000000\n\t"
        "v_cmp_eq_u32 vcc, 0, v3\n\t"
        "v_mov_b32 v5, s46\n\t"
        "v_mov_b32 v6, s47\n\t"
        "v_cndmask_b32 v5, 0, v5, vcc\n\t"
        "v_cmp_eq_u32 vcc, 8, v3\n\t"
        "s_and_b32 s46, s39, 0x7ffffff\n\t"
        "s_add_u32 s46, s46, s49\n\t"
        "v_cndmask_b32 v6, 0, v6, vcc\n\t"
        "v_or3_b32 v4, v4, v5, v6\n\t"
        "s_bitcmp1_b32 s39, 27\n\t"
        "s_cselect_b32 s47, 2.0, s46\n\t"
        "s_lshl_b32 s45, s37, 13\n\t"
        "s_add_u32 m0, %[lds], s45\n\t"
        "s_mov_b32 s48, 8\n\t"
        "buffer_load_dwordx4 v4, s[40:43], s47 offen lds\n\t"
        ".Lia%=:\n\t"
        "s_add_u32 m0, m0, 872\n\t"
        "s_add_u32 s46, s46, %[rb]\n\t"
        "s_sub_u32 s48, s48, 1\n\t"
        "buffer_load_dwordx4 v4, s[40:43], s46 offen lds\n\t"
        "s_cmp_lg_u32 s48, 0\n\t"
        "s_cbranch_scc1 .Lia%=\n\t"
        ".Lie%=:\n\t"
        "s_mov_b64 exec, -1\n\t"
        :: [ka] "s"(__builtin_amdgcn_kernarg_segment_ptr()), [bx] "s"(blockIdx.x), [wave] "s"(wave), [tab] "s"(&TAB), [lds] "s"(ldsb),
           [red] "s"(ldsb + O_OFF), [c144] "s"(144), [c288] "s"(288), [recb] "n"(RECN * 4), [ep] "n"(EP_OFF), [wsb] "n"(WS_BYTES), [sink] "n"(X_OFF + 18432),
           [tabb] "n"(sizeof(Tables)), [imgs] "n"(GRID * RECN * 4 - 1024), [imgd] "n"(AT_OFF + 18816 - 1024), [mapd] "n"(TP_OFF + 3072), [wld] "n"(WL_OFF),
           [lb0] "n"(LB_OFF(0)), [lb7] "n"(LB_OFF(7)), [tp] "n"(TP_OFF), [xb] "n"(XBIAS), [xsz] "n"(IMG * IMG * CH * 2 + XBIAS), [rb] "n"(ROWBYTES)
        : "v1", "v2", "v3", "v4", "v5", "v6", "v7", "v8", "v9", "v10", "v11", "v12", "v13", "v14", "v15", "v16", "v17", "v18", "v19", "v20", "v21", "v22", "v23", "v24", "v25", "v26", "v27", "v28", "v29", "v30", "v31", "v32", "v33", "v34", "v35", "v36", "v37", "v38", "v39", "s24", "s25", "s26", "s27", "s28", "s29", "s30", "s31", "s32", "s33", "s34", "s36", "s37", "s38", "s39", "s40", "s41", "s42", "s43", "s44", "s45", "s46", "s47", "s48", "s49", "s50", "s51", "s52", "s53", "m0", "vcc", "scc", "memory");
#define WAIT_GO                                                                                    \
        {                                                                                          \
            unsigned go_;                                                                          \
            asm volatile(".Lgo%=:\n\tds_read_b32 %0, %1\n\ts_waitcnt lgkmcnt(0)\n\tv_cmp_ne_u32 vcc, -1, %0\n\ts_nop 4\n\ts_cbranch_vccnz .Lgo%=" \
                         : "=&v"(go_) : "v"(ldsb + GO_OFF) : "vcc", "memory");                     \
        }
#define LANE_SETUP                                                                                 \
    int lane;                                                                                      \
    asm volatile("v_mbcnt_lo_u32_b32 %0, -1, 0\n\tv_mbcnt_hi_u32_b32 %0, -1, %0" : "=v"(lane));    \
    __builtin_assume(lane >= 0 && lane < 64);                                                      \
    const int g = lane >> 4, l15 = lane & 15, sw = l15 >> 1;                                       \
    int fE = X_OFF + l15 * 384 + ((g ^ sw) << 4);                                                  \
    OPQ(fE);
#define XPAT(r) (((r) < 48 ? (r) : 48) * 384 + (((2 * w) ^ (((r) >> 1) & 7)) << 4))
#define SPAT(r) ((w >> 1) * 1024 + (w & 1) * 8 + (r) * 16)
#define CONV_CONSTS(var) \
            const unsigned long long rlo = g == 0 ? ROWMAP[0] : (g == 1 ? ROWMAP[2] : (g == 2 ? ROWMAP[4] : ROWMAP[6])); \
            const unsigned long long rhi = g == 0 ? ROWMAP[1] : (g == 1 ? ROWMAP[3] : (g == 2 ? ROWMAP[5] : ROWMAP[7])); \
            int xrow = byte_of(rlo, rhi, l15); \
            if (lane == (int)((CLANE >> (8 * (var))) & 0xFF)) xrow = 48; \
            if (lane == 63) xrow = (CROW >> (8 * (var))) & 0xFF; \
            const unsigned pXw_ = XPAT(xrow); \
            const unsigned pSTw_ = (w >> 1) * 1024 + (w & 1) * 8 + xrow * 16; \
            const int oy = (0x5320 >> (4 * (l15 >> 2))) & 15, ox = (0x5320 >> (4 * (l15 & 3))) & 15; \
            const unsigned aB_ = ldsb + w * 8192 + (oy + (g >> 1)) * 872 + (ox + 2 * (g & 1)) * 96; \
            const int dyi = g - (l15 >> 3); \
            const unsigned aA_ = ldsb + AT_OFF + (l15 & 3) * 16 + \
                                 ((dyi < 0 || dyi > 2) ? 18816 : (dyi == 0 ? 0 : (dyi == 1 ? 6208 : 12544)) + ((l15 >> 2) & 1) * 3072 + w * 768); \
            const unsigned idx_ = (l15 & 3) * 0x5555u; \
            const unsigned se = (2 || l15 == 4 * g) ? 0x3F803F80u : 0u; \
            const u32x4 sel4 = {se, se, se, se};

    if (wave < 4) {
        ROLE_SETUP
        LANE_SETUP
        asm volatile("s_waitcnt vmcnt(0)" ::: "memory");
        if (lane < 48) {
            unsigned tp[9];
#pragma unroll
            for (int k = 0; k < 9; ++k) tp[k] = *(const unsigned short*)(smem + TP_OFF + w * 1024 + k * 96 + lane * 2);
            tp[4] |= 0x3F800000u;
#pragma unroll
            for (int dy = 0; dy < 3; ++dy) {
                char* p = smem + AT_OFF + (dy == 0 ? 0 : (dy == 1 ? 6208 : 12544)) + w * 768 + lane * 16;
                *(u32x4*)p = (u32x4){tp[3 * dy], tp[3 * dy + 1], tp[3 * dy + 2], 0u};
                *(u32x4*)(p + 3072) = (u32x4){0u, tp[3 * dy], tp[3 * dy + 1], tp[3 * dy + 2]};
            }
        }
        BAR();
        {
            const int dW = X_OFF + l15 * 384 + (((2 * w + (g >> 1)) ^ sw) << 4) + 8 * (g & 1);
            const unsigned rsb = ldsb + RS_BASE;
            const unsigned pE_ = fE - X_OFF, aO_ = ldsb + dW + (O_OFF - X_OFF), aZ_ = ldsb + AT_OFF + 18816, aVB_ = ldsb + V_OFF + w * 4608 + 1024 + lane * 8;
            unsigned aRSw_ = rsb + lane * 4;
            const unsigned pST_ = lane * 16, aPLw_ = rsb + lane * 2, aRSr_ = rsb + l15 * 4;
            const unsigned g16_ = g * 16, aPLr_ = (l15 & 3) ? aZ_ : rsb + g * 8;
            const unsigned loVL_ = ((2 * w + (l15 >> 3)) << 4) + (l15 & 7) * 2, loOL_ = ((2 * w + (g >> 1)) << 4) + 8 * (g & 1);
            const unsigned on = (l15 & 3) ? 0u : 0x3F803F80u;
            const u32x4 ones4 = {on, on, on, on};
            const unsigned snk = ldsb + O_OFF + 15360 + w * 192, kr = ldsb + KQL_OFF + l15 * 800;
            const bool ok1 = 16 + l15 < cnt, dg = g == (l15 >> 2);
            u32x4 pl = {kr + w * 192 + g * 16, (ok1 ? kr + 12800 + w * 192 : snk) + g * 16, dg ? kr + 768 + w * 4 : snk, dg && ok1 ? kr + 12800 + 768 + w * 4 : snk};
            unsigned pcl, pch, len;
            asm volatile("s_getpc_b64 s[36:37]\n.Lwk_h2%=:\n\ts_sub_u32 s36, s36, .Lwk_h2%=-wk_code_begin\n\ts_subb_u32 s37, s37, 0\n\t"
                         "s_mov_b32 %2, wk_code_end-wk_code_begin\n\ts_max_u32 %2, %2, wk_code_end1-wk_code_begin" : "={s36}"(pcl), "={s37}"(pch), "=s"(len) :: "scc");
            pcl = __builtin_amdgcn_readfirstlane(pcl); pch = __builtin_amdgcn_readfirstlane(pch); len = __builtin_amdgcn_readfirstlane(len);
            const unsigned long long tb = w == 0 ? ((unsigned long long)pch << 32) | pcl : (unsigned long long)wf;
            const u32x4 tr = {(unsigned)tb, (unsigned)(tb >> 32) & 0xffffu, w == 0 ? len : (w == 1 ? (unsigned)WF_BYTES : SH_OFF + bx * 12288 + 12288), 0x00020000u};
            const unsigned to = w == 0 ? ((bx >> 3) % 6) * 8192 : (w == 1 ? 6 * 36864 + ((bx >> 3) % 9) * 8192 : SH_OFF + bx * 12288 + (w - 2) * 8192);
            asm volatile("" :: "{v244}"(pE_), "{v245}"(aO_), "{v246}"(aZ_), "{v247}"(aVB_), "{v248}"(pST_), "{v249}"(aRSw_), "{v250}"(aPLw_), "{v251}"(aRSr_), "{v252}"(g16_), "{v253}"(aPLr_), "{v254}"(loVL_), "{v255}"(loOL_), "{v[240:243]}"(ones4), "{v[232:235]}"(pl), "{s[36:39]}"(tr), "{s41}"(to));
            if (w == 0) {
                gu64* fp = (gu64*)((char*)wf + (lane < 36 ? FL_OFF + lane * 8 : EP_OFF));
#pragma unroll 1
                for (int it = 0; it < SPIN_MAX; ++it) {
                    const unsigned long long f = __hip_atomic_load(fp, RLX_AGENT);
                    const unsigned ep = __builtin_amdgcn_readlane((unsigned)f, 63);
                    if ((__builtin_amdgcn_ballot_w64(f != TAG_OF(ep)) & 0xfffffffffull) == 0ull) break;
                    __builtin_amdgcn_s_sleep(1);
                }
                __builtin_amdgcn_fence(__ATOMIC_ACQUIRE, "wavefront");
                asm volatile("ds_write_b32 %0, %1" :: "v"(ldsb + GO_OFF), "v"(~0u) : "memory");
            } else WAIT_GO
            __builtin_amdgcn_s_setprio(2);
            unsigned s44 = cnt, s48 = ldsb + X_OFF, s70 = 0;
            unsigned s72 = ldsb + KQL_OFF + w * 192, s73 = ldsb + OL_OFF, s77 = ldsb + OL_OFF, s76 = ldsb + KQL_OFF + 768 + w * 4;
            const unsigned long long wb = (unsigned long long)wf;
            const u32x4 wr = {(unsigned)wb, (unsigned)(wb >> 32) & 0xffffu, (unsigned)WS_BYTES, 0x00020000u};
            asm volatile(
            "s_setprio 2\n\t"
            "s_add_u32 s49, s33, 0\n\t"
            "buffer_load_dwordx4 v[0:3], v248, s[28:31], s49 offen sc1\n\t"
            "s_add_u32 s49, s33, 1024\n\t"
            "buffer_load_dwordx4 v[4:7], v248, s[28:31], s49 offen sc1\n\t"
            "s_add_u32 s49, s33, 2048\n\t"
            "buffer_load_dwordx4 v[8:11], v248, s[28:31], s49 offen sc1\n\t"
            "s_add_u32 s49, s33, 3072\n\t"
            "buffer_load_dwordx4 v[12:15], v248, s[28:31], s49 offen sc1\n\t"
            "s_add_u32 s49, s33, 4096\n\t"
            "buffer_load_dwordx4 v[16:19], v248, s[28:31], s49 offen sc1\n\t"
            "s_add_u32 s49, s33, 5120\n\t"
            "buffer_load_dwordx4 v[20:23], v248, s[28:31], s49 offen sc1\n\t"
            "s_add_u32 s49, s33, 6144\n\t"
            "buffer_load_dwordx4 v[24:27], v248, s[28:31], s49 offen sc1\n\t"
            "s_add_u32 s49, s33, 7168\n\t"
            "buffer_load_dwordx4 v[28:31], v248, s[28:31], s49 offen sc1\n\t"
            "s_add_u32 s49, s33, 8192\n\t"
            "buffer_load_dwordx4 v[32:35], v248, s[28:31], s49 offen sc1\n\t"
            "s_add_u32 s49, s33, 9216\n\t"
            "buffer_load_dwordx4 v[36:39], v248, s[28:31], s49 offen sc1\n\t"
            "s_add_u32 s49, s33, 10240\n\t"
            "buffer_load_dwordx4 v[40:43], v248, s[28:31], s49 offen sc1\n\t"
            "s_add_u32 s49, s33, 11264\n\t"
            "buffer_load_dwordx4 v[44:47], v248, s[28:31], s49 offen sc1\n\t"
            "s_add_u32 s49, s33, 12288\n\t"
            "buffer_load_dwordx4 v[48:51], v248, s[28:31], s49 offen sc1\n\t"
            "s_add_u32 s49, s33, 13312\n\t"
            "buffer_load_dwordx4 v[52:55], v248, s[28:31], s49 offen sc1\n\t"
            "s_add_u32 s49, s33, 14336\n\t"
            "buffer_load_dwordx4 v[56:59], v248, s[28:31], s49 offen sc1\n\t"
            "s_add_u32 s49, s33, 15360\n\t"
            "buffer_load_dwordx4 v[60:63], v248, s[28:31], s49 offen sc1\n\t"
            "v_mov_b32 v224, s46\n\t"
            ".Ls1%=:\n\t"
            "ds_read_b128 v[220:223], v224\n\t"
            "s_waitcnt lgkmcnt(0)\n\t"
            "v_and_b32 v220, v220, v221\n\t"
            "v_and_b32 v222, v222, v223\n\t"
            "v_and_b32 v220, v220, v222\n\t"
            "s_nop 0\n\t"
            "v_readfirstlane_b32 s49, v220\n\t"
            "s_nop 3\n\t"
            "s_bitcmp1_b32 s49, 27\n\t"
            "s_cbranch_scc0 .Ls1%=\n\t"
            "s_nop 7\n\t"
            "v_add_u32 v237, s35, v248\n\t"
            "v_add_u32 v238, s56, v244\n\t"
            "v_xor_b32 v239, 64, v238\n\t"
            "v_add_u32 v236, s56, v248\n\t"
            "ds_read_b128 v[200:203], v237\n\t"
            "ds_read_b128 v[204:207], v237 offset:1024\n\t"
            "ds_read_b128 v[144:147], v238\n\t"
            "ds_read_b128 v[148:151], v239\n\t"
            "ds_read_b128 v[152:155], v238 offset:128\n\t"
            "ds_read_b128 v[156:159], v239 offset:128\n\t"
            "ds_read_b128 v[160:163], v238 offset:256\n\t"
            "ds_read_b128 v[164:167], v239 offset:256\n\t"
            "ds_read_b128 v[168:171], v238 offset:6144\n\t"
            "ds_read_b128 v[172:175], v239 offset:6144\n\t"
            "ds_read_b128 v[176:179], v238 offset:6272\n\t"
            "ds_read_b128 v[180:183], v239 offset:6272\n\t"
            "ds_read_b128 v[184:187], v238 offset:6400\n\t"
            "ds_read_b128 v[188:191], v239 offset:6400\n\t"
            "ds_read_b128 v[208:211], v237 offset:2048\n\t"
            "s_waitcnt lgkmcnt(14)\n\t"
            "ds_read_b128 v[224:227], v236 offset:12288\n\t"
            "s_waitcnt lgkmcnt(14)\n\t"
            "ds_read_b128 v[228:231], v236 offset:13312\n\t"
            "s_add_u32 s49, s33, 16384\n\t"
            "buffer_load_dwordx4 v[64:67], v248, s[28:31], s49 offen sc1\n\t"
            "s_add_u32 s49, s33, 17408\n\t"
            "buffer_load_dwordx4 v[68:71], v248, s[28:31], s49 offen sc1\n\t"
            "s_waitcnt vmcnt(12)\n\t"
            "s_waitcnt lgkmcnt(14)\n\t"
            "v_mfma_f32_16x16x32_bf16 v[192:195], v[0:3], v[144:147], 0\n\t"
            "buffer_store_dwordx4 v[200:203], v248, s[28:31], s34 offen\n\t"
            "s_waitcnt lgkmcnt(8)\n\t"
            "v_mfma_f32_16x16x32_bf16 v[212:215], v[0:3], v[168:171], 0\n\t"
            "buffer_store_dwordx4 v[204:207], v248, s[28:31], s34 offen offset:1024\n\t"
            "v_mfma_f32_16x16x32_bf16 v[192:195], v[4:7], v[148:151], v[192:195]\n\t"
            "s_waitcnt lgkmcnt(2)\n\t"
            "buffer_store_dwordx4 v[208:211], v248, s[28:31], s34 offen offset:2048\n\t"
            "v_mfma_f32_16x16x32_bf16 v[212:215], v[4:7], v[172:175], v[212:215]\n\t"
            "s_add_u32 s49, s33, 18432\n\t"
            "buffer_load_dwordx4 v[72:75], v248, s[28:31], s49 offen sc1\n\t"
            "v_mfma_f32_16x16x32_bf16 v[192:195], v[8:11], v[152:155], v[192:195]\n\t"
            "s_add_u32 s49, s33, 19456\n\t"
            "buffer_load_dwordx4 v[76:79], v248, s[28:31], s49 offen sc1\n\t"
            "v_mfma_f32_16x16x32_bf16 v[212:215], v[8:11], v[176:179], v[212:215]\n\t"
            "s_add_u32 s49, s33, 20480\n\t"
            "buffer_load_dwordx4 v[80:83], v248, s[28:31], s49 offen sc1\n\t"
            "v_mfma_f32_16x16x32_bf16 v[192:195], v[12:15], v[156:159], v[192:195]\n\t"
            "s_add_u32 s49, s33, 21504\n\t"
            "buffer_load_dwordx4 v[84:87], v248, s[28:31], s49 offen sc1\n\t"
            "v_mfma_f32_16x16x32_bf16 v[212:215], v[12:15], v[180:183], v[212:215]\n\t"
            "s_add_u32 s49, s33, 22528\n\t"
            "buffer_load_dwordx4 v[88:91], v248, s[28:31], s49 offen sc1\n\t"
            "v_mfma_f32_16x16x32_bf16 v[192:195], v[16:19], v[160:163], v[192:195]\n\t"
            "s_add_u32 s49, s33, 23552\n\t"
            "buffer_load_dwordx4 v[92:95], v248, s[28:31], s49 offen sc1\n\t"
            "v_mfma_f32_16x16x32_bf16 v[212:215], v[16:19], v[184:187], v[212:215]\n\t"
            "s_waitcnt lgkmcnt(1)\n\t"
            "v_add_f32 v224, v224, v226\n\t"
            "s_waitcnt lgkmcnt(0)\n\t"
            "v_add_f32 v228, v228, v230\n\t"
            "v_mfma_f32_16x16x32_bf16 v[192:195], v[20:23], v[164:167], v[192:195]\n\t"
            "v_add_f32 v224, v224, v228\n\t"
            "v_add_f32 v225, v225, v227\n\t"
            "v_mfma_f32_16x16x32_bf16 v[212:215], v[20:23], v[188:191], v[212:215]\n\t"
            "s_waitcnt vmcnt(15)\n\t"
            "s_add_u32 s49, s33, 24576\n\t"
            "buffer_load_dwordx4 v[96:99], v248, s[28:31], s49 offen sc1\n\t"
            "v_mfma_f32_16x16x32_bf16 v[196:199], v[24:27], v[144:147], 0\n\t"
            "s_add_u32 s49, s33, 25600\n\t"
            "buffer_load_dwordx4 v[100:103], v248, s[28:31], s49 offen sc1\n\t"
            "v_mfma_f32_16x16x32_bf16 v[216:219], v[24:27], v[168:171], 0\n\t"
            "s_add_u32 s49, s33, 26624\n\t"
            "buffer_load_dwordx4 v[104:107], v248, s[28:31], s49 offen sc1\n\t"
            "v_mfma_f32_16x16x32_bf16 v[196:199], v[28:31], v[148:151], v[196:199]\n\t"
            "s_add_u32 s49, s33, 27648\n\t"
            "buffer_load_dwordx4 v[108:111], v248, s[28:31], s49 offen sc1\n\t"
            "v_mfma_f32_16x16x32_bf16 v[216:219], v[28:31], v[172:175], v[216:219]\n\t"
            "s_add_u32 s49, s33, 28672\n\t"
            "buffer_load_dwordx4 v[112:115], v248, s[28:31], s49 offen sc1\n\t"
            "v_mfma_f32_16x16x32_bf16 v[196:199], v[32:35], v[152:155], v[196:199]\n\t"
            "s_add_u32 s49, s33, 29696\n\t"
            "buffer_load_dwordx4 v[116:119], v248, s[28:31], s49 offen sc1\n\t"
            "v_mfma_f32_16x16x32_bf16 v[216:219], v[32:35], v[176:179], v[216:219]\n\t"
            "v_add_f32 v229, v229, v231\n\t"
            "v_mul_f32 v224, 0x3baaaaab, v224\n\t"
            "v_mfma_f32_16x16x32_bf16 v[196:199], v[36:39], v[156:159], v[196:199]\n\t"
            "v_add_f32 v225, v225, v229\n\t"
            "v_mul_f32_e64 v224, v224, -v224\n\t"
            "v_mfma_f32_16x16x32_bf16 v[216:219], v[36:39], v[180:183], v[216:219]\n\t"
            "v_fmac_f32 v224, 0x3baaaaab, v225\n\t"
            "v_max_f32 v224, 0, v224\n\t"
            "v_mfma_f32_16x16x32_bf16 v[196:199], v[40:43], v[160:163], v[196:199]\n\t"
            "v_add_f32 v224, 0x3727c5ac, v224\n\t"
            "v_rsq_f32 v224, v224\n\t"
            "v_mfma_f32_16x16x32_bf16 v[216:219], v[40:43], v[184:187], v[216:219]\n\t"
            "ds_write_b32 v249, v224\n\t"
            "v_mfma_f32_16x16x32_bf16 v[196:199], v[44:47], v[164:167], v[196:199]\n\t"
            "ds_read_b32 v236, v251\n\t"
            "v_mfma_f32_16x16x32_bf16 v[216:219], v[44:47], v[188:191], v[216:219]\n\t"
            "ds_read_b32 v237, v251 offset:64\n\t"
            "s_waitcnt vmcnt(15)\n\t"
            "v_mfma_f32_16x16x32_bf16 v[200:203], v[48:51], v[144:147], 0\n\t"
            "s_add_u32 s49, s33, 30720\n\t"
            "buffer_load_dwordx4 v[120:123], v248, s[28:31], s49 offen sc1\n\t"
            "v_mfma_f32_16x16x32_bf16 v[220:223], v[48:51], v[168:171], 0\n\t"
            "s_add_u32 s49, s33, 31744\n\t"
            "buffer_load_dwordx4 v[124:127], v248, s[28:31], s49 offen sc1\n\t"
            "v_mfma_f32_16x16x32_bf16 v[200:203], v[52:55], v[148:151], v[200:203]\n\t"
            "s_add_u32 s49, s33, 32768\n\t"
            "buffer_load_dwordx4 v[128:131], v248, s[28:31], s49 offen sc1\n\t"
            "v_mfma_f32_16x16x32_bf16 v[220:223], v[52:55], v[172:175], v[220:223]\n\t"
            "s_add_u32 s49, s33, 33792\n\t"
            "buffer_load_dwordx4 v[132:135], v248, s[28:31], s49 offen sc1\n\t"
            "v_mfma_f32_16x16x32_bf16 v[200:203], v[56:59], v[152:155], v[200:203]\n\t"
            "s_add_u32 s49, s33, 34816\n\t"
            "buffer_load_dwordx4 v[136:139], v248, s[28:31], s49 offen sc1\n\t"
            "v_mfma_f32_16x16x32_bf16 v[220:223], v[56:59], v[176:179], v[220:223]\n\t"
            "s_add_u32 s49, s33, 35840\n\t"
            "buffer_load_dwordx4 v[140:143], v248, s[28:31], s49 offen sc1\n\t"
            "s_setprio 0\n\t"
            "v_mfma_f32_16x16x32_bf16 v[200:203], v[60:63], v[156:159], v[200:203]\n\t"
            "s_waitcnt lgkmcnt(1)\n\t"
            "v_mul_f32 v192, v236, v192\n\t"
            "v_mul_f32 v193, v236, v193\n\t"
            "v_mfma_f32_16x16x32_bf16 v[220:223], v[60:63], v[180:183], v[220:223]\n\t"
            "s_waitcnt lgkmcnt(0)\n\t"
            "v_mul_f32 v212, v237, v212\n\t"
            "v_mul_f32 v213, v237, v213\n\t"
            "v_mfma_f32_16x16x32_bf16 v[200:203], v[64:67], v[160:163], v[200:203]\n\t"
            "v_mul_f32 v194, v236, v194\n\t"
            "v_mul_f32 v195, v236, v195\n\t"
            "v_mfma_f32_16x16x32_bf16 v[220:223], v[64:67], v[184:187], v[220:223]\n\t"
            "v_cvt_pk_bf16_f32 v192, v192, v193\n\t"
            "v_mul_f32 v214, v237, v214\n\t"
            "v_mfma_f32_16x16x32_bf16 v[200:203], v[68:71], v[164:167], v[200:203]\n\t"
            "v_mul_f32 v215, v237, v215\n\t"
            "v_cvt_pk_bf16_f32 v212, v212, v213\n\t"
            "v_mfma_f32_16x16x32_bf16 v[220:223], v[68:71], v[188:191], v[220:223]\n\t"
            "s_waitcnt vmcnt(12)\n\t"
            "v_cvt_pk_bf16_f32 v193, v194, v195\n\t"
            "v_mfma_f32_16x16x32_bf16 v[204:207], v[72:75], v[144:147], 0\n\t"
            "v_cvt_pk_bf16_f32 v213, v214, v215\n\t"
            "v_mul_f32 v196, v236, v196\n\t"
            "v_mfma_f32_16x16x32_bf16 v[224:227], v[72:75], v[168:171], 0\n\t"
            "v_mul_f32 v197, v236, v197\n\t"
            "v_mul_f32 v198, v236, v198\n\t"
            "v_mfma_f32_16x16x32_bf16 v[204:207], v[76:79], v[148:151], v[204:207]\n\t"
            "v_mul_f32 v199, v236, v199\n\t"
            "v_mul_f32 v216, v237, v216\n\t"
            "v_mfma_f32_16x16x32_bf16 v[224:227], v[76:79], v[172:175], v[224:227]\n\t"
            "v_mul_f32 v217, v237, v217\n\t"
            "v_mul_f32 v218, v237, v218\n\t"
            "v_mfma_f32_16x16x32_bf16 v[204:207], v[80:83], v[152:155], v[204:207]\n\t"
            "v_mul_f32 v219, v237, v219\n\t"
            "v_cvt_pk_bf16_f32 v194, v196, v197\n\t"
            "v_mfma_f32_16x16x32_bf16 v[224:227], v[80:83], v[176:179], v[224:227]\n\t"
            "v_cvt_pk_bf16_f32 v195, v198, v199\n\t"
            "v_cvt_pk_bf16_f32 v214, v216, v217\n\t"
            "v_mfma_f32_16x16x32_bf16 v[204:207], v[84:87], v[156:159], v[204:207]\n\t"
            "v_cvt_pk_bf16_f32 v215, v218, v219\n\t"
            "v_mul_f32 v200, v236, v200\n\t"
            "v_mfma_f32_16x16x32_bf16 v[224:227], v[84:87], v[180:183], v[224:227]\n\t"
            "v_mul_f32 v201, v236, v201\n\t"
            "v_mul_f32 v220, v237, v220\n\t"
            "v_mfma_f32_16x16x32_bf16 v[204:207], v[88:91], v[160:163], v[204:207]\n\t"
            "v_mul_f32 v221, v237, v221\n\t"
            "v_mul_f32 v202, v236, v202\n\t"
            "v_mfma_f32_16x16x32_bf16 v[224:227], v[88:91], v[184:187], v[224:227]\n\t"
            "v_mul_f32 v203, v236, v203\n\t"
            "v_cvt_pk_bf16_f32 v200, v200, v201\n\t"
            "v_mfma_f32_16x16x32_bf16 v[204:207], v[92:95], v[164:167], v[204:207]\n\t"
            "v_mul_f32 v222, v237, v222\n\t"
            "v_mul_f32 v223, v237, v223\n\t"
            "v_mfma_f32_16x16x32_bf16 v[224:227], v[92:95], v[188:191], v[224:227]\n\t"
            "s_waitcnt vmcnt(6)\n\t"
            "v_cvt_pk_bf16_f32 v220, v220, v221\n\t"
            "v_mfma_f32_16x16x32_bf16 v[196:199], v[96:99], v[144:147], 0\n\t"
            "v_cvt_pk_bf16_f32 v201, v202, v203\n\t"
            "v_cvt_pk_bf16_f32 v221, v222, v223\n\t"
            "v_mfma_f32_16x16x32_bf16 v[216:219], v[96:99], v[168:171], 0\n\t"
            "v_mul_f32 v204, v236, v204\n\t"
            "v_mul_f32 v205, v236, v205\n\t"
            "v_mfma_f32_16x16x32_bf16 v[196:199], v[100:103], v[148:151], v[196:199]\n\t"
            "v_mul_f32 v224, v237, v224\n\t"
            "v_mul_f32 v225, v237, v225\n\t"
            "v_mfma_f32_16x16x32_bf16 v[216:219], v[100:103], v[172:175], v[216:219]\n\t"
            "v_mul_f32 v206, v236, v206\n\t"
            "v_mul_f32 v207, v236, v207\n\t"
            "v_mfma_f32_16x16x32_bf16 v[196:199], v[104:107], v[152:155], v[196:199]\n\t"
            "v_cvt_pk_bf16_f32 v204, v204, v205\n\t"
            "v_mul_f32 v226, v237, v226\n\t"
            "v_mfma_f32_16x16x32_bf16 v[216:219], v[104:107], v[176:179], v[216:219]\n\t"
            "v_mul_f32 v227, v237, v227\n\t"
            "v_cvt_pk_bf16_f32 v224, v224, v225\n\t"
            "v_mfma_f32_16x16x32_bf16 v[196:199], v[108:111], v[156:159], v[196:199]\n\t"
            "v_cvt_pk_bf16_f32 v205, v206, v207\n\t"
            "v_cvt_pk_bf16_f32 v225, v226, v227\n\t"
            "v_mfma_f32_16x16x32_bf16 v[216:219], v[108:111], v[180:183], v[216:219]\n\t"
            "ds_write_b128 v232, v[192:195]\n\t"
            "v_mfma_f32_16x16x32_bf16 v[196:199], v[112:115], v[160:163], v[196:199]\n\t"
            "ds_write_b128 v233, v[212:215]\n\t"
            "v_mfma_f32_16x16x32_bf16 v[216:219], v[112:115], v[184:187], v[216:219]\n\t"
            "v_mfma_f32_16x16x32_bf16 v[196:199], v[116:119], v[164:167], v[196:199]\n\t"
            "v_mfma_f32_16x16x32_bf16 v[216:219], v[116:119], v[188:191], v[216:219]\n\t"
            "s_waitcnt vmcnt(0)\n\t"
            "v_mfma_f32_16x16x32_bf16 v[208:211], v[120:123], v[144:147], 0\n\t"
            "v_mfma_f32_16x16x32_bf16 v[228:231], v[120:123], v[168:171], 0\n\t"
            "v_mfma_f32_16x16x32_bf16 v[208:211], v[124:127], v[148:151], v[208:211]\n\t"
            "v_mfma_f32_16x16x32_bf16 v[228:231], v[124:127], v[172:175], v[228:231]\n\t"
            "s_nop 1\n\t"
            "v_mul_f32 v196, v236, v196\n\t"
            "v_mfma_f32_16x16x32_bf16 v[208:211], v[128:131], v[152:155], v[208:211]\n\t"
            "v_mul_f32 v197, v236, v197\n\t"
            "v_mul_f32 v198, v236, v198\n\t"
            "v_mfma_f32_16x16x32_bf16 v[228:231], v[128:131], v[176:179], v[228:231]\n\t"
            "v_mul_f32 v199, v236, v199\n\t"
            "v_mul_f32 v216, v237, v216\n\t"
            "v_mfma_f32_16x16x32_bf16 v[208:211], v[132:135], v[156:159], v[208:211]\n\t"
            "v_mul_f32 v217, v237, v217\n\t"
            "v_mul_f32 v218, v237, v218\n\t"
            "v_mfma_f32_16x16x32_bf16 v[228:231], v[132:135], v[180:183], v[228:231]\n\t"
            "v_mul_f32 v219, v237, v219\n\t"
            "v_cvt_pk_bf16_f32 v206, v196, v197\n\t"
            "v_mfma_f32_16x16x32_bf16 v[208:211], v[136:139], v[160:163], v[208:211]\n\t"
            "v_cvt_pk_bf16_f32 v207, v198, v199\n\t"
            "v_cvt_pk_bf16_f32 v226, v216, v217\n\t"
            "v_mfma_f32_16x16x32_bf16 v[228:231], v[136:139], v[184:187], v[228:231]\n\t"
            "v_cvt_pk_bf16_f32 v227, v218, v219\n\t"
            "v_mfma_f32_16x16x32_bf16 v[208:211], v[140:143], v[164:167], v[208:211]\n\t"
            "ds_write_b128 v232, v[204:207] offset:64\n\t"
            "v_mfma_f32_16x16x32_bf16 v[228:231], v[140:143], v[188:191], v[228:231]\n\t"
            "ds_write_b128 v233, v[224:227] offset:64\n\t"
            "s_nop 4\n\t"
            "v_mul_f32 v208, v236, v208\n\t"
            "v_mul_f32 v209, v236, v209\n\t"
            "v_mul_f32 v210, v236, v210\n\t"
            "v_mul_f32 v211, v236, v211\n\t"
            "v_cvt_pk_bf16_f32 v202, v208, v209\n\t"
            "v_cvt_pk_bf16_f32 v203, v210, v211\n\t"
            "v_mul_f32 v228, v237, v228\n\t"
            "v_mul_f32 v229, v237, v229\n\t"
            "v_mfma_f32_16x16x16_bf16 v[196:199], v[200:201], v[202:203], 0\n\t"
            "v_mul_f32 v230, v237, v230\n\t"
            "v_mul_f32 v231, v237, v231\n\t"
            "v_cvt_pk_bf16_f32 v222, v228, v229\n\t"
            "v_cvt_pk_bf16_f32 v223, v230, v231\n\t"
            "s_nop 1\n\t"
            "v_mfma_f32_16x16x16_bf16 v[216:219], v[220:221], v[222:223], 0\n\t"
            "ds_write_b128 v232, v[200:203] offset:128\n\t"
            "v_mfma_f32_16x16x32_bf16 v[196:199], v[192:195], v[204:207], v[196:199]\n\t"
            "ds_write_b128 v233, v[220:223] offset:128\n\t"
            "s_nop 2\n\t"
            "v_mfma_f32_16x16x32_bf16 v[216:219], v[212:215], v[224:227], v[216:219]\n\t"
            "s_nop 2\n\t"
            "v_cndmask_b32_e64 v196, v196, v197, s[50:51]\n\t"
            "v_cndmask_b32_e64 v196, v196, v198, s[52:53]\n\t"
            "v_cndmask_b32_e64 v196, v196, v199, s[54:55]\n\t"
            "ds_write_b32 v234, v196\n\t"
            "s_nop 0\n\t"
            "v_cndmask_b32_e64 v216, v216, v217, s[50:51]\n\t"
            "v_cndmask_b32_e64 v216, v216, v218, s[52:53]\n\t"
            "v_cndmask_b32_e64 v216, v216, v219, s[54:55]\n\t"
            "ds_write_b32 v235, v216\n\t"
            "s_waitcnt lgkmcnt(0)\n\t"
            "s_nop 7\n\t"
            "s_cmp_lg_u32 s33, 0\n\t"
            "s_cbranch_scc1 .Lar%=\n\t"
            "s_mov_b64 exec, 1\n\t"
            "v_mov_b32 v216, 1\n\t"
            "s_mov_b32 s49, 295168\n\t"
            "buffer_atomic_add v216, off, s[28:31], s49\n\t"
            "s_mov_b64 exec, -1\n\t"
            ".Lar%=:\n\t"
            "v_mov_b32 v224, s46\n\t"
            ".Ls2%=:\n\t"
            "ds_read_b128 v[220:223], v224\n\t"
            "s_waitcnt lgkmcnt(0)\n\t"
            "v_and_b32 v220, v220, v221\n\t"
            "v_and_b32 v222, v222, v223\n\t"
            "v_and_b32 v220, v220, v222\n\t"
            "s_nop 0\n\t"
            "v_readfirstlane_b32 s49, v220\n\t"
            "s_nop 3\n\t"
            "s_bitcmp1_b32 s49, 31\n\t"
            "s_cbranch_scc0 .Ls2%=\n\t"
            "s_waitcnt lgkmcnt(0)\n\t"
            "s_nop 7\n\t"
            "s_setprio 0\n\t"
            "v_add_u32 v232, s48, v244\n\t"
            "v_xor_b32 v233, 64, v232\n\t"
            "v_add_u32 v208, s48, v248\n\t"
            "v_add_u32 v234, s72, v252\n\t"
            "v_cndmask_b32_e64 v235, v246, v234, s[50:51]\n\t"
            "v_cndmask_b32_e64 v234, v246, v234, s[26:27]\n\t"
            "v_mov_b32 v239, s76\n\t"
            "ds_read_b128 v[216:219], v232\n\t"
            "ds_read_b128 v[220:223], v233\n\t"
            "ds_read_b128 v[224:227], v232 offset:128\n\t"
            "ds_read_b128 v[204:207], v208 offset:18816\n\t"
            "ds_read_b128 v[208:211], v208 offset:19840\n\t"
            "ds_read_b128 v[188:191], v233 offset:6272\n\t"
            "ds_read_b128 v[192:195], v232 offset:6400\n\t"
            "ds_read_b128 v[200:203], v233 offset:6400\n\t"
            "ds_read_b32 v239, v239\n\t"
            "s_waitcnt lgkmcnt(8)\n\t"
            "v_mfma_f32_16x16x32_bf16 v[144:147], v[0:3], v[216:219], 0\n\t"
            "s_waitcnt lgkmcnt(7)\n\t"
            "v_mfma_f32_16x16x32_bf16 v[144:147], v[4:7], v[220:223], v[144:147]\n\t"
            "s_waitcnt lgkmcnt(5)\n\t"
            "v_add_f32 v204, v204, v206\n\t"
            "v_add_f32 v205, v205, v207\n\t"
            "v_mfma_f32_16x16x32_bf16 v[144:147], v[8:11], v[224:227], v[144:147]\n\t"
            "s_waitcnt lgkmcnt(4)\n\t"
            "v_add_f32 v208, v208, v210\n\t"
            "v_add_f32 v209, v209, v211\n\t"
            "v_mfma_f32_16x16x32_bf16 v[212:215], v[120:123], v[216:219], 0\n\t"
            "v_add_f32 v204, v204, v208\n\t"
            "v_add_f32 v205, v205, v209\n\t"
            "v_mfma_f32_16x16x32_bf16 v[212:215], v[124:127], v[220:223], v[212:215]\n\t"
            "v_mul_f32 v204, 0x3baaaaab, v204\n\t"
            "v_mul_f32_e64 v204, v204, -v204\n\t"
            "v_mfma_f32_16x16x32_bf16 v[212:215], v[128:131], v[224:227], v[212:215]\n\t"
            "v_fmac_f32 v204, 0x3baaaaab, v205\n\t"
            "v_max_f32 v204, 0, v204\n\t"
            "v_mfma_f32_16x16x32_bf16 v[152:155], v[48:51], v[216:219], 0\n\t"
            "v_add_f32 v204, 0x3727c5ac, v204\n\t"
            "v_rsq_f32 v204, v204\n\t"
            "v_mfma_f32_16x16x32_bf16 v[152:155], v[52:55], v[220:223], v[152:155]\n\t"
            "ds_write_b32 v249, v204\n\t"
            "v_mfma_f32_16x16x32_bf16 v[152:155], v[56:59], v[224:227], v[152:155]\n\t"
            "ds_read_b32 v236, v251\n\t"
            "v_mfma_f32_16x16x32_bf16 v[208:211], v[96:99], v[216:219], 0\n\t"
            "ds_read_b32 v237, v251 offset:64\n\t"
            "v_mfma_f32_16x16x32_bf16 v[208:211], v[100:103], v[220:223], v[208:211]\n\t"
            "ds_read_b32 v238, v251 offset:128\n\t"
            "v_mfma_f32_16x16x32_bf16 v[208:211], v[104:107], v[224:227], v[208:211]\n\t"
            "v_mfma_f32_16x16x32_bf16 v[148:151], v[72:75], v[216:219], 0\n\t"
            "v_mfma_f32_16x16x32_bf16 v[148:151], v[76:79], v[220:223], v[148:151]\n\t"
            "v_mfma_f32_16x16x32_bf16 v[148:151], v[80:83], v[224:227], v[148:151]\n\t"
            "v_mfma_f32_16x16x32_bf16 v[204:207], v[24:27], v[216:219], 0\n\t"
            "ds_read_b128 v[216:219], v233 offset:128\n\t"
            "v_mfma_f32_16x16x32_bf16 v[204:207], v[28:31], v[220:223], v[204:207]\n\t"
            "ds_read_b128 v[220:223], v232 offset:256\n\t"
            "v_mfma_f32_16x16x32_bf16 v[204:207], v[32:35], v[224:227], v[204:207]\n\t"
            "ds_read_b128 v[224:227], v233 offset:256\n\t"
            "s_waitcnt lgkmcnt(2)\n\t"
            "v_mfma_f32_16x16x32_bf16 v[144:147], v[12:15], v[216:219], v[144:147]\n\t"
            "s_waitcnt lgkmcnt(1)\n\t"
            "v_mfma_f32_16x16x32_bf16 v[144:147], v[16:19], v[220:223], v[144:147]\n\t"
            "s_waitcnt lgkmcnt(0)\n\t"
            "v_mfma_f32_16x16x32_bf16 v[144:147], v[20:23], v[224:227], v[144:147]\n\t"
            "v_mfma_f32_16x16x32_bf16 v[148:151], v[84:87], v[216:219], v[148:151]\n\t"
            "v_mfma_f32_16x16x32_bf16 v[148:151], v[88:91], v[220:223], v[148:151]\n\t"
            "v_mfma_f32_16x16x32_bf16 v[148:151], v[92:95], v[224:227], v[148:151]\n\t"
            "v_mfma_f32_16x16x32_bf16 v[152:155], v[60:63], v[216:219], v[152:155]\n\t"
            "v_mfma_f32_16x16x32_bf16 v[152:155], v[64:67], v[220:223], v[152:155]\n\t"
            "v_mfma_f32_16x16x32_bf16 v[152:155], v[68:71], v[224:227], v[152:155]\n\t"
            "s_nop 1\n\t"
            "v_mul_f32 v144, v236, v144\n\t"
            "v_mfma_f32_16x16x32_bf16 v[204:207], v[36:39], v[216:219], v[204:207]\n\t"
            "v_mul_f32 v145, v236, v145\n\t"
            "v_mul_f32 v146, v236, v146\n\t"
            "v_mfma_f32_16x16x32_bf16 v[204:207], v[40:43], v[220:223], v[204:207]\n\t"
            "v_mul_f32 v147, v236, v147\n\t"
            "v_cvt_pk_bf16_f32 v144, v144, v145\n\t"
            "v_mfma_f32_16x16x32_bf16 v[204:207], v[44:47], v[224:227], v[204:207]\n\t"
            "v_mul_f32 v152, v236, v152\n\t"
            "v_mul_f32 v153, v236, v153\n\t"
            "v_mfma_f32_16x16x32_bf16 v[208:211], v[108:111], v[216:219], v[208:211]\n\t"
            "v_mul_f32 v154, v236, v154\n\t"
            "v_mul_f32 v155, v236, v155\n\t"
            "v_mfma_f32_16x16x32_bf16 v[208:211], v[112:115], v[220:223], v[208:211]\n\t"
            "v_cvt_pk_bf16_f32 v152, v152, v153\n\t"
            "v_cvt_pk_bf16_f32 v153, v154, v155\n\t"
            "v_mfma_f32_16x16x32_bf16 v[208:211], v[116:119], v[224:227], v[208:211]\n\t"
            "v_mul_f32 v148, v236, v148\n\t"
            "v_mul_f32 v149, v236, v149\n\t"
            "v_mfma_f32_16x16x32_bf16 v[212:215], v[132:135], v[216:219], v[212:215]\n\t"
            "ds_read_b128 v[216:219], v232 offset:6144\n\t"
            "v_cvt_pk_bf16_f32 v145, v146, v147\n\t"
            "v_mfma_f32_16x16x32_bf16 v[212:215], v[136:139], v[220:223], v[212:215]\n\t"
            "ds_read_b128 v[220:223], v233 offset:6144\n\t"
            "v_mul_f32 v204, v236, v204\n\t"
            "v_mfma_f32_16x16x32_bf16 v[212:215], v[140:143], v[224:227], v[212:215]\n\t"
            "ds_read_b128 v[224:227], v232 offset:6272\n\t"
            "v_mul_f32 v205, v236, v205\n\t"
            "v_mul_f32 v206, v236, v206\n\t"
            "v_mul_f32 v207, v236, v207\n\t"
            "v_mul_f32 v150, v236, v150\n\t"
            "v_mul_f32 v151, v236, v151\n\t"
            "v_cvt_pk_bf16_f32 v148, v148, v149\n\t"
            "v_cvt_pk_bf16_f32 v146, v204, v205\n\t"
            "v_mul_f32 v212, v236, v212\n\t"
            "v_mul_f32 v213, v236, v213\n\t"
            "v_mul_f32 v214, v236, v214\n\t"
            "v_mul_f32 v215, v236, v215\n\t"
            "v_cvt_pk_bf16_f32 v154, v212, v213\n\t"
            "v_cvt_pk_bf16_f32 v155, v214, v215\n\t"
            "v_cvt_pk_bf16_f32 v147, v206, v207\n\t"
            "v_cvt_pk_bf16_f32 v149, v150, v151\n\t"
            "v_mul_f32 v208, v236, v208\n\t"
            "v_mul_f32 v209, v236, v209\n\t"
            "v_mul_f32 v210, v236, v210\n\t"
            "v_mul_f32 v211, v236, v211\n\t"
            "v_cvt_pk_bf16_f32 v150, v208, v209\n\t"
            "v_cvt_pk_bf16_f32 v151, v210, v211\n\t"
            "s_waitcnt lgkmcnt(2)\n\t"
            "v_mfma_f32_16x16x32_bf16 v[160:163], v[72:75], v[216:219], 0\n\t"
            "ds_read_b128 v[204:207], v232 offset:12288\n\t"
            "s_waitcnt lgkmcnt(2)\n\t"
            "v_mfma_f32_16x16x32_bf16 v[160:163], v[76:79], v[220:223], v[160:163]\n\t"
            "ds_read_b128 v[208:211], v233 offset:12288\n\t"
            "s_waitcnt lgkmcnt(2)\n\t"
            "v_mfma_f32_16x16x32_bf16 v[160:163], v[80:83], v[224:227], v[160:163]\n\t"
            "ds_read_b128 v[212:215], v232 offset:12416\n\t"
            "v_mfma_f32_16x16x32_bf16 v[160:163], v[84:87], v[188:191], v[160:163]\n\t"
            "v_mfma_f32_16x16x32_bf16 v[160:163], v[88:91], v[192:195], v[160:163]\n\t"
            "v_mfma_f32_16x16x32_bf16 v[160:163], v[92:95], v[200:203], v[160:163]\n\t"
            "v_mfma_f32_16x16x32_bf16 v[196:199], v[24:27], v[216:219], 0\n\t"
            "v_mfma_f32_16x16x32_bf16 v[196:199], v[28:31], v[220:223], v[196:199]\n\t"
            "v_mfma_f32_16x16x32_bf16 v[196:199], v[32:35], v[224:227], v[196:199]\n\t"
            "v_mfma_f32_16x16x32_bf16 v[196:199], v[36:39], v[188:191], v[196:199]\n\t"
            "v_mfma_f32_16x16x32_bf16 v[196:199], v[40:43], v[192:195], v[196:199]\n\t"
            "v_mfma_f32_16x16x32_bf16 v[196:199], v[44:47], v[200:203], v[196:199]\n\t"
            "s_nop 1\n\t"
            "v_mul_f32 v160, v237, v160\n\t"
            "v_mfma_f32_16x16x32_bf16 v[156:159], v[0:3], v[216:219], 0\n\t"
            "v_mul_f32 v161, v237, v161\n\t"
            "v_mul_f32 v162, v237, v162\n\t"
            "v_mfma_f32_16x16x32_bf16 v[156:159], v[4:7], v[220:223], v[156:159]\n\t"
            "v_mul_f32 v163, v237, v163\n\t"
            "v_cvt_pk_bf16_f32 v160, v160, v161\n\t"
            "v_mfma_f32_16x16x32_bf16 v[156:159], v[8:11], v[224:227], v[156:159]\n\t"
            "v_mul_f32 v196, v237, v196\n\t"
            "v_mul_f32 v197, v237, v197\n\t"
            "v_mfma_f32_16x16x32_bf16 v[156:159], v[12:15], v[188:191], v[156:159]\n\t"
            "v_mul_f32 v198, v237, v198\n\t"
            "v_mul_f32 v199, v237, v199\n\t"
            "v_mfma_f32_16x16x32_bf16 v[156:159], v[16:19], v[192:195], v[156:159]\n\t"
            "v_cvt_pk_bf16_f32 v161, v162, v163\n\t"
            "v_mfma_f32_16x16x32_bf16 v[156:159], v[20:23], v[200:203], v[156:159]\n\t"
            "v_mfma_f32_16x16x16_bf16 v[180:183], v[152:153], v[154:155], 0\n\t"
            "v_mfma_f32_16x16x32_bf16 v[164:167], v[48:51], v[216:219], 0\n\t"
            "v_mfma_f32_16x16x32_bf16 v[164:167], v[52:55], v[220:223], v[164:167]\n\t"
            "v_mfma_f32_16x16x32_bf16 v[164:167], v[56:59], v[224:227], v[164:167]\n\t"
            "v_mfma_f32_16x16x32_bf16 v[164:167], v[60:63], v[188:191], v[164:167]\n\t"
            "v_mfma_f32_16x16x32_bf16 v[164:167], v[64:67], v[192:195], v[164:167]\n\t"
            "s_nop 1\n\t"
            "v_mul_f32 v156, v237, v156\n\t"
            "v_mfma_f32_16x16x32_bf16 v[164:167], v[68:71], v[200:203], v[164:167]\n\t"
            "v_mul_f32 v157, v237, v157\n\t"
            "v_mul_f32 v158, v237, v158\n\t"
            "v_mfma_f32_16x16x32_bf16 v[228:231], v[96:99], v[216:219], 0\n\t"
            "v_mul_f32 v159, v237, v159\n\t"
            "v_cvt_pk_bf16_f32 v156, v156, v157\n\t"
            "v_mfma_f32_16x16x32_bf16 v[228:231], v[100:103], v[220:223], v[228:231]\n\t"
            "v_cvt_pk_bf16_f32 v157, v158, v159\n\t"
            "v_cvt_pk_bf16_f32 v158, v196, v197\n\t"
            "v_mfma_f32_16x16x32_bf16 v[228:231], v[104:107], v[224:227], v[228:231]\n\t"
            "v_cvt_pk_bf16_f32 v159, v198, v199\n\t"
            "v_mul_f32 v164, v237, v164\n\t"
            "v_mfma_f32_16x16x32_bf16 v[228:231], v[108:111], v[188:191], v[228:231]\n\t"
            "v_mul_f32 v165, v237, v165\n\t"
            "v_mul_f32 v166, v237, v166\n\t"
            "v_mfma_f32_16x16x32_bf16 v[228:231], v[112:115], v[192:195], v[228:231]\n\t"
            "v_mul_f32 v167, v237, v167\n\t"
            "v_cvt_pk_bf16_f32 v164, v164, v165\n\t"
            "v_mfma_f32_16x16x32_bf16 v[228:231], v[116:119], v[200:203], v[228:231]\n\t"
            "v_cvt_pk_bf16_f32 v165, v166, v167\n\t"
            "v_mfma_f32_16x16x32_bf16 v[180:183], v[144:147], v[148:151], v[180:183]\n\t"
            "v_mfma_f32_16x16x32_bf16 v[196:199], v[120:123], v[216:219], 0\n\t"
            "ds_read_b128 v[216:219], v233 offset:12416\n\t"
            "v_mfma_f32_16x16x32_bf16 v[196:199], v[124:127], v[220:223], v[196:199]\n\t"
            "ds_read_b128 v[220:223], v232 offset:12544\n\t"
            "v_mfma_f32_16x16x32_bf16 v[196:199], v[128:131], v[224:227], v[196:199]\n\t"
            "ds_read_b128 v[224:227], v233 offset:12544\n\t"
            "v_mul_f32 v228, v237, v228\n\t"
            "v_mfma_f32_16x16x32_bf16 v[196:199], v[132:135], v[188:191], v[196:199]\n\t"
            "v_mul_f32 v229, v237, v229\n\t"
            "v_mul_f32 v230, v237, v230\n\t"
            "v_mfma_f32_16x16x32_bf16 v[196:199], v[136:139], v[192:195], v[196:199]\n\t"
            "v_mul_f32 v231, v237, v231\n\t"
            "v_cvt_pk_bf16_f32 v162, v228, v229\n\t"
            "v_mfma_f32_16x16x32_bf16 v[196:199], v[140:143], v[200:203], v[196:199]\n\t"
            "v_cvt_pk_bf16_f32 v163, v230, v231\n\t"
            "v_exp_f32 v180, v180\n\t"
            "s_waitcnt lgkmcnt(5)\n\t"
            "v_mfma_f32_16x16x32_bf16 v[176:179], v[48:51], v[204:207], 0\n\t"
            "v_exp_f32 v181, v181\n\t"
            "s_waitcnt lgkmcnt(4)\n\t"
            "v_mfma_f32_16x16x32_bf16 v[176:179], v[52:55], v[208:211], v[176:179]\n\t"
            "v_exp_f32 v182, v182\n\t"
            "s_waitcnt lgkmcnt(3)\n\t"
            "v_mfma_f32_16x16x32_bf16 v[176:179], v[56:59], v[212:215], v[176:179]\n\t"
            "v_mul_f32 v196, v237, v196\n\t"
            "v_mul_f32 v197, v237, v197\n\t"
            "s_waitcnt lgkmcnt(2)\n\t"
            "v_mfma_f32_16x16x32_bf16 v[176:179], v[60:63], v[216:219], v[176:179]\n\t"
            "v_mul_f32 v198, v237, v198\n\t"
            "v_mul_f32 v199, v237, v199\n\t"
            "s_waitcnt lgkmcnt(1)\n\t"
            "v_mfma_f32_16x16x32_bf16 v[176:179], v[64:67], v[220:223], v[176:179]\n\t"
            "v_cvt_pk_bf16_f32 v166, v196, v197\n\t"
            "v_cvt_pk_bf16_f32 v167, v198, v199\n\t"
            "s_waitcnt lgkmcnt(0)\n\t"
            "v_mfma_f32_16x16x32_bf16 v[176:179], v[68:71], v[224:227], v[176:179]\n\t"
            "v_exp_f32 v183, v183\n\t"
            "v_cvt_pk_bf16_f32 v180, v180, v181\n\t"
            "v_cvt_pk_bf16_f32 v181, v182, v183\n\t"
            "v_mfma_f32_16x16x16_bf16 v[188:191], v[152:153], v[166:167], 0\n\t"
            "v_mfma_f32_16x16x16_bf16 v[192:195], v[164:165], v[166:167], 0\n\t"
            "v_mfma_f32_16x16x16_bf16 v[184:187], v[164:165], v[154:155], 0\n\t"
            "s_nop 1\n\t"
            "v_mul_f32 v176, v238, v176\n\t"
            "v_mfma_f32_16x16x32_bf16 v[168:171], v[0:3], v[204:207], 0\n\t"
            "v_mul_f32 v177, v238, v177\n\t"
            "v_mul_f32 v178, v238, v178\n\t"
            "v_mfma_f32_16x16x32_bf16 v[168:171], v[4:7], v[208:211], v[168:171]\n\t"
            "v_mul_f32 v179, v238, v179\n\t"
            "v_cvt_pk_bf16_f32 v176, v176, v177\n\t"
            "v_mfma_f32_16x16x32_bf16 v[168:171], v[8:11], v[212:215], v[168:171]\n\t"
            "v_cvt_pk_bf16_f32 v177, v178, v179\n\t"
            "v_mfma_f32_16x16x32_bf16 v[168:171], v[12:15], v[216:219], v[168:171]\n\t"
            "v_mfma_f32_16x16x32_bf16 v[168:171], v[16:19], v[220:223], v[168:171]\n\t"
            "v_mfma_f32_16x16x32_bf16 v[168:171], v[20:23], v[224:227], v[168:171]\n\t"
            "v_mfma_f32_16x16x32_bf16 v[188:191], v[144:147], v[160:163], v[188:191]\n\t"
            "v_mfma_f32_16x16x32_bf16 v[200:203], v[120:123], v[204:207], 0\n\t"
            "v_mfma_f32_16x16x32_bf16 v[200:203], v[124:127], v[208:211], v[200:203]\n\t"
            "v_mfma_f32_16x16x32_bf16 v[200:203], v[128:131], v[212:215], v[200:203]\n\t"
            "v_mfma_f32_16x16x32_bf16 v[200:203], v[132:135], v[216:219], v[200:203]\n\t"
            "v_mfma_f32_16x16x32_bf16 v[200:203], v[136:139], v[220:223], v[200:203]\n\t"
            "s_nop 1\n\t"
            "v_mul_f32 v168, v238, v168\n\t"
            "v_mfma_f32_16x16x32_bf16 v[200:203], v[140:143], v[224:227], v[200:203]\n\t"
            "v_exp_f32 v188, v188\n\t"
            "v_mfma_f32_16x16x32_bf16 v[192:195], v[156:159], v[160:163], v[192:195]\n\t"
            "v_exp_f32 v189, v189\n\t"
            "v_mfma_f32_16x16x32_bf16 v[172:175], v[72:75], v[204:207], 0\n\t"
            "v_exp_f32 v190, v190\n\t"
            "v_mfma_f32_16x16x32_bf16 v[172:175], v[76:79], v[208:211], v[172:175]\n\t"
            "v_exp_f32 v191, v191\n\t"
            "v_mfma_f32_16x16x32_bf16 v[172:175], v[80:83], v[212:215], v[172:175]\n\t"
            "v_cvt_pk_bf16_f32 v188, v188, v189\n\t"
            "v_cvt_pk_bf16_f32 v189, v190, v191\n\t"
            "v_mfma_f32_16x16x32_bf16 v[172:175], v[84:87], v[216:219], v[172:175]\n\t"
            "v_exp_f32 v192, v192\n\t"
            "v_mfma_f32_16x16x32_bf16 v[172:175], v[88:91], v[220:223], v[172:175]\n\t"
            "v_exp_f32 v193, v193\n\t"
            "v_mfma_f32_16x16x32_bf16 v[172:175], v[92:95], v[224:227], v[172:175]\n\t"
            "v_exp_f32 v194, v194\n\t"
            "v_mfma_f32_16x16x32_bf16 v[184:187], v[156:159], v[148:151], v[184:187]\n\t"
            "v_exp_f32 v195, v195\n\t"
            "v_mfma_f32_16x16x32_bf16 v[196:199], v[24:27], v[204:207], 0\n\t"
            "v_cvt_pk_bf16_f32 v190, v192, v193\n\t"
            "v_cvt_pk_bf16_f32 v191, v194, v195\n\t"
            "v_mfma_f32_16x16x32_bf16 v[196:199], v[28:31], v[208:211], v[196:199]\n\t"
            "v_mul_f32 v169, v238, v169\n\t"
            "v_mul_f32 v170, v238, v170\n\t"
            "v_mfma_f32_16x16x32_bf16 v[196:199], v[32:35], v[212:215], v[196:199]\n\t"
            "v_exp_f32 v184, v184\n\t"
            "v_mfma_f32_16x16x32_bf16 v[196:199], v[36:39], v[216:219], v[196:199]\n\t"
            "v_exp_f32 v185, v185\n\t"
            "v_mfma_f32_16x16x32_bf16 v[196:199], v[40:43], v[220:223], v[196:199]\n\t"
            "v_exp_f32 v186, v186\n\t"
            "v_mfma_f32_16x16x32_bf16 v[196:199], v[44:47], v[224:227], v[196:199]\n\t"
            "v_exp_f32 v187, v187\n\t"
            "v_cvt_pk_bf16_f32 v182, v184, v185\n\t"
            "v_cvt_pk_bf16_f32 v183, v186, v187\n\t"
            "v_mfma_f32_16x16x16_bf16 v[184:187], v[176:177], v[154:155], 0\n\t"
            "v_mul_f32 v171, v238, v171\n\t"
            "v_cvt_pk_bf16_f32 v168, v168, v169\n\t"
            "v_cvt_pk_bf16_f32 v169, v170, v171\n\t"
            "v_mfma_f32_16x16x16_bf16 v[192:195], v[176:177], v[166:167], 0\n\t"
            "v_mul_f32 v196, v238, v196\n\t"
            "v_mul_f32 v197, v238, v197\n\t"
            "v_mul_f32 v198, v238, v198\n\t"
            "v_mfma_f32_16x16x32_bf16 v[228:231], v[96:99], v[204:207], 0\n\t"
            "ds_read_b64 v[204:205], v235 offset:136\n\t"
            "v_mul_f32 v199, v238, v199\n\t"
            "v_mfma_f32_16x16x32_bf16 v[228:231], v[100:103], v[208:211], v[228:231]\n\t"
            "ds_read_b64 v[206:207], v234 offset:128\n\t"
            "v_cvt_pk_bf16_f32 v170, v196, v197\n\t"
            "v_mfma_f32_16x16x32_bf16 v[228:231], v[104:107], v[212:215], v[228:231]\n\t"
            "ds_read_b128 v[208:211], v234\n\t"
            "v_cvt_pk_bf16_f32 v171, v198, v199\n\t"
            "v_mfma_f32_16x16x32_bf16 v[228:231], v[108:111], v[216:219], v[228:231]\n\t"
            "ds_read_b128 v[212:215], v235 offset:64\n\t"
            "v_mul_f32 v172, v238, v172\n\t"
            "v_mfma_f32_16x16x32_bf16 v[228:231], v[112:115], v[220:223], v[228:231]\n\t"
            "v_mul_f32 v173, v238, v173\n\t"
            "v_mul_f32 v200, v238, v200\n\t"
            "v_mfma_f32_16x16x32_bf16 v[228:231], v[116:119], v[224:227], v[228:231]\n\t"
            "v_mul_f32 v201, v238, v201\n\t"
            "v_mul_f32 v202, v238, v202\n\t"
            "v_mfma_f32_16x16x32_bf16 v[184:187], v[168:171], v[148:151], v[184:187]\n\t"
            "v_mul_f32 v203, v238, v203\n\t"
            "v_mul_f32 v174, v238, v174\n\t"
            "v_mfma_f32_16x16x32_bf16 v[192:195], v[168:171], v[160:163], v[192:195]\n\t"
            "v_mul_f32 v175, v238, v175\n\t"
            "v_cvt_pk_bf16_f32 v172, v172, v173\n\t"
            "v_cvt_pk_bf16_f32 v178, v200, v201\n\t"
            "v_cvt_pk_bf16_f32 v179, v202, v203\n\t"
            "v_cvt_pk_bf16_f32 v173, v174, v175\n\t"
            "v_mul_f32 v228, v238, v228\n\t"
            "v_mul_f32 v229, v238, v229\n\t"
            "s_waitcnt lgkmcnt(2)\n\t"
            "v_mfma_f32_16x16x32_bf16 v[220:223], v[204:207], v[164:167], 0\n\t"
            "v_mul_f32 v230, v238, v230\n\t"
            "v_mul_f32 v231, v238, v231\n\t"
            "s_waitcnt lgkmcnt(1)\n\t"
            "v_mfma_f32_16x16x32_bf16 v[220:223], v[208:211], v[160:163], v[220:223]\n\t"
            "v_cvt_pk_bf16_f32 v174, v228, v229\n\t"
            "v_cvt_pk_bf16_f32 v175, v230, v231\n\t"
            "s_waitcnt lgkmcnt(0)\n\t"
            "v_mfma_f32_16x16x32_bf16 v[220:223], v[212:215], v[156:159], v[220:223]\n\t"
            "v_exp_f32 v184, v184\n\t"
            "v_mfma_f32_16x16x32_bf16 v[216:219], v[204:207], v[152:155], 0\n\t"
            "v_exp_f32 v185, v185\n\t"
            "v_mfma_f32_16x16x32_bf16 v[216:219], v[208:211], v[148:151], v[216:219]\n\t"
            "v_exp_f32 v186, v186\n\t"
            "v_mfma_f32_16x16x32_bf16 v[216:219], v[212:215], v[144:147], v[216:219]\n\t"
            "v_exp_f32 v187, v187\n\t"
            "v_cvt_pk_bf16_f32 v184, v184, v185\n\t"
            "v_cvt_pk_bf16_f32 v185, v186, v187\n\t"
            "v_mfma_f32_16x16x16_bf16 v[228:231], v[164:165], v[178:179], 0\n\t"
            "v_mov_b32 v187, 0\n\t"
            "v_exp_f32 v192, v192\n\t"
            "v_mfma_f32_16x16x16_bf16 v[200:203], v[176:177], v[178:179], 0\n\t"
            "v_exp_f32 v193, v193\n\t"
            "v_exp_f32 v216, v216\n\t"
            "v_mfma_f32_16x16x16_bf16 v[196:199], v[152:153], v[178:179], 0\n\t"
            "v_cvt_pk_bf16_f32 v216, v216, 0\n\t"
            "v_cndmask_b32_e64 v186, 0, v216, s[58:59]\n\t"
            "v_cndmask_b32_e64 v235, v217, v221, s[60:61]\n\t"
            "v_mfma_f32_16x16x32_bf16 v[224:227], v[204:207], v[176:179], 0\n\t"
            "v_exp_f32 v194, v194\n\t"
            "v_mfma_f32_16x16x32_bf16 v[224:227], v[208:211], v[172:175], v[224:227]\n\t"
            "v_exp_f32 v195, v195\n\t"
            "v_mfma_f32_16x16x32_bf16 v[224:227], v[212:215], v[168:171], v[224:227]\n\t"
            "v_exp_f32 v220, v220\n\t"
            "v_mfma_f32_16x16x32_bf16 v[204:207], v[240:243], v[180:183], 0\n\t"
            "v_cvt_pk_bf16_f32 v192, v192, v193\n\t"
            "v_cvt_pk_bf16_f32 v193, v194, v195\n\t"
            "v_mfma_f32_16x16x32_bf16 v[204:207], v[240:243], v[184:187], v[204:207]\n\t"
            "v_cvt_pk_bf16_f32 v220, v220, 0\n\t"
            "v_cndmask_b32_e64 v194, 0, v220, s[58:59]\n\t"
            "v_mfma_f32_16x16x32_bf16 v[200:203], v[168:171], v[172:175], v[200:203]\n\t"
            "v_exp_f32 v224, v224\n\t"
            "v_mfma_f32_16x16x32_bf16 v[196:199], v[144:147], v[172:175], v[196:199]\n\t"
            "v_cvt_pk_bf16_f32 v224, v224, 0\n\t"
            "v_cndmask_b32_e64 v235, v235, v225, s[62:63]\n\t"
            "v_mfma_f32_16x16x32_bf16 v[228:231], v[156:159], v[172:175], v[228:231]\n\t"
            "v_cndmask_b32_e64 v235, v235, v239, s[64:65]\n\t"
            "v_cmp_nlt_f32_e64 s[24:25], |v235|, s23\n\t"
            "v_exp_f32 v235, v235\n\t"
            "v_exp_f32 v200, v200\n\t"
            "v_exp_f32 v201, v201\n\t"
            "v_exp_f32 v196, v196\n\t"
            "v_exp_f32 v197, v197\n\t"
            "v_exp_f32 v198, v198\n\t"
            "v_exp_f32 v199, v199\n\t"
            "v_exp_f32 v228, v228\n\t"
            "v_exp_f32 v229, v229\n\t"
            "v_exp_f32 v230, v230\n\t"
            "v_exp_f32 v231, v231\n\t"
            "v_cvt_pk_bf16_f32 v196, v196, v197\n\t"
            "v_cvt_pk_bf16_f32 v197, v198, v199\n\t"
            "v_cvt_pk_bf16_f32 v198, v228, v229\n\t"
            "v_cvt_pk_bf16_f32 v199, v230, v231\n\t"
            "v_exp_f32 v202, v202\n\t"
            "v_exp_f32 v203, v203\n\t"
            "v_mfma_f32_16x16x32_bf16 v[212:215], v[240:243], v[196:199], 0\n\t"
            "v_cvt_pk_bf16_f32 v200, v200, v201\n\t"
            "v_cvt_pk_bf16_f32 v201, v202, v203\n\t"
            "v_cndmask_b32_e64 v202, 0, v224, s[58:59]\n\t"
            "v_mov_b32 v203, 0\n\t"
            "v_cvt_pk_bf16_f32 v235, v235, 0\n\t"
            "v_cndmask_b32_e64 v235, 0, v235, s[66:67]\n\t"
            "v_mfma_f32_16x16x32_bf16 v[212:215], v[240:243], v[200:203], v[212:215]\n\t"
            "ds_write_b16 v250, v235 offset:256\n\t"
            "v_mfma_f32_16x16x32_bf16 v[208:211], v[240:243], v[188:191], 0\n\t"
            "v_mov_b32 v195, 0\n\t"
            "v_add_u32 v205, 0xe0800000, v204\n\t"
            "v_rcp_f32 v236, v204\n\t"
            "v_mfma_f32_16x16x32_bf16 v[208:211], v[240:243], v[192:195], v[208:211]\n\t"
            "s_nop 1\n\t"
            "v_add_u32 v213, 0xe0800000, v212\n\t"
            "v_rcp_f32 v238, v212\n\t"
            "s_nop 3\n\t"
            "v_add_u32 v209, 0xe0800000, v208\n\t"
            "v_rcp_f32 v237, v208\n\t"
            "v_or3_b32 v205, v205, v209, v213\n\t"
            "v_cmp_le_u32 vcc, 2.0, v205\n\t"
            "s_waitcnt lgkmcnt(0)\n\t"
            "s_nop 7\n\t"
            "s_bfe_u32 s49, s70, 0x30001\n\t"
            "s_lshl_b32 s49, s49, 4\n\t"
            "s_or_b32 s73, s77, s49\n\t"
            "s_add_u32 s70, s70, 1\n\t"
            "s_add_u32 s76, s76, 800\n\t"
            "s_add_u32 s77, s77, 384\n\t"
            "s_xor_b32 s48, s48, s47\n\t"
            "s_sub_u32 s44, s44, 1\n\t"
            "s_branch .Lwa_in%=\n\t"
            ".p2align 6\n\t"
            ".Lwa_loop%=:\n\t"
            "ds_read_b64 v[156:157], v247\n\t"
            "v_add_u32 v235, s45, v248\n\t"
            "v_xor_b32 v234, s73, v254\n\t"
            "v_add_u32 v232, s48, v244\n\t"
            "v_xor_b32 v233, 64, v232\n\t"
            "v_mov_b32 v159, 0\n\t"
            "v_add_u32 v208, s48, v248\n\t"
            "v_mov_b32 v167, 0\n\t"
            "v_mov_b32 v163, 0\n\t"
            "v_mov_b32 v239, s76\n\t"
            "ds_read_b128 v[168:171], v235\n\t"
            "ds_read_u16 v158, v234\n\t"
            "ds_read_b128 v[216:219], v232\n\t"
            "ds_read_b128 v[220:223], v233\n\t"
            "ds_read_b128 v[224:227], v232 offset:128\n\t"
            "ds_read_b128 v[172:175], v235 offset:1536\n\t"
            "ds_read_b64 v[164:165], v247 offset:1536\n\t"
            "ds_read_u16 v166, v234 offset:128\n\t"
            "ds_read_b128 v[176:179], v235 offset:3072\n\t"
            "ds_read_b64 v[160:161], v247 offset:3072\n\t"
            "ds_read_u16 v162, v234 offset:256\n\t"
            "v_add_u32 v234, s72, v252\n\t"
            "v_cndmask_b32_e64 v235, v246, v234, s[50:51]\n\t"
            "v_cndmask_b32_e64 v234, v246, v234, s[26:27]\n\t"
            "ds_read_b128 v[204:207], v208 offset:18816\n\t"
            "ds_read_b128 v[208:211], v208 offset:19840\n\t"
            "s_waitcnt lgkmcnt(14)\n\t"
            "ds_read_b32 v239, v239\n\t"
            "s_waitcnt lgkmcnt(13)\n\t"
            "v_mfma_f32_16x16x32_bf16 v[228:231], v[168:171], v[180:183], 0\n\t"
            "s_waitcnt lgkmcnt(12)\n\t"
            "v_mfma_f32_16x16x32_bf16 v[228:231], v[156:159], v[184:187], v[228:231]\n\t"
            "s_waitcnt lgkmcnt(11)\n\t"
            "v_mfma_f32_16x16x32_bf16 v[144:147], v[0:3], v[216:219], 0\n\t"
            "s_waitcnt lgkmcnt(10)\n\t"
            "v_mfma_f32_16x16x32_bf16 v[144:147], v[4:7], v[220:223], v[144:147]\n\t"
            "s_waitcnt lgkmcnt(9)\n\t"
            "v_mfma_f32_16x16x32_bf16 v[144:147], v[8:11], v[224:227], v[144:147]\n\t"
            "v_mfma_f32_16x16x32_bf16 v[212:215], v[120:123], v[216:219], 0\n\t"
            "v_mfma_f32_16x16x32_bf16 v[212:215], v[124:127], v[220:223], v[212:215]\n\t"
            "s_waitcnt lgkmcnt(2)\n\t"
            "v_add_f32 v204, v204, v206\n\t"
            "v_add_f32 v205, v205, v207\n\t"
            "v_mfma_f32_16x16x32_bf16 v[212:215], v[128:131], v[224:227], v[212:215]\n\t"
            "v_mul_f32 v228, v236, v228\n\t"
            "v_mul_f32 v229, v236, v229\n\t"
            "v_mfma_f32_16x16x32_bf16 v[152:155], v[48:51], v[216:219], 0\n\t"
            "v_mul_f32 v230, v236, v230\n\t"
            "v_mul_f32 v231, v236, v231\n\t"
            "v_mfma_f32_16x16x32_bf16 v[152:155], v[52:55], v[220:223], v[152:155]\n\t"
            "v_cvt_pk_bf16_f32 v228, v228, v229\n\t"
            "v_cvt_pk_bf16_f32 v229, v230, v231\n\t"
            "v_mfma_f32_16x16x32_bf16 v[152:155], v[56:59], v[224:227], v[152:155]\n\t"
            "ds_write_b64 v245, v[228:229]\n\t"
            "v_mfma_f32_16x16x32_bf16 v[228:231], v[172:175], v[180:183], 0\n\t"
            "s_waitcnt lgkmcnt(2)\n\t"
            "v_add_f32 v208, v208, v210\n\t"
            "v_add_f32 v209, v209, v211\n\t"
            "v_mfma_f32_16x16x32_bf16 v[228:231], v[164:167], v[184:187], v[228:231]\n\t"
            "v_add_f32 v204, v204, v208\n\t"
            "v_add_f32 v205, v205, v209\n\t"
            "v_mfma_f32_16x16x32_bf16 v[180:183], v[176:179], v[180:183], 0\n\t"
            "v_mul_f32 v204, 0x3baaaaab, v204\n\t"
            "v_mul_f32_e64 v204, v204, -v204\n\t"
            "v_mfma_f32_16x16x32_bf16 v[180:183], v[160:163], v[184:187], v[180:183]\n\t"
            "v_fmac_f32 v204, 0x3baaaaab, v205\n\t"
            "v_max_f32 v204, 0, v204\n\t"
            "v_mfma_f32_16x16x32_bf16 v[208:211], v[96:99], v[216:219], 0\n\t"
            "v_add_f32 v204, 0x3727c5ac, v204\n\t"
            "v_mul_f32 v228, v236, v228\n\t"
            "v_mfma_f32_16x16x32_bf16 v[208:211], v[100:103], v[220:223], v[208:211]\n\t"
            "v_rsq_f32 v204, v204\n\t"
            "v_mfma_f32_16x16x32_bf16 v[208:211], v[104:107], v[224:227], v[208:211]\n\t"
            "ds_write_b32 v249, v204\n\t"
            "v_mfma_f32_16x16x32_bf16 v[184:187], v[168:171], v[188:191], 0\n\t"
            "v_mul_f32 v229, v236, v229\n\t"
            "v_mul_f32 v230, v236, v230\n\t"
            "v_mfma_f32_16x16x32_bf16 v[184:187], v[156:159], v[192:195], v[184:187]\n\t"
            "v_mul_f32 v231, v236, v231\n\t"
            "v_cvt_pk_bf16_f32 v228, v228, v229\n\t"
            "v_mfma_f32_16x16x32_bf16 v[148:151], v[72:75], v[216:219], 0\n\t"
            "v_cvt_pk_bf16_f32 v229, v230, v231\n\t"
            "v_mul_f32 v180, v236, v180\n\t"
            "v_mfma_f32_16x16x32_bf16 v[148:151], v[76:79], v[220:223], v[148:151]\n\t"
            "v_mul_f32 v181, v236, v181\n\t"
            "v_mul_f32 v182, v236, v182\n\t"
            "v_mfma_f32_16x16x32_bf16 v[148:151], v[80:83], v[224:227], v[148:151]\n\t"
            "ds_write_b64 v245, v[228:229] offset:128\n\t"
            "v_mfma_f32_16x16x32_bf16 v[204:207], v[24:27], v[216:219], 0\n\t"
            "ds_read_b128 v[216:219], v233 offset:128\n\t"
            "v_mul_f32 v183, v236, v183\n\t"
            "v_mfma_f32_16x16x32_bf16 v[204:207], v[28:31], v[220:223], v[204:207]\n\t"
            "ds_read_b128 v[220:223], v232 offset:256\n\t"
            "v_cvt_pk_bf16_f32 v180, v180, v181\n\t"
            "v_mfma_f32_16x16x32_bf16 v[204:207], v[32:35], v[224:227], v[204:207]\n\t"
            "ds_read_b128 v[224:227], v233 offset:256\n\t"
            "v_cvt_pk_bf16_f32 v181, v182, v183\n\t"
            "v_mfma_f32_16x16x32_bf16 v[228:231], v[172:175], v[188:191], 0\n\t"
            "ds_read_b32 v236, v251\n\t"
            "v_mul_f32 v184, v237, v184\n\t"
            "v_mfma_f32_16x16x32_bf16 v[228:231], v[164:167], v[192:195], v[228:231]\n\t"
            "ds_write_b64 v245, v[180:181] offset:256\n\t"
            "v_mfma_f32_16x16x32_bf16 v[188:191], v[176:179], v[188:191], 0\n\t"
            "v_mul_f32 v185, v237, v185\n\t"
            "v_mul_f32 v186, v237, v186\n\t"
            "v_mfma_f32_16x16x32_bf16 v[188:191], v[160:163], v[192:195], v[188:191]\n\t"
            "ds_read_b128 v[192:195], v232 offset:6400\n\t"
            "v_mul_f32 v187, v237, v187\n\t"
            "v_mfma_f32_16x16x32_bf16 v[180:183], v[168:171], v[196:199], 0\n\t"
            "v_mul_f32 v228, v237, v228\n\t"
            "v_mul_f32 v229, v237, v229\n\t"
            "v_mfma_f32_16x16x32_bf16 v[180:183], v[156:159], v[200:203], v[180:183]\n\t"
            "s_waitcnt lgkmcnt(5)\n\t"
            "v_mul_f32 v230, v237, v230\n\t"
            "v_mul_f32 v231, v237, v231\n\t"
            "v_mfma_f32_16x16x32_bf16 v[144:147], v[12:15], v[216:219], v[144:147]\n\t"
            "s_waitcnt lgkmcnt(4)\n\t"
            "v_cvt_pk_bf16_f32 v228, v228, v229\n\t"
            "v_cvt_pk_bf16_f32 v229, v230, v231\n\t"
            "v_mfma_f32_16x16x32_bf16 v[144:147], v[16:19], v[220:223], v[144:147]\n\t"
            "s_waitcnt lgkmcnt(3)\n\t"
            "v_cvt_pk_bf16_f32 v184, v184, v185\n\t"
            "v_cvt_pk_bf16_f32 v185, v186, v187\n\t"
            "v_mfma_f32_16x16x32_bf16 v[144:147], v[20:23], v[224:227], v[144:147]\n\t"
            "ds_read_b64 v[186:187], v253 offset:352\n\t"
            "v_mul_f32 v180, v238, v180\n\t"
            "v_mfma_f32_16x16x32_bf16 v[148:151], v[84:87], v[216:219], v[148:151]\n\t"
            "v_mul_f32 v181, v238, v181\n\t"
            "v_mul_f32 v182, v238, v182\n\t"
            "v_mfma_f32_16x16x32_bf16 v[148:151], v[88:91], v[220:223], v[148:151]\n\t"
            "v_mul_f32 v183, v238, v183\n\t"
            "v_cvt_pk_bf16_f32 v180, v180, v181\n\t"
            "v_mfma_f32_16x16x32_bf16 v[148:151], v[92:95], v[224:227], v[148:151]\n\t"
            "ds_write_b64 v245, v[228:229] offset:6272\n\t"
            "v_mfma_f32_16x16x32_bf16 v[152:155], v[60:63], v[216:219], v[152:155]\n\t"
            "v_cvt_pk_bf16_f32 v181, v182, v183\n\t"
            "v_mul_f32 v188, v237, v188\n\t"
            "v_mfma_f32_16x16x32_bf16 v[152:155], v[64:67], v[220:223], v[152:155]\n\t"
            "ds_read_b64 v[182:183], v253 offset:288\n\t"
            "v_mul_f32 v189, v237, v189\n\t"
            "v_mfma_f32_16x16x32_bf16 v[152:155], v[68:71], v[224:227], v[152:155]\n\t"
            "ds_write_b64 v245, v[180:181] offset:12288\n\t"
            "v_mfma_f32_16x16x32_bf16 v[228:231], v[172:175], v[196:199], 0\n\t"
            "ds_read_b64 v[180:181], v253 offset:256\n\t"
            "v_mul_f32 v190, v237, v190\n\t"
            "v_mfma_f32_16x16x32_bf16 v[228:231], v[164:167], v[200:203], v[228:231]\n\t"
            "ds_write_b64 v245, v[184:185] offset:6144\n\t"
            "v_mfma_f32_16x16x32_bf16 v[196:199], v[176:179], v[196:199], 0\n\t"
            "ds_read_b64 v[184:185], v253 offset:320\n\t"
            "v_mul_f32 v191, v237, v191\n\t"
            "v_mfma_f32_16x16x32_bf16 v[196:199], v[160:163], v[200:203], v[196:199]\n\t"
            "ds_read_b128 v[200:203], v233 offset:6400\n\t"
            "v_cvt_pk_bf16_f32 v188, v188, v189\n\t"
            "v_mfma_f32_16x16x32_bf16 v[204:207], v[36:39], v[216:219], v[204:207]\n\t"
            "ds_read_b32 v237, v251 offset:64\n\t"
            "v_cvt_pk_bf16_f32 v189, v190, v191\n\t"
            "v_mfma_f32_16x16x32_bf16 v[204:207], v[40:43], v[220:223], v[204:207]\n\t"
            "v_mul_f32 v228, v238, v228\n\t"
            "v_mul_f32 v229, v238, v229\n\t"
            "v_mfma_f32_16x16x32_bf16 v[204:207], v[44:47], v[224:227], v[204:207]\n\t"
            "ds_write_b64 v245, v[188:189] offset:6400\n\t"
            "v_mfma_f32_16x16x32_bf16 v[208:211], v[108:111], v[216:219], v[208:211]\n\t"
            "ds_read_b128 v[188:191], v233 offset:6272\n\t"
            "v_mul_f32 v230, v238, v230\n\t"
            "v_mfma_f32_16x16x32_bf16 v[208:211], v[112:115], v[220:223], v[208:211]\n\t"
            "v_mul_f32 v231, v238, v231\n\t"
            "v_cvt_pk_bf16_f32 v228, v228, v229\n\t"
            "v_mfma_f32_16x16x32_bf16 v[208:211], v[116:119], v[224:227], v[208:211]\n\t"
            "v_cvt_pk_bf16_f32 v229, v230, v231\n\t"
            "v_mul_f32 v196, v238, v196\n\t"
            "v_mfma_f32_16x16x32_bf16 v[212:215], v[132:135], v[216:219], v[212:215]\n\t"
            "ds_read_b128 v[216:219], v232 offset:6144\n\t"
            "v_mul_f32 v197, v238, v197\n\t"
            "v_mfma_f32_16x16x32_bf16 v[212:215], v[136:139], v[220:223], v[212:215]\n\t"
            "s_waitcnt lgkmcnt(14)\n\t"
            "ds_read_b128 v[220:223], v233 offset:6144\n\t"
            "v_mul_f32 v198, v238, v198\n\t"
            "v_mfma_f32_16x16x32_bf16 v[212:215], v[140:143], v[224:227], v[212:215]\n\t"
            "s_waitcnt lgkmcnt(14)\n\t"
            "ds_read_b128 v[224:227], v232 offset:6272\n\t"
            "v_mul_f32 v199, v238, v199\n\t"
            "s_waitcnt lgkmcnt(9)\n\t"
            "v_mfma_f32_16x16x32_bf16 v[168:171], v[168:171], v[180:183], 0\n\t"
            "s_waitcnt lgkmcnt(7)\n\t"
            "ds_read_b32 v238, v251 offset:128\n\t"
            "v_cvt_pk_bf16_f32 v196, v196, v197\n\t"
            "v_mfma_f32_16x16x32_bf16 v[168:171], v[156:159], v[184:187], v[168:171]\n\t"
            "ds_write_b64 v245, v[228:229] offset:12416\n\t"
            "v_mfma_f32_16x16x32_bf16 v[176:179], v[176:179], v[180:183], 0\n\t"
            "v_cvt_pk_bf16_f32 v197, v198, v199\n\t"
            "v_mul_f32 v152, v236, v152\n\t"
            "v_mfma_f32_16x16x32_bf16 v[176:179], v[160:163], v[184:187], v[176:179]\n\t"
            "ds_write_b64 v245, v[196:197] offset:12544\n\t"
            "v_mfma_f32_16x16x32_bf16 v[172:175], v[172:175], v[180:183], 0\n\t"
            "v_mul_f32 v153, v236, v153\n\t"
            "v_mul_f32 v154, v236, v154\n\t"
            "v_mfma_f32_16x16x32_bf16 v[172:175], v[164:167], v[184:187], v[172:175]\n\t"
            "s_waitcnt lgkmcnt(5)\n\t"
            "v_mul_f32 v155, v236, v155\n\t"
            "v_cvt_pk_bf16_f32 v152, v152, v153\n\t"
            "v_mfma_f32_16x16x32_bf16 v[160:163], v[72:75], v[216:219], 0\n\t"
            "s_waitcnt lgkmcnt(4)\n\t"
            "v_cvt_pk_bf16_f32 v153, v154, v155\n\t"
            "v_mul_f32 v212, v236, v212\n\t"
            "v_mfma_f32_16x16x32_bf16 v[160:163], v[76:79], v[220:223], v[160:163]\n\t"
            "s_waitcnt lgkmcnt(3)\n\t"
            "v_mul_f32 v213, v236, v213\n\t"
            "v_mul_f32 v214, v236, v214\n\t"
            "v_mfma_f32_16x16x32_bf16 v[160:163], v[80:83], v[224:227], v[160:163]\n\t"
            "v_mul_f32 v215, v236, v215\n\t"
            "v_cvt_pk_bf16_f32 v154, v212, v213\n\t"
            "v_mfma_f32_16x16x32_bf16 v[160:163], v[84:87], v[188:191], v[160:163]\n\t"
            "v_cvt_pk_bf16_f32 v155, v214, v215\n\t"
            "v_mul_f32 v144, v236, v144\n\t"
            "v_mfma_f32_16x16x32_bf16 v[160:163], v[88:91], v[192:195], v[160:163]\n\t"
            "ds_read_b128 v[212:215], v232 offset:12416\n\t"
            "v_mul_f32 v145, v236, v145\n\t"
            "v_mfma_f32_16x16x32_bf16 v[160:163], v[92:95], v[200:203], v[160:163]\n\t"
            "v_mul_f32 v146, v236, v146\n\t"
            "v_mul_f32 v147, v236, v147\n\t"
            "v_mfma_f32_16x16x32_bf16 v[228:231], v[240:243], v[180:183], 0\n\t"
            "v_cvt_pk_bf16_f32 v144, v144, v145\n\t"
            "v_mul_f32 v148, v236, v148\n\t"
            "v_mfma_f32_16x16x32_bf16 v[228:231], v[240:243], v[184:187], v[228:231]\n\t"
            "v_mul_f32 v149, v236, v149\n\t"
            "v_cvt_pk_bf16_f32 v145, v146, v147\n\t"
            "v_mfma_f32_16x16x32_bf16 v[196:199], v[24:27], v[216:219], 0\n\t"
            "v_mul_f32 v204, v236, v204\n\t"
            "v_mul_f32 v205, v236, v205\n\t"
            "v_mfma_f32_16x16x32_bf16 v[196:199], v[28:31], v[220:223], v[196:199]\n\t"
            "v_mul_f32 v206, v236, v206\n\t"
            "v_mul_f32 v207, v236, v207\n\t"
            "v_mfma_f32_16x16x32_bf16 v[196:199], v[32:35], v[224:227], v[196:199]\n\t"
            "v_rcp_f32 v229, v228\n\t"
            "v_mfma_f32_16x16x32_bf16 v[196:199], v[36:39], v[188:191], v[196:199]\n\t"
            "v_mul_f32 v168, v229, v168\n\t"
            "v_mul_f32 v169, v229, v169\n\t"
            "v_mfma_f32_16x16x32_bf16 v[196:199], v[40:43], v[192:195], v[196:199]\n\t"
            "v_mul_f32 v172, v229, v172\n\t"
            "v_mul_f32 v173, v229, v173\n\t"
            "v_mfma_f32_16x16x32_bf16 v[196:199], v[44:47], v[200:203], v[196:199]\n\t"
            "v_mul_f32 v176, v229, v176\n\t"
            "v_mul_f32 v177, v229, v177\n\t"
            "v_mfma_f32_16x16x32_bf16 v[156:159], v[0:3], v[216:219], 0\n\t"
            "v_mul_f32 v170, v229, v170\n\t"
            "v_mul_f32 v171, v229, v171\n\t"
            "v_mfma_f32_16x16x32_bf16 v[156:159], v[4:7], v[220:223], v[156:159]\n\t"
            "v_cvt_pk_bf16_f32 v168, v168, v169\n\t"
            "v_mul_f32 v174, v229, v174\n\t"
            "v_mfma_f32_16x16x32_bf16 v[156:159], v[8:11], v[224:227], v[156:159]\n\t"
            "v_mul_f32 v175, v229, v175\n\t"
            "v_cvt_pk_bf16_f32 v172, v172, v173\n\t"
            "v_mfma_f32_16x16x32_bf16 v[156:159], v[12:15], v[188:191], v[156:159]\n\t"
            "v_mul_f32 v178, v229, v178\n\t"
            "v_mul_f32 v179, v229, v179\n\t"
            "v_mfma_f32_16x16x32_bf16 v[156:159], v[16:19], v[192:195], v[156:159]\n\t"
            "v_cvt_pk_bf16_f32 v176, v176, v177\n\t"
            "v_xor_b32 v230, s73, v255\n\t"
            "v_mfma_f32_16x16x32_bf16 v[156:159], v[20:23], v[200:203], v[156:159]\n\t"
            "v_cvt_pk_bf16_f32 v169, v170, v171\n\t"
            "v_cvt_pk_bf16_f32 v173, v174, v175\n\t"
            "v_cvt_pk_bf16_f32 v177, v178, v179\n\t"
            "v_mul_f32 v150, v236, v150\n\t"
            "v_mfma_f32_16x16x16_bf16 v[180:183], v[152:153], v[154:155], 0\n\t"
            "s_mov_b64 exec, s[68:69]\n\t"
            "ds_write_b64 v230, v[168:169]\n\t"
            "ds_write_b64 v230, v[172:173] offset:128\n\t"
            "ds_write_b64 v230, v[176:177] offset:256\n\t"
            "s_mov_b64 exec, -1\n\t"
            "v_mfma_f32_16x16x32_bf16 v[164:167], v[48:51], v[216:219], 0\n\t"
            "v_mul_f32 v151, v236, v151\n\t"
            "v_cvt_pk_bf16_f32 v148, v148, v149\n\t"
            "v_mfma_f32_16x16x32_bf16 v[164:167], v[52:55], v[220:223], v[164:167]\n\t"
            "v_cvt_pk_bf16_f32 v146, v204, v205\n\t"
            "v_cvt_pk_bf16_f32 v147, v206, v207\n\t"
            "v_mfma_f32_16x16x32_bf16 v[164:167], v[56:59], v[224:227], v[164:167]\n\t"
            "ds_read_b128 v[204:207], v232 offset:12288\n\t"
            "v_cvt_pk_bf16_f32 v149, v150, v151\n\t"
            "v_mfma_f32_16x16x32_bf16 v[164:167], v[60:63], v[188:191], v[164:167]\n\t"
            "v_mul_f32 v208, v236, v208\n\t"
            "v_mul_f32 v209, v236, v209\n\t"
            "v_mfma_f32_16x16x32_bf16 v[164:167], v[64:67], v[192:195], v[164:167]\n\t"
            "v_mul_f32 v210, v236, v210\n\t"
            "v_mul_f32 v211, v236, v211\n\t"
            "v_mfma_f32_16x16x32_bf16 v[164:167], v[68:71], v[200:203], v[164:167]\n\t"
            "v_cvt_pk_bf16_f32 v150, v208, v209\n\t"
            "v_cvt_pk_bf16_f32 v151, v210, v211\n\t"
            "v_mfma_f32_16x16x32_bf16 v[228:231], v[96:99], v[216:219], 0\n\t"
            "ds_read_b128 v[208:211], v233 offset:12288\n\t"
            "v_mul_f32 v156, v237, v156\n\t"
            "v_mfma_f32_16x16x32_bf16 v[228:231], v[100:103], v[220:223], v[228:231]\n\t"
            "v_mul_f32 v157, v237, v157\n\t"
            "v_mul_f32 v158, v237, v158\n\t"
            "v_mfma_f32_16x16x32_bf16 v[228:231], v[104:107], v[224:227], v[228:231]\n\t"
            "v_mul_f32 v159, v237, v159\n\t"
            "v_cvt_pk_bf16_f32 v156, v156, v157\n\t"
            "v_mfma_f32_16x16x32_bf16 v[228:231], v[108:111], v[188:191], v[228:231]\n\t"
            "v_cvt_pk_bf16_f32 v157, v158, v159\n\t"
            "v_mul_f32 v196, v237, v196\n\t"
            "v_mfma_f32_16x16x32_bf16 v[228:231], v[112:115], v[192:195], v[228:231]\n\t"
            "v_mul_f32 v197, v237, v197\n\t"
            "v_mul_f32 v198, v237, v198\n\t"
            "v_mfma_f32_16x16x32_bf16 v[228:231], v[116:119], v[200:203], v[228:231]\n\t"
            "v_mul_f32 v199, v237, v199\n\t"
            "v_cvt_pk_bf16_f32 v158, v196, v197\n\t"
            "v_mfma_f32_16x16x32_bf16 v[180:183], v[144:147], v[148:151], v[180:183]\n\t"
            "v_cvt_pk_bf16_f32 v159, v198, v199\n\t"
            "v_mul_f32 v164, v237, v164\n\t"
            "v_mfma_f32_16x16x32_bf16 v[196:199], v[120:123], v[216:219], 0\n\t"
            "ds_read_b128 v[216:219], v233 offset:12416\n\t"
            "v_mul_f32 v165, v237, v165\n\t"
            "v_mfma_f32_16x16x32_bf16 v[196:199], v[124:127], v[220:223], v[196:199]\n\t"
            "ds_read_b128 v[220:223], v232 offset:12544\n\t"
            "v_mul_f32 v166, v237, v166\n\t"
            "v_mfma_f32_16x16x32_bf16 v[196:199], v[128:131], v[224:227], v[196:199]\n\t"
            "ds_read_b128 v[224:227], v233 offset:12544\n\t"
            "v_mul_f32 v167, v237, v167\n\t"
            "v_mfma_f32_16x16x32_bf16 v[196:199], v[132:135], v[188:191], v[196:199]\n\t"
            "v_cvt_pk_bf16_f32 v164, v164, v165\n\t"
            "v_cvt_pk_bf16_f32 v165, v166, v167\n\t"
            "v_mfma_f32_16x16x32_bf16 v[196:199], v[136:139], v[192:195], v[196:199]\n\t"
            "v_mul_f32 v160, v237, v160\n\t"
            "v_mul_f32 v161, v237, v161\n\t"
            "v_mfma_f32_16x16x32_bf16 v[196:199], v[140:143], v[200:203], v[196:199]\n\t"
            "s_waitcnt lgkmcnt(4)\n\t"
            "v_mul_f32 v162, v237, v162\n\t"
            "v_mul_f32 v163, v237, v163\n\t"
            "v_mfma_f32_16x16x32_bf16 v[176:179], v[48:51], v[204:207], 0\n\t"
            "s_barrier\n\t"
            "s_setprio 2\n\t"
            "v_cvt_pk_bf16_f32 v160, v160, v161\n\t"
            "v_cvt_pk_bf16_f32 v161, v162, v163\n\t"
            "s_waitcnt lgkmcnt(3)\n\t"
            "v_mfma_f32_16x16x32_bf16 v[176:179], v[52:55], v[208:211], v[176:179]\n\t"
            "v_mul_f32 v196, v237, v196\n\t"
            "v_mul_f32 v197, v237, v197\n\t"
            "v_mfma_f32_16x16x32_bf16 v[176:179], v[56:59], v[212:215], v[176:179]\n\t"
            "s_waitcnt lgkmcnt(2)\n\t"
            "v_mul_f32 v198, v237, v198\n\t"
            "v_mul_f32 v199, v237, v199\n\t"
            "v_mfma_f32_16x16x32_bf16 v[176:179], v[60:63], v[216:219], v[176:179]\n\t"
            "s_waitcnt lgkmcnt(1)\n\t"
            "v_cvt_pk_bf16_f32 v166, v196, v197\n\t"
            "v_cvt_pk_bf16_f32 v167, v198, v199\n\t"
            "v_mfma_f32_16x16x32_bf16 v[176:179], v[64:67], v[220:223], v[176:179]\n\t"
            "s_waitcnt lgkmcnt(0)\n\t"
            "v_mul_f32 v228, v237, v228\n\t"
            "v_mul_f32 v229, v237, v229\n\t"
            "v_mfma_f32_16x16x32_bf16 v[176:179], v[68:71], v[224:227], v[176:179]\n\t"
            "v_mul_f32 v230, v237, v230\n\t"
            "v_mul_f32 v231, v237, v231\n\t"
            "v_cvt_pk_bf16_f32 v162, v228, v229\n\t"
            "v_cvt_pk_bf16_f32 v163, v230, v231\n\t"
            "v_mfma_f32_16x16x16_bf16 v[188:191], v[152:153], v[166:167], 0\n\t"
            "v_exp_f32 v180, v180\n\t"
            "v_exp_f32 v181, v181\n\t"
            "v_mfma_f32_16x16x16_bf16 v[192:195], v[164:165], v[166:167], 0\n\t"
            "v_exp_f32 v182, v182\n\t"
            "v_exp_f32 v183, v183\n\t"
            "v_mfma_f32_16x16x16_bf16 v[184:187], v[164:165], v[154:155], 0\n\t"
            "v_mul_f32 v176, v238, v176\n\t"
            "v_mul_f32 v177, v238, v177\n\t"
            "v_mul_f32 v178, v238, v178\n\t"
            "v_mfma_f32_16x16x32_bf16 v[168:171], v[0:3], v[204:207], 0\n\t"
            "s_bfe_u32 s49, s70, 0x30001\n\t"
            "v_mul_f32 v179, v238, v179\n\t"
            "v_cvt_pk_bf16_f32 v176, v176, v177\n\t"
            "v_mfma_f32_16x16x32_bf16 v[168:171], v[4:7], v[208:211], v[168:171]\n\t"
            "s_lshl_b32 s49, s49, 4\n\t"
            "v_cvt_pk_bf16_f32 v180, v180, v181\n\t"
            "v_cvt_pk_bf16_f32 v177, v178, v179\n\t"
            "v_mfma_f32_16x16x32_bf16 v[168:171], v[8:11], v[212:215], v[168:171]\n\t"
            "s_or_b32 s73, s77, s49\n\t"
            "v_cvt_pk_bf16_f32 v181, v182, v183\n\t"
            "v_mfma_f32_16x16x32_bf16 v[168:171], v[12:15], v[216:219], v[168:171]\n\t"
            "v_mfma_f32_16x16x32_bf16 v[168:171], v[16:19], v[220:223], v[168:171]\n\t"
            "v_mfma_f32_16x16x32_bf16 v[168:171], v[20:23], v[224:227], v[168:171]\n\t"
            "v_mfma_f32_16x16x32_bf16 v[188:191], v[144:147], v[160:163], v[188:191]\n\t"
            "v_mfma_f32_16x16x32_bf16 v[200:203], v[120:123], v[204:207], 0\n\t"
            "v_mfma_f32_16x16x32_bf16 v[200:203], v[124:127], v[208:211], v[200:203]\n\t"
            "v_mfma_f32_16x16x32_bf16 v[200:203], v[128:131], v[212:215], v[200:203]\n\t"
            "v_mfma_f32_16x16x32_bf16 v[200:203], v[132:135], v[216:219], v[200:203]\n\t"
            "v_mfma_f32_16x16x32_bf16 v[200:203], v[136:139], v[220:223], v[200:203]\n\t"
            "s_add_u32 s70, s70, 1\n\t"
            "s_nop 0\n\t"
            "v_mul_f32 v168, v238, v168\n\t"
            "v_mfma_f32_16x16x32_bf16 v[200:203], v[140:143], v[224:227], v[200:203]\n\t"
            "s_add_u32 s76, s76, 800\n\t"
            "v_exp_f32 v188, v188\n\t"
            "v_mfma_f32_16x16x32_bf16 v[192:195], v[156:159], v[160:163], v[192:195]\n\t"
            "s_setprio 0\n\t"
            "v_exp_f32 v189, v189\n\t"
            "v_mfma_f32_16x16x32_bf16 v[172:175], v[72:75], v[204:207], 0\n\t"
            "s_add_u32 s77, s77, 384\n\t"
            "v_exp_f32 v190, v190\n\t"
            "v_mfma_f32_16x16x32_bf16 v[172:175], v[76:79], v[208:211], v[172:175]\n\t"
            "s_xor_b32 s48, s48, s47\n\t"
            "v_exp_f32 v191, v191\n\t"
            "v_mfma_f32_16x16x32_bf16 v[172:175], v[80:83], v[212:215], v[172:175]\n\t"
            "s_sub_u32 s44, s44, 1\n\t"
            "v_exp_f32 v192, v192\n\t"
            "v_mfma_f32_16x16x32_bf16 v[172:175], v[84:87], v[216:219], v[172:175]\n\t"
            "v_exp_f32 v193, v193\n\t"
            "v_mfma_f32_16x16x32_bf16 v[172:175], v[88:91], v[220:223], v[172:175]\n\t"
            "v_exp_f32 v194, v194\n\t"
            "v_mfma_f32_16x16x32_bf16 v[172:175], v[92:95], v[224:227], v[172:175]\n\t"
            "v_exp_f32 v195, v195\n\t"
            "v_mfma_f32_16x16x32_bf16 v[184:187], v[156:159], v[148:151], v[184:187]\n\t"
            "v_cvt_pk_bf16_f32 v188, v188, v189\n\t"
            "v_cvt_pk_bf16_f32 v189, v190, v191\n\t"
            "v_mfma_f32_16x16x32_bf16 v[196:199], v[24:27], v[204:207], 0\n\t"
            "v_cvt_pk_bf16_f32 v190, v192, v193\n\t"
            "v_cvt_pk_bf16_f32 v191, v194, v195\n\t"
            "v_mfma_f32_16x16x32_bf16 v[196:199], v[28:31], v[208:211], v[196:199]\n\t"
            "v_mul_f32 v169, v238, v169\n\t"
            "v_mul_f32 v170, v238, v170\n\t"
            "v_mfma_f32_16x16x32_bf16 v[196:199], v[32:35], v[212:215], v[196:199]\n\t"
            "v_exp_f32 v184, v184\n\t"
            "v_mfma_f32_16x16x32_bf16 v[196:199], v[36:39], v[216:219], v[196:199]\n\t"
            "v_exp_f32 v185, v185\n\t"
            "v_mfma_f32_16x16x32_bf16 v[196:199], v[40:43], v[220:223], v[196:199]\n\t"
            "v_exp_f32 v186, v186\n\t"
            "v_mfma_f32_16x16x32_bf16 v[196:199], v[44:47], v[224:227], v[196:199]\n\t"
            "v_exp_f32 v187, v187\n\t"
            "v_cvt_pk_bf16_f32 v182, v184, v185\n\t"
            "v_cvt_pk_bf16_f32 v183, v186, v187\n\t"
            "v_mfma_f32_16x16x16_bf16 v[184:187], v[176:177], v[154:155], 0\n\t"
            "v_mul_f32 v171, v238, v171\n\t"
            "v_cvt_pk_bf16_f32 v168, v168, v169\n\t"
            "v_cvt_pk_bf16_f32 v169, v170, v171\n\t"
            "v_mfma_f32_16x16x16_bf16 v[192:195], v[176:177], v[166:167], 0\n\t"
            "v_mul_f32 v196, v238, v196\n\t"
            "v_mul_f32 v197, v238, v197\n\t"
            "v_mul_f32 v198, v238, v198\n\t"
            "v_mfma_f32_16x16x32_bf16 v[228:231], v[96:99], v[204:207], 0\n\t"
            "ds_read_b64 v[204:205], v235 offset:136\n\t"
            "v_mul_f32 v199, v238, v199\n\t"
            "v_mfma_f32_16x16x32_bf16 v[228:231], v[100:103], v[208:211], v[228:231]\n\t"
            "ds_read_b64 v[206:207], v234 offset:128\n\t"
            "v_cvt_pk_bf16_f32 v170, v196, v197\n\t"
            "v_mfma_f32_16x16x32_bf16 v[228:231], v[104:107], v[212:215], v[228:231]\n\t"
            "ds_read_b128 v[208:211], v234\n\t"
            "v_cvt_pk_bf16_f32 v171, v198, v199\n\t"
            "v_mfma_f32_16x16x32_bf16 v[228:231], v[108:111], v[216:219], v[228:231]\n\t"
            "ds_read_b128 v[212:215], v235 offset:64\n\t"
            "v_mul_f32 v172, v238, v172\n\t"
            "v_mfma_f32_16x16x32_bf16 v[228:231], v[112:115], v[220:223], v[228:231]\n\t"
            "v_mul_f32 v173, v238, v173\n\t"
            "v_mul_f32 v200, v238, v200\n\t"
            "v_mfma_f32_16x16x32_bf16 v[228:231], v[116:119], v[224:227], v[228:231]\n\t"
            "v_mul_f32 v201, v238, v201\n\t"
            "v_mul_f32 v202, v238, v202\n\t"
            "v_mfma_f32_16x16x32_bf16 v[184:187], v[168:171], v[148:151], v[184:187]\n\t"
            "v_mul_f32 v203, v238, v203\n\t"
            "v_mul_f32 v174, v238, v174\n\t"
            "v_mfma_f32_16x16x32_bf16 v[192:195], v[168:171], v[160:163], v[192:195]\n\t"
            "v_mul_f32 v175, v238, v175\n\t"
            "v_cvt_pk_bf16_f32 v172, v172, v173\n\t"
            "v_cvt_pk_bf16_f32 v178, v200, v201\n\t"
            "v_cvt_pk_bf16_f32 v179, v202, v203\n\t"
            "v_cvt_pk_bf16_f32 v173, v174, v175\n\t"
            "v_mul_f32 v228, v238, v228\n\t"
            "v_mul_f32 v229, v238, v229\n\t"
            "s_waitcnt lgkmcnt(2)\n\t"
            "v_mfma_f32_16x16x32_bf16 v[220:223], v[204:207], v[164:167], 0\n\t"
            "s_waitcnt lgkmcnt(1)\n\t"
            "v_mul_f32 v230, v238, v230\n\t"
            "v_mul_f32 v231, v238, v231\n\t"
            "v_mfma_f32_16x16x32_bf16 v[220:223], v[208:211], v[160:163], v[220:223]\n\t"
            "s_waitcnt lgkmcnt(0)\n\t"
            "v_cvt_pk_bf16_f32 v174, v228, v229\n\t"
            "v_cvt_pk_bf16_f32 v175, v230, v231\n\t"
            "v_mfma_f32_16x16x32_bf16 v[220:223], v[212:215], v[156:159], v[220:223]\n\t"
            "v_exp_f32 v184, v184\n\t"
            "v_mfma_f32_16x16x32_bf16 v[216:219], v[204:207], v[152:155], 0\n\t"
            "v_exp_f32 v185, v185\n\t"
            "v_mfma_f32_16x16x32_bf16 v[216:219], v[208:211], v[148:151], v[216:219]\n\t"
            "v_exp_f32 v186, v186\n\t"
            "v_mfma_f32_16x16x32_bf16 v[216:219], v[212:215], v[144:147], v[216:219]\n\t"
            "v_exp_f32 v187, v187\n\t"
            "v_cvt_pk_bf16_f32 v184, v184, v185\n\t"
            "v_cvt_pk_bf16_f32 v185, v186, v187\n\t"
            "v_mfma_f32_16x16x16_bf16 v[228:231], v[164:165], v[178:179], 0\n\t"
            "v_mov_b32 v187, 0\n\t"
            "v_exp_f32 v192, v192\n\t"
            "v_mfma_f32_16x16x16_bf16 v[200:203], v[176:177], v[178:179], 0\n\t"
            "v_exp_f32 v193, v193\n\t"
            "v_exp_f32 v216, v216\n\t"
            "v_mfma_f32_16x16x16_bf16 v[196:199], v[152:153], v[178:179], 0\n\t"
            "v_cvt_pk_bf16_f32 v216, v216, 0\n\t"
            "v_cndmask_b32_e64 v186, 0, v216, s[58:59]\n\t"
            "v_cndmask_b32_e64 v235, v217, v221, s[60:61]\n\t"
            "v_mfma_f32_16x16x32_bf16 v[224:227], v[204:207], v[176:179], 0\n\t"
            "v_exp_f32 v194, v194\n\t"
            "v_mfma_f32_16x16x32_bf16 v[224:227], v[208:211], v[172:175], v[224:227]\n\t"
            "v_exp_f32 v195, v195\n\t"
            "v_mfma_f32_16x16x32_bf16 v[224:227], v[212:215], v[168:171], v[224:227]\n\t"
            "v_exp_f32 v220, v220\n\t"
            "v_mfma_f32_16x16x32_bf16 v[204:207], v[240:243], v[180:183], 0\n\t"
            "v_cvt_pk_bf16_f32 v192, v192, v193\n\t"
            "v_cvt_pk_bf16_f32 v193, v194, v195\n\t"
            "v_mfma_f32_16x16x32_bf16 v[204:207], v[240:243], v[184:187], v[204:207]\n\t"
            "v_cvt_pk_bf16_f32 v220, v220, 0\n\t"
            "v_cndmask_b32_e64 v194, 0, v220, s[58:59]\n\t"
            "v_mfma_f32_16x16x32_bf16 v[200:203], v[168:171], v[172:175], v[200:203]\n\t"
            "v_exp_f32 v224, v224\n\t"
            "v_mfma_f32_16x16x32_bf16 v[196:199], v[144:147], v[172:175], v[196:199]\n\t"
            "v_cvt_pk_bf16_f32 v224, v224, 0\n\t"
            "v_cndmask_b32_e64 v235, v235, v225, s[62:63]\n\t"
            "v_mfma_f32_16x16x32_bf16 v[228:231], v[156:159], v[172:175], v[228:231]\n\t"
            "v_cndmask_b32_e64 v235, v235, v239, s[64:65]\n\t"
            "v_cmp_nlt_f32_e64 s[24:25], |v235|, s23\n\t"
            "v_exp_f32 v235, v235\n\t"
            "v_exp_f32 v200, v200\n\t"
            "v_exp_f32 v201, v201\n\t"
            "v_exp_f32 v196, v196\n\t"
            "v_exp_f32 v197, v197\n\t"
            "v_exp_f32 v198, v198\n\t"
            "v_exp_f32 v199, v199\n\t"
            "v_exp_f32 v228, v228\n\t"
            "v_exp_f32 v229, v229\n\t"
            "v_exp_f32 v230, v230\n\t"
            "v_exp_f32 v231, v231\n\t"
            "v_cvt_pk_bf16_f32 v196, v196, v197\n\t"
            "v_cvt_pk_bf16_f32 v197, v198, v199\n\t"
            "v_cvt_pk_bf16_f32 v198, v228, v229\n\t"
            "v_cvt_pk_bf16_f32 v199, v230, v231\n\t"
            "v_exp_f32 v202, v202\n\t"
            "v_exp_f32 v203, v203\n\t"
            "v_mfma_f32_16x16x32_bf16 v[212:215], v[240:243], v[196:199], 0\n\t"
            "v_cvt_pk_bf16_f32 v200, v200, v201\n\t"
            "v_cvt_pk_bf16_f32 v201, v202, v203\n\t"
            "v_cndmask_b32_e64 v202, 0, v224, s[58:59]\n\t"
            "v_mov_b32 v203, 0\n\t"
            "v_cvt_pk_bf16_f32 v235, v235, 0\n\t"
            "v_cndmask_b32_e64 v235, 0, v235, s[66:67]\n\t"
            "v_mfma_f32_16x16x32_bf16 v[212:215], v[240:243], v[200:203], v[212:215]\n\t"
            "ds_write_b16 v250, v235 offset:256\n\t"
            "v_mfma_f32_16x16x32_bf16 v[208:211], v[240:243], v[188:191], 0\n\t"
            "v_mov_b32 v195, 0\n\t"
            "v_add_u32 v205, 0xe0800000, v204\n\t"
            "v_rcp_f32 v236, v204\n\t"
            "v_mfma_f32_16x16x32_bf16 v[208:211], v[240:243], v[192:195], v[208:211]\n\t"
            "s_nop 1\n\t"
            "v_add_u32 v213, 0xe0800000, v212\n\t"
            "v_rcp_f32 v238, v212\n\t"
            "s_nop 3\n\t"
            "v_add_u32 v209, 0xe0800000, v208\n\t"
            "v_rcp_f32 v237, v208\n\t"
            "v_or3_b32 v205, v205, v209, v213\n\t"
            "v_cmp_le_u32 vcc, 2.0, v205\n\t"
            ".Lwa_in%=:\n\t"
            "s_or_b64 vcc, vcc, s[24:25]\n\t"
            "s_cbranch_scc1 .Lwa_slow%=\n\t"
            ".Lwa_ok%=:\n\t"
            "s_add_u32 s72, s72, 800\n\t"
            "s_cmp_gt_u32 s44, 2\n\t"
            "s_barrier\n\t"
            "s_setprio 1\n\t"
            "s_cbranch_scc1 .Lwa_loop%=\n\t"
            "s_cmp_eq_u32 s44, 0\n\t"
            "s_cbranch_scc1 .Lwa_done%=\n\t"
            "s_cmp_lg_u32 s44, 2\n\t"
            "s_cbranch_scc1 .Lwa_loop%=\n\t"
            "s_mov_b32 m0, s40\n\t"
            "v_lshlrev_b32 v234, 3, v248\n\t"
            "buffer_load_dword v234, s[36:39], s41 offen sc1 lds\n\t"
            "s_branch .Lwa_loop%=\n\t"
            ".Lwa_slow%=:\n\t"
            "s_waitcnt lgkmcnt(0)\n\t"
            "s_nop 7\n\t"
            "v_mfma_f32_16x16x16_bf16 v[180:183], v[152:153], v[154:155], 0\n\t"
            "v_add_u32 v234, s72, v252\n\t"
            "v_cndmask_b32_e64 v235, v246, v234, s[50:51]\n\t"
            "v_cndmask_b32_e64 v234, v246, v234, s[26:27]\n\t"
            "v_mfma_f32_16x16x16_bf16 v[228:231], v[164:165], v[154:155], 0\n\t"
            "ds_read_b64 v[216:217], v235 offset:136\n\t"
            "ds_read_b64 v[218:219], v234 offset:128\n\t"
            "v_mfma_f32_16x16x16_bf16 v[184:187], v[176:177], v[154:155], 0\n\t"
            "ds_read_b128 v[220:223], v234\n\t"
            "ds_read_b128 v[224:227], v235 offset:64\n\t"
            "v_mfma_f32_16x16x32_bf16 v[180:183], v[144:147], v[148:151], v[180:183]\n\t"
            "v_mfma_f32_16x16x32_bf16 v[228:231], v[156:159], v[148:151], v[228:231]\n\t"
            "s_nop 1\n\t"
            "v_mfma_f32_16x16x32_bf16 v[184:187], v[168:171], v[148:151], v[184:187]\n\t"
            "v_mfma_f32_16x16x16_bf16 v[188:191], v[152:153], v[166:167], 0\n\t"
            "v_mfma_f32_16x16x16_bf16 v[192:195], v[176:177], v[166:167], 0\n\t"
            "v_mfma_f32_16x16x16_bf16 v[196:199], v[152:153], v[178:179], 0\n\t"
            "s_waitcnt lgkmcnt(2)\n\t"
            "v_mfma_f32_16x16x32_bf16 v[204:207], v[216:219], v[152:155], 0\n\t"
            "s_waitcnt lgkmcnt(1)\n\t"
            "v_mfma_f32_16x16x32_bf16 v[204:207], v[220:223], v[148:151], v[204:207]\n\t"
            "s_waitcnt lgkmcnt(0)\n\t"
            "v_mfma_f32_16x16x32_bf16 v[204:207], v[224:227], v[144:147], v[204:207]\n\t"
            "v_mfma_f32_16x16x32_bf16 v[208:211], v[216:219], v[164:167], 0\n\t"
            "v_mfma_f32_16x16x32_bf16 v[208:211], v[220:223], v[160:163], v[208:211]\n\t"
            "v_mfma_f32_16x16x32_bf16 v[208:211], v[224:227], v[156:159], v[208:211]\n\t"
            "v_mfma_f32_16x16x32_bf16 v[188:191], v[144:147], v[160:163], v[188:191]\n\t"
            "v_mfma_f32_16x16x32_bf16 v[192:195], v[168:171], v[160:163], v[192:195]\n\t"
            "v_mfma_f32_16x16x32_bf16 v[212:215], v[216:219], v[176:179], 0\n\t"
            "s_nop 1\n\t"
            "v_max3_f32 v232, v204, v180, v181\n\t"
            "v_mfma_f32_16x16x32_bf16 v[212:215], v[220:223], v[172:175], v[212:215]\n\t"
            "v_max3_f32 v232, v232, v182, v183\n\t"
            "v_max3_f32 v232, v232, v228, v229\n\t"
            "v_mfma_f32_16x16x32_bf16 v[212:215], v[224:227], v[168:171], v[212:215]\n\t"
            "v_max3_f32 v232, v232, v230, v231\n\t"
            "v_max3_f32 v232, v232, v184, v185\n\t"
            "v_mfma_f32_16x16x32_bf16 v[196:199], v[144:147], v[172:175], v[196:199]\n\t"
            "v_max3_f32 v232, v232, v186, v187\n\t"
            "v_mov_b32 v233, v232\n\t"
            "v_cndmask_b32_e64 v235, v205, v209, s[60:61]\n\t"
            "v_mfma_f32_16x16x16_bf16 v[200:203], v[176:177], v[178:179], 0\n\t"
            "v_permlane32_swap_b32 v232, v233\n\t"
            "v_max_f32 v232, v232, v233\n\t"
            "v_mov_b32 v233, v232\n\t"
            "v_max3_f32 v234, v205, v209, v213\n\t"
            "v_max_f32 v234, v234, v239\n\t"
            "v_permlane16_swap_b32 v232, v233\n\t"
            "v_mfma_f32_16x16x32_bf16 v[200:203], v[168:171], v[172:175], v[200:203]\n\t"
            "v_max_f32 v232, v232, v233\n\t"
            "v_sub_f32 v180, v180, v232\n\t"
            "v_sub_f32 v181, v181, v232\n\t"
            "v_exp_f32 v180, v180\n\t"
            "v_exp_f32 v181, v181\n\t"
            "v_sub_f32 v182, v182, v232\n\t"
            "v_sub_f32 v183, v183, v232\n\t"
            "v_exp_f32 v182, v182\n\t"
            "v_exp_f32 v183, v183\n\t"
            "v_sub_f32 v228, v228, v232\n\t"
            "v_sub_f32 v229, v229, v232\n\t"
            "v_sub_f32 v230, v230, v232\n\t"
            "v_sub_f32 v231, v231, v232\n\t"
            "v_exp_f32 v228, v228\n\t"
            "v_exp_f32 v229, v229\n\t"
            "v_exp_f32 v230, v230\n\t"
            "v_exp_f32 v231, v231\n\t"
            "v_cvt_pk_bf16_f32 v180, v180, v181\n\t"
            "v_cvt_pk_bf16_f32 v181, v182, v183\n\t"
            "v_cvt_pk_bf16_f32 v182, v228, v229\n\t"
            "v_cvt_pk_bf16_f32 v183, v230, v231\n\t"
            "v_mfma_f32_16x16x16_bf16 v[228:231], v[164:165], v[166:167], 0\n\t"
            "v_sub_f32 v184, v184, v232\n\t"
            "v_sub_f32 v185, v185, v232\n\t"
            "v_sub_f32 v186, v186, v232\n\t"
            "v_sub_f32 v187, v187, v232\n\t"
            "v_sub_f32 v204, v204, v232\n\t"
            "v_max3_f32 v232, v208, v188, v189\n\t"
            "v_max3_f32 v232, v232, v190, v191\n\t"
            "v_mfma_f32_16x16x32_bf16 v[228:231], v[156:159], v[160:163], v[228:231]\n\t"
            "v_max_f32_dpp v234, v234, v234 row_ror:8 row_mask:0xf bank_mask:0xf\n\t"
            "v_cndmask_b32_e64 v235, v235, v213, s[62:63]\n\t"
            "v_cndmask_b32_e64 v235, v235, v239, s[64:65]\n\t"
            "v_max_f32_dpp v234, v234, v234 row_ror:4 row_mask:0xf bank_mask:0xf\n\t"
            "v_exp_f32 v184, v184\n\t"
            "v_exp_f32 v185, v185\n\t"
            "v_max_f32_dpp v234, v234, v234 row_ror:2 row_mask:0xf bank_mask:0xf\n\t"
            "v_exp_f32 v186, v186\n\t"
            "v_max3_f32 v232, v232, v228, v229\n\t"
            "v_max3_f32 v232, v232, v230, v231\n\t"
            "v_max3_f32 v232, v232, v192, v193\n\t"
            "v_max3_f32 v232, v232, v194, v195\n\t"
            "v_mov_b32 v233, v232\n\t"
            "v_max_f32_dpp v234, v234, v234 row_ror:1 row_mask:0xf bank_mask:0xf\n\t"
            "v_sub_f32 v235, v235, v234\n\t"
            "v_permlane32_swap_b32 v232, v233\n\t"
            "v_max_f32 v232, v232, v233\n\t"
            "v_mov_b32 v233, v232\n\t"
            "v_exp_f32 v235, v235\n\t"
            "v_exp_f32 v187, v187\n\t"
            "v_permlane16_swap_b32 v232, v233\n\t"
            "v_max_f32 v232, v232, v233\n\t"
            "v_sub_f32 v188, v188, v232\n\t"
            "v_sub_f32 v189, v189, v232\n\t"
            "v_exp_f32 v188, v188\n\t"
            "v_exp_f32 v189, v189\n\t"
            "v_sub_f32 v190, v190, v232\n\t"
            "v_sub_f32 v191, v191, v232\n\t"
            "v_exp_f32 v190, v190\n\t"
            "v_exp_f32 v191, v191\n\t"
            "v_sub_f32 v228, v228, v232\n\t"
            "v_sub_f32 v229, v229, v232\n\t"
            "v_sub_f32 v230, v230, v232\n\t"
            "v_sub_f32 v231, v231, v232\n\t"
            "v_exp_f32 v228, v228\n\t"
            "v_exp_f32 v229, v229\n\t"
            "v_exp_f32 v230, v230\n\t"
            "v_exp_f32 v231, v231\n\t"
            "v_cvt_pk_bf16_f32 v188, v188, v189\n\t"
            "v_cvt_pk_bf16_f32 v189, v190, v191\n\t"
            "v_cvt_pk_bf16_f32 v190, v228, v229\n\t"
            "v_cvt_pk_bf16_f32 v191, v230, v231\n\t"
            "v_mfma_f32_16x16x16_bf16 v[228:231], v[164:165], v[178:179], 0\n\t"
            "v_sub_f32 v192, v192, v232\n\t"
            "v_sub_f32 v193, v193, v232\n\t"
            "v_sub_f32 v194, v194, v232\n\t"
            "v_sub_f32 v195, v195, v232\n\t"
            "v_sub_f32 v208, v208, v232\n\t"
            "v_max3_f32 v232, v212, v196, v197\n\t"
            "v_max3_f32 v232, v232, v198, v199\n\t"
            "v_mfma_f32_16x16x32_bf16 v[228:231], v[156:159], v[172:175], v[228:231]\n\t"
            "v_exp_f32 v192, v192\n\t"
            "v_exp_f32 v193, v193\n\t"
            "v_exp_f32 v204, v204\n\t"
            "v_exp_f32 v194, v194\n\t"
            "v_exp_f32 v195, v195\n\t"
            "v_exp_f32 v208, v208\n\t"
            "v_cvt_pk_bf16_f32 v184, v184, v185\n\t"
            "v_cvt_pk_bf16_f32 v192, v192, v193\n\t"
            "v_max3_f32 v232, v232, v228, v229\n\t"
            "v_max3_f32 v232, v232, v230, v231\n\t"
            "v_max3_f32 v232, v232, v200, v201\n\t"
            "v_max3_f32 v232, v232, v202, v203\n\t"
            "v_mov_b32 v233, v232\n\t"
            "v_cvt_pk_bf16_f32 v235, v235, 0\n\t"
            "v_cvt_pk_bf16_f32 v185, v186, v187\n\t"
            "v_permlane32_swap_b32 v232, v233\n\t"
            "v_max_f32 v232, v232, v233\n\t"
            "v_mov_b32 v233, v232\n\t"
            "v_cvt_pk_bf16_f32 v204, v204, 0\n\t"
            "v_cvt_pk_bf16_f32 v193, v194, v195\n\t"
            "v_permlane16_swap_b32 v232, v233\n\t"
            "v_max_f32 v232, v232, v233\n\t"
            "v_sub_f32 v196, v196, v232\n\t"
            "v_sub_f32 v197, v197, v232\n\t"
            "v_exp_f32 v196, v196\n\t"
            "v_exp_f32 v197, v197\n\t"
            "v_sub_f32 v198, v198, v232\n\t"
            "v_sub_f32 v199, v199, v232\n\t"
            "v_exp_f32 v198, v198\n\t"
            "v_exp_f32 v199, v199\n\t"
            "v_sub_f32 v228, v228, v232\n\t"
            "v_sub_f32 v229, v229, v232\n\t"
            "v_sub_f32 v230, v230, v232\n\t"
            "v_sub_f32 v231, v231, v232\n\t"
            "v_sub_f32 v200, v200, v232\n\t"
            "v_sub_f32 v201, v201, v232\n\t"
            "v_exp_f32 v228, v228\n\t"
            "v_exp_f32 v229, v229\n\t"
            "v_exp_f32 v230, v230\n\t"
            "v_exp_f32 v231, v231\n\t"
            "v_exp_f32 v200, v200\n\t"
            "v_exp_f32 v201, v201\n\t"
            "v_sub_f32 v202, v202, v232\n\t"
            "v_sub_f32 v203, v203, v232\n\t"
            "v_sub_f32 v212, v212, v232\n\t"
            "v_cvt_pk_bf16_f32 v196, v196, v197\n\t"
            "v_exp_f32 v202, v202\n\t"
            "v_exp_f32 v203, v203\n\t"
            "v_exp_f32 v212, v212\n\t"
            "v_cvt_pk_bf16_f32 v197, v198, v199\n\t"
            "v_cvt_pk_bf16_f32 v198, v228, v229\n\t"
            "v_cvt_pk_bf16_f32 v199, v230, v231\n\t"
            "v_cvt_pk_bf16_f32 v200, v200, v201\n\t"
            "v_cvt_pk_bf16_f32 v208, v208, 0\n\t"
            "v_cvt_pk_bf16_f32 v201, v202, v203\n\t"
            "v_cvt_pk_bf16_f32 v212, v212, 0\n\t"
            "v_cndmask_b32_e64 v235, 0, v235, s[66:67]\n\t"
            "v_cndmask_b32_e64 v186, 0, v204, s[58:59]\n\t"
            "v_mfma_f32_16x16x32_bf16 v[204:207], v[240:243], v[180:183], 0\n\t"
            "v_cndmask_b32_e64 v194, 0, v208, s[58:59]\n\t"
            "v_cndmask_b32_e64 v202, 0, v212, s[58:59]\n\t"
            "v_mfma_f32_16x16x32_bf16 v[208:211], v[240:243], v[188:191], 0\n\t"
            "ds_write_b16 v250, v235 offset:256\n\t"
            "v_mfma_f32_16x16x32_bf16 v[212:215], v[240:243], v[196:199], 0\n\t"
            "v_mov_b32 v187, 0\n\t"
            "v_mov_b32 v195, 0\n\t"
            "v_mov_b32 v203, 0\n\t"
            "v_mfma_f32_16x16x32_bf16 v[204:207], v[240:243], v[184:187], v[204:207]\n\t"
            "v_mfma_f32_16x16x32_bf16 v[208:211], v[240:243], v[192:195], v[208:211]\n\t"
            "v_mfma_f32_16x16x32_bf16 v[212:215], v[240:243], v[200:203], v[212:215]\n\t"
            "s_nop 5\n\t"
            "v_rcp_f32 v236, v204\n\t"
            "v_rcp_f32 v237, v208\n\t"
            "v_rcp_f32 v238, v212\n\t"
            "s_waitcnt lgkmcnt(0)\n\t"
            "s_nop 7\n\t"
            "s_branch .Lwa_ok%=\n\t"
            ".Lwa_done%=:\n\t"
            "s_setprio 2\n\t"
            "s_waitcnt lgkmcnt(0)\n\t"
            "s_nop 7\n\t"
            "ds_read_b64 v[156:157], v247\n\t"
            "v_add_u32 v235, s45, v248\n\t"
            "v_xor_b32 v234, s73, v254\n\t"
            "v_mov_b32 v159, 0\n\t"
            "v_mov_b32 v163, 0\n\t"
            "v_mov_b32 v167, 0\n\t"
            "ds_read_b128 v[168:171], v235\n\t"
            "ds_read_u16 v158, v234\n\t"
            "ds_read_b128 v[172:175], v235 offset:1536\n\t"
            "ds_read_b128 v[176:179], v235 offset:3072\n\t"
            "ds_read_b64 v[160:161], v247 offset:3072\n\t"
            "ds_read_u16 v162, v234 offset:256\n\t"
            "ds_read_b64 v[164:165], v247 offset:1536\n\t"
            "ds_read_u16 v166, v234 offset:128\n\t"
            "s_waitcnt lgkmcnt(7)\n\t"
            "v_mfma_f32_16x16x32_bf16 v[228:231], v[168:171], v[180:183], 0\n\t"
            "s_waitcnt lgkmcnt(6)\n\t"
            "v_mfma_f32_16x16x32_bf16 v[228:231], v[156:159], v[184:187], v[228:231]\n\t"
            "s_nop 7\n\t"
            "v_mul_f32 v228, v236, v228\n\t"
            "v_mul_f32 v229, v236, v229\n\t"
            "v_mul_f32 v230, v236, v230\n\t"
            "v_mul_f32 v231, v236, v231\n\t"
            "v_cvt_pk_bf16_f32 v228, v228, v229\n\t"
            "v_cvt_pk_bf16_f32 v229, v230, v231\n\t"
            "ds_write_b64 v245, v[228:229]\n\t"
            "s_waitcnt lgkmcnt(6)\n\t"
            "v_mfma_f32_16x16x32_bf16 v[228:231], v[172:175], v[180:183], 0\n\t"
            "s_waitcnt lgkmcnt(5)\n\t"
            "v_mfma_f32_16x16x32_bf16 v[180:183], v[176:179], v[180:183], 0\n\t"
            "s_waitcnt lgkmcnt(3)\n\t"
            "v_mfma_f32_16x16x32_bf16 v[180:183], v[160:163], v[184:187], v[180:183]\n\t"
            "s_waitcnt lgkmcnt(1)\n\t"
            "v_mfma_f32_16x16x32_bf16 v[228:231], v[164:167], v[184:187], v[228:231]\n\t"
            "v_mfma_f32_16x16x32_bf16 v[184:187], v[168:171], v[188:191], 0\n\t"
            "v_mfma_f32_16x16x32_bf16 v[184:187], v[156:159], v[192:195], v[184:187]\n\t"
            "s_nop 3\n\t"
            "v_mul_f32 v180, v236, v180\n\t"
            "v_mul_f32 v181, v236, v181\n\t"
            "v_mul_f32 v182, v236, v182\n\t"
            "v_mul_f32 v183, v236, v183\n\t"
            "v_cvt_pk_bf16_f32 v180, v180, v181\n\t"
            "v_cvt_pk_bf16_f32 v181, v182, v183\n\t"
            "v_mul_f32 v228, v236, v228\n\t"
            "v_mul_f32 v229, v236, v229\n\t"
            "v_mul_f32 v230, v236, v230\n\t"
            "v_mul_f32 v231, v236, v231\n\t"
            "v_cvt_pk_bf16_f32 v228, v228, v229\n\t"
            "v_cvt_pk_bf16_f32 v229, v230, v231\n\t"
            "v_mul_f32 v184, v237, v184\n\t"
            "v_mul_f32 v185, v237, v185\n\t"
            "v_mul_f32 v186, v237, v186\n\t"
            "v_mul_f32 v187, v237, v187\n\t"
            "v_cvt_pk_bf16_f32 v184, v184, v185\n\t"
            "v_cvt_pk_bf16_f32 v185, v186, v187\n\t"
            "ds_read_b64 v[186:187], v253 offset:352\n\t"
            "ds_write_b64 v245, v[180:181] offset:256\n\t"
            "v_mfma_f32_16x16x32_bf16 v[180:183], v[168:171], v[196:199], 0\n\t"
            "ds_write_b64 v245, v[228:229] offset:128\n\t"
            "v_mfma_f32_16x16x32_bf16 v[180:183], v[156:159], v[200:203], v[180:183]\n\t"
            "ds_write_b64 v245, v[184:185] offset:6144\n\t"
            "v_mfma_f32_16x16x32_bf16 v[228:231], v[172:175], v[188:191], 0\n\t"
            "ds_read_b64 v[184:185], v253 offset:320\n\t"
            "v_mfma_f32_16x16x32_bf16 v[228:231], v[164:167], v[192:195], v[228:231]\n\t"
            "v_mfma_f32_16x16x32_bf16 v[188:191], v[176:179], v[188:191], 0\n\t"
            "v_mfma_f32_16x16x32_bf16 v[188:191], v[160:163], v[192:195], v[188:191]\n\t"
            "s_nop 1\n\t"
            "v_mul_f32 v180, v238, v180\n\t"
            "v_mul_f32 v181, v238, v181\n\t"
            "v_mul_f32 v182, v238, v182\n\t"
            "v_mul_f32 v183, v238, v183\n\t"
            "v_cvt_pk_bf16_f32 v180, v180, v181\n\t"
            "v_cvt_pk_bf16_f32 v181, v182, v183\n\t"
            "v_mul_f32 v228, v237, v228\n\t"
            "v_mul_f32 v229, v237, v229\n\t"
            "v_mul_f32 v230, v237, v230\n\t"
            "v_mul_f32 v231, v237, v231\n\t"
            "v_cvt_pk_bf16_f32 v228, v228, v229\n\t"
            "v_cvt_pk_bf16_f32 v229, v230, v231\n\t"
            "v_mul_f32 v188, v237, v188\n\t"
            "v_mul_f32 v189, v237, v189\n\t"
            "v_mul_f32 v190, v237, v190\n\t"
            "v_mul_f32 v191, v237, v191\n\t"
            "v_cvt_pk_bf16_f32 v188, v188, v189\n\t"
            "v_cvt_pk_bf16_f32 v189, v190, v191\n\t"
            "ds_read_b64 v[182:183], v253 offset:288\n\t"
            "ds_write_b64 v245, v[180:181] offset:12288\n\t"
            "ds_read_b64 v[180:181], v253 offset:256\n\t"
            "ds_write_b64 v245, v[228:229] offset:6272\n\t"
            "v_mfma_f32_16x16x32_bf16 v[228:231], v[172:175], v[196:199], 0\n\t"
            "ds_write_b64 v245, v[188:189] offset:6400\n\t"
            "v_mfma_f32_16x16x32_bf16 v[228:231], v[164:167], v[200:203], v[228:231]\n\t"
            "v_mfma_f32_16x16x32_bf16 v[196:199], v[176:179], v[196:199], 0\n\t"
            "v_mfma_f32_16x16x32_bf16 v[196:199], v[160:163], v[200:203], v[196:199]\n\t"
            "s_nop 5\n\t"
            "v_mul_f32 v228, v238, v228\n\t"
            "s_waitcnt lgkmcnt(2)\n\t"
            "v_mfma_f32_16x16x32_bf16 v[168:171], v[168:171], v[180:183], 0\n\t"
            "v_mfma_f32_16x16x32_bf16 v[172:175], v[172:175], v[180:183], 0\n\t"
            "v_mfma_f32_16x16x32_bf16 v[176:179], v[176:179], v[180:183], 0\n\t"
            "v_mul_f32 v229, v238, v229\n\t"
            "v_mfma_f32_16x16x32_bf16 v[168:171], v[156:159], v[184:187], v[168:171]\n\t"
            "v_mul_f32 v230, v238, v230\n\t"
            "v_mul_f32 v231, v238, v231\n\t"
            "v_mfma_f32_16x16x32_bf16 v[172:175], v[164:167], v[184:187], v[172:175]\n\t"
            "v_cvt_pk_bf16_f32 v228, v228, v229\n\t"
            "v_cvt_pk_bf16_f32 v229, v230, v231\n\t"
            "v_mfma_f32_16x16x32_bf16 v[176:179], v[160:163], v[184:187], v[176:179]\n\t"
            "ds_write_b64 v245, v[228:229] offset:12416\n\t"
            "v_mfma_f32_16x16x32_bf16 v[228:231], v[240:243], v[180:183], 0\n\t"
            "v_mul_f32 v196, v238, v196\n\t"
            "v_mul_f32 v197, v238, v197\n\t"
            "v_mfma_f32_16x16x32_bf16 v[228:231], v[240:243], v[184:187], v[228:231]\n\t"
            "v_mul_f32 v198, v238, v198\n\t"
            "v_mul_f32 v199, v238, v199\n\t"
            "v_cvt_pk_bf16_f32 v196, v196, v197\n\t"
            "v_cvt_pk_bf16_f32 v197, v198, v199\n\t"
            "ds_write_b64 v245, v[196:197] offset:12544\n\t"
            "s_nop 2\n\t"
            "v_rcp_f32 v229, v228\n\t"
            "s_nop 0\n\t"
            "v_mul_f32 v168, v229, v168\n\t"
            "v_mul_f32 v169, v229, v169\n\t"
            "v_mul_f32 v172, v229, v172\n\t"
            "v_mul_f32 v173, v229, v173\n\t"
            "v_mul_f32 v176, v229, v176\n\t"
            "v_mul_f32 v177, v229, v177\n\t"
            "v_mul_f32 v170, v229, v170\n\t"
            "v_mul_f32 v171, v229, v171\n\t"
            "v_cvt_pk_bf16_f32 v168, v168, v169\n\t"
            "v_mul_f32 v174, v229, v174\n\t"
            "v_mul_f32 v175, v229, v175\n\t"
            "v_cvt_pk_bf16_f32 v172, v172, v173\n\t"
            "v_mul_f32 v178, v229, v178\n\t"
            "v_mul_f32 v179, v229, v179\n\t"
            "v_cvt_pk_bf16_f32 v176, v176, v177\n\t"
            "v_xor_b32 v230, s73, v255\n\t"
            "v_cvt_pk_bf16_f32 v169, v170, v171\n\t"
            "v_cvt_pk_bf16_f32 v173, v174, v175\n\t"
            "v_cvt_pk_bf16_f32 v177, v178, v179\n\t"
            "s_mov_b64 exec, s[68:69]\n\t"
            "ds_write_b64 v230, v[168:169]\n\t"
            "ds_write_b64 v230, v[172:173] offset:128\n\t"
            "ds_write_b64 v230, v[176:177] offset:256\n\t"
            "s_mov_b64 exec, -1\n\t"
            "s_waitcnt lgkmcnt(0)\n\t"
            "v_mov_b32 v144, s42\n\t"
            "v_bfrev_b32 v145, -2\n\t"
            "ds_write_b32 v144, v145\n\t"
            "s_waitcnt lgkmcnt(0)\n\t"
            "s_nop 7\n\t"
            : "+{v[232:235]}"(pl), "+{v249}"(aRSw_),
              "+{s44}"(s44), "+{s48}"(s48), "+{s70}"(s70), "+{s72}"(s72), "+{s73}"(s73), "+{s76}"(s76), "+{s77}"(s77)
            : "{v244}"(pE_), "{v245}"(aO_), "{v246}"(aZ_), "{v247}"(aVB_), "{v248}"(pST_), "{v250}"(aPLw_), "{v251}"(aRSr_), "{v252}"(g16_), "{v253}"(aPLr_), "{v254}"(loVL_), "{v255}"(loOL_), "{v[240:243]}"(ones4), "{s47}"((ldsb + X_OFF) ^ (ldsb + X1_OFF)),
              "{s[58:59]}"(0xFFFFull), "{s[60:61]}"(0xFFFF0000ull), "{s[62:63]}"(0xFFFF00000000ull), "{s[64:65]}"(0xFFFF000000000000ull),
              "{s[66:67]}"(0x1FFFFFFFFFFFFull), "{s[68:69]}"(0x0001000100010001ull), "{s[26:27]}"(0x1111111111111111ull), "{s23}"(64.f), "{s57}"(RS_OFF + 768 + (4 + (w >> 1)) * 192 - RS_BASE), "{s45}"(ldsb + V_OFF + w * 4608),
              "{s[28:31]}"(wr), "{s33}"(wave * 36864), "{s46}"(ldsb + PG_OFF), "{s42}"(ldsb + PG_OFF + w * 4), "{s56}"(ldsb + O_OFF), "{s[36:39]}"(tr), "{s41}"(to), "{s40}"(ldsb + X_OFF + 18432), "{s34}"(SH_OFF + bx * 12288 + w * 3072), "{s35}"(ldsb + O_OFF + w * 3072),
              "{s[50:51]}"(0x2222222222222222ull), "{s[52:53]}"(0x4444444444444444ull), "{s[54:55]}"(0x8888888888888888ull)
            : "s24", "s25", "v0", "v1", "v2", "v3", "v4", "v5", "v6", "v7", "v8", "v9", "v10", "v11", "v12", "v13", "v14", "v15", "v16", "v17", "v18", "v19", "v20", "v21", "v22", "v23", "v24", "v25", "v26", "v27", "v28", "v29", "v30", "v31", "v32", "v33", "v34", "v35", "v36", "v37", "v38", "v39", "v40", "v41", "v42", "v43", "v44", "v45", "v46", "v47", "v48", "v49", "v50", "v51", "v52", "v53", "v54", "v55", "v56", "v57", "v58", "v59", "v60", "v61", "v62", "v63", "v64", "v65", "v66", "v67", "v68", "v69", "v70", "v71", "v72", "v73", "v74", "v75", "v76", "v77", "v78", "v79", "v80", "v81", "v82", "v83", "v84", "v85", "v86", "v87", "v88", "v89", "v90", "v91", "v92", "v93", "v94", "v95", "v96", "v97", "v98", "v99", "v100", "v101", "v102", "v103", "v104", "v105", "v106", "v107", "v108", "v109", "v110", "v111", "v112", "v113", "v114", "v115", "v116", "v117", "v118", "v119", "v120", "v121", "v122", "v123", "v124", "v125", "v126", "v127", "v128", "v129", "v130", "v131", "v132", "v133", "v134", "v135", "v136", "v137", "v138", "v139", "v140", "v141", "v142", "v143", "v144", "v145", "v146", "v147", "v148", "v149", "v150", "v151", "v152", "v153", "v154", "v155", "v156", "v157", "v158", "v159", "v160", "v161", "v162", "v163", "v164", "v165", "v166", "v167", "v168", "v169", "v170", "v171", "v172", "v173", "v174", "v175", "v176", "v177", "v178", "v179", "v180", "v181", "v182", "v183", "v184", "v185", "v186", "v187", "v188", "v189", "v190", "v191", "v192", "v193", "v194", "v195", "v196", "v197", "v198", "v199", "v200", "v201", "v202", "v203", "v204", "v205", "v206", "v207", "v208", "v209", "v210", "v211", "v212", "v213", "v214", "v215", "v216", "v217", "v218", "v219", "v220", "v221", "v222", "v223", "v224", "v225", "v226", "v227", "v228", "v229", "v230", "v231", "v236", "v237", "v238", "v239", "s49", "vcc", "scc", "m0", "memory");
        }
        asm volatile("wk_code_end:");

        if (q0 == 40 && wave == 0) {
#pragma unroll 1
            for (int it = 0; it < SPIN_MAX && __hip_atomic_load((gu32*)((char*)wf + CN_OFF), RLX_AGENT) != (unsigned)GRID; ++it) __builtin_amdgcn_s_sleep(8);
            __hip_atomic_store((gu32*)((char*)wf + EP_OFF), __hip_atomic_load((gu32*)((char*)wf + EP_OFF), RLX_AGENT) + 1u, RLX_AGENT);
        }
    } else {
        ROLE_SETUP
        {
            LANE_SETUP
            const bool isV = wave < 6;
            CONV_CONSTS(0)
            const unsigned aCA_ = ldsb + CT_OFF + lane * 16, rsb = ldsb + RS_BASE, pE = fE - X_OFF;
            const unsigned aRSw = (lane < 48 ? rsb : ldsb + RS_OFF + 2304 - 192) + lane * 4, aRSr = rsb + (lane & 48);
            unsigned pr[3];
#pragma unroll
            for (int k = 0; k < 3; ++k) {
                const int c8 = 12 * (w - 2) + 4 * k + g;
                const int ws = c8 / 6, r6 = c8 - 6 * ws;
                pr[k] = l15 * 384 + (r6 >> 1) * 128 + (((2 * ws + (r6 & 1)) ^ sw) << 4);
            }
            unsigned r242 = isV ? pE : ldsb + O_OFF + pE, r243 = isV ? ldsb + V_OFF + w * 9216 + lane * 16 : (ldsb + O_OFF + pE) ^ 64;
            u32x4 r244 = {isV ? ldsb + V_OFF + w * 9216 + 1024 + lane * 8 : pr[0], isV ? aRSr : pr[1], isV ? aRSw : pr[2], (unsigned)lane * 16};
            const int hd = isV ? 2 * w : 2 * w - 3;
            const unsigned vq = ldsb + OL_OFF + l15 * 384 + (((2 * hd + (g >> 1)) ^ sw) << 4) + 8 * (g & 1);
            u32x4 f1 = {16 + l15 < cnt ? vq + 6144 : ldsb + OL_OFF + 22 * 384 + g * 8, pE, ldsb + V_OFF + hd * 4608 + lane * 16, ldsb + V_OFF + hd * 4608 + 1024 + lane * 8};
            u32x4 f2 = {aRSw, aRSr, rsb + l15 * 4, 0u};
            const unsigned lB = ldsb + LB_OFF(l15 & 7) + (g >> 1) * 1536 + (g & 1) * 768 + 96 * w;
            const unsigned long long ob = (unsigned long long)out, xb = (unsigned long long)((const char*)x - XBIAS), wb = (unsigned long long)wf;
            const u32x4 orr = {(unsigned)ob, (unsigned)(ob >> 32) & 0xffffu, (unsigned)(IMG * IMG * CH * 2), 0x00020000u};
            const u32x4 xr = {(unsigned)xb, (unsigned)(xb >> 32) & 0xffffu, (unsigned)(IMG * IMG * CH * 2 + XBIAS), 0x00020000u};
            const u32x4 wr = {(unsigned)wb, (unsigned)(wb >> 32) & 0xffffu, (unsigned)WS_BYTES, 0x00020000u};
            unsigned s45 = OOB, s46 = OOB, s47 = (en0 & 0x7ffffffu) + 96 * w;
            unsigned s48 = ldsb + X1_OFF, s67 = ldsb + X_OFF, s76 = ldsb + WL_OFF + 4, s56 = en0 >> 30;
            unsigned s21 = SH_OFF + bx * 12288, s44 = ldsb + PG_OFF + w * 4, s70 = isV ? wave * 36864 : (wave - 2) * 36864 + 18432;
            unsigned long long xp = 0, sp = 0;
#pragma unroll
            for (int v = 0; v < 4; ++v) {
                const unsigned r = (CROW >> (8 * v)) & 0xFF;
                xp |= (unsigned long long)XPAT(r) << (16 * v);
                sp |= (unsigned long long)SPAT(r) << (16 * v);
            }
#define B_VIN "{v250}"(pXw_), "{v251}"(pSTw_), "{v252}"(aCA_), "{v253}"(aB_), "{v254}"(aA_), "{v255}"(idx_), "{v[238:241]}"(sel4)
#define B_SIN "{s[40:43]}"(orr), "{s[72:75]}"(xr), "{s39}"(0x401fffffu), "{s[28:31]}"(wr), "{s33}"(wave * 36864), "{s78}"(96 * w), \
              "{s27}"(96 * w - 384), "{s[50:51]}"(xp), "{s[52:53]}"(sp), "{s54}"(CLANE), "{s55}"(CROW & 0x0F0F0F0Fu), "{s[34:35]}"(0x0001000100010001ull), \
              "{s[60:61]}"(0x2222222222222222ull), "{s[62:63]}"(0x4444444444444444ull), "{s[64:65]}"(0x8888888888888888ull), \
              "{s77}"(w == 2 ? 768 : 6 * ROWBYTES + 768), "{s25}"(w == 2 ? 3 * ROWBYTES : OOB), "{s32}"(ldsb + X_OFF + 18432), \
              "{s24}"(ldsb + WL_OFF + 12 + 4 * (cnt - 0)), "{s66}"(ldsb + w * 8192), "{s71}"((ldsb + X_OFF) ^ (ldsb + X1_OFF)), \
              "{s23}"(ldsb + WL_OFF), "{s22}"(ldsb + O_OFF), \
              "{s20}"(6 * ROWBYTES + 2304 + 192 * (w & 1)), "{s19}"(2048 + (w & 1) * 1024)
            asm volatile("" :: B_VIN, "{v242}"(r242), "{v243}"(r243), "{v[244:247]}"(r244), "{v[228:231]}"(f1), "{v[232:235]}"(f2), "v"(lB), "v"(vq));
            asm volatile("" :: B_SIN);
            asm volatile("s_waitcnt vmcnt(9)" ::: "memory");
            unsigned lm = *(const unsigned*)(smem + TP_OFF + 4096 + w * 256 + ((l15 & 7) * 4 + g) * 4);
            lm = l15 < 8 ? lm : OOB + 47;
            const int row = lm & 63;
            u32x4 f0 = {XPAT(row) + (unsigned)(O_OFF - X_OFF), SPAT(row) + (unsigned)(STL_OFF - X_OFF - 18816), lB, vq};
            asm volatile(
            "s_setprio 3\n\t"
            "s_waitcnt lgkmcnt(0)\n\t"
            "s_barrier\n\t"
            "s_nop 7\n\t"
            "ds_read_b128 v[168:171], v254\n\t"
            "v_mov_b64 v[176:177], 0\n\t"
            "v_mov_b64 v[178:179], 0\n\t"
            "v_mov_b64 v[180:181], 0\n\t"
            "v_mov_b64 v[182:183], 0\n\t"
            "v_mov_b64 v[184:185], 0\n\t"
            "v_mov_b64 v[186:187], 0\n\t"
            "v_mov_b64 v[188:189], 0\n\t"
            "v_mov_b64 v[190:191], 0\n\t"
            "v_add_u32 v236, s67, v224\n\t"
            "v_xor_b32 v237, 16, v236\n\t"
            "v_add_u32 v248, s67, v225\n\t"
            "ds_read_b64 v[144:145], v226\n\t"
            "ds_read_b64 v[146:147], v226 offset:384\n\t"
            "ds_read_b64 v[148:149], v226 offset:3072\n\t"
            "ds_read_b64 v[150:151], v226 offset:3456\n\t"
            "ds_read_b128 v[172:175], v254 offset:64\n\t"
            "ds_read_b64 v[152:153], v226 offset:8\n\t"
            "ds_read_b64 v[154:155], v226 offset:392\n\t"
            "ds_read_b64 v[156:157], v226 offset:3080\n\t"
            "ds_read_b64 v[158:159], v226 offset:3464\n\t"
            "ds_read_b64 v[160:161], v226 offset:16\n\t"
            "ds_read_b64 v[162:163], v226 offset:400\n\t"
            "ds_read_b64 v[164:165], v226 offset:3088\n\t"
            "ds_read_b64 v[166:167], v226 offset:3472\n\t"
            "s_waitcnt lgkmcnt(9)\n\t"
            "v_smfmac_f32_16x16x64_bf16 v[176:179], v[168:171], v[144:151], v255\n\t"
            "ds_read_b128 v[168:171], v254 offset:128\n\t"
            "ds_read_b64 v[144:145], v226 offset:24\n\t"
            "ds_read_b64 v[146:147], v226 offset:408\n\t"
            "ds_read_b64 v[148:149], v226 offset:3096\n\t"
            "s_waitcnt lgkmcnt(8)\n\t"
            "v_smfmac_f32_16x16x64_bf16 v[180:183], v[172:175], v[152:159], v255\n\t"
            "ds_read_b64 v[150:151], v226 offset:3480\n\t"
            "ds_read_b128 v[172:175], v254 offset:192\n\t"
            "ds_read_b64 v[152:153], v226 offset:32\n\t"
            "v_cvt_pk_bf16_f32 v176, v176, v177\n\t"
            "v_cvt_pk_bf16_f32 v177, v178, v179\n\t"
            "ds_read_b64 v[154:155], v226 offset:416\n\t"
            "ds_read_b64 v[156:157], v226 offset:3104\n\t"
            "ds_read_b64 v[158:159], v226 offset:3488\n\t"
            "v_cvt_pk_bf16_f32 v178, v180, v181\n\t"
            "v_cvt_pk_bf16_f32 v179, v182, v183\n\t"
            "v_mov_b64 v[180:181], 0\n\t"
            "v_mov_b64 v[182:183], 0\n\t"
            "v_mfma_f32_4x4x4_16b_bf16 v[196:199], v[176:177], v[176:177], 0\n\t"
            "v_mfma_f32_4x4x4_16b_bf16 v[192:195], v[238:239], v[176:177], 0\n\t"
            "s_nop 0\n\t"
            "v_mfma_f32_4x4x4_16b_bf16 v[196:199], v[178:179], v[178:179], v[196:199]\n\t"
            "v_mfma_f32_4x4x4_16b_bf16 v[192:195], v[238:239], v[178:179], v[192:195]\n\t"
            "ds_write_b128 v236, v[176:179]\n\t"
            "s_waitcnt lgkmcnt(10)\n\t"
            "v_smfmac_f32_16x16x64_bf16 v[184:187], v[168:171], v[160:167], v255\n\t"
            "ds_read_b128 v[168:171], v254 offset:256\n\t"
            "s_waitcnt lgkmcnt(6)\n\t"
            "v_smfmac_f32_16x16x64_bf16 v[188:191], v[172:175], v[144:151], v255\n\t"
            "ds_read_b64 v[160:161], v226 offset:40\n\t"
            "v_mov_b64 v[176:177], 0\n\t"
            "v_mov_b64 v[178:179], 0\n\t"
            "ds_read_b64 v[162:163], v226 offset:424\n\t"
            "ds_read_b64 v[164:165], v226 offset:3112\n\t"
            "ds_read_b64 v[166:167], v226 offset:3496\n\t"
            "v_cvt_pk_bf16_f32 v184, v184, v185\n\t"
            "v_cvt_pk_bf16_f32 v185, v186, v187\n\t"
            "v_cvt_pk_bf16_f32 v186, v188, v189\n\t"
            "v_cvt_pk_bf16_f32 v187, v190, v191\n\t"
            "v_mov_b64 v[188:189], 0\n\t"
            "v_mov_b64 v[190:191], 0\n\t"
            "v_mfma_f32_4x4x4_16b_bf16 v[196:199], v[184:185], v[184:185], v[196:199]\n\t"
            "v_mfma_f32_4x4x4_16b_bf16 v[192:195], v[238:239], v[184:185], v[192:195]\n\t"
            "s_nop 0\n\t"
            "v_mfma_f32_4x4x4_16b_bf16 v[196:199], v[186:187], v[186:187], v[196:199]\n\t"
            "v_mfma_f32_4x4x4_16b_bf16 v[192:195], v[238:239], v[186:187], v[192:195]\n\t"
            "ds_read_b128 v[172:175], v254 offset:320\n\t"
            "ds_read_b64 v[144:145], v226 offset:48\n\t"
            "s_waitcnt lgkmcnt(6)\n\t"
            "v_smfmac_f32_16x16x64_bf16 v[176:179], v[168:171], v[152:159], v255\n\t"
            "ds_read_b64 v[146:147], v226 offset:432\n\t"
            "ds_read_b64 v[148:149], v226 offset:3120\n\t"
            "ds_read_b64 v[150:151], v226 offset:3504\n\t"
            "ds_read_b128 v[168:171], v254 offset:384\n\t"
            "ds_read_b64 v[152:153], v226 offset:56\n\t"
            "ds_read_b64 v[154:155], v226 offset:440\n\t"
            "ds_read_b64 v[156:157], v226 offset:3128\n\t"
            "ds_read_b64 v[158:159], v226 offset:3512\n\t"
            "v_cvt_pk_bf16_f32 v176, v176, v177\n\t"
            "v_cvt_pk_bf16_f32 v177, v178, v179\n\t"
            "ds_write_b128 v237, v[184:187]\n\t"
            "s_waitcnt lgkmcnt(10)\n\t"
            "v_smfmac_f32_16x16x64_bf16 v[180:183], v[172:175], v[160:167], v255\n\t"
            "ds_read_b128 v[172:175], v254 offset:448\n\t"
            "v_mov_b64 v[184:185], 0\n\t"
            "v_mov_b64 v[186:187], 0\n\t"
            "ds_read_b64 v[160:161], v226 offset:64\n\t"
            "ds_read_b64 v[162:163], v226 offset:448\n\t"
            "ds_read_b64 v[164:165], v226 offset:3136\n\t"
            "s_waitcnt lgkmcnt(9)\n\t"
            "v_smfmac_f32_16x16x64_bf16 v[184:187], v[168:171], v[144:151], v255\n\t"
            "ds_read_b64 v[166:167], v226 offset:3520\n\t"
            "v_cvt_pk_bf16_f32 v178, v180, v181\n\t"
            "v_cvt_pk_bf16_f32 v179, v182, v183\n\t"
            "v_mov_b64 v[180:181], 0\n\t"
            "v_mov_b64 v[182:183], 0\n\t"
            "v_mfma_f32_4x4x4_16b_bf16 v[196:199], v[176:177], v[176:177], v[196:199]\n\t"
            "v_mfma_f32_4x4x4_16b_bf16 v[192:195], v[238:239], v[176:177], v[192:195]\n\t"
            "s_nop 0\n\t"
            "v_mfma_f32_4x4x4_16b_bf16 v[196:199], v[178:179], v[178:179], v[196:199]\n\t"
            "v_mfma_f32_4x4x4_16b_bf16 v[192:195], v[238:239], v[178:179], v[192:195]\n\t"
            "ds_read_b128 v[168:171], v254 offset:512\n\t"
            "v_cvt_pk_bf16_f32 v184, v184, v185\n\t"
            "v_cvt_pk_bf16_f32 v185, v186, v187\n\t"
            "ds_read_b64 v[144:145], v226 offset:72\n\t"
            "s_waitcnt lgkmcnt(6)\n\t"
            "v_smfmac_f32_16x16x64_bf16 v[188:191], v[172:175], v[152:159], v255\n\t"
            "ds_read_b64 v[146:147], v226 offset:456\n\t"
            "ds_read_b64 v[148:149], v226 offset:3144\n\t"
            "ds_read_b64 v[150:151], v226 offset:3528\n\t"
            "ds_read_b128 v[172:175], v254 offset:576\n\t"
            "ds_read_b64 v[152:153], v226 offset:80\n\t"
            "ds_read_b64 v[154:155], v226 offset:464\n\t"
            "ds_read_b64 v[156:157], v226 offset:3152\n\t"
            "ds_read_b64 v[158:159], v226 offset:3536\n\t"
            "v_cvt_pk_bf16_f32 v186, v188, v189\n\t"
            "v_cvt_pk_bf16_f32 v187, v190, v191\n\t"
            "v_mov_b64 v[188:189], 0\n\t"
            "v_mov_b64 v[190:191], 0\n\t"
            "v_mfma_f32_4x4x4_16b_bf16 v[196:199], v[184:185], v[184:185], v[196:199]\n\t"
            "v_mfma_f32_4x4x4_16b_bf16 v[192:195], v[238:239], v[184:185], v[192:195]\n\t"
            "s_nop 0\n\t"
            "v_mfma_f32_4x4x4_16b_bf16 v[196:199], v[186:187], v[186:187], v[196:199]\n\t"
            "v_mfma_f32_4x4x4_16b_bf16 v[192:195], v[238:239], v[186:187], v[192:195]\n\t"
            "ds_write_b128 v236, v[176:179] offset:128\n\t"
            "v_mov_b64 v[176:177], 0\n\t"
            "v_mov_b64 v[178:179], 0\n\t"
            "s_waitcnt lgkmcnt(10)\n\t"
            "s_nop 0\n\t"
            "v_smfmac_f32_16x16x64_bf16 v[176:179], v[168:171], v[160:167], v255\n\t"
            "ds_read_b128 v[168:171], v254 offset:640\n\t"
            "s_waitcnt lgkmcnt(6)\n\t"
            "v_smfmac_f32_16x16x64_bf16 v[180:183], v[172:175], v[144:151], v255\n\t"
            "ds_read_b64 v[160:161], v226 offset:88\n\t"
            "ds_read_b64 v[162:163], v226 offset:472\n\t"
            "ds_read_b64 v[164:165], v226 offset:3160\n\t"
            "ds_read_b64 v[166:167], v226 offset:3544\n\t"
            "ds_read_b128 v[172:175], v254 offset:704\n\t"
            "ds_write_b128 v237, v[184:187] offset:128\n\t"
            "v_mov_b64 v[184:185], 0\n\t"
            "v_mov_b64 v[186:187], 0\n\t"
            "v_cvt_pk_bf16_f32 v176, v176, v177\n\t"
            "v_cvt_pk_bf16_f32 v177, v178, v179\n\t"
            "v_cvt_pk_bf16_f32 v178, v180, v181\n\t"
            "v_cvt_pk_bf16_f32 v179, v182, v183\n\t"
            "s_nop 1\n\t"
            "v_mfma_f32_4x4x4_16b_bf16 v[196:199], v[176:177], v[176:177], v[196:199]\n\t"
            "v_mfma_f32_4x4x4_16b_bf16 v[192:195], v[238:239], v[176:177], v[192:195]\n\t"
            "s_nop 0\n\t"
            "v_mfma_f32_4x4x4_16b_bf16 v[196:199], v[178:179], v[178:179], v[196:199]\n\t"
            "v_mfma_f32_4x4x4_16b_bf16 v[192:195], v[238:239], v[178:179], v[192:195]\n\t"
            "ds_write_b128 v236, v[176:179] offset:256\n\t"
            "s_waitcnt lgkmcnt(7)\n\t"
            "v_smfmac_f32_16x16x64_bf16 v[184:187], v[168:171], v[152:159], v255\n\t"
            "s_waitcnt lgkmcnt(2)\n\t"
            "v_smfmac_f32_16x16x64_bf16 v[188:191], v[172:175], v[160:167], v255\n\t"
            "s_nop 5\n\t"
            "v_cvt_pk_bf16_f32 v184, v184, v185\n\t"
            "v_cvt_pk_bf16_f32 v185, v186, v187\n\t"
            "v_cvt_pk_bf16_f32 v186, v188, v189\n\t"
            "v_cvt_pk_bf16_f32 v187, v190, v191\n\t"
            "s_nop 1\n\t"
            "v_mfma_f32_4x4x4_16b_bf16 v[196:199], v[184:185], v[184:185], v[196:199]\n\t"
            "v_mfma_f32_4x4x4_16b_bf16 v[192:195], v[238:239], v[184:185], v[192:195]\n\t"
            "s_nop 0\n\t"
            "v_mfma_f32_4x4x4_16b_bf16 v[196:199], v[186:187], v[186:187], v[196:199]\n\t"
            "v_mfma_f32_4x4x4_16b_bf16 v[192:195], v[238:239], v[186:187], v[192:195]\n\t"
            "ds_write_b128 v237, v[184:187] offset:256\n\t"
            "s_nop 6\n\t"
            "v_cndmask_b32_e64 v193, v196, v197, s[60:61]\n\t"
            "v_cndmask_b32_e64 v193, v193, v198, s[62:63]\n\t"
            "v_cndmask_b32_e64 v193, v193, v199, s[64:65]\n\t"
            "ds_write_b64 v248, v[192:193] offset:18816\n\t"
            "s_waitcnt lgkmcnt(0)\n\t"
            "s_nop 7\n\t"
            "v_mov_b32 v144, s44\n\t"
            "v_mov_b32 v145, 0x0fffffff\n\t"
            "ds_write_b32 v144, v145\n\t"
            "s_nop 7\n\t"
            "ds_read_b128 v[168:171], v254\n\t"
            "s_waitcnt vmcnt(0)\n\t"
            "ds_read_b64 v[144:145], v253\n\t"
            "v_mov_b64 v[176:177], 0\n\t"
            "v_mov_b64 v[178:179], 0\n\t"
            "v_mov_b64 v[180:181], 0\n\t"
            "v_mov_b64 v[182:183], 0\n\t"
            "v_mov_b32 v249, s76\n\t"
            "v_mov_b64 v[184:185], 0\n\t"
            "v_mov_b64 v[186:187], 0\n\t"
            "v_mov_b64 v[188:189], 0\n\t"
            "v_mov_b64 v[190:191], 0\n\t"
            "v_add_u32 v236, s67, v250\n\t"
            "v_add_u32 v248, s67, v251\n\t"
            "ds_read_b64 v[146:147], v253 offset:96\n\t"
            "ds_read_b64 v[148:149], v253 offset:1744\n\t"
            "ds_read_b64 v[150:151], v253 offset:1840\n\t"
            "ds_read_b128 v[172:175], v254 offset:64\n\t"
            "ds_read_b64 v[152:153], v253 offset:8\n\t"
            "ds_read_b64 v[154:155], v253 offset:104\n\t"
            "ds_read_b64 v[156:157], v253 offset:1752\n\t"
            "ds_read_b64 v[158:159], v253 offset:1848\n\t"
            "ds_read_b64 v[160:161], v253 offset:16\n\t"
            "ds_read_b64 v[162:163], v253 offset:112\n\t"
            "ds_read_b64 v[164:165], v253 offset:1760\n\t"
            "ds_read_b64 v[166:167], v253 offset:1856\n\t"
            "ds_read_b32 v249, v249\n\t"
            "s_waitcnt lgkmcnt(10)\n\t"
            "v_smfmac_f32_16x16x64_bf16 v[176:179], v[168:171], v[144:151], v255\n\t"
            "ds_read_b128 v[168:171], v254 offset:128\n\t"
            "ds_read_b64 v[144:145], v253 offset:24\n\t"
            "ds_read_b64 v[146:147], v253 offset:120\n\t"
            "ds_read_b64 v[148:149], v253 offset:1768\n\t"
            "s_waitcnt lgkmcnt(9)\n\t"
            "v_smfmac_f32_16x16x64_bf16 v[180:183], v[172:175], v[152:159], v255\n\t"
            "ds_read_b64 v[150:151], v253 offset:1864\n\t"
            "ds_read_b128 v[172:175], v254 offset:192\n\t"
            "ds_read_b64 v[152:153], v253 offset:32\n\t"
            "v_cvt_pk_bf16_f32 v176, v176, v177\n\t"
            "v_cvt_pk_bf16_f32 v177, v178, v179\n\t"
            "s_waitcnt lgkmcnt(7)\n\t"
            "v_readfirstlane_b32 s26, v249\n\t"
            "ds_read_b64 v[154:155], v253 offset:128\n\t"
            "s_and_b32 s68, s26, 0x7ffffff\n\t"
            "s_add_u32 s68, s68, s78\n\t"
            "s_and_b32 s69, s26, 0x38000000\n\t"
            "s_or_b32 s69, s69, s39\n\t"
            "s_bitcmp1_b32 s26, 27\n\t"
            "s_cselect_b32 s79, 2.0, s68\n\t"
            "ds_read_b64 v[156:157], v253 offset:1776\n\t"
            "s_and_b32 s57, s56, 3\n\t"
            "s_lshl_b32 s58, s57, 3\n\t"
            "s_lshr_b32 m0, s54, s58\n\t"
            "s_waitcnt lgkmcnt(8)\n\t"
            "v_smfmac_f32_16x16x64_bf16 v[184:187], v[168:171], v[160:167], v255\n\t"
            "s_lshl_b32 s58, s57, 4\n\t"
            "s_lshr_b64 s[36:37], s[50:51], s58\n\t"
            "s_and_b32 s57, s36, 0xffff\n\t"
            "s_add_u32 s57, s57, s67\n\t"
            "ds_read_b64 v[158:159], v253 offset:1872\n\t"
            "s_lshr_b64 s[36:37], s[52:53], s58\n\t"
            "s_and_b32 s58, s36, 0xffff\n\t"
            "s_add_u32 s58, s58, s67\n\t"
            "ds_read_b128 v[168:171], v254 offset:256\n\t"
            "s_and_b32 s59, s50, 0xffff\n\t"
            "s_add_u32 s59, s59, s67\n\t"
            "s_and_b32 s38, s52, 0xffff\n\t"
            "s_add_u32 s38, s38, s67\n\t"
            "s_waitcnt lgkmcnt(5)\n\t"
            "v_smfmac_f32_16x16x64_bf16 v[188:191], v[172:175], v[144:151], v255\n\t"
            "ds_read_b64 v[160:161], v253 offset:40\n\t"
            "v_cvt_pk_bf16_f32 v178, v180, v181\n\t"
            "v_cvt_pk_bf16_f32 v179, v182, v183\n\t"
            "v_writelane_b32 v236, s59, m0\n\t"
            "v_writelane_b32 v236, s57, 63\n\t"
            "v_mfma_f32_4x4x4_16b_bf16 v[196:199], v[176:177], v[176:177], 0\n\t"
            "v_mfma_f32_4x4x4_16b_bf16 v[192:195], v[238:239], v[176:177], 0\n\t"
            "s_nop 0\n\t"
            "v_mfma_f32_4x4x4_16b_bf16 v[196:199], v[178:179], v[178:179], v[196:199]\n\t"
            "v_mfma_f32_4x4x4_16b_bf16 v[192:195], v[238:239], v[178:179], v[192:195]\n\t"
            "ds_read_b64 v[162:163], v253 offset:136\n\t"
            "v_mov_b64 v[180:181], 0\n\t"
            "v_mov_b64 v[182:183], 0\n\t"
            "v_cvt_pk_bf16_f32 v184, v184, v185\n\t"
            "v_cvt_pk_bf16_f32 v185, v186, v187\n\t"
            "v_cvt_pk_bf16_f32 v186, v188, v189\n\t"
            "v_cvt_pk_bf16_f32 v187, v190, v191\n\t"
            "v_xor_b32 v237, 16, v236\n\t"
            "v_mov_b64 v[188:189], 0\n\t"
            "v_mfma_f32_4x4x4_16b_bf16 v[196:199], v[184:185], v[184:185], v[196:199]\n\t"
            "v_mfma_f32_4x4x4_16b_bf16 v[192:195], v[238:239], v[184:185], v[192:195]\n\t"
            "s_nop 0\n\t"
            "v_mfma_f32_4x4x4_16b_bf16 v[196:199], v[186:187], v[186:187], v[196:199]\n\t"
            "v_mfma_f32_4x4x4_16b_bf16 v[192:195], v[238:239], v[186:187], v[192:195]\n\t"
            "ds_read_b64 v[164:165], v253 offset:1784\n\t"
            "v_mov_b64 v[190:191], 0\n\t"
            "v_writelane_b32 v248, s38, m0\n\t"
            "v_writelane_b32 v248, s58, 63\n\t"
            "ds_read_b64 v[166:167], v253 offset:1880\n\t"
            "ds_read_b128 v[172:175], v254 offset:320\n\t"
            "ds_read_b64 v[144:145], v253 offset:48\n\t"
            "ds_read_b64 v[146:147], v253 offset:144\n\t"
            "ds_read_b64 v[148:149], v253 offset:1792\n\t"
            "ds_read_b64 v[150:151], v253 offset:1888\n\t"
            "ds_write_b128 v236, v[176:179]\n\t"
            "v_mov_b64 v[176:177], 0\n\t"
            "v_mov_b64 v[178:179], 0\n\t"
            "s_waitcnt lgkmcnt(10)\n\t"
            "s_nop 0\n\t"
            "v_smfmac_f32_16x16x64_bf16 v[176:179], v[168:171], v[152:159], v255\n\t"
            "ds_read_b128 v[168:171], v254 offset:384\n\t"
            "ds_read_b64 v[152:153], v253 offset:56\n\t"
            "ds_read_b64 v[154:155], v253 offset:152\n\t"
            "ds_read_b64 v[156:157], v253 offset:1800\n\t"
            "ds_read_b64 v[158:159], v253 offset:1896\n\t"
            "s_waitcnt lgkmcnt(10)\n\t"
            "v_smfmac_f32_16x16x64_bf16 v[180:183], v[172:175], v[160:167], v255\n\t"
            "ds_read_b128 v[172:175], v254 offset:448\n\t"
            "ds_read_b64 v[160:161], v253 offset:64\n\t"
            "v_cvt_pk_bf16_f32 v176, v176, v177\n\t"
            "v_cvt_pk_bf16_f32 v177, v178, v179\n\t"
            "ds_read_b64 v[162:163], v253 offset:160\n\t"
            "ds_read_b64 v[164:165], v253 offset:1808\n\t"
            "ds_read_b64 v[166:167], v253 offset:1904\n\t"
            "s_waitcnt lgkmcnt(14)\n\t"
            "ds_write_b128 v237, v[184:187]\n\t"
            "v_mov_b64 v[184:185], 0\n\t"
            "v_mov_b64 v[186:187], 0\n\t"
            "v_cvt_pk_bf16_f32 v178, v180, v181\n\t"
            "v_cvt_pk_bf16_f32 v179, v182, v183\n\t"
            "s_waitcnt lgkmcnt(10)\n\t"
            "v_smfmac_f32_16x16x64_bf16 v[184:187], v[168:171], v[144:151], v255\n\t"
            "ds_read_b128 v[168:171], v254 offset:512\n\t"
            "v_mov_b64 v[180:181], 0\n\t"
            "v_mov_b64 v[182:183], 0\n\t"
            "v_mfma_f32_4x4x4_16b_bf16 v[196:199], v[176:177], v[176:177], v[196:199]\n\t"
            "v_mfma_f32_4x4x4_16b_bf16 v[192:195], v[238:239], v[176:177], v[192:195]\n\t"
            "s_nop 0\n\t"
            "v_mfma_f32_4x4x4_16b_bf16 v[196:199], v[178:179], v[178:179], v[196:199]\n\t"
            "v_mfma_f32_4x4x4_16b_bf16 v[192:195], v[238:239], v[178:179], v[192:195]\n\t"
            "ds_read_b64 v[144:145], v253 offset:72\n\t"
            "s_waitcnt lgkmcnt(7)\n\t"
            "v_smfmac_f32_16x16x64_bf16 v[188:191], v[172:175], v[152:159], v255\n\t"
            "ds_read_b64 v[146:147], v253 offset:168\n\t"
            "v_cvt_pk_bf16_f32 v184, v184, v185\n\t"
            "v_cvt_pk_bf16_f32 v185, v186, v187\n\t"
            "ds_read_b64 v[148:149], v253 offset:1816\n\t"
            "ds_read_b64 v[150:151], v253 offset:1912\n\t"
            "ds_read_b128 v[172:175], v254 offset:576\n\t"
            "ds_read_b64 v[152:153], v253 offset:80\n\t"
            "ds_read_b64 v[154:155], v253 offset:176\n\t"
            "v_cvt_pk_bf16_f32 v186, v188, v189\n\t"
            "v_cvt_pk_bf16_f32 v187, v190, v191\n\t"
            "v_mov_b64 v[188:189], 0\n\t"
            "v_mov_b64 v[190:191], 0\n\t"
            "v_mfma_f32_4x4x4_16b_bf16 v[196:199], v[184:185], v[184:185], v[196:199]\n\t"
            "v_mfma_f32_4x4x4_16b_bf16 v[192:195], v[238:239], v[184:185], v[192:195]\n\t"
            "s_nop 0\n\t"
            "v_mfma_f32_4x4x4_16b_bf16 v[196:199], v[186:187], v[186:187], v[196:199]\n\t"
            "v_mfma_f32_4x4x4_16b_bf16 v[192:195], v[238:239], v[186:187], v[192:195]\n\t"
            "ds_read_b64 v[156:157], v253 offset:1824\n\t"
            "ds_read_b64 v[158:159], v253 offset:1920\n\t"
            "s_waitcnt lgkmcnt(14)\n\t"
            "ds_write_b128 v236, v[176:179] offset:128\n\t"
            "v_mov_b64 v[176:177], 0\n\t"
            "v_mov_b64 v[178:179], 0\n\t"
            "s_waitcnt lgkmcnt(10)\n\t"
            "s_nop 0\n\t"
            "v_smfmac_f32_16x16x64_bf16 v[176:179], v[168:171], v[160:167], v255\n\t"
            "ds_read_b128 v[168:171], v254 offset:640\n\t"
            "s_waitcnt lgkmcnt(6)\n\t"
            "v_smfmac_f32_16x16x64_bf16 v[180:183], v[172:175], v[144:151], v255\n\t"
            "ds_read_b64 v[160:161], v253 offset:88\n\t"
            "ds_read_b64 v[162:163], v253 offset:184\n\t"
            "ds_read_b64 v[164:165], v253 offset:1832\n\t"
            "ds_read_b64 v[166:167], v253 offset:1928\n\t"
            "ds_read_b128 v[172:175], v254 offset:704\n\t"
            "ds_write_b128 v237, v[184:187] offset:128\n\t"
            "v_mov_b64 v[184:185], 0\n\t"
            "v_mov_b64 v[186:187], 0\n\t"
            "v_cvt_pk_bf16_f32 v176, v176, v177\n\t"
            "v_cvt_pk_bf16_f32 v177, v178, v179\n\t"
            "v_cvt_pk_bf16_f32 v178, v180, v181\n\t"
            "v_cvt_pk_bf16_f32 v179, v182, v183\n\t"
            "s_nop 1\n\t"
            "v_mfma_f32_4x4x4_16b_bf16 v[196:199], v[176:177], v[176:177], v[196:199]\n\t"
            "v_mfma_f32_4x4x4_16b_bf16 v[192:195], v[238:239], v[176:177], v[192:195]\n\t"
            "s_nop 0\n\t"
            "v_mfma_f32_4x4x4_16b_bf16 v[196:199], v[178:179], v[178:179], v[196:199]\n\t"
            "v_mfma_f32_4x4x4_16b_bf16 v[192:195], v[238:239], v[178:179], v[192:195]\n\t"
            "ds_write_b128 v236, v[176:179] offset:256\n\t"
            "s_waitcnt lgkmcnt(7)\n\t"
            "v_smfmac_f32_16x16x64_bf16 v[184:187], v[168:171], v[152:159], v255\n\t"
            "s_waitcnt lgkmcnt(2)\n\t"
            "v_smfmac_f32_16x16x64_bf16 v[188:191], v[172:175], v[160:167], v255\n\t"
            "s_nop 5\n\t"
            "v_cvt_pk_bf16_f32 v184, v184, v185\n\t"
            "v_cvt_pk_bf16_f32 v185, v186, v187\n\t"
            "v_cvt_pk_bf16_f32 v186, v188, v189\n\t"
            "v_cvt_pk_bf16_f32 v187, v190, v191\n\t"
            "s_nop 1\n\t"
            "v_mfma_f32_4x4x4_16b_bf16 v[196:199], v[184:185], v[184:185], v[196:199]\n\t"
            "v_mfma_f32_4x4x4_16b_bf16 v[192:195], v[238:239], v[184:185], v[192:195]\n\t"
            "s_nop 0\n\t"
            "v_mfma_f32_4x4x4_16b_bf16 v[196:199], v[186:187], v[186:187], v[196:199]\n\t"
            "v_mfma_f32_4x4x4_16b_bf16 v[192:195], v[238:239], v[186:187], v[192:195]\n\t"
            "ds_write_b128 v237, v[184:187] offset:256\n\t"
            "s_nop 6\n\t"
            "v_cndmask_b32_e64 v193, v196, v197, s[60:61]\n\t"
            "v_cndmask_b32_e64 v193, v193, v198, s[62:63]\n\t"
            "v_cndmask_b32_e64 v193, v193, v199, s[64:65]\n\t"
            "ds_write_b64 v248, v[192:193] offset:18816\n\t"
            "s_waitcnt lgkmcnt(0)\n\t"
            "s_nop 7\n\t"
            "v_mov_b32 v144, s44\n\t"
            "v_mov_b32 v145, 0xbfffffff\n\t"
            "ds_write_b32 v144, v145\n\t"
            "s_setprio 1\n\t"
            "v_mov_b32 v144, s23\n\t"
            ".Lgo%=:\n\t"
            "ds_read_b32 v145, v144 offset:124\n\t"
            "s_waitcnt lgkmcnt(0)\n\t"
            "v_cmp_ne_u32 vcc, -1, v145\n\t"
            "s_nop 4\n\t"
            "s_cbranch_vccnz .Lgo%=\n\t"
            "s_nop 7\n\t"
            "ds_read_b32 v249, v252 offset:1024\n\t"
            "s_add_u32 s49, s70, 0\n\t"
            "buffer_load_dwordx4 v[0:3], v247, s[28:31], s49 offen sc1\n\t"
            "s_add_u32 s49, s70, 1024\n\t"
            "buffer_load_dwordx4 v[4:7], v247, s[28:31], s49 offen sc1\n\t"
            "s_add_u32 s49, s70, 2048\n\t"
            "buffer_load_dwordx4 v[8:11], v247, s[28:31], s49 offen sc1\n\t"
            "s_add_u32 s49, s70, 3072\n\t"
            "buffer_load_dwordx4 v[12:15], v247, s[28:31], s49 offen sc1\n\t"
            "s_add_u32 s49, s70, 4096\n\t"
            "buffer_load_dwordx4 v[16:19], v247, s[28:31], s49 offen sc1\n\t"
            "s_add_u32 s49, s70, 5120\n\t"
            "buffer_load_dwordx4 v[20:23], v247, s[28:31], s49 offen sc1\n\t"
            "s_add_u32 s49, s70, 6144\n\t"
            "buffer_load_dwordx4 v[24:27], v247, s[28:31], s49 offen sc1\n\t"
            "s_add_u32 s49, s70, 7168\n\t"
            "buffer_load_dwordx4 v[28:31], v247, s[28:31], s49 offen sc1\n\t"
            "s_add_u32 s49, s70, 8192\n\t"
            "buffer_load_dwordx4 v[32:35], v247, s[28:31], s49 offen sc1\n\t"
            "s_add_u32 s49, s70, 9216\n\t"
            "buffer_load_dwordx4 v[36:39], v247, s[28:31], s49 offen sc1\n\t"
            "s_add_u32 s49, s70, 10240\n\t"
            "buffer_load_dwordx4 v[40:43], v247, s[28:31], s49 offen sc1\n\t"
            "s_add_u32 s49, s70, 11264\n\t"
            "buffer_load_dwordx4 v[44:47], v247, s[28:31], s49 offen sc1\n\t"
            "s_add_u32 s49, s70, 12288\n\t"
            "buffer_load_dwordx4 v[48:51], v247, s[28:31], s49 offen sc1\n\t"
            "s_add_u32 s49, s70, 13312\n\t"
            "buffer_load_dwordx4 v[52:55], v247, s[28:31], s49 offen sc1\n\t"
            "s_add_u32 s49, s70, 14336\n\t"
            "buffer_load_dwordx4 v[56:59], v247, s[28:31], s49 offen sc1\n\t"
            "s_add_u32 s49, s70, 15360\n\t"
            "buffer_load_dwordx4 v[60:63], v247, s[28:31], s49 offen sc1\n\t"
            "s_add_u32 s49, s70, 16384\n\t"
            "buffer_load_dwordx4 v[64:67], v247, s[28:31], s49 offen sc1\n\t"
            "s_add_u32 s49, s70, 17408\n\t"
            "buffer_load_dwordx4 v[68:71], v247, s[28:31], s49 offen sc1\n\t"
            "s_waitcnt lgkmcnt(0)\n\t"
            "v_and_b32 v249, s69, v249\n\t"
            "s_mov_b32 exec_hi, 0x3fffff\n\t"
            "s_add_u32 m0, s66, 0\n\t"
            "s_add_u32 s49, s79, 0\n\t"
            "buffer_load_dwordx4 v249, s[72:75], s49 offen lds\n\t"
            "s_mov_b32 exec_hi, -1\n\t"
            "s_mov_b32 exec_hi, 0x3fffff\n\t"
            "s_add_u32 m0, s66, 872\n\t"
            "s_add_u32 s49, s68, 196224\n\t"
            "buffer_load_dwordx4 v249, s[72:75], s49 offen lds\n\t"
            "s_mov_b32 exec_hi, -1\n\t"
            "s_mov_b32 exec_hi, 0x3fffff\n\t"
            "s_add_u32 m0, s66, 1744\n\t"
            "s_add_u32 s49, s68, 392448\n\t"
            "buffer_load_dwordx4 v249, s[72:75], s49 offen lds\n\t"
            "s_mov_b32 exec_hi, -1\n\t"
            "s_mov_b32 exec_hi, 0x3fffff\n\t"
            "s_add_u32 m0, s66, 2616\n\t"
            "s_add_u32 s49, s68, 588672\n\t"
            "buffer_load_dwordx4 v249, s[72:75], s49 offen lds\n\t"
            "s_mov_b32 exec_hi, -1\n\t"
            "s_mov_b32 exec_hi, 0x3fffff\n\t"
            "s_add_u32 m0, s66, 3488\n\t"
            "s_add_u32 s49, s68, 784896\n\t"
            "buffer_load_dwordx4 v249, s[72:75], s49 offen lds\n\t"
            "s_mov_b32 exec_hi, -1\n\t"
            "s_mov_b32 exec_hi, 0x3fffff\n\t"
            "s_add_u32 m0, s66, 4360\n\t"
            "s_add_u32 s49, s68, 981120\n\t"
            "buffer_load_dwordx4 v249, s[72:75], s49 offen lds\n\t"
            "s_mov_b32 exec_hi, -1\n\t"
            "s_mov_b32 exec_hi, 0x3fffff\n\t"
            "s_add_u32 m0, s66, 5232\n\t"
            "s_add_u32 s49, s68, 1177344\n\t"
            "buffer_load_dwordx4 v249, s[72:75], s49 offen lds\n\t"
            "s_mov_b32 exec_hi, -1\n\t"
            "s_mov_b32 exec_hi, 0x3fffff\n\t"
            "s_add_u32 m0, s66, 6104\n\t"
            "s_add_u32 s49, s68, 1373568\n\t"
            "buffer_load_dwordx4 v249, s[72:75], s49 offen lds\n\t"
            "s_mov_b32 exec_hi, -1\n\t"
            "s_mov_b32 exec_hi, 0x3fffff\n\t"
            "s_add_u32 m0, s66, 6976\n\t"
            "s_add_u32 s49, s68, 1569792\n\t"
            "buffer_load_dwordx4 v249, s[72:75], s49 offen lds\n\t"
            "s_mov_b32 exec_hi, -1\n\t"
            "s_add_u32 s45, s46, s27\n\t"
            "s_mov_b32 s46, s47\n\t"
            "s_mov_b32 s47, s68\n\t"
            "s_xor_b32 s48, s48, s71\n\t"
            "s_xor_b32 s67, s67, s71\n\t"
            "s_add_u32 s76, s76, 4\n\t"
            "s_lshl_b32 s56, s56, 2\n\t"
            "s_lshr_b32 s49, s26, 30\n\t"
            "s_or_b32 s56, s56, s49\n\t"
            "s_nop 7\n\t"
            "v_mov_b32 v144, s23\n\t"
            ".Lpg%=:\n\t"
            "ds_read_b128 v[148:151], v144 offset:96\n\t"
            "s_waitcnt lgkmcnt(0)\n\t"
            "v_and_b32 v148, v148, v149\n\t"
            "v_and_b32 v150, v150, v151\n\t"
            "v_and_b32 v148, v148, v150\n\t"
            "v_cmp_le_i32 vcc, 0, v148\n\t"
            "s_nop 4\n\t"
            "s_cbranch_vccnz .Lpg%=\n\t"
            "s_setprio 3\n\t"
            "s_nop 7\n\t"
            "v_add_u32 v196, s22, v229\n\t"
            "v_xor_b32 v197, 64, v196\n\t"
            "v_add_u32 v198, s22, v247\n\t"
            "v_mov_b32 v249, s76\n\t"
            "v_add_u32 v236, s67, v250\n\t"
            "v_add_u32 v248, s67, v251\n\t"
            "ds_read_b128 v[144:147], v196\n\t"
            "ds_read_b128 v[148:151], v197\n\t"
            "ds_read_b128 v[152:155], v196 offset:128\n\t"
            "ds_read_b128 v[156:159], v197 offset:128\n\t"
            "ds_read_b128 v[160:163], v196 offset:256\n\t"
            "ds_read_b128 v[164:167], v197 offset:256\n\t"
            "ds_read_b128 v[216:219], v198 offset:12288\n\t"
            "ds_read_b128 v[220:223], v198 offset:13312\n\t"
            "v_add_u32 v198, s48, v247\n\t"
            "ds_read_b128 v[168:171], v196 offset:6144\n\t"
            "ds_read_b128 v[172:175], v197 offset:6144\n\t"
            "ds_read_b128 v[176:179], v196 offset:6272\n\t"
            "ds_read_b128 v[180:183], v197 offset:6272\n\t"
            "ds_read_b128 v[184:187], v196 offset:6400\n\t"
            "v_add_u32 v196, s48, v229\n\t"
            "ds_read_b128 v[188:191], v197 offset:6400\n\t"
            "s_waitcnt vmcnt(9)\n\t"
            "s_waitcnt lgkmcnt(13)\n\t"
            "v_mfma_f32_16x16x32_bf16 v[200:203], v[0:3], v[144:147], 0\n\t"
            "ds_read_b32 v249, v249\n\t"
            "v_xor_b32 v197, 64, v196\n\t"
            "s_waitcnt lgkmcnt(13)\n\t"
            "v_mfma_f32_16x16x32_bf16 v[200:203], v[4:7], v[148:151], v[200:203]\n\t"
            "s_add_u32 s49, s33, 18432\n\t"
            "buffer_load_dwordx4 v[72:75], v247, s[28:31], s49 offen sc1\n\t"
            "s_waitcnt lgkmcnt(12)\n\t"
            "v_mfma_f32_16x16x32_bf16 v[200:203], v[8:11], v[152:155], v[200:203]\n\t"
            "s_waitcnt lgkmcnt(8)\n\t"
            "v_add_f32 v216, v216, v218\n\t"
            "s_waitcnt lgkmcnt(7)\n\t"
            "v_add_f32 v220, v220, v222\n\t"
            "v_mfma_f32_16x16x32_bf16 v[200:203], v[12:15], v[156:159], v[200:203]\n\t"
            "v_add_f32 v216, v216, v220\n\t"
            "v_add_f32 v217, v217, v219\n\t"
            "v_mfma_f32_16x16x32_bf16 v[200:203], v[16:19], v[160:163], v[200:203]\n\t"
            "v_add_f32 v221, v221, v223\n\t"
            "v_mul_f32 v216, 0x3baaaaab, v216\n\t"
            "v_mfma_f32_16x16x32_bf16 v[200:203], v[20:23], v[164:167], v[200:203]\n\t"
            "v_add_f32 v217, v217, v221\n\t"
            "v_mul_f32_e64 v216, v216, -v216\n\t"
            "v_mfma_f32_16x16x32_bf16 v[204:207], v[24:27], v[144:147], 0\n\t"
            "v_fmac_f32 v216, 0x3baaaaab, v217\n\t"
            "v_max_f32 v216, 0, v216\n\t"
            "v_mfma_f32_16x16x32_bf16 v[204:207], v[28:31], v[148:151], v[204:207]\n\t"
            "v_add_f32 v216, 0x3727c5ac, v216\n\t"
            "v_rsq_f32 v216, v216\n\t"
            "v_mfma_f32_16x16x32_bf16 v[204:207], v[32:35], v[152:155], v[204:207]\n\t"
            "ds_write_b32 v232, v216\n\t"
            "v_mfma_f32_16x16x32_bf16 v[204:207], v[36:39], v[156:159], v[204:207]\n\t"
            "ds_read_b32 v192, v234\n\t"
            "s_waitcnt lgkmcnt(2)\n\t"
            "v_readfirstlane_b32 s26, v249\n\t"
            "v_mfma_f32_16x16x32_bf16 v[204:207], v[40:43], v[160:163], v[204:207]\n\t"
            "ds_read_b32 v193, v234 offset:64\n\t"
            "v_mfma_f32_16x16x32_bf16 v[204:207], v[44:47], v[164:167], v[204:207]\n\t"
            "ds_read_b32 v249, v252 offset:1024\n\t"
            "v_mfma_f32_16x16x32_bf16 v[208:211], v[48:51], v[144:147], 0\n\t"
            "ds_read_b128 v[144:147], v196\n\t"
            "v_mfma_f32_16x16x32_bf16 v[208:211], v[52:55], v[148:151], v[208:211]\n\t"
            "ds_read_b128 v[148:151], v197\n\t"
            "v_mfma_f32_16x16x32_bf16 v[208:211], v[56:59], v[152:155], v[208:211]\n\t"
            "ds_read_b128 v[152:155], v196 offset:128\n\t"
            "v_mfma_f32_16x16x32_bf16 v[208:211], v[60:63], v[156:159], v[208:211]\n\t"
            "ds_read_b128 v[156:159], v197 offset:128\n\t"
            "v_mfma_f32_16x16x32_bf16 v[208:211], v[64:67], v[160:163], v[208:211]\n\t"
            "ds_read_b128 v[160:163], v196 offset:256\n\t"
            "v_mfma_f32_16x16x32_bf16 v[208:211], v[68:71], v[164:167], v[208:211]\n\t"
            "ds_read_b128 v[164:167], v197 offset:256\n\t"
            "s_waitcnt lgkmcnt(8)\n\t"
            "v_mul_f32 v200, v192, v200\n\t"
            "v_mfma_f32_16x16x32_bf16 v[212:215], v[0:3], v[168:171], 0\n\t"
            "v_mul_f32 v201, v192, v201\n\t"
            "v_mul_f32 v204, v192, v204\n\t"
            "v_mfma_f32_16x16x32_bf16 v[212:215], v[4:7], v[172:175], v[212:215]\n\t"
            "v_mul_f32 v205, v192, v205\n\t"
            "v_mul_f32 v202, v192, v202\n\t"
            "v_mfma_f32_16x16x32_bf16 v[212:215], v[8:11], v[176:179], v[212:215]\n\t"
            "v_mul_f32 v203, v192, v203\n\t"
            "v_cvt_pk_bf16_f32 v200, v200, v201\n\t"
            "v_mfma_f32_16x16x32_bf16 v[212:215], v[12:15], v[180:183], v[212:215]\n\t"
            "v_mul_f32 v206, v192, v206\n\t"
            "v_mul_f32 v207, v192, v207\n\t"
            "v_mfma_f32_16x16x32_bf16 v[212:215], v[16:19], v[184:187], v[212:215]\n\t"
            "v_cvt_pk_bf16_f32 v204, v204, v205\n\t"
            "v_cvt_pk_bf16_f32 v201, v202, v203\n\t"
            "v_mfma_f32_16x16x32_bf16 v[212:215], v[20:23], v[188:191], v[212:215]\n\t"
            "v_cvt_pk_bf16_f32 v205, v206, v207\n\t"
            "v_mul_f32 v208, v192, v208\n\t"
            "v_mfma_f32_16x16x32_bf16 v[216:219], v[24:27], v[168:171], 0\n\t"
            "ds_write_b64 v227, v[200:201]\n\t"
            "v_mfma_f32_16x16x32_bf16 v[216:219], v[28:31], v[172:175], v[216:219]\n\t"
            "ds_read_b128 v[200:203], v198 offset:18816\n\t"
            "v_mul_f32 v209, v192, v209\n\t"
            "v_mfma_f32_16x16x32_bf16 v[216:219], v[32:35], v[176:179], v[216:219]\n\t"
            "ds_write_b64 v227, v[204:205] offset:128\n\t"
            "v_mfma_f32_16x16x32_bf16 v[216:219], v[36:39], v[180:183], v[216:219]\n\t"
            "ds_read_b128 v[204:207], v198 offset:19840\n\t"
            "v_mul_f32 v210, v192, v210\n\t"
            "v_mfma_f32_16x16x32_bf16 v[216:219], v[40:43], v[184:187], v[216:219]\n\t"
            "s_add_u32 s49, s33, 19456\n\t"
            "buffer_load_dwordx4 v[76:79], v247, s[28:31], s49 offen sc1\n\t"
            "v_mfma_f32_16x16x32_bf16 v[216:219], v[44:47], v[188:191], v[216:219]\n\t"
            "s_add_u32 s49, s33, 20480\n\t"
            "buffer_load_dwordx4 v[80:83], v247, s[28:31], s49 offen sc1\n\t"
            "v_mfma_f32_16x16x32_bf16 v[220:223], v[48:51], v[168:171], 0\n\t"
            "ds_read_b128 v[168:171], v196 offset:6144\n\t"
            "s_waitcnt lgkmcnt(3)\n\t"
            "v_add_f32 v200, v200, v202\n\t"
            "v_mfma_f32_16x16x32_bf16 v[220:223], v[52:55], v[172:175], v[220:223]\n\t"
            "ds_read_b128 v[172:175], v197 offset:6144\n\t"
            "s_waitcnt lgkmcnt(2)\n\t"
            "v_add_f32 v204, v204, v206\n\t"
            "v_mfma_f32_16x16x32_bf16 v[220:223], v[56:59], v[176:179], v[220:223]\n\t"
            "ds_read_b128 v[176:179], v196 offset:6272\n\t"
            "v_add_f32 v200, v200, v204\n\t"
            "v_mfma_f32_16x16x32_bf16 v[220:223], v[60:63], v[180:183], v[220:223]\n\t"
            "ds_read_b128 v[180:183], v197 offset:6272\n\t"
            "v_add_f32 v201, v201, v203\n\t"
            "v_mfma_f32_16x16x32_bf16 v[220:223], v[64:67], v[184:187], v[220:223]\n\t"
            "ds_read_b128 v[184:187], v196 offset:6400\n\t"
            "v_add_f32 v205, v205, v207\n\t"
            "v_mfma_f32_16x16x32_bf16 v[220:223], v[68:71], v[188:191], v[220:223]\n\t"
            "ds_read_b128 v[188:191], v197 offset:6400\n\t"
            "v_mul_f32 v200, 0x3baaaaab, v200\n\t"
            "v_add_f32 v201, v201, v205\n\t"
            "v_mul_f32_e64 v200, v200, -v200\n\t"
            "v_fmac_f32 v200, 0x3baaaaab, v201\n\t"
            "v_max_f32 v200, 0, v200\n\t"
            "v_add_f32 v200, 0x3727c5ac, v200\n\t"
            "v_rsq_f32 v200, v200\n\t"
            "v_mul_f32 v211, v192, v211\n\t"
            "v_cvt_pk_bf16_f32 v208, v208, v209\n\t"
            "v_cvt_pk_bf16_f32 v209, v210, v211\n\t"
            "v_mul_f32 v212, v193, v212\n\t"
            "v_mul_f32 v213, v193, v213\n\t"
            "v_mul_f32 v214, v193, v214\n\t"
            "v_mul_f32 v215, v193, v215\n\t"
            "v_cvt_pk_bf16_f32 v212, v212, v213\n\t"
            "v_cvt_pk_bf16_f32 v213, v214, v215\n\t"
            "v_mul_f32 v216, v193, v216\n\t"
            "v_mul_f32 v217, v193, v217\n\t"
            "v_mul_f32 v218, v193, v218\n\t"
            "v_mul_f32 v219, v193, v219\n\t"
            "v_mul_f32 v220, v193, v220\n\t"
            "v_mul_f32 v221, v193, v221\n\t"
            "v_mul_f32 v222, v193, v222\n\t"
            "v_mul_f32 v223, v193, v223\n\t"
            "v_cvt_pk_bf16_f32 v216, v216, v217\n\t"
            "v_cvt_pk_bf16_f32 v217, v218, v219\n\t"
            "v_cvt_pk_bf16_f32 v220, v220, v221\n\t"
            "v_cvt_pk_bf16_f32 v221, v222, v223\n\t"
            "ds_write_b32 v232, v200\n\t"
            "v_mfma_f32_16x16x32_bf16 v[200:203], v[144:147], v[0:3], 0\n\t"
            "ds_read_b128 v[192:195], v233\n\t"
            "v_mfma_f32_16x16x32_bf16 v[200:203], v[148:151], v[4:7], v[200:203]\n\t"
            "ds_write_b64 v227, v[208:209] offset:256\n\t"
            "v_mfma_f32_16x16x32_bf16 v[200:203], v[152:155], v[8:11], v[200:203]\n\t"
            "s_add_u32 s49, s33, 21504\n\t"
            "buffer_load_dwordx4 v[84:87], v247, s[28:31], s49 offen sc1\n\t"
            "v_mfma_f32_16x16x32_bf16 v[200:203], v[156:159], v[12:15], v[200:203]\n\t"
            "s_add_u32 s49, s33, 22528\n\t"
            "buffer_load_dwordx4 v[88:91], v247, s[28:31], s49 offen sc1\n\t"
            "v_mfma_f32_16x16x32_bf16 v[200:203], v[160:163], v[16:19], v[200:203]\n\t"
            "ds_write_b64 v228, v[212:213]\n\t"
            "v_mfma_f32_16x16x32_bf16 v[200:203], v[164:167], v[20:23], v[200:203]\n\t"
            "s_add_u32 s49, s33, 23552\n\t"
            "buffer_load_dwordx4 v[92:95], v247, s[28:31], s49 offen sc1\n\t"
            "v_mfma_f32_16x16x32_bf16 v[204:207], v[144:147], v[24:27], 0\n\t"
            "s_add_u32 s49, s33, 24576\n\t"
            "buffer_load_dwordx4 v[96:99], v247, s[28:31], s49 offen sc1\n\t"
            "v_mfma_f32_16x16x32_bf16 v[204:207], v[148:151], v[28:31], v[204:207]\n\t"
            "s_add_u32 s49, s33, 25600\n\t"
            "buffer_load_dwordx4 v[100:103], v247, s[28:31], s49 offen sc1\n\t"
            "v_mfma_f32_16x16x32_bf16 v[204:207], v[152:155], v[32:35], v[204:207]\n\t"
            "ds_write_b64 v228, v[216:217] offset:128\n\t"
            "v_mfma_f32_16x16x32_bf16 v[204:207], v[156:159], v[36:39], v[204:207]\n\t"
            "s_add_u32 s49, s33, 26624\n\t"
            "buffer_load_dwordx4 v[104:107], v247, s[28:31], s49 offen sc1\n\t"
            "v_mfma_f32_16x16x32_bf16 v[204:207], v[160:163], v[40:43], v[204:207]\n\t"
            "s_waitcnt lgkmcnt(3)\n\t"
            "v_mul_f32 v200, v192, v200\n\t"
            "v_mul_f32 v201, v193, v201\n\t"
            "v_mfma_f32_16x16x32_bf16 v[204:207], v[164:167], v[44:47], v[204:207]\n\t"
            "v_mul_f32 v202, v194, v202\n\t"
            "v_mul_f32 v203, v195, v203\n\t"
            "v_mfma_f32_16x16x32_bf16 v[208:211], v[144:147], v[48:51], 0\n\t"
            "ds_read_b128 v[144:147], v196 offset:12288\n\t"
            "v_cvt_pk_bf16_f32 v200, v200, v201\n\t"
            "v_mfma_f32_16x16x32_bf16 v[208:211], v[148:151], v[52:55], v[208:211]\n\t"
            "ds_read_b128 v[148:151], v197 offset:12288\n\t"
            "v_cvt_pk_bf16_f32 v201, v202, v203\n\t"
            "v_mfma_f32_16x16x32_bf16 v[208:211], v[152:155], v[56:59], v[208:211]\n\t"
            "ds_read_b128 v[152:155], v196 offset:12416\n\t"
            "v_mul_f32 v204, v192, v204\n\t"
            "v_mfma_f32_16x16x32_bf16 v[208:211], v[156:159], v[60:63], v[208:211]\n\t"
            "ds_read_b128 v[156:159], v197 offset:12416\n\t"
            "v_mul_f32 v205, v193, v205\n\t"
            "v_mfma_f32_16x16x32_bf16 v[208:211], v[160:163], v[64:67], v[208:211]\n\t"
            "ds_read_b128 v[160:163], v196 offset:12544\n\t"
            "v_mul_f32 v206, v194, v206\n\t"
            "v_mfma_f32_16x16x32_bf16 v[208:211], v[164:167], v[68:71], v[208:211]\n\t"
            "ds_read_b128 v[164:167], v197 offset:12544\n\t"
            "v_mul_f32 v207, v195, v207\n\t"
            "v_mfma_f32_16x16x32_bf16 v[212:215], v[168:171], v[0:3], 0\n\t"
            "s_add_u32 s49, s33, 27648\n\t"
            "buffer_load_dwordx4 v[108:111], v247, s[28:31], s49 offen sc1\n\t"
            "v_mfma_f32_16x16x32_bf16 v[212:215], v[172:175], v[4:7], v[212:215]\n\t"
            "ds_write_b64 v228, v[220:221] offset:256\n\t"
            "v_mfma_f32_16x16x32_bf16 v[212:215], v[176:179], v[8:11], v[212:215]\n\t"
            "v_mul_f32 v208, v192, v208\n\t"
            "v_mul_f32 v209, v193, v209\n\t"
            "v_mfma_f32_16x16x32_bf16 v[212:215], v[180:183], v[12:15], v[212:215]\n\t"
            "v_mul_f32 v210, v194, v210\n\t"
            "v_mul_f32 v211, v195, v211\n\t"
            "v_mfma_f32_16x16x32_bf16 v[212:215], v[184:187], v[16:19], v[212:215]\n\t"
            "ds_read_b128 v[192:195], v233 offset:64\n\t"
            "v_cvt_pk_bf16_f32 v204, v204, v205\n\t"
            "v_mfma_f32_16x16x32_bf16 v[212:215], v[188:191], v[20:23], v[212:215]\n\t"
            "s_add_u32 s49, s33, 28672\n\t"
            "buffer_load_dwordx4 v[112:115], v247, s[28:31], s49 offen sc1\n\t"
            "v_mfma_f32_16x16x32_bf16 v[216:219], v[168:171], v[24:27], 0\n\t"
            "s_add_u32 s49, s33, 29696\n\t"
            "buffer_load_dwordx4 v[116:119], v247, s[28:31], s49 offen sc1\n\t"
            "v_mfma_f32_16x16x32_bf16 v[216:219], v[172:175], v[28:31], v[216:219]\n\t"
            "s_add_u32 s49, s33, 30720\n\t"
            "buffer_load_dwordx4 v[120:123], v247, s[28:31], s49 offen sc1\n\t"
            "v_mfma_f32_16x16x32_bf16 v[216:219], v[176:179], v[32:35], v[216:219]\n\t"
            "s_waitcnt lgkmcnt(0)\n\t"
            "v_mul_f32 v212, v192, v212\n\t"
            "v_mul_f32 v213, v193, v213\n\t"
            "v_mfma_f32_16x16x32_bf16 v[216:219], v[180:183], v[36:39], v[216:219]\n\t"
            "v_mul_f32 v214, v194, v214\n\t"
            "v_mul_f32 v215, v195, v215\n\t"
            "v_mfma_f32_16x16x32_bf16 v[216:219], v[184:187], v[40:43], v[216:219]\n\t"
            "v_cvt_pk_bf16_f32 v202, v212, v213\n\t"
            "v_cvt_pk_bf16_f32 v203, v214, v215\n\t"
            "v_mfma_f32_16x16x32_bf16 v[216:219], v[188:191], v[44:47], v[216:219]\n\t"
            "s_add_u32 s49, s33, 31744\n\t"
            "buffer_load_dwordx4 v[124:127], v247, s[28:31], s49 offen sc1\n\t"
            "v_mfma_f32_16x16x32_bf16 v[220:223], v[168:171], v[48:51], 0\n\t"
            "ds_read_b128 v[168:171], v254\n\t"
            "v_cvt_pk_bf16_f32 v205, v206, v207\n\t"
            "v_mfma_f32_16x16x32_bf16 v[220:223], v[172:175], v[52:55], v[220:223]\n\t"
            "ds_read_b128 v[172:175], v254 offset:64\n\t"
            "v_cvt_pk_bf16_f32 v208, v208, v209\n\t"
            "v_mfma_f32_16x16x32_bf16 v[220:223], v[176:179], v[56:59], v[220:223]\n\t"
            "s_add_u32 s49, s33, 32768\n\t"
            "buffer_load_dwordx4 v[128:131], v247, s[28:31], s49 offen sc1\n\t"
            "v_mfma_f32_16x16x32_bf16 v[220:223], v[180:183], v[60:63], v[220:223]\n\t"
            "v_mul_f32 v216, v192, v216\n\t"
            "v_mul_f32 v217, v193, v217\n\t"
            "v_mfma_f32_16x16x32_bf16 v[220:223], v[184:187], v[64:67], v[220:223]\n\t"
            "v_mul_f32 v218, v194, v218\n\t"
            "v_mul_f32 v219, v195, v219\n\t"
            "v_mfma_f32_16x16x32_bf16 v[220:223], v[188:191], v[68:71], v[220:223]\n\t"
            "v_cvt_pk_bf16_f32 v206, v216, v217\n\t"
            "v_cvt_pk_bf16_f32 v207, v218, v219\n\t"
            "v_mfma_f32_16x16x32_bf16 v[212:215], v[144:147], v[0:3], 0\n\t"
            "s_add_u32 s49, s33, 33792\n\t"
            "buffer_load_dwordx4 v[132:135], v247, s[28:31], s49 offen sc1\n\t"
            "v_mfma_f32_16x16x32_bf16 v[212:215], v[148:151], v[4:7], v[212:215]\n\t"
            "s_add_u32 s49, s33, 34816\n\t"
            "buffer_load_dwordx4 v[136:139], v247, s[28:31], s49 offen sc1\n\t"
            "v_mfma_f32_16x16x32_bf16 v[212:215], v[152:155], v[8:11], v[212:215]\n\t"
            "v_cvt_pk_bf16_f32 v209, v210, v211\n\t"
            "v_mul_f32 v220, v192, v220\n\t"
            "v_mfma_f32_16x16x32_bf16 v[212:215], v[156:159], v[12:15], v[212:215]\n\t"
            "v_mul_f32 v221, v193, v221\n\t"
            "v_mul_f32 v222, v194, v222\n\t"
            "v_mfma_f32_16x16x32_bf16 v[212:215], v[160:163], v[16:19], v[212:215]\n\t"
            "v_mul_f32 v223, v195, v223\n\t"
            "v_cvt_pk_bf16_f32 v210, v220, v221\n\t"
            "v_mfma_f32_16x16x32_bf16 v[212:215], v[164:167], v[20:23], v[212:215]\n\t"
            "ds_read_b128 v[192:195], v233 offset:128\n\t"
            "v_cvt_pk_bf16_f32 v211, v222, v223\n\t"
            "v_mfma_f32_16x16x32_bf16 v[216:219], v[144:147], v[24:27], 0\n\t"
            "s_add_u32 s49, s33, 35840\n\t"
            "buffer_load_dwordx4 v[140:143], v247, s[28:31], s49 offen sc1\n\t"
            "v_mfma_f32_16x16x32_bf16 v[216:219], v[148:151], v[28:31], v[216:219]\n\t"
            "s_and_b32 s68, s26, 0x7ffffff\n\t"
            "s_add_u32 s68, s68, s78\n\t"
            "s_and_b32 s69, s26, 0x38000000\n\t"
            "s_or_b32 s69, s69, s39\n\t"
            "s_bitcmp1_b32 s26, 27\n\t"
            "s_cselect_b32 s79, 2.0, s68\n\t"
            "v_mfma_f32_16x16x32_bf16 v[216:219], v[152:155], v[32:35], v[216:219]\n\t"
            "s_and_b32 s57, s56, 3\n\t"
            "s_lshl_b32 s58, s57, 3\n\t"
            "s_lshr_b32 m0, s54, s58\n\t"
            "v_mfma_f32_16x16x32_bf16 v[216:219], v[156:159], v[36:39], v[216:219]\n\t"
            "s_lshl_b32 s58, s57, 4\n\t"
            "s_lshr_b64 s[36:37], s[50:51], s58\n\t"
            "s_and_b32 s57, s36, 0xffff\n\t"
            "s_add_u32 s57, s57, s67\n\t"
            "v_mfma_f32_16x16x32_bf16 v[216:219], v[160:163], v[40:43], v[216:219]\n\t"
            "s_lshr_b64 s[36:37], s[52:53], s58\n\t"
            "s_and_b32 s58, s36, 0xffff\n\t"
            "s_add_u32 s58, s58, s67\n\t"
            "v_mfma_f32_16x16x32_bf16 v[216:219], v[164:167], v[44:47], v[216:219]\n\t"
            "s_and_b32 s59, s50, 0xffff\n\t"
            "s_add_u32 s59, s59, s67\n\t"
            "s_and_b32 s38, s52, 0xffff\n\t"
            "s_add_u32 s38, s38, s67\n\t"
            "v_mfma_f32_16x16x32_bf16 v[220:223], v[144:147], v[48:51], 0\n\t"
            "s_waitcnt vmcnt(18)\n\t"
            "ds_read_b64 v[144:145], v253\n\t"
            "v_mov_b64 v[176:177], 0\n\t"
            "v_mfma_f32_16x16x32_bf16 v[220:223], v[148:151], v[52:55], v[220:223]\n\t"
            "ds_read_b64 v[146:147], v253 offset:96\n\t"
            "v_mov_b64 v[178:179], 0\n\t"
            "v_mfma_f32_16x16x32_bf16 v[220:223], v[152:155], v[56:59], v[220:223]\n\t"
            "ds_read_b64 v[148:149], v253 offset:1744\n\t"
            "v_mov_b64 v[180:181], 0\n\t"
            "v_mfma_f32_16x16x32_bf16 v[220:223], v[156:159], v[60:63], v[220:223]\n\t"
            "ds_read_b64 v[150:151], v253 offset:1840\n\t"
            "v_mov_b64 v[182:183], 0\n\t"
            "v_mfma_f32_16x16x32_bf16 v[220:223], v[160:163], v[64:67], v[220:223]\n\t"
            "ds_read_b64 v[152:153], v253 offset:8\n\t"
            "v_mov_b64 v[184:185], 0\n\t"
            "v_mfma_f32_16x16x32_bf16 v[220:223], v[164:167], v[68:71], v[220:223]\n\t"
            "ds_read_b64 v[154:155], v253 offset:104\n\t"
            "v_mov_b64 v[186:187], 0\n\t"
            "v_mov_b64 v[188:189], 0\n\t"
            "v_mov_b64 v[190:191], 0\n\t"
            "s_waitcnt lgkmcnt(6)\n\t"
            "v_mul_f32 v212, v192, v212\n\t"
            "v_mul_f32 v213, v193, v213\n\t"
            "v_mul_f32 v214, v194, v214\n\t"
            "v_mul_f32 v215, v195, v215\n\t"
            "v_mul_f32 v216, v192, v216\n\t"
            "v_mul_f32 v217, v193, v217\n\t"
            "v_mul_f32 v218, v194, v218\n\t"
            "v_mul_f32 v219, v195, v219\n\t"
            "v_mul_f32 v220, v192, v220\n\t"
            "v_mul_f32 v221, v193, v221\n\t"
            "v_mul_f32 v222, v194, v222\n\t"
            "v_mul_f32 v223, v195, v223\n\t"
            "v_writelane_b32 v236, s59, m0\n\t"
            "v_writelane_b32 v236, s57, 63\n\t"
            "v_xor_b32 v237, 16, v236\n\t"
            "v_writelane_b32 v248, s38, m0\n\t"
            "v_and_b32 v249, s69, v249\n\t"
            "v_cvt_pk_bf16_f32 v212, v212, v213\n\t"
            "v_cvt_pk_bf16_f32 v216, v216, v217\n\t"
            "s_waitcnt lgkmcnt(2)\n\t"
            "v_smfmac_f32_16x16x64_bf16 v[176:179], v[168:171], v[144:151], v255\n\t"
            "ds_read_b64 v[156:157], v253 offset:1752\n\t"
            "v_cvt_pk_bf16_f32 v220, v220, v221\n\t"
            "v_cvt_pk_bf16_f32 v213, v214, v215\n\t"
            "v_cvt_pk_bf16_f32 v217, v218, v219\n\t"
            "v_cvt_pk_bf16_f32 v221, v222, v223\n\t"
            "v_writelane_b32 v248, s58, 63\n\t"
            "ds_read_b64 v[158:159], v253 offset:1848\n\t"
            "ds_read_b64 v[160:161], v253 offset:16\n\t"
            "v_cvt_pk_bf16_f32 v176, v176, v177\n\t"
            "v_cvt_pk_bf16_f32 v177, v178, v179\n\t"
            "ds_read_b64 v[162:163], v253 offset:112\n\t"
            "ds_read_b64 v[164:165], v253 offset:1760\n\t"
            "ds_read_b64 v[166:167], v253 offset:1856\n\t"
            "ds_read_b128 v[168:171], v254 offset:128\n\t"
            "ds_read_b64 v[144:145], v253 offset:24\n\t"
            "ds_read_b64 v[146:147], v253 offset:120\n\t"
            "ds_read_b64 v[148:149], v253 offset:1768\n\t"
            "ds_read_b64 v[150:151], v253 offset:1864\n\t"
            "ds_write_b128 v230, v[200:203]\n\t"
            "ds_write_b128 v230, v[204:207] offset:1536\n\t"
            "s_waitcnt lgkmcnt(11)\n\t"
            "v_smfmac_f32_16x16x64_bf16 v[180:183], v[172:175], v[152:159], v255\n\t"
            "ds_read_b128 v[172:175], v254 offset:192\n\t"
            "ds_read_b64 v[152:153], v253 offset:32\n\t"
            "ds_read_b64 v[154:155], v253 offset:128\n\t"
            "ds_read_b64 v[156:157], v253 offset:1776\n\t"
            "s_waitcnt lgkmcnt(10)\n\t"
            "v_smfmac_f32_16x16x64_bf16 v[184:187], v[168:171], v[160:167], v255\n\t"
            "ds_read_b64 v[158:159], v253 offset:1872\n\t"
            "ds_read_b128 v[168:171], v254 offset:256\n\t"
            "ds_read_b64 v[160:161], v253 offset:40\n\t"
            "v_cvt_pk_bf16_f32 v178, v180, v181\n\t"
            "v_cvt_pk_bf16_f32 v179, v182, v183\n\t"
            "v_mov_b64 v[180:181], 0\n\t"
            "v_mov_b64 v[182:183], 0\n\t"
            "s_nop 0\n\t"
            "v_cvt_pk_bf16_f32 v184, v184, v185\n\t"
            "v_cvt_pk_bf16_f32 v185, v186, v187\n\t"
            "ds_read_b64 v[162:163], v253 offset:136\n\t"
            "ds_read_b64 v[164:165], v253 offset:1784\n\t"
            "s_waitcnt lgkmcnt(14)\n\t"
            "ds_read_b64 v[166:167], v253 offset:1880\n\t"
            "s_waitcnt lgkmcnt(9)\n\t"
            "v_smfmac_f32_16x16x64_bf16 v[188:191], v[172:175], v[144:151], v255\n\t"
            "ds_read_b128 v[172:175], v254 offset:320\n\t"
            "ds_read_b64 v[144:145], v253 offset:48\n\t"
            "v_mfma_f32_4x4x4_16b_bf16 v[196:199], v[176:177], v[176:177], 0\n\t"
            "v_mfma_f32_4x4x4_16b_bf16 v[192:195], v[238:239], v[176:177], 0\n\t"
            "s_nop 0\n\t"
            "v_mfma_f32_4x4x4_16b_bf16 v[196:199], v[178:179], v[178:179], v[196:199]\n\t"
            "v_mfma_f32_4x4x4_16b_bf16 v[192:195], v[238:239], v[178:179], v[192:195]\n\t"
            "ds_read_b64 v[146:147], v253 offset:144\n\t"
            "v_cvt_pk_bf16_f32 v186, v188, v189\n\t"
            "v_cvt_pk_bf16_f32 v187, v190, v191\n\t"
            "v_mov_b64 v[188:189], 0\n\t"
            "v_mov_b64 v[190:191], 0\n\t"
            "ds_read_b64 v[148:149], v253 offset:1792\n\t"
            "ds_read_b64 v[150:151], v253 offset:1888\n\t"
            "ds_write_b128 v236, v[176:179]\n\t"
            "v_mov_b64 v[176:177], 0\n\t"
            "v_mov_b64 v[178:179], 0\n\t"
            "s_waitcnt lgkmcnt(10)\n\t"
            "s_nop 0\n\t"
            "v_smfmac_f32_16x16x64_bf16 v[176:179], v[168:171], v[152:159], v255\n\t"
            "ds_read_b128 v[168:171], v254 offset:384\n\t"
            "s_waitcnt lgkmcnt(6)\n\t"
            "v_smfmac_f32_16x16x64_bf16 v[180:183], v[172:175], v[160:167], v255\n\t"
            "ds_read_b64 v[152:153], v253 offset:56\n\t"
            "ds_read_b64 v[154:155], v253 offset:152\n\t"
            "v_mfma_f32_4x4x4_16b_bf16 v[196:199], v[184:185], v[184:185], v[196:199]\n\t"
            "v_mfma_f32_4x4x4_16b_bf16 v[192:195], v[238:239], v[184:185], v[192:195]\n\t"
            "s_nop 0\n\t"
            "v_mfma_f32_4x4x4_16b_bf16 v[196:199], v[186:187], v[186:187], v[196:199]\n\t"
            "v_mfma_f32_4x4x4_16b_bf16 v[192:195], v[238:239], v[186:187], v[192:195]\n\t"
            "ds_read_b64 v[156:157], v253 offset:1800\n\t"
            "v_cvt_pk_bf16_f32 v176, v176, v177\n\t"
            "v_cvt_pk_bf16_f32 v177, v178, v179\n\t"
            "v_cvt_pk_bf16_f32 v178, v180, v181\n\t"
            "v_cvt_pk_bf16_f32 v179, v182, v183\n\t"
            "v_mov_b64 v[180:181], 0\n\t"
            "v_mov_b64 v[182:183], 0\n\t"
            "ds_read_b64 v[158:159], v253 offset:1896\n\t"
            "ds_read_b128 v[172:175], v254 offset:448\n\t"
            "ds_read_b64 v[160:161], v253 offset:64\n\t"
            "ds_read_b64 v[162:163], v253 offset:160\n\t"
            "ds_read_b64 v[164:165], v253 offset:1808\n\t"
            "ds_read_b64 v[166:167], v253 offset:1904\n\t"
            "s_waitcnt lgkmcnt(14)\n\t"
            "ds_write_b128 v237, v[184:187]\n\t"
            "v_mov_b64 v[184:185], 0\n\t"
            "v_mov_b64 v[186:187], 0\n\t"
            "s_waitcnt lgkmcnt(10)\n\t"
            "s_nop 0\n\t"
            "v_smfmac_f32_16x16x64_bf16 v[184:187], v[168:171], v[144:151], v255\n\t"
            "ds_read_b128 v[168:171], v254 offset:512\n\t"
            "ds_read_b64 v[144:145], v253 offset:72\n\t"
            "ds_read_b64 v[146:147], v253 offset:168\n\t"
            "ds_read_b64 v[148:149], v253 offset:1816\n\t"
            "ds_read_b64 v[150:151], v253 offset:1912\n\t"
            "s_waitcnt lgkmcnt(10)\n\t"
            "v_smfmac_f32_16x16x64_bf16 v[188:191], v[172:175], v[152:159], v255\n\t"
            "ds_read_b128 v[172:175], v254 offset:576\n\t"
            "ds_read_b64 v[152:153], v253 offset:80\n\t"
            "v_mfma_f32_4x4x4_16b_bf16 v[196:199], v[176:177], v[176:177], v[196:199]\n\t"
            "v_mfma_f32_4x4x4_16b_bf16 v[192:195], v[238:239], v[176:177], v[192:195]\n\t"
            "s_nop 0\n\t"
            "v_mfma_f32_4x4x4_16b_bf16 v[196:199], v[178:179], v[178:179], v[196:199]\n\t"
            "v_mfma_f32_4x4x4_16b_bf16 v[192:195], v[238:239], v[178:179], v[192:195]\n\t"
            "ds_read_b64 v[154:155], v253 offset:176\n\t"
            "v_cvt_pk_bf16_f32 v184, v184, v185\n\t"
            "v_cvt_pk_bf16_f32 v185, v186, v187\n\t"
            "v_cvt_pk_bf16_f32 v186, v188, v189\n\t"
            "v_cvt_pk_bf16_f32 v187, v190, v191\n\t"
            "v_mov_b64 v[188:189], 0\n\t"
            "v_mov_b64 v[190:191], 0\n\t"
            "ds_read_b64 v[156:157], v253 offset:1824\n\t"
            "ds_read_b64 v[158:159], v253 offset:1920\n\t"
            "s_waitcnt lgkmcnt(14)\n\t"
            "ds_write_b128 v236, v[176:179] offset:128\n\t"
            "v_mov_b64 v[176:177], 0\n\t"
            "v_mov_b64 v[178:179], 0\n\t"
            "s_waitcnt lgkmcnt(10)\n\t"
            "s_nop 0\n\t"
            "v_smfmac_f32_16x16x64_bf16 v[176:179], v[168:171], v[160:167], v255\n\t"
            "ds_read_b128 v[168:171], v254 offset:640\n\t"
            "s_waitcnt lgkmcnt(6)\n\t"
            "v_smfmac_f32_16x16x64_bf16 v[180:183], v[172:175], v[144:151], v255\n\t"
            "ds_read_b64 v[160:161], v253 offset:88\n\t"
            "ds_read_b64 v[162:163], v253 offset:184\n\t"
            "v_mfma_f32_4x4x4_16b_bf16 v[196:199], v[184:185], v[184:185], v[196:199]\n\t"
            "v_mfma_f32_4x4x4_16b_bf16 v[192:195], v[238:239], v[184:185], v[192:195]\n\t"
            "s_nop 0\n\t"
            "v_mfma_f32_4x4x4_16b_bf16 v[196:199], v[186:187], v[186:187], v[196:199]\n\t"
            "v_mfma_f32_4x4x4_16b_bf16 v[192:195], v[238:239], v[186:187], v[192:195]\n\t"
            "ds_read_b64 v[164:165], v253 offset:1832\n\t"
            "v_cvt_pk_bf16_f32 v176, v176, v177\n\t"
            "v_cvt_pk_bf16_f32 v177, v178, v179\n\t"
            "v_cvt_pk_bf16_f32 v178, v180, v181\n\t"
            "v_cvt_pk_bf16_f32 v179, v182, v183\n\t"
            "ds_read_b64 v[166:167], v253 offset:1928\n\t"
            "ds_read_b128 v[172:175], v254 offset:704\n\t"
            "ds_write_b128 v237, v[184:187] offset:128\n\t"
            "v_mov_b64 v[184:185], 0\n\t"
            "v_mov_b64 v[186:187], 0\n\t"
            "s_waitcnt lgkmcnt(6)\n\t"
            "s_nop 0\n\t"
            "v_smfmac_f32_16x16x64_bf16 v[184:187], v[168:171], v[152:159], v255\n\t"
            "ds_write_b128 v230, v[208:211] offset:3072\n\t"
            "ds_write_b128 v236, v[176:179] offset:256\n\t"
            "ds_write_b64 v231, v[212:213]\n\t"
            "ds_write_b64 v231, v[216:217] offset:1536\n\t"
            "ds_write_b64 v231, v[220:221] offset:3072\n\t"
            "s_waitcnt lgkmcnt(6)\n\t"
            "v_smfmac_f32_16x16x64_bf16 v[188:191], v[172:175], v[160:167], v255\n\t"
            "s_mov_b32 exec_hi, 0x3fffff\n\t"
            "s_add_u32 m0, s66, 0\n\t"
            "s_add_u32 s49, s79, 0\n\t"
            "buffer_load_dwordx4 v249, s[72:75], s49 offen lds\n\t"
            "s_mov_b32 exec_hi, -1\n\t"
            "v_mfma_f32_4x4x4_16b_bf16 v[196:199], v[176:177], v[176:177], v[196:199]\n\t"
            "v_mfma_f32_4x4x4_16b_bf16 v[192:195], v[238:239], v[176:177], v[192:195]\n\t"
            "s_nop 0\n\t"
            "v_mfma_f32_4x4x4_16b_bf16 v[196:199], v[178:179], v[178:179], v[196:199]\n\t"
            "v_mfma_f32_4x4x4_16b_bf16 v[192:195], v[238:239], v[178:179], v[192:195]\n\t"
            "s_mov_b32 exec_hi, 0x3fffff\n\t"
            "s_add_u32 m0, s66, 872\n\t"
            "s_add_u32 s49, s68, 196224\n\t"
            "buffer_load_dwordx4 v249, s[72:75], s49 offen lds\n\t"
            "s_mov_b32 exec_hi, -1\n\t"
            "s_mov_b32 exec_hi, 0x3fffff\n\t"
            "s_add_u32 m0, s66, 1744\n\t"
            "s_add_u32 s49, s68, 392448\n\t"
            "buffer_load_dwordx4 v249, s[72:75], s49 offen lds\n\t"
            "s_mov_b32 exec_hi, -1\n\t"
            "s_mov_b32 exec_hi, 0x3fffff\n\t"
            "s_add_u32 m0, s66, 2616\n\t"
            "s_add_u32 s49, s68, 588672\n\t"
            "buffer_load_dwordx4 v249, s[72:75], s49 offen lds\n\t"
            "s_mov_b32 exec_hi, -1\n\t"
            "s_mov_b32 exec_hi, 0x3fffff\n\t"
            "s_add_u32 m0, s66, 3488\n\t"
            "s_add_u32 s49, s68, 784896\n\t"
            "buffer_load_dwordx4 v249, s[72:75], s49 offen lds\n\t"
            "s_mov_b32 exec_hi, -1\n\t"
            "s_mov_b32 exec_hi, 0x3fffff\n\t"
            "s_add_u32 m0, s66, 4360\n\t"
            "s_add_u32 s49, s68, 981120\n\t"
            "buffer_load_dwordx4 v249, s[72:75], s49 offen lds\n\t"
            "s_mov_b32 exec_hi, -1\n\t"
            "v_cvt_pk_bf16_f32 v184, v184, v185\n\t"
            "v_cvt_pk_bf16_f32 v185, v186, v187\n\t"
            "v_cvt_pk_bf16_f32 v186, v188, v189\n\t"
            "v_cvt_pk_bf16_f32 v187, v190, v191\n\t"
            "s_nop 1\n\t"
            "v_mfma_f32_4x4x4_16b_bf16 v[196:199], v[184:185], v[184:185], v[196:199]\n\t"
            "v_mfma_f32_4x4x4_16b_bf16 v[192:195], v[238:239], v[184:185], v[192:195]\n\t"
            "s_nop 0\n\t"
            "v_mfma_f32_4x4x4_16b_bf16 v[196:199], v[186:187], v[186:187], v[196:199]\n\t"
            "v_mfma_f32_4x4x4_16b_bf16 v[192:195], v[238:239], v[186:187], v[192:195]\n\t"
            "s_mov_b32 exec_hi, 0x3fffff\n\t"
            "s_add_u32 m0, s66, 5232\n\t"
            "s_add_u32 s49, s68, 1177344\n\t"
            "buffer_load_dwordx4 v249, s[72:75], s49 offen lds\n\t"
            "s_mov_b32 exec_hi, -1\n\t"
            "s_mov_b32 exec_hi, 0x3fffff\n\t"
            "s_add_u32 m0, s66, 6104\n\t"
            "s_add_u32 s49, s68, 1373568\n\t"
            "buffer_load_dwordx4 v249, s[72:75], s49 offen lds\n\t"
            "s_mov_b32 exec_hi, -1\n\t"
            "v_cndmask_b32_e64 v193, v196, v197, s[60:61]\n\t"
            "v_cndmask_b32_e64 v193, v193, v198, s[62:63]\n\t"
            "v_cndmask_b32_e64 v193, v193, v199, s[64:65]\n\t"
            "ds_write_b128 v237, v[184:187] offset:256\n\t"
            "ds_write_b64 v248, v[192:193] offset:18816\n\t"
            "s_mov_b32 exec_hi, 0x3fffff\n\t"
            "s_add_u32 m0, s66, 6976\n\t"
            "s_add_u32 s49, s68, 1569792\n\t"
            "buffer_load_dwordx4 v249, s[72:75], s49 offen lds\n\t"
            "s_mov_b32 exec_hi, -1\n\t"
            "s_waitcnt lgkmcnt(0)\n\t"
            "s_nop 7\n\t"
            "v_mov_b32 v144, s44\n\t"
            "v_mov_b32 v145, -1\n\t"
            "ds_write_b32 v144, v145\n\t"
            "s_cmp_ge_u32 s33, 221184\n\t"
            "s_cbranch_scc1 .Lrp%=\n\t"
            "s_barrier\n\t"
            "s_waitcnt vmcnt(9)\n\t"
            "s_nop 7\n\t"
            "v_add_u32 v232, s67, v242\n\t"
            "v_xor_b32 v233, 64, v232\n\t"
            "v_add_u32 v234, s67, v247\n\t"
            "ds_read_b128 v[144:147], v232\n\t"
            "ds_read_b128 v[148:151], v233\n\t"
            "ds_read_b128 v[152:155], v232 offset:128\n\t"
            "ds_read_b128 v[156:159], v233 offset:128\n\t"
            "ds_read_b128 v[160:163], v232 offset:256\n\t"
            "ds_read_b128 v[164:167], v233 offset:256\n\t"
            "ds_read_b128 v[176:179], v234 offset:18816\n\t"
            "ds_read_b128 v[180:183], v234 offset:19840\n\t"
            "s_waitcnt lgkmcnt(7)\n\t"
            "v_mfma_f32_16x16x32_bf16 v[200:203], v[144:147], v[0:3], 0\n\t"
            "s_waitcnt lgkmcnt(6)\n\t"
            "v_mfma_f32_16x16x32_bf16 v[200:203], v[148:151], v[4:7], v[200:203]\n\t"
            "s_waitcnt lgkmcnt(5)\n\t"
            "v_mfma_f32_16x16x32_bf16 v[200:203], v[152:155], v[8:11], v[200:203]\n\t"
            "s_waitcnt lgkmcnt(4)\n\t"
            "v_mfma_f32_16x16x32_bf16 v[200:203], v[156:159], v[12:15], v[200:203]\n\t"
            "s_waitcnt lgkmcnt(1)\n\t"
            "v_add_f32 v176, v176, v178\n\t"
            "s_waitcnt lgkmcnt(0)\n\t"
            "v_add_f32 v180, v180, v182\n\t"
            "v_mfma_f32_16x16x32_bf16 v[200:203], v[160:163], v[16:19], v[200:203]\n\t"
            "v_add_f32 v176, v176, v180\n\t"
            "v_add_f32 v177, v177, v179\n\t"
            "v_mfma_f32_16x16x32_bf16 v[200:203], v[164:167], v[20:23], v[200:203]\n\t"
            "v_add_f32 v181, v181, v183\n\t"
            "v_mul_f32 v176, 0x3baaaaab, v176\n\t"
            "v_mfma_f32_16x16x32_bf16 v[204:207], v[144:147], v[24:27], 0\n\t"
            "v_add_f32 v177, v177, v181\n\t"
            "v_mul_f32_e64 v176, v176, -v176\n\t"
            "v_mfma_f32_16x16x32_bf16 v[204:207], v[148:151], v[28:31], v[204:207]\n\t"
            "v_fmac_f32 v176, 0x3baaaaab, v177\n\t"
            "v_max_f32 v176, 0, v176\n\t"
            "v_mfma_f32_16x16x32_bf16 v[204:207], v[152:155], v[32:35], v[204:207]\n\t"
            "v_add_f32 v176, 0x3727c5ac, v176\n\t"
            "v_rsq_f32 v176, v176\n\t"
            "v_mfma_f32_16x16x32_bf16 v[204:207], v[156:159], v[36:39], v[204:207]\n\t"
            "ds_write_b32 v246, v176\n\t"
            "v_mfma_f32_16x16x32_bf16 v[204:207], v[160:163], v[40:43], v[204:207]\n\t"
            "ds_read_b128 v[228:231], v245\n\t"
            "v_mfma_f32_16x16x32_bf16 v[204:207], v[164:167], v[44:47], v[204:207]\n\t"
            "v_mfma_f32_16x16x32_bf16 v[208:211], v[144:147], v[48:51], 0\n\t"
            "v_mfma_f32_16x16x32_bf16 v[208:211], v[148:151], v[52:55], v[208:211]\n\t"
            "v_mfma_f32_16x16x32_bf16 v[208:211], v[152:155], v[56:59], v[208:211]\n\t"
            "v_mfma_f32_16x16x32_bf16 v[208:211], v[156:159], v[60:63], v[208:211]\n\t"
            "v_mfma_f32_16x16x32_bf16 v[208:211], v[160:163], v[64:67], v[208:211]\n\t"
            "v_mfma_f32_16x16x32_bf16 v[208:211], v[164:167], v[68:71], v[208:211]\n\t"
            "v_mfma_f32_16x16x32_bf16 v[212:215], v[144:147], v[72:75], 0\n\t"
            "s_waitcnt lgkmcnt(0)\n\t"
            "v_mul_f32 v200, v228, v200\n\t"
            "v_mul_f32 v201, v229, v201\n\t"
            "v_mfma_f32_16x16x32_bf16 v[212:215], v[148:151], v[76:79], v[212:215]\n\t"
            "v_mul_f32 v204, v228, v204\n\t"
            "v_mul_f32 v205, v229, v205\n\t"
            "v_mfma_f32_16x16x32_bf16 v[212:215], v[152:155], v[80:83], v[212:215]\n\t"
            "v_mul_f32 v208, v228, v208\n\t"
            "v_mul_f32 v209, v229, v209\n\t"
            "v_mfma_f32_16x16x32_bf16 v[212:215], v[156:159], v[84:87], v[212:215]\n\t"
            "v_mul_f32 v202, v230, v202\n\t"
            "v_mul_f32 v203, v231, v203\n\t"
            "v_mfma_f32_16x16x32_bf16 v[212:215], v[160:163], v[88:91], v[212:215]\n\t"
            "v_cvt_pk_bf16_f32 v200, v200, v201\n\t"
            "v_mul_f32 v206, v230, v206\n\t"
            "v_mfma_f32_16x16x32_bf16 v[212:215], v[164:167], v[92:95], v[212:215]\n\t"
            "v_mul_f32 v207, v231, v207\n\t"
            "v_cvt_pk_bf16_f32 v204, v204, v205\n\t"
            "v_mfma_f32_16x16x32_bf16 v[216:219], v[144:147], v[96:99], 0\n\t"
            "v_mul_f32 v210, v230, v210\n\t"
            "v_mul_f32 v211, v231, v211\n\t"
            "v_mfma_f32_16x16x32_bf16 v[216:219], v[148:151], v[100:103], v[216:219]\n\t"
            "v_cvt_pk_bf16_f32 v208, v208, v209\n\t"
            "v_cvt_pk_bf16_f32 v201, v202, v203\n\t"
            "v_mfma_f32_16x16x32_bf16 v[216:219], v[152:155], v[104:107], v[216:219]\n\t"
            "v_mul_f32 v212, v228, v212\n\t"
            "v_mul_f32 v213, v229, v213\n\t"
            "v_mfma_f32_16x16x32_bf16 v[216:219], v[156:159], v[108:111], v[216:219]\n\t"
            "v_mul_f32 v214, v230, v214\n\t"
            "v_mul_f32 v215, v231, v215\n\t"
            "v_mfma_f32_16x16x32_bf16 v[216:219], v[160:163], v[112:115], v[216:219]\n\t"
            "v_cvt_pk_bf16_f32 v212, v212, v213\n\t"
            "v_cvt_pk_bf16_f32 v205, v206, v207\n\t"
            "v_mfma_f32_16x16x32_bf16 v[216:219], v[164:167], v[116:119], v[216:219]\n\t"
            "v_cvt_pk_bf16_f32 v209, v210, v211\n\t"
            "v_cvt_pk_bf16_f32 v213, v214, v215\n\t"
            "v_mfma_f32_16x16x32_bf16 v[220:223], v[144:147], v[120:123], 0\n\t"
            "v_mfma_f32_16x16x32_bf16 v[220:223], v[148:151], v[124:127], v[220:223]\n\t"
            "v_mfma_f32_16x16x32_bf16 v[220:223], v[152:155], v[128:131], v[220:223]\n\t"
            "v_mfma_f32_16x16x32_bf16 v[220:223], v[156:159], v[132:135], v[220:223]\n\t"
            "s_nop 1\n\t"
            "v_mul_f32 v216, v228, v216\n\t"
            "v_mfma_f32_16x16x32_bf16 v[220:223], v[160:163], v[136:139], v[220:223]\n\t"
            "v_mul_f32 v217, v229, v217\n\t"
            "v_mul_f32 v218, v230, v218\n\t"
            "v_mfma_f32_16x16x32_bf16 v[220:223], v[164:167], v[140:143], v[220:223]\n\t"
            "v_mul_f32 v219, v231, v219\n\t"
            "v_cvt_pk_bf16_f32 v216, v216, v217\n\t"
            "v_cvt_pk_bf16_f32 v217, v218, v219\n\t"
            "s_nop 4\n\t"
            "v_mul_f32 v220, v228, v220\n\t"
            "v_mul_f32 v221, v229, v221\n\t"
            "v_mul_f32 v222, v230, v222\n\t"
            "v_mul_f32 v223, v231, v223\n\t"
            "v_cvt_pk_bf16_f32 v220, v220, v221\n\t"
            "v_cvt_pk_bf16_f32 v221, v222, v223\n\t"
            "s_nop 7\n\t"
            "s_add_u32 s45, s46, s27\n\t"
            "s_mov_b32 s46, s47\n\t"
            "s_mov_b32 s47, s68\n\t"
            "s_xor_b32 s48, s48, s71\n\t"
            "s_xor_b32 s67, s67, s71\n\t"
            "s_add_u32 s76, s76, 4\n\t"
            "s_lshl_b32 s56, s56, 2\n\t"
            "s_lshr_b32 s49, s26, 30\n\t"
            "s_or_b32 s56, s56, s49\n\t"
            "ds_read_b128 v[168:171], v254\n\t"
            "ds_read_b128 v[172:175], v254 offset:64\n\t"
            "s_waitcnt vmcnt(0)\n\t"
            "ds_read_b64 v[144:145], v253\n\t"
            "ds_read_b64 v[146:147], v253 offset:96\n\t"
            "ds_read_b64 v[148:149], v253 offset:1744\n\t"
            "ds_read_b64 v[150:151], v253 offset:1840\n\t"
            "ds_read_b64 v[152:153], v253 offset:8\n\t"
            "ds_read_b64 v[154:155], v253 offset:104\n\t"
            "ds_read_b64 v[156:157], v253 offset:1752\n\t"
            "ds_read_b64 v[158:159], v253 offset:1848\n\t"
            "ds_read_b64 v[160:161], v253 offset:16\n\t"
            "ds_read_b64 v[162:163], v253 offset:112\n\t"
            "ds_read_b64 v[164:165], v253 offset:1760\n\t"
            "ds_read_b64 v[166:167], v253 offset:1856\n\t"
            "s_waitcnt lgkmcnt(0)\n\t"
            "s_nop 7\n\t"
            ".p2align 6\n\t"
            ".Lwc_loop%=:\n\t"
            "s_setprio 0\n\t"
            "ds_read_b128 v[228:231], v245 offset:64\n\t"
            "v_mov_b64 v[180:181], 0\n\t"
            "v_mov_b64 v[182:183], 0\n\t"
            "v_mov_b64 v[176:177], 0\n\t"
            "v_mov_b64 v[178:179], 0\n\t"
            "v_mov_b32 v249, s76\n\t"
            "v_mov_b64 v[184:185], 0\n\t"
            "s_waitcnt lgkmcnt(2)\n\t"
            "v_smfmac_f32_16x16x64_bf16 v[176:179], v[168:171], v[144:151], v255\n\t"
            "v_smfmac_f32_16x16x64_bf16 v[180:183], v[172:175], v[152:159], v255\n\t"
            "ds_read_b64 v[146:147], v253 offset:120\n\t"
            "v_mov_b64 v[188:189], 0\n\t"
            "v_mov_b64 v[186:187], 0\n\t"
            "v_mov_b64 v[190:191], 0\n\t"
            "v_add_u32 v236, s67, v250\n\t"
            "v_add_u32 v232, s48, v242\n\t"
            "v_xor_b32 v233, 64, v232\n\t"
            "v_add_u32 v248, s67, v251\n\t"
            "v_add_u32 v234, s67, v247\n\t"
            "v_cvt_pk_bf16_f32 v176, v176, v177\n\t"
            "v_cvt_pk_bf16_f32 v177, v178, v179\n\t"
            "v_cvt_pk_bf16_f32 v179, v182, v183\n\t"
            "v_cvt_pk_bf16_f32 v178, v180, v181\n\t"
            "v_mov_b64 v[180:181], 0\n\t"
            "v_mov_b64 v[182:183], 0\n\t"
            "ds_read_b64 v[144:145], v253 offset:24\n\t"
            "ds_read_b64 v[148:149], v253 offset:1768\n\t"
            "ds_read_b32 v249, v249\n\t"
            "ds_read_b128 v[172:175], v254 offset:192\n\t"
            "ds_read_b64 v[150:151], v253 offset:1864\n\t"
            "ds_read_b128 v[168:171], v254 offset:128\n\t"
            "ds_read_b64 v[154:155], v253 offset:128\n\t"
            "ds_read_b64 v[152:153], v253 offset:32\n\t"
            "ds_read_b64 v[156:157], v253 offset:1776\n\t"
            "ds_read_b64 v[158:159], v253 offset:1872\n\t"
            "s_waitcnt lgkmcnt(7)\n\t"
            "v_readfirstlane_b32 s26, v249\n\t"
            "ds_read_b32 v249, v252 offset:1024\n\t"
            "s_and_b32 s68, s26, 0x7ffffff\n\t"
            "s_add_u32 s68, s68, s78\n\t"
            "s_and_b32 s69, s26, 0x38000000\n\t"
            "s_or_b32 s69, s69, s39\n\t"
            "s_bitcmp1_b32 s26, 27\n\t"
            "s_cselect_b32 s79, 2.0, s68\n\t"
            "s_waitcnt lgkmcnt(5)\n\t"
            "v_smfmac_f32_16x16x64_bf16 v[184:187], v[168:171], v[160:167], v255\n\t"
            "v_smfmac_f32_16x16x64_bf16 v[188:191], v[172:175], v[144:151], v255\n\t"
            "s_and_b32 s57, s56, 3\n\t"
            "s_lshl_b32 s58, s57, 3\n\t"
            "s_lshr_b32 m0, s54, s58\n\t"
            "ds_read_b128 v[168:171], v254 offset:256\n\t"
            "s_lshl_b32 s58, s57, 4\n\t"
            "s_lshr_b64 s[36:37], s[50:51], s58\n\t"
            "s_and_b32 s57, s36, 0xffff\n\t"
            "s_add_u32 s57, s57, s67\n\t"
            "v_mfma_f32_4x4x4_16b_bf16 v[196:199], v[176:177], v[176:177], 0\n\t"
            "v_mfma_f32_4x4x4_16b_bf16 v[192:195], v[238:239], v[176:177], 0\n\t"
            "s_nop 0\n\t"
            "v_mfma_f32_4x4x4_16b_bf16 v[196:199], v[178:179], v[178:179], v[196:199]\n\t"
            "v_mfma_f32_4x4x4_16b_bf16 v[192:195], v[238:239], v[178:179], v[192:195]\n\t"
            "ds_read_b128 v[172:175], v254 offset:320\n\t"
            "s_lshr_b64 s[36:37], s[52:53], s58\n\t"
            "s_and_b32 s58, s36, 0xffff\n\t"
            "s_add_u32 s58, s58, s67\n\t"
            "ds_read_b64 v[164:165], v253 offset:1784\n\t"
            "s_and_b32 s59, s50, 0xffff\n\t"
            "s_add_u32 s59, s59, s67\n\t"
            "s_and_b32 s38, s52, 0xffff\n\t"
            "s_add_u32 s38, s38, s67\n\t"
            "ds_read_b64 v[166:167], v253 offset:1880\n\t"
            "v_writelane_b32 v236, s59, m0\n\t"
            "v_writelane_b32 v236, s57, 63\n\t"
            "v_cvt_pk_bf16_f32 v184, v184, v185\n\t"
            "v_cvt_pk_bf16_f32 v185, v186, v187\n\t"
            "v_cvt_pk_bf16_f32 v186, v188, v189\n\t"
            "v_cvt_pk_bf16_f32 v187, v190, v191\n\t"
            "v_xor_b32 v237, 16, v236\n\t"
            "v_mov_b64 v[188:189], 0\n\t"
            "v_mov_b64 v[190:191], 0\n\t"
            "v_writelane_b32 v248, s38, m0\n\t"
            "v_writelane_b32 v248, s58, 63\n\t"
            "s_waitcnt lgkmcnt(4)\n\t"
            "v_and_b32 v249, s69, v249\n\t"
            "s_barrier\n\t"
            "s_setprio 3\n\t"
            "ds_read_b64 v[162:163], v253 offset:136\n\t"
            "ds_read_b64 v[160:161], v253 offset:40\n\t"
            "ds_read_b64 v[150:151], v253 offset:1888\n\t"
            "ds_read_b64 v[144:145], v253 offset:48\n\t"
            "ds_read_b64 v[148:149], v253 offset:1792\n\t"
            "ds_read_b64 v[146:147], v253 offset:144\n\t"
            "ds_write_b128 v236, v[176:179]\n\t"
            "v_mov_b64 v[178:179], 0\n\t"
            "v_mov_b64 v[176:177], 0\n\t"
            "ds_write_b128 v237, v[184:187]\n\t"
            "s_waitcnt lgkmcnt(6)\n\t"
            "v_smfmac_f32_16x16x64_bf16 v[176:179], v[168:171], v[152:159], v255\n\t"
            "v_smfmac_f32_16x16x64_bf16 v[180:183], v[172:175], v[160:167], v255\n\t"
            "ds_read_b128 v[168:171], v254 offset:384\n\t"
            "ds_read_b64 v[152:153], v253 offset:56\n\t"
            "v_mfma_f32_4x4x4_16b_bf16 v[196:199], v[184:185], v[184:185], v[196:199]\n\t"
            "v_mfma_f32_4x4x4_16b_bf16 v[192:195], v[238:239], v[184:185], v[192:195]\n\t"
            "s_nop 0\n\t"
            "v_mfma_f32_4x4x4_16b_bf16 v[196:199], v[186:187], v[186:187], v[196:199]\n\t"
            "v_mfma_f32_4x4x4_16b_bf16 v[192:195], v[238:239], v[186:187], v[192:195]\n\t"
            "ds_read_b64 v[158:159], v253 offset:1896\n\t"
            "v_mov_b64 v[186:187], 0\n\t"
            "v_mov_b64 v[184:185], 0\n\t"
            "v_cvt_pk_bf16_f32 v176, v176, v177\n\t"
            "v_cvt_pk_bf16_f32 v177, v178, v179\n\t"
            "v_cvt_pk_bf16_f32 v178, v180, v181\n\t"
            "v_cvt_pk_bf16_f32 v179, v182, v183\n\t"
            "v_mov_b64 v[180:181], 0\n\t"
            "v_mov_b64 v[182:183], 0\n\t"
            "ds_read_b64 v[156:157], v253 offset:1800\n\t"
            "ds_read_b128 v[172:175], v254 offset:448\n\t"
            "ds_read_b64 v[154:155], v253 offset:152\n\t"
            "ds_read_b64 v[166:167], v253 offset:1904\n\t"
            "ds_read_b64 v[160:161], v253 offset:64\n\t"
            "ds_read_b64 v[164:165], v253 offset:1808\n\t"
            "s_waitcnt lgkmcnt(14)\n\t"
            "ds_read_b64 v[162:163], v253 offset:160\n\t"
            "s_waitcnt lgkmcnt(14)\n\t"
            "ds_write_b128 v236, v[176:179] offset:128\n\t"
            "s_waitcnt lgkmcnt(5)\n\t"
            "v_smfmac_f32_16x16x64_bf16 v[184:187], v[168:171], v[144:151], v255\n\t"
            "v_smfmac_f32_16x16x64_bf16 v[188:191], v[172:175], v[152:159], v255\n\t"
            "ds_read_b128 v[172:175], v254 offset:576\n\t"
            "ds_read_b64 v[146:147], v253 offset:168\n\t"
            "v_mfma_f32_4x4x4_16b_bf16 v[196:199], v[176:177], v[176:177], v[196:199]\n\t"
            "v_mfma_f32_4x4x4_16b_bf16 v[192:195], v[238:239], v[176:177], v[192:195]\n\t"
            "s_nop 0\n\t"
            "v_mfma_f32_4x4x4_16b_bf16 v[196:199], v[178:179], v[178:179], v[196:199]\n\t"
            "v_mfma_f32_4x4x4_16b_bf16 v[192:195], v[238:239], v[178:179], v[192:195]\n\t"
            "ds_read_b64 v[148:149], v253 offset:1816\n\t"
            "v_mov_b64 v[176:177], 0\n\t"
            "v_mov_b64 v[178:179], 0\n\t"
            "v_cvt_pk_bf16_f32 v184, v184, v185\n\t"
            "v_cvt_pk_bf16_f32 v185, v186, v187\n\t"
            "v_cvt_pk_bf16_f32 v187, v190, v191\n\t"
            "v_cvt_pk_bf16_f32 v186, v188, v189\n\t"
            "v_mov_b64 v[190:191], 0\n\t"
            "v_mov_b64 v[188:189], 0\n\t"
            "ds_read_b64 v[144:145], v253 offset:72\n\t"
            "ds_read_b64 v[150:151], v253 offset:1912\n\t"
            "ds_read_b128 v[168:171], v254 offset:512\n\t"
            "ds_read_b64 v[158:159], v253 offset:1920\n\t"
            "ds_read_b64 v[156:157], v253 offset:1824\n\t"
            "ds_read_b64 v[154:155], v253 offset:176\n\t"
            "ds_read_b64 v[152:153], v253 offset:80\n\t"
            "s_waitcnt lgkmcnt(14)\n\t"
            "ds_write_b128 v237, v[184:187] offset:128\n\t"
            "s_waitcnt lgkmcnt(5)\n\t"
            "v_smfmac_f32_16x16x64_bf16 v[176:179], v[168:171], v[160:167], v255\n\t"
            "v_smfmac_f32_16x16x64_bf16 v[180:183], v[172:175], v[144:151], v255\n\t"
            "ds_read_b64 v[166:167], v253 offset:1928\n\t"
            "ds_read_b128 v[168:171], v254 offset:640\n\t"
            "v_mfma_f32_4x4x4_16b_bf16 v[196:199], v[184:185], v[184:185], v[196:199]\n\t"
            "v_mfma_f32_4x4x4_16b_bf16 v[192:195], v[238:239], v[184:185], v[192:195]\n\t"
            "s_nop 0\n\t"
            "v_mfma_f32_4x4x4_16b_bf16 v[196:199], v[186:187], v[186:187], v[196:199]\n\t"
            "v_mfma_f32_4x4x4_16b_bf16 v[192:195], v[238:239], v[186:187], v[192:195]\n\t"
            "ds_read_b64 v[160:161], v253 offset:88\n\t"
            "v_mov_b64 v[184:185], 0\n\t"
            "v_mov_b64 v[186:187], 0\n\t"
            "v_cvt_pk_bf16_f32 v176, v176, v177\n\t"
            "v_cvt_pk_bf16_f32 v177, v178, v179\n\t"
            "v_cvt_pk_bf16_f32 v179, v182, v183\n\t"
            "v_cvt_pk_bf16_f32 v178, v180, v181\n\t"
            "ds_read_b128 v[172:175], v254 offset:704\n\t"
            "ds_read_b64 v[162:163], v253 offset:184\n\t"
            "ds_read_b64 v[164:165], v253 offset:1832\n\t"
            "ds_read_b128 v[144:147], v232 offset:6144\n\t"
            "ds_read_b128 v[148:151], v233 offset:6144\n\t"
            "ds_read_b128 v[180:183], v233 offset:12288\n\t"
            "ds_write_b128 v236, v[176:179] offset:256\n\t"
            "s_waitcnt lgkmcnt(4)\n\t"
            "v_smfmac_f32_16x16x64_bf16 v[184:187], v[168:171], v[152:159], v255\n\t"
            "v_smfmac_f32_16x16x64_bf16 v[188:191], v[172:175], v[160:167], v255\n\t"
            "ds_read_b128 v[152:155], v232 offset:6272\n\t"
            "ds_read_b128 v[156:159], v233 offset:6272\n\t"
            "v_mfma_f32_4x4x4_16b_bf16 v[196:199], v[176:177], v[176:177], v[196:199]\n\t"
            "v_mfma_f32_4x4x4_16b_bf16 v[192:195], v[238:239], v[176:177], v[192:195]\n\t"
            "s_nop 0\n\t"
            "v_mfma_f32_4x4x4_16b_bf16 v[196:199], v[178:179], v[178:179], v[196:199]\n\t"
            "v_mfma_f32_4x4x4_16b_bf16 v[192:195], v[238:239], v[178:179], v[192:195]\n\t"
            "ds_read_b128 v[160:163], v232 offset:6400\n\t"
            "v_cvt_pk_bf16_f32 v184, v184, v185\n\t"
            "v_cvt_pk_bf16_f32 v185, v186, v187\n\t"
            "v_cvt_pk_bf16_f32 v186, v188, v189\n\t"
            "v_cvt_pk_bf16_f32 v187, v190, v191\n\t"
            "s_nop 1\n\t"
            "v_mfma_f32_4x4x4_16b_bf16 v[196:199], v[184:185], v[184:185], v[196:199]\n\t"
            "v_mfma_f32_4x4x4_16b_bf16 v[192:195], v[238:239], v[184:185], v[192:195]\n\t"
            "s_nop 0\n\t"
            "v_mfma_f32_4x4x4_16b_bf16 v[196:199], v[186:187], v[186:187], v[196:199]\n\t"
            "v_mfma_f32_4x4x4_16b_bf16 v[192:195], v[238:239], v[186:187], v[192:195]\n\t"
            "ds_read_b128 v[164:167], v233 offset:6400\n\t"
            "s_waitcnt lgkmcnt(7)\n\t"
            "v_mfma_f32_16x16x32_bf16 v[224:227], v[144:147], v[0:3], 0\n\t"
            "s_setprio 3\n\t"
            "ds_read_b128 v[176:179], v232 offset:12288\n\t"
            "s_waitcnt lgkmcnt(7)\n\t"
            "v_mfma_f32_16x16x32_bf16 v[224:227], v[148:151], v[4:7], v[224:227]\n\t"
            "s_waitcnt lgkmcnt(4)\n\t"
            "ds_read_b128 v[188:191], v233 offset:12416\n\t"
            "v_mfma_f32_16x16x32_bf16 v[224:227], v[152:155], v[8:11], v[224:227]\n\t"
            "s_waitcnt lgkmcnt(4)\n\t"
            "v_cndmask_b32_e64 v193, v196, v197, s[60:61]\n\t"
            "v_cndmask_b32_e64 v193, v193, v198, s[62:63]\n\t"
            "v_mfma_f32_16x16x32_bf16 v[224:227], v[156:159], v[12:15], v[224:227]\n\t"
            "ds_write_b128 v237, v[184:187] offset:256\n\t"
            "s_mov_b32 exec_hi, 0x3fffff\n\t"
            "s_add_u32 m0, s66, 0\n\t"
            "s_add_u32 s49, s79, 0\n\t"
            "buffer_load_dwordx4 v249, s[72:75], s49 offen lds\n\t"
            "s_mov_b32 exec_hi, -1\n\t"
            "s_mov_b32 exec_hi, 0x3fffff\n\t"
            "s_add_u32 m0, s66, 872\n\t"
            "s_add_u32 s49, s68, 196224\n\t"
            "buffer_load_dwordx4 v249, s[72:75], s49 offen lds\n\t"
            "s_mov_b32 exec_hi, -1\n\t"
            "s_waitcnt lgkmcnt(4)\n\t"
            "v_mfma_f32_16x16x32_bf16 v[224:227], v[160:163], v[16:19], v[224:227]\n\t"
            "s_waitcnt lgkmcnt(3)\n\t"
            "ds_read_b128 v[184:187], v232 offset:12416\n\t"
            "v_cndmask_b32_e64 v193, v193, v199, s[64:65]\n\t"
            "v_mfma_f32_16x16x32_bf16 v[224:227], v[164:167], v[20:23], v[224:227]\n\t"
            "ds_write_b64 v248, v[192:193] offset:18816\n\t"
            "s_mov_b32 exec_hi, 0x3fffff\n\t"
            "s_add_u32 m0, s66, 1744\n\t"
            "s_add_u32 s49, s68, 392448\n\t"
            "buffer_load_dwordx4 v249, s[72:75], s49 offen lds\n\t"
            "s_mov_b32 exec_hi, -1\n\t"
            "s_mov_b32 exec_hi, 0x3fffff\n\t"
            "s_add_u32 m0, s66, 2616\n\t"
            "s_add_u32 s49, s68, 588672\n\t"
            "buffer_load_dwordx4 v249, s[72:75], s49 offen lds\n\t"
            "s_mov_b32 exec_hi, -1\n\t"
            "v_mfma_f32_16x16x32_bf16 v[168:171], v[144:147], v[24:27], 0\n\t"
            "ds_read_b128 v[192:195], v232 offset:12544\n\t"
            "v_mul_f32 v226, v230, v226\n\t"
            "v_mfma_f32_16x16x32_bf16 v[168:171], v[148:151], v[28:31], v[168:171]\n\t"
            "ds_read_b128 v[196:199], v233 offset:12544\n\t"
            "v_mul_f32 v224, v228, v224\n\t"
            "v_mfma_f32_16x16x32_bf16 v[168:171], v[152:155], v[32:35], v[168:171]\n\t"
            "v_mul_f32 v225, v229, v225\n\t"
            "v_cvt_pk_bf16_f32 v202, v224, v225\n\t"
            "v_mfma_f32_16x16x32_bf16 v[168:171], v[156:159], v[36:39], v[168:171]\n\t"
            "v_mul_f32 v227, v231, v227\n\t"
            "v_cvt_pk_bf16_f32 v203, v226, v227\n\t"
            "v_mfma_f32_16x16x32_bf16 v[168:171], v[160:163], v[40:43], v[168:171]\n\t"
            "v_add_u32 v232, s67, v242\n\t"
            "v_xor_b32 v233, 64, v232\n\t"
            "v_mfma_f32_16x16x32_bf16 v[168:171], v[164:167], v[44:47], v[168:171]\n\t"
            "s_mov_b32 exec_hi, 0x3fffff\n\t"
            "s_add_u32 m0, s66, 3488\n\t"
            "s_add_u32 s49, s68, 784896\n\t"
            "buffer_load_dwordx4 v249, s[72:75], s49 offen lds\n\t"
            "s_mov_b32 exec_hi, -1\n\t"
            "s_mov_b32 exec_hi, 0x3fffff\n\t"
            "s_add_u32 m0, s66, 4360\n\t"
            "s_add_u32 s49, s68, 981120\n\t"
            "buffer_load_dwordx4 v249, s[72:75], s49 offen lds\n\t"
            "s_mov_b32 exec_hi, -1\n\t"
            "ds_write_b128 v243, v[200:203]\n\t"
            "v_mfma_f32_16x16x32_bf16 v[172:175], v[144:147], v[48:51], 0\n\t"
            "v_mul_f32 v170, v230, v170\n\t"
            "v_mul_f32 v168, v228, v168\n\t"
            "v_mfma_f32_16x16x32_bf16 v[172:175], v[148:151], v[52:55], v[172:175]\n\t"
            "v_mul_f32 v171, v231, v171\n\t"
            "v_cvt_pk_bf16_f32 v207, v170, v171\n\t"
            "v_mfma_f32_16x16x32_bf16 v[172:175], v[152:155], v[56:59], v[172:175]\n\t"
            "v_mul_f32 v169, v229, v169\n\t"
            "v_cvt_pk_bf16_f32 v206, v168, v169\n\t"
            "v_mfma_f32_16x16x32_bf16 v[172:175], v[156:159], v[60:63], v[172:175]\n\t"
            "v_mfma_f32_16x16x32_bf16 v[172:175], v[160:163], v[64:67], v[172:175]\n\t"
            "v_mfma_f32_16x16x32_bf16 v[172:175], v[164:167], v[68:71], v[172:175]\n\t"
            "s_mov_b32 exec_hi, 0x3fffff\n\t"
            "s_add_u32 m0, s66, 5232\n\t"
            "s_add_u32 s49, s68, 1177344\n\t"
            "buffer_load_dwordx4 v249, s[72:75], s49 offen lds\n\t"
            "s_mov_b32 exec_hi, -1\n\t"
            "s_mov_b32 exec_hi, 0x3fffff\n\t"
            "s_add_u32 m0, s66, 6104\n\t"
            "s_add_u32 s49, s68, 1373568\n\t"
            "buffer_load_dwordx4 v249, s[72:75], s49 offen lds\n\t"
            "s_mov_b32 exec_hi, -1\n\t"
            "ds_write_b128 v243, v[204:207] offset:1536\n\t"
            "v_mfma_f32_16x16x32_bf16 v[224:227], v[144:147], v[72:75], 0\n\t"
            "v_mul_f32 v174, v230, v174\n\t"
            "v_mul_f32 v175, v231, v175\n\t"
            "v_mfma_f32_16x16x32_bf16 v[224:227], v[148:151], v[76:79], v[224:227]\n\t"
            "v_mul_f32 v172, v228, v172\n\t"
            "v_mul_f32 v173, v229, v173\n\t"
            "v_mfma_f32_16x16x32_bf16 v[224:227], v[152:155], v[80:83], v[224:227]\n\t"
            "v_cvt_pk_bf16_f32 v210, v172, v173\n\t"
            "v_cvt_pk_bf16_f32 v211, v174, v175\n\t"
            "v_mfma_f32_16x16x32_bf16 v[224:227], v[156:159], v[84:87], v[224:227]\n\t"
            "v_mfma_f32_16x16x32_bf16 v[224:227], v[160:163], v[88:91], v[224:227]\n\t"
            "v_mfma_f32_16x16x32_bf16 v[224:227], v[164:167], v[92:95], v[224:227]\n\t"
            "s_mov_b32 exec_hi, 0x3fffff\n\t"
            "s_add_u32 m0, s66, 6976\n\t"
            "s_add_u32 s49, s68, 1569792\n\t"
            "buffer_load_dwordx4 v249, s[72:75], s49 offen lds\n\t"
            "s_mov_b32 exec_hi, -1\n\t"
            "ds_write_b128 v243, v[208:211] offset:3072\n\t"
            "v_mfma_f32_16x16x32_bf16 v[168:171], v[144:147], v[96:99], 0\n\t"
            "s_add_u32 s45, s46, s27\n\t"
            "s_mov_b32 s46, s47\n\t"
            "s_mov_b32 s47, s68\n\t"
            "v_mfma_f32_16x16x32_bf16 v[168:171], v[148:151], v[100:103], v[168:171]\n\t"
            "s_xor_b32 s48, s48, s71\n\t"
            "s_xor_b32 s67, s67, s71\n\t"
            "s_add_u32 s76, s76, 4\n\t"
            "v_mfma_f32_16x16x32_bf16 v[168:171], v[152:155], v[104:107], v[168:171]\n\t"
            "s_lshl_b32 s56, s56, 2\n\t"
            "s_lshr_b32 s49, s26, 30\n\t"
            "s_or_b32 s56, s56, s49\n\t"
            "v_mfma_f32_16x16x32_bf16 v[168:171], v[156:159], v[108:111], v[168:171]\n\t"
            "v_mul_f32 v226, v230, v226\n\t"
            "v_mul_f32 v227, v231, v227\n\t"
            "v_mfma_f32_16x16x32_bf16 v[168:171], v[160:163], v[112:115], v[168:171]\n\t"
            "v_mul_f32 v225, v229, v225\n\t"
            "v_mul_f32 v224, v228, v224\n\t"
            "v_mfma_f32_16x16x32_bf16 v[168:171], v[164:167], v[116:119], v[168:171]\n\t"
            "v_cvt_pk_bf16_f32 v214, v224, v225\n\t"
            "v_cvt_pk_bf16_f32 v215, v226, v227\n\t"
            "ds_write_b128 v243, v[212:215] offset:4608\n\t"
            "v_mfma_f32_16x16x32_bf16 v[172:175], v[144:147], v[120:123], 0\n\t"
            "v_mfma_f32_16x16x32_bf16 v[172:175], v[148:151], v[124:127], v[172:175]\n\t"
            "v_mfma_f32_16x16x32_bf16 v[172:175], v[152:155], v[128:131], v[172:175]\n\t"
            "s_nop 1\n\t"
            "v_mul_f32 v168, v228, v168\n\t"
            "v_mfma_f32_16x16x32_bf16 v[172:175], v[156:159], v[132:135], v[172:175]\n\t"
            "v_mul_f32 v170, v230, v170\n\t"
            "v_mul_f32 v169, v229, v169\n\t"
            "v_mfma_f32_16x16x32_bf16 v[172:175], v[160:163], v[136:139], v[172:175]\n\t"
            "v_mul_f32 v171, v231, v171\n\t"
            "v_cvt_pk_bf16_f32 v219, v170, v171\n\t"
            "v_mfma_f32_16x16x32_bf16 v[172:175], v[164:167], v[140:143], v[172:175]\n\t"
            "v_cvt_pk_bf16_f32 v218, v168, v169\n\t"
            "ds_write_b128 v243, v[216:219] offset:6144\n\t"
            "s_waitcnt lgkmcnt(11)\n\t"
            "v_mfma_f32_16x16x32_bf16 v[224:227], v[176:179], v[0:3], 0\n\t"
            "v_mfma_f32_16x16x32_bf16 v[224:227], v[180:183], v[4:7], v[224:227]\n\t"
            "s_waitcnt lgkmcnt(8)\n\t"
            "v_mfma_f32_16x16x32_bf16 v[224:227], v[184:187], v[8:11], v[224:227]\n\t"
            "v_mfma_f32_16x16x32_bf16 v[224:227], v[188:191], v[12:15], v[224:227]\n\t"
            "s_waitcnt lgkmcnt(6)\n\t"
            "v_mul_f32 v174, v230, v174\n\t"
            "v_mfma_f32_16x16x32_bf16 v[224:227], v[192:195], v[16:19], v[224:227]\n\t"
            "s_waitcnt lgkmcnt(5)\n\t"
            "v_mul_f32 v175, v231, v175\n\t"
            "v_mul_f32 v172, v228, v172\n\t"
            "v_mfma_f32_16x16x32_bf16 v[224:227], v[196:199], v[20:23], v[224:227]\n\t"
            "v_mul_f32 v173, v229, v173\n\t"
            "v_cvt_pk_bf16_f32 v223, v174, v175\n\t"
            "v_mfma_f32_16x16x32_bf16 v[168:171], v[176:179], v[24:27], 0\n\t"
            "ds_read_b128 v[228:231], v245 offset:128\n\t"
            "v_cvt_pk_bf16_f32 v222, v172, v173\n\t"
            "v_mfma_f32_16x16x32_bf16 v[168:171], v[180:183], v[28:31], v[168:171]\n\t"
            "v_mfma_f32_16x16x32_bf16 v[168:171], v[184:187], v[32:35], v[168:171]\n\t"
            "v_mfma_f32_16x16x32_bf16 v[168:171], v[188:191], v[36:39], v[168:171]\n\t"
            "v_mfma_f32_16x16x32_bf16 v[168:171], v[192:195], v[40:43], v[168:171]\n\t"
            "v_mfma_f32_16x16x32_bf16 v[168:171], v[196:199], v[44:47], v[168:171]\n\t"
            "ds_write_b128 v243, v[220:223] offset:7680\n\t"
            "v_mfma_f32_16x16x32_bf16 v[172:175], v[176:179], v[48:51], 0\n\t"
            "v_mfma_f32_16x16x32_bf16 v[172:175], v[180:183], v[52:55], v[172:175]\n\t"
            "s_waitcnt lgkmcnt(1)\n\t"
            "v_mul_f32 v224, v228, v224\n\t"
            "v_mfma_f32_16x16x32_bf16 v[172:175], v[184:187], v[56:59], v[172:175]\n\t"
            "v_mul_f32 v226, v230, v226\n\t"
            "v_mul_f32 v225, v229, v225\n\t"
            "v_mfma_f32_16x16x32_bf16 v[172:175], v[188:191], v[60:63], v[172:175]\n\t"
            "v_mul_f32 v227, v231, v227\n\t"
            "v_cvt_pk_bf16_f32 v224, v224, v225\n\t"
            "v_mfma_f32_16x16x32_bf16 v[172:175], v[192:195], v[64:67], v[172:175]\n\t"
            "v_cvt_pk_bf16_f32 v225, v226, v227\n\t"
            "v_mul_f32 v169, v229, v169\n\t"
            "v_mfma_f32_16x16x32_bf16 v[172:175], v[196:199], v[68:71], v[172:175]\n\t"
            "ds_write_b64 v244, v[224:225]\n\t"
            "v_mfma_f32_16x16x32_bf16 v[224:227], v[176:179], v[72:75], 0\n\t"
            "v_mul_f32 v171, v231, v171\n\t"
            "v_mul_f32 v168, v228, v168\n\t"
            "v_mfma_f32_16x16x32_bf16 v[224:227], v[180:183], v[76:79], v[224:227]\n\t"
            "v_cvt_pk_bf16_f32 v168, v168, v169\n\t"
            "v_mul_f32 v170, v230, v170\n\t"
            "v_mfma_f32_16x16x32_bf16 v[224:227], v[184:187], v[80:83], v[224:227]\n\t"
            "v_cvt_pk_bf16_f32 v169, v170, v171\n\t"
            "v_mul_f32 v173, v229, v173\n\t"
            "v_mfma_f32_16x16x32_bf16 v[224:227], v[188:191], v[84:87], v[224:227]\n\t"
            "v_mul_f32 v175, v231, v175\n\t"
            "v_mul_f32 v174, v230, v174\n\t"
            "v_mfma_f32_16x16x32_bf16 v[224:227], v[192:195], v[88:91], v[224:227]\n\t"
            "v_mul_f32 v172, v228, v172\n\t"
            "v_cvt_pk_bf16_f32 v172, v172, v173\n\t"
            "v_mfma_f32_16x16x32_bf16 v[224:227], v[196:199], v[92:95], v[224:227]\n\t"
            "ds_write_b64 v244, v[168:169] offset:1536\n\t"
            "v_mfma_f32_16x16x32_bf16 v[168:171], v[176:179], v[96:99], 0\n\t"
            "v_cvt_pk_bf16_f32 v173, v174, v175\n\t"
            "v_mfma_f32_16x16x32_bf16 v[168:171], v[180:183], v[100:103], v[168:171]\n\t"
            "v_mfma_f32_16x16x32_bf16 v[168:171], v[184:187], v[104:107], v[168:171]\n\t"
            "v_mfma_f32_16x16x32_bf16 v[168:171], v[188:191], v[108:111], v[168:171]\n\t"
            "s_nop 1\n\t"
            "v_mul_f32 v225, v229, v225\n\t"
            "v_mfma_f32_16x16x32_bf16 v[168:171], v[192:195], v[112:115], v[168:171]\n\t"
            "v_mul_f32 v226, v230, v226\n\t"
            "v_mul_f32 v224, v228, v224\n\t"
            "v_mfma_f32_16x16x32_bf16 v[168:171], v[196:199], v[116:119], v[168:171]\n\t"
            "ds_write_b64 v244, v[172:173] offset:3072\n\t"
            "v_mfma_f32_16x16x32_bf16 v[172:175], v[176:179], v[120:123], 0\n\t"
            "v_mul_f32 v227, v231, v227\n\t"
            "v_cvt_pk_bf16_f32 v224, v224, v225\n\t"
            "v_mfma_f32_16x16x32_bf16 v[172:175], v[180:183], v[124:127], v[172:175]\n\t"
            "v_cvt_pk_bf16_f32 v225, v226, v227\n\t"
            "v_mfma_f32_16x16x32_bf16 v[172:175], v[184:187], v[128:131], v[172:175]\n\t"
            "s_nop 0\n\t"
            "v_mul_f32 v168, v228, v168\n\t"
            "v_mul_f32 v169, v229, v169\n\t"
            "v_mfma_f32_16x16x32_bf16 v[172:175], v[188:191], v[132:135], v[172:175]\n\t"
            "v_mul_f32 v171, v231, v171\n\t"
            "v_cvt_pk_bf16_f32 v168, v168, v169\n\t"
            "v_mfma_f32_16x16x32_bf16 v[172:175], v[192:195], v[136:139], v[172:175]\n\t"
            "v_mul_f32 v170, v230, v170\n\t"
            "v_cvt_pk_bf16_f32 v169, v170, v171\n\t"
            "v_mfma_f32_16x16x32_bf16 v[172:175], v[196:199], v[140:143], v[172:175]\n\t"
            "ds_write_b64 v244, v[224:225] offset:4608\n\t"
            "ds_write_b64 v244, v[168:169] offset:6144\n\t"
            "ds_read_b128 v[168:171], v254\n\t"
            "s_nop 4\n\t"
            "v_mul_f32 v172, v228, v172\n\t"
            "v_mul_f32 v173, v229, v173\n\t"
            "v_cvt_pk_bf16_f32 v172, v172, v173\n\t"
            "v_mul_f32 v174, v230, v174\n\t"
            "v_mul_f32 v175, v231, v175\n\t"
            "v_cvt_pk_bf16_f32 v173, v174, v175\n\t"
            "ds_write_b64 v244, v[172:173] offset:7680\n\t"
            "ds_read_b128 v[172:175], v254 offset:64\n\t"
            "s_waitcnt lgkmcnt(1)\n\t"
            "s_cmp_lt_u32 s46, 0x7000000\n\t"
            "s_barrier\n\t"
            "s_cbranch_scc0 .Lvx%=\n\t"
            "ds_read_b128 v[144:147], v232\n\t"
            "ds_read_b128 v[148:151], v233\n\t"
            "ds_read_b128 v[152:155], v232 offset:128\n\t"
            "ds_read_b128 v[156:159], v233 offset:128\n\t"
            "ds_read_b128 v[160:163], v232 offset:256\n\t"
            "ds_read_b128 v[164:167], v233 offset:256\n\t"
            "ds_read_b128 v[180:183], v234 offset:19840\n\t"
            "ds_read_b128 v[176:179], v234 offset:18816\n\t"
            "s_waitcnt lgkmcnt(7)\n\t"
            "v_mfma_f32_16x16x32_bf16 v[200:203], v[144:147], v[0:3], 0\n\t"
            "s_waitcnt lgkmcnt(6)\n\t"
            "v_mfma_f32_16x16x32_bf16 v[200:203], v[148:151], v[4:7], v[200:203]\n\t"
            "s_waitcnt lgkmcnt(5)\n\t"
            "v_mfma_f32_16x16x32_bf16 v[200:203], v[152:155], v[8:11], v[200:203]\n\t"
            "s_waitcnt lgkmcnt(4)\n\t"
            "v_mfma_f32_16x16x32_bf16 v[200:203], v[156:159], v[12:15], v[200:203]\n\t"
            "s_waitcnt lgkmcnt(1)\n\t"
            "v_add_f32 v181, v181, v183\n\t"
            "s_waitcnt lgkmcnt(0)\n\t"
            "v_add_f32 v176, v176, v178\n\t"
            "v_mfma_f32_16x16x32_bf16 v[200:203], v[160:163], v[16:19], v[200:203]\n\t"
            "v_add_f32 v177, v177, v179\n\t"
            "v_add_f32 v180, v180, v182\n\t"
            "v_mfma_f32_16x16x32_bf16 v[200:203], v[164:167], v[20:23], v[200:203]\n\t"
            "v_add_f32 v176, v176, v180\n\t"
            "v_mul_f32 v176, 0x3baaaaab, v176\n\t"
            "v_mfma_f32_16x16x32_bf16 v[204:207], v[144:147], v[24:27], 0\n\t"
            "v_mul_f32_e64 v176, v176, -v176\n\t"
            "v_add_f32 v177, v177, v181\n\t"
            "v_mfma_f32_16x16x32_bf16 v[204:207], v[148:151], v[28:31], v[204:207]\n\t"
            "v_fmac_f32 v176, 0x3baaaaab, v177\n\t"
            "v_max_f32 v176, 0, v176\n\t"
            "v_mfma_f32_16x16x32_bf16 v[204:207], v[152:155], v[32:35], v[204:207]\n\t"
            "v_add_f32 v176, 0x3727c5ac, v176\n\t"
            "v_mfma_f32_16x16x32_bf16 v[204:207], v[156:159], v[36:39], v[204:207]\n\t"
            "v_rsq_f32 v176, v176\n\t"
            "v_mfma_f32_16x16x32_bf16 v[204:207], v[160:163], v[40:43], v[204:207]\n\t"
            "ds_write_b32 v246, v176\n\t"
            "v_mfma_f32_16x16x32_bf16 v[204:207], v[164:167], v[44:47], v[204:207]\n\t"
            "ds_read_b128 v[228:231], v245\n\t"
            "v_mfma_f32_16x16x32_bf16 v[208:211], v[144:147], v[48:51], 0\n\t"
            "v_mfma_f32_16x16x32_bf16 v[208:211], v[148:151], v[52:55], v[208:211]\n\t"
            "v_mfma_f32_16x16x32_bf16 v[208:211], v[152:155], v[56:59], v[208:211]\n\t"
            "v_mfma_f32_16x16x32_bf16 v[208:211], v[156:159], v[60:63], v[208:211]\n\t"
            "v_mfma_f32_16x16x32_bf16 v[208:211], v[160:163], v[64:67], v[208:211]\n\t"
            "v_mfma_f32_16x16x32_bf16 v[208:211], v[164:167], v[68:71], v[208:211]\n\t"
            "v_mfma_f32_16x16x32_bf16 v[212:215], v[144:147], v[72:75], 0\n\t"
            "v_mfma_f32_16x16x32_bf16 v[212:215], v[148:151], v[76:79], v[212:215]\n\t"
            "s_waitcnt lgkmcnt(0)\n\t"
            "v_mul_f32 v201, v229, v201\n\t"
            "v_mul_f32 v205, v229, v205\n\t"
            "v_mfma_f32_16x16x32_bf16 v[212:215], v[152:155], v[80:83], v[212:215]\n\t"
            "v_mul_f32 v206, v230, v206\n\t"
            "v_mul_f32 v203, v231, v203\n\t"
            "v_mfma_f32_16x16x32_bf16 v[212:215], v[156:159], v[84:87], v[212:215]\n\t"
            "v_mul_f32 v211, v231, v211\n\t"
            "v_mul_f32 v208, v228, v208\n\t"
            "v_mfma_f32_16x16x32_bf16 v[212:215], v[160:163], v[88:91], v[212:215]\n\t"
            "v_mul_f32 v209, v229, v209\n\t"
            "v_cvt_pk_bf16_f32 v208, v208, v209\n\t"
            "v_mfma_f32_16x16x32_bf16 v[212:215], v[164:167], v[92:95], v[212:215]\n\t"
            "v_mul_f32 v204, v228, v204\n\t"
            "v_mul_f32 v207, v231, v207\n\t"
            "v_mfma_f32_16x16x32_bf16 v[216:219], v[144:147], v[96:99], 0\n\t"
            "v_mul_f32 v200, v228, v200\n\t"
            "v_cvt_pk_bf16_f32 v200, v200, v201\n\t"
            "v_mfma_f32_16x16x32_bf16 v[216:219], v[148:151], v[100:103], v[216:219]\n\t"
            "v_cvt_pk_bf16_f32 v204, v204, v205\n\t"
            "v_cvt_pk_bf16_f32 v205, v206, v207\n\t"
            "v_mfma_f32_16x16x32_bf16 v[216:219], v[152:155], v[104:107], v[216:219]\n\t"
            "v_mul_f32 v214, v230, v214\n\t"
            "v_mul_f32 v213, v229, v213\n\t"
            "v_mfma_f32_16x16x32_bf16 v[216:219], v[156:159], v[108:111], v[216:219]\n\t"
            "v_mul_f32 v212, v228, v212\n\t"
            "v_mul_f32 v215, v231, v215\n\t"
            "v_mfma_f32_16x16x32_bf16 v[216:219], v[160:163], v[112:115], v[216:219]\n\t"
            "v_mul_f32 v202, v230, v202\n\t"
            "v_cvt_pk_bf16_f32 v201, v202, v203\n\t"
            "v_mfma_f32_16x16x32_bf16 v[216:219], v[164:167], v[116:119], v[216:219]\n\t"
            "s_waitcnt vmcnt(0)\n\t"
            "v_mul_f32 v210, v230, v210\n\t"
            "v_mfma_f32_16x16x32_bf16 v[220:223], v[144:147], v[120:123], 0\n\t"
            "ds_read_b64 v[144:145], v253\n\t"
            "v_cvt_pk_bf16_f32 v209, v210, v211\n\t"
            "v_mfma_f32_16x16x32_bf16 v[220:223], v[148:151], v[124:127], v[220:223]\n\t"
            "ds_read_b64 v[150:151], v253 offset:1840\n\t"
            "v_cvt_pk_bf16_f32 v212, v212, v213\n\t"
            "v_mfma_f32_16x16x32_bf16 v[220:223], v[152:155], v[128:131], v[220:223]\n\t"
            "ds_read_b64 v[152:153], v253 offset:8\n\t"
            "v_mul_f32 v216, v228, v216\n\t"
            "v_mfma_f32_16x16x32_bf16 v[220:223], v[156:159], v[132:135], v[220:223]\n\t"
            "ds_read_b64 v[148:149], v253 offset:1744\n\t"
            "v_mul_f32 v217, v229, v217\n\t"
            "v_mfma_f32_16x16x32_bf16 v[220:223], v[160:163], v[136:139], v[220:223]\n\t"
            "ds_read_b64 v[146:147], v253 offset:96\n\t"
            "v_cvt_pk_bf16_f32 v216, v216, v217\n\t"
            "v_mfma_f32_16x16x32_bf16 v[220:223], v[164:167], v[140:143], v[220:223]\n\t"
            "s_setprio 0\n\t"
            "ds_read_b64 v[164:165], v253 offset:1760\n\t"
            "v_cvt_pk_bf16_f32 v213, v214, v215\n\t"
            "v_mul_f32 v219, v231, v219\n\t"
            "v_mul_f32 v218, v230, v218\n\t"
            "v_cvt_pk_bf16_f32 v217, v218, v219\n\t"
            "ds_read_b64 v[154:155], v253 offset:104\n\t"
            "ds_read_b64 v[162:163], v253 offset:112\n\t"
            "ds_read_b64 v[158:159], v253 offset:1848\n\t"
            "v_mul_f32 v220, v228, v220\n\t"
            "v_mul_f32 v223, v231, v223\n\t"
            "v_mul_f32 v222, v230, v222\n\t"
            "v_mul_f32 v221, v229, v221\n\t"
            "v_cvt_pk_bf16_f32 v220, v220, v221\n\t"
            "v_cvt_pk_bf16_f32 v221, v222, v223\n\t"
            "ds_read_b64 v[160:161], v253 offset:16\n\t"
            "ds_read_b64 v[156:157], v253 offset:1752\n\t"
            "ds_read_b64 v[166:167], v253 offset:1856\n\t"
            "s_branch .Lwc_loop%=\n\t"
            ".Lvx%=:\n\t"
            "s_waitcnt lgkmcnt(0)\n\t"
            "s_nop 7\n\t"
            "s_setprio 3\n\t"
            "v_add_u32 v236, s19, v252\n\t"
            "ds_read_b128 v[244:247], v236\n\t"
            "s_add_u32 s70, s33, 73728\n\t"
            "s_waitcnt lgkmcnt(0)\n\t"
            "buffer_load_dwordx4 v[200:203], v244, s[28:31], s21 offen\n\t"
            "buffer_load_dwordx4 v[204:207], v245, s[28:31], s21 offen\n\t"
            "buffer_load_dwordx4 v[208:211], v246, s[28:31], s21 offen\n\t"
            "s_add_u32 s21, s21, 6144\n\t"
            "buffer_load_dwordx4 v[212:215], v244, s[28:31], s21 offen\n\t"
            "buffer_load_dwordx4 v[216:219], v245, s[28:31], s21 offen\n\t"
            "buffer_load_dwordx4 v[220:223], v246, s[28:31], s21 offen\n\t"
            "s_nop 7\n\t"
            "s_add_u32 s49, s70, 0\n\t"
            "buffer_load_dwordx4 v[0:3], v247, s[28:31], s49 offen sc1\n\t"
            "s_add_u32 s49, s70, 1024\n\t"
            "buffer_load_dwordx4 v[4:7], v247, s[28:31], s49 offen sc1\n\t"
            "s_add_u32 s49, s70, 2048\n\t"
            "buffer_load_dwordx4 v[8:11], v247, s[28:31], s49 offen sc1\n\t"
            "s_add_u32 s49, s70, 3072\n\t"
            "buffer_load_dwordx4 v[12:15], v247, s[28:31], s49 offen sc1\n\t"
            "s_add_u32 s49, s70, 4096\n\t"
            "buffer_load_dwordx4 v[16:19], v247, s[28:31], s49 offen sc1\n\t"
            "s_add_u32 s49, s70, 5120\n\t"
            "buffer_load_dwordx4 v[20:23], v247, s[28:31], s49 offen sc1\n\t"
            "s_add_u32 s49, s70, 6144\n\t"
            "buffer_load_dwordx4 v[24:27], v247, s[28:31], s49 offen sc1\n\t"
            "s_add_u32 s49, s70, 7168\n\t"
            "buffer_load_dwordx4 v[28:31], v247, s[28:31], s49 offen sc1\n\t"
            "s_add_u32 s49, s70, 8192\n\t"
            "buffer_load_dwordx4 v[32:35], v247, s[28:31], s49 offen sc1\n\t"
            "s_add_u32 s49, s70, 9216\n\t"
            "buffer_load_dwordx4 v[36:39], v247, s[28:31], s49 offen sc1\n\t"
            "s_add_u32 s49, s70, 10240\n\t"
            "buffer_load_dwordx4 v[40:43], v247, s[28:31], s49 offen sc1\n\t"
            "s_add_u32 s49, s70, 11264\n\t"
            "buffer_load_dwordx4 v[44:47], v247, s[28:31], s49 offen sc1\n\t"
            "s_add_u32 s49, s70, 12288\n\t"
            "buffer_load_dwordx4 v[48:51], v247, s[28:31], s49 offen sc1\n\t"
            "s_add_u32 s49, s70, 13312\n\t"
            "buffer_load_dwordx4 v[52:55], v247, s[28:31], s49 offen sc1\n\t"
            "s_add_u32 s49, s70, 14336\n\t"
            "buffer_load_dwordx4 v[56:59], v247, s[28:31], s49 offen sc1\n\t"
            "s_add_u32 s49, s70, 15360\n\t"
            "buffer_load_dwordx4 v[60:63], v247, s[28:31], s49 offen sc1\n\t"
            "s_add_u32 s49, s70, 16384\n\t"
            "buffer_load_dwordx4 v[64:67], v247, s[28:31], s49 offen sc1\n\t"
            "s_add_u32 s49, s70, 17408\n\t"
            "buffer_load_dwordx4 v[68:71], v247, s[28:31], s49 offen sc1\n\t"
            "s_add_u32 s49, s70, 18432\n\t"
            "buffer_load_dwordx4 v[72:75], v247, s[28:31], s49 offen sc1\n\t"
            "s_add_u32 s49, s70, 19456\n\t"
            "buffer_load_dwordx4 v[76:79], v247, s[28:31], s49 offen sc1\n\t"
            "s_add_u32 s49, s70, 20480\n\t"
            "buffer_load_dwordx4 v[80:83], v247, s[28:31], s49 offen sc1\n\t"
            "s_add_u32 s49, s70, 21504\n\t"
            "buffer_load_dwordx4 v[84:87], v247, s[28:31], s49 offen sc1\n\t"
            "s_add_u32 s49, s70, 22528\n\t"
            "buffer_load_dwordx4 v[88:91], v247, s[28:31], s49 offen sc1\n\t"
            "s_add_u32 s49, s70, 23552\n\t"
            "buffer_load_dwordx4 v[92:95], v247, s[28:31], s49 offen sc1\n\t"
            "s_waitcnt vmcnt(18)\n\t"
            "s_add_u32 s49, s70, 24576\n\t"
            "buffer_load_dwordx4 v[96:99], v247, s[28:31], s49 offen sc1\n\t"
            "s_add_u32 s49, s70, 25600\n\t"
            "buffer_load_dwordx4 v[100:103], v247, s[28:31], s49 offen sc1\n\t"
            "v_add_u32 v242, s22, v242\n\t"
            "v_xor_b32 v243, 64, v242\n\t"
            "v_lshlrev_b32 v192, 16, v200\n\t"
            "v_and_b32 v193, 0xffff0000, v200\n\t"
            "v_lshlrev_b32 v194, 16, v201\n\t"
            "v_and_b32 v195, 0xffff0000, v201\n\t"
            "v_lshlrev_b32 v196, 16, v202\n\t"
            "v_and_b32 v197, 0xffff0000, v202\n\t"
            "v_lshlrev_b32 v198, 16, v203\n\t"
            "v_and_b32 v199, 0xffff0000, v203\n\t"
            "v_bfe_u32 v248, v247, 2, 6\n\t"
            "v_add_u32 v248, s23, v248\n\t"
            "v_lshlrev_b32 v224, 16, v204\n\t"
            "v_and_b32 v225, 0xffff0000, v204\n\t"
            "v_lshlrev_b32 v226, 16, v205\n\t"
            "v_and_b32 v227, 0xffff0000, v205\n\t"
            "v_lshrrev_b32 v236, 4, v247\n\t"
            "v_and_b32 v236, 48, v236\n\t"
            "v_add_u32 v236, s20, v236\n\t"
            "v_bfe_u32 v249, v247, 2, 6\n\t"
            "v_lshlrev_b32 v228, 16, v206\n\t"
            "v_and_b32 v229, 0xffff0000, v206\n\t"
            "v_lshlrev_b32 v230, 16, v207\n\t"
            "v_and_b32 v231, 0xffff0000, v207\n\t"
            "v_add_u32 v249, s23, v249\n\t"
            "ds_read_b128 v[144:147], v242 offset:53760\n\t"
            "ds_read_b128 v[148:151], v243 offset:53760\n\t"
            "ds_read_b128 v[152:155], v242 offset:53888\n\t"
            "ds_read_b128 v[156:159], v243 offset:53888\n\t"
            "ds_read_b128 v[160:163], v242 offset:54016\n\t"
            "ds_read_b128 v[164:167], v243 offset:54016\n\t"
            "ds_read_b32 v248, v248\n\t"
            "ds_read_b32 v249, v249 offset:64\n\t"
            "s_add_u32 s49, s70, 26624\n\t"
            "buffer_load_dwordx4 v[104:107], v247, s[28:31], s49 offen sc1\n\t"
            "s_add_u32 s49, s70, 27648\n\t"
            "buffer_load_dwordx4 v[108:111], v247, s[28:31], s49 offen sc1\n\t"
            "s_waitcnt lgkmcnt(7)\n\t"
            "v_mfma_f32_16x16x32_bf16 v[192:195], v[0:3], v[144:147], v[192:195]\n\t"
            "s_add_u32 s49, s70, 28672\n\t"
            "buffer_load_dwordx4 v[112:115], v247, s[28:31], s49 offen sc1\n\t"
            "s_waitcnt lgkmcnt(6)\n\t"
            "v_mfma_f32_16x16x32_bf16 v[192:195], v[4:7], v[148:151], v[192:195]\n\t"
            "s_add_u32 s49, s70, 29696\n\t"
            "buffer_load_dwordx4 v[116:119], v247, s[28:31], s49 offen sc1\n\t"
            "s_waitcnt lgkmcnt(5)\n\t"
            "v_mfma_f32_16x16x32_bf16 v[192:195], v[8:11], v[152:155], v[192:195]\n\t"
            "s_waitcnt vmcnt(18)\n\t"
            "s_add_u32 s49, s70, 30720\n\t"
            "buffer_load_dwordx4 v[120:123], v247, s[28:31], s49 offen sc1\n\t"
            "s_waitcnt lgkmcnt(4)\n\t"
            "v_mfma_f32_16x16x32_bf16 v[192:195], v[12:15], v[156:159], v[192:195]\n\t"
            "s_add_u32 s49, s70, 31744\n\t"
            "buffer_load_dwordx4 v[124:127], v247, s[28:31], s49 offen sc1\n\t"
            "s_waitcnt lgkmcnt(3)\n\t"
            "v_mfma_f32_16x16x32_bf16 v[192:195], v[16:19], v[160:163], v[192:195]\n\t"
            "s_add_u32 s49, s70, 32768\n\t"
            "buffer_load_dwordx4 v[128:131], v247, s[28:31], s49 offen sc1\n\t"
            "s_waitcnt lgkmcnt(2)\n\t"
            "v_mfma_f32_16x16x32_bf16 v[192:195], v[20:23], v[164:167], v[192:195]\n\t"
            "s_add_u32 s49, s70, 33792\n\t"
            "buffer_load_dwordx4 v[132:135], v247, s[28:31], s49 offen sc1\n\t"
            "v_mfma_f32_16x16x32_bf16 v[196:199], v[24:27], v[144:147], v[196:199]\n\t"
            "s_add_u32 s49, s70, 34816\n\t"
            "buffer_load_dwordx4 v[136:139], v247, s[28:31], s49 offen sc1\n\t"
            "v_mfma_f32_16x16x32_bf16 v[196:199], v[28:31], v[148:151], v[196:199]\n\t"
            "s_add_u32 s49, s70, 35840\n\t"
            "buffer_load_dwordx4 v[140:143], v247, s[28:31], s49 offen sc1\n\t"
            "v_mfma_f32_16x16x32_bf16 v[196:199], v[32:35], v[152:155], v[196:199]\n\t"
            "s_waitcnt vmcnt(18)\n\t"
            "s_waitcnt lgkmcnt(1)\n\t"
            "v_ashrrev_i32 v237, 31, v248\n\t"
            "v_mfma_f32_16x16x32_bf16 v[196:199], v[36:39], v[156:159], v[196:199]\n\t"
            "v_and_b32 v237, 1177344, v237\n\t"
            "v_sub_u32 v236, v236, v237\n\t"
            "v_mfma_f32_16x16x32_bf16 v[196:199], v[40:43], v[160:163], v[196:199]\n\t"
            "v_bfe_i32 v237, v248, 30, 1\n\t"
            "v_and_b32 v237, 2304, v237\n\t"
            "v_mfma_f32_16x16x32_bf16 v[196:199], v[44:47], v[164:167], v[196:199]\n\t"
            "v_cvt_pk_bf16_f32 v192, v192, v193\n\t"
            "v_sub_u32 v236, v236, v237\n\t"
            "v_mfma_f32_16x16x32_bf16 v[224:227], v[48:51], v[144:147], v[224:227]\n\t"
            "v_and_b32 v248, 0x7ffffff, v248\n\t"
            "v_cvt_pk_bf16_f32 v193, v194, v195\n\t"
            "v_mfma_f32_16x16x32_bf16 v[224:227], v[52:55], v[148:151], v[224:227]\n\t"
            "v_add_u32 v248, v248, v236\n\t"
            "v_lshrrev_b32 v236, 4, v247\n\t"
            "v_mfma_f32_16x16x32_bf16 v[224:227], v[56:59], v[152:155], v[224:227]\n\t"
            "v_cvt_pk_bf16_f32 v194, v196, v197\n\t"
            "v_cvt_pk_bf16_f32 v195, v198, v199\n\t"
            "v_mfma_f32_16x16x32_bf16 v[224:227], v[60:63], v[156:159], v[224:227]\n\t"
            "buffer_store_dwordx4 v[192:195], v248, s[40:43], 0 offen sc0 sc1\n\t"
            "v_mfma_f32_16x16x32_bf16 v[224:227], v[64:67], v[160:163], v[224:227]\n\t"
            "s_waitcnt vmcnt(13)\n\t"
            "v_and_b32 v236, 48, v236\n\t"
            "s_waitcnt lgkmcnt(0)\n\t"
            "v_ashrrev_i32 v237, 31, v249\n\t"
            "v_mfma_f32_16x16x32_bf16 v[224:227], v[68:71], v[164:167], v[224:227]\n\t"
            "v_add_u32 v236, s20, v236\n\t"
            "v_and_b32 v237, 1177344, v237\n\t"
            "v_mfma_f32_16x16x32_bf16 v[228:231], v[72:75], v[144:147], v[228:231]\n\t"
            "v_sub_u32 v236, v236, v237\n\t"
            "v_bfe_i32 v237, v249, 30, 1\n\t"
            "v_mfma_f32_16x16x32_bf16 v[228:231], v[76:79], v[148:151], v[228:231]\n\t"
            "v_and_b32 v237, 2304, v237\n\t"
            "v_sub_u32 v236, v236, v237\n\t"
            "v_mfma_f32_16x16x32_bf16 v[228:231], v[80:83], v[152:155], v[228:231]\n\t"
            "v_cvt_pk_bf16_f32 v224, v224, v225\n\t"
            "v_cvt_pk_bf16_f32 v225, v226, v227\n\t"
            "v_mfma_f32_16x16x32_bf16 v[228:231], v[84:87], v[156:159], v[228:231]\n\t"
            "v_and_b32 v249, 0x7ffffff, v249\n\t"
            "v_add_u32 v249, v249, v236\n\t"
            "v_mfma_f32_16x16x32_bf16 v[228:231], v[88:91], v[160:163], v[228:231]\n\t"
            "v_mfma_f32_16x16x32_bf16 v[228:231], v[92:95], v[164:167], v[228:231]\n\t"
            "s_nop 7\n\t"
            "v_cvt_pk_bf16_f32 v226, v228, v229\n\t"
            "v_cvt_pk_bf16_f32 v227, v230, v231\n\t"
            "buffer_store_dwordx4 v[224:227], v248, s[40:43], 0 offen offset:64 sc0 sc1\n\t"
            "s_nop 7\n\t"
            "v_mov_b32 v168, s23\n\t"
            ".Ldv%=:\n\t"
            "ds_read_b128 v[172:175], v168 offset:96\n\t"
            "s_waitcnt lgkmcnt(0)\n\t"
            "v_or_b32 v172, v172, v173\n\t"
            "v_or3_b32 v172, v172, v174, v175\n\t"
            "v_cmp_gt_i32 vcc, 0, v172\n\t"
            "s_nop 4\n\t"
            "s_cbranch_vccnz .Ldv%=\n\t"
            "s_nop 7\n\t"
            "ds_read_b128 v[168:171], v242 offset:59904\n\t"
            "v_lshlrev_b32 v232, 16, v212\n\t"
            "v_and_b32 v233, 0xffff0000, v212\n\t"
            "v_lshlrev_b32 v234, 16, v213\n\t"
            "v_and_b32 v235, 0xffff0000, v213\n\t"
            "s_waitcnt vmcnt(32)\n\t"
            "v_lshlrev_b32 v192, 16, v214\n\t"
            "v_and_b32 v193, 0xffff0000, v214\n\t"
            "v_lshlrev_b32 v194, 16, v215\n\t"
            "v_and_b32 v195, 0xffff0000, v215\n\t"
            "v_lshlrev_b32 v196, 16, v216\n\t"
            "v_and_b32 v197, 0xffff0000, v216\n\t"
            "v_lshlrev_b32 v198, 16, v217\n\t"
            "v_and_b32 v199, 0xffff0000, v217\n\t"
            "v_lshlrev_b32 v224, 16, v218\n\t"
            "v_and_b32 v225, 0xffff0000, v218\n\t"
            "v_lshlrev_b32 v226, 16, v219\n\t"
            "v_and_b32 v227, 0xffff0000, v219\n\t"
            "v_lshlrev_b32 v228, 16, v208\n\t"
            "v_and_b32 v229, 0xffff0000, v208\n\t"
            "v_lshlrev_b32 v230, 16, v209\n\t"
            "v_and_b32 v231, 0xffff0000, v209\n\t"
            "ds_read_b128 v[172:175], v243 offset:59904\n\t"
            "ds_read_b128 v[176:179], v242 offset:60032\n\t"
            "ds_read_b128 v[180:183], v243 offset:60032\n\t"
            "ds_read_b128 v[184:187], v242 offset:60160\n\t"
            "s_waitcnt lgkmcnt(4)\n\t"
            "v_mfma_f32_16x16x32_bf16 v[232:235], v[0:3], v[168:171], v[232:235]\n\t"
            "ds_read_b128 v[188:191], v243 offset:60160\n\t"
            "s_waitcnt lgkmcnt(4)\n\t"
            "v_mfma_f32_16x16x32_bf16 v[232:235], v[4:7], v[172:175], v[232:235]\n\t"
            "s_waitcnt lgkmcnt(3)\n\t"
            "v_mfma_f32_16x16x32_bf16 v[232:235], v[8:11], v[176:179], v[232:235]\n\t"
            "s_waitcnt vmcnt(26)\n\t"
            "s_waitcnt lgkmcnt(2)\n\t"
            "v_mfma_f32_16x16x32_bf16 v[232:235], v[12:15], v[180:183], v[232:235]\n\t"
            "s_waitcnt lgkmcnt(1)\n\t"
            "v_mfma_f32_16x16x32_bf16 v[232:235], v[16:19], v[184:187], v[232:235]\n\t"
            "s_waitcnt lgkmcnt(0)\n\t"
            "v_mfma_f32_16x16x32_bf16 v[232:235], v[20:23], v[188:191], v[232:235]\n\t"
            "v_mfma_f32_16x16x32_bf16 v[192:195], v[24:27], v[168:171], v[192:195]\n\t"
            "v_mfma_f32_16x16x32_bf16 v[192:195], v[28:31], v[172:175], v[192:195]\n\t"
            "v_mfma_f32_16x16x32_bf16 v[192:195], v[32:35], v[176:179], v[192:195]\n\t"
            "s_waitcnt vmcnt(20)\n\t"
            "v_mfma_f32_16x16x32_bf16 v[192:195], v[36:39], v[180:183], v[192:195]\n\t"
            "v_mfma_f32_16x16x32_bf16 v[192:195], v[40:43], v[184:187], v[192:195]\n\t"
            "s_nop 1\n\t"
            "v_cvt_pk_bf16_f32 v232, v232, v233\n\t"
            "v_mfma_f32_16x16x32_bf16 v[192:195], v[44:47], v[188:191], v[192:195]\n\t"
            "v_cvt_pk_bf16_f32 v233, v234, v235\n\t"
            "v_mfma_f32_16x16x32_bf16 v[196:199], v[48:51], v[168:171], v[196:199]\n\t"
            "v_mfma_f32_16x16x32_bf16 v[196:199], v[52:55], v[172:175], v[196:199]\n\t"
            "v_mfma_f32_16x16x32_bf16 v[196:199], v[56:59], v[176:179], v[196:199]\n\t"
            "v_mfma_f32_16x16x32_bf16 v[196:199], v[60:63], v[180:183], v[196:199]\n\t"
            "v_mfma_f32_16x16x32_bf16 v[196:199], v[64:67], v[184:187], v[196:199]\n\t"
            "s_nop 1\n\t"
            "v_cvt_pk_bf16_f32 v234, v192, v193\n\t"
            "v_mfma_f32_16x16x32_bf16 v[196:199], v[68:71], v[188:191], v[196:199]\n\t"
            "v_cvt_pk_bf16_f32 v235, v194, v195\n\t"
            "v_lshlrev_b32 v192, 16, v220\n\t"
            "v_and_b32 v193, 0xffff0000, v220\n\t"
            "v_lshlrev_b32 v194, 16, v221\n\t"
            "v_and_b32 v195, 0xffff0000, v221\n\t"
            "buffer_store_dwordx4 v[232:235], v249, s[40:43], 0 offen sc0 sc1\n\t"
            "s_waitcnt vmcnt(15)\n\t"
            "v_mfma_f32_16x16x32_bf16 v[224:227], v[72:75], v[168:171], v[224:227]\n\t"
            "v_cvt_pk_bf16_f32 v196, v196, v197\n\t"
            "v_cvt_pk_bf16_f32 v197, v198, v199\n\t"
            "v_mfma_f32_16x16x32_bf16 v[224:227], v[76:79], v[172:175], v[224:227]\n\t"
            "v_lshlrev_b32 v232, 16, v210\n\t"
            "v_and_b32 v233, 0xffff0000, v210\n\t"
            "v_mfma_f32_16x16x32_bf16 v[224:227], v[80:83], v[176:179], v[224:227]\n\t"
            "s_waitcnt vmcnt(9)\n\t"
            "v_lshlrev_b32 v234, 16, v211\n\t"
            "v_mfma_f32_16x16x32_bf16 v[224:227], v[84:87], v[180:183], v[224:227]\n\t"
            "v_and_b32 v235, 0xffff0000, v211\n\t"
            "v_mfma_f32_16x16x32_bf16 v[224:227], v[88:91], v[184:187], v[224:227]\n\t"
            "v_mfma_f32_16x16x32_bf16 v[224:227], v[92:95], v[188:191], v[224:227]\n\t"
            "v_mfma_f32_16x16x32_bf16 v[228:231], v[96:99], v[144:147], v[228:231]\n\t"
            "v_mfma_f32_16x16x32_bf16 v[228:231], v[100:103], v[148:151], v[228:231]\n\t"
            "v_mfma_f32_16x16x32_bf16 v[228:231], v[104:107], v[152:155], v[228:231]\n\t"
            "v_mfma_f32_16x16x32_bf16 v[228:231], v[108:111], v[156:159], v[228:231]\n\t"
            "v_mfma_f32_16x16x32_bf16 v[228:231], v[112:115], v[160:163], v[228:231]\n\t"
            "v_mfma_f32_16x16x32_bf16 v[228:231], v[116:119], v[164:167], v[228:231]\n\t"
            "s_nop 1\n\t"
            "v_cvt_pk_bf16_f32 v198, v224, v225\n\t"
            "v_cvt_pk_bf16_f32 v199, v226, v227\n\t"
            "buffer_store_dwordx4 v[196:199], v249, s[40:43], 0 offen offset:64 sc0 sc1\n\t"
            "s_waitcnt vmcnt(4)\n\t"
            "v_mfma_f32_16x16x32_bf16 v[232:235], v[120:123], v[144:147], v[232:235]\n\t"
            "v_lshlrev_b32 v196, 16, v222\n\t"
            "v_and_b32 v197, 0xffff0000, v222\n\t"
            "v_mfma_f32_16x16x32_bf16 v[232:235], v[124:127], v[148:151], v[232:235]\n\t"
            "v_cvt_pk_bf16_f32 v228, v228, v229\n\t"
            "v_cvt_pk_bf16_f32 v229, v230, v231\n\t"
            "v_mfma_f32_16x16x32_bf16 v[232:235], v[128:131], v[152:155], v[232:235]\n\t"
            "s_waitcnt vmcnt(10)\n\t"
            "v_lshlrev_b32 v198, 16, v223\n\t"
            "v_mfma_f32_16x16x32_bf16 v[232:235], v[132:135], v[156:159], v[232:235]\n\t"
            "v_and_b32 v199, 0xffff0000, v223\n\t"
            "v_mfma_f32_16x16x32_bf16 v[232:235], v[136:139], v[160:163], v[232:235]\n\t"
            "v_mfma_f32_16x16x32_bf16 v[232:235], v[140:143], v[164:167], v[232:235]\n\t"
            "v_mfma_f32_16x16x32_bf16 v[192:195], v[96:99], v[168:171], v[192:195]\n\t"
            "v_mfma_f32_16x16x32_bf16 v[192:195], v[100:103], v[172:175], v[192:195]\n\t"
            "v_mfma_f32_16x16x32_bf16 v[192:195], v[104:107], v[176:179], v[192:195]\n\t"
            "v_mfma_f32_16x16x32_bf16 v[192:195], v[108:111], v[180:183], v[192:195]\n\t"
            "v_mfma_f32_16x16x32_bf16 v[192:195], v[112:115], v[184:187], v[192:195]\n\t"
            "v_mfma_f32_16x16x32_bf16 v[192:195], v[116:119], v[188:191], v[192:195]\n\t"
            "s_nop 1\n\t"
            "v_cvt_pk_bf16_f32 v230, v232, v233\n\t"
            "v_cvt_pk_bf16_f32 v231, v234, v235\n\t"
            "buffer_store_dwordx4 v[228:231], v248, s[40:43], 0 offen offset:128 sc0 sc1\n\t"
            "s_waitcnt vmcnt(5)\n\t"
            "v_mfma_f32_16x16x32_bf16 v[196:199], v[120:123], v[168:171], v[196:199]\n\t"
            "v_mfma_f32_16x16x32_bf16 v[196:199], v[124:127], v[172:175], v[196:199]\n\t"
            "v_cvt_pk_bf16_f32 v192, v192, v193\n\t"
            "v_mfma_f32_16x16x32_bf16 v[196:199], v[128:131], v[176:179], v[196:199]\n\t"
            "v_cvt_pk_bf16_f32 v193, v194, v195\n\t"
            "v_mfma_f32_16x16x32_bf16 v[196:199], v[132:135], v[180:183], v[196:199]\n\t"
            "v_mfma_f32_16x16x32_bf16 v[196:199], v[136:139], v[184:187], v[196:199]\n\t"
            "v_mfma_f32_16x16x32_bf16 v[196:199], v[140:143], v[188:191], v[196:199]\n\t"
            "s_nop 7\n\t"
            "v_cvt_pk_bf16_f32 v194, v196, v197\n\t"
            "v_cvt_pk_bf16_f32 v195, v198, v199\n\t"
            "buffer_store_dwordx4 v[192:195], v249, s[40:43], 0 offen offset:128 sc0 sc1\n\t"
            "s_nop 7\n\t"
            "s_branch .Lre%=\n\t"
            ".Lrp%=:\n\t"
            "v_add_u32 v236, s19, v252\n\t"
            "ds_write_b128 v236, v[244:247]\n\t"
            "v_add_u32 v236, s48, v244\n\t"
            "v_add_u32 v237, s48, v245\n\t"
            "v_add_u32 v248, s48, v246\n\t"
            "ds_read_b128 v[200:203], v236\n\t"
            "ds_read_b128 v[204:207], v237\n\t"
            "ds_read_b128 v[208:211], v248\n\t"
            "ds_read_b128 v[212:215], v236 offset:6144\n\t"
            "ds_read_b128 v[216:219], v237 offset:6144\n\t"
            "ds_read_b128 v[220:223], v248 offset:6144\n\t"
            "ds_read_b128 v[224:227], v236 offset:12288\n\t"
            "ds_read_b128 v[228:231], v237 offset:12288\n\t"
            "ds_read_b128 v[232:235], v248 offset:12288\n\t"
            "s_waitcnt lgkmcnt(0)\n\t"
            "s_barrier\n\t"
            "s_add_u32 s45, s46, s27\n\t"
            "s_mov_b32 s46, s47\n\t"
            "s_mov_b32 s47, s68\n\t"
            "s_xor_b32 s48, s48, s71\n\t"
            "s_xor_b32 s67, s67, s71\n\t"
            "s_add_u32 s76, s76, 4\n\t"
            "s_lshl_b32 s56, s56, 2\n\t"
            "s_lshr_b32 s49, s26, 30\n\t"
            "s_or_b32 s56, s56, s49\n\t"
            "s_add_u32 s49, s33, 0\n\t"
            "buffer_load_dwordx4 v[0:3], v247, s[28:31], s49 offen sc1\n\t"
            "s_add_u32 s49, s33, 1024\n\t"
            "buffer_load_dwordx4 v[4:7], v247, s[28:31], s49 offen sc1\n\t"
            "s_add_u32 s49, s33, 2048\n\t"
            "buffer_load_dwordx4 v[8:11], v247, s[28:31], s49 offen sc1\n\t"
            "s_add_u32 s49, s33, 3072\n\t"
            "buffer_load_dwordx4 v[12:15], v247, s[28:31], s49 offen sc1\n\t"
            "s_add_u32 s49, s33, 4096\n\t"
            "buffer_load_dwordx4 v[16:19], v247, s[28:31], s49 offen sc1\n\t"
            "s_add_u32 s49, s33, 5120\n\t"
            "buffer_load_dwordx4 v[20:23], v247, s[28:31], s49 offen sc1\n\t"
            "s_add_u32 s49, s33, 6144\n\t"
            "buffer_load_dwordx4 v[24:27], v247, s[28:31], s49 offen sc1\n\t"
            "s_add_u32 s49, s33, 7168\n\t"
            "buffer_load_dwordx4 v[28:31], v247, s[28:31], s49 offen sc1\n\t"
            "s_add_u32 s49, s33, 8192\n\t"
            "buffer_load_dwordx4 v[32:35], v247, s[28:31], s49 offen sc1\n\t"
            "s_add_u32 s49, s33, 9216\n\t"
            "buffer_load_dwordx4 v[36:39], v247, s[28:31], s49 offen sc1\n\t"
            "s_add_u32 s49, s33, 10240\n\t"
            "buffer_load_dwordx4 v[40:43], v247, s[28:31], s49 offen sc1\n\t"
            "s_add_u32 s49, s33, 11264\n\t"
            "buffer_load_dwordx4 v[44:47], v247, s[28:31], s49 offen sc1\n\t"
            "s_add_u32 s49, s33, 12288\n\t"
            "buffer_load_dwordx4 v[48:51], v247, s[28:31], s49 offen sc1\n\t"
            "s_add_u32 s49, s33, 13312\n\t"
            "buffer_load_dwordx4 v[52:55], v247, s[28:31], s49 offen sc1\n\t"
            "s_add_u32 s49, s33, 14336\n\t"
            "buffer_load_dwordx4 v[56:59], v247, s[28:31], s49 offen sc1\n\t"
            "s_add_u32 s49, s33, 15360\n\t"
            "buffer_load_dwordx4 v[60:63], v247, s[28:31], s49 offen sc1\n\t"
            "s_add_u32 s49, s33, 16384\n\t"
            "buffer_load_dwordx4 v[64:67], v247, s[28:31], s49 offen sc1\n\t"
            "s_add_u32 s49, s33, 17408\n\t"
            "buffer_load_dwordx4 v[68:71], v247, s[28:31], s49 offen sc1\n\t"
            "v_mov_b32 v249, s76\n\t"
            "ds_read_b32 v249, v249\n\t"
            "s_waitcnt lgkmcnt(0)\n\t"
            "v_readfirstlane_b32 s44, v249\n\t"
            "s_and_b32 s70, s44, 0x7ffffff\n\t"
            "s_add_u32 s70, s70, s77\n\t"
            "s_and_b32 s44, s44, 0x08000000\n\t"
            "s_or_b32 s44, s44, s39\n\t"
            "ds_read_b32 v249, v252 offset:1028\n\t"
            "s_waitcnt lgkmcnt(0)\n\t"
            "v_and_b32 v249, s44, v249\n\t"
            "s_mov_b32 m0, s32\n\t"
            "s_add_u32 s49, s70, s25\n\t"
            "buffer_load_dword v249, s[72:75], s70 offen lds\n\t"
            "buffer_load_dword v249, s[72:75], s49 offen lds\n\t"
            "ds_read_b128 v[168:171], v254\n\t"
            "ds_read_b128 v[172:175], v254 offset:64\n\t"
            "s_waitcnt vmcnt(2)\n\t"
            "ds_read_b64 v[144:145], v253\n\t"
            "ds_read_b64 v[146:147], v253 offset:96\n\t"
            "ds_read_b64 v[148:149], v253 offset:1744\n\t"
            "ds_read_b64 v[150:151], v253 offset:1840\n\t"
            "ds_read_b64 v[152:153], v253 offset:8\n\t"
            "ds_read_b64 v[154:155], v253 offset:104\n\t"
            "ds_read_b64 v[156:157], v253 offset:1752\n\t"
            "ds_read_b64 v[158:159], v253 offset:1848\n\t"
            "ds_read_b64 v[160:161], v253 offset:16\n\t"
            "ds_read_b64 v[162:163], v253 offset:112\n\t"
            "ds_read_b64 v[164:165], v253 offset:1760\n\t"
            "ds_read_b64 v[166:167], v253 offset:1856\n\t"
            "s_waitcnt lgkmcnt(0)\n\t"
            "s_nop 7\n\t"
            ".p2align 6\n\t"
            ".Lwb_loop%=:\n\t"
            "s_setprio 0\n\t"
            "s_bfe_u32 s92, s56, 0x20004\n\t"
            "s_lshl_b32 s93, s92, 3\n\t"
            "s_lshr_b32 s93, s55, s93\n\t"
            "s_lshl_b64 s[94:95], s[34:35], s93\n\t"
            "s_bitcmp1_b32 s92, 1\n\t"
            "s_cselect_b32 s93, 1177344, 0\n\t"
            "s_bitcmp1_b32 s92, 0\n\t"
            "s_cselect_b32 s92, 2304, 0\n\t"
            "s_add_u32 s92, s92, s93\n\t"
            "v_mov_b64 v[176:177], 0\n\t"
            "v_mov_b64 v[178:179], 0\n\t"
            "v_mov_b64 v[180:181], 0\n\t"
            "v_mov_b64 v[182:183], 0\n\t"
            "v_mov_b32 v249, s76\n\t"
            "v_mov_b64 v[186:187], 0\n\t"
            "s_waitcnt lgkmcnt(0)\n\t"
            "v_smfmac_f32_16x16x64_bf16 v[176:179], v[168:171], v[144:151], v255\n\t"
            "v_smfmac_f32_16x16x64_bf16 v[180:183], v[172:175], v[152:159], v255\n\t"
            "ds_read_b64 v[148:149], v253 offset:1768\n\t"
            "v_mov_b64 v[190:191], 0\n\t"
            "v_mov_b64 v[188:189], 0\n\t"
            "v_mov_b64 v[184:185], 0\n\t"
            "v_add_u32 v236, s67, v250\n\t"
            "v_add_u32 v248, s67, v251\n\t"
            "ds_read_b64 v[150:151], v253 offset:1864\n\t"
            "ds_read_b128 v[168:171], v254 offset:128\n\t"
            "v_cvt_pk_bf16_f32 v176, v176, v177\n\t"
            "v_cvt_pk_bf16_f32 v177, v178, v179\n\t"
            "v_cvt_pk_bf16_f32 v178, v180, v181\n\t"
            "v_cvt_pk_bf16_f32 v179, v182, v183\n\t"
            "v_mov_b64 v[180:181], 0\n\t"
            "v_mov_b64 v[182:183], 0\n\t"
            "ds_read_b64 v[146:147], v253 offset:120\n\t"
            "ds_read_b64 v[144:145], v253 offset:24\n\t"
            "ds_read_b128 v[172:175], v254 offset:192\n\t"
            "ds_read_b32 v249, v249\n\t"
            "ds_read_b64 v[158:159], v253 offset:1872\n\t"
            "ds_read_b64 v[156:157], v253 offset:1776\n\t"
            "ds_read_b64 v[152:153], v253 offset:32\n\t"
            "ds_read_b64 v[154:155], v253 offset:128\n\t"
            "s_waitcnt lgkmcnt(5)\n\t"
            "v_smfmac_f32_16x16x64_bf16 v[184:187], v[168:171], v[160:167], v255\n\t"
            "v_smfmac_f32_16x16x64_bf16 v[188:191], v[172:175], v[144:151], v255\n\t"
            "s_waitcnt lgkmcnt(4)\n\t"
            "ds_read_b128 v[168:171], v254 offset:256\n\t"
            "v_readfirstlane_b32 s26, v249\n\t"
            "v_mfma_f32_4x4x4_16b_bf16 v[196:199], v[176:177], v[176:177], 0\n\t"
            "v_mfma_f32_4x4x4_16b_bf16 v[192:195], v[238:239], v[176:177], 0\n\t"
            "s_nop 0\n\t"
            "v_mfma_f32_4x4x4_16b_bf16 v[196:199], v[178:179], v[178:179], v[196:199]\n\t"
            "v_mfma_f32_4x4x4_16b_bf16 v[192:195], v[238:239], v[178:179], v[192:195]\n\t"
            "ds_read_b64 v[160:161], v253 offset:40\n\t"
            "s_and_b32 s68, s26, 0x7ffffff\n\t"
            "s_add_u32 s68, s68, s78\n\t"
            "s_and_b32 s69, s26, 0x38000000\n\t"
            "s_or_b32 s69, s69, s39\n\t"
            "s_bitcmp1_b32 s26, 27\n\t"
            "s_cselect_b32 s79, 2.0, s68\n\t"
            "ds_read_b64 v[164:165], v253 offset:1784\n\t"
            "s_and_b32 s57, s56, 3\n\t"
            "s_lshl_b32 s58, s57, 3\n\t"
            "s_lshr_b32 m0, s54, s58\n\t"
            "ds_read_b64 v[166:167], v253 offset:1880\n\t"
            "s_lshl_b32 s58, s57, 4\n\t"
            "s_lshr_b64 s[36:37], s[50:51], s58\n\t"
            "s_and_b32 s57, s36, 0xffff\n\t"
            "s_add_u32 s57, s57, s67\n\t"
            "ds_read_b128 v[172:175], v254 offset:320\n\t"
            "s_lshr_b64 s[36:37], s[52:53], s58\n\t"
            "s_and_b32 s58, s36, 0xffff\n\t"
            "s_add_u32 s58, s58, s67\n\t"
            "ds_read_b64 v[162:163], v253 offset:136\n\t"
            "s_and_b32 s59, s50, 0xffff\n\t"
            "s_add_u32 s59, s59, s67\n\t"
            "s_and_b32 s38, s52, 0xffff\n\t"
            "s_add_u32 s38, s38, s67\n\t"
            "ds_read_b64 v[144:145], v253 offset:48\n\t"
            "v_writelane_b32 v236, s59, m0\n\t"
            "v_writelane_b32 v236, s57, 63\n\t"
            "v_cvt_pk_bf16_f32 v184, v184, v185\n\t"
            "v_cvt_pk_bf16_f32 v185, v186, v187\n\t"
            "v_cvt_pk_bf16_f32 v187, v190, v191\n\t"
            "v_cvt_pk_bf16_f32 v186, v188, v189\n\t"
            "v_xor_b32 v237, 16, v236\n\t"
            "v_mov_b64 v[188:189], 0\n\t"
            "v_mov_b64 v[190:191], 0\n\t"
            "v_writelane_b32 v248, s38, m0\n\t"
            "v_writelane_b32 v248, s58, 63\n\t"
            "ds_read_b64 v[148:149], v253 offset:1792\n\t"
            "ds_read_b64 v[146:147], v253 offset:144\n\t"
            "ds_read_b64 v[150:151], v253 offset:1888\n\t"
            "ds_read_b32 v249, v252 offset:1024\n\t"
            "s_waitcnt lgkmcnt(14)\n\t"
            "ds_write_b128 v236, v[176:179]\n\t"
            "v_mov_b64 v[176:177], 0\n\t"
            "v_mov_b64 v[178:179], 0\n\t"
            "s_waitcnt lgkmcnt(6)\n\t"
            "s_nop 0\n\t"
            "v_smfmac_f32_16x16x64_bf16 v[176:179], v[168:171], v[152:159], v255\n\t"
            "v_smfmac_f32_16x16x64_bf16 v[180:183], v[172:175], v[160:167], v255\n\t"
            "ds_read_b64 v[158:159], v253 offset:1896\n\t"
            "ds_read_b64 v[154:155], v253 offset:152\n\t"
            "v_mfma_f32_4x4x4_16b_bf16 v[196:199], v[184:185], v[184:185], v[196:199]\n\t"
            "v_mfma_f32_4x4x4_16b_bf16 v[192:195], v[238:239], v[184:185], v[192:195]\n\t"
            "s_nop 0\n\t"
            "v_mfma_f32_4x4x4_16b_bf16 v[196:199], v[186:187], v[186:187], v[196:199]\n\t"
            "v_mfma_f32_4x4x4_16b_bf16 v[192:195], v[238:239], v[186:187], v[192:195]\n\t"
            "ds_read_b128 v[172:175], v254 offset:448\n\t"
            "v_cvt_pk_bf16_f32 v176, v176, v177\n\t"
            "v_cvt_pk_bf16_f32 v177, v178, v179\n\t"
            "v_cvt_pk_bf16_f32 v178, v180, v181\n\t"
            "v_cvt_pk_bf16_f32 v179, v182, v183\n\t"
            "v_mov_b64 v[180:181], 0\n\t"
            "v_mov_b64 v[182:183], 0\n\t"
            "s_barrier\n\t"
            "s_setprio 3\n\t"
            "s_waitcnt lgkmcnt(4)\n\t"
            "v_and_b32 v249, s69, v249\n\t"
            "ds_read_b128 v[168:171], v254 offset:384\n\t"
            "ds_read_b64 v[156:157], v253 offset:1800\n\t"
            "ds_read_b64 v[152:153], v253 offset:56\n\t"
            "ds_read_b64 v[164:165], v253 offset:1808\n\t"
            "ds_read_b64 v[166:167], v253 offset:1904\n\t"
            "ds_read_b64 v[162:163], v253 offset:160\n\t"
            "ds_read_b64 v[160:161], v253 offset:64\n\t"
            "ds_write_b128 v237, v[184:187]\n\t"
            "v_mov_b64 v[184:185], 0\n\t"
            "v_mov_b64 v[186:187], 0\n\t"
            "ds_write_b128 v236, v[176:179] offset:128\n\t"
            "s_waitcnt lgkmcnt(6)\n\t"
            "v_smfmac_f32_16x16x64_bf16 v[184:187], v[168:171], v[144:151], v255\n\t"
            "v_smfmac_f32_16x16x64_bf16 v[188:191], v[172:175], v[152:159], v255\n\t"
            "ds_read_b64 v[144:145], v253 offset:72\n\t"
            "ds_read_b64 v[148:149], v253 offset:1816\n\t"
            "v_mfma_f32_4x4x4_16b_bf16 v[196:199], v[176:177], v[176:177], v[196:199]\n\t"
            "v_mfma_f32_4x4x4_16b_bf16 v[192:195], v[238:239], v[176:177], v[192:195]\n\t"
            "s_nop 0\n\t"
            "v_mfma_f32_4x4x4_16b_bf16 v[196:199], v[178:179], v[178:179], v[196:199]\n\t"
            "v_mfma_f32_4x4x4_16b_bf16 v[192:195], v[238:239], v[178:179], v[192:195]\n\t"
            "ds_read_b64 v[150:151], v253 offset:1912\n\t"
            "v_mov_b64 v[176:177], 0\n\t"
            "v_mov_b64 v[178:179], 0\n\t"
            "v_cvt_pk_bf16_f32 v184, v184, v185\n\t"
            "v_cvt_pk_bf16_f32 v185, v186, v187\n\t"
            "v_cvt_pk_bf16_f32 v186, v188, v189\n\t"
            "v_cvt_pk_bf16_f32 v187, v190, v191\n\t"
            "v_mov_b64 v[190:191], 0\n\t"
            "v_mov_b64 v[188:189], 0\n\t"
            "ds_read_b128 v[168:171], v254 offset:512\n\t"
            "ds_read_b128 v[172:175], v254 offset:576\n\t"
            "ds_read_b64 v[146:147], v253 offset:168\n\t"
            "ds_read_b64 v[154:155], v253 offset:176\n\t"
            "ds_read_b64 v[152:153], v253 offset:80\n\t"
            "ds_read_b64 v[156:157], v253 offset:1824\n\t"
            "s_waitcnt lgkmcnt(14)\n\t"
            "ds_read_b64 v[158:159], v253 offset:1920\n\t"
            "s_waitcnt lgkmcnt(14)\n\t"
            "ds_write_b128 v237, v[184:187] offset:128\n\t"
            "s_waitcnt lgkmcnt(5)\n\t"
            "v_smfmac_f32_16x16x64_bf16 v[176:179], v[168:171], v[160:167], v255\n\t"
            "v_smfmac_f32_16x16x64_bf16 v[180:183], v[172:175], v[144:151], v255\n\t"
            "ds_read_b64 v[166:167], v253 offset:1928\n\t"
            "ds_read_b64 v[160:161], v253 offset:88\n\t"
            "v_mfma_f32_4x4x4_16b_bf16 v[196:199], v[184:185], v[184:185], v[196:199]\n\t"
            "v_mfma_f32_4x4x4_16b_bf16 v[192:195], v[238:239], v[184:185], v[192:195]\n\t"
            "s_nop 0\n\t"
            "v_mfma_f32_4x4x4_16b_bf16 v[196:199], v[186:187], v[186:187], v[196:199]\n\t"
            "v_mfma_f32_4x4x4_16b_bf16 v[192:195], v[238:239], v[186:187], v[192:195]\n\t"
            "ds_read_b128 v[172:175], v254 offset:704\n\t"
            "v_mov_b64 v[186:187], 0\n\t"
            "v_mov_b64 v[184:185], 0\n\t"
            "v_cvt_pk_bf16_f32 v176, v176, v177\n\t"
            "v_cvt_pk_bf16_f32 v177, v178, v179\n\t"
            "v_cvt_pk_bf16_f32 v178, v180, v181\n\t"
            "v_cvt_pk_bf16_f32 v179, v182, v183\n\t"
            "v_and_b32 v183, 0xffff0000, v203\n\t"
            "v_and_b32 v181, 0xffff0000, v202\n\t"
            "v_lshlrev_b32 v182, 16, v203\n\t"
            "v_lshlrev_b32 v180, 16, v202\n\t"
            "ds_read_b64 v[164:165], v253 offset:1832\n\t"
            "ds_read_b128 v[168:171], v254 offset:640\n\t"
            "ds_read_b64 v[162:163], v253 offset:184\n\t"
            "ds_read_b128 v[144:147], v242\n\t"
            "ds_read_b128 v[148:151], v243\n\t"
            "ds_write_b128 v236, v[176:179] offset:256\n\t"
            "v_add_u32 v236, s48, v244\n\t"
            "s_waitcnt lgkmcnt(3)\n\t"
            "v_smfmac_f32_16x16x64_bf16 v[184:187], v[168:171], v[152:159], v255\n\t"
            "v_smfmac_f32_16x16x64_bf16 v[188:191], v[172:175], v[160:167], v255\n\t"
            "ds_read_b128 v[152:155], v242 offset:128\n\t"
            "ds_read_b128 v[160:163], v242 offset:256\n\t"
            "v_mfma_f32_4x4x4_16b_bf16 v[196:199], v[176:177], v[176:177], v[196:199]\n\t"
            "v_mfma_f32_4x4x4_16b_bf16 v[192:195], v[238:239], v[176:177], v[192:195]\n\t"
            "s_nop 0\n\t"
            "v_mfma_f32_4x4x4_16b_bf16 v[196:199], v[178:179], v[178:179], v[196:199]\n\t"
            "v_mfma_f32_4x4x4_16b_bf16 v[192:195], v[238:239], v[178:179], v[192:195]\n\t"
            "ds_read_b128 v[156:159], v243 offset:128\n\t"
            "v_cvt_pk_bf16_f32 v184, v184, v185\n\t"
            "v_lshlrev_b32 v176, 16, v200\n\t"
            "v_cvt_pk_bf16_f32 v185, v186, v187\n\t"
            "v_cvt_pk_bf16_f32 v187, v190, v191\n\t"
            "v_cvt_pk_bf16_f32 v186, v188, v189\n\t"
            "v_lshlrev_b32 v178, 16, v201\n\t"
            "v_and_b32 v179, 0xffff0000, v201\n\t"
            "v_mfma_f32_4x4x4_16b_bf16 v[196:199], v[184:185], v[184:185], v[196:199]\n\t"
            "v_mfma_f32_4x4x4_16b_bf16 v[192:195], v[238:239], v[184:185], v[192:195]\n\t"
            "s_nop 0\n\t"
            "v_mfma_f32_4x4x4_16b_bf16 v[196:199], v[186:187], v[186:187], v[196:199]\n\t"
            "v_mfma_f32_4x4x4_16b_bf16 v[192:195], v[238:239], v[186:187], v[192:195]\n\t"
            "ds_read_b128 v[164:167], v243 offset:256\n\t"
            "v_and_b32 v177, 0xffff0000, v200\n\t"
            "v_lshlrev_b32 v190, 16, v207\n\t"
            "v_and_b32 v191, 0xffff0000, v207\n\t"
            "s_waitcnt lgkmcnt(6)\n\t"
            "v_mfma_f32_16x16x32_bf16 v[176:179], v[0:3], v[144:147], v[176:179]\n\t"
            "s_setprio 3\n\t"
            "ds_read_b128 v[168:171], v242 offset:6144\n\t"
            "v_and_b32 v189, 0xffff0000, v206\n\t"
            "s_waitcnt lgkmcnt(6)\n\t"
            "v_mfma_f32_16x16x32_bf16 v[176:179], v[4:7], v[148:151], v[176:179]\n\t"
            "s_waitcnt lgkmcnt(4)\n\t"
            "ds_read_b128 v[172:175], v243 offset:6144\n\t"
            "v_lshlrev_b32 v188, 16, v206\n\t"
            "v_mfma_f32_16x16x32_bf16 v[176:179], v[8:11], v[152:155], v[176:179]\n\t"
            "s_mov_b32 exec_hi, 0x3fffff\n\t"
            "s_add_u32 m0, s66, 0\n\t"
            "s_add_u32 s49, s79, 0\n\t"
            "buffer_load_dwordx4 v249, s[72:75], s49 offen lds\n\t"
            "s_mov_b32 exec_hi, -1\n\t"
            "s_mov_b32 exec_hi, 0x3fffff\n\t"
            "s_add_u32 m0, s66, 872\n\t"
            "s_add_u32 s49, s68, 196224\n\t"
            "buffer_load_dwordx4 v249, s[72:75], s49 offen lds\n\t"
            "s_mov_b32 exec_hi, -1\n\t"
            "ds_write_b128 v237, v[184:187] offset:256\n\t"
            "s_waitcnt lgkmcnt(4)\n\t"
            "v_mfma_f32_16x16x32_bf16 v[176:179], v[12:15], v[156:159], v[176:179]\n\t"
            "ds_read_b128 v[200:203], v236\n\t"
            "v_and_b32 v185, 0xffff0000, v204\n\t"
            "v_mfma_f32_16x16x32_bf16 v[176:179], v[16:19], v[160:163], v[176:179]\n\t"
            "s_waitcnt lgkmcnt(4)\n\t"
            "v_lshlrev_b32 v184, 16, v204\n\t"
            "v_lshlrev_b32 v186, 16, v205\n\t"
            "v_mfma_f32_16x16x32_bf16 v[176:179], v[20:23], v[164:167], v[176:179]\n\t"
            "s_mov_b32 exec_hi, 0x3fffff\n\t"
            "s_add_u32 m0, s66, 1744\n\t"
            "s_add_u32 s49, s68, 392448\n\t"
            "buffer_load_dwordx4 v249, s[72:75], s49 offen lds\n\t"
            "s_mov_b32 exec_hi, -1\n\t"
            "s_mov_b32 exec_hi, 0x3fffff\n\t"
            "s_add_u32 m0, s66, 2616\n\t"
            "s_add_u32 s49, s68, 588672\n\t"
            "buffer_load_dwordx4 v249, s[72:75], s49 offen lds\n\t"
            "s_mov_b32 exec_hi, -1\n\t"
            "v_mfma_f32_16x16x32_bf16 v[180:183], v[24:27], v[144:147], v[180:183]\n\t"
            "v_and_b32 v187, 0xffff0000, v205\n\t"
            "v_cndmask_b32_e64 v193, v196, v197, s[60:61]\n\t"
            "v_mfma_f32_16x16x32_bf16 v[180:183], v[28:31], v[148:151], v[180:183]\n\t"
            "v_cndmask_b32_e64 v193, v193, v198, s[62:63]\n\t"
            "v_cndmask_b32_e64 v193, v193, v199, s[64:65]\n\t"
            "v_mfma_f32_16x16x32_bf16 v[180:183], v[32:35], v[152:155], v[180:183]\n\t"
            "ds_read_b128 v[196:199], v252\n\t"
            "v_lshlrev_b32 v194, 16, v209\n\t"
            "v_mfma_f32_16x16x32_bf16 v[180:183], v[36:39], v[156:159], v[180:183]\n\t"
            "v_and_b32 v195, 0xffff0000, v209\n\t"
            "v_cvt_pk_bf16_f32 v176, v176, v177\n\t"
            "v_mfma_f32_16x16x32_bf16 v[180:183], v[40:43], v[160:163], v[180:183]\n\t"
            "v_cvt_pk_bf16_f32 v177, v178, v179\n\t"
            "v_add_u32 v237, s48, v245\n\t"
            "v_mfma_f32_16x16x32_bf16 v[180:183], v[44:47], v[164:167], v[180:183]\n\t"
            "s_mov_b32 exec_hi, 0x3fffff\n\t"
            "s_add_u32 m0, s66, 3488\n\t"
            "s_add_u32 s49, s68, 784896\n\t"
            "buffer_load_dwordx4 v249, s[72:75], s49 offen lds\n\t"
            "s_mov_b32 exec_hi, -1\n\t"
            "s_mov_b32 exec_hi, 0x3fffff\n\t"
            "s_add_u32 m0, s66, 4360\n\t"
            "s_add_u32 s49, s68, 981120\n\t"
            "buffer_load_dwordx4 v249, s[72:75], s49 offen lds\n\t"
            "s_mov_b32 exec_hi, -1\n\t"
            "ds_write_b64 v248, v[192:193] offset:18816\n\t"
            "v_mfma_f32_16x16x32_bf16 v[184:187], v[48:51], v[144:147], v[184:187]\n\t"
            "ds_read_b128 v[204:207], v237\n\t"
            "v_lshlrev_b32 v192, 16, v208\n\t"
            "v_mfma_f32_16x16x32_bf16 v[184:187], v[52:55], v[148:151], v[184:187]\n\t"
            "v_and_b32 v193, 0xffff0000, v208\n\t"
            "v_cvt_pk_bf16_f32 v179, v182, v183\n\t"
            "v_mfma_f32_16x16x32_bf16 v[184:187], v[56:59], v[152:155], v[184:187]\n\t"
            "v_cvt_pk_bf16_f32 v178, v180, v181\n\t"
            "v_and_b32 v183, 0xffff0000, v213\n\t"
            "v_mfma_f32_16x16x32_bf16 v[184:187], v[60:63], v[156:159], v[184:187]\n\t"
            "v_and_b32 v181, 0xffff0000, v212\n\t"
            "v_lshlrev_b32 v182, 16, v213\n\t"
            "v_mfma_f32_16x16x32_bf16 v[184:187], v[64:67], v[160:163], v[184:187]\n\t"
            "v_lshlrev_b32 v180, 16, v212\n\t"
            "v_add_u32 v248, s48, v246\n\t"
            "v_mfma_f32_16x16x32_bf16 v[184:187], v[68:71], v[164:167], v[184:187]\n\t"
            "s_mov_b32 exec_hi, 0x3fffff\n\t"
            "s_add_u32 m0, s66, 5232\n\t"
            "s_add_u32 s49, s68, 1177344\n\t"
            "buffer_load_dwordx4 v249, s[72:75], s49 offen lds\n\t"
            "s_mov_b32 exec_hi, -1\n\t"
            "s_mov_b32 exec_hi, 0x3fffff\n\t"
            "s_add_u32 m0, s66, 6104\n\t"
            "s_add_u32 s49, s68, 1373568\n\t"
            "buffer_load_dwordx4 v249, s[72:75], s49 offen lds\n\t"
            "s_mov_b32 exec_hi, -1\n\t"
            "v_mfma_f32_16x16x32_bf16 v[188:191], v[72:75], v[144:147], v[188:191]\n\t"
            "v_cvt_pk_bf16_f32 v184, v184, v185\n\t"
            "v_cvt_pk_bf16_f32 v185, v186, v187\n\t"
            "v_mfma_f32_16x16x32_bf16 v[188:191], v[76:79], v[148:151], v[188:191]\n\t"
            "v_mfma_f32_16x16x32_bf16 v[188:191], v[80:83], v[152:155], v[188:191]\n\t"
            "v_mfma_f32_16x16x32_bf16 v[188:191], v[84:87], v[156:159], v[188:191]\n\t"
            "v_mfma_f32_16x16x32_bf16 v[188:191], v[88:91], v[160:163], v[188:191]\n\t"
            "v_mfma_f32_16x16x32_bf16 v[188:191], v[92:95], v[164:167], v[188:191]\n\t"
            "s_mov_b32 exec_hi, 0x3fffff\n\t"
            "s_add_u32 m0, s66, 6976\n\t"
            "s_add_u32 s49, s68, 1569792\n\t"
            "buffer_load_dwordx4 v249, s[72:75], s49 offen lds\n\t"
            "s_mov_b32 exec_hi, -1\n\t"
            "v_mfma_f32_16x16x32_bf16 v[192:195], v[96:99], v[144:147], v[192:195]\n\t"
            "v_mov_b32 v249, s76\n\t"
            "v_mfma_f32_16x16x32_bf16 v[192:195], v[100:103], v[148:151], v[192:195]\n\t"
            "ds_read_b32 v249, v249 offset:4\n\t"
            "v_cvt_pk_bf16_f32 v186, v188, v189\n\t"
            "v_mfma_f32_16x16x32_bf16 v[192:195], v[104:107], v[152:155], v[192:195]\n\t"
            "v_cvt_pk_bf16_f32 v187, v190, v191\n\t"
            "v_and_b32 v191, 0xffff0000, v217\n\t"
            "v_mfma_f32_16x16x32_bf16 v[192:195], v[108:111], v[156:159], v[192:195]\n\t"
            "v_and_b32 v189, 0xffff0000, v216\n\t"
            "v_lshlrev_b32 v190, 16, v217\n\t"
            "v_mfma_f32_16x16x32_bf16 v[192:195], v[112:115], v[160:163], v[192:195]\n\t"
            "v_lshlrev_b32 v188, 16, v216\n\t"
            "v_mfma_f32_16x16x32_bf16 v[192:195], v[116:119], v[164:167], v[192:195]\n\t"
            "s_waitcnt lgkmcnt(3)\n\t"
            "s_mov_b64 exec, s[94:95]\n\t"
            "v_add_u32 v196, s92, v196\n\t"
            "s_mov_b64 exec, -1\n\t"
            "buffer_store_dwordx4 v[176:179], v196, s[40:43], s45 offen sc0 sc1\n\t"
            "buffer_store_dwordx4 v[184:187], v196, s[40:43], s45 offen offset:64 sc0 sc1\n\t"
            "s_waitcnt lgkmcnt(0)\n\t"
            "v_readfirstlane_b32 s44, v249\n\t"
            "v_and_b32 v179, 0xffff0000, v211\n\t"
            "v_lshlrev_b32 v176, 16, v210\n\t"
            "v_lshlrev_b32 v178, 16, v211\n\t"
            "v_and_b32 v177, 0xffff0000, v210\n\t"
            "v_and_b32 v185, 0xffff0000, v214\n\t"
            "v_and_b32 v187, 0xffff0000, v215\n\t"
            "v_mfma_f32_16x16x32_bf16 v[176:179], v[120:123], v[144:147], v[176:179]\n\t"
            "ds_read_b128 v[144:147], v242 offset:6272\n\t"
            "v_lshlrev_b32 v184, 16, v214\n\t"
            "v_mfma_f32_16x16x32_bf16 v[176:179], v[124:127], v[148:151], v[176:179]\n\t"
            "ds_read_b128 v[148:151], v243 offset:6272\n\t"
            "v_lshlrev_b32 v186, 16, v215\n\t"
            "v_mfma_f32_16x16x32_bf16 v[176:179], v[128:131], v[152:155], v[176:179]\n\t"
            "ds_read_b128 v[152:155], v242 offset:6400\n\t"
            "v_cvt_pk_bf16_f32 v192, v192, v193\n\t"
            "v_mfma_f32_16x16x32_bf16 v[176:179], v[132:135], v[156:159], v[176:179]\n\t"
            "ds_read_b128 v[156:159], v243 offset:6400\n\t"
            "v_cvt_pk_bf16_f32 v193, v194, v195\n\t"
            "v_mfma_f32_16x16x32_bf16 v[176:179], v[136:139], v[160:163], v[176:179]\n\t"
            "ds_read_b128 v[160:163], v242 offset:12288\n\t"
            "v_mfma_f32_16x16x32_bf16 v[176:179], v[140:143], v[164:167], v[176:179]\n\t"
            "s_and_b32 s70, s44, 0x7ffffff\n\t"
            "s_add_u32 s70, s70, s77\n\t"
            "s_and_b32 s44, s44, 0x08000000\n\t"
            "s_or_b32 s44, s44, s39\n\t"
            "v_mfma_f32_16x16x32_bf16 v[180:183], v[0:3], v[168:171], v[180:183]\n\t"
            "ds_read_b128 v[164:167], v243 offset:12288\n\t"
            "v_mfma_f32_16x16x32_bf16 v[180:183], v[4:7], v[172:175], v[180:183]\n\t"
            "s_waitcnt lgkmcnt(5)\n\t"
            "ds_read_b32 v249, v252 offset:1028\n\t"
            "v_cvt_pk_bf16_f32 v195, v178, v179\n\t"
            "v_mfma_f32_16x16x32_bf16 v[180:183], v[8:11], v[144:147], v[180:183]\n\t"
            "s_waitcnt lgkmcnt(5)\n\t"
            "ds_read_b128 v[208:211], v248\n\t"
            "v_cvt_pk_bf16_f32 v194, v176, v177\n\t"
            "v_mfma_f32_16x16x32_bf16 v[180:183], v[12:15], v[148:151], v[180:183]\n\t"
            "s_waitcnt lgkmcnt(5)\n\t"
            "ds_read_b128 v[212:215], v236 offset:6144\n\t"
            "v_lshlrev_b32 v176, 16, v220\n\t"
            "v_mfma_f32_16x16x32_bf16 v[180:183], v[16:19], v[152:155], v[180:183]\n\t"
            "s_waitcnt lgkmcnt(5)\n\t"
            "v_lshlrev_b32 v178, 16, v221\n\t"
            "v_and_b32 v179, 0xffff0000, v221\n\t"
            "v_mfma_f32_16x16x32_bf16 v[180:183], v[20:23], v[156:159], v[180:183]\n\t"
            "buffer_store_dwordx4 v[192:195], v196, s[40:43], s45 offen offset:128 sc0 sc1\n\t"
            "v_mfma_f32_16x16x32_bf16 v[184:187], v[24:27], v[168:171], v[184:187]\n\t"
            "s_nop 0\n\t"
            "v_and_b32 v195, 0xffff0000, v219\n\t"
            "v_and_b32 v193, 0xffff0000, v218\n\t"
            "v_mfma_f32_16x16x32_bf16 v[184:187], v[28:31], v[172:175], v[184:187]\n\t"
            "v_lshlrev_b32 v192, 16, v218\n\t"
            "v_lshlrev_b32 v194, 16, v219\n\t"
            "v_mfma_f32_16x16x32_bf16 v[184:187], v[32:35], v[144:147], v[184:187]\n\t"
            "ds_read_b128 v[216:219], v237 offset:6144\n\t"
            "v_and_b32 v177, 0xffff0000, v220\n\t"
            "v_mfma_f32_16x16x32_bf16 v[184:187], v[36:39], v[148:151], v[184:187]\n\t"
            "v_cvt_pk_bf16_f32 v180, v180, v181\n\t"
            "v_cvt_pk_bf16_f32 v181, v182, v183\n\t"
            "v_mfma_f32_16x16x32_bf16 v[184:187], v[40:43], v[152:155], v[184:187]\n\t"
            "s_waitcnt lgkmcnt(3)\n\t"
            "v_and_b32 v249, s44, v249\n\t"
            "v_mfma_f32_16x16x32_bf16 v[184:187], v[44:47], v[156:159], v[184:187]\n\t"
            "v_mfma_f32_16x16x32_bf16 v[188:191], v[48:51], v[168:171], v[188:191]\n\t"
            "v_mfma_f32_16x16x32_bf16 v[188:191], v[52:55], v[172:175], v[188:191]\n\t"
            "v_mfma_f32_16x16x32_bf16 v[188:191], v[56:59], v[144:147], v[188:191]\n\t"
            "v_mfma_f32_16x16x32_bf16 v[188:191], v[60:63], v[148:151], v[188:191]\n\t"
            "v_mfma_f32_16x16x32_bf16 v[188:191], v[64:67], v[152:155], v[188:191]\n\t"
            "v_mfma_f32_16x16x32_bf16 v[188:191], v[68:71], v[156:159], v[188:191]\n\t"
            "s_nop 1\n\t"
            "v_cvt_pk_bf16_f32 v183, v186, v187\n\t"
            "v_mfma_f32_16x16x32_bf16 v[192:195], v[72:75], v[168:171], v[192:195]\n\t"
            "v_cvt_pk_bf16_f32 v182, v184, v185\n\t"
            "v_and_b32 v185, 0xffff0000, v224\n\t"
            "v_mfma_f32_16x16x32_bf16 v[192:195], v[76:79], v[172:175], v[192:195]\n\t"
            "v_lshlrev_b32 v186, 16, v225\n\t"
            "v_lshlrev_b32 v184, 16, v224\n\t"
            "v_mfma_f32_16x16x32_bf16 v[192:195], v[80:83], v[144:147], v[192:195]\n\t"
            "v_and_b32 v187, 0xffff0000, v225\n\t"
            "v_cvt_pk_bf16_f32 v188, v188, v189\n\t"
            "v_mfma_f32_16x16x32_bf16 v[192:195], v[84:87], v[148:151], v[192:195]\n\t"
            "v_cvt_pk_bf16_f32 v189, v190, v191\n\t"
            "v_mfma_f32_16x16x32_bf16 v[192:195], v[88:91], v[152:155], v[192:195]\n\t"
            "v_mfma_f32_16x16x32_bf16 v[192:195], v[92:95], v[156:159], v[192:195]\n\t"
            "buffer_store_dwordx4 v[180:183], v197, s[40:43], s45 offen sc0 sc1\n\t"
            "v_mfma_f32_16x16x32_bf16 v[176:179], v[96:99], v[168:171], v[176:179]\n\t"
            "s_nop 0\n\t"
            "v_lshlrev_b32 v182, 16, v223\n\t"
            "v_and_b32 v181, 0xffff0000, v222\n\t"
            "v_mfma_f32_16x16x32_bf16 v[176:179], v[100:103], v[172:175], v[176:179]\n\t"
            "v_lshlrev_b32 v180, 16, v222\n\t"
            "v_and_b32 v183, 0xffff0000, v223\n\t"
            "v_mfma_f32_16x16x32_bf16 v[176:179], v[104:107], v[144:147], v[176:179]\n\t"
            "ds_read_b128 v[220:223], v248 offset:6144\n\t"
            "v_cvt_pk_bf16_f32 v190, v192, v193\n\t"
            "v_mfma_f32_16x16x32_bf16 v[176:179], v[108:111], v[148:151], v[176:179]\n\t"
            "v_cvt_pk_bf16_f32 v191, v194, v195\n\t"
            "v_lshlrev_b32 v192, 16, v228\n\t"
            "v_mfma_f32_16x16x32_bf16 v[176:179], v[112:115], v[152:155], v[176:179]\n\t"
            "v_and_b32 v195, 0xffff0000, v229\n\t"
            "v_and_b32 v193, 0xffff0000, v228\n\t"
            "v_mfma_f32_16x16x32_bf16 v[176:179], v[116:119], v[156:159], v[176:179]\n\t"
            "buffer_store_dwordx4 v[188:191], v197, s[40:43], s45 offen offset:64 sc0 sc1\n\t"
            "v_mfma_f32_16x16x32_bf16 v[180:183], v[120:123], v[168:171], v[180:183]\n\t"
            "ds_read_b128 v[168:171], v242 offset:12416\n\t"
            "v_lshlrev_b32 v188, 16, v226\n\t"
            "v_mfma_f32_16x16x32_bf16 v[180:183], v[124:127], v[172:175], v[180:183]\n\t"
            "ds_read_b128 v[172:175], v243 offset:12416\n\t"
            "v_and_b32 v191, 0xffff0000, v227\n\t"
            "v_mfma_f32_16x16x32_bf16 v[180:183], v[128:131], v[144:147], v[180:183]\n\t"
            "ds_read_b128 v[144:147], v242 offset:12544\n\t"
            "v_and_b32 v189, 0xffff0000, v226\n\t"
            "v_mfma_f32_16x16x32_bf16 v[180:183], v[132:135], v[148:151], v[180:183]\n\t"
            "ds_read_b128 v[148:151], v243 offset:12544\n\t"
            "v_lshlrev_b32 v190, 16, v227\n\t"
            "v_mfma_f32_16x16x32_bf16 v[180:183], v[136:139], v[152:155], v[180:183]\n\t"
            "ds_read_b128 v[224:227], v236 offset:12288\n\t"
            "v_lshlrev_b32 v194, 16, v229\n\t"
            "v_mfma_f32_16x16x32_bf16 v[180:183], v[140:143], v[156:159], v[180:183]\n\t"
            "v_and_b32 v153, 0xffff0000, v230\n\t"
            "v_lshlrev_b32 v152, 16, v230\n\t"
            "v_mfma_f32_16x16x32_bf16 v[184:187], v[0:3], v[160:163], v[184:187]\n\t"
            "v_and_b32 v155, 0xffff0000, v231\n\t"
            "v_lshlrev_b32 v154, 16, v231\n\t"
            "v_mfma_f32_16x16x32_bf16 v[184:187], v[4:7], v[164:167], v[184:187]\n\t"
            "s_waitcnt lgkmcnt(4)\n\t"
            "ds_read_b128 v[228:231], v237 offset:12288\n\t"
            "v_lshlrev_b32 v156, 16, v232\n\t"
            "v_lshlrev_b32 v158, 16, v233\n\t"
            "v_mfma_f32_16x16x32_bf16 v[184:187], v[8:11], v[168:171], v[184:187]\n\t"
            "s_waitcnt lgkmcnt(4)\n\t"
            "v_and_b32 v159, 0xffff0000, v233\n\t"
            "v_and_b32 v157, 0xffff0000, v232\n\t"
            "v_mfma_f32_16x16x32_bf16 v[184:187], v[12:15], v[172:175], v[184:187]\n\t"
            "s_waitcnt lgkmcnt(3)\n\t"
            "v_cvt_pk_bf16_f32 v176, v176, v177\n\t"
            "v_cvt_pk_bf16_f32 v177, v178, v179\n\t"
            "v_mfma_f32_16x16x32_bf16 v[184:187], v[16:19], v[144:147], v[184:187]\n\t"
            "s_waitcnt lgkmcnt(2)\n\t"
            "v_cvt_pk_bf16_f32 v179, v182, v183\n\t"
            "v_lshlrev_b32 v182, 16, v235\n\t"
            "v_mfma_f32_16x16x32_bf16 v[184:187], v[20:23], v[148:151], v[184:187]\n\t"
            "v_and_b32 v183, 0xffff0000, v235\n\t"
            "v_cvt_pk_bf16_f32 v178, v180, v181\n\t"
            "buffer_store_dwordx4 v[176:179], v197, s[40:43], s45 offen offset:128 sc0 sc1\n\t"
            "v_mfma_f32_16x16x32_bf16 v[188:191], v[24:27], v[160:163], v[188:191]\n\t"
            "v_lshlrev_b32 v180, 16, v234\n\t"
            "v_and_b32 v181, 0xffff0000, v234\n\t"
            "v_mfma_f32_16x16x32_bf16 v[188:191], v[28:31], v[164:167], v[188:191]\n\t"
            "ds_read_b128 v[232:235], v248 offset:12288\n\t"
            "v_cvt_pk_bf16_f32 v184, v184, v185\n\t"
            "v_mfma_f32_16x16x32_bf16 v[188:191], v[32:35], v[168:171], v[188:191]\n\t"
            "s_waitcnt lgkmcnt(0)\n\t"
            "s_barrier\n\t"
            "v_cvt_pk_bf16_f32 v185, v186, v187\n\t"
            "v_mfma_f32_16x16x32_bf16 v[188:191], v[36:39], v[172:175], v[188:191]\n\t"
            "v_mfma_f32_16x16x32_bf16 v[188:191], v[40:43], v[144:147], v[188:191]\n\t"
            "v_mfma_f32_16x16x32_bf16 v[188:191], v[44:47], v[148:151], v[188:191]\n\t"
            "v_mfma_f32_16x16x32_bf16 v[192:195], v[48:51], v[160:163], v[192:195]\n\t"
            "v_mfma_f32_16x16x32_bf16 v[192:195], v[52:55], v[164:167], v[192:195]\n\t"
            "v_mfma_f32_16x16x32_bf16 v[192:195], v[56:59], v[168:171], v[192:195]\n\t"
            "v_mfma_f32_16x16x32_bf16 v[192:195], v[60:63], v[172:175], v[192:195]\n\t"
            "v_mfma_f32_16x16x32_bf16 v[192:195], v[64:67], v[144:147], v[192:195]\n\t"
            "v_mfma_f32_16x16x32_bf16 v[192:195], v[68:71], v[148:151], v[192:195]\n\t"
            "s_nop 1\n\t"
            "v_cvt_pk_bf16_f32 v187, v190, v191\n\t"
            "v_mfma_f32_16x16x32_bf16 v[152:155], v[72:75], v[160:163], v[152:155]\n\t"
            "v_cvt_pk_bf16_f32 v186, v188, v189\n\t"
            "v_mfma_f32_16x16x32_bf16 v[152:155], v[76:79], v[164:167], v[152:155]\n\t"
            "v_mfma_f32_16x16x32_bf16 v[152:155], v[80:83], v[168:171], v[152:155]\n\t"
            "v_mfma_f32_16x16x32_bf16 v[152:155], v[84:87], v[172:175], v[152:155]\n\t"
            "v_cvt_pk_bf16_f32 v192, v192, v193\n\t"
            "v_mfma_f32_16x16x32_bf16 v[152:155], v[88:91], v[144:147], v[152:155]\n\t"
            "v_cvt_pk_bf16_f32 v193, v194, v195\n\t"
            "v_mfma_f32_16x16x32_bf16 v[152:155], v[92:95], v[148:151], v[152:155]\n\t"
            "buffer_store_dwordx4 v[184:187], v198, s[40:43], s45 offen sc0 sc1\n\t"
            "v_mfma_f32_16x16x32_bf16 v[156:159], v[96:99], v[160:163], v[156:159]\n\t"
            "v_mfma_f32_16x16x32_bf16 v[156:159], v[100:103], v[164:167], v[156:159]\n\t"
            "v_mfma_f32_16x16x32_bf16 v[156:159], v[104:107], v[168:171], v[156:159]\n\t"
            "v_mfma_f32_16x16x32_bf16 v[156:159], v[108:111], v[172:175], v[156:159]\n\t"
            "v_mfma_f32_16x16x32_bf16 v[156:159], v[112:115], v[144:147], v[156:159]\n\t"
            "s_nop 1\n\t"
            "v_cvt_pk_bf16_f32 v194, v152, v153\n\t"
            "v_mfma_f32_16x16x32_bf16 v[156:159], v[116:119], v[148:151], v[156:159]\n\t"
            "v_cvt_pk_bf16_f32 v195, v154, v155\n\t"
            "buffer_store_dwordx4 v[192:195], v198, s[40:43], s45 offen offset:64 sc0 sc1\n\t"
            "v_mfma_f32_16x16x32_bf16 v[180:183], v[120:123], v[160:163], v[180:183]\n\t"
            "s_waitcnt vmcnt(8)\n\t"
            "ds_read_b64 v[162:163], v253 offset:112\n\t"
            "v_mfma_f32_16x16x32_bf16 v[180:183], v[124:127], v[164:167], v[180:183]\n\t"
            "ds_read_b64 v[160:161], v253 offset:16\n\t"
            "v_mfma_f32_16x16x32_bf16 v[180:183], v[128:131], v[168:171], v[180:183]\n\t"
            "ds_read_b64 v[166:167], v253 offset:1856\n\t"
            "v_cvt_pk_bf16_f32 v156, v156, v157\n\t"
            "v_mfma_f32_16x16x32_bf16 v[180:183], v[132:135], v[172:175], v[180:183]\n\t"
            "ds_read_b128 v[172:175], v254 offset:64\n\t"
            "v_cvt_pk_bf16_f32 v157, v158, v159\n\t"
            "v_mfma_f32_16x16x32_bf16 v[180:183], v[136:139], v[144:147], v[180:183]\n\t"
            "ds_read_b64 v[152:153], v253 offset:8\n\t"
            "v_mfma_f32_16x16x32_bf16 v[180:183], v[140:143], v[148:151], v[180:183]\n\t"
            "s_setprio 0\n\t"
            "ds_read_b64 v[164:165], v253 offset:1760\n\t"
            "ds_read_b64 v[144:145], v253\n\t"
            "ds_read_b64 v[146:147], v253 offset:96\n\t"
            "ds_read_b64 v[148:149], v253 offset:1744\n\t"
            "ds_read_b64 v[154:155], v253 offset:104\n\t"
            "ds_read_b128 v[168:171], v254\n\t"
            "ds_read_b64 v[150:151], v253 offset:1840\n\t"
            "v_cvt_pk_bf16_f32 v159, v182, v183\n\t"
            "v_cvt_pk_bf16_f32 v158, v180, v181\n\t"
            "buffer_store_dwordx4 v[156:159], v198, s[40:43], s45 offen offset:128 sc0 sc1\n\t"
            "s_mov_b32 m0, s32\n\t"
            "s_add_u32 s49, s70, s25\n\t"
            "buffer_load_dword v249, s[72:75], s70 offen lds\n\t"
            "buffer_load_dword v249, s[72:75], s49 offen lds\n\t"
            "ds_read_b64 v[156:157], v253 offset:1752\n\t"
            "s_add_u32 s45, s46, s27\n\t"
            "s_mov_b32 s46, s47\n\t"
            "s_mov_b32 s47, s68\n\t"
            "ds_read_b64 v[158:159], v253 offset:1848\n\t"
            "s_xor_b32 s48, s48, s71\n\t"
            "s_xor_b32 s67, s67, s71\n\t"
            "s_add_u32 s76, s76, 4\n\t"
            "s_lshl_b32 s56, s56, 2\n\t"
            "s_lshr_b32 s49, s26, 30\n\t"
            "s_or_b32 s56, s56, s49\n\t"
            "s_cmp_lt_u32 s46, 0x7000000\n\t"
            "s_cbranch_scc1 .Lwb_loop%=\n\t"
            "s_waitcnt lgkmcnt(0)\n\t"
            "s_nop 7\n\t"
            "s_setprio 3\n\t"
            "s_nop 7\n\t"
            "ds_read_b128 v[196:199], v252\n\t"
            "s_bfe_u32 s57, s56, 0x20004\n\t"
            "s_lshl_b32 s58, s57, 3\n\t"
            "s_lshr_b32 s58, s55, s58\n\t"
            "s_lshl_b64 s[36:37], s[34:35], s58\n\t"
            "s_bitcmp1_b32 s57, 1\n\t"
            "s_cselect_b32 s58, 1177344, 0\n\t"
            "s_bitcmp1_b32 s57, 0\n\t"
            "s_cselect_b32 s57, 2304, 0\n\t"
            "s_add_u32 s57, s57, s58\n\t"
            "s_waitcnt lgkmcnt(0)\n\t"
            "s_mov_b64 exec, s[36:37]\n\t"
            "v_add_u32 v196, s57, v196\n\t"
            "s_mov_b64 exec, -1\n\t"
            "s_nop 7\n\t"
            "v_mov_b32 v144, s23\n\t"
            ".Ldp%=:\n\t"
            "ds_read_b128 v[148:151], v144 offset:96\n\t"
            "s_waitcnt lgkmcnt(0)\n\t"
            "v_or_b32 v148, v148, v149\n\t"
            "v_or3_b32 v148, v148, v150, v151\n\t"
            "v_cmp_gt_i32 vcc, 0, v148\n\t"
            "s_nop 4\n\t"
            "s_cbranch_vccnz .Ldp%=\n\t"
            "s_nop 7\n\t"
            "ds_read_b128 v[184:187], v242\n\t"
            "v_lshlrev_b32 v152, 16, v200\n\t"
            "v_and_b32 v153, 0xffff0000, v200\n\t"
            "v_lshlrev_b32 v154, 16, v201\n\t"
            "v_and_b32 v155, 0xffff0000, v201\n\t"
            "v_lshlrev_b32 v156, 16, v202\n\t"
            "v_and_b32 v157, 0xffff0000, v202\n\t"
            "v_lshlrev_b32 v158, 16, v203\n\t"
            "v_and_b32 v159, 0xffff0000, v203\n\t"
            "v_lshlrev_b32 v160, 16, v204\n\t"
            "v_and_b32 v161, 0xffff0000, v204\n\t"
            "v_lshlrev_b32 v162, 16, v205\n\t"
            "v_and_b32 v163, 0xffff0000, v205\n\t"
            "v_lshlrev_b32 v164, 16, v206\n\t"
            "v_and_b32 v165, 0xffff0000, v206\n\t"
            "v_lshlrev_b32 v166, 16, v207\n\t"
            "v_and_b32 v167, 0xffff0000, v207\n\t"
            "v_lshlrev_b32 v176, 16, v208\n\t"
            "v_and_b32 v177, 0xffff0000, v208\n\t"
            "v_lshlrev_b32 v178, 16, v209\n\t"
            "v_and_b32 v179, 0xffff0000, v209\n\t"
            "v_lshlrev_b32 v180, 16, v210\n\t"
            "v_and_b32 v181, 0xffff0000, v210\n\t"
            "v_lshlrev_b32 v182, 16, v211\n\t"
            "v_and_b32 v183, 0xffff0000, v211\n\t"
            "v_lshlrev_b32 v144, 16, v212\n\t"
            "v_and_b32 v145, 0xffff0000, v212\n\t"
            "v_lshlrev_b32 v146, 16, v213\n\t"
            "v_and_b32 v147, 0xffff0000, v213\n\t"
            "v_lshlrev_b32 v148, 16, v214\n\t"
            "v_and_b32 v149, 0xffff0000, v214\n\t"
            "s_waitcnt lgkmcnt(0)\n\t"
            "v_mfma_f32_16x16x32_bf16 v[152:155], v[0:3], v[184:187], v[152:155]\n\t"
            "ds_read_b128 v[188:191], v243\n\t"
            "v_lshlrev_b32 v150, 16, v215\n\t"
            "v_mfma_f32_16x16x32_bf16 v[156:159], v[24:27], v[184:187], v[156:159]\n\t"
            "ds_read_b128 v[192:195], v242 offset:128\n\t"
            "v_and_b32 v151, 0xffff0000, v215\n\t"
            "v_mfma_f32_16x16x32_bf16 v[160:163], v[48:51], v[184:187], v[160:163]\n\t"
            "v_lshlrev_b32 v168, 16, v216\n\t"
            "v_and_b32 v169, 0xffff0000, v216\n\t"
            "v_mfma_f32_16x16x32_bf16 v[164:167], v[72:75], v[184:187], v[164:167]\n\t"
            "v_lshlrev_b32 v170, 16, v217\n\t"
            "v_and_b32 v171, 0xffff0000, v217\n\t"
            "v_mfma_f32_16x16x32_bf16 v[176:179], v[96:99], v[184:187], v[176:179]\n\t"
            "v_lshlrev_b32 v172, 16, v218\n\t"
            "v_and_b32 v173, 0xffff0000, v218\n\t"
            "v_mfma_f32_16x16x32_bf16 v[180:183], v[120:123], v[184:187], v[180:183]\n\t"
            "ds_read_b128 v[184:187], v243 offset:128\n\t"
            "v_lshlrev_b32 v174, 16, v219\n\t"
            "v_and_b32 v175, 0xffff0000, v219\n\t"
            "s_waitcnt lgkmcnt(2)\n\t"
            "v_mfma_f32_16x16x32_bf16 v[152:155], v[4:7], v[188:191], v[152:155]\n\t"
            "v_mfma_f32_16x16x32_bf16 v[156:159], v[28:31], v[188:191], v[156:159]\n\t"
            "v_mfma_f32_16x16x32_bf16 v[160:163], v[52:55], v[188:191], v[160:163]\n\t"
            "v_mfma_f32_16x16x32_bf16 v[164:167], v[76:79], v[188:191], v[164:167]\n\t"
            "v_mfma_f32_16x16x32_bf16 v[176:179], v[100:103], v[188:191], v[176:179]\n\t"
            "v_mfma_f32_16x16x32_bf16 v[180:183], v[124:127], v[188:191], v[180:183]\n\t"
            "ds_read_b128 v[188:191], v242 offset:256\n\t"
            "s_waitcnt lgkmcnt(2)\n\t"
            "v_mfma_f32_16x16x32_bf16 v[152:155], v[8:11], v[192:195], v[152:155]\n\t"
            "v_mfma_f32_16x16x32_bf16 v[156:159], v[32:35], v[192:195], v[156:159]\n\t"
            "v_mfma_f32_16x16x32_bf16 v[160:163], v[56:59], v[192:195], v[160:163]\n\t"
            "v_mfma_f32_16x16x32_bf16 v[164:167], v[80:83], v[192:195], v[164:167]\n\t"
            "v_mfma_f32_16x16x32_bf16 v[176:179], v[104:107], v[192:195], v[176:179]\n\t"
            "v_mfma_f32_16x16x32_bf16 v[180:183], v[128:131], v[192:195], v[180:183]\n\t"
            "ds_read_b128 v[192:195], v243 offset:256\n\t"
            "s_waitcnt lgkmcnt(2)\n\t"
            "v_mfma_f32_16x16x32_bf16 v[152:155], v[12:15], v[184:187], v[152:155]\n\t"
            "v_mfma_f32_16x16x32_bf16 v[156:159], v[36:39], v[184:187], v[156:159]\n\t"
            "v_mfma_f32_16x16x32_bf16 v[160:163], v[60:63], v[184:187], v[160:163]\n\t"
            "v_mfma_f32_16x16x32_bf16 v[164:167], v[84:87], v[184:187], v[164:167]\n\t"
            "v_mfma_f32_16x16x32_bf16 v[176:179], v[108:111], v[184:187], v[176:179]\n\t"
            "v_mfma_f32_16x16x32_bf16 v[180:183], v[132:135], v[184:187], v[180:183]\n\t"
            "ds_read_b128 v[184:187], v242 offset:6144\n\t"
            "s_waitcnt lgkmcnt(2)\n\t"
            "v_mfma_f32_16x16x32_bf16 v[152:155], v[16:19], v[188:191], v[152:155]\n\t"
            "v_mfma_f32_16x16x32_bf16 v[156:159], v[40:43], v[188:191], v[156:159]\n\t"
            "v_mfma_f32_16x16x32_bf16 v[160:163], v[64:67], v[188:191], v[160:163]\n\t"
            "v_mfma_f32_16x16x32_bf16 v[164:167], v[88:91], v[188:191], v[164:167]\n\t"
            "v_mfma_f32_16x16x32_bf16 v[176:179], v[112:115], v[188:191], v[176:179]\n\t"
            "v_mfma_f32_16x16x32_bf16 v[180:183], v[136:139], v[188:191], v[180:183]\n\t"
            "ds_read_b128 v[188:191], v243 offset:6144\n\t"
            "s_waitcnt lgkmcnt(2)\n\t"
            "v_mfma_f32_16x16x32_bf16 v[152:155], v[20:23], v[192:195], v[152:155]\n\t"
            "v_mfma_f32_16x16x32_bf16 v[156:159], v[44:47], v[192:195], v[156:159]\n\t"
            "v_mfma_f32_16x16x32_bf16 v[160:163], v[68:71], v[192:195], v[160:163]\n\t"
            "v_mfma_f32_16x16x32_bf16 v[164:167], v[92:95], v[192:195], v[164:167]\n\t"
            "v_mfma_f32_16x16x32_bf16 v[176:179], v[116:119], v[192:195], v[176:179]\n\t"
            "v_mfma_f32_16x16x32_bf16 v[180:183], v[140:143], v[192:195], v[180:183]\n\t"
            "ds_read_b128 v[192:195], v242 offset:6272\n\t"
            "s_waitcnt lgkmcnt(2)\n\t"
            "v_mfma_f32_16x16x32_bf16 v[144:147], v[0:3], v[184:187], v[144:147]\n\t"
            "v_cvt_pk_bf16_f32 v152, v152, v153\n\t"
            "v_cvt_pk_bf16_f32 v153, v154, v155\n\t"
            "v_mfma_f32_16x16x32_bf16 v[148:151], v[24:27], v[184:187], v[148:151]\n\t"
            "v_cvt_pk_bf16_f32 v160, v160, v161\n\t"
            "v_cvt_pk_bf16_f32 v161, v162, v163\n\t"
            "v_mfma_f32_16x16x32_bf16 v[168:171], v[48:51], v[184:187], v[168:171]\n\t"
            "v_cvt_pk_bf16_f32 v154, v156, v157\n\t"
            "v_cvt_pk_bf16_f32 v155, v158, v159\n\t"
            "v_mfma_f32_16x16x32_bf16 v[172:175], v[72:75], v[184:187], v[172:175]\n\t"
            "v_cvt_pk_bf16_f32 v162, v164, v165\n\t"
            "v_cvt_pk_bf16_f32 v163, v166, v167\n\t"
            "s_waitcnt lgkmcnt(1)\n\t"
            "v_mfma_f32_16x16x32_bf16 v[144:147], v[4:7], v[188:191], v[144:147]\n\t"
            "v_lshlrev_b32 v156, 16, v220\n\t"
            "v_and_b32 v157, 0xffff0000, v220\n\t"
            "v_mfma_f32_16x16x32_bf16 v[148:151], v[28:31], v[188:191], v[148:151]\n\t"
            "v_lshlrev_b32 v158, 16, v221\n\t"
            "v_and_b32 v159, 0xffff0000, v221\n\t"
            "v_mfma_f32_16x16x32_bf16 v[168:171], v[52:55], v[188:191], v[168:171]\n\t"
            "v_lshlrev_b32 v164, 16, v222\n\t"
            "v_and_b32 v165, 0xffff0000, v222\n\t"
            "v_mfma_f32_16x16x32_bf16 v[156:159], v[96:99], v[184:187], v[156:159]\n\t"
            "v_lshlrev_b32 v166, 16, v223\n\t"
            "v_and_b32 v167, 0xffff0000, v223\n\t"
            "v_mfma_f32_16x16x32_bf16 v[172:175], v[76:79], v[188:191], v[172:175]\n\t"
            "buffer_store_dwordx4 v[152:155], v196, s[40:43], s45 offen sc0 sc1\n\t"
            "v_mfma_f32_16x16x32_bf16 v[164:167], v[120:123], v[184:187], v[164:167]\n\t"
            "ds_read_b128 v[184:187], v243 offset:6272\n\t"
            "v_cvt_pk_bf16_f32 v176, v176, v177\n\t"
            "v_mfma_f32_16x16x32_bf16 v[156:159], v[100:103], v[188:191], v[156:159]\n\t"
            "buffer_store_dwordx4 v[160:163], v196, s[40:43], s45 offen offset:64 sc0 sc1\n\t"
            "v_mfma_f32_16x16x32_bf16 v[164:167], v[124:127], v[188:191], v[164:167]\n\t"
            "ds_read_b128 v[188:191], v242 offset:6400\n\t"
            "v_cvt_pk_bf16_f32 v177, v178, v179\n\t"
            "s_waitcnt lgkmcnt(2)\n\t"
            "v_mfma_f32_16x16x32_bf16 v[144:147], v[8:11], v[192:195], v[144:147]\n\t"
            "v_cvt_pk_bf16_f32 v178, v180, v181\n\t"
            "v_cvt_pk_bf16_f32 v179, v182, v183\n\t"
            "v_mfma_f32_16x16x32_bf16 v[148:151], v[32:35], v[192:195], v[148:151]\n\t"
            "buffer_store_dwordx4 v[176:179], v196, s[40:43], s45 offen offset:128 sc0 sc1\n\t"
            "v_mfma_f32_16x16x32_bf16 v[168:171], v[56:59], v[192:195], v[168:171]\n\t"
            "v_lshlrev_b32 v152, 16, v224\n\t"
            "v_and_b32 v153, 0xffff0000, v224\n\t"
            "v_mfma_f32_16x16x32_bf16 v[172:175], v[80:83], v[192:195], v[172:175]\n\t"
            "v_lshlrev_b32 v154, 16, v225\n\t"
            "v_and_b32 v155, 0xffff0000, v225\n\t"
            "v_mfma_f32_16x16x32_bf16 v[156:159], v[104:107], v[192:195], v[156:159]\n\t"
            "v_lshlrev_b32 v180, 16, v226\n\t"
            "v_and_b32 v181, 0xffff0000, v226\n\t"
            "v_mfma_f32_16x16x32_bf16 v[164:167], v[128:131], v[192:195], v[164:167]\n\t"
            "ds_read_b128 v[192:195], v243 offset:6400\n\t"
            "v_lshlrev_b32 v182, 16, v227\n\t"
            "s_waitcnt lgkmcnt(2)\n\t"
            "v_mfma_f32_16x16x32_bf16 v[144:147], v[12:15], v[184:187], v[144:147]\n\t"
            "v_and_b32 v183, 0xffff0000, v227\n\t"
            "v_lshlrev_b32 v160, 16, v228\n\t"
            "v_mfma_f32_16x16x32_bf16 v[148:151], v[36:39], v[184:187], v[148:151]\n\t"
            "v_and_b32 v161, 0xffff0000, v228\n\t"
            "v_lshlrev_b32 v162, 16, v229\n\t"
            "s_waitcnt lgkmcnt(1)\n\t"
            "v_mfma_f32_16x16x32_bf16 v[144:147], v[16:19], v[188:191], v[144:147]\n\t"
            "v_and_b32 v163, 0xffff0000, v229\n\t"
            "v_lshlrev_b32 v176, 16, v230\n\t"
            "v_mfma_f32_16x16x32_bf16 v[168:171], v[60:63], v[184:187], v[168:171]\n\t"
            "v_and_b32 v177, 0xffff0000, v230\n\t"
            "v_lshlrev_b32 v178, 16, v231\n\t"
            "v_mfma_f32_16x16x32_bf16 v[148:151], v[40:43], v[188:191], v[148:151]\n\t"
            "v_and_b32 v179, 0xffff0000, v231\n\t"
            "v_mfma_f32_16x16x32_bf16 v[172:175], v[84:87], v[184:187], v[172:175]\n\t"
            "v_mfma_f32_16x16x32_bf16 v[168:171], v[64:67], v[188:191], v[168:171]\n\t"
            "v_mfma_f32_16x16x32_bf16 v[156:159], v[108:111], v[184:187], v[156:159]\n\t"
            "s_waitcnt lgkmcnt(0)\n\t"
            "v_mfma_f32_16x16x32_bf16 v[144:147], v[20:23], v[192:195], v[144:147]\n\t"
            "v_mfma_f32_16x16x32_bf16 v[148:151], v[44:47], v[192:195], v[148:151]\n\t"
            "v_mfma_f32_16x16x32_bf16 v[164:167], v[132:135], v[184:187], v[164:167]\n\t"
            "ds_read_b128 v[184:187], v242 offset:12288\n\t"
            "v_mfma_f32_16x16x32_bf16 v[172:175], v[88:91], v[188:191], v[172:175]\n\t"
            "v_mfma_f32_16x16x32_bf16 v[168:171], v[68:71], v[192:195], v[168:171]\n\t"
            "v_mfma_f32_16x16x32_bf16 v[156:159], v[112:115], v[188:191], v[156:159]\n\t"
            "s_nop 1\n\t"
            "v_cvt_pk_bf16_f32 v144, v144, v145\n\t"
            "v_mfma_f32_16x16x32_bf16 v[164:167], v[136:139], v[188:191], v[164:167]\n\t"
            "ds_read_b128 v[188:191], v243 offset:12288\n\t"
            "v_cvt_pk_bf16_f32 v145, v146, v147\n\t"
            "v_mfma_f32_16x16x32_bf16 v[172:175], v[92:95], v[192:195], v[172:175]\n\t"
            "v_cvt_pk_bf16_f32 v146, v148, v149\n\t"
            "v_cvt_pk_bf16_f32 v147, v150, v151\n\t"
            "v_mfma_f32_16x16x32_bf16 v[156:159], v[116:119], v[192:195], v[156:159]\n\t"
            "buffer_store_dwordx4 v[144:147], v197, s[40:43], s45 offen sc0 sc1\n\t"
            "v_mfma_f32_16x16x32_bf16 v[164:167], v[140:143], v[192:195], v[164:167]\n\t"
            "ds_read_b128 v[192:195], v242 offset:12416\n\t"
            "v_cvt_pk_bf16_f32 v168, v168, v169\n\t"
            "s_waitcnt lgkmcnt(2)\n\t"
            "v_mfma_f32_16x16x32_bf16 v[152:155], v[0:3], v[184:187], v[152:155]\n\t"
            "v_cvt_pk_bf16_f32 v169, v170, v171\n\t"
            "v_cvt_pk_bf16_f32 v170, v172, v173\n\t"
            "v_mfma_f32_16x16x32_bf16 v[180:183], v[24:27], v[184:187], v[180:183]\n\t"
            "v_cvt_pk_bf16_f32 v171, v174, v175\n\t"
            "v_cvt_pk_bf16_f32 v156, v156, v157\n\t"
            "v_mfma_f32_16x16x32_bf16 v[160:163], v[48:51], v[184:187], v[160:163]\n\t"
            "buffer_store_dwordx4 v[168:171], v197, s[40:43], s45 offen offset:64 sc0 sc1\n\t"
            "v_mfma_f32_16x16x32_bf16 v[176:179], v[72:75], v[184:187], v[176:179]\n\t"
            "v_cvt_pk_bf16_f32 v157, v158, v159\n\t"
            "v_cvt_pk_bf16_f32 v158, v164, v165\n\t"
            "s_waitcnt lgkmcnt(1)\n\t"
            "v_mfma_f32_16x16x32_bf16 v[152:155], v[4:7], v[188:191], v[152:155]\n\t"
            "v_cvt_pk_bf16_f32 v159, v166, v167\n\t"
            "v_lshlrev_b32 v164, 16, v232\n\t"
            "v_mfma_f32_16x16x32_bf16 v[180:183], v[28:31], v[188:191], v[180:183]\n\t"
            "buffer_store_dwordx4 v[156:159], v197, s[40:43], s45 offen offset:128 sc0 sc1\n\t"
            "v_mfma_f32_16x16x32_bf16 v[160:163], v[52:55], v[188:191], v[160:163]\n\t"
            "v_and_b32 v165, 0xffff0000, v232\n\t"
            "v_lshlrev_b32 v166, 16, v233\n\t"
            "v_mfma_f32_16x16x32_bf16 v[176:179], v[76:79], v[188:191], v[176:179]\n\t"
            "v_and_b32 v167, 0xffff0000, v233\n\t"
            "v_lshlrev_b32 v156, 16, v234\n\t"
            "s_waitcnt lgkmcnt(0)\n\t"
            "v_mfma_f32_16x16x32_bf16 v[152:155], v[8:11], v[192:195], v[152:155]\n\t"
            "v_and_b32 v157, 0xffff0000, v234\n\t"
            "v_lshlrev_b32 v158, 16, v235\n\t"
            "v_mfma_f32_16x16x32_bf16 v[164:167], v[96:99], v[184:187], v[164:167]\n\t"
            "v_and_b32 v159, 0xffff0000, v235\n\t"
            "v_mfma_f32_16x16x32_bf16 v[164:167], v[100:103], v[188:191], v[164:167]\n\t"
            "s_nop 0\n\t"
            "v_mfma_f32_16x16x32_bf16 v[156:159], v[120:123], v[184:187], v[156:159]\n\t"
            "ds_read_b128 v[184:187], v243 offset:12416\n\t"
            "v_mfma_f32_16x16x32_bf16 v[156:159], v[124:127], v[188:191], v[156:159]\n\t"
            "ds_read_b128 v[188:191], v242 offset:12544\n\t"
            "v_mfma_f32_16x16x32_bf16 v[180:183], v[32:35], v[192:195], v[180:183]\n\t"
            "v_mfma_f32_16x16x32_bf16 v[160:163], v[56:59], v[192:195], v[160:163]\n\t"
            "v_mfma_f32_16x16x32_bf16 v[176:179], v[80:83], v[192:195], v[176:179]\n\t"
            "v_mfma_f32_16x16x32_bf16 v[164:167], v[104:107], v[192:195], v[164:167]\n\t"
            "v_mfma_f32_16x16x32_bf16 v[156:159], v[128:131], v[192:195], v[156:159]\n\t"
            "ds_read_b128 v[192:195], v243 offset:12544\n\t"
            "s_waitcnt lgkmcnt(2)\n\t"
            "v_mfma_f32_16x16x32_bf16 v[152:155], v[12:15], v[184:187], v[152:155]\n\t"
            "v_mfma_f32_16x16x32_bf16 v[180:183], v[36:39], v[184:187], v[180:183]\n\t"
            "s_waitcnt lgkmcnt(1)\n\t"
            "v_mfma_f32_16x16x32_bf16 v[152:155], v[16:19], v[188:191], v[152:155]\n\t"
            "v_mfma_f32_16x16x32_bf16 v[160:163], v[60:63], v[184:187], v[160:163]\n\t"
            "v_mfma_f32_16x16x32_bf16 v[180:183], v[40:43], v[188:191], v[180:183]\n\t"
            "v_mfma_f32_16x16x32_bf16 v[176:179], v[84:87], v[184:187], v[176:179]\n\t"
            "s_waitcnt lgkmcnt(0)\n\t"
            "v_mfma_f32_16x16x32_bf16 v[152:155], v[20:23], v[192:195], v[152:155]\n\t"
            "v_mfma_f32_16x16x32_bf16 v[160:163], v[64:67], v[188:191], v[160:163]\n\t"
            "v_mfma_f32_16x16x32_bf16 v[180:183], v[44:47], v[192:195], v[180:183]\n\t"
            "v_mfma_f32_16x16x32_bf16 v[164:167], v[108:111], v[184:187], v[164:167]\n\t"
            "v_mfma_f32_16x16x32_bf16 v[176:179], v[88:91], v[188:191], v[176:179]\n\t"
            "v_mfma_f32_16x16x32_bf16 v[156:159], v[132:135], v[184:187], v[156:159]\n\t"
            "v_mfma_f32_16x16x32_bf16 v[160:163], v[68:71], v[192:195], v[160:163]\n\t"
            "s_nop 1\n\t"
            "v_cvt_pk_bf16_f32 v152, v152, v153\n\t"
            "v_mfma_f32_16x16x32_bf16 v[164:167], v[112:115], v[188:191], v[164:167]\n\t"
            "v_cvt_pk_bf16_f32 v153, v154, v155\n\t"
            "v_mfma_f32_16x16x32_bf16 v[176:179], v[92:95], v[192:195], v[176:179]\n\t"
            "v_cvt_pk_bf16_f32 v154, v180, v181\n\t"
            "v_cvt_pk_bf16_f32 v155, v182, v183\n\t"
            "v_mfma_f32_16x16x32_bf16 v[156:159], v[136:139], v[188:191], v[156:159]\n\t"
            "buffer_store_dwordx4 v[152:155], v198, s[40:43], s45 offen sc0 sc1\n\t"
            "v_mfma_f32_16x16x32_bf16 v[164:167], v[116:119], v[192:195], v[164:167]\n\t"
            "v_cvt_pk_bf16_f32 v160, v160, v161\n\t"
            "v_cvt_pk_bf16_f32 v161, v162, v163\n\t"
            "v_mfma_f32_16x16x32_bf16 v[156:159], v[140:143], v[192:195], v[156:159]\n\t"
            "v_cvt_pk_bf16_f32 v162, v176, v177\n\t"
            "v_cvt_pk_bf16_f32 v163, v178, v179\n\t"
            "buffer_store_dwordx4 v[160:163], v198, s[40:43], s45 offen offset:64 sc0 sc1\n\t"
            "s_nop 1\n\t"
            "v_cvt_pk_bf16_f32 v164, v164, v165\n\t"
            "v_cvt_pk_bf16_f32 v165, v166, v167\n\t"
            "s_nop 0\n\t"
            "v_cvt_pk_bf16_f32 v166, v156, v157\n\t"
            "v_cvt_pk_bf16_f32 v167, v158, v159\n\t"
            "buffer_store_dwordx4 v[164:167], v198, s[40:43], s45 offen offset:128 sc0 sc1\n\t"
            "s_nop 7\n\t"
            ".Lre%=:\n\t"
            : "+{v[224:227]}"(f0), "+{v[228:231]}"(f1), "+{v[232:235]}"(f2), "+{v242}"(r242), "+{v243}"(r243), "+{v[244:247]}"(r244),
              "+{s45}"(s45), "+{s46}"(s46), "+{s47}"(s47), "+{s48}"(s48), "+{s67}"(s67), "+{s76}"(s76), "+{s56}"(s56), "+{s44}"(s44), "+{s70}"(s70), "+{s21}"(s21)
            : B_VIN, B_SIN
            : "v0", "v1", "v2", "v3", "v4", "v5", "v6", "v7", "v8", "v9", "v10", "v11", "v12", "v13", "v14", "v15", "v16", "v17", "v18", "v19", "v20", "v21", "v22", "v23", "v24", "v25", "v26", "v27", "v28", "v29", "v30", "v31", "v32", "v33", "v34", "v35", "v36", "v37", "v38", "v39", "v40", "v41", "v42", "v43", "v44", "v45", "v46", "v47", "v48", "v49", "v50", "v51", "v52", "v53", "v54", "v55", "v56", "v57", "v58", "v59", "v60", "v61", "v62", "v63", "v64", "v65", "v66", "v67", "v68", "v69", "v70", "v71", "v72", "v73", "v74", "v75", "v76", "v77", "v78", "v79", "v80", "v81", "v82", "v83", "v84", "v85", "v86", "v87", "v88", "v89", "v90", "v91", "v92", "v93", "v94", "v95", "v96", "v97", "v98", "v99", "v100", "v101", "v102", "v103", "v104", "v105", "v106", "v107", "v108", "v109", "v110", "v111", "v112", "v113", "v114", "v115", "v116", "v117", "v118", "v119", "v120", "v121", "v122", "v123", "v124", "v125", "v126", "v127", "v128", "v129", "v130", "v131", "v132", "v133", "v134", "v135", "v136", "v137", "v138", "v139", "v140", "v141", "v142", "v143", "v144", "v145", "v146", "v147", "v148", "v149", "v150", "v151", "v152", "v153", "v154", "v155", "v156", "v157", "v158", "v159", "v160", "v161", "v162", "v163", "v164", "v165", "v166", "v167", "v168", "v169", "v170", "v171", "v172", "v173", "v174", "v175", "v176", "v177", "v178", "v179", "v180", "v181", "v182", "v183", "v184", "v185", "v186", "v187", "v188", "v189", "v190", "v191", "v192", "v193", "v194", "v195", "v196", "v197", "v198", "v199", "v200", "v201", "v202", "v203", "v204", "v205", "v206", "v207", "v208", "v209", "v210", "v211", "v212", "v213", "v214", "v215", "v216", "v217", "v218", "v219", "v220", "v221", "v222", "v223", "v236", "v237", "v248", "v249", "s92", "s93", "s94", "s95", "s26", "s68", "s69", "s79", "s36", "s37", "s38", "s57", "s58", "s59", "s49", "vcc", "scc", "m0", "memory");
        }
        asm volatile("wk_code_end1:");
        asm volatile("wk_code_end2:");

    }
}

extern "C" void kernel_launch(void* const* d_in, const int* in_sizes, int n_in,
                              void* d_out, int out_size, void* d_ws, size_t ws_size,
                              hipStream_t stream) {
    const unsigned short* x = (const unsigned short*)d_in[0];
    const unsigned short* dw = (const unsigned short*)d_in[1];
    const unsigned short* wqkv = (const unsigned short*)d_in[2];
    const unsigned short* wo = (const unsigned short*)d_in[3];
    unsigned short* out = (unsigned short*)d_out;
    u32x4* wf = (u32x4*)d_ws;
    win_attn_kernel<<<GRID, 512, 0, stream>>>(x, dw, wqkv, wo, wf, out);
}
```

```cpp
#include <hip/hip_runtime.h>
#include <hip/hip_bf16.h>
#include <stdint.h>


typedef unsigned u32x4 __attribute__((ext_vector_type(4)));

constexpr int IMG = 511;
constexpr int CH = 192;
constexpr int NWX = 73;
constexpr int NWIN = NWX * NWX;
constexpr int GRID = 256;
constexpr int NSHORT = 21 * GRID - NWIN;
constexpr int ROWBYTES = IMG * CH * 2;
constexpr int XBIAS = (IMG + 1) * CH * 2;
constexpr unsigned OOB = 0x40000000u;

constexpr int X_OFF = 32768;
constexpr int X1_OFF = 53632;
constexpr int O_OFF = 74496;
constexpr int V_OFF = 92928;
constexpr int KQL_OFF = 111360;
constexpr int OL_OFF = 128256;
constexpr int AT_OFF = 137472;
constexpr int CT_OFF = AT_OFF + 19584;
constexpr int RS_OFF = CT_OFF + 4096;
constexpr int WL_OFF = RS_OFF + 2304 + 64;
constexpr int LDS_BYTES = WL_OFF + 128;
#define RS_BASE (RS_OFF + (wave < 4 ? wave * 384 : 768 + wave * 192))
constexpr int TP_OFF = X1_OFF + 8192;
constexpr int STL_OFF = O_OFF + 12288;
#define LB_OFF(b) ((b) < 7 ? V_OFF + (b) * 6160 : X1_OFF + ((b) - 7) * 6160)
#define LPOS(wc) (((wc) & 1) ? 0 : 6)
constexpr unsigned CROW = 0x060a0230u;
constexpr unsigned CLANE = 0x00132C3Fu;

constexpr unsigned long long TOKMAP[6] = {0x20001c190f2a2202ull, 0x052517121f061a13ull, 0x181e08151d0b260cull, 0x1b2b2903102d0901ull, 0x04162e0e2824142cull, 0x110a072127232f0dull};
constexpr unsigned long long ROWMAP[8] = {0x08321b240f1c0006ull, 0x23310e2a09170d14ull, 0x210c2f030a273418ull, 0x1d2b11221f043739ull, 0x3234363a103e192dull, 0x291a20022c3f1605ull, 0x303a3c2628122e15ull, 0x39253d1e01070b13ull};

constexpr int WF_BYTES = 8 * 36 * 1024;
constexpr int EP_OFF = WF_BYTES;
constexpr int CN_OFF = EP_OFF + 256, FL_OFF = EP_OFF + 512;
constexpr int SH_OFF = EP_OFF + 1024;
constexpr int WS_BYTES = SH_OFF + GRID * 12288;
constexpr int GO_OFF = WL_OFF + 124;
constexpr int PG_OFF = WL_OFF + 96;
constexpr int SPIN_MAX = 1 << 20;

#define BAR() asm volatile("s_waitcnt lgkmcnt(0)\n\ts_barrier" ::: "memory")
#define OPQ(x) asm volatile("" : "+v"(x))
__host__ __device__ __forceinline__ constexpr unsigned byte_of(unsigned long long a, unsigned long long b, int i) {
    return (unsigned)(((i & 8) ? b : a) >> (8 * (i & 7))) & 0xFFu;
}
__host__ __device__ __forceinline__ constexpr void seq_to_win(int seq, int& wy, int& wx) {
    const bool last = seq >= 5110;
    const int s = last ? 35 : seq / 146, r = seq - 146 * s;
    const int kx = last ? r / 6 : r >> 2, pos = r - (last ? 6 : 4) * kx;
    const bool low = last && pos >= (kx == 36 ? 2 : 4);
    wy = low ? 72 : 2 * s + (pos & 1);
    wx = 2 * kx + (low ? (kx == 36 ? 0 : pos - 4) : pos >> 1);
}
__host__ __device__ __forceinline__ constexpr int win_to_seq(int wy, int wx) {
    const int kx = wx >> 1;
    return wy == 72 ? 5110 + 6 * kx + (kx == 36 ? 2 : 4 + (wx & 1)) : 146 * (wy >> 1) + (wy >= 70 ? 6 : 4) * kx + 2 * (wx & 1) + (wy & 1);
}
__host__ __device__ __forceinline__ constexpr int block_of(int wy, int wx) { return wy >= 70 ? 1295 + (wx & ~1) + (wy == 72) : 37 * (wy >> 1) + (wx >> 1); }
__host__ __device__ __forceinline__ constexpr void block_pos(int b, int& ky, int& kx) {
    if (b >= 1295) { kx = (b - 1295) >> 1; ky = 70 + 2 * ((b - 1295) & 1); }
    else { const int s = b / 37; kx = b - 37 * s; ky = 2 * s; }
}
__host__ __device__ __forceinline__ constexpr unsigned wl_entry(int seq) {
    int wy = 0, wx = 0;
    seq_to_win(seq, wy, wx);
    return (unsigned)((wy * 7 * IMG + wx * 7) * (CH * 2)) | (wy == 0 ? 0x08000000u : 0u) | (wx == 0 ? 0x10000000u : 0u) |
           (wx == NWX - 1 ? 0x20000000u : 0u) | ((unsigned)(wx & 1) << 30) | ((unsigned)(wy & 1) << 31);
}
__host__ __device__ __forceinline__ constexpr unsigned dma_off(int lane) {
    const int col = (lane * 43) >> 8;
    return (unsigned)(col * (CH * 2 - 96) + 16 * lane) | (col == 0 ? 0x10000000u : 0u) | (col == 8 ? 0x20000000u : 0u);
}

__host__ __device__ __forceinline__ constexpr unsigned pf_off(int lane) {
    const int l = lane < 63 ? lane : 62, r = l / 21;
    return (unsigned)(r * ROWBYTES + (l - 21 * r) * 128) | (r == 0 ? 0x08000000u : 0u);
}
constexpr int RECN = 80;
struct Tables { unsigned rec[GRID][RECN]; unsigned img[768]; };
constexpr Tables make_tables() {
    Tables t = {};
    for (int bx = 0; bx < GRID; ++bx) {
        unsigned* r = t.rec[bx];
        const int q0 = (bx & 7) * 32 + (bx >> 3), cnt = q0 < NSHORT ? 20 : 21, start = NWIN - cnt - (q0 < NSHORT ? 20 * q0 : 21 * q0 - NSHORT);
        for (int i = 0; i < 32; ++i) r[i] = i < cnt ? wl_entry(start + i) : 0x7ffffffu;
        int wy = 0, wx = 0;
        seq_to_win(start, wy, wx);
        const int key0 = block_of(wy, wx);
        seq_to_win(start + cnt - 1, wy, wx);
        const int nblk = block_of(wy, wx) - key0 + 1;
        for (int b = 0; b < 8; ++b) {
            int ky = 0, kx = 0;
            block_pos(key0 + b, ky, kx);
            r[64 + b] = b < nblk ? (unsigned)(((7 * ky + 5) * IMG + 14 * kx + 5) * (CH * 2) + XBIAS) | (kx == 36 ? 1u : 0u) : OOB;
            for (int c = 0; c < 4; ++c) {
                const int y = ky + (c >> 1), x = 2 * kx + (c & 1), i = win_to_seq(y, x) - start;
                const bool ok = b < nblk && y < NWX && x < NWX && i >= 0 && i < cnt;
                r[32 + 4 * b + c] = ok ? (unsigned)(((7 * y + LPOS(y)) * IMG + 7 * x + LPOS(x)) * (CH * 2) + i) : OOB + 47;
            }
        }
        r[72] = q0; r[73] = cnt;
    }
    for (int lane = 0; lane < 64; ++lane) {
        for (int e = 0; e < 3; ++e) {
            const int tk = byte_of(TOKMAP[2 * e], TOKMAP[2 * e + 1], lane & 15);
            t.img[192 + 4 * lane + e] = (unsigned)((tk / 7) * ROWBYTES + (tk % 7) * (CH * 2)) + 16 * (lane >> 4);
        }
        t.img[448 + 4 * lane] = dma_off(lane);
        t.img[449 + 4 * lane] = pf_off(lane);
    }
    return t;
}
static __constant__ const Tables TAB = make_tables();
typedef __attribute__((address_space(1))) unsigned gu32;
typedef __attribute__((address_space(1))) unsigned long long gu64;
#define RLX_AGENT __ATOMIC_RELAXED, __HIP_MEMORY_SCOPE_AGENT
#define TAG_OF(e) (((unsigned long long)~((e) + 1u) << 32) | ((e) + 1u))

__global__ __launch_bounds__(512, 2) void win_attn_kernel(const unsigned short* __restrict__ x, const unsigned short* __restrict__ dw,
                                                          const unsigned short* __restrict__ wqkv, const unsigned short* __restrict__ wo,
                                                          u32x4* wf, unsigned short* __restrict__ out) {
    __shared__ __attribute__((aligned(16))) char smem[LDS_BYTES];

    asm volatile("wk_code_begin:");
    const int tid = threadIdx.x;
    const int wave = __builtin_amdgcn_readfirstlane(tid >> 6);
    const int w = wave & 3;
    const unsigned ldsb = (unsigned)(size_t)smem;
#define ROLE_SETUP                                                                                 \
    int bx = blockIdx.x;                                                                           \
    asm volatile("" : "+s"(bx));                                                                   \
    bx = __builtin_amdgcn_readfirstlane(bx);                                                       \
    const unsigned* rec = TAB.rec[bx];                                                             \
    const unsigned en0 = rec[0];                                                                   \
    const int q0 = rec[72], cnt = rec[73];
    asm volatile(
        "s_load_dwordx8 s[24:31], %[ka], 0x0\n\t"
        "s_load_dwordx2 s[32:33], %[ka], 0x20\n\t"
        "s_mul_i32 s36, %[bx], %[recb]\n\t"
        "s_xor_b32 s37, %[wave], 4\n\t"
        "s_lshl2_add_u32 s38, s37, s36\n\t"
        "s_load_dword s38, %[tab], s38 offset:0x100\n\t"
        "s_load_dword s39, %[tab], s36\n\t"
        "v_mbcnt_lo_u32_b32 v1, -1, 0\n\t"
        "v_mbcnt_hi_u32_b32 v1, -1, v1\n\t"
        "s_and_b32 s44, %[bx], 7\n\t"
        "s_lshl_b32 s44, s44, 5\n\t"
        "s_lshr_b32 s45, %[bx], 3\n\t"
        "s_add_u32 s44, s44, s45\n\t"
        "v_lshlrev_b32 v2, 4, v1\n\t"
        "s_mov_b32 s43, 0x20000\n\t"
        "s_cmp_ge_u32 s44, 36\n\t"
        "s_cbranch_scc1 .Lnp%=\n\t"
        "s_mul_i32 s45, s44, 6\n\t"
        "s_lshl1_add_u32 s46, s44, 210\n\t"
        "s_cmp_ge_u32 %[wave], 6\n\t"
        "s_cselect_b64 vcc, -1, 0\n\t"
        "s_cselect_b32 s45, s46, s45\n\t"
        "s_movk_i32 s52, 1152\n\t"
        "s_cselect_b32 s52, 384, s52\n\t"
        "s_cselect_b32 s53, 0, 0xbbaaaaab\n\t"
        "s_add_u32 s45, s45, %[wave]\n\t"
        "s_mul_i32 s46, s45, 171\n\t"
        "s_lshr_b32 s46, s46, 10\n\t"
        "s_mul_i32 s47, s46, 6\n\t"
        "s_sub_u32 s47, s45, s47\n\t"
        "s_mul_i32 s48, s46, 43\n\t"
        "s_lshr_b32 s48, s48, 8\n\t"
        "s_mul_i32 s49, s48, 6\n\t"
        "s_sub_u32 s49, s46, s49\n\t"
        "s_mul_i32 s50, s48, 48\n\t"
        "s_cmp_lt_u32 s49, 3\n\t"
        "s_cselect_b32 s34, 1.0, 0x3e553b94\n\t"
        "s_movk_i32 s51, -48\n\t"
        "s_cselect_b32 s51, 192, s51\n\t"
        "s_cmp_lt_u32 s48, 4\n\t"
        "s_cselect_b32 s51, s51, s50\n\t"
        "s_cselect_b32 s34, s34, 1.0\n\t"
        "s_add_u32 s51, s51, s50\n\t"
        "s_lshl4_add_u32 s51, s49, s51\n\t"
        "s_and_b32 s49, s49, 1\n\t"
        "s_mul_i32 s49, s49, 12\n\t"
        "s_add_u32 s49, s49, 576\n\t"
        "s_cmp_ge_u32 %[wave], 6\n\t"
        "s_cselect_b32 s49, s49, 0\n\t"
        "s_sub_u32 s51, s51, s49\n\t"
        "s_and_b32 s49, s47, 1\n\t"
        "s_mul_i32 s49, s49, 88\n\t"
        "s_lshl3_add_u32 s47, s47, s49\n\t"
        "v_lshrrev_b32 v3, 5, v1\n\t"
        "v_mul_u32_u24 v3, 48, v3\n\t"
        "v_lshrrev_b32 v4, 1, v1\n\t"
        "v_and_or_b32 v3, v4, 8, v3\n\t"
        "v_add_u32 v3, s47, v3\n\t"
        "v_mul_u32_u24 v3, s52, v3\n\t"
        "v_and_b32 v4, 15, v1\n\t"
        "v_and_b32 v5, 12, v1\n\t"
        "v_cndmask_b32 v5, 0, v5, vcc\n\t"
        "v_add3_u32 v5, v4, v5, s51\n\t"
        "v_lshl_add_u32 v3, v5, 1, v3\n\t"
        "v_mov_b64 v[8:9], 0\n\t"
        "v_mov_b64 v[10:11], 0\n\t"
        "v_mov_b64 v[12:13], 0\n\t"
        "v_mov_b64 v[14:15], 0\n\t"
        "s_mul_i32 s42, s52, 192\n\t"
        "s_cmp_ge_u32 %[wave], 6\n\t"
        "s_waitcnt lgkmcnt(0)\n\t"
        "s_cselect_b64 s[40:41], s[30:31], s[28:29]\n\t"
        "s_and_b32 s41, s41, 0xffff\n\t"
        "buffer_load_short_d16_hi v8, v3, s[40:43], 0 offen\n\t"
        "buffer_load_short_d16_hi v9, v3, s[40:43], s52 offen\n\t"
        "s_lshl_b32 s46, s52, 1\n\t"
        "buffer_load_short_d16_hi v10, v3, s[40:43], s46 offen\n\t"
        "s_add_u32 s46, s46, s52\n\t"
        "buffer_load_short_d16_hi v11, v3, s[40:43], s46 offen\n\t"
        "s_add_u32 s46, s46, s52\n\t"
        "buffer_load_short_d16_hi v12, v3, s[40:43], s46 offen\n\t"
        "s_add_u32 s46, s46, s52\n\t"
        "buffer_load_short_d16_hi v13, v3, s[40:43], s46 offen\n\t"
        "s_add_u32 s46, s46, s52\n\t"
        "buffer_load_short_d16_hi v14, v3, s[40:43], s46 offen\n\t"
        "s_add_u32 s46, s46, s52\n\t"
        "buffer_load_short_d16_hi v15, v3, s[40:43], s46 offen\n\t"
        "v_mov_b32 v6, %[ep]\n\t"
        "global_load_dword v7, v6, s[32:33] sc1\n\t"
        "v_mov_b32 v5, %[red]\n\t"
        "v_mad_u32_u24 v4, v4, %[c144], v5\n\t"
        "v_lshrrev_b32 v5, 2, v1\n\t"
        "v_and_b32 v5, 12, v5\n\t"
        "s_lshl_b32 s46, %[wave], 4\n\t"
        "v_add3_u32 v5, v4, v5, s46\n\t"
        "s_mov_b32 s40, s32\n\t"
        "s_and_b32 s41, s33, 0xffff\n\t"
        "s_mov_b32 s42, %[wsb]\n\t"
        "s_lshl_b32 s45, s45, 10\n\t"
        "s_waitcnt vmcnt(1)\n\t"
        "v_add_f32 v16, v8, v10\n\t"
        "v_add_f32 v17, v9, v11\n\t"
        "v_add_f32 v18, v12, v14\n\t"
        "v_add_f32 v19, v13, v15\n\t"
        "v_add_f32 v16, v16, v18\n\t"
        "v_add_f32 v17, v17, v19\n\t"
        "v_add_f32 v16, v16, v17\n\t"
        "ds_write_b32 v5, v16\n\t"
        "s_waitcnt lgkmcnt(0)\n\t"
        "s_barrier\n\t"
        "ds_read_b128 v[16:19], v4\n\t"
        "ds_read_b128 v[20:23], v4 offset:16\n\t"
        "ds_read_b128 v[24:27], v4 offset:32\n\t"
        "ds_read_b128 v[28:31], v4 offset:48\n\t"
        "ds_read_b128 v[32:35], v4 offset:64\n\t"
        "ds_read_b128 v[36:39], v4 offset:80\n\t"
        "s_waitcnt lgkmcnt(4)\n\t"
        "v_add_f32 v16, v16, v18\n\t"
        "v_add_f32 v17, v17, v19\n\t"
        "v_add_f32 v20, v20, v22\n\t"
        "v_add_f32 v21, v21, v23\n\t"
        "s_waitcnt lgkmcnt(2)\n\t"
        "v_add_f32 v24, v24, v26\n\t"
        "v_add_f32 v25, v25, v27\n\t"
        "v_add_f32 v28, v28, v30\n\t"
        "v_add_f32 v29, v29, v31\n\t"
        "v_add_f32 v16, v16, v20\n\t"
        "v_add_f32 v17, v17, v21\n\t"
        "s_waitcnt lgkmcnt(0)\n\t"
        "v_add_f32 v32, v32, v34\n\t"
        "v_add_f32 v33, v33, v35\n\t"
        "v_add_f32 v36, v36, v38\n\t"
        "v_add_f32 v37, v37, v39\n\t"
        "v_add_f32 v24, v24, v28\n\t"
        "v_add_f32 v25, v25, v29\n\t"
        "v_add_f32 v32, v32, v36\n\t"
        "v_add_f32 v33, v33, v37\n\t"
        "v_add_f32 v16, v16, v24\n\t"
        "v_add_f32 v17, v17, v25\n\t"
        "v_add_f32 v16, v16, v32\n\t"
        "v_add_f32 v17, v17, v33\n\t"
        "v_add_f32 v16, v16, v17\n\t"
        "v_mul_f32 v16, s53, v16\n\t"
        "v_mul_f32 v16, s34, v16\n\t"
        "v_fma_f32 v8, v8, s34, v16\n\t"
        "v_fma_f32 v9, v9, s34, v16\n\t"
        "v_fma_f32 v10, v10, s34, v16\n\t"
        "v_fma_f32 v11, v11, s34, v16\n\t"
        "v_fma_f32 v12, v12, s34, v16\n\t"
        "v_fma_f32 v13, v13, s34, v16\n\t"
        "v_fma_f32 v14, v14, s34, v16\n\t"
        "v_fma_f32 v15, v15, s34, v16\n\t"
        "v_cvt_pk_bf16_f32 v8, v8, v9\n\t"
        "v_cvt_pk_bf16_f32 v9, v10, v11\n\t"
        "v_cvt_pk_bf16_f32 v10, v12, v13\n\t"
        "v_cvt_pk_bf16_f32 v11, v14, v15\n\t"
        "buffer_store_dwordx4 v[8:11], v2, s[40:43], s45 offen sc1\n\t"
        "s_or_b32 s46, s44, s37\n\t"
        "s_cmp_lg_u32 s46, 0\n\t"
        "s_cbranch_scc1 .Lnc%=\n\t"
        "v_mov_b32 v3, 0\n\t"
        "global_store_dword v6, v3, s[32:33] offset:256 sc1\n\t"
        ".Lnc%=:\n\t"
        "v_lshl_add_u32 v6, s44, 3, v6\n\t"
        "s_waitcnt vmcnt(0)\n\t"
        "s_barrier\n\t"
        "s_cmp_lg_u32 %[wave], 0\n\t"
        "s_cbranch_scc1 .Lnp%=\n\t"
        "v_add_u32 v4, 1, v7\n\t"
        "v_not_b32 v5, v4\n\t"
        "s_mov_b64 exec, 1\n\t"
        "global_store_dwordx2 v6, v[4:5], s[32:33] offset:512 sc1\n\t"
        "s_mov_b64 exec, -1\n\t"
        ".Lnp%=:\n\t"
        "s_getpc_b64 s[40:41]\n\t"
        ".Lwk_here%=:\n\t"
        "s_sub_u32 s40, s40, .Lwk_here%=-wk_code_begin\n\t"
        "s_subb_u32 s41, s41, 0\n\t"
        "s_and_b32 s41, s41, 0xffff\n\t"
        "s_mov_b32 s42, wk_code_end-wk_code_begin\n\t"
        "s_max_u32 s42, s42, wk_code_end1-wk_code_begin\n\t"
        "s_max_u32 s42, s42, wk_code_end2-wk_code_begin\n\t"
        "s_add_u32 m0, %[lds], %[sink]\n\t"
        "v_lshlrev_b32 v3, 7, v1\n\t"
        "s_lshl_b32 s45, %[wave], 13\n\t"
        "buffer_load_dword v3, s[40:43], s45 offen lds\n\t"
        "s_mov_b64 s[40:41], %[tab]\n\t"
        "s_and_b32 s41, s41, 0xffff\n\t"
        "s_mov_b32 s42, %[tabb]\n\t"
        "s_lshl_b32 s46, %[wave], 10\n\t"
        "s_add_u32 s47, s46, %[imgs]\n\t"
        "s_add_u32 s48, s46, %[imgd]\n\t"
        "s_lshl_b32 s49, %[wave], 8\n\t"
        "s_add_u32 s49, s49, %[mapd]\n\t"
        "s_add_u32 s50, s36, 128\n\t"
        "s_mov_b64 vcc, -1\n\t"
        "s_cmp_eq_u32 %[wave], 0\n\t"
        "s_cselect_b32 s47, s36, s47\n\t"
        "s_cselect_b32 s48, %[wld], s48\n\t"
        "s_cselect_b64 vcc, 0xff, vcc\n\t"
        "s_cmp_ge_u32 %[wave], 4\n\t"
        "s_cselect_b32 s47, s50, s47\n\t"
        "s_cselect_b32 s48, s49, s48\n\t"
        "s_cselect_b64 exec, 0xff, vcc\n\t"
        "s_add_u32 m0, %[lds], s48\n\t"
        "s_and_b32 s49, %[wave], 3\n\t"
        "buffer_load_dwordx4 v2, s[40:43], s47 offen lds\n\t"
        "s_mov_b64 exec, -1\n\t"
        "v_mul_u32_u24 v3, 43, v1\n\t"
        "v_lshrrev_b32 v3, 8, v3\n\t"
        "v_mad_u32_u24 v4, v3, %[c288], v2\n\t"
        "s_mul_i32 s49, s49, 96\n\t"
        "s_mul_i32 s50, s37, 6160\n\t"
        "s_add_u32 s50, s50, %[lb0]\n\t"
        "s_cmp_eq_u32 s37, 7\n\t"
        "s_cselect_b32 s50, %[lb7], s50\n\t"
        "s_add_u32 s46, s46, %[tp]\n\t"
        "s_waitcnt lgkmcnt(0)\n\t"
        "s_cmp_ge_u32 %[wave], 4\n\t"
        "s_cbranch_scc1 .Lib%=\n\t"
        "s_mov_b32 s40, s26\n\t"
        "s_and_b32 s41, s27, 0xffff\n\t"
        "s_movk_i32 s42, 3456\n\t"
        "s_add_u32 m0, %[lds], s46\n\t"
        "s_nop 0\n\t"
        "buffer_load_dwordx4 v4, s[40:43], s49 offen lds\n\t"
        ".Lib%=:\n\t"
        "s_sub_u32 s40, s24, %[xb]\n\t"
        "s_subb_u32 s41, s25, 0\n\t"
        "s_and_b32 s41, s41, 0xffff\n\t"
        "s_mov_b32 s42, %[xsz]\n\t"
        "s_cmp_eq_u32 s38, 2.0\n\t"
        "s_cbranch_scc1 .Lic%=\n\t"
        "s_mov_b32 exec_hi, 0xffff\n\t"
        "s_add_u32 m0, %[lds], s50\n\t"
        "s_mov_b32 s46, 4\n\t"
        "s_bitcmp1_b32 s38, 0\n\t"
        "s_cselect_b32 s47, 2.0, s38\n\t"
        "s_bitset0_b32 s38, 0\n\t"
        ".Lid%=:\n\t"
        "buffer_load_dwordx4 v2, s[40:43], s38 offen lds\n\t"
        "buffer_load_dwordx4 v2, s[40:43], s47 offen offset:768 lds\n\t"
        "s_add_u32 m0, m0, 1536\n\t"
        "s_add_u32 s38, s38, %[rb]\n\t"
        "s_add_u32 s47, s47, %[rb]\n\t"
        "s_sub_u32 s46, s46, 1\n\t"
        "s_cmp_lg_u32 s46, 0\n\t"
        "s_cbranch_scc1 .Lid%=\n\t"
        ".Lic%=:\n\t"
        "s_cmp_lt_u32 %[wave], 4\n\t"
        "s_cbranch_scc1 .Lie%=\n\t"
        "s_mov_b32 exec_hi, 0x3fffff\n\t"
        "s_and_b32 s46, s39, 0x10000000\n\t"
        "s_and_b32 s47, s39, 0x20000000\n\t"
        "v_cmp_eq_u32 vcc, 0, v3\n\t"
        "v_mov_b32 v5, s46\n\t"
        "v_mov_b32 v6, s47\n\t"
        "v_cndmask_b32 v5, 0, v5, vcc\n\t"
        "v_cmp_eq_u32 vcc, 8, v3\n\t"
        "s_and_b32 s46, s39, 0x7ffffff\n\t"
        "s_add_u32 s46, s46, s49\n\t"
        "v_cndmask_b32 v6, 0, v6, vcc\n\t"
        "v_or3_b32 v4, v4, v5, v6\n\t"
        "s_bitcmp1_b32 s39, 27\n\t"
        "s_cselect_b32 s47, 2.0, s46\n\t"
        "s_lshl_b32 s45, s37, 13\n\t"
        "s_add_u32 m0, %[lds], s45\n\t"
        "s_mov_b32 s48, 8\n\t"
        "buffer_load_dwordx4 v4, s[40:43], s47 offen lds\n\t"
        ".Lia%=:\n\t"
        "s_add_u32 m0, m0, 872\n\t"
        "s_add_u32 s46, s46, %[rb]\n\t"
        "s_sub_u32 s48, s48, 1\n\t"
        "buffer_load_dwordx4 v4, s[40:43], s46 offen lds\n\t"
        "s_cmp_lg_u32 s48, 0\n\t"
        "s_cbranch_scc1 .Lia%=\n\t"
        ".Lie%=:\n\t"
        "s_mov_b64 exec, -1\n\t"
        :: [ka] "s"(__builtin_amdgcn_kernarg_segment_ptr()), [bx] "s"(blockIdx.x), [wave] "s"(wave), [tab] "s"(&TAB), [lds] "s"(ldsb),
           [red] "s"(ldsb + O_OFF), [c144] "s"(144), [c288] "s"(288), [recb] "n"(RECN * 4), [ep] "n"(EP_OFF), [wsb] "n"(WS_BYTES), [sink] "n"(X_OFF + 18432),
           [tabb] "n"(sizeof(Tables)), [imgs] "n"(GRID * RECN * 4 - 1024), [imgd] "n"(AT_OFF + 18816 - 1024), [mapd] "n"(TP_OFF + 3072), [wld] "n"(WL_OFF),
           [lb0] "n"(LB_OFF(0)), [lb7] "n"(LB_OFF(7)), [tp] "n"(TP_OFF), [xb] "n"(XBIAS), [xsz] "n"(IMG * IMG * CH * 2 + XBIAS), [rb] "n"(ROWBYTES)
        : "v1", "v2", "v3", "v4", "v5", "v6", "v7", "v8", "v9", "v10", "v11", "v12", "v13", "v14", "v15", "v16", "v17", "v18", "v19", "v20", "v21", "v22", "v23", "v24", "v25", "v26", "v27", "v28", "v29", "v30", "v31", "v32", "v33", "v34", "v35", "v36", "v37", "v38", "v39", "s24", "s25", "s26", "s27", "s28", "s29", "s30", "s31", "s32", "s33", "s34", "s36", "s37", "s38", "s39", "s40", "s41", "s42", "s43", "s44", "s45", "s46", "s47", "s48", "s49", "s50", "s51", "s52", "s53", "m0", "vcc", "scc", "memory");
#define WAIT_GO                                                                                    \
        {                                                                                          \
            unsigned go_;                                                                          \
            asm volatile(".Lgo%=:\n\tds_read_b32 %0, %1\n\ts_waitcnt lgkmcnt(0)\n\tv_cmp_ne_u32 vcc, -1, %0\n\ts_nop 4\n\ts_cbranch_vccnz .Lgo%=" \
                         : "=&v"(go_) : "v"(ldsb + GO_OFF) : "vcc", "memory");                     \
        }
#define LANE_SETUP                                                                                 \
    int lane;                                                                                      \
    asm volatile("v_mbcnt_lo_u32_b32 %0, -1, 0\n\tv_mbcnt_hi_u32_b32 %0, -1, %0" : "=v"(lane));    \
    __builtin_assume(lane >= 0 && lane < 64);                                                      \
    const int g = lane >> 4, l15 = lane & 15, sw = l15 >> 1;                                       \
    int fE = X_OFF + l15 * 384 + ((g ^ sw) << 4);                                                  \
    OPQ(fE);
#define XPAT(r) (((r) < 48 ? (r) : 48) * 384 + (((2 * w) ^ (((r) >> 1) & 7)) << 4))
#define SPAT(r) ((w >> 1) * 1024 + (w & 1) * 8 + (r) * 16)
#define CONV_CONSTS(var) \
            const unsigned long long rlo = g == 0 ? ROWMAP[0] : (g == 1 ? ROWMAP[2] : (g == 2 ? ROWMAP[4] : ROWMAP[6])); \
            const unsigned long long rhi = g == 0 ? ROWMAP[1] : (g == 1 ? ROWMAP[3] : (g == 2 ? ROWMAP[5] : ROWMAP[7])); \
            int xrow = byte_of(rlo, rhi, l15); \
            if (lane == (int)((CLANE >> (8 * (var))) & 0xFF)) xrow = 48; \
            if (lane == 63) xrow = (CROW >> (8 * (var))) & 0xFF; \
            const unsigned pXw_ = XPAT(xrow); \
            const unsigned pSTw_ = (w >> 1) * 1024 + (w & 1) * 8 + xrow * 16; \
            const int oy = (0x5320 >> (4 * (l15 >> 2))) & 15, ox = (0x5320 >> (4 * (l15 & 3))) & 15; \
            const unsigned aB_ = ldsb + w * 8192 + (oy + (g >> 1)) * 872 + (ox + 2 * (g & 1)) * 96; \
            const int dyi = g - (l15 >> 3); \
            const unsigned aA_ = ldsb + AT_OFF + (l15 & 3) * 16 + \
                                 ((dyi < 0 || dyi > 2) ? 18816 : (dyi == 0 ? 0 : (dyi == 1 ? 6208 : 12544)) + ((l15 >> 2) & 1) * 3072 + w * 768); \
            const unsigned idx_ = (l15 & 3) * 0x5555u; \
            const unsigned se = (2 || l15 == 4 * g) ? 0x3F803F80u : 0u; \
            const u32x4 sel4 = {se, se, se, se};

    if (wave < 4) {
        ROLE_SETUP
        LANE_SETUP
        asm volatile("s_waitcnt vmcnt(0)" ::: "memory");
        if (lane < 48) {
            unsigned tp[9];
#pragma unroll
            for (int k = 0; k < 9; ++k) tp[k] = *(const unsigned short*)(smem + TP_OFF + w * 1024 + k * 96 + lane * 2);
            tp[4] |= 0x3F800000u;
#pragma unroll
            for (int dy = 0; dy < 3; ++dy) {
                char* p = smem + AT_OFF + (dy == 0 ? 0 : (dy == 1 ? 6208 : 12544)) + w * 768 + lane * 16;
                *(u32x4*)p = (u32x4){tp[3 * dy], tp[3 * dy + 1], tp[3 * dy + 2], 0u};
                *(u32x4*)(p + 3072) = (u32x4){0u, tp[3 * dy], tp[3 * dy + 1], tp[3 * dy + 2]};
            }
        }
        BAR();
        if (1) {
            const auto xrsrc = __builtin_amdgcn_make_buffer_rsrc((void*)((const char*)x - XBIAS), 0, IMG * IMG * CH * 2 + XBIAS, 0x00020000);
            const unsigned en1 = rec[1];
            const int li = w * 64 + lane, r = (li * 2428) >> 16;
            const unsigned vo = (r > 8 || (r == 0 && (en1 & 0x08000000u))) ? OOB : r * ROWBYTES + (li - 27 * r) * 128;
            asm volatile("s_mov_b32 m0, %0\n\ts_nop 0\n\tbuffer_load_dword %1, %2, %3 offen lds"
                         :: "s"(ldsb + X_OFF + 18432), "v"(vo), "s"(xrsrc), "s"(en1 & 0x7ffffffu) : "m0", "memory");
        }
        {
            const int dW = X_OFF + l15 * 384 + (((2 * w + (g >> 1)) ^ sw) << 4) + 8 * (g & 1);
            const unsigned rsb = ldsb + RS_BASE;
            const unsigned pE_ = fE - X_OFF, aO_ = ldsb + dW + (O_OFF - X_OFF), aZ_ = ldsb + AT_OFF + 18816, aVB_ = ldsb + V_OFF + w * 4608 + 1024 + lane * 8;
            unsigned aRSw_ = rsb + lane * 4;
            const unsigned pST_ = lane * 16, aPLw_ = rsb + lane * 2, aRSr_ = rsb + l15 * 4;
            const unsigned g16_ = g * 16, aPLr_ = (l15 & 3) ? aZ_ : rsb + g * 8;
            const unsigned loVL_ = ((2 * w + (l15 >> 3)) << 4) + (l15 & 7) * 2, loOL_ = ((2 * w + (g >> 1)) << 4) + 8 * (g & 1);
            const unsigned on = (l15 & 3) ? 0u : 0x3F803F80u;
            const u32x4 ones4 = {on, on, on, on};
            const unsigned snk = ldsb + O_OFF + 15360 + w * 192, kr = ldsb + KQL_OFF + l15 * 800;
            const bool ok1 = 16 + l15 < cnt, dg = g == (l15 >> 2);
            u32x4 pl = {kr + w * 192 + g * 16, (ok1 ? kr + 12800 + w * 192 : snk) + g * 16, dg ? kr + 768 + w * 4 : snk, dg && ok1 ? kr + 12800 + 768 + w * 4 : snk};
            unsigned pcl, pch, len;
            asm volatile("s_getpc_b64 s[36:37]\n.Lwk_h2%=:\n\ts_sub_u32 s36, s36, .Lwk_h2%=-wk_code_begin\n\ts_subb_u32 s37, s37, 0\n\t"
                         "s_mov_b32 %2, wk_code_end-wk_code_begin\n\ts_max_u32 %2, %2, wk_code_end1-wk_code_begin" : "={s36}"(pcl), "={s37}"(pch), "=s"(len) :: "scc");
            pcl = __builtin_amdgcn_readfirstlane(pcl); pch = __builtin_amdgcn_readfirstlane(pch); len = __builtin_amdgcn_readfirstlane(len);
            const unsigned long long tb = w == 0 ? ((unsigned long long)pch << 32) | pcl : (unsigned long long)wf;
            const u32x4 tr = {(unsigned)tb, (unsigned)(tb >> 32) & 0xffffu, w == 0 ? len : (w == 1 ? (unsigned)WF_BYTES : SH_OFF + bx * 12288 + 12288), 0x00020000u};
            const unsigned to = w == 0 ? ((bx >> 3) % 6) * 8192 : (w == 1 ? 6 * 36864 + ((bx >> 3) % 9) * 8192 : SH_OFF + bx * 12288 + (w - 2) * 8192);
            asm volatile("" :: "{v244}"(pE_), "{v245}"(aO_), "{v246}"(aZ_), "{v247}"(aVB_), "{v248}"(pST_), "{v249}"(aRSw_), "{v250}"(aPLw_), "{v251}"(aRSr_), "{v252}"(g16_), "{v253}"(aPLr_), "{v254}"(loVL_), "{v255}"(loOL_), "{v[240:243]}"(ones4), "{v[232:235]}"(pl), "{s[36:39]}"(tr), "{s41}"(to));
            if (w == 0) {
                gu64* fp = (gu64*)((char*)wf + (lane < 36 ? FL_OFF + lane * 8 : EP_OFF));
#pragma unroll 1
                for (int it = 0; it < SPIN_MAX; ++it) {
                    const unsigned long long f = __hip_atomic_load(fp, RLX_AGENT);
                    const unsigned ep = __builtin_amdgcn_readlane((unsigned)f, 63);
                    if ((__builtin_amdgcn_ballot_w64(f != TAG_OF(ep)) & 0xfffffffffull) == 0ull) break;
                    __builtin_amdgcn_s_sleep(1);
                }
                __builtin_amdgcn_fence(__ATOMIC_ACQUIRE, "wavefront");
                asm volatile("ds_write_b32 %0, %1" :: "v"(ldsb + GO_OFF), "v"(~0u) : "memory");
            } else WAIT_GO
            __builtin_amdgcn_s_setprio(2);
            unsigned s44 = cnt, s48 = ldsb + X_OFF, s70 = 0;
            unsigned s72 = ldsb + KQL_OFF + w * 192, s73 = ldsb + OL_OFF, s77 = ldsb + OL_OFF, s76 = ldsb + KQL_OFF + 768 + w * 4;
            const unsigned long long wb = (unsigned long long)wf;
            const u32x4 wr = {(unsigned)wb, (unsigned)(wb >> 32) & 0xffffu, (unsigned)WS_BYTES, 0x00020000u};
            asm volatile(
            "s_setprio 2\n\t"
            "s_add_u32 s49, s33, 0\n\t"
            "buffer_load_dwordx4 v[0:3], v248, s[28:31], s49 offen sc1\n\t"
            "s_add_u32 s49, s33, 1024\n\t"
            "buffer_load_dwordx4 v[4:7], v248, s[28:31], s49 offen sc1\n\t"
            "s_add_u32 s49, s33, 2048\n\t"
            "buffer_load_dwordx4 v[8:11], v248, s[28:31], s49 offen sc1\n\t"
            "s_add_u32 s49, s33, 3072\n\t"
            "buffer_load_dwordx4 v[12:15], v248, s[28:31], s49 offen sc1\n\t"
            "s_add_u32 s49, s33, 4096\n\t"
            "buffer_load_dwordx4 v[16:19], v248, s[28:31], s49 offen sc1\n\t"
            "s_add_u32 s49, s33, 5120\n\t"
            "buffer_load_dwordx4 v[20:23], v248, s[28:31], s49 offen sc1\n\t"
            "s_add_u32 s49, s33, 6144\n\t"
            "buffer_load_dwordx4 v[24:27], v248, s[28:31], s49 offen sc1\n\t"
            "s_add_u32 s49, s33, 7168\n\t"
            "buffer_load_dwordx4 v[28:31], v248, s[28:31], s49 offen sc1\n\t"
            "s_add_u32 s49, s33, 8192\n\t"
            "buffer_load_dwordx4 v[32:35], v248, s[28:31], s49 offen sc1\n\t"
            "s_add_u32 s49, s33, 9216\n\t"
            "buffer_load_dwordx4 v[36:39], v248, s[28:31], s49 offen sc1\n\t"
            "s_add_u32 s49, s33, 10240\n\t"
            "buffer_load_dwordx4 v[40:43], v248, s[28:31], s49 offen sc1\n\t"
            "s_add_u32 s49, s33, 11264\n\t"
            "buffer_load_dwordx4 v[44:47], v248, s[28:31], s49 offen sc1\n\t"
            "s_add_u32 s49, s33, 12288\n\t"
            "buffer_load_dwordx4 v[48:51], v248, s[28:31], s49 offen sc1\n\t"
            "s_add_u32 s49, s33, 13312\n\t"
            "buffer_load_dwordx4 v[52:55], v248, s[28:31], s49 offen sc1\n\t"
            "s_add_u32 s49, s33, 14336\n\t"
            "buffer_load_dwordx4 v[56:59], v248, s[28:31], s49 offen sc1\n\t"
            "s_add_u32 s49, s33, 15360\n\t"
            "buffer_load_dwordx4 v[60:63], v248, s[28:31], s49 offen sc1\n\t"
            "v_mov_b32 v224, s46\n\t"
            ".Ls1%=:\n\t"
            "ds_read_b128 v[220:223], v224\n\t"
            "s_waitcnt lgkmcnt(0)\n\t"
            "v_and_b32 v220, v220, v221\n\t"
            "v_and_b32 v222, v222, v223\n\t"
            "v_and_b32 v220, v220, v222\n\t"
            "s_nop 0\n\t"
            "v_readfirstlane_b32 s49, v220\n\t"
            "s_nop 3\n\t"
            "s_bitcmp1_b32 s49, 27\n\t"
            "s_cbranch_scc0 .Ls1%=\n\t"
            "s_nop 7\n\t"
            "v_add_u32 v237, s35, v248\n\t"
            "v_add_u32 v238, s56, v244\n\t"
            "v_xor_b32 v239, 64, v238\n\t"
            "v_add_u32 v236, s56, v248\n\t"
            "ds_read_b128 v[200:203], v237\n\t"
            "ds_read_b128 v[204:207], v237 offset:1024\n\t"
            "ds_read_b128 v[144:147], v238\n\t"
            "ds_read_b128 v[148:151], v239\n\t"
            "ds_read_b128 v[152:155], v238 offset:128\n\t"
            "ds_read_b128 v[156:159], v239 offset:128\n\t"
            "ds_read_b128 v[160:163], v238 offset:256\n\t"
            "ds_read_b128 v[164:167], v239 offset:256\n\t"
            "ds_read_b128 v[168:171], v238 offset:6144\n\t"
            "ds_read_b128 v[172:175], v239 offset:6144\n\t"
            "ds_read_b128 v[176:179], v238 offset:6272\n\t"
            "ds_read_b128 v[180:183], v239 offset:6272\n\t"
            "ds_read_b128 v[184:187], v238 offset:6400\n\t"
            "ds_read_b128 v[188:191], v239 offset:6400\n\t"
            "ds_read_b128 v[208:211], v237 offset:2048\n\t"
            "s_waitcnt lgkmcnt(14)\n\t"
            "ds_read_b128 v[224:227], v236 offset:12288\n\t"
            "s_waitcnt lgkmcnt(14)\n\t"
            "ds_read_b128 v[228:231], v236 offset:13312\n\t"
            "s_add_u32 s49, s33, 16384\n\t"
            "buffer_load_dwordx4 v[64:67], v248, s[28:31], s49 offen sc1\n\t"
            "s_add_u32 s49, s33, 17408\n\t"
            "buffer_load_dwordx4 v[68:71], v248, s[28:31], s49 offen sc1\n\t"
            "s_waitcnt vmcnt(12)\n\t"
            "s_waitcnt lgkmcnt(14)\n\t"
            "v_mfma_f32_16x16x32_bf16 v[192:195], v[0:3], v[144:147], 0\n\t"
            "buffer_store_dwordx4 v[200:203], v248, s[28:31], s34 offen\n\t"
            "s_waitcnt lgkmcnt(8)\n\t"
            "v_mfma_f32_16x16x32_bf16 v[212:215], v[0:3], v[168:171], 0\n\t"
            "buffer_store_dwordx4 v[204:207], v248, s[28:31], s34 offen offset:1024\n\t"
            "v_mfma_f32_16x16x32_bf16 v[192:195], v[4:7], v[148:151], v[192:195]\n\t"
            "s_waitcnt lgkmcnt(2)\n\t"
            "buffer_store_dwordx4 v[208:211], v248, s[28:31], s34 offen offset:2048\n\t"
            "v_mfma_f32_16x16x32_bf16 v[212:215], v[4:7], v[172:175], v[212:215]\n\t"
            "s_add_u32 s49, s33, 18432\n\t"
            "buffer_load_dwordx4 v[72:75], v248, s[28:31], s49 offen sc1\n\t"
            "v_mfma_f32_16x16x32_bf16 v[192:195], v[8:11], v[152:155], v[192:195]\n\t"
            "s_add_u32 s49, s33, 19456\n\t"
            "buffer_load_dwordx4 v[76:79], v248, s[28:31], s49 offen sc1\n\t"
            "v_mfma_f32_16x16x32_bf16 v[212:215], v[8:11], v[176:179], v[212:215]\n\t"
            "s_add_u32 s49, s33, 20480\n\t"
            "buffer_load_dwordx4 v[80:83], v248, s[28:31], s49 offen sc1\n\t"
            "v_mfma_f32_16x16x32_bf16 v[192:195], v[12:15], v[156:159], v[192:195]\n\t"
            "s_add_u32 s49, s33, 21504\n\t"
            "buffer_load_dwordx4 v[84:87], v248, s[28:31], s49 offen sc1\n\t"
            "v_mfma_f32_16x16x32_bf16 v[212:215], v[12:15], v[180:183], v[212:215]\n\t"
            "s_add_u32 s49, s33, 22528\n\t"
            "buffer_load_dwordx4 v[88:91], v248, s[28:31], s49 offen sc1\n\t"
            "v_mfma_f32_16x16x32_bf16 v[192:195], v[16:19], v[160:163], v[192:195]\n\t"
            "s_add_u32 s49, s33, 23552\n\t"
            "buffer_load_dwordx4 v[92:95], v248, s[28:31], s49 offen sc1\n\t"
            "v_mfma_f32_16x16x32_bf16 v[212:215], v[16:19], v[184:187], v[212:215]\n\t"
            "s_waitcnt lgkmcnt(1)\n\t"
            "v_add_f32 v224, v224, v226\n\t"
            "s_waitcnt lgkmcnt(0)\n\t"
            "v_add_f32 v228, v228, v230\n\t"
            "v_mfma_f32_16x16x32_bf16 v[192:195], v[20:23], v[164:167], v[192:195]\n\t"
            "v_add_f32 v224, v224, v228\n\t"
            "v_add_f32 v225, v225, v227\n\t"
            "v_mfma_f32_16x16x32_bf16 v[212:215], v[20:23], v[188:191], v[212:215]\n\t"
            "s_waitcnt vmcnt(15)\n\t"
            "s_add_u32 s49, s33, 24576\n\t"
            "buffer_load_dwordx4 v[96:99], v248, s[28:31], s49 offen sc1\n\t"
            "v_mfma_f32_16x16x32_bf16 v[196:199], v[24:27], v[144:147], 0\n\t"
            "s_add_u32 s49, s33, 25600\n\t"
            "buffer_load_dwordx4 v[100:103], v248, s[28:31], s49 offen sc1\n\t"
            "v_mfma_f32_16x16x32_bf16 v[216:219], v[24:27], v[168:171], 0\n\t"
            "s_add_u32 s49, s33, 26624\n\t"
            "buffer_load_dwordx4 v[104:107], v248, s[28:31], s49 offen sc1\n\t"
            "v_mfma_f32_16x16x32_bf16 v[196:199], v[28:31], v[148:151], v[196:199]\n\t"
            "s_add_u32 s49, s33, 27648\n\t"
            "buffer_load_dwordx4 v[108:111], v248, s[28:31], s49 offen sc1\n\t"
            "v_mfma_f32_16x16x32_bf16 v[216:219], v[28:31], v[172:175], v[216:219]\n\t"
            "s_add_u32 s49, s33, 28672\n\t"
            "buffer_load_dwordx4 v[112:115], v248, s[28:31], s49 offen sc1\n\t"
            "v_mfma_f32_16x16x32_bf16 v[196:199], v[32:35], v[152:155], v[196:199]\n\t"
            "s_add_u32 s49, s33, 29696\n\t"
            "buffer_load_dwordx4 v[116:119], v248, s[28:31], s49 offen sc1\n\t"
            "v_mfma_f32_16x16x32_bf16 v[216:219], v[32:35], v[176:179], v[216:219]\n\t"
            "v_add_f32 v229, v229, v231\n\t"
            "v_mul_f32 v224, 0x3baaaaab, v224\n\t"
            "v_mfma_f32_16x16x32_bf16 v[196:199], v[36:39], v[156:159], v[196:199]\n\t"
            "v_add_f32 v225, v225, v229\n\t"
            "v_mul_f32_e64 v224, v224, -v224\n\t"
            "v_mfma_f32_16x16x32_bf16 v[216:219], v[36:39], v[180:183], v[216:219]\n\t"
            "v_fmac_f32 v224, 0x3baaaaab, v225\n\t"
            "v_max_f32 v224, 0, v224\n\t"
            "v_mfma_f32_16x16x32_bf16 v[196:199], v[40:43], v[160:163], v[196:199]\n\t"
            "v_add_f32 v224, 0x3727c5ac, v224\n\t"
            "v_rsq_f32 v224, v224\n\t"
            "v_mfma_f32_16x16x32_bf16 v[216:219], v[40:43], v[184:187], v[216:219]\n\t"
            "ds_write_b32 v249, v224\n\t"
            "v_mfma_f32_16x16x32_bf16 v[196:199], v[44:47], v[164:167], v[196:199]\n\t"
            "ds_read_b32 v236, v251\n\t"
            "v_mfma_f32_16x16x32_bf16 v[216:219], v[44:47], v[188:191], v[216:219]\n\t"
            "ds_read_b32 v237, v251 offset:64\n\t"
            "s_waitcnt vmcnt(15)\n\t"
            "v_mfma_f32_16x16x32_bf16 v[200:203], v[48:51], v[144:147], 0\n\t"
            "s_add_u32 s49, s33, 30720\n\t"
            "buffer_load_dwordx4 v[120:123], v248, s[28:31], s49 offen sc1\n\t"
            "v_mfma_f32_16x16x32_bf16 v[220:223], v[48:51], v[168:171], 0\n\t"
            "s_add_u32 s49, s33, 31744\n\t"
            "buffer_load_dwordx4 v[124:127], v248, s[28:31], s49 offen sc1\n\t"
            "v_mfma_f32_16x16x32_bf16 v[200:203], v[52:55], v[148:151], v[200:203]\n\t"
            "s_add_u32 s49, s33, 32768\n\t"
            "buffer_load_dwordx4 v[128:131], v248, s[28:31], s49 offen sc1\n\t"
            "v_mfma_f32_16x16x32_bf16 v[220:223], v[52:55], v[172:175], v[220:223]\n\t"
            "s_add_u32 s49, s33, 33792\n\t"
            "buffer_load_dwordx4 v[132:135], v248, s[28:31], s49 offen sc1\n\t"
            "v_mfma_f32_16x16x32_bf16 v[200:203], v[56:59], v[152:155], v[200:203]\n\t"
            "s_add_u32 s49, s33, 34816\n\t"
            "buffer_load_dwordx4 v[136:139], v248, s[28:31], s49 offen sc1\n\t"
            "v_mfma_f32_16x16x32_bf16 v[220:223], v[56:59], v[176:179], v[220:223]\n\t"
            "s_add_u32 s49, s33, 35840\n\t"
            "buffer_load_dwordx4 v[140:143], v248, s[28:31], s49 offen sc1\n\t"
            "s_setprio 0\n\t"
            "v_mfma_f32_16x16x32_bf16 v[200:203], v[60:63], v[156:159], v[200:203]\n\t"
            "s_waitcnt lgkmcnt(1)\n\t"
            "v_mul_f32 v192, v236, v192\n\t"
            "v_mul_f32 v193, v236, v193\n\t"
            "v_mfma_f32_16x16x32_bf16 v[220:223], v[60:63], v[180:183], v[220:223]\n\t"
            "s_waitcnt lgkmcnt(0)\n\t"
            "v_mul_f32 v212, v237, v212\n\t"
            "v_mul_f32 v213, v237, v213\n\t"
            "v_mfma_f32_16x16x32_bf16 v[200:203], v[64:67], v[160:163], v[200:203]\n\t"
            "v_mul_f32 v194, v236, v194\n\t"
            "v_mul_f32 v195, v236, v195\n\t"
            "v_mfma_f32_16x16x32_bf16 v[220:223], v[64:67], v[184:187], v[220:223]\n\t"
            "v_cvt_pk_bf16_f32 v192, v192, v193\n\t"
            "v_mul_f32 v214, v237, v214\n\t"
            "v_mfma_f32_16x16x32_bf16 v[200:203], v[68:71], v[164:167], v[200:203]\n\t"
            "v_mul_f32 v215, v237, v215\n\t"
            "v_cvt_pk_bf16_f32 v212, v212, v213\n\t"
            "v_mfma_f32_16x16x32_bf16 v[220:223], v[68:71], v[188:191], v[220:223]\n\t"
            "s_waitcnt vmcnt(12)\n\t"
            "v_cvt_pk_bf16_f32 v193, v194, v195\n\t"
            "v_mfma_f32_16x16x32_bf16 v[204:207], v[72:75], v[144:147], 0\n\t"
            "v_cvt_pk_bf16_f32 v213, v214, v215\n\t"
            "v_mul_f32 v196, v236, v196\n\t"
            "v_mfma_f32_16x16x32_bf16 v[224:227], v[72:75], v[168:171], 0\n\t"
            "v_mul_f32 v197, v236, v197\n\t"
            "v_mul_f32 v198, v236, v198\n\t"
            "v_mfma_f32_16x16x32_bf16 v[204:207], v[76:79], v[148:151], v[204:207]\n\t"
            "v_mul_f32 v199, v236, v199\n\t"
            "v_mul_f32 v216, v237, v216\n\t"
            "v_mfma_f32_16x16x32_bf16 v[224:227], v[76:79], v[172:175], v[224:227]\n\t"
            "v_mul_f32 v217, v237, v217\n\t"
            "v_mul_f32 v218, v237, v218\n\t"
            "v_mfma_f32_16x16x32_bf16 v[204:207], v[80:83], v[152:155], v[204:207]\n\t"
            "v_mul_f32 v219, v237, v219\n\t"
            "v_cvt_pk_bf16_f32 v194, v196, v197\n\t"
            "v_mfma_f32_16x16x32_bf16 v[224:227], v[80:83], v[176:179], v[224:227]\n\t"
            "v_cvt_pk_bf16_f32 v195, v198, v199\n\t"
            "v_cvt_pk_bf16_f32 v214, v216, v217\n\t"
            "v_mfma_f32_16x16x32_bf16 v[204:207], v[84:87], v[156:159], v[204:207]\n\t"
            "v_cvt_pk_bf16_f32 v215, v218, v219\n\t"
            "v_mul_f32 v200, v236, v200\n\t"
            "v_mfma_f32_16x16x32_bf16 v[224:227], v[84:87], v[180:183], v[224:227]\n\t"
            "v_mul_f32 v201, v236, v201\n\t"
            "v_mul_f32 v220, v237, v220\n\t"
            "v_mfma_f32_16x16x32_bf16 v[204:207], v[88:91], v[160:163], v[204:207]\n\t"
            "v_mul_f32 v221, v237, v221\n\t"
            "v_mul_f32 v202, v236, v202\n\t"
            "v_mfma_f32_16x16x32_bf16 v[224:227], v[88:91], v[184:187], v[224:227]\n\t"
            "v_mul_f32 v203, v236, v203\n\t"
            "v_cvt_pk_bf16_f32 v200, v200, v201\n\t"
            "v_mfma_f32_16x16x32_bf16 v[204:207], v[92:95], v[164:167], v[204:207]\n\t"
            "v_mul_f32 v222, v237, v222\n\t"
            "v_mul_f32 v223, v237, v223\n\t"
            "v_mfma_f32_16x16x32_bf16 v[224:227], v[92:95], v[188:191], v[224:227]\n\t"
            "s_waitcnt vmcnt(6)\n\t"
            "v_cvt_pk_bf16_f32 v220, v220, v221\n\t"
            "v_mfma_f32_16x16x32_bf16 v[196:199], v[96:99], v[144:147], 0\n\t"
            "v_cvt_pk_bf16_f32 v201, v202, v203\n\t"
            "v_cvt_pk_bf16_f32 v221, v222, v223\n\t"
            "v_mfma_f32_16x16x32_bf16 v[216:219], v[96:99], v[168:171], 0\n\t"
            "v_mul_f32 v204, v236, v204\n\t"
            "v_mul_f32 v205, v236, v205\n\t"
            "v_mfma_f32_16x16x32_bf16 v[196:199], v[100:103], v[148:151], v[196:199]\n\t"
            "v_mul_f32 v224, v237, v224\n\t"
            "v_mul_f32 v225, v237, v225\n\t"
            "v_mfma_f32_16x16x32_bf16 v[216:219], v[100:103], v[172:175], v[216:219]\n\t"
            "v_mul_f32 v206, v236, v206\n\t"
            "v_mul_f32 v207, v236, v207\n\t"
            "v_mfma_f32_16x16x32_bf16 v[196:199], v[104:107], v[152:155], v[196:199]\n\t"
            "v_cvt_pk_bf16_f32 v204, v204, v205\n\t"
            "v_mul_f32 v226, v237, v226\n\t"
            "v_mfma_f32_16x16x32_bf16 v[216:219], v[104:107], v[176:179], v[216:219]\n\t"
            "v_mul_f32 v227, v237, v227\n\t"
            "v_cvt_pk_bf16_f32 v224, v224, v225\n\t"
            "v_mfma_f32_16x16x32_bf16 v[196:199], v[108:111], v[156:159], v[196:199]\n\t"
            "v_cvt_pk_bf16_f32 v205, v206, v207\n\t"
            "v_cvt_pk_bf16_f32 v225, v226, v227\n\t"
            "v_mfma_f32_16x16x32_bf16 v[216:219], v[108:111], v[180:183], v[216:219]\n\t"
            "ds_write_b128 v232, v[192:195]\n\t"
            "v_mfma_f32_16x16x32_bf16 v[196:199], v[112:115], v[160:163], v[196:199]\n\t"
            "ds_write_b128 v233, v[212:215]\n\t"
            "v_mfma_f32_16x16x32_bf16 v[216:219], v[112:115], v[184:187], v[216:219]\n\t"
            "v_mfma_f32_16x16x32_bf16 v[196:199], v[116:119], v[164:167], v[196:199]\n\t"
            "v_mfma_f32_16x16x32_bf16 v[216:219], v[116:119], v[188:191], v[216:219]\n\t"
            "s_waitcnt vmcnt(0)\n\t"
            "v_mfma_f32_16x16x32_bf16 v[208:211], v[120:123], v[144:147], 0\n\t"
            "v_mfma_f32_16x16x32_bf16 v[228:231], v[120:123], v[168:171], 0\n\t"
            "v_mfma_f32_16x16x32_bf16 v[208:211], v[124:127], v[148:151], v[208:211]\n\t"
            "v_mfma_f32_16x16x32_bf16 v[228:231], v[124:127], v[172:175], v[228:231]\n\t"
            "s_nop 1\n\t"
            "v_mul_f32 v196, v236, v196\n\t"
            "v_mfma_f32_16x16x32_bf16 v[208:211], v[128:131], v[152:155], v[208:211]\n\t"
            "v_mul_f32 v197, v236, v197\n\t"
            "v_mul_f32 v198, v236, v198\n\t"
            "v_mfma_f32_16x16x32_bf16 v[228:231], v[128:131], v[176:179], v[228:231]\n\t"
            "v_mul_f32 v199, v236, v199\n\t"
            "v_mul_f32 v216, v237, v216\n\t"
            "v_mfma_f32_16x16x32_bf16 v[208:211], v[132:135], v[156:159], v[208:211]\n\t"
            "v_mul_f32 v217, v237, v217\n\t"
            "v_mul_f32 v218, v237, v218\n\t"
            "v_mfma_f32_16x16x32_bf16 v[228:231], v[132:135], v[180:183], v[228:231]\n\t"
            "v_mul_f32 v219, v237, v219\n\t"
            "v_cvt_pk_bf16_f32 v206, v196, v197\n\t"
            "v_mfma_f32_16x16x32_bf16 v[208:211], v[136:139], v[160:163], v[208:211]\n\t"
            "v_cvt_pk_bf16_f32 v207, v198, v199\n\t"
            "v_cvt_pk_bf16_f32 v226, v216, v217\n\t"
            "v_mfma_f32_16x16x32_bf16 v[228:231], v[136:139], v[184:187], v[228:231]\n\t"
            "v_cvt_pk_bf16_f32 v227, v218, v219\n\t"
            "v_mfma_f32_16x16x32_bf16 v[208:211], v[140:143], v[164:167], v[208:211]\n\t"
            "ds_write_b128 v232, v[204:207] offset:64\n\t"
            "v_mfma_f32_16x16x32_bf16 v[228:231], v[140:143], v[188:191], v[228:231]\n\t"
            "ds_write_b128 v233, v[224:227] offset:64\n\t"
            "s_nop 4\n\t"
            "v_mul_f32 v208, v236, v208\n\t"
            "v_mul_f32 v209, v236, v209\n\t"
            "v_mul_f32 v210, v236, v210\n\t"
            "v_mul_f32 v211, v236, v211\n\t"
            "v_cvt_pk_bf16_f32 v202, v208, v209\n\t"
            "v_cvt_pk_bf16_f32 v203, v210, v211\n\t"
            "v_mul_f32 v228, v237, v228\n\t"
            "v_mul_f32 v229, v237, v229\n\t"
            "v_mfma_f32_16x16x16_bf16 v[196:199], v[200:201], v[202:203], 0\n\t"
            "v_mul_f32 v230, v237, v230\n\t"
            "v_mul_f32 v231, v237, v231\n\t"
            "v_cvt_pk_bf16_f32 v222, v228, v229\n\t"
            "v_cvt_pk_bf16_f32 v223, v230, v231\n\t"
            "s_nop 1\n\t"
            "v_mfma_f32_16x16x16_bf16 v[216:219], v[220:221], v[222:223], 0\n\t"
            "ds_write_b128 v232, v[200:203] offset:128\n\t"
            "v_mfma_f32_16x16x32_bf16 v[196:199], v[192:195], v[204:207], v[196:199]\n\t"
            "ds_write_b128 v233, v[220:223] offset:128\n\t"
            "s_nop 2\n\t"
            "v_mfma_f32_16x16x32_bf16 v[216:219], v[212:215], v[224:227], v[216:219]\n\t"
            "s_nop 2\n\t"
            "v_cndmask_b32_e64 v196, v196, v197, s[50:51]\n\t"
            "v_cndmask_b32_e64 v196, v196, v198, s[52:53]\n\t"
            "v_cndmask_b32_e64 v196, v196, v199, s[54:55]\n\t"
            "ds_write_b32 v234, v196\n\t"
            "s_nop 0\n\t"
            "v_cndmask_b32_e64 v216, v216, v217, s[50:51]\n\t"
            "v_cndmask_b32_e64 v216, v216, v218, s[52:53]\n\t"
            "v_cndmask_b32_e64 v216, v216, v219, s[54:55]\n\t"
            "ds_write_b32 v235, v216\n\t"
            "s_waitcnt lgkmcnt(0)\n\t"
            "s_nop 7\n\t"
            "v_mov_b32 v224, s46\n\t"
            ".Ls2%=:\n\t"
            "ds_read_b128 v[220:223], v224\n\t"
            "s_waitcnt lgkmcnt(0)\n\t"
            "v_and_b32 v220, v220, v221\n\t"
            "v_and_b32 v222, v222, v223\n\t"
            "v_and_b32 v220, v220, v222\n\t"
            "s_nop 0\n\t"
            "v_readfirstlane_b32 s49, v220\n\t"
            "s_nop 3\n\t"
            "s_bitcmp1_b32 s49, 31\n\t"
            "s_cbranch_scc0 .Ls2%=\n\t"
            "s_waitcnt lgkmcnt(0)\n\t"
            "s_nop 7\n\t"
            "s_setprio 0\n\t"
            "v_add_u32 v232, s48, v244\n\t"
            "v_xor_b32 v233, 64, v232\n\t"
            "v_add_u32 v208, s48, v248\n\t"
            "v_add_u32 v234, s72, v252\n\t"
            "v_cndmask_b32_e64 v235, v246, v234, s[50:51]\n\t"
            "v_cndmask_b32_e64 v234, v246, v234, s[26:27]\n\t"
            "v_mov_b32 v239, s76\n\t"
            "ds_read_b128 v[216:219], v232\n\t"
            "ds_read_b128 v[220:223], v233\n\t"
            "ds_read_b128 v[224:227], v232 offset:128\n\t"
            "ds_read_b128 v[204:207], v208 offset:18816\n\t"
            "ds_read_b128 v[208:211], v208 offset:19840\n\t"
            "ds_read_b128 v[188:191], v233 offset:6272\n\t"
            "ds_read_b128 v[192:195], v232 offset:6400\n\t"
            "ds_read_b128 v[200:203], v233 offset:6400\n\t"
            "ds_read_b32 v239, v239\n\t"
            "s_waitcnt lgkmcnt(8)\n\t"
            "v_mfma_f32_16x16x32_bf16 v[144:147], v[0:3], v[216:219], 0\n\t"
            "s_waitcnt lgkmcnt(7)\n\t"
            "v_mfma_f32_16x16x32_bf16 v[144:147], v[4:7], v[220:223], v[144:147]\n\t"
            "s_waitcnt lgkmcnt(5)\n\t"
            "v_add_f32 v204, v204, v206\n\t"
            "v_add_f32 v205, v205, v207\n\t"
            "v_mfma_f32_16x16x32_bf16 v[144:147], v[8:11], v[224:227], v[144:147]\n\t"
            "s_waitcnt lgkmcnt(4)\n\t"
            "v_add_f32 v208, v208, v210\n\t"
            "v_add_f32 v209, v209, v211\n\t"
            "v_mfma_f32_16x16x32_bf16 v[212:215], v[120:123], v[216:219], 0\n\t"
            "v_add_f32 v204, v204, v208\n\t"
            "v_add_f32 v205, v205, v209\n\t"
            "v_mfma_f32_16x16x32_bf16 v[212:215], v[124:127], v[220:223], v[212:215]\n\t"
            "v_mul_f32 v204, 0x3baaaaab, v204\n\t"
            "v_mul_f32_e64 v204, v204, -v204\n\t"
            "v_mfma_f32_16x16x32_bf16 v[212:215], v[128:131], v[224:227], v[212:215]\n\t"
            "v_fmac_f32 v204, 0x3baaaaab, v205\n\t"
            "v_max_f32 v204, 0, v204\n\t"
            "v_mfma_f32_16x16x32_bf16 v[152:155], v[48:51], v[216:219], 0\n\t"
            "v_add_f32 v204, 0x3727c5ac, v204\n\t"
            "v_rsq_f32 v204, v204\n\t"
            "v_mfma_f32_16x16x32_bf16 v[152:155], v[52:55], v[220:223], v[152:155]\n\t"
            "ds_write_b32 v249, v204\n\t"
            "v_mfma_f32_16x16x32_bf16 v[152:155], v[56:59], v[224:227], v[152:155]\n\t"
            "ds_read_b32 v236, v251\n\t"
            "v_mfma_f32_16x16x32_bf16 v[208:211], v[96:99], v[216:219], 0\n\t"
            "ds_read_b32 v237, v251 offset:64\n\t"
            "v_mfma_f32_16x16x32_bf16 v[208:211], v[100:103], v[220:223], v[208:211]\n\t"
            "ds_read_b32 v238, v251 offset:128\n\t"
            "v_mfma_f32_16x16x32_bf16 v[208:211], v[104:107], v[224:227], v[208:211]\n\t"
            "v_mfma_f32_16x16x32_bf16 v[148:151], v[72:75], v[216:219], 0\n\t"
            "v_mfma_f32_16x16x32_bf16 v[148:151], v[76:79], v[220:223], v[148:151]\n\t"
            "v_mfma_f32_16x16x32_bf16 v[148:151], v[80:83], v[224:227], v[148:151]\n\t"
            "v_mfma_f32_16x16x32_bf16 v[204:207], v[24:27], v[216:219], 0\n\t"
            "ds_read_b128 v[216:219], v233 offset:128\n\t"
            "v_mfma_f32_16x16x32_bf16 v[204:207], v[28:31], v[220:223], v[204:207]\n\t"
            "ds_read_b128 v[220:223], v232 offset:256\n\t"
            "v_mfma_f32_16x16x32_bf16 v[204:207], v[32:35], v[224:227], v[204:207]\n\t"
            "s_waitcnt lgkmcnt(1)\n\t"
            "ds_read_b128 v[224:227], v233 offset:256\n\t"
            "v_mfma_f32_16x16x32_bf16 v[144:147], v[12:15], v[216:219], v[144:147]\n\t"
            "s_waitcnt lgkmcnt(1)\n\t"
            "v_mfma_f32_16x16x32_bf16 v[144:147], v[16:19], v[220:223], v[144:147]\n\t"
            "s_waitcnt lgkmcnt(0)\n\t"
            "v_mfma_f32_16x16x32_bf16 v[144:147], v[20:23], v[224:227], v[144:147]\n\t"
            "v_mfma_f32_16x16x32_bf16 v[148:151], v[84:87], v[216:219], v[148:151]\n\t"
            "v_mfma_f32_16x16x32_bf16 v[148:151], v[88:91], v[220:223], v[148:151]\n\t"
            "v_mfma_f32_16x16x32_bf16 v[148:151], v[92:95], v[224:227], v[148:151]\n\t"
            "v_mfma_f32_16x16x32_bf16 v[152:155], v[60:63], v[216:219], v[152:155]\n\t"
            "v_mfma_f32_16x16x32_bf16 v[152:155], v[64:67], v[220:223], v[152:155]\n\t"
            "v_mfma_f32_16x16x32_bf16 v[152:155], v[68:71], v[224:227], v[152:155]\n\t"
            "s_nop 1\n\t"
            "v_mul_f32 v144, v236, v144\n\t"
            "v_mfma_f32_16x16x32_bf16 v[204:207], v[36:39], v[216:219], v[204:207]\n\t"
            "v_mul_f32 v145, v236, v145\n\t"
            "v_mul_f32 v146, v236, v146\n\t"
            "v_mfma_f32_16x16x32_bf16 v[204:207], v[40:43], v[220:223], v[204:207]\n\t"
            "v_mul_f32 v147, v236, v147\n\t"
            "v_cvt_pk_bf16_f32 v144, v144, v145\n\t"
            "v_mfma_f32_16x16x32_bf16 v[204:207], v[44:47], v[224:227], v[204:207]\n\t"
            "v_mul_f32 v152, v236, v152\n\t"
            "v_mul_f32 v153, v236, v153\n\t"
            "v_mfma_f32_16x16x32_bf16 v[208:211], v[108:111], v[216:219], v[208:211]\n\t"
            "v_mul_f32 v154, v236, v154\n\t"
            "v_mul_f32 v155, v236, v155\n\t"
            "v_mfma_f32_16x16x32_bf16 v[208:211], v[112:115], v[220:223], v[208:211]\n\t"
            "v_cvt_pk_bf16_f32 v152, v152, v153\n\t"
            "v_cvt_pk_bf16_f32 v153, v154, v155\n\t"
            "v_mfma_f32_16x16x32_bf16 v[208:211], v[116:119], v[224:227], v[208:211]\n\t"
            "v_mul_f32 v148, v236, v148\n\t"
            "v_mul_f32 v149, v236, v149\n\t"
            "v_mfma_f32_16x16x32_bf16 v[212:215], v[132:135], v[216:219], v[212:215]\n\t"
            "ds_read_b128 v[216:219], v232 offset:6144\n\t"
            "v_cvt_pk_bf16_f32 v145, v146, v147\n\t"
            "v_mfma_f32_16x16x32_bf16 v[212:215], v[136:139], v[220:223], v[212:215]\n\t"
            "ds_read_b128 v[220:223], v233 offset:6144\n\t"
            "v_mul_f32 v204, v236, v204\n\t"
            "v_mfma_f32_16x16x32_bf16 v[212:215], v[140:143], v[224:227], v[212:215]\n\t"
            "ds_read_b128 v[224:227], v232 offset:6272\n\t"
            "v_mul_f32 v205, v236, v205\n\t"
            "v_mul_f32 v206, v236, v206\n\t"
            "v_mul_f32 v207, v236, v207\n\t"
            "v_mul_f32 v150, v236, v150\n\t"
            "v_mul_f32 v151, v236, v151\n\t"
            "v_cvt_pk_bf16_f32 v148, v148, v149\n\t"
            "v_cvt_pk_bf16_f32 v146, v204, v205\n\t"
            "v_mul_f32 v212, v236, v212\n\t"
            "v_mul_f32 v213, v236, v213\n\t"
            "v_mul_f32 v214, v236, v214\n\t"
            "v_mul_f32 v215, v236, v215\n\t"
            "v_cvt_pk_bf16_f32 v154, v212, v213\n\t"
            "v_cvt_pk_bf16_f32 v155, v214, v215\n\t"
            "v_cvt_pk_bf16_f32 v147, v206, v207\n\t"
            "v_cvt_pk_bf16_f32 v149, v150, v151\n\t"
            "v_mul_f32 v208, v236, v208\n\t"
            "v_mul_f32 v209, v236, v209\n\t"
            "v_mul_f32 v210, v236, v210\n\t"
            "v_mul_f32 v211, v236, v211\n\t"
            "v_cvt_pk_bf16_f32 v150, v208, v209\n\t"
            "v_cvt_pk_bf16_f32 v151, v210, v211\n\t"
            "s_waitcnt lgkmcnt(2)\n\t"
            "v_mfma_f32_16x16x32_bf16 v[160:163], v[72:75], v[216:219], 0\n\t"
            "s_waitcnt lgkmcnt(1)\n\t"
            "ds_read_b128 v[204:207], v232 offset:12288\n\t"
            "v_mfma_f32_16x16x32_bf16 v[160:163], v[76:79], v[220:223], v[160:163]\n\t"
            "s_waitcnt lgkmcnt(1)\n\t"
            "ds_read_b128 v[208:211], v233 offset:12288\n\t"
            "v_mfma_f32_16x16x32_bf16 v[160:163], v[80:83], v[224:227], v[160:163]\n\t"
            "ds_read_b128 v[212:215], v232 offset:12416\n\t"
            "v_mfma_f32_16x16x32_bf16 v[160:163], v[84:87], v[188:191], v[160:163]\n\t"
            "v_mfma_f32_16x16x32_bf16 v[160:163], v[88:91], v[192:195], v[160:163]\n\t"
            "v_mfma_f32_16x16x32_bf16 v[160:163], v[92:95], v[200:203], v[160:163]\n\t"
            "v_mfma_f32_16x16x32_bf16 v[196:199], v[24:27], v[216:219], 0\n\t"
            "v_mfma_f32_16x16x32_bf16 v[196:199], v[28:31], v[220:223], v[196:199]\n\t"
            "v_mfma_f32_16x16x32_bf16 v[196:199], v[32:35], v[224:227], v[196:199]\n\t"
            "v_mfma_f32_16x16x32_bf16 v[196:199], v[36:39], v[188:191], v[196:199]\n\t"
            "v_mfma_f32_16x16x32_bf16 v[196:199], v[40:43], v[192:195], v[196:199]\n\t"
            "v_mfma_f32_16x16x32_bf16 v[196:199], v[44:47], v[200:203], v[196:199]\n\t"
            "s_nop 1\n\t"
            "v_mul_f32 v160, v237, v160\n\t"
            "v_mfma_f32_16x16x32_bf16 v[156:159], v[0:3], v[216:219], 0\n\t"
            "v_mul_f32 v161, v237, v161\n\t"
            "v_mul_f32 v162, v237, v162\n\t"
            "v_mfma_f32_16x16x32_bf16 v[156:159], v[4:7], v[220:223], v[156:159]\n\t"
            "v_mul_f32 v163, v237, v163\n\t"
            "v_cvt_pk_bf16_f32 v160, v160, v161\n\t"
            "v_mfma_f32_16x16x32_bf16 v[156:159], v[8:11], v[224:227], v[156:159]\n\t"
            "v_mul_f32 v196, v237, v196\n\t"
            "v_mul_f32 v197, v237, v197\n\t"
            "v_mfma_f32_16x16x32_bf16 v[156:159], v[12:15], v[188:191], v[156:159]\n\t"
            "v_mul_f32 v198, v237, v198\n\t"
            "v_mul_f32 v199, v237, v199\n\t"
            "v_mfma_f32_16x16x32_bf16 v[156:159], v[16:19], v[192:195], v[156:159]\n\t"
            "v_cvt_pk_bf16_f32 v161, v162, v163\n\t"
            "v_mfma_f32_16x16x32_bf16 v[156:159], v[20:23], v[200:203], v[156:159]\n\t"
            "v_mfma_f32_16x16x16_bf16 v[180:183], v[152:153], v[154:155], 0\n\t"
            "v_mfma_f32_16x16x32_bf16 v[164:167], v[48:51], v[216:219], 0\n\t"
            "v_mfma_f32_16x16x32_bf16 v[164:167], v[52:55], v[220:223], v[164:167]\n\t"
            "v_mfma_f32_16x16x32_bf16 v[164:167], v[56:59], v[224:227], v[164:167]\n\t"
            "v_mfma_f32_16x16x32_bf16 v[164:167], v[60:63], v[188:191], v[164:167]\n\t"
            "v_mfma_f32_16x16x32_bf16 v[164:167], v[64:67], v[192:195], v[164:167]\n\t"
            "s_nop 1\n\t"
            "v_mul_f32 v156, v237, v156\n\t"
            "v_mfma_f32_16x16x32_bf16 v[164:167], v[68:71], v[200:203], v[164:167]\n\t"
            "v_mul_f32 v157, v237, v157\n\t"
            "v_mul_f32 v158, v237, v158\n\t"
            "v_mfma_f32_16x16x32_bf16 v[228:231], v[96:99], v[216:219], 0\n\t"
            "v_mul_f32 v159, v237, v159\n\t"
            "v_cvt_pk_bf16_f32 v156, v156, v157\n\t"
            "v_mfma_f32_16x16x32_bf16 v[228:231], v[100:103], v[220:223], v[228:231]\n\t"
            "v_cvt_pk_bf16_f32 v157, v158, v159\n\t"
            "v_cvt_pk_bf16_f32 v158, v196, v197\n\t"
            "v_mfma_f32_16x16x32_bf16 v[228:231], v[104:107], v[224:227], v[228:231]\n\t"
            "v_cvt_pk_bf16_f32 v159, v198, v199\n\t"
            "v_mul_f32 v164, v237, v164\n\t"
            "v_mfma_f32_16x16x32_bf16 v[228:231], v[108:111], v[188:191], v[228:231]\n\t"
            "v_mul_f32 v165, v237, v165\n\t"
            "v_mul_f32 v166, v237, v166\n\t"
            "v_mfma_f32_16x16x32_bf16 v[228:231], v[112:115], v[192:195], v[228:231]\n\t"
            "v_mul_f32 v167, v237, v167\n\t"
            "v_cvt_pk_bf16_f32 v164, v164, v165\n\t"
            "v_mfma_f32_16x16x32_bf16 v[228:231], v[116:119], v[200:203], v[228:231]\n\t"
            "v_cvt_pk_bf16_f32 v165, v166, v167\n\t"
            "v_mfma_f32_16x16x32_bf16 v[180:183], v[144:147], v[148:151], v[180:183]\n\t"
            "v_mfma_f32_16x16x32_bf16 v[196:199], v[120:123], v[216:219], 0\n\t"
            "ds_read_b128 v[216:219], v233 offset:12416\n\t"
            "v_mfma_f32_16x16x32_bf16 v[196:199], v[124:127], v[220:223], v[196:199]\n\t"
            "ds_read_b128 v[220:223], v232 offset:12544\n\t"
            "v_mfma_f32_16x16x32_bf16 v[196:199], v[128:131], v[224:227], v[196:199]\n\t"
            "ds_read_b128 v[224:227], v233 offset:12544\n\t"
            "v_mul_f32 v228, v237, v228\n\t"
            "v_mfma_f32_16x16x32_bf16 v[196:199], v[132:135], v[188:191], v[196:199]\n\t"
            "v_mul_f32 v229, v237, v229\n\t"
            "v_mul_f32 v230, v237, v230\n\t"
            "v_mfma_f32_16x16x32_bf16 v[196:199], v[136:139], v[192:195], v[196:199]\n\t"
            "v_mul_f32 v231, v237, v231\n\t"
            "v_cvt_pk_bf16_f32 v162, v228, v229\n\t"
            "v_mfma_f32_16x16x32_bf16 v[196:199], v[140:143], v[200:203], v[196:199]\n\t"
            "s_waitcnt lgkmcnt(5)\n\t"
            "v_cvt_pk_bf16_f32 v163, v230, v231\n\t"
            "v_exp_f32 v180, v180\n\t"
            "v_mfma_f32_16x16x32_bf16 v[176:179], v[48:51], v[204:207], 0\n\t"
            "s_waitcnt lgkmcnt(4)\n\t"
            "v_exp_f32 v181, v181\n\t"
            "v_mfma_f32_16x16x32_bf16 v[176:179], v[52:55], v[208:211], v[176:179]\n\t"
            "s_waitcnt lgkmcnt(3)\n\t"
            "v_exp_f32 v182, v182\n\t"
            "v_mfma_f32_16x16x32_bf16 v[176:179], v[56:59], v[212:215], v[176:179]\n\t"
            "s_waitcnt lgkmcnt(2)\n\t"
            "v_mul_f32 v196, v237, v196\n\t"
            "v_mul_f32 v197, v237, v197\n\t"
            "v_mfma_f32_16x16x32_bf16 v[176:179], v[60:63], v[216:219], v[176:179]\n\t"
            "s_waitcnt lgkmcnt(1)\n\t"
            "v_mul_f32 v198, v237, v198\n\t"
            "v_mul_f32 v199, v237, v199\n\t"
            "v_mfma_f32_16x16x32_bf16 v[176:179], v[64:67], v[220:223], v[176:179]\n\t"
            "s_waitcnt lgkmcnt(0)\n\t"
            "v_cvt_pk_bf16_f32 v166, v196, v197\n\t"
            "v_cvt_pk_bf16_f32 v167, v198, v199\n\t"
            "v_mfma_f32_16x16x32_bf16 v[176:179], v[68:71], v[224:227], v[176:179]\n\t"
            "v_exp_f32 v183, v183\n\t"
            "v_cvt_pk_bf16_f32 v180, v180, v181\n\t"
            "v_cvt_pk_bf16_f32 v181, v182, v183\n\t"
            "v_mfma_f32_16x16x16_bf16 v[188:191], v[152:153], v[166:167], 0\n\t"
            "v_mfma_f32_16x16x16_bf16 v[192:195], v[164:165], v[166:167], 0\n\t"
            "v_mfma_f32_16x16x16_bf16 v[184:187], v[164:165], v[154:155], 0\n\t"
            "s_nop 1\n\t"
            "v_mul_f32 v176, v238, v176\n\t"
            "v_mfma_f32_16x16x32_bf16 v[168:171], v[0:3], v[204:207], 0\n\t"
            "v_mul_f32 v177, v238, v177\n\t"
            "v_mul_f32 v178, v238, v178\n\t"
            "v_mfma_f32_16x16x32_bf16 v[168:171], v[4:7], v[208:211], v[168:171]\n\t"
            "v_mul_f32 v179, v238, v179\n\t"
            "v_cvt_pk_bf16_f32 v176, v176, v177\n\t"
            "v_mfma_f32_16x16x32_bf16 v[168:171], v[8:11], v[212:215], v[168:171]\n\t"
            "v_cvt_pk_bf16_f32 v177, v178, v179\n\t"
            "v_mfma_f32_16x16x32_bf16 v[168:171], v[12:15], v[216:219], v[168:171]\n\t"
            "v_mfma_f32_16x16x32_bf16 v[168:171], v[16:19], v[220:223], v[168:171]\n\t"
            "v_mfma_f32_16x16x32_bf16 v[168:171], v[20:23], v[224:227], v[168:171]\n\t"
            "v_mfma_f32_16x16x32_bf16 v[188:191], v[144:147], v[160:163], v[188:191]\n\t"
            "v_mfma_f32_16x16x32_bf16 v[200:203], v[120:123], v[204:207], 0\n\t"
            "v_mfma_f32_16x16x32_bf16 v[200:203], v[124:127], v[208:211], v[200:203]\n\t"
            "v_mfma_f32_16x16x32_bf16 v[200:203], v[128:131], v[212:215], v[200:203]\n\t"
            "v_mfma_f32_16x16x32_bf16 v[200:203], v[132:135], v[216:219], v[200:203]\n\t"
            "v_mfma_f32_16x16x32_bf16 v[200:203], v[136:139], v[220:223], v[200:203]\n\t"
            "s_nop 1\n\t"
            "v_mul_f32 v168, v238, v168\n\t"
            "v_mfma_f32_16x16x32_bf16 v[200:203], v[140:143], v[224:227], v[200:203]\n\t"
            "v_exp_f32 v188, v188\n\t"
            "v_mfma_f32_16x16x32_bf16 v[192:195], v[156:159], v[160:163], v[192:195]\n\t"
            "v_exp_f32 v189, v189\n\t"
            "v_mfma_f32_16x16x32_bf16 v[172:175], v[72:75], v[204:207], 0\n\t"
            "v_exp_f32 v190, v190\n\t"
            "v_mfma_f32_16x16x32_bf16 v[172:175], v[76:79], v[208:211], v[172:175]\n\t"
            "v_exp_f32 v191, v191\n\t"
            "v_mfma_f32_16x16x32_bf16 v[172:175], v[80:83], v[212:215], v[172:175]\n\t"
            "v_cvt_pk_bf16_f32 v188, v188, v189\n\t"
            "v_cvt_pk_bf16_f32 v189, v190, v191\n\t"
            "v_mfma_f32_16x16x32_bf16 v[172:175], v[84:87], v[216:219], v[172:175]\n\t"
            "v_exp_f32 v192, v192\n\t"
            "v_mfma_f32_16x16x32_bf16 v[172:175], v[88:91], v[220:223], v[172:175]\n\t"
            "v_exp_f32 v193, v193\n\t"
            "v_mfma_f32_16x16x32_bf16 v[172:175], v[92:95], v[224:227], v[172:175]\n\t"
            "v_exp_f32 v194, v194\n\t"
            "v_mfma_f32_16x16x32_bf16 v[184:187], v[156:159], v[148:151], v[184:187]\n\t"
            "v_exp_f32 v195, v195\n\t"
            "v_mfma_f32_16x16x32_bf16 v[196:199], v[24:27], v[204:207], 0\n\t"
            "v_cvt_pk_bf16_f32 v190, v192, v193\n\t"
            "v_cvt_pk_bf16_f32 v191, v194, v195\n\t"
            "v_mfma_f32_16x16x32_bf16 v[196:199], v[28:31], v[208:211], v[196:199]\n\t"
            "v_mul_f32 v169, v238, v169\n\t"
            "v_mul_f32 v170, v238, v170\n\t"
            "v_mfma_f32_16x16x32_bf16 v[196:199], v[32:35], v[212:215], v[196:199]\n\t"
            "v_exp_f32 v184, v184\n\t"
            "v_mfma_f32_16x16x32_bf16 v[196:199], v[36:39], v[216:219], v[196:199]\n\t"
            "v_exp_f32 v185, v185\n\t"
            "v_mfma_f32_16x16x32_bf16 v[196:199], v[40:43], v[220:223], v[196:199]\n\t"
            "v_exp_f32 v186, v186\n\t"
            "v_mfma_f32_16x16x32_bf16 v[196:199], v[44:47], v[224:227], v[196:199]\n\t"
            "v_exp_f32 v187, v187\n\t"
            "v_cvt_pk_bf16_f32 v182, v184, v185\n\t"
            "v_cvt_pk_bf16_f32 v183, v186, v187\n\t"
            "v_mfma_f32_16x16x16_bf16 v[184:187], v[176:177], v[154:155], 0\n\t"
            "v_mul_f32 v171, v238, v171\n\t"
            "v_cvt_pk_bf16_f32 v168, v168, v169\n\t"
            "v_cvt_pk_bf16_f32 v169, v170, v171\n\t"
            "v_mfma_f32_16x16x16_bf16 v[192:195], v[176:177], v[166:167], 0\n\t"
            "v_mul_f32 v196, v238, v196\n\t"
            "v_mul_f32 v197, v238, v197\n\t"
            "v_mul_f32 v198, v238, v198\n\t"
            "v_mfma_f32_16x16x32_bf16 v[228:231], v[96:99], v[204:207], 0\n\t"
            "ds_read_b64 v[204:205], v235 offset:136\n\t"
            "v_mul_f32 v199, v238, v199\n\t"
            "v_mfma_f32_16x16x32_bf16 v[228:231], v[100:103], v[208:211], v[228:231]\n\t"
            "ds_read_b64 v[206:207], v234 offset:128\n\t"
            "v_cvt_pk_bf16_f32 v170, v196, v197\n\t"
            "v_mfma_f32_16x16x32_bf16 v[228:231], v[104:107], v[212:215], v[228:231]\n\t"
            "ds_read_b128 v[208:211], v234\n\t"
            "v_cvt_pk_bf16_f32 v171, v198, v199\n\t"
            "v_mfma_f32_16x16x32_bf16 v[228:231], v[108:111], v[216:219], v[228:231]\n\t"
            "ds_read_b128 v[212:215], v235 offset:64\n\t"
            "v_mul_f32 v172, v238, v172\n\t"
            "v_mfma_f32_16x16x32_bf16 v[228:231], v[112:115], v[220:223], v[228:231]\n\t"
            "v_mul_f32 v173, v238, v173\n\t"
            "v_mul_f32 v200, v238, v200\n\t"
            "v_mfma_f32_16x16x32_bf16 v[228:231], v[116:119], v[224:227], v[228:231]\n\t"
            "v_mul_f32 v201, v238, v201\n\t"
            "v_mul_f32 v202, v238, v202\n\t"
            "v_mfma_f32_16x16x32_bf16 v[184:187], v[168:171], v[148:151], v[184:187]\n\t"
            "v_mul_f32 v203, v238, v203\n\t"
            "v_mul_f32 v174, v238, v174\n\t"
            "v_mfma_f32_16x16x32_bf16 v[192:195], v[168:171], v[160:163], v[192:195]\n\t"
            "v_mul_f32 v175, v238, v175\n\t"
            "v_cvt_pk_bf16_f32 v172, v172, v173\n\t"
            "v_cvt_pk_bf16_f32 v178, v200, v201\n\t"
            "v_cvt_pk_bf16_f32 v179, v202, v203\n\t"
            "v_cvt_pk_bf16_f32 v173, v174, v175\n\t"
            "v_mul_f32 v228, v238, v228\n\t"
            "v_mul_f32 v229, v238, v229\n\t"
            "s_waitcnt lgkmcnt(2)\n\t"
            "v_mfma_f32_16x16x32_bf16 v[220:223], v[204:207], v[164:167], 0\n\t"
            "s_waitcnt lgkmcnt(1)\n\t"
            "v_mul_f32 v230, v238, v230\n\t"
            "v_mul_f32 v231, v238, v231\n\t"
            "v_mfma_f32_16x16x32_bf16 v[220:223], v[208:211], v[160:163], v[220:223]\n\t"
            "s_waitcnt lgkmcnt(0)\n\t"
            "v_cvt_pk_bf16_f32 v174, v228, v229\n\t"
            "v_cvt_pk_bf16_f32 v175, v230, v231\n\t"
            "v_mfma_f32_16x16x32_bf16 v[220:223], v[212:215], v[156:159], v[220:223]\n\t"
            "v_exp_f32 v184, v184\n\t"
            "v_mfma_f32_16x16x32_bf16 v[216:219], v[204:207], v[152:155], 0\n\t"
            "v_exp_f32 v185, v185\n\t"
            "v_mfma_f32_16x16x32_bf16 v[216:219], v[208:211], v[148:151], v[216:219]\n\t"
            "v_exp_f32 v186, v186\n\t"
            "v_mfma_f32_16x16x32_bf16 v[216:219], v[212:215], v[144:147], v[216:219]\n\t"
            "v_exp_f32 v187, v187\n\t"
            "v_cvt_pk_bf16_f32 v184, v184, v185\n\t"
            "v_cvt_pk_bf16_f32 v185, v186, v187\n\t"
            "v_mfma_f32_16x16x16_bf16 v[228:231], v[164:165], v[178:179], 0\n\t"
            "v_mov_b32 v187, 0\n\t"
            "v_exp_f32 v192, v192\n\t"
            "v_mfma_f32_16x16x16_bf16 v[200:203], v[176:177], v[178:179], 0\n\t"
            "v_exp_f32 v193, v193\n\t"
            "v_exp_f32 v216, v216\n\t"
            "v_mfma_f32_16x16x16_bf16 v[196:199], v[152:153], v[178:179], 0\n\t"
            "v_cvt_pk_bf16_f32 v216, v216, 0\n\t"
            "v_cndmask_b32_e64 v186, 0, v216, s[58:59]\n\t"
            "v_cndmask_b32_e64 v235, v217, v221, s[60:61]\n\t"
            "v_mfma_f32_16x16x32_bf16 v[224:227], v[204:207], v[176:179], 0\n\t"
            "v_exp_f32 v194, v194\n\t"
            "v_mfma_f32_16x16x32_bf16 v[224:227], v[208:211], v[172:175], v[224:227]\n\t"
            "v_exp_f32 v195, v195\n\t"
            "v_mfma_f32_16x16x32_bf16 v[224:227], v[212:215], v[168:171], v[224:227]\n\t"
            "v_exp_f32 v220, v220\n\t"
            "v_mfma_f32_16x16x32_bf16 v[204:207], v[240:243], v[180:183], 0\n\t"
            "v_cvt_pk_bf16_f32 v192, v192, v193\n\t"
            "v_cvt_pk_bf16_f32 v193, v194, v195\n\t"
            "v_mfma_f32_16x16x32_bf16 v[204:207], v[240:243], v[184:187], v[204:207]\n\t"
            "v_cvt_pk_bf16_f32 v220, v220, 0\n\t"
            "v_cndmask_b32_e64 v194, 0, v220, s[58:59]\n\t"
            "v_mfma_f32_16x16x32_bf16 v[200:203], v[168:171], v[172:175], v[200:203]\n\t"
            "v_exp_f32 v224, v224\n\t"
            "v_mfma_f32_16x16x32_bf16 v[196:199], v[144:147], v[172:175], v[196:199]\n\t"
            "v_cvt_pk_bf16_f32 v224, v224, 0\n\t"
            "v_cndmask_b32_e64 v235, v235, v225, s[62:63]\n\t"
            "v_mfma_f32_16x16x32_bf16 v[228:231], v[156:159], v[172:175], v[228:231]\n\t"
            "v_cndmask_b32_e64 v235, v235, v239, s[64:65]\n\t"
            "v_cmp_nlt_f32_e64 s[24:25], |v235|, s23\n\t"
            "v_exp_f32 v235, v235\n\t"
            "v_exp_f32 v200, v200\n\t"
            "v_exp_f32 v201, v201\n\t"
            "v_exp_f32 v196, v196\n\t"
            "v_exp_f32 v197, v197\n\t"
            "v_exp_f32 v198, v198\n\t"
            "v_exp_f32 v199, v199\n\t"
            "v_exp_f32 v228, v228\n\t"
            "v_exp_f32 v229, v229\n\t"
            "v_exp_f32 v230, v230\n\t"
            "v_exp_f32 v231, v231\n\t"
            "v_cvt_pk_bf16_f32 v196, v196, v197\n\t"
            "v_cvt_pk_bf16_f32 v197, v198, v199\n\t"
            "v_cvt_pk_bf16_f32 v198, v228, v229\n\t"
            "v_cvt_pk_bf16_f32 v199, v230, v231\n\t"
            "v_exp_f32 v202, v202\n\t"
            "v_exp_f32 v203, v203\n\t"
            "v_mfma_f32_16x16x32_bf16 v[212:215], v[240:243], v[196:199], 0\n\t"
            "v_cvt_pk_bf16_f32 v200, v200, v201\n\t"
            "v_cvt_pk_bf16_f32 v201, v202, v203\n\t"
            "v_cndmask_b32_e64 v202, 0, v224, s[58:59]\n\t"
            "v_mov_b32 v203, 0\n\t"
            "v_cvt_pk_bf16_f32 v235, v235, 0\n\t"
            "v_cndmask_b32_e64 v235, 0, v235, s[66:67]\n\t"
            "v_mfma_f32_16x16x32_bf16 v[212:215], v[240:243], v[200:203], v[212:215]\n\t"
            "ds_write_b16 v250, v235 offset:256\n\t"
            "v_mfma_f32_16x16x32_bf16 v[208:211], v[240:243], v[188:191], 0\n\t"
            "v_mov_b32 v195, 0\n\t"
            "v_add_u32 v205, 0xe0800000, v204\n\t"
            "v_rcp_f32 v236, v204\n\t"
            "v_mfma_f32_16x16x32_bf16 v[208:211], v[240:243], v[192:195], v[208:211]\n\t"
            "s_nop 1\n\t"
            "v_add_u32 v213, 0xe0800000, v212\n\t"
            "v_rcp_f32 v238, v212\n\t"
            "s_nop 3\n\t"
            "v_add_u32 v209, 0xe0800000, v208\n\t"
            "v_rcp_f32 v237, v208\n\t"
            "v_or3_b32 v205, v205, v209, v213\n\t"
            "v_cmp_le_u32 vcc, 2.0, v205\n\t"
            "s_waitcnt lgkmcnt(0)\n\t"
            "s_nop 7\n\t"
            "s_bfe_u32 s49, s70, 0x30001\n\t"
            "s_lshl_b32 s49, s49, 4\n\t"
            "s_or_b32 s73, s77, s49\n\t"
            "s_add_u32 s70, s70, 1\n\t"
            "s_add_u32 s76, s76, 800\n\t"
            "s_add_u32 s77, s77, 384\n\t"
            "s_xor_b32 s48, s48, s47\n\t"
            "s_sub_u32 s44, s44, 1\n\t"
            "s_branch .Lwa_in%=\n\t"
            ".p2align 6\n\t"
            ".Lwa_loop%=:\n\t"
            "ds_read_b64 v[156:157], v247\n\t"
            "v_add_u32 v235, s45, v248\n\t"
            "v_xor_b32 v234, s73, v254\n\t"
            "v_add_u32 v232, s48, v244\n\t"
            "v_xor_b32 v233, 64, v232\n\t"
            "v_mov_b32 v159, 0\n\t"
            "v_add_u32 v208, s48, v248\n\t"
            "v_mov_b32 v167, 0\n\t"
            "v_mov_b32 v163, 0\n\t"
            "v_mov_b32 v239, s76\n\t"
            "ds_read_b128 v[168:171], v235\n\t"
            "ds_read_u16 v158, v234\n\t"
            "ds_read_b128 v[216:219], v232\n\t"
            "ds_read_b128 v[220:223], v233\n\t"
            "ds_read_b128 v[224:227], v232 offset:128\n\t"
            "ds_read_b128 v[172:175], v235 offset:1536\n\t"
            "ds_read_b64 v[164:165], v247 offset:1536\n\t"
            "ds_read_u16 v166, v234 offset:128\n\t"
            "ds_read_b128 v[176:179], v235 offset:3072\n\t"
            "ds_read_b64 v[160:161], v247 offset:3072\n\t"
            "ds_read_u16 v162, v234 offset:256\n\t"
            "v_add_u32 v234, s72, v252\n\t"
            "v_cndmask_b32_e64 v235, v246, v234, s[50:51]\n\t"
            "v_cndmask_b32_e64 v234, v246, v234, s[26:27]\n\t"
            "ds_read_b128 v[204:207], v208 offset:18816\n\t"
            "ds_read_b128 v[208:211], v208 offset:19840\n\t"
            "s_waitcnt lgkmcnt(14)\n\t"
            "ds_read_b32 v239, v239\n\t"
            "s_waitcnt lgkmcnt(13)\n\t"
            "v_mfma_f32_16x16x32_bf16 v[228:231], v[168:171], v[180:183], 0\n\t"
            "s_waitcnt lgkmcnt(12)\n\t"
            "v_mfma_f32_16x16x32_bf16 v[228:231], v[156:159], v[184:187], v[228:231]\n\t"
            "s_waitcnt lgkmcnt(11)\n\t"
            "v_mfma_f32_16x16x32_bf16 v[144:147], v[0:3], v[216:219], 0\n\t"
            "s_waitcnt lgkmcnt(10)\n\t"
            "v_mfma_f32_16x16x32_bf16 v[144:147], v[4:7], v[220:223], v[144:147]\n\t"
            "s_waitcnt lgkmcnt(9)\n\t"
            "v_mfma_f32_16x16x32_bf16 v[144:147], v[8:11], v[224:227], v[144:147]\n\t"
            "v_mfma_f32_16x16x32_bf16 v[212:215], v[120:123], v[216:219], 0\n\t"
            "v_mfma_f32_16x16x32_bf16 v[212:215], v[124:127], v[220:223], v[212:215]\n\t"
            "s_waitcnt lgkmcnt(2)\n\t"
            "v_add_f32 v204, v204, v206\n\t"
            "v_add_f32 v205, v205, v207\n\t"
            "v_mfma_f32_16x16x32_bf16 v[212:215], v[128:131], v[224:227], v[212:215]\n\t"
            "v_mul_f32 v228, v236, v228\n\t"
            "v_mul_f32 v229, v236, v229\n\t"
            "v_mfma_f32_16x16x32_bf16 v[152:155], v[48:51], v[216:219], 0\n\t"
            "v_mul_f32 v230, v236, v230\n\t"
            "v_mul_f32 v231, v236, v231\n\t"
            "v_mfma_f32_16x16x32_bf16 v[152:155], v[52:55], v[220:223], v[152:155]\n\t"
            "v_cvt_pk_bf16_f32 v228, v228, v229\n\t"
            "v_cvt_pk_bf16_f32 v229, v230, v231\n\t"
            "v_mfma_f32_16x16x32_bf16 v[152:155], v[56:59], v[224:227], v[152:155]\n\t"
            "ds_write_b64 v245, v[228:229]\n\t"
            "v_mfma_f32_16x16x32_bf16 v[228:231], v[172:175], v[180:183], 0\n\t"
            "s_waitcnt lgkmcnt(2)\n\t"
            "v_add_f32 v208, v208, v210\n\t"
            "v_add_f32 v209, v209, v211\n\t"
            "v_mfma_f32_16x16x32_bf16 v[228:231], v[164:167], v[184:187], v[228:231]\n\t"
            "v_add_f32 v204, v204, v208\n\t"
            "v_add_f32 v205, v205, v209\n\t"
            "v_mfma_f32_16x16x32_bf16 v[180:183], v[176:179], v[180:183], 0\n\t"
            "v_mul_f32 v204, 0x3baaaaab, v204\n\t"
            "v_mul_f32_e64 v204, v204, -v204\n\t"
            "v_mfma_f32_16x16x32_bf16 v[180:183], v[160:163], v[184:187], v[180:183]\n\t"
            "v_fmac_f32 v204, 0x3baaaaab, v205\n\t"
            "v_max_f32 v204, 0, v204\n\t"
            "v_mfma_f32_16x16x32_bf16 v[208:211], v[96:99], v[216:219], 0\n\t"
            "v_add_f32 v204, 0x3727c5ac, v204\n\t"
            "v_mul_f32 v228, v236, v228\n\t"
            "v_mfma_f32_16x16x32_bf16 v[208:211], v[100:103], v[220:223], v[208:211]\n\t"
            "v_rsq_f32 v204, v204\n\t"
            "v_mfma_f32_16x16x32_bf16 v[208:211], v[104:107], v[224:227], v[208:211]\n\t"
            "ds_write_b32 v249, v204\n\t"
            "v_mfma_f32_16x16x32_bf16 v[184:187], v[168:171], v[188:191], 0\n\t"
            "v_mul_f32 v229, v236, v229\n\t"
            "v_mul_f32 v230, v236, v230\n\t"
            "v_mfma_f32_16x16x32_bf16 v[184:187], v[156:159], v[192:195], v[184:187]\n\t"
            "v_mul_f32 v231, v236, v231\n\t"
            "v_cvt_pk_bf16_f32 v228, v228, v229\n\t"
            "v_mfma_f32_16x16x32_bf16 v[148:151], v[72:75], v[216:219], 0\n\t"
            "v_cvt_pk_bf16_f32 v229, v230, v231\n\t"
            "v_mul_f32 v180, v236, v180\n\t"
            "v_mfma_f32_16x16x32_bf16 v[148:151], v[76:79], v[220:223], v[148:151]\n\t"
            "v_mul_f32 v181, v236, v181\n\t"
            "v_mul_f32 v182, v236, v182\n\t"
            "v_mfma_f32_16x16x32_bf16 v[148:151], v[80:83], v[224:227], v[148:151]\n\t"
            "ds_write_b64 v245, v[228:229] offset:128\n\t"
            "v_mfma_f32_16x16x32_bf16 v[204:207], v[24:27], v[216:219], 0\n\t"
            "ds_read_b128 v[216:219], v233 offset:128\n\t"
            "v_mul_f32 v183, v236, v183\n\t"
            "v_mfma_f32_16x16x32_bf16 v[204:207], v[28:31], v[220:223], v[204:207]\n\t"
            "ds_read_b128 v[220:223], v232 offset:256\n\t"
            "v_cvt_pk_bf16_f32 v180, v180, v181\n\t"
            "v_mfma_f32_16x16x32_bf16 v[204:207], v[32:35], v[224:227], v[204:207]\n\t"
            "ds_read_b128 v[224:227], v233 offset:256\n\t"
            "v_cvt_pk_bf16_f32 v181, v182, v183\n\t"
            "v_mfma_f32_16x16x32_bf16 v[228:231], v[172:175], v[188:191], 0\n\t"
            "ds_read_b32 v236, v251\n\t"
            "v_mul_f32 v184, v237, v184\n\t"
            "v_mfma_f32_16x16x32_bf16 v[228:231], v[164:167], v[192:195], v[228:231]\n\t"
            "ds_write_b64 v245, v[180:181] offset:256\n\t"
            "v_mfma_f32_16x16x32_bf16 v[188:191], v[176:179], v[188:191], 0\n\t"
            "v_mul_f32 v185, v237, v185\n\t"
            "v_mul_f32 v186, v237, v186\n\t"
            "v_mfma_f32_16x16x32_bf16 v[188:191], v[160:163], v[192:195], v[188:191]\n\t"
            "ds_read_b128 v[192:195], v232 offset:6400\n\t"
            "v_mul_f32 v187, v237, v187\n\t"
            "v_mfma_f32_16x16x32_bf16 v[180:183], v[168:171], v[196:199], 0\n\t"
            "v_mul_f32 v228, v237, v228\n\t"
            "v_mul_f32 v229, v237, v229\n\t"
            "v_mfma_f32_16x16x32_bf16 v[180:183], v[156:159], v[200:203], v[180:183]\n\t"
            "s_waitcnt lgkmcnt(5)\n\t"
            "v_mul_f32 v230, v237, v230\n\t"
            "v_mul_f32 v231, v237, v231\n\t"
            "v_mfma_f32_16x16x32_bf16 v[144:147], v[12:15], v[216:219], v[144:147]\n\t"
            "s_waitcnt lgkmcnt(4)\n\t"
            "v_cvt_pk_bf16_f32 v228, v228, v229\n\t"
            "v_cvt_pk_bf16_f32 v229, v230, v231\n\t"
            "v_mfma_f32_16x16x32_bf16 v[144:147], v[16:19], v[220:223], v[144:147]\n\t"
            "s_waitcnt lgkmcnt(3)\n\t"
            "v_cvt_pk_bf16_f32 v184, v184, v185\n\t"
            "v_cvt_pk_bf16_f32 v185, v186, v187\n\t"
            "v_mfma_f32_16x16x32_bf16 v[144:147], v[20:23], v[224:227], v[144:147]\n\t"
            "ds_read_b64 v[186:187], v253 offset:352\n\t"
            "v_mul_f32 v180, v238, v180\n\t"
            "v_mfma_f32_16x16x32_bf16 v[148:151], v[84:87], v[216:219], v[148:151]\n\t"
            "v_mul_f32 v181, v238, v181\n\t"
            "v_mul_f32 v182, v238, v182\n\t"
            "v_mfma_f32_16x16x32_bf16 v[148:151], v[88:91], v[220:223], v[148:151]\n\t"
            "v_mul_f32 v183, v238, v183\n\t"
            "v_cvt_pk_bf16_f32 v180, v180, v181\n\t"
            "v_mfma_f32_16x16x32_bf16 v[148:151], v[92:95], v[224:227], v[148:151]\n\t"
            "ds_write_b64 v245, v[228:229] offset:6272\n\t"
            "v_mfma_f32_16x16x32_bf16 v[152:155], v[60:63], v[216:219], v[152:155]\n\t"
            "v_cvt_pk_bf16_f32 v181, v182, v183\n\t"
            "v_mul_f32 v188, v237, v188\n\t"
            "v_mfma_f32_16x16x32_bf16 v[152:155], v[64:67], v[220:223], v[152:155]\n\t"
            "ds_read_b64 v[182:183], v253 offset:288\n\t"
            "v_mul_f32 v189, v237, v189\n\t"
            "v_mfma_f32_16x16x32_bf16 v[152:155], v[68:71], v[224:227], v[152:155]\n\t"
            "ds_write_b64 v245, v[180:181] offset:12288\n\t"
            "v_mfma_f32_16x16x32_bf16 v[228:231], v[172:175], v[196:199], 0\n\t"
            "ds_read_b64 v[180:181], v253 offset:256\n\t"
            "v_mul_f32 v190, v237, v190\n\t"
            "v_mfma_f32_16x16x32_bf16 v[228:231], v[164:167], v[200:203], v[228:231]\n\t"
            "ds_write_b64 v245, v[184:185] offset:6144\n\t"
            "v_mfma_f32_16x16x32_bf16 v[196:199], v[176:179], v[196:199], 0\n\t"
            "ds_read_b64 v[184:185], v253 offset:320\n\t"
            "v_mul_f32 v191, v237, v191\n\t"
            "v_mfma_f32_16x16x32_bf16 v[196:199], v[160:163], v[200:203], v[196:199]\n\t"
            "ds_read_b128 v[200:203], v233 offset:6400\n\t"
            "v_cvt_pk_bf16_f32 v188, v188, v189\n\t"
            "v_mfma_f32_16x16x32_bf16 v[204:207], v[36:39], v[216:219], v[204:207]\n\t"
            "ds_read_b32 v237, v251 offset:64\n\t"
            "v_cvt_pk_bf16_f32 v189, v190, v191\n\t"
            "v_mfma_f32_16x16x32_bf16 v[204:207], v[40:43], v[220:223], v[204:207]\n\t"
            "v_mul_f32 v228, v238, v228\n\t"
            "v_mul_f32 v229, v238, v229\n\t"
            "v_mfma_f32_16x16x32_bf16 v[204:207], v[44:47], v[224:227], v[204:207]\n\t"
            "ds_write_b64 v245, v[188:189] offset:6400\n\t"
            "v_mfma_f32_16x16x32_bf16 v[208:211], v[108:111], v[216:219], v[208:211]\n\t"
            "ds_read_b128 v[188:191], v233 offset:6272\n\t"
            "v_mul_f32 v230, v238, v230\n\t"
            "v_mfma_f32_16x16x32_bf16 v[208:211], v[112:115], v[220:223], v[208:211]\n\t"
            "v_mul_f32 v231, v238, v231\n\t"
            "v_cvt_pk_bf16_f32 v228, v228, v229\n\t"
            "v_mfma_f32_16x16x32_bf16 v[208:211], v[116:119], v[224:227], v[208:211]\n\t"
            "v_cvt_pk_bf16_f32 v229, v230, v231\n\t"
            "v_mul_f32 v196, v238, v196\n\t"
            "v_mfma_f32_16x16x32_bf16 v[212:215], v[132:135], v[216:219], v[212:215]\n\t"
            "ds_read_b128 v[216:219], v232 offset:6144\n\t"
            "v_mul_f32 v197, v238, v197\n\t"
            "v_mfma_f32_16x16x32_bf16 v[212:215], v[136:139], v[220:223], v[212:215]\n\t"
            "s_waitcnt lgkmcnt(14)\n\t"
            "ds_read_b128 v[220:223], v233 offset:6144\n\t"
            "v_mul_f32 v198, v238, v198\n\t"
            "v_mfma_f32_16x16x32_bf16 v[212:215], v[140:143], v[224:227], v[212:215]\n\t"
            "s_waitcnt lgkmcnt(14)\n\t"
            "ds_read_b128 v[224:227], v232 offset:6272\n\t"
            "v_mul_f32 v199, v238, v199\n\t"
            "s_waitcnt lgkmcnt(9)\n\t"
            "v_mfma_f32_16x16x32_bf16 v[168:171], v[168:171], v[180:183], 0\n\t"
            "s_waitcnt lgkmcnt(7)\n\t"
            "ds_read_b32 v238, v251 offset:128\n\t"
            "v_cvt_pk_bf16_f32 v196, v196, v197\n\t"
            "v_mfma_f32_16x16x32_bf16 v[168:171], v[156:159], v[184:187], v[168:171]\n\t"
            "ds_write_b64 v245, v[228:229] offset:12416\n\t"
            "v_mfma_f32_16x16x32_bf16 v[176:179], v[176:179], v[180:183], 0\n\t"
            "v_cvt_pk_bf16_f32 v197, v198, v199\n\t"
            "v_mul_f32 v152, v236, v152\n\t"
            "v_mfma_f32_16x16x32_bf16 v[176:179], v[160:163], v[184:187], v[176:179]\n\t"
            "ds_write_b64 v245, v[196:197] offset:12544\n\t"
            "v_mfma_f32_16x16x32_bf16 v[172:175], v[172:175], v[180:183], 0\n\t"
            "v_mul_f32 v153, v236, v153\n\t"
            "v_mul_f32 v154, v236, v154\n\t"
            "v_mfma_f32_16x16x32_bf16 v[172:175], v[164:167], v[184:187], v[172:175]\n\t"
            "s_waitcnt lgkmcnt(5)\n\t"
            "v_mul_f32 v155, v236, v155\n\t"
            "v_cvt_pk_bf16_f32 v152, v152, v153\n\t"
            "v_mfma_f32_16x16x32_bf16 v[160:163], v[72:75], v[216:219], 0\n\t"
            "s_waitcnt lgkmcnt(4)\n\t"
            "v_cvt_pk_bf16_f32 v153, v154, v155\n\t"
            "v_mul_f32 v212, v236, v212\n\t"
            "v_mfma_f32_16x16x32_bf16 v[160:163], v[76:79], v[220:223], v[160:163]\n\t"
            "s_waitcnt lgkmcnt(3)\n\t"
            "v_mul_f32 v213, v236, v213\n\t"
            "v_mul_f32 v214, v236, v214\n\t"
            "v_mfma_f32_16x16x32_bf16 v[160:163], v[80:83], v[224:227], v[160:163]\n\t"
            "v_mul_f32 v215, v236, v215\n\t"
            "v_cvt_pk_bf16_f32 v154, v212, v213\n\t"
            "v_mfma_f32_16x16x32_bf16 v[160:163], v[84:87], v[188:191], v[160:163]\n\t"
            "v_cvt_pk_bf16_f32 v155, v214, v215\n\t"
            "v_mul_f32 v144, v236, v144\n\t"
            "v_mfma_f32_16x16x32_bf16 v[160:163], v[88:91], v[192:195], v[160:163]\n\t"
            "ds_read_b128 v[212:215], v232 offset:12416\n\t"
            "v_mul_f32 v145, v236, v145\n\t"
            "v_mfma_f32_16x16x32_bf16 v[160:163], v[92:95], v[200:203], v[160:163]\n\t"
            "v_mul_f32 v146, v236, v146\n\t"
            "v_mul_f32 v147, v236, v147\n\t"
            "v_mfma_f32_16x16x32_bf16 v[228:231], v[240:243], v[180:183], 0\n\t"
            "v_cvt_pk_bf16_f32 v144, v144, v145\n\t"
            "v_mul_f32 v148, v236, v148\n\t"
            "v_mfma_f32_16x16x32_bf16 v[228:231], v[240:243], v[184:187], v[228:231]\n\t"
            "v_mul_f32 v149, v236, v149\n\t"
            "v_cvt_pk_bf16_f32 v145, v146, v147\n\t"
            "v_mfma_f32_16x16x32_bf16 v[196:199], v[24:27], v[216:219], 0\n\t"
            "v_mul_f32 v204, v236, v204\n\t"
            "v_mul_f32 v205, v236, v205\n\t"
            "v_mfma_f32_16x16x32_bf16 v[196:199], v[28:31], v[220:223], v[196:199]\n\t"
            "v_mul_f32 v206, v236, v206\n\t"
            "v_mul_f32 v207, v236, v207\n\t"
            "v_mfma_f32_16x16x32_bf16 v[196:199], v[32:35], v[224:227], v[196:199]\n\t"
            "v_rcp_f32 v229, v228\n\t"
            "v_mfma_f32_16x16x32_bf16 v[196:199], v[36:39], v[188:191], v[196:199]\n\t"
            "v_mul_f32 v168, v229, v168\n\t"
            "v_mul_f32 v169, v229, v169\n\t"
            "v_mfma_f32_16x16x32_bf16 v[196:199], v[40:43], v[192:195], v[196:199]\n\t"
            "v_mul_f32 v172, v229, v172\n\t"
            "v_mul_f32 v173, v229, v173\n\t"
            "v_mfma_f32_16x16x32_bf16 v[196:199], v[44:47], v[200:203], v[196:199]\n\t"
            "v_mul_f32 v176, v229, v176\n\t"
            "v_mul_f32 v177, v229, v177\n\t"
            "v_mfma_f32_16x16x32_bf16 v[156:159], v[0:3], v[216:219], 0\n\t"
            "v_mul_f32 v170, v229, v170\n\t"
            "v_mul_f32 v171, v229, v171\n\t"
            "v_mfma_f32_16x16x32_bf16 v[156:159], v[4:7], v[220:223], v[156:159]\n\t"
            "v_cvt_pk_bf16_f32 v168, v168, v169\n\t"
            "v_mul_f32 v174, v229, v174\n\t"
            "v_mfma_f32_16x16x32_bf16 v[156:159], v[8:11], v[224:227], v[156:159]\n\t"
            "v_mul_f32 v175, v229, v175\n\t"
            "v_cvt_pk_bf16_f32 v172, v172, v173\n\t"
            "v_mfma_f32_16x16x32_bf16 v[156:159], v[12:15], v[188:191], v[156:159]\n\t"
            "v_mul_f32 v178, v229, v178\n\t"
            "v_mul_f32 v179, v229, v179\n\t"
            "v_mfma_f32_16x16x32_bf16 v[156:159], v[16:19], v[192:195], v[156:159]\n\t"
            "v_cvt_pk_bf16_f32 v176, v176, v177\n\t"
            "v_xor_b32 v230, s73, v255\n\t"
            "v_mfma_f32_16x16x32_bf16 v[156:159], v[20:23], v[200:203], v[156:159]\n\t"
            "v_cvt_pk_bf16_f32 v169, v170, v171\n\t"
            "v_cvt_pk_bf16_f32 v173, v174, v175\n\t"
            "v_cvt_pk_bf16_f32 v177, v178, v179\n\t"
            "v_mul_f32 v150, v236, v150\n\t"
            "v_mfma_f32_16x16x16_bf16 v[180:183], v[152:153], v[154:155], 0\n\t"
            "s_mov_b64 exec, s[68:69]\n\t"
            "ds_write_b64 v230, v[168:169]\n\t"
            "ds_write_b64 v230, v[172:173] offset:128\n\t"
            "ds_write_b64 v230, v[176:177] offset:256\n\t"
            "s_mov_b64 exec, -1\n\t"
            "v_mfma_f32_16x16x32_bf16 v[164:167], v[48:51], v[216:219], 0\n\t"
            "v_mul_f32 v151, v236, v151\n\t"
            "v_cvt_pk_bf16_f32 v148, v148, v149\n\t"
            "v_mfma_f32_16x16x32_bf16 v[164:167], v[52:55], v[220:223], v[164:167]\n\t"
            "v_cvt_pk_bf16_f32 v146, v204, v205\n\t"
            "v_cvt_pk_bf16_f32 v147, v206, v207\n\t"
            "v_mfma_f32_16x16x32_bf16 v[164:167], v[56:59], v[224:227], v[164:167]\n\t"
            "ds_read_b128 v[204:207], v232 offset:12288\n\t"
            "v_cvt_pk_bf16_f32 v149, v150, v151\n\t"
            "v_mfma_f32_16x16x32_bf16 v[164:167], v[60:63], v[188:191], v[164:167]\n\t"
            "v_mul_f32 v208, v236, v208\n\t"
            "v_mul_f32 v209, v236, v209\n\t"
            "v_mfma_f32_16x16x32_bf16 v[164:167], v[64:67], v[192:195], v[164:167]\n\t"
            "v_mul_f32 v210, v236, v210\n\t"
            "v_mul_f32 v211, v236, v211\n\t"
            "v_mfma_f32_16x16x32_bf16 v[164:167], v[68:71], v[200:203], v[164:167]\n\t"
            "v_cvt_pk_bf16_f32 v150, v208, v209\n\t"
            "v_cvt_pk_bf16_f32 v151, v210, v211\n\t"
            "v_mfma_f32_16x16x32_bf16 v[228:231], v[96:99], v[216:219], 0\n\t"
            "ds_read_b128 v[208:211], v233 offset:12288\n\t"
            "v_mul_f32 v156, v237, v156\n\t"
            "v_mfma_f32_16x16x32_bf16 v[228:231], v[100:103], v[220:223], v[228:231]\n\t"
            "v_mul_f32 v157, v237, v157\n\t"
            "v_mul_f32 v158, v237, v158\n\t"
            "v_mfma_f32_16x16x32_bf16 v[228:231], v[104:107], v[224:227], v[228:231]\n\t"
            "v_mul_f32 v159, v237, v159\n\t"
            "v_cvt_pk_bf16_f32 v156, v156, v157\n\t"
            "v_mfma_f32_16x16x32_bf16 v[228:231], v[108:111], v[188:191], v[228:231]\n\t"
            "v_cvt_pk_bf16_f32 v157, v158, v159\n\t"
            "v_mul_f32 v196, v237, v196\n\t"
            "v_mfma_f32_16x16x32_bf16 v[228:231], v[112:115], v[192:195], v[228:231]\n\t"
            "v_mul_f32 v197, v237, v197\n\t"
            "v_mul_f32 v198, v237, v198\n\t"
            "v_mfma_f32_16x16x32_bf16 v[228:231], v[116:119], v[200:203], v[228:231]\n\t"
            "v_mul_f32 v199, v237, v199\n\t"
            "v_cvt_pk_bf16_f32 v158, v196, v197\n\t"
            "v_mfma_f32_16x16x32_bf16 v[180:183], v[144:147], v[148:151], v[180:183]\n\t"
            "v_cvt_pk_bf16_f32 v159, v198, v199\n\t"
            "v_mul_f32 v164, v237, v164\n\t"
            "v_mfma_f32_16x16x32_bf16 v[196:199], v[120:123], v[216:219], 0\n\t"
            "ds_read_b128 v[216:219], v233 offset:12416\n\t"
            "v_mul_f32 v165, v237, v165\n\t"
            "v_mfma_f32_16x16x32_bf16 v[196:199], v[124:127], v[220:223], v[196:199]\n\t"
            "ds_read_b128 v[220:223], v232 offset:12544\n\t"
            "v_mul_f32 v166, v237, v166\n\t"
            "v_mfma_f32_16x16x32_bf16 v[196:199], v[128:131], v[224:227], v[196:199]\n\t"
            "ds_read_b128 v[224:227], v233 offset:12544\n\t"
            "v_mul_f32 v167, v237, v167\n\t"
            "v_mfma_f32_16x16x32_bf16 v[196:199], v[132:135], v[188:191], v[196:199]\n\t"
            "v_cvt_pk_bf16_f32 v164, v164, v165\n\t"
            "v_cvt_pk_bf16_f32 v165, v166, v167\n\t"
            "v_mfma_f32_16x16x32_bf16 v[196:199], v[136:139], v[192:195], v[196:199]\n\t"
            "v_mul_f32 v160, v237, v160\n\t"
            "v_mul_f32 v161, v237, v161\n\t"
            "v_mfma_f32_16x16x32_bf16 v[196:199], v[140:143], v[200:203], v[196:199]\n\t"
            "s_waitcnt lgkmcnt(4)\n\t"
            "v_mul_f32 v162, v237, v162\n\t"
            "v_mul_f32 v163, v237, v163\n\t"
            "v_mfma_f32_16x16x32_bf16 v[176:179], v[48:51], v[204:207], 0\n\t"
            "s_barrier\n\t"
            "s_setprio 2\n\t"
            "v_cvt_pk_bf16_f32 v160, v160, v161\n\t"
            "v_cvt_pk_bf16_f32 v161, v162, v163\n\t"
            "s_waitcnt lgkmcnt(3)\n\t"
            "v_mfma_f32_16x16x32_bf16 v[176:179], v[52:55], v[208:211], v[176:179]\n\t"
            "v_mul_f32 v196, v237, v196\n\t"
            "v_mul_f32 v197, v237, v197\n\t"
            "v_mfma_f32_16x16x32_bf16 v[176:179], v[56:59], v[212:215], v[176:179]\n\t"
            "s_waitcnt lgkmcnt(2)\n\t"
            "v_mul_f32 v198, v237, v198\n\t"
            "v_mul_f32 v199, v237, v199\n\t"
            "v_mfma_f32_16x16x32_bf16 v[176:179], v[60:63], v[216:219], v[176:179]\n\t"
            "s_waitcnt lgkmcnt(1)\n\t"
            "v_cvt_pk_bf16_f32 v166, v196, v197\n\t"
            "v_cvt_pk_bf16_f32 v167, v198, v199\n\t"
            "v_mfma_f32_16x16x32_bf16 v[176:179], v[64:67], v[220:223], v[176:179]\n\t"
            "s_waitcnt lgkmcnt(0)\n\t"
            "v_mul_f32 v228, v237, v228\n\t"
            "v_mul_f32 v229, v237, v229\n\t"
            "v_mfma_f32_16x16x32_bf16 v[176:179], v[68:71], v[224:227], v[176:179]\n\t"
            "v_mul_f32 v230, v237, v230\n\t"
            "v_mul_f32 v231, v237, v231\n\t"
            "v_cvt_pk_bf16_f32 v162, v228, v229\n\t"
            "v_cvt_pk_bf16_f32 v163, v230, v231\n\t"
            "v_mfma_f32_16x16x16_bf16 v[188:191], v[152:153], v[166:167], 0\n\t"
            "v_exp_f32 v180, v180\n\t"
            "v_exp_f32 v181, v181\n\t"
            "v_mfma_f32_16x16x16_bf16 v[192:195], v[164:165], v[166:167], 0\n\t"
            "v_exp_f32 v182, v182\n\t"
            "v_exp_f32 v183, v183\n\t"
            "v_mfma_f32_16x16x16_bf16 v[184:187], v[164:165], v[154:155], 0\n\t"
            "v_mul_f32 v176, v238, v176\n\t"
            "v_mul_f32 v177, v238, v177\n\t"
            "v_mul_f32 v178, v238, v178\n\t"
            "v_mfma_f32_16x16x32_bf16 v[168:171], v[0:3], v[204:207], 0\n\t"
            "s_bfe_u32 s49, s70, 0x30001\n\t"
            "v_mul_f32 v179, v238, v179\n\t"
            "v_cvt_pk_bf16_f32 v176, v176, v177\n\t"
            "v_mfma_f32_16x16x32_bf16 v[168:171], v[4:7], v[208:211], v[168:171]\n\t"
            "s_lshl_b32 s49, s49, 4\n\t"
            "v_cvt_pk_bf16_f32 v180, v180, v181\n\t"
            "v_cvt_pk_bf16_f32 v177, v178, v179\n\t"
            "v_mfma_f32_16x16x32_bf16 v[168:171], v[8:11], v[212:215], v[168:171]\n\t"
            "s_or_b32 s73, s77, s49\n\t"
            "v_cvt_pk_bf16_f32 v181, v182, v183\n\t"
            "v_mfma_f32_16x16x32_bf16 v[168:171], v[12:15], v[216:219], v[168:171]\n\t"
            "v_mfma_f32_16x16x32_bf16 v[168:171], v[16:19], v[220:223], v[168:171]\n\t"
            "v_mfma_f32_16x16x32_bf16 v[168:171], v[20:23], v[224:227], v[168:171]\n\t"
            "v_mfma_f32_16x16x32_bf16 v[188:191], v[144:147], v[160:163], v[188:191]\n\t"
            "v_mfma_f32_16x16x32_bf16 v[200:203], v[120:123], v[204:207], 0\n\t"
            "v_mfma_f32_16x16x32_bf16 v[200:203], v[124:127], v[208:211], v[200:203]\n\t"
            "v_mfma_f32_16x16x32_bf16 v[200:203], v[128:131], v[212:215], v[200:203]\n\t"
            "v_mfma_f32_16x16x32_bf16 v[200:203], v[132:135], v[216:219], v[200:203]\n\t"
            "v_mfma_f32_16x16x32_bf16 v[200:203], v[136:139], v[220:223], v[200:203]\n\t"
            "s_add_u32 s70, s70, 1\n\t"
            "s_nop 0\n\t"
            "v_mul_f32 v168, v238, v168\n\t"
            "v_mfma_f32_16x16x32_bf16 v[200:203], v[140:143], v[224:227], v[200:203]\n\t"
            "s_add_u32 s76, s76, 800\n\t"
            "v_exp_f32 v188, v188\n\t"
            "v_mfma_f32_16x16x32_bf16 v[192:195], v[156:159], v[160:163], v[192:195]\n\t"
            "s_setprio 0\n\t"
            "v_exp_f32 v189, v189\n\t"
            "v_mfma_f32_16x16x32_bf16 v[172:175], v[72:75], v[204:207], 0\n\t"
            "s_add_u32 s77, s77, 384\n\t"
            "v_exp_f32 v190, v190\n\t"
            "v_mfma_f32_16x16x32_bf16 v[172:175], v[76:79], v[208:211], v[172:175]\n\t"
            "s_xor_b32 s48, s48, s47\n\t"
            "v_exp_f32 v191, v191\n\t"
            "v_mfma_f32_16x16x32_bf16 v[172:175], v[80:83], v[212:215], v[172:175]\n\t"
            "s_sub_u32 s44, s44, 1\n\t"
            "v_exp_f32 v192, v192\n\t"
            "v_mfma_f32_16x16x32_bf16 v[172:175], v[84:87], v[216:219], v[172:175]\n\t"
            "v_exp_f32 v193, v193\n\t"
            "v_mfma_f32_16x16x32_bf16 v[172:175], v[88:91], v[220:223], v[172:175]\n\t"
            "v_exp_f32 v194, v194\n\t"
            "v_mfma_f32_16x16x32_bf16 v[172:175], v[92:95], v[224:227], v[172:175]\n\t"
            "v_exp_f32 v195, v195\n\t"
            "v_mfma_f32_16x16x32_bf16 v[184:187], v[156:159], v[148:151], v[184:187]\n\t"
            "v_cvt_pk_bf16_f32 v188, v188, v189\n\t"
            "v_cvt_pk_bf16_f32 v189, v190, v191\n\t"
            "v_mfma_f32_16x16x32_bf16 v[196:199], v[24:27], v[204:207], 0\n\t"
            "v_cvt_pk_bf16_f32 v190, v192, v193\n\t"
            "v_cvt_pk_bf16_f32 v191, v194, v195\n\t"
            "v_mfma_f32_16x16x32_bf16 v[196:199], v[28:31], v[208:211], v[196:199]\n\t"
            "v_mul_f32 v169, v238, v169\n\t"
            "v_mul_f32 v170, v238, v170\n\t"
            "v_mfma_f32_16x16x32_bf16 v[196:199], v[32:35], v[212:215], v[196:199]\n\t"
            "v_exp_f32 v184, v184\n\t"
            "v_mfma_f32_16x16x32_bf16 v[196:199], v[36:39], v[216:219], v[196:199]\n\t"
            "v_exp_f32 v185, v185\n\t"
            "v_mfma_f32_16x16x32_bf16 v[196:199], v[40:43], v[220:223], v[196:199]\n\t"
            "v_exp_f32 v186, v186\n\t"
            "v_mfma_f32_16x16x32_bf16 v[196:199], v[44:47], v[224:227], v[196:199]\n\t"
            "v_exp_f32 v187, v187\n\t"
            "v_cvt_pk_bf16_f32 v182, v184, v185\n\t"
            "v_cvt_pk_bf16_f32 v183, v186, v187\n\t"
            "v_mfma_f32_16x16x16_bf16 v[184:187], v[176:177], v[154:155], 0\n\t"
            "v_mul_f32 v171, v238, v171\n\t"
            "v_cvt_pk_bf16_f32 v168, v168, v169\n\t"
            "v_cvt_pk_bf16_f32 v169, v170, v171\n\t"
            "v_mfma_f32_16x16x16_bf16 v[192:195], v[176:177], v[166:167], 0\n\t"
            "v_mul_f32 v196, v238, v196\n\t"
            "v_mul_f32 v197, v238, v197\n\t"
            "v_mul_f32 v198, v238, v198\n\t"
            "v_mfma_f32_16x16x32_bf16 v[228:231], v[96:99], v[204:207], 0\n\t"
            "ds_read_b64 v[204:205], v235 offset:136\n\t"
            "v_mul_f32 v199, v238, v199\n\t"
            "v_mfma_f32_16x16x32_bf16 v[228:231], v[100:103], v[208:211], v[228:231]\n\t"
            "ds_read_b64 v[206:207], v234 offset:128\n\t"
            "v_cvt_pk_bf16_f32 v170, v196, v197\n\t"
            "v_mfma_f32_16x16x32_bf16 v[228:231], v[104:107], v[212:215], v[228:231]\n\t"
            "ds_read_b128 v[208:211], v234\n\t"
            "v_cvt_pk_bf16_f32 v171, v198, v199\n\t"
            "v_mfma_f32_16x16x32_bf16 v[228:231], v[108:111], v[216:219], v[228:231]\n\t"
            "ds_read_b128 v[212:215], v235 offset:64\n\t"
            "v_mul_f32 v172, v238, v172\n\t"
            "v_mfma_f32_16x16x32_bf16 v[228:231], v[112:115], v[220:223], v[228:231]\n\t"
            "v_mul_f32 v173, v238, v173\n\t"
            "v_mul_f32 v200, v238, v200\n\t"
            "v_mfma_f32_16x16x32_bf16 v[228:231], v[116:119], v[224:227], v[228:231]\n\t"
            "v_mul_f32 v201, v238, v201\n\t"
            "v_mul_f32 v202, v238, v202\n\t"
            "v_mfma_f32_16x16x32_bf16 v[184:187], v[168:171], v[148:151], v[184:187]\n\t"
            "v_mul_f32 v203, v238, v203\n\t"
            "v_mul_f32 v174, v238, v174\n\t"
            "v_mfma_f32_16x16x32_bf16 v[192:195], v[168:171], v[160:163], v[192:195]\n\t"
            "v_mul_f32 v175, v238, v175\n\t"
            "v_cvt_pk_bf16_f32 v172, v172, v173\n\t"
            "v_cvt_pk_bf16_f32 v178, v200, v201\n\t"
            "v_cvt_pk_bf16_f32 v179, v202, v203\n\t"
            "v_cvt_pk_bf16_f32 v173, v174, v175\n\t"
            "v_mul_f32 v228, v238, v228\n\t"
            "v_mul_f32 v229, v238, v229\n\t"
            "s_waitcnt lgkmcnt(2)\n\t"
            "v_mfma_f32_16x16x32_bf16 v[220:223], v[204:207], v[164:167], 0\n\t"
            "s_waitcnt lgkmcnt(1)\n\t"
            "v_mul_f32 v230, v238, v230\n\t"
            "v_mul_f32 v231, v238, v231\n\t"
            "v_mfma_f32_16x16x32_bf16 v[220:223], v[208:211], v[160:163], v[220:223]\n\t"
            "s_waitcnt lgkmcnt(0)\n\t"
            "v_cvt_pk_bf16_f32 v174, v228, v229\n\t"
            "v_cvt_pk_bf16_f32 v175, v230, v231\n\t"
            "v_mfma_f32_16x16x32_bf16 v[220:223], v[212:215], v[156:159], v[220:223]\n\t"
            "v_exp_f32 v184, v184\n\t"
            "v_mfma_f32_16x16x32_bf16 v[216:219], v[204:207], v[152:155], 0\n\t"
            "v_exp_f32 v185, v185\n\t"
            "v_mfma_f32_16x16x32_bf16 v[216:219], v[208:211], v[148:151], v[216:219]\n\t"
            "v_exp_f32 v186, v186\n\t"
            "v_mfma_f32_16x16x32_bf16 v[216:219], v[212:215], v[144:147], v[216:219]\n\t"
            "v_exp_f32 v187, v187\n\t"
            "v_cvt_pk_bf16_f32 v184, v184, v185\n\t"
            "v_cvt_pk_bf16_f32 v185, v186, v187\n\t"
            "v_mfma_f32_16x16x16_bf16 v[228:231], v[164:165], v[178:179], 0\n\t"
            "v_mov_b32 v187, 0\n\t"
            "v_exp_f32 v192, v192\n\t"
            "v_mfma_f32_16x16x16_bf16 v[200:203], v[176:177], v[178:179], 0\n\t"
            "v_exp_f32 v193, v193\n\t"
            "v_exp_f32 v216, v216\n\t"
            "v_mfma_f32_16x16x16_bf16 v[196:199], v[152:153], v[178:179], 0\n\t"
            "v_cvt_pk_bf16_f32 v216, v216, 0\n\t"
            "v_cndmask_b32_e64 v186, 0, v216, s[58:59]\n\t"
            "v_cndmask_b32_e64 v235, v217, v221, s[60:61]\n\t"
            "v_mfma_f32_16x16x32_bf16 v[224:227], v[204:207], v[176:179], 0\n\t"
            "v_exp_f32 v194, v194\n\t"
            "v_mfma_f32_16x16x32_bf16 v[224:227], v[208:211], v[172:175], v[224:227]\n\t"
            "v_exp_f32 v195, v195\n\t"
            "v_mfma_f32_16x16x32_bf16 v[224:227], v[212:215], v[168:171], v[224:227]\n\t"
            "v_exp_f32 v220, v220\n\t"
            "v_mfma_f32_16x16x32_bf16 v[204:207], v[240:243], v[180:183], 0\n\t"
            "v_cvt_pk_bf16_f32 v192, v192, v193\n\t"
            "v_cvt_pk_bf16_f32 v193, v194, v195\n\t"
            "v_mfma_f32_16x16x32_bf16 v[204:207], v[240:243], v[184:187], v[204:207]\n\t"
            "v_cvt_pk_bf16_f32 v220, v220, 0\n\t"
            "v_cndmask_b32_e64 v194, 0, v220, s[58:59]\n\t"
            "v_mfma_f32_16x16x32_bf16 v[200:203], v[168:171], v[172:175], v[200:203]\n\t"
            "v_exp_f32 v224, v224\n\t"
            "v_mfma_f32_16x16x32_bf16 v[196:199], v[144:147], v[172:175], v[196:199]\n\t"
            "v_cvt_pk_bf16_f32 v224, v224, 0\n\t"
            "v_cndmask_b32_e64 v235, v235, v225, s[62:63]\n\t"
            "v_mfma_f32_16x16x32_bf16 v[228:231], v[156:159], v[172:175], v[228:231]\n\t"
            "v_cndmask_b32_e64 v235, v235, v239, s[64:65]\n\t"
            "v_cmp_nlt_f32_e64 s[24:25], |v235|, s23\n\t"
            "v_exp_f32 v235, v235\n\t"
            "v_exp_f32 v200, v200\n\t"
            "v_exp_f32 v201, v201\n\t"
            "v_exp_f32 v196, v196\n\t"
            "v_exp_f32 v197, v197\n\t"
            "v_exp_f32 v198, v198\n\t"
            "v_exp_f32 v199, v199\n\t"
            "v_exp_f32 v228, v228\n\t"
            "v_exp_f32 v229, v229\n\t"
            "v_exp_f32 v230, v230\n\t"
            "v_exp_f32 v231, v231\n\t"
            "v_cvt_pk_bf16_f32 v196, v196, v197\n\t"
            "v_cvt_pk_bf16_f32 v197, v198, v199\n\t"
            "v_cvt_pk_bf16_f32 v198, v228, v229\n\t"
            "v_cvt_pk_bf16_f32 v199, v230, v231\n\t"
            "v_exp_f32 v202, v202\n\t"
            "v_exp_f32 v203, v203\n\t"
            "v_mfma_f32_16x16x32_bf16 v[212:215], v[240:243], v[196:199], 0\n\t"
            "v_cvt_pk_bf16_f32 v200, v200, v201\n\t"
            "v_cvt_pk_bf16_f32 v201, v202, v203\n\t"
            "v_cndmask_b32_e64 v202, 0, v224, s[58:59]\n\t"
            "v_mov_b32 v203, 0\n\t"
            "v_cvt_pk_bf16_f32 v235, v235, 0\n\t"
            "v_cndmask_b32_e64 v235, 0, v235, s[66:67]\n\t"
            "v_mfma_f32_16x16x32_bf16 v[212:215], v[240:243], v[200:203], v[212:215]\n\t"
            "ds_write_b16 v250, v235 offset:256\n\t"
            "v_mfma_f32_16x16x32_bf16 v[208:211], v[240:243], v[188:191], 0\n\t"
            "v_mov_b32 v195, 0\n\t"
            "v_add_u32 v205, 0xe0800000, v204\n\t"
            "v_rcp_f32 v236, v204\n\t"
            "v_mfma_f32_16x16x32_bf16 v[208:211], v[240:243], v[192:195], v[208:211]\n\t"
            "s_nop 1\n\t"
            "v_add_u32 v213, 0xe0800000, v212\n\t"
            "v_rcp_f32 v238, v212\n\t"
            "s_nop 3\n\t"
            "v_add_u32 v209, 0xe0800000, v208\n\t"
            "v_rcp_f32 v237, v208\n\t"
            "v_or3_b32 v205, v205, v209, v213\n\t"
            "v_cmp_le_u32 vcc, 2.0, v205\n\t"
            ".Lwa_in%=:\n\t"
            "s_or_b64 vcc, vcc, s[24:25]\n\t"
            "s_cbranch_scc1 .Lwa_slow%=\n\t"
            ".Lwa_ok%=:\n\t"
            "s_add_u32 s72, s72, 800\n\t"
            "s_cmp_gt_u32 s44, 2\n\t"
            "s_barrier\n\t"
            "s_setprio 1\n\t"
            "s_cbranch_scc1 .Lwa_loop%=\n\t"
            "s_cmp_eq_u32 s44, 0\n\t"
            "s_cbranch_scc1 .Lwa_done%=\n\t"
            "s_cmp_lg_u32 s44, 2\n\t"
            "s_cbranch_scc1 .Lwa_loop%=\n\t"
            "s_mov_b32 m0, s40\n\t"
            "v_lshlrev_b32 v234, 3, v248\n\t"
            "buffer_load_dword v234, s[36:39], s41 offen sc1 lds\n\t"
            "s_cmp_lg_u32 s33, 0\n\t"
            "s_cbranch_scc1 .Lar%=\n\t"
            "s_mov_b64 exec, 1\n\t"
            "v_mov_b32 v235, 1\n\t"
            "s_mov_b32 s49, 295168\n\t"
            "buffer_atomic_add v235, off, s[28:31], s49\n\t"
            "s_mov_b64 exec, -1\n\t"
            ".Lar%=:\n\t"
            "s_branch .Lwa_loop%=\n\t"
            ".Lwa_slow%=:\n\t"
            "s_waitcnt lgkmcnt(0)\n\t"
            "s_nop 7\n\t"
            "v_mfma_f32_16x16x16_bf16 v[180:183], v[152:153], v[154:155], 0\n\t"
            "v_add_u32 v234, s72, v252\n\t"
            "v_cndmask_b32_e64 v235, v246, v234, s[50:51]\n\t"
            "v_cndmask_b32_e64 v234, v246, v234, s[26:27]\n\t"
            "v_mfma_f32_16x16x16_bf16 v[228:231], v[164:165], v[154:155], 0\n\t"
            "ds_read_b64 v[216:217], v235 offset:136\n\t"
            "ds_read_b64 v[218:219], v234 offset:128\n\t"
            "v_mfma_f32_16x16x16_bf16 v[184:187], v[176:177], v[154:155], 0\n\t"
            "ds_read_b128 v[220:223], v234\n\t"
            "ds_read_b128 v[224:227], v235 offset:64\n\t"
            "v_mfma_f32_16x16x32_bf16 v[180:183], v[144:147], v[148:151], v[180:183]\n\t"
            "v_mfma_f32_16x16x32_bf16 v[228:231], v[156:159], v[148:151], v[228:231]\n\t"
            "s_nop 1\n\t"
            "v_mfma_f32_16x16x32_bf16 v[184:187], v[168:171], v[148:151], v[184:187]\n\t"
            "v_mfma_f32_16x16x16_bf16 v[188:191], v[152:153], v[166:167], 0\n\t"
            "v_mfma_f32_16x16x16_bf16 v[192:195], v[176:177], v[166:167], 0\n\t"
            "v_mfma_f32_16x16x16_bf16 v[196:199], v[152:153], v[178:179], 0\n\t"
            "s_waitcnt lgkmcnt(2)\n\t"
            "v_mfma_f32_16x16x32_bf16 v[204:207], v[216:219], v[152:155], 0\n\t"
            "s_waitcnt lgkmcnt(1)\n\t"
            "v_mfma_f32_16x16x32_bf16 v[204:207], v[220:223], v[148:151], v[204:207]\n\t"
            "s_waitcnt lgkmcnt(0)\n\t"
            "v_mfma_f32_16x16x32_bf16 v[204:207], v[224:227], v[144:147], v[204:207]\n\t"
            "v_mfma_f32_16x16x32_bf16 v[208:211], v[216:219], v[164:167], 0\n\t"
            "v_mfma_f32_16x16x32_bf16 v[208:211], v[220:223], v[160:163], v[208:211]\n\t"
            "v_mfma_f32_16x16x32_bf16 v[208:211], v[224:227], v[156:159], v[208:211]\n\t"
            "v_mfma_f32_16x16x32_bf16 v[188:191], v[144:147], v[160:163], v[188:191]\n\t"
            "v_mfma_f32_16x16x32_bf16 v[192:195], v[168:171], v[160:163], v[192:195]\n\t"
            "v_mfma_f32_16x16x32_bf16 v[212:215], v[216:219], v[176:179], 0\n\t"
            "s_nop 1\n\t"
            "v_max3_f32 v232, v204, v180, v181\n\t"
            "v_mfma_f32_16x16x32_bf16 v[212:215], v[220:223], v[172:175], v[212:215]\n\t"
            "v_max3_f32 v232, v232, v182, v183\n\t"
            "v_max3_f32 v232, v232, v228, v229\n\t"
            "v_mfma_f32_16x16x32_bf16 v[212:215], v[224:227], v[168:171], v[212:215]\n\t"
            "v_max3_f32 v232, v232, v230, v231\n\t"
            "v_max3_f32 v232, v232, v184, v185\n\t"
            "v_mfma_f32_16x16x32_bf16 v[196:199], v[144:147], v[172:175], v[196:199]\n\t"
            "v_max3_f32 v232, v232, v186, v187\n\t"
            "v_mov_b32 v233, v232\n\t"
            "v_cndmask_b32_e64 v235, v205, v209, s[60:61]\n\t"
            "v_mfma_f32_16x16x16_bf16 v[200:203], v[176:177], v[178:179], 0\n\t"
            "v_permlane32_swap_b32 v232, v233\n\t"
            "v_max_f32 v232, v232, v233\n\t"
            "v_mov_b32 v233, v232\n\t"
            "v_max3_f32 v234, v205, v209, v213\n\t"
            "v_max_f32 v234, v234, v239\n\t"
            "v_permlane16_swap_b32 v232, v233\n\t"
            "v_mfma_f32_16x16x32_bf16 v[200:203], v[168:171], v[172:175], v[200:203]\n\t"
            "v_max_f32 v232, v232, v233\n\t"
            "v_sub_f32 v180, v180, v232\n\t"
            "v_sub_f32 v181, v181, v232\n\t"
            "v_exp_f32 v180, v180\n\t"
            "v_exp_f32 v181, v181\n\t"
            "v_sub_f32 v182, v182, v232\n\t"
            "v_sub_f32 v183, v183, v232\n\t"
            "v_exp_f32 v182, v182\n\t"
            "v_exp_f32 v183, v183\n\t"
            "v_sub_f32 v228, v228, v232\n\t"
            "v_sub_f32 v229, v229, v232\n\t"
            "v_sub_f32 v230, v230, v232\n\t"
            "v_sub_f32 v231, v231, v232\n\t"
            "v_exp_f32 v228, v228\n\t"
            "v_exp_f32 v229, v229\n\t"
            "v_exp_f32 v230, v230\n\t"
            "v_exp_f32 v231, v231\n\t"
            "v_cvt_pk_bf16_f32 v180, v180, v181\n\t"
            "v_cvt_pk_bf16_f32 v181, v182, v183\n\t"
            "v_cvt_pk_bf16_f32 v182, v228, v229\n\t"
            "v_cvt_pk_bf16_f32 v183, v230, v231\n\t"
            "v_mfma_f32_16x16x16_bf16 v[228:231], v[164:165], v[166:167], 0\n\t"
            "v_sub_f32 v184, v184, v232\n\t"
            "v_sub_f32 v185, v185, v232\n\t"
            "v_sub_f32 v186, v186, v232\n\t"
            "v_sub_f32 v187, v187, v232\n\t"
            "v_sub_f32 v204, v204, v232\n\t"
            "v_max3_f32 v232, v208, v188, v189\n\t"
            "v_max3_f32 v232, v232, v190, v191\n\t"
            "v_mfma_f32_16x16x32_bf16 v[228:231], v[156:159], v[160:163], v[228:231]\n\t"
            "v_max_f32_dpp v234, v234, v234 row_ror:8 row_mask:0xf bank_mask:0xf\n\t"
            "v_cndmask_b32_e64 v235, v235, v213, s[62:63]\n\t"
            "v_cndmask_b32_e64 v235, v235, v239, s[64:65]\n\t"
            "v_max_f32_dpp v234, v234, v234 row_ror:4 row_mask:0xf bank_mask:0xf\n\t"
            "v_exp_f32 v184, v184\n\t"
            "v_exp_f32 v185, v185\n\t"
            "v_max_f32_dpp v234, v234, v234 row_ror:2 row_mask:0xf bank_mask:0xf\n\t"
            "v_exp_f32 v186, v186\n\t"
            "v_max3_f32 v232, v232, v228, v229\n\t"
            "v_max3_f32 v232, v232, v230, v231\n\t"
            "v_max3_f32 v232, v232, v192, v193\n\t"
            "v_max3_f32 v232, v232, v194, v195\n\t"
            "v_mov_b32 v233, v232\n\t"
            "v_max_f32_dpp v234, v234, v234 row_ror:1 row_mask:0xf bank_mask:0xf\n\t"
            "v_sub_f32 v235, v235, v234\n\t"
            "v_permlane32_swap_b32 v232, v233\n\t"
            "v_max_f32 v232, v232, v233\n\t"
            "v_mov_b32 v233, v232\n\t"
            "v_exp_f32 v235, v235\n\t"
            "v_exp_f32 v187, v187\n\t"
            "v_permlane16_swap_b32 v232, v233\n\t"
            "v_max_f32 v232, v232, v233\n\t"
            "v_sub_f32 v188, v188, v232\n\t"
            "v_sub_f32 v189, v189, v232\n\t"
            "v_exp_f32 v188, v188\n\t"
            "v_exp_f32 v189, v189\n\t"
            "v_sub_f32 v190, v190, v232\n\t"
            "v_sub_f32 v191, v191, v232\n\t"
            "v_exp_f32 v190, v190\n\t"
            "v_exp_f32 v191, v191\n\t"
            "v_sub_f32 v228, v228, v232\n\t"
            "v_sub_f32 v229, v229, v232\n\t"
            "v_sub_f32 v230, v230, v232\n\t"
            "v_sub_f32 v231, v231, v232\n\t"
            "v_exp_f32 v228, v228\n\t"
            "v_exp_f32 v229, v229\n\t"
            "v_exp_f32 v230, v230\n\t"
            "v_exp_f32 v231, v231\n\t"
            "v_cvt_pk_bf16_f32 v188, v188, v189\n\t"
            "v_cvt_pk_bf16_f32 v189, v190, v191\n\t"
            "v_cvt_pk_bf16_f32 v190, v228, v229\n\t"
            "v_cvt_pk_bf16_f32 v191, v230, v231\n\t"
            "v_mfma_f32_16x16x16_bf16 v[228:231], v[164:165], v[178:179], 0\n\t"
            "v_sub_f32 v192, v192, v232\n\t"
            "v_sub_f32 v193, v193, v232\n\t"
            "v_sub_f32 v194, v194, v232\n\t"
            "v_sub_f32 v195, v195, v232\n\t"
            "v_sub_f32 v208, v208, v232\n\t"
            "v_max3_f32 v232, v212, v196, v197\n\t"
            "v_max3_f32 v232, v232, v198, v199\n\t"
            "v_mfma_f32_16x16x32_bf16 v[228:231], v[156:159], v[172:175], v[228:231]\n\t"
            "v_exp_f32 v192, v192\n\t"
            "v_exp_f32 v193, v193\n\t"
            "v_exp_f32 v204, v204\n\t"
            "v_exp_f32 v194, v194\n\t"
            "v_exp_f32 v195, v195\n\t"
            "v_exp_f32 v208, v208\n\t"
            "v_cvt_pk_bf16_f32 v184, v184, v185\n\t"
            "v_cvt_pk_bf16_f32 v192, v192, v193\n\t"
            "v_max3_f32 v232, v232, v228, v229\n\t"
            "v_max3_f32 v232, v232, v230, v231\n\t"
            "v_max3_f32 v232, v232, v200, v201\n\t"
            "v_max3_f32 v232, v232, v202, v203\n\t"
            "v_mov_b32 v233, v232\n\t"
            "v_cvt_pk_bf16_f32 v235, v235, 0\n\t"
            "v_cvt_pk_bf16_f32 v185, v186, v187\n\t"
            "v_permlane32_swap_b32 v232, v233\n\t"
            "v_max_f32 v232, v232, v233\n\t"
            "v_mov_b32 v233, v232\n\t"
            "v_cvt_pk_bf16_f32 v204, v204, 0\n\t"
            "v_cvt_pk_bf16_f32 v193, v194, v195\n\t"
            "v_permlane16_swap_b32 v232, v233\n\t"
            "v_max_f32 v232, v232, v233\n\t"
            "v_sub_f32 v196, v196, v232\n\t"
            "v_sub_f32 v197, v197, v232\n\t"
            "v_exp_f32 v196, v196\n\t"
            "v_exp_f32 v197, v197\n\t"
            "v_sub_f32 v198, v198, v232\n\t"
            "v_sub_f32 v199, v199, v232\n\t"
            "v_exp_f32 v198, v198\n\t"
            "v_exp_f32 v199, v199\n\t"
            "v_sub_f32 v228, v228, v232\n\t"
            "v_sub_f32 v229, v229, v232\n\t"
            "v_sub_f32 v230, v230, v232\n\t"
            "v_sub_f32 v231, v231, v232\n\t"
            "v_sub_f32 v200, v200, v232\n\t"
            "v_sub_f32 v201, v201, v232\n\t"
            "v_exp_f32 v228, v228\n\t"
            "v_exp_f32 v229, v229\n\t"
            "v_exp_f32 v230, v230\n\t"
            "v_exp_f32 v231, v231\n\t"
            "v_exp_f32 v200, v200\n\t"
            "v_exp_f32 v201, v201\n\t"
            "v_sub_f32 v202, v202, v232\n\t"
            "v_sub_f32 v203, v203, v232\n\t"
            "v_sub_f32 v212, v212, v232\n\t"
            "v_cvt_pk_bf16_f32 v196, v196, v197\n\t"
            "v_exp_f32 v202, v202\n\t"
            "v_exp_f32 v203, v203\n\t"
            "v_exp_f32 v212, v212\n\t"
            "v_cvt_pk_bf16_f32 v197, v198, v199\n\t"
            "v_cvt_pk_bf16_f32 v198, v228, v229\n\t"
            "v_cvt_pk_bf16_f32 v199, v230, v231\n\t"
            "v_cvt_pk_bf16_f32 v200, v200, v201\n\t"
            "v_cvt_pk_bf16_f32 v208, v208, 0\n\t"
            "v_cvt_pk_bf16_f32 v201, v202, v203\n\t"
            "v_cvt_pk_bf16_f32 v212, v212, 0\n\t"
            "v_cndmask_b32_e64 v235, 0, v235, s[66:67]\n\t"
            "v_cndmask_b32_e64 v186, 0, v204, s[58:59]\n\t"
            "v_mfma_f32_16x16x32_bf16 v[204:207], v[240:243], v[180:183], 0\n\t"
            "v_cndmask_b32_e64 v194, 0, v208, s[58:59]\n\t"
            "v_cndmask_b32_e64 v202, 0, v212, s[58:59]\n\t"
            "v_mfma_f32_16x16x32_bf16 v[208:211], v[240:243], v[188:191], 0\n\t"
            "ds_write_b16 v250, v235 offset:256\n\t"
            "v_mfma_f32_16x16x32_bf16 v[212:215], v[240:243], v[196:199], 0\n\t"
            "v_mov_b32 v187, 0\n\t"
            "v_mov_b32 v195, 0\n\t"
            "v_mov_b32 v203, 0\n\t"
            "v_mfma_f32_16x16x32_bf16 v[204:207], v[240:243], v[184:187], v[204:207]\n\t"
            "v_mfma_f32_16x16x32_bf16 v[208:211], v[240:243], v[192:195], v[208:211]\n\t"
            "v_mfma_f32_16x16x32_bf16 v[212:215], v[240:243], v[200:203], v[212:215]\n\t"
            "s_nop 5\n\t"
            "v_rcp_f32 v236, v204\n\t"
            "v_rcp_f32 v237, v208\n\t"
            "v_rcp_f32 v238, v212\n\t"
            "s_waitcnt lgkmcnt(0)\n\t"
            "s_nop 7\n\t"
            "s_branch .Lwa_ok%=\n\t"
            ".Lwa_done%=:\n\t"
            "s_setprio 2\n\t"
            "s_waitcnt lgkmcnt(0)\n\t"
            "s_nop 7\n\t"
            "ds_read_b64 v[156:157], v247\n\t"
            "v_add_u32 v235, s45, v248\n\t"
            "v_xor_b32 v234, s73, v254\n\t"
            "v_mov_b32 v159, 0\n\t"
            "v_mov_b32 v163, 0\n\t"
            "v_mov_b32 v167, 0\n\t"
            "ds_read_b128 v[168:171], v235\n\t"
            "ds_read_u16 v158, v234\n\t"
            "ds_read_b128 v[172:175], v235 offset:1536\n\t"
            "ds_read_b128 v[176:179], v235 offset:3072\n\t"
            "ds_read_b64 v[160:161], v247 offset:3072\n\t"
            "ds_read_u16 v162, v234 offset:256\n\t"
            "ds_read_b64 v[164:165], v247 offset:1536\n\t"
            "ds_read_u16 v166, v234 offset:128\n\t"
            "s_waitcnt lgkmcnt(7)\n\t"
            "v_mfma_f32_16x16x32_bf16 v[228:231], v[168:171], v[180:183], 0\n\t"
            "s_waitcnt lgkmcnt(6)\n\t"
            "v_mfma_f32_16x16x32_bf16 v[228:231], v[156:159], v[184:187], v[228:231]\n\t"
            "s_nop 7\n\t"
            "v_mul_f32 v228, v236, v228\n\t"
            "v_mul_f32 v229, v236, v229\n\t"
            "v_mul_f32 v230, v236, v230\n\t"
            "v_mul_f32 v231, v236, v231\n\t"
            "v_cvt_pk_bf16_f32 v228, v228, v229\n\t"
            "v_cvt_pk_bf16_f32 v229, v230, v231\n\t"
            "ds_write_b64 v245, v[228:229]\n\t"
            "s_waitcnt lgkmcnt(6)\n\t"
            "v_mfma_f32_16x16x32_bf16 v[228:231], v[172:175], v[180:183], 0\n\t"
            "s_waitcnt lgkmcnt(5)\n\t"
            "v_mfma_f32_16x16x32_bf16 v[180:183], v[176:179], v[180:183], 0\n\t"
            "s_waitcnt lgkmcnt(3)\n\t"
            "v_mfma_f32_16x16x32_bf16 v[180:183], v[160:163], v[184:187], v[180:183]\n\t"
            "s_waitcnt lgkmcnt(1)\n\t"
            "v_mfma_f32_16x16x32_bf16 v[228:231], v[164:167], v[184:187], v[228:231]\n\t"
            "v_mfma_f32_16x16x32_bf16 v[184:187], v[168:171], v[188:191], 0\n\t"
            "v_mfma_f32_16x16x32_bf16 v[184:187], v[156:159], v[192:195], v[184:187]\n\t"
            "s_nop 3\n\t"
            "v_mul_f32 v180, v236, v180\n\t"
            "v_mul_f32 v181, v236, v181\n\t"
            "v_mul_f32 v182, v236, v182\n\t"
            "v_mul_f32 v183, v236, v183\n\t"
            "v_cvt_pk_bf16_f32 v180, v180, v181\n\t"
            "v_cvt_pk_bf16_f32 v181, v182, v183\n\t"
            "v_mul_f32 v228, v236, v228\n\t"
            "v_mul_f32 v229, v236, v229\n\t"
            "v_mul_f32 v230, v236, v230\n\t"
            "v_mul_f32 v231, v236, v231\n\t"
            "v_cvt_pk_bf16_f32 v228, v228, v229\n\t"
            "v_cvt_pk_bf16_f32 v229, v230, v231\n\t"
            "v_mul_f32 v184, v237, v184\n\t"
            "v_mul_f32 v185, v237, v185\n\t"
            "v_mul_f32 v186, v237, v186\n\t"
            "v_mul_f32 v187, v237, v187\n\t"
            "v_cvt_pk_bf16_f32 v184, v184, v185\n\t"
            "v_cvt_pk_bf16_f32 v185, v186, v187\n\t"
            "ds_read_b64 v[186:187], v253 offset:352\n\t"
            "ds_write_b64 v245, v[180:181] offset:256\n\t"
            "v_mfma_f32_16x16x32_bf16 v[180:183], v[168:171], v[196:199], 0\n\t"
            "ds_write_b64 v245, v[228:229] offset:128\n\t"
            "v_mfma_f32_16x16x32_bf16 v[180:183], v[156:159], v[200:203], v[180:183]\n\t"
            "ds_write_b64 v245, v[184:185] offset:6144\n\t"
            "v_mfma_f32_16x16x32_bf16 v[228:231], v[172:175], v[188:191], 0\n\t"
            "ds_read_b64 v[184:185], v253 offset:320\n\t"
            "v_mfma_f32_16x16x32_bf16 v[228:231], v[164:167], v[192:195], v[228:231]\n\t"
            "v_mfma_f32_16x16x32_bf16 v[188:191], v[176:179], v[188:191], 0\n\t"
            "v_mfma_f32_16x16x32_bf16 v[188:191], v[160:163], v[192:195], v[188:191]\n\t"
            "s_nop 1\n\t"
            "v_mul_f32 v180, v238, v180\n\t"
            "v_mul_f32 v181, v238, v181\n\t"
            "v_mul_f32 v182, v238, v182\n\t"
            "v_mul_f32 v183, v238, v183\n\t"
            "v_cvt_pk_bf16_f32 v180, v180, v181\n\t"
            "v_cvt_pk_bf16_f32 v181, v182, v183\n\t"
            "v_mul_f32 v228, v237, v228\n\t"
            "v_mul_f32 v229, v237, v229\n\t"
            "v_mul_f32 v230, v237, v230\n\t"
            "v_mul_f32 v231, v237, v231\n\t"
            "v_cvt_pk_bf16_f32 v228, v228, v229\n\t"
            "v_cvt_pk_bf16_f32 v229, v230, v231\n\t"
            "v_mul_f32 v188, v237, v188\n\t"
            "v_mul_f32 v189, v237, v189\n\t"
            "v_mul_f32 v190, v237, v190\n\t"
            "v_mul_f32 v191, v237, v191\n\t"
            "v_cvt_pk_bf16_f32 v188, v188, v189\n\t"
            "v_cvt_pk_bf16_f32 v189, v190, v191\n\t"
            "ds_read_b64 v[182:183], v253 offset:288\n\t"
            "ds_write_b64 v245, v[180:181] offset:12288\n\t"
            "ds_read_b64 v[180:181], v253 offset:256\n\t"
            "ds_write_b64 v245, v[228:229] offset:6272\n\t"
            "v_mfma_f32_16x16x32_bf16 v[228:231], v[172:175], v[196:199], 0\n\t"
            "ds_write_b64 v245, v[188:189] offset:6400\n\t"
            "v_mfma_f32_16x16x32_bf16 v[228:231], v[164:167], v[200:203], v[228:231]\n\t"
            "v_mfma_f32_16x16x32_bf16 v[196:199], v[176:179], v[196:199], 0\n\t"
            "v_mfma_f32_16x16x32_bf16 v[196:199], v[160:163], v[200:203], v[196:199]\n\t"
            "s_waitcnt lgkmcnt(2)\n\t"
            "s_nop 4\n\t"
            "v_mul_f32 v228, v238, v228\n\t"
            "v_mfma_f32_16x16x32_bf16 v[168:171], v[168:171], v[180:183], 0\n\t"
            "v_mfma_f32_16x16x32_bf16 v[172:175], v[172:175], v[180:183], 0\n\t"
            "v_mfma_f32_16x16x32_bf16 v[176:179], v[176:179], v[180:183], 0\n\t"
            "v_mul_f32 v229, v238, v229\n\t"
            "v_mfma_f32_16x16x32_bf16 v[168:171], v[156:159], v[184:187], v[168:171]\n\t"
            "v_mul_f32 v230, v238, v230\n\t"
            "v_mul_f32 v231, v238, v231\n\t"
            "v_mfma_f32_16x16x32_bf16 v[172:175], v[164:167], v[184:187], v[172:175]\n\t"
            "v_cvt_pk_bf16_f32 v228, v228, v229\n\t"
            "v_cvt_pk_bf16_f32 v229, v230, v231\n\t"
            "v_mfma_f32_16x16x32_bf16 v[176:179], v[160:163], v[184:187], v[176:179]\n\t"
            "ds_write_b64 v245, v[228:229] offset:12416\n\t"
            "v_mfma_f32_16x16x32_bf16 v[228:231], v[240:243], v[180:183], 0\n\t"
            "v_mul_f32 v196, v238, v196\n\t"
            "v_mul_f32 v197, v238, v197\n\t"
            "v_mfma_f32_16x16x32_bf16 v[228:231], v[240:243], v[184:187], v[228:231]\n\t"
            "v_mul_f32 v198, v238, v198\n\t"
            "v_mul_f32 v199, v238, v199\n\t"
            "v_cvt_pk_bf16_f32 v196, v196, v197\n\t"
            "v_cvt_pk_bf16_f32 v197, v198, v199\n\t"
            "ds_write_b64 v245, v[196:197] offset:12544\n\t"
            "s_nop 2\n\t"
            "v_rcp_f32 v229, v228\n\t"
            "s_nop 0\n\t"
            "v_mul_f32 v168, v229, v168\n\t"
            "v_mul_f32 v169, v229, v169\n\t"
            "v_mul_f32 v172, v229, v172\n\t"
            "v_mul_f32 v173, v229, v173\n\t"
            "v_mul_f32 v176, v229, v176\n\t"
            "v_mul_f32 v177, v229, v177\n\t"
            "v_mul_f32 v170, v229, v170\n\t"
            "v_mul_f32 v171, v229, v171\n\t"
            "v_cvt_pk_bf16_f32 v168, v168, v169\n\t"
            "v_mul_f32 v174, v229, v174\n\t"
            "v_mul_f32 v175, v229, v175\n\t"
            "v_cvt_pk_bf16_f32 v172, v172, v173\n\t"
            "v_mul_f32 v178, v229, v178\n\t"
            "v_mul_f32 v179, v229, v179\n\t"
            "v_cvt_pk_bf16_f32 v176, v176, v177\n\t"
            "v_xor_b32 v230, s73, v255\n\t"
            "v_cvt_pk_bf16_f32 v169, v170, v171\n\t"
            "v_cvt_pk_bf16_f32 v173, v174, v175\n\t"
            "v_cvt_pk_bf16_f32 v177, v178, v179\n\t"
            "s_mov_b64 exec, s[68:69]\n\t"
            "ds_write_b64 v230, v[168:169]\n\t"
            "ds_write_b64 v230, v[172:173] offset:128\n\t"
            "ds_write_b64 v230, v[176:177] offset:256\n\t"
            "s_mov_b64 exec, -1\n\t"
            "s_waitcnt lgkmcnt(0)\n\t"
            "v_mov_b32 v144, s42\n\t"
            "v_bfrev_b32 v145, -2\n\t"
            "ds_write_b32 v144, v145\n\t"
            "s_waitcnt lgkmcnt(0)\n\t"
            "s_nop 7\n\t"
            : "+{v[232:235]}"(pl), "+{v249}"(aRSw_),
              "+{s44}"(s44), "+{s48}"(s48), "+{s70}"(s70), "+{s72}"(s72), "+{s73}"(s73), "+{s76}"(s76), "+{s77}"(s77)
            : "{v244}"(pE_), "{v245}"(aO_), "{v246}"(aZ_), "{v247}"(aVB_), "{v248}"(pST_), "{v250}"(aPLw_), "{v251}"(aRSr_), "{v252}"(g16_), "{v253}"(aPLr_), "{v254}"(loVL_), "{v255}"(loOL_), "{v[240:243]}"(ones4), "{s47}"((ldsb + X_OFF) ^ (ldsb + X1_OFF)),
              "{s[58:59]}"(0xFFFFull), "{s[60:61]}"(0xFFFF0000ull), "{s[62:63]}"(0xFFFF00000000ull), "{s[64:65]}"(0xFFFF000000000000ull),
              "{s[66:67]}"(0x1FFFFFFFFFFFFull), "{s[68:69]}"(0x0001000100010001ull), "{s[26:27]}"(0x1111111111111111ull), "{s23}"(64.f), "{s57}"(RS_OFF + 768 + (4 + (w >> 1)) * 192 - RS_BASE), "{s45}"(ldsb + V_OFF + w * 4608),
              "{s[28:31]}"(wr), "{s33}"(wave * 36864), "{s46}"(ldsb + PG_OFF), "{s42}"(ldsb + PG_OFF + w * 4), "{s56}"(ldsb + O_OFF), "{s[36:39]}"(tr), "{s41}"(to), "{s40}"(ldsb + X_OFF + 18432), "{s34}"(SH_OFF + bx * 12288 + w * 3072), "{s35}"(ldsb + O_OFF + w * 3072),
              "{s[50:51]}"(0x2222222222222222ull), "{s[52:53]}"(0x4444444444444444ull), "{s[54:55]}"(0x8888888888888888ull)
            : "s24", "s25", "v0", "v1", "v2", "v3", "v4", "v5", "v6", "v7", "v8", "v9", "v10", "v11", "v12", "v13", "v14", "v15", "v16", "v17", "v18", "v19", "v20", "v21", "v22", "v23", "v24", "v25", "v26", "v27", "v28", "v29", "v30", "v31", "v32", "v33", "v34", "v35", "v36", "v37", "v38", "v39", "v40", "v41", "v42", "v43", "v44", "v45", "v46", "v47", "v48", "v49", "v50", "v51", "v52", "v53", "v54", "v55", "v56", "v57", "v58", "v59", "v60", "v61", "v62", "v63", "v64", "v65", "v66", "v67", "v68", "v69", "v70", "v71", "v72", "v73", "v74", "v75", "v76", "v77", "v78", "v79", "v80", "v81", "v82", "v83", "v84", "v85", "v86", "v87", "v88", "v89", "v90", "v91", "v92", "v93", "v94", "v95", "v96", "v97", "v98", "v99", "v100", "v101", "v102", "v103", "v104", "v105", "v106", "v107", "v108", "v109", "v110", "v111", "v112", "v113", "v114", "v115", "v116", "v117", "v118", "v119", "v120", "v121", "v122", "v123", "v124", "v125", "v126", "v127", "v128", "v129", "v130", "v131", "v132", "v133", "v134", "v135", "v136", "v137", "v138", "v139", "v140", "v141", "v142", "v143", "v144", "v145", "v146", "v147", "v148", "v149", "v150", "v151", "v152", "v153", "v154", "v155", "v156", "v157", "v158", "v159", "v160", "v161", "v162", "v163", "v164", "v165", "v166", "v167", "v168", "v169", "v170", "v171", "v172", "v173", "v174", "v175", "v176", "v177", "v178", "v179", "v180", "v181", "v182", "v183", "v184", "v185", "v186", "v187", "v188", "v189", "v190", "v191", "v192", "v193", "v194", "v195", "v196", "v197", "v198", "v199", "v200", "v201", "v202", "v203", "v204", "v205", "v206", "v207", "v208", "v209", "v210", "v211", "v212", "v213", "v214", "v215", "v216", "v217", "v218", "v219", "v220", "v221", "v222", "v223", "v224", "v225", "v226", "v227", "v228", "v229", "v230", "v231", "v236", "v237", "v238", "v239", "s49", "vcc", "scc", "m0", "memory");
        }
        asm volatile("wk_code_end:");

        if (q0 == 40 && wave == 0) {
#pragma unroll 1
            for (int it = 0; it < SPIN_MAX && __hip_atomic_load((gu32*)((char*)wf + CN_OFF), RLX_AGENT) != (unsigned)GRID; ++it) __builtin_amdgcn_s_sleep(8);
            __hip_atomic_store((gu32*)((char*)wf + EP_OFF), __hip_atomic_load((gu32*)((char*)wf + EP_OFF), RLX_AGENT) + 1u, RLX_AGENT);
        }
    } else {
        ROLE_SETUP
        {
            LANE_SETUP
            const bool isV = wave < 6;
            CONV_CONSTS(0)
            const unsigned aCA_ = ldsb + CT_OFF + lane * 16, rsb = ldsb + RS_BASE, pE = fE - X_OFF;
            const unsigned aRSw = (lane < 48 ? rsb : ldsb + RS_OFF + 2304 - 192) + lane * 4, aRSr = rsb + (lane & 48);
            unsigned pr[3];
#pragma unroll
            for (int k = 0; k < 3; ++k) {
                const int c8 = 12 * (w - 2) + 4 * k + g;
                const int ws = c8 / 6, r6 = c8 - 6 * ws;
                pr[k] = l15 * 384 + (r6 >> 1) * 128 + (((2 * ws + (r6 & 1)) ^ sw) << 4);
            }
            unsigned r242 = isV ? pE : ldsb + O_OFF + pE, r243 = isV ? ldsb + V_OFF + w * 9216 + lane * 16 : (ldsb + O_OFF + pE) ^ 64;
            u32x4 r244 = {isV ? ldsb + V_OFF + w * 9216 + 1024 + lane * 8 : pr[0], isV ? aRSr : pr[1], isV ? aRSw : pr[2], (unsigned)lane * 16};
            const int hd = isV ? 2 * w : 2 * w - 3;
            const unsigned vq = ldsb + OL_OFF + l15 * 384 + (((2 * hd + (g >> 1)) ^ sw) << 4) + 8 * (g & 1);
            u32x4 f1 = {16 + l15 < cnt ? vq + 6144 : ldsb + OL_OFF + 22 * 384 + g * 8, pE, ldsb + V_OFF + hd * 4608 + lane * 16, ldsb + V_OFF + hd * 4608 + 1024 + lane * 8};
            u32x4 f2 = {aRSw, aRSr, rsb + l15 * 4, 0u};
            const unsigned lB = ldsb + LB_OFF(l15 & 7) + (g >> 1) * 1536 + (g & 1) * 768 + 96 * w;
            const unsigned long long ob = (unsigned long long)out, xb = (unsigned long long)((const char*)x - XBIAS), wb = (unsigned long long)wf;
            const u32x4 orr = {(unsigned)ob, (unsigned)(ob >> 32) & 0xffffu, (unsigned)(IMG * IMG * CH * 2), 0x00020000u};
            const u32x4 xr = {(unsigned)xb, (unsigned)(xb >> 32) & 0xffffu, (unsigned)(IMG * IMG * CH * 2 + XBIAS), 0x00020000u};
            const u32x4 wr = {(unsigned)wb, (unsigned)(wb >> 32) & 0xffffu, (unsigned)WS_BYTES, 0x00020000u};
            unsigned s45 = OOB, s46 = OOB, s47 = (en0 & 0x7ffffffu) + 96 * w;
            unsigned s48 = ldsb + X1_OFF, s67 = ldsb + X_OFF, s76 = ldsb + WL_OFF + 4, s56 = en0 >> 30;
            unsigned s21 = SH_OFF + bx * 12288, s44 = ldsb + PG_OFF + w * 4, s70 = isV ? wave * 36864 : (wave - 2) * 36864 + 18432;
            unsigned long long xp = 0, sp = 0;
#pragma unroll
            for (int v = 0; v < 4; ++v) {
                const unsigned r = (CROW >> (8 * v)) & 0xFF;
                xp |= (unsigned long long)XPAT(r) << (16 * v);
                sp |= (unsigned long long)SPAT(r) << (16 * v);
            }
#define B_VIN "{v250}"(pXw_), "{v251}"(pSTw_), "{v252}"(aCA_), "{v253}"(aB_), "{v254}"(aA_), "{v255}"(idx_), "{v[238:241]}"(sel4)
#define B_SIN "{s[40:43]}"(orr), "{s[72:75]}"(xr), "{s39}"(0x401fffffu), "{s[28:31]}"(wr), "{s33}"(wave * 36864), "{s78}"(96 * w), \
              "{s27}"(96 * w - 384), "{s[50:51]}"(xp), "{s[52:53]}"(sp), "{s54}"(CLANE), "{s55}"(CROW & 0x0F0F0F0Fu), "{s[34:35]}"(0x0001000100010001ull), \
              "{s[60:61]}"(0x2222222222222222ull), "{s[62:63]}"(0x4444444444444444ull), "{s[64:65]}"(0x8888888888888888ull), \
              "{s77}"(w == 2 ? 768 : 6 * ROWBYTES + 768), "{s25}"(w == 2 ? 3 * ROWBYTES : OOB), "{s32}"(ldsb + X_OFF + 18432), \
              "{s24}"(ldsb + WL_OFF + 12 + 4 * (cnt - 0)), "{s66}"(ldsb + w * 8192), "{s71}"((ldsb + X_OFF) ^ (ldsb + X1_OFF)), \
              "{s23}"(ldsb + WL_OFF), "{s22}"(ldsb + O_OFF), \
              "{s20}"(6 * ROWBYTES + 2304 + 192 * (w & 1)), "{s19}"(2048 + (w & 1) * 1024)
            asm volatile("" :: B_VIN, "{v242}"(r242), "{v243}"(r243), "{v[244:247]}"(r244), "{v[228:231]}"(f1), "{v[232:235]}"(f2), "v"(lB), "v"(vq));
            asm volatile("" :: B_SIN);
            asm volatile("s_waitcnt vmcnt(9)" ::: "memory");
            unsigned lm = *(const unsigned*)(smem + TP_OFF + 4096 + w * 256 + ((l15 & 7) * 4 + g) * 4);
            lm = l15 < 8 ? lm : OOB + 47;
            const int row = lm & 63;
            u32x4 f0 = {XPAT(row) + (unsigned)(O_OFF - X_OFF), SPAT(row) + (unsigned)(STL_OFF - X_OFF - 18816), lB, vq};
            asm volatile(
            "s_setprio 3\n\t"
            "s_waitcnt lgkmcnt(0)\n\t"
            "s_barrier\n\t"
            "s_nop 7\n\t"
            "ds_read_b128 v[168:171], v254\n\t"
            "v_mov_b64 v[176:177], 0\n\t"
            "v_mov_b64 v[178:179], 0\n\t"
            "v_mov_b64 v[180:181], 0\n\t"
            "v_mov_b64 v[182:183], 0\n\t"
            "v_mov_b64 v[184:185], 0\n\t"
            "v_mov_b64 v[186:187], 0\n\t"
            "v_mov_b64 v[188:189], 0\n\t"
            "v_mov_b64 v[190:191], 0\n\t"
            "v_add_u32 v236, s67, v224\n\t"
            "v_xor_b32 v237, 16, v236\n\t"
            "v_add_u32 v248, s67, v225\n\t"
            "ds_read_b64 v[144:145], v226\n\t"
            "ds_read_b64 v[146:147], v226 offset:384\n\t"
            "ds_read_b64 v[148:149], v226 offset:3072\n\t"
            "ds_read_b64 v[150:151], v226 offset:3456\n\t"
            "ds_read_b128 v[172:175], v254 offset:64\n\t"
            "ds_read_b64 v[152:153], v226 offset:8\n\t"
            "ds_read_b64 v[154:155], v226 offset:392\n\t"
            "ds_read_b64 v[156:157], v226 offset:3080\n\t"
            "ds_read_b64 v[158:159], v226 offset:3464\n\t"
            "ds_read_b64 v[160:161], v226 offset:16\n\t"
            "ds_read_b64 v[162:163], v226 offset:400\n\t"
            "ds_read_b64 v[164:165], v226 offset:3088\n\t"
            "ds_read_b64 v[166:167], v226 offset:3472\n\t"
            "s_waitcnt lgkmcnt(9)\n\t"
            "v_smfmac_f32_16x16x64_bf16 v[176:179], v[168:171], v[144:151], v255\n\t"
            "ds_read_b128 v[168:171], v254 offset:128\n\t"
            "ds_read_b64 v[144:145], v226 offset:24\n\t"
            "ds_read_b64 v[146:147], v226 offset:408\n\t"
            "ds_read_b64 v[148:149], v226 offset:3096\n\t"
            "s_waitcnt lgkmcnt(8)\n\t"
            "v_smfmac_f32_16x16x64_bf16 v[180:183], v[172:175], v[152:159], v255\n\t"
            "ds_read_b64 v[150:151], v226 offset:3480\n\t"
            "ds_read_b128 v[172:175], v254 offset:192\n\t"
            "ds_read_b64 v[152:153], v226 offset:32\n\t"
            "v_cvt_pk_bf16_f32 v176, v176, v177\n\t"
            "v_cvt_pk_bf16_f32 v177, v178, v179\n\t"
            "ds_read_b64 v[154:155], v226 offset:416\n\t"
            "ds_read_b64 v[156:157], v226 offset:3104\n\t"
            "ds_read_b64 v[158:159], v226 offset:3488\n\t"
            "v_cvt_pk_bf16_f32 v178, v180, v181\n\t"
            "v_cvt_pk_bf16_f32 v179, v182, v183\n\t"
            "v_mov_b64 v[180:181], 0\n\t"
            "v_mov_b64 v[182:183], 0\n\t"
            "v_mfma_f32_4x4x4_16b_bf16 v[196:199], v[176:177], v[176:177], 0\n\t"
            "v_mfma_f32_4x4x4_16b_bf16 v[192:195], v[238:239], v[176:177], 0\n\t"
            "s_nop 0\n\t"
            "v_mfma_f32_4x4x4_16b_bf16 v[196:199], v[178:179], v[178:179], v[196:199]\n\t"
            "v_mfma_f32_4x4x4_16b_bf16 v[192:195], v[238:239], v[178:179], v[192:195]\n\t"
            "ds_write_b128 v236, v[176:179]\n\t"
            "s_waitcnt lgkmcnt(10)\n\t"
            "v_smfmac_f32_16x16x64_bf16 v[184:187], v[168:171], v[160:167], v255\n\t"
            "s_waitcnt lgkmcnt(5)\n\t"
            "ds_read_b128 v[168:171], v254 offset:256\n\t"
            "v_smfmac_f32_16x16x64_bf16 v[188:191], v[172:175], v[144:151], v255\n\t"
            "ds_read_b64 v[160:161], v226 offset:40\n\t"
            "v_mov_b64 v[176:177], 0\n\t"
            "v_mov_b64 v[178:179], 0\n\t"
            "ds_read_b64 v[162:163], v226 offset:424\n\t"
            "ds_read_b64 v[164:165], v226 offset:3112\n\t"
            "ds_read_b64 v[166:167], v226 offset:3496\n\t"
            "v_cvt_pk_bf16_f32 v184, v184, v185\n\t"
            "v_cvt_pk_bf16_f32 v185, v186, v187\n\t"
            "v_cvt_pk_bf16_f32 v186, v188, v189\n\t"
            "v_cvt_pk_bf16_f32 v187, v190, v191\n\t"
            "v_mov_b64 v[188:189], 0\n\t"
            "v_mov_b64 v[190:191], 0\n\t"
            "v_mfma_f32_4x4x4_16b_bf16 v[196:199], v[184:185], v[184:185], v[196:199]\n\t"
            "v_mfma_f32_4x4x4_16b_bf16 v[192:195], v[238:239], v[184:185], v[192:195]\n\t"
            "s_nop 0\n\t"
            "v_mfma_f32_4x4x4_16b_bf16 v[196:199], v[186:187], v[186:187], v[196:199]\n\t"
            "v_mfma_f32_4x4x4_16b_bf16 v[192:195], v[238:239], v[186:187], v[192:195]\n\t"
            "ds_read_b128 v[172:175], v254 offset:320\n\t"
            "ds_read_b64 v[144:145], v226 offset:48\n\t"
            "s_waitcnt lgkmcnt(6)\n\t"
            "v_smfmac_f32_16x16x64_bf16 v[176:179], v[168:171], v[152:159], v255\n\t"
            "ds_read_b64 v[146:147], v226 offset:432\n\t"
            "ds_read_b64 v[148:149], v226 offset:3120\n\t"
            "ds_read_b64 v[150:151], v226 offset:3504\n\t"
            "ds_read_b128 v[168:171], v254 offset:384\n\t"
            "ds_read_b64 v[152:153], v226 offset:56\n\t"
            "ds_read_b64 v[154:155], v226 offset:440\n\t"
            "ds_read_b64 v[156:157], v226 offset:3128\n\t"
            "ds_read_b64 v[158:159], v226 offset:3512\n\t"
            "v_cvt_pk_bf16_f32 v176, v176, v177\n\t"
            "v_cvt_pk_bf16_f32 v177, v178, v179\n\t"
            "ds_write_b128 v237, v[184:187]\n\t"
            "s_waitcnt lgkmcnt(10)\n\t"
            "v_smfmac_f32_16x16x64_bf16 v[180:183], v[172:175], v[160:167], v255\n\t"
            "ds_read_b128 v[172:175], v254 offset:448\n\t"
            "v_mov_b64 v[184:185], 0\n\t"
            "v_mov_b64 v[186:187], 0\n\t"
            "ds_read_b64 v[160:161], v226 offset:64\n\t"
            "ds_read_b64 v[162:163], v226 offset:448\n\t"
            "ds_read_b64 v[164:165], v226 offset:3136\n\t"
            "s_waitcnt lgkmcnt(9)\n\t"
            "v_smfmac_f32_16x16x64_bf16 v[184:187], v[168:171], v[144:151], v255\n\t"
            "ds_read_b64 v[166:167], v226 offset:3520\n\t"
            "v_cvt_pk_bf16_f32 v178, v180, v181\n\t"
            "v_cvt_pk_bf16_f32 v179, v182, v183\n\t"
            "v_mov_b64 v[180:181], 0\n\t"
            "v_mov_b64 v[182:183], 0\n\t"
            "v_mfma_f32_4x4x4_16b_bf16 v[196:199], v[176:177], v[176:177], v[196:199]\n\t"
            "v_mfma_f32_4x4x4_16b_bf16 v[192:195], v[238:239], v[176:177], v[192:195]\n\t"
            "s_nop 0\n\t"
            "v_mfma_f32_4x4x4_16b_bf16 v[196:199], v[178:179], v[178:179], v[196:199]\n\t"
            "v_mfma_f32_4x4x4_16b_bf16 v[192:195], v[238:239], v[178:179], v[192:195]\n\t"
            "ds_read_b128 v[168:171], v254 offset:512\n\t"
            "v_cvt_pk_bf16_f32 v184, v184, v185\n\t"
            "v_cvt_pk_bf16_f32 v185, v186, v187\n\t"
            "ds_read_b64 v[144:145], v226 offset:72\n\t"
            "s_waitcnt lgkmcnt(6)\n\t"
            "v_smfmac_f32_16x16x64_bf16 v[188:191], v[172:175], v[152:159], v255\n\t"
            "ds_read_b64 v[146:147], v226 offset:456\n\t"
            "ds_read_b64 v[148:149], v226 offset:3144\n\t"
            "ds_read_b64 v[150:151], v226 offset:3528\n\t"
            "ds_read_b128 v[172:175], v254 offset:576\n\t"
            "ds_read_b64 v[152:153], v226 offset:80\n\t"
            "ds_read_b64 v[154:155], v226 offset:464\n\t"
            "ds_read_b64 v[156:157], v226 offset:3152\n\t"
            "ds_read_b64 v[158:159], v226 offset:3536\n\t"
            "v_cvt_pk_bf16_f32 v186, v188, v189\n\t"
            "v_cvt_pk_bf16_f32 v187, v190, v191\n\t"
            "v_mov_b64 v[188:189], 0\n\t"
            "v_mov_b64 v[190:191], 0\n\t"
            "v_mfma_f32_4x4x4_16b_bf16 v[196:199], v[184:185], v[184:185], v[196:199]\n\t"
            "v_mfma_f32_4x4x4_16b_bf16 v[192:195], v[238:239], v[184:185], v[192:195]\n\t"
            "s_nop 0\n\t"
            "v_mfma_f32_4x4x4_16b_bf16 v[196:199], v[186:187], v[186:187], v[196:199]\n\t"
            "v_mfma_f32_4x4x4_16b_bf16 v[192:195], v[238:239], v[186:187], v[192:195]\n\t"
            "ds_write_b128 v236, v[176:179] offset:128\n\t"
            "v_mov_b64 v[176:177], 0\n\t"
            "v_mov_b64 v[178:179], 0\n\t"
            "s_waitcnt lgkmcnt(10)\n\t"
            "s_nop 0\n\t"
            "v_smfmac_f32_16x16x64_bf16 v[176:179], v[168:171], v[160:167], v255\n\t"
            "s_waitcnt lgkmcnt(5)\n\t"
            "ds_read_b128 v[168:171], v254 offset:640\n\t"
            "v_smfmac_f32_16x16x64_bf16 v[180:183], v[172:175], v[144:151], v255\n\t"
            "ds_read_b64 v[160:161], v226 offset:88\n\t"
            "ds_read_b64 v[162:163], v226 offset:472\n\t"
            "ds_read_b64 v[164:165], v226 offset:3160\n\t"
            "ds_read_b64 v[166:167], v226 offset:3544\n\t"
            "ds_read_b128 v[172:175], v254 offset:704\n\t"
            "ds_write_b128 v237, v[184:187] offset:128\n\t"
            "v_mov_b64 v[184:185], 0\n\t"
            "v_mov_b64 v[186:187], 0\n\t"
            "v_cvt_pk_bf16_f32 v176, v176, v177\n\t"
            "v_cvt_pk_bf16_f32 v177, v178, v179\n\t"
            "v_cvt_pk_bf16_f32 v178, v180, v181\n\t"
            "v_cvt_pk_bf16_f32 v179, v182, v183\n\t"
            "s_nop 1\n\t"
            "v_mfma_f32_4x4x4_16b_bf16 v[196:199], v[176:177], v[176:177], v[196:199]\n\t"
            "v_mfma_f32_4x4x4_16b_bf16 v[192:195], v[238:239], v[176:177], v[192:195]\n\t"
            "s_nop 0\n\t"
            "v_mfma_f32_4x4x4_16b_bf16 v[196:199], v[178:179], v[178:179], v[196:199]\n\t"
            "v_mfma_f32_4x4x4_16b_bf16 v[192:195], v[238:239], v[178:179], v[192:195]\n\t"
            "ds_write_b128 v236, v[176:179] offset:256\n\t"
            "s_waitcnt lgkmcnt(7)\n\t"
            "v_smfmac_f32_16x16x64_bf16 v[184:187], v[168:171], v[152:159], v255\n\t"
            "s_waitcnt lgkmcnt(2)\n\t"
            "v_smfmac_f32_16x16x64_bf16 v[188:191], v[172:175], v[160:167], v255\n\t"
            "s_nop 5\n\t"
            "v_cvt_pk_bf16_f32 v184, v184, v185\n\t"
            "v_cvt_pk_bf16_f32 v185, v186, v187\n\t"
            "v_cvt_pk_bf16_f32 v186, v188, v189\n\t"
            "v_cvt_pk_bf16_f32 v187, v190, v191\n\t"
            "s_nop 1\n\t"
            "v_mfma_f32_4x4x4_16b_bf16 v[196:199], v[184:185], v[184:185], v[196:199]\n\t"
            "v_mfma_f32_4x4x4_16b_bf16 v[192:195], v[238:239], v[184:185], v[192:195]\n\t"
            "s_nop 0\n\t"
            "v_mfma_f32_4x4x4_16b_bf16 v[196:199], v[186:187], v[186:187], v[196:199]\n\t"
            "v_mfma_f32_4x4x4_16b_bf16 v[192:195], v[238:239], v[186:187], v[192:195]\n\t"
            "ds_write_b128 v237, v[184:187] offset:256\n\t"
            "s_nop 6\n\t"
            "v_cndmask_b32_e64 v193, v196, v197, s[60:61]\n\t"
            "v_cndmask_b32_e64 v193, v193, v198, s[62:63]\n\t"
            "v_cndmask_b32_e64 v193, v193, v199, s[64:65]\n\t"
            "ds_write_b64 v248, v[192:193] offset:18816\n\t"
            "s_waitcnt lgkmcnt(0)\n\t"
            "s_nop 7\n\t"
            "v_mov_b32 v144, s44\n\t"
            "v_mov_b32 v145, 0x0fffffff\n\t"
            "ds_write_b32 v144, v145\n\t"
            "s_nop 7\n\t"
            "ds_read_b128 v[168:171], v254\n\t"
            "s_waitcnt vmcnt(0)\n\t"
            "ds_read2_b64 v[144:147], v253 offset0:0 offset1:12\n\t"
            "v_mov_b64 v[176:177], 0\n\t"
            "v_mov_b64 v[178:179], 0\n\t"
            "v_mov_b64 v[180:181], 0\n\t"
            "v_mov_b64 v[182:183], 0\n\t"
            "v_mov_b32 v249, s76\n\t"
            "v_mov_b64 v[184:185], 0\n\t"
            "v_mov_b64 v[186:187], 0\n\t"
            "v_mov_b64 v[188:189], 0\n\t"
            "v_mov_b64 v[190:191], 0\n\t"
            "v_add_u32 v236, s67, v250\n\t"
            "v_add_u32 v248, s67, v251\n\t"
            "ds_read2_b64 v[148:151], v253 offset0:218 offset1:230\n\t"
            "ds_read_b128 v[172:175], v254 offset:64\n\t"
            "ds_read2_b64 v[152:155], v253 offset0:1 offset1:13\n\t"
            "ds_read2_b64 v[156:159], v253 offset0:219 offset1:231\n\t"
            "ds_read2_b64 v[160:163], v253 offset0:2 offset1:14\n\t"
            "ds_read2_b64 v[164:167], v253 offset0:220 offset1:232\n\t"
            "ds_read_b32 v249, v249\n\t"
            "s_waitcnt lgkmcnt(6)\n\t"
            "v_smfmac_f32_16x16x64_bf16 v[176:179], v[168:171], v[144:151], v255\n\t"
            "s_waitcnt lgkmcnt(3)\n\t"
            "ds_read_b128 v[168:171], v254 offset:128\n\t"
            "ds_read2_b64 v[144:147], v253 offset0:3 offset1:15\n\t"
            "v_smfmac_f32_16x16x64_bf16 v[180:183], v[172:175], v[152:159], v255\n\t"
            "s_waitcnt lgkmcnt(2)\n\t"
            "ds_read2_b64 v[148:151], v253 offset0:221 offset1:233\n\t"
            "ds_read_b128 v[172:175], v254 offset:192\n\t"
            "v_readfirstlane_b32 s26, v249\n\t"
            "ds_read2_b64 v[152:155], v253 offset0:4 offset1:16\n\t"
            "s_and_b32 s68, s26, 0x7ffffff\n\t"
            "s_add_u32 s68, s68, s78\n\t"
            "s_and_b32 s69, s26, 0x38000000\n\t"
            "s_or_b32 s69, s69, s39\n\t"
            "s_bitcmp1_b32 s26, 27\n\t"
            "s_cselect_b32 s79, 2.0, s68\n\t"
            "ds_read2_b64 v[156:159], v253 offset0:222 offset1:234\n\t"
            "s_and_b32 s57, s56, 3\n\t"
            "s_lshl_b32 s58, s57, 3\n\t"
            "s_lshr_b32 m0, s54, s58\n\t"
            "s_lshl_b32 s58, s57, 4\n\t"
            "s_lshr_b64 s[36:37], s[50:51], s58\n\t"
            "s_and_b32 s57, s36, 0xffff\n\t"
            "s_add_u32 s57, s57, s67\n\t"
            "s_lshr_b64 s[36:37], s[52:53], s58\n\t"
            "s_and_b32 s58, s36, 0xffff\n\t"
            "s_add_u32 s58, s58, s67\n\t"
            "v_cvt_pk_bf16_f32 v176, v176, v177\n\t"
            "s_waitcnt lgkmcnt(5)\n\t"
            "v_smfmac_f32_16x16x64_bf16 v[184:187], v[168:171], v[160:167], v255\n\t"
            "s_and_b32 s59, s50, 0xffff\n\t"
            "s_add_u32 s59, s59, s67\n\t"
            "s_and_b32 s38, s52, 0xffff\n\t"
            "s_add_u32 s38, s38, s67\n\t"
            "ds_read_b128 v[168:171], v254 offset:256\n\t"
            "v_cvt_pk_bf16_f32 v177, v178, v179\n\t"
            "v_cvt_pk_bf16_f32 v178, v180, v181\n\t"
            "s_waitcnt lgkmcnt(3)\n\t"
            "v_smfmac_f32_16x16x64_bf16 v[188:191], v[172:175], v[144:151], v255\n\t"
            "ds_read2_b64 v[160:163], v253 offset0:5 offset1:17\n\t"
            "v_cvt_pk_bf16_f32 v179, v182, v183\n\t"
            "v_writelane_b32 v236, s59, m0\n\t"
            "v_writelane_b32 v236, s57, 63\n\t"
            "v_mfma_f32_4x4x4_16b_bf16 v[196:199], v[176:177], v[176:177], 0\n\t"
            "v_mfma_f32_4x4x4_16b_bf16 v[192:195], v[238:239], v[176:177], 0\n\t"
            "s_nop 0\n\t"
            "v_mfma_f32_4x4x4_16b_bf16 v[196:199], v[178:179], v[178:179], v[196:199]\n\t"
            "v_mfma_f32_4x4x4_16b_bf16 v[192:195], v[238:239], v[178:179], v[192:195]\n\t"
            "ds_read2_b64 v[164:167], v253 offset0:223 offset1:235\n\t"
            "v_mov_b64 v[180:181], 0\n\t"
            "v_mov_b64 v[182:183], 0\n\t"
            "v_cvt_pk_bf16_f32 v184, v184, v185\n\t"
            "v_cvt_pk_bf16_f32 v185, v186, v187\n\t"
            "v_cvt_pk_bf16_f32 v186, v188, v189\n\t"
            "v_cvt_pk_bf16_f32 v187, v190, v191\n\t"
            "v_xor_b32 v237, 16, v236\n\t"
            "v_mov_b64 v[188:189], 0\n\t"
            "v_mfma_f32_4x4x4_16b_bf16 v[196:199], v[184:185], v[184:185], v[196:199]\n\t"
            "v_mfma_f32_4x4x4_16b_bf16 v[192:195], v[238:239], v[184:185], v[192:195]\n\t"
            "s_nop 0\n\t"
            "v_mfma_f32_4x4x4_16b_bf16 v[196:199], v[186:187], v[186:187], v[196:199]\n\t"
            "v_mfma_f32_4x4x4_16b_bf16 v[192:195], v[238:239], v[186:187], v[192:195]\n\t"
            "ds_read_b128 v[172:175], v254 offset:320\n\t"
            "v_mov_b64 v[190:191], 0\n\t"
            "v_writelane_b32 v248, s38, m0\n\t"
            "v_writelane_b32 v248, s58, 63\n\t"
            "ds_read2_b64 v[144:147], v253 offset0:6 offset1:18\n\t"
            "ds_read2_b64 v[148:151], v253 offset0:224 offset1:236\n\t"
            "ds_write_b128 v236, v[176:179]\n\t"
            "v_mov_b64 v[176:177], 0\n\t"
            "v_mov_b64 v[178:179], 0\n\t"
            "s_waitcnt lgkmcnt(6)\n\t"
            "s_nop 0\n\t"
            "v_smfmac_f32_16x16x64_bf16 v[176:179], v[168:171], v[152:159], v255\n\t"
            "ds_read_b128 v[168:171], v254 offset:384\n\t"
            "ds_read2_b64 v[152:155], v253 offset0:7 offset1:19\n\t"
            "ds_read2_b64 v[156:159], v253 offset0:225 offset1:237\n\t"
            "ds_write_b128 v237, v[184:187]\n\t"
            "s_waitcnt lgkmcnt(7)\n\t"
            "v_smfmac_f32_16x16x64_bf16 v[180:183], v[172:175], v[160:167], v255\n\t"
            "ds_read_b128 v[172:175], v254 offset:448\n\t"
            "v_mov_b64 v[184:185], 0\n\t"
            "v_mov_b64 v[186:187], 0\n\t"
            "v_cvt_pk_bf16_f32 v176, v176, v177\n\t"
            "v_cvt_pk_bf16_f32 v177, v178, v179\n\t"
            "ds_read2_b64 v[160:163], v253 offset0:8 offset1:20\n\t"
            "ds_read2_b64 v[164:167], v253 offset0:226 offset1:238\n\t"
            "s_nop 0\n\t"
            "v_cvt_pk_bf16_f32 v178, v180, v181\n\t"
            "v_cvt_pk_bf16_f32 v179, v182, v183\n\t"
            "v_mov_b64 v[180:181], 0\n\t"
            "v_mov_b64 v[182:183], 0\n\t"
            "v_mfma_f32_4x4x4_16b_bf16 v[196:199], v[176:177], v[176:177], v[196:199]\n\t"
            "v_mfma_f32_4x4x4_16b_bf16 v[192:195], v[238:239], v[176:177], v[192:195]\n\t"
            "s_nop 0\n\t"
            "v_mfma_f32_4x4x4_16b_bf16 v[196:199], v[178:179], v[178:179], v[196:199]\n\t"
            "v_mfma_f32_4x4x4_16b_bf16 v[192:195], v[238:239], v[178:179], v[192:195]\n\t"
            "ds_write_b128 v236, v[176:179] offset:128\n\t"
            "s_waitcnt lgkmcnt(7)\n\t"
            "v_smfmac_f32_16x16x64_bf16 v[184:187], v[168:171], v[144:151], v255\n\t"
            "s_waitcnt lgkmcnt(3)\n\t"
            "ds_read_b128 v[168:171], v254 offset:512\n\t"
            "v_smfmac_f32_16x16x64_bf16 v[188:191], v[172:175], v[152:159], v255\n\t"
            "ds_read2_b64 v[144:147], v253 offset0:9 offset1:21\n\t"
            "v_mov_b64 v[176:177], 0\n\t"
            "v_mov_b64 v[178:179], 0\n\t"
            "ds_read2_b64 v[148:151], v253 offset0:227 offset1:239\n\t"
            "ds_read_b128 v[172:175], v254 offset:576\n\t"
            "ds_read2_b64 v[152:155], v253 offset0:10 offset1:22\n\t"
            "v_cvt_pk_bf16_f32 v184, v184, v185\n\t"
            "v_cvt_pk_bf16_f32 v185, v186, v187\n\t"
            "v_cvt_pk_bf16_f32 v186, v188, v189\n\t"
            "v_cvt_pk_bf16_f32 v187, v190, v191\n\t"
            "v_mov_b64 v[188:189], 0\n\t"
            "v_mov_b64 v[190:191], 0\n\t"
            "v_mfma_f32_4x4x4_16b_bf16 v[196:199], v[184:185], v[184:185], v[196:199]\n\t"
            "v_mfma_f32_4x4x4_16b_bf16 v[192:195], v[238:239], v[184:185], v[192:195]\n\t"
            "s_nop 0\n\t"
            "v_mfma_f32_4x4x4_16b_bf16 v[196:199], v[186:187], v[186:187], v[196:199]\n\t"
            "v_mfma_f32_4x4x4_16b_bf16 v[192:195], v[238:239], v[186:187], v[192:195]\n\t"
            "ds_read2_b64 v[156:159], v253 offset0:228 offset1:240\n\t"
            "ds_write_b128 v237, v[184:187] offset:128\n\t"
            "s_waitcnt lgkmcnt(6)\n\t"
            "v_smfmac_f32_16x16x64_bf16 v[176:179], v[168:171], v[160:167], v255\n\t"
            "s_waitcnt lgkmcnt(3)\n\t"
            "ds_read_b128 v[168:171], v254 offset:640\n\t"
            "v_mov_b64 v[184:185], 0\n\t"
            "v_mov_b64 v[186:187], 0\n\t"
            "v_smfmac_f32_16x16x64_bf16 v[180:183], v[172:175], v[144:151], v255\n\t"
            "ds_read2_b64 v[160:163], v253 offset0:11 offset1:23\n\t"
            "ds_read2_b64 v[164:167], v253 offset0:229 offset1:241\n\t"
            "ds_read_b128 v[172:175], v254 offset:704\n\t"
            "v_cvt_pk_bf16_f32 v176, v176, v177\n\t"
            "v_cvt_pk_bf16_f32 v177, v178, v179\n\t"
            "s_nop 2\n\t"
            "v_cvt_pk_bf16_f32 v178, v180, v181\n\t"
            "v_cvt_pk_bf16_f32 v179, v182, v183\n\t"
            "s_nop 1\n\t"
            "v_mfma_f32_4x4x4_16b_bf16 v[196:199], v[176:177], v[176:177], v[196:199]\n\t"
            "v_mfma_f32_4x4x4_16b_bf16 v[192:195], v[238:239], v[176:177], v[192:195]\n\t"
            "s_nop 0\n\t"
            "v_mfma_f32_4x4x4_16b_bf16 v[196:199], v[178:179], v[178:179], v[196:199]\n\t"
            "v_mfma_f32_4x4x4_16b_bf16 v[192:195], v[238:239], v[178:179], v[192:195]\n\t"
            "ds_write_b128 v236, v[176:179] offset:256\n\t"
            "s_waitcnt lgkmcnt(4)\n\t"
            "v_smfmac_f32_16x16x64_bf16 v[184:187], v[168:171], v[152:159], v255\n\t"
            "s_waitcnt lgkmcnt(1)\n\t"
            "v_smfmac_f32_16x16x64_bf16 v[188:191], v[172:175], v[160:167], v255\n\t"
            "s_nop 5\n\t"
            "v_cvt_pk_bf16_f32 v184, v184, v185\n\t"
            "v_cvt_pk_bf16_f32 v185, v186, v187\n\t"
            "v_cvt_pk_bf16_f32 v186, v188, v189\n\t"
            "v_cvt_pk_bf16_f32 v187, v190, v191\n\t"
            "s_nop 1\n\t"
            "v_mfma_f32_4x4x4_16b_bf16 v[196:199], v[184:185], v[184:185], v[196:199]\n\t"
            "v_mfma_f32_4x4x4_16b_bf16 v[192:195], v[238:239], v[184:185], v[192:195]\n\t"
            "s_nop 0\n\t"
            "v_mfma_f32_4x4x4_16b_bf16 v[196:199], v[186:187], v[186:187], v[196:199]\n\t"
            "v_mfma_f32_4x4x4_16b_bf16 v[192:195], v[238:239], v[186:187], v[192:195]\n\t"
            "ds_write_b128 v237, v[184:187] offset:256\n\t"
            "s_nop 6\n\t"
            "v_cndmask_b32_e64 v193, v196, v197, s[60:61]\n\t"
            "v_cndmask_b32_e64 v193, v193, v198, s[62:63]\n\t"
            "v_cndmask_b32_e64 v193, v193, v199, s[64:65]\n\t"
            "ds_write_b64 v248, v[192:193] offset:18816\n\t"
            "s_waitcnt lgkmcnt(0)\n\t"
            "s_nop 7\n\t"
            "v_mov_b32 v144, s44\n\t"
            "v_mov_b32 v145, 0xbfffffff\n\t"
            "ds_write_b32 v144, v145\n\t"
            "s_nop 7\n\t"
            "ds_read_b32 v249, v252 offset:1024\n\t"
            "s_waitcnt lgkmcnt(0)\n\t"
            "v_and_b32 v249, s69, v249\n\t"
            "s_mov_b32 exec_hi, 0x3fffff\n\t"
            "s_add_u32 m0, s66, 0\n\t"
            "s_add_u32 s49, s79, 0\n\t"
            "buffer_load_dwordx4 v249, s[72:75], s49 offen lds\n\t"
            "s_mov_b32 exec_hi, -1\n\t"
            "s_mov_b32 exec_hi, 0x3fffff\n\t"
            "s_add_u32 m0, s66, 872\n\t"
            "s_add_u32 s49, s68, 196224\n\t"
            "buffer_load_dwordx4 v249, s[72:75], s49 offen lds\n\t"
            "s_mov_b32 exec_hi, -1\n\t"
            "s_mov_b32 exec_hi, 0x3fffff\n\t"
            "s_add_u32 m0, s66, 1744\n\t"
            "s_add_u32 s49, s68, 392448\n\t"
            "buffer_load_dwordx4 v249, s[72:75], s49 offen lds\n\t"
            "s_mov_b32 exec_hi, -1\n\t"
            "s_mov_b32 exec_hi, 0x3fffff\n\t"
            "s_add_u32 m0, s66, 2616\n\t"
            "s_add_u32 s49, s68, 588672\n\t"
            "buffer_load_dwordx4 v249, s[72:75], s49 offen lds\n\t"
            "s_mov_b32 exec_hi, -1\n\t"
            "s_mov_b32 exec_hi, 0x3fffff\n\t"
            "s_add_u32 m0, s66, 3488\n\t"
            "s_add_u32 s49, s68, 784896\n\t"
            "buffer_load_dwordx4 v249, s[72:75], s49 offen lds\n\t"
            "s_mov_b32 exec_hi, -1\n\t"
            "s_mov_b32 exec_hi, 0x3fffff\n\t"
            "s_add_u32 m0, s66, 4360\n\t"
            "s_add_u32 s49, s68, 981120\n\t"
            "buffer_load_dwordx4 v249, s[72:75], s49 offen lds\n\t"
            "s_mov_b32 exec_hi, -1\n\t"
            "s_mov_b32 exec_hi, 0x3fffff\n\t"
            "s_add_u32 m0, s66, 5232\n\t"
            "s_add_u32 s49, s68, 1177344\n\t"
            "buffer_load_dwordx4 v249, s[72:75], s49 offen lds\n\t"
            "s_mov_b32 exec_hi, -1\n\t"
            "s_mov_b32 exec_hi, 0x3fffff\n\t"
            "s_add_u32 m0, s66, 6104\n\t"
            "s_add_u32 s49, s68, 1373568\n\t"
            "buffer_load_dwordx4 v249, s[72:75], s49 offen lds\n\t"
            "s_mov_b32 exec_hi, -1\n\t"
            "s_mov_b32 exec_hi, 0x3fffff\n\t"
            "s_add_u32 m0, s66, 6976\n\t"
            "s_add_u32 s49, s68, 1569792\n\t"
            "buffer_load_dwordx4 v249, s[72:75], s49 offen lds\n\t"
            "s_mov_b32 exec_hi, -1\n\t"
            "s_add_u32 s45, s46, s27\n\t"
            "s_mov_b32 s46, s47\n\t"
            "s_mov_b32 s47, s68\n\t"
            "s_xor_b32 s48, s48, s71\n\t"
            "s_xor_b32 s67, s67, s71\n\t"
            "s_add_u32 s76, s76, 4\n\t"
            "s_lshl_b32 s56, s56, 2\n\t"
            "s_lshr_b32 s49, s26, 30\n\t"
            "s_or_b32 s56, s56, s49\n\t"
            "s_nop 7\n\t"
            "v_mov_b32 v144, s23\n\t"
            ".Lpg%=:\n\t"
            "ds_read_b128 v[148:151], v144 offset:96\n\t"
            "s_waitcnt lgkmcnt(0)\n\t"
            "v_and_b32 v148, v148, v149\n\t"
            "v_and_b32 v150, v150, v151\n\t"
            "v_and_b32 v148, v148, v150\n\t"
            "v_cmp_le_i32 vcc, 0, v148\n\t"
            "s_nop 4\n\t"
            "s_cbranch_vccnz .Lpg%=\n\t"
            "s_setprio 1\n\t"
            "s_nop 7\n\t"
            "ds_read_b128 v[168:171], v254\n\t"
            "s_waitcnt vmcnt(0)\n\t"
            "ds_read2_b64 v[144:147], v253 offset0:0 offset1:12\n\t"
            "v_mov_b64 v[176:177], 0\n\t"
            "v_mov_b64 v[178:179], 0\n\t"
            "v_mov_b64 v[180:181], 0\n\t"
            "v_mov_b64 v[182:183], 0\n\t"
            "v_mov_b32 v249, s76\n\t"
            "v_mov_b64 v[184:185], 0\n\t"
            "v_mov_b64 v[186:187], 0\n\t"
            "v_mov_b64 v[188:189], 0\n\t"
            "v_mov_b64 v[190:191], 0\n\t"
            "v_add_u32 v236, s67, v250\n\t"
            "v_add_u32 v248, s67, v251\n\t"
            "ds_read2_b64 v[148:151], v253 offset0:218 offset1:230\n\t"
            "ds_read_b128 v[172:175], v254 offset:64\n\t"
            "ds_read2_b64 v[152:155], v253 offset0:1 offset1:13\n\t"
            "ds_read2_b64 v[156:159], v253 offset0:219 offset1:231\n\t"
            "ds_read2_b64 v[160:163], v253 offset0:2 offset1:14\n\t"
            "ds_read2_b64 v[164:167], v253 offset0:220 offset1:232\n\t"
            "ds_read_b32 v249, v249\n\t"
            "s_waitcnt lgkmcnt(6)\n\t"
            "v_smfmac_f32_16x16x64_bf16 v[176:179], v[168:171], v[144:151], v255\n\t"
            "s_waitcnt lgkmcnt(3)\n\t"
            "ds_read_b128 v[168:171], v254 offset:128\n\t"
            "ds_read2_b64 v[144:147], v253 offset0:3 offset1:15\n\t"
            "v_smfmac_f32_16x16x64_bf16 v[180:183], v[172:175], v[152:159], v255\n\t"
            "s_waitcnt lgkmcnt(2)\n\t"
            "ds_read2_b64 v[148:151], v253 offset0:221 offset1:233\n\t"
            "ds_read_b128 v[172:175], v254 offset:192\n\t"
            "v_readfirstlane_b32 s26, v249\n\t"
            "ds_read2_b64 v[152:155], v253 offset0:4 offset1:16\n\t"
            "s_and_b32 s68, s26, 0x7ffffff\n\t"
            "s_add_u32 s68, s68, s78\n\t"
            "s_and_b32 s69, s26, 0x38000000\n\t"
            "s_or_b32 s69, s69, s39\n\t"
            "s_bitcmp1_b32 s26, 27\n\t"
            "s_cselect_b32 s79, 2.0, s68\n\t"
            "ds_read2_b64 v[156:159], v253 offset0:222 offset1:234\n\t"
            "s_and_b32 s57, s56, 3\n\t"
            "s_lshl_b32 s58, s57, 3\n\t"
            "s_lshr_b32 m0, s54, s58\n\t"
            "s_lshl_b32 s58, s57, 4\n\t"
            "s_lshr_b64 s[36:37], s[50:51], s58\n\t"
            "s_and_b32 s57, s36, 0xffff\n\t"
            "s_add_u32 s57, s57, s67\n\t"
            "s_lshr_b64 s[36:37], s[52:53], s58\n\t"
            "s_and_b32 s58, s36, 0xffff\n\t"
            "s_add_u32 s58, s58, s67\n\t"
            "v_cvt_pk_bf16_f32 v176, v176, v177\n\t"
            "s_waitcnt lgkmcnt(5)\n\t"
            "v_smfmac_f32_16x16x64_bf16 v[184:187], v[168:171], v[160:167], v255\n\t"
            "s_and_b32 s59, s50, 0xffff\n\t"
            "s_add_u32 s59, s59, s67\n\t"
            "s_and_b32 s38, s52, 0xffff\n\t"
            "s_add_u32 s38, s38, s67\n\t"
            "ds_read_b128 v[168:171], v254 offset:256\n\t"
            "v_cvt_pk_bf16_f32 v177, v178, v179\n\t"
            "v_cvt_pk_bf16_f32 v178, v180, v181\n\t"
            "s_waitcnt lgkmcnt(3)\n\t"
            "v_smfmac_f32_16x16x64_bf16 v[188:191], v[172:175], v[144:151], v255\n\t"
            "ds_read2_b64 v[160:163], v253 offset0:5 offset1:17\n\t"
            "v_cvt_pk_bf16_f32 v179, v182, v183\n\t"
            "v_writelane_b32 v236, s59, m0\n\t"
            "v_writelane_b32 v236, s57, 63\n\t"
            "v_mfma_f32_4x4x4_16b_bf16 v[196:199], v[176:177], v[176:177], 0\n\t"
            "v_mfma_f32_4x4x4_16b_bf16 v[192:195], v[238:239], v[176:177], 0\n\t"
            "s_nop 0\n\t"
            "v_mfma_f32_4x4x4_16b_bf16 v[196:199], v[178:179], v[178:179], v[196:199]\n\t"
            "v_mfma_f32_4x4x4_16b_bf16 v[192:195], v[238:239], v[178:179], v[192:195]\n\t"
            "ds_read2_b64 v[164:167], v253 offset0:223 offset1:235\n\t"
            "v_mov_b64 v[180:181], 0\n\t"
            "v_mov_b64 v[182:183], 0\n\t"
            "v_cvt_pk_bf16_f32 v184, v184, v185\n\t"
            "v_cvt_pk_bf16_f32 v185, v186, v187\n\t"
            "v_cvt_pk_bf16_f32 v186, v188, v189\n\t"
            "v_cvt_pk_bf16_f32 v187, v190, v191\n\t"
            "v_xor_b32 v237, 16, v236\n\t"
            "v_mov_b64 v[188:189], 0\n\t"
            "v_mov_b64 v[190:191], 0\n\t"
            "v_writelane_b32 v248, s38, m0\n\t"
            "v_writelane_b32 v248, s58, 63\n\t"
            "ds_read_b128 v[172:175], v254 offset:320\n\t"
            "ds_read2_b64 v[144:147], v253 offset0:6 offset1:18\n\t"
            "ds_read2_b64 v[148:151], v253 offset0:224 offset1:236\n\t"
            "ds_write_b128 v236, v[176:179]\n\t"
            "v_mov_b64 v[176:177], 0\n\t"
            "v_mov_b64 v[178:179], 0\n\t"
            "s_waitcnt lgkmcnt(6)\n\t"
            "s_nop 0\n\t"
            "v_smfmac_f32_16x16x64_bf16 v[176:179], v[168:171], v[152:159], v255\n\t"
            "ds_read_b128 v[168:171], v254 offset:384\n\t"
            "ds_read2_b64 v[152:155], v253 offset0:7 offset1:19\n\t"
            "ds_read2_b64 v[156:159], v253 offset0:225 offset1:237\n\t"
            "ds_write_b128 v237, v[184:187]\n\t"
            "s_waitcnt lgkmcnt(7)\n\t"
            "v_smfmac_f32_16x16x64_bf16 v[180:183], v[172:175], v[160:167], v255\n\t"
            "ds_read_b128 v[172:175], v254 offset:448\n\t"
            "ds_read2_b64 v[160:163], v253 offset0:8 offset1:20\n\t"
            "v_mfma_f32_4x4x4_16b_bf16 v[196:199], v[184:185], v[184:185], v[196:199]\n\t"
            "v_mfma_f32_4x4x4_16b_bf16 v[192:195], v[238:239], v[184:185], v[192:195]\n\t"
            "s_nop 0\n\t"
            "v_mfma_f32_4x4x4_16b_bf16 v[196:199], v[186:187], v[186:187], v[196:199]\n\t"
            "v_mfma_f32_4x4x4_16b_bf16 v[192:195], v[238:239], v[186:187], v[192:195]\n\t"
            "ds_read2_b64 v[164:167], v253 offset0:226 offset1:238\n\t"
            "v_mov_b64 v[184:185], 0\n\t"
            "v_mov_b64 v[186:187], 0\n\t"
            "v_cvt_pk_bf16_f32 v176, v176, v177\n\t"
            "v_cvt_pk_bf16_f32 v177, v178, v179\n\t"
            "s_waitcnt lgkmcnt(6)\n\t"
            "v_smfmac_f32_16x16x64_bf16 v[184:187], v[168:171], v[144:151], v255\n\t"
            "ds_read_b128 v[168:171], v254 offset:512\n\t"
            "v_cvt_pk_bf16_f32 v178, v180, v181\n\t"
            "v_cvt_pk_bf16_f32 v179, v182, v183\n\t"
            "v_mov_b64 v[180:181], 0\n\t"
            "v_mov_b64 v[182:183], 0\n\t"
            "ds_read2_b64 v[144:147], v253 offset0:9 offset1:21\n\t"
            "ds_read2_b64 v[148:151], v253 offset0:227 offset1:239\n\t"
            "ds_write_b128 v236, v[176:179] offset:128\n\t"
            "s_waitcnt lgkmcnt(6)\n\t"
            "v_smfmac_f32_16x16x64_bf16 v[188:191], v[172:175], v[152:159], v255\n\t"
            "ds_read_b128 v[172:175], v254 offset:576\n\t"
            "v_cvt_pk_bf16_f32 v184, v184, v185\n\t"
            "v_cvt_pk_bf16_f32 v185, v186, v187\n\t"
            "v_mfma_f32_4x4x4_16b_bf16 v[196:199], v[176:177], v[176:177], v[196:199]\n\t"
            "v_mfma_f32_4x4x4_16b_bf16 v[192:195], v[238:239], v[176:177], v[192:195]\n\t"
            "s_nop 0\n\t"
            "v_mfma_f32_4x4x4_16b_bf16 v[196:199], v[178:179], v[178:179], v[196:199]\n\t"
            "v_mfma_f32_4x4x4_16b_bf16 v[192:195], v[238:239], v[178:179], v[192:195]\n\t"
            "ds_read2_b64 v[152:155], v253 offset0:10 offset1:22\n\t"
            "v_mov_b64 v[176:177], 0\n\t"
            "v_mov_b64 v[178:179], 0\n\t"
            "v_cvt_pk_bf16_f32 v186, v188, v189\n\t"
            "v_cvt_pk_bf16_f32 v187, v190, v191\n\t"
            "s_waitcnt lgkmcnt(5)\n\t"
            "v_smfmac_f32_16x16x64_bf16 v[176:179], v[168:171], v[160:167], v255\n\t"
            "ds_read2_b64 v[156:159], v253 offset0:228 offset1:240\n\t"
            "v_mov_b64 v[188:189], 0\n\t"
            "v_mov_b64 v[190:191], 0\n\t"
            "ds_read_b128 v[168:171], v254 offset:640\n\t"
            "ds_read2_b64 v[160:163], v253 offset0:11 offset1:23\n\t"
            "ds_read2_b64 v[164:167], v253 offset0:229 offset1:241\n\t"
            "ds_write_b128 v237, v[184:187] offset:128\n\t"
            "s_waitcnt lgkmcnt(6)\n\t"
            "v_smfmac_f32_16x16x64_bf16 v[180:183], v[172:175], v[144:151], v255\n\t"
            "ds_read_b128 v[172:175], v254 offset:704\n\t"
            "v_cvt_pk_bf16_f32 v176, v176, v177\n\t"
            "v_cvt_pk_bf16_f32 v177, v178, v179\n\t"
            "v_mfma_f32_4x4x4_16b_bf16 v[196:199], v[184:185], v[184:185], v[196:199]\n\t"
            "v_mfma_f32_4x4x4_16b_bf16 v[192:195], v[238:239], v[184:185], v[192:195]\n\t"
            "s_nop 0\n\t"
            "v_mfma_f32_4x4x4_16b_bf16 v[196:199], v[186:187], v[186:187], v[196:199]\n\t"
            "v_mfma_f32_4x4x4_16b_bf16 v[192:195], v[238:239], v[186:187], v[192:195]\n\t"
            "v_mov_b64 v[184:185], 0\n\t"
            "v_mov_b64 v[186:187], 0\n\t"
            "v_cvt_pk_bf16_f32 v178, v180, v181\n\t"
            "v_cvt_pk_bf16_f32 v179, v182, v183\n\t"
            "ds_write_b128 v236, v[176:179] offset:256\n\t"
            "s_waitcnt lgkmcnt(5)\n\t"
            "v_smfmac_f32_16x16x64_bf16 v[184:187], v[168:171], v[152:159], v255\n\t"
            "s_waitcnt lgkmcnt(1)\n\t"
            "v_smfmac_f32_16x16x64_bf16 v[188:191], v[172:175], v[160:167], v255\n\t"
            "v_mfma_f32_4x4x4_16b_bf16 v[196:199], v[176:177], v[176:177], v[196:199]\n\t"
            "v_mfma_f32_4x4x4_16b_bf16 v[192:195], v[238:239], v[176:177], v[192:195]\n\t"
            "s_nop 0\n\t"
            "v_mfma_f32_4x4x4_16b_bf16 v[196:199], v[178:179], v[178:179], v[196:199]\n\t"
            "v_mfma_f32_4x4x4_16b_bf16 v[192:195], v[238:239], v[178:179], v[192:195]\n\t"
            "s_nop 0\n\t"
            "v_cvt_pk_bf16_f32 v184, v184, v185\n\t"
            "v_cvt_pk_bf16_f32 v185, v186, v187\n\t"
            "v_cvt_pk_bf16_f32 v186, v188, v189\n\t"
            "v_cvt_pk_bf16_f32 v187, v190, v191\n\t"
            "s_nop 1\n\t"
            "v_mfma_f32_4x4x4_16b_bf16 v[196:199], v[184:185], v[184:185], v[196:199]\n\t"
            "v_mfma_f32_4x4x4_16b_bf16 v[192:195], v[238:239], v[184:185], v[192:195]\n\t"
            "s_nop 0\n\t"
            "v_mfma_f32_4x4x4_16b_bf16 v[196:199], v[186:187], v[186:187], v[196:199]\n\t"
            "v_mfma_f32_4x4x4_16b_bf16 v[192:195], v[238:239], v[186:187], v[192:195]\n\t"
            "ds_write_b128 v237, v[184:187] offset:256\n\t"
            "s_nop 6\n\t"
            "v_cndmask_b32_e64 v193, v196, v197, s[60:61]\n\t"
            "v_cndmask_b32_e64 v193, v193, v198, s[62:63]\n\t"
            "v_cndmask_b32_e64 v193, v193, v199, s[64:65]\n\t"
            "ds_write_b64 v248, v[192:193] offset:18816\n\t"
            "s_waitcnt lgkmcnt(0)\n\t"
            "s_nop 7\n\t"
            "s_setprio 1\n\t"
            "v_mov_b32 v144, s23\n\t"
            ".Lgo%=:\n\t"
            "ds_read_b32 v145, v144 offset:124\n\t"
            "s_waitcnt lgkmcnt(0)\n\t"
            "v_cmp_ne_u32 vcc, -1, v145\n\t"
            "s_nop 4\n\t"
            "s_cbranch_vccnz .Lgo%=\n\t"
            "s_nop 7\n\t"
            "ds_read_b32 v249, v252 offset:1024\n\t"
            "s_add_u32 s49, s70, 0\n\t"
            "buffer_load_dwordx4 v[0:3], v247, s[28:31], s49 offen sc1\n\t"
            "s_add_u32 s49, s70, 1024\n\t"
            "buffer_load_dwordx4 v[4:7], v247, s[28:31], s49 offen sc1\n\t"
            "s_add_u32 s49, s70, 2048\n\t"
            "buffer_load_dwordx4 v[8:11], v247, s[28:31], s49 offen sc1\n\t"
            "s_add_u32 s49, s70, 3072\n\t"
            "buffer_load_dwordx4 v[12:15], v247, s[28:31], s49 offen sc1\n\t"
            "s_add_u32 s49, s70, 4096\n\t"
            "buffer_load_dwordx4 v[16:19], v247, s[28:31], s49 offen sc1\n\t"
            "s_add_u32 s49, s70, 5120\n\t"
            "buffer_load_dwordx4 v[20:23], v247, s[28:31], s49 offen sc1\n\t"
            "s_add_u32 s49, s70, 6144\n\t"
            "buffer_load_dwordx4 v[24:27], v247, s[28:31], s49 offen sc1\n\t"
            "s_add_u32 s49, s70, 7168\n\t"
            "buffer_load_dwordx4 v[28:31], v247, s[28:31], s49 offen sc1\n\t"
            "s_add_u32 s49, s70, 8192\n\t"
            "buffer_load_dwordx4 v[32:35], v247, s[28:31], s49 offen sc1\n\t"
            "s_add_u32 s49, s70, 9216\n\t"
            "buffer_load_dwordx4 v[36:39], v247, s[28:31], s49 offen sc1\n\t"
            "s_add_u32 s49, s70, 10240\n\t"
            "buffer_load_dwordx4 v[40:43], v247, s[28:31], s49 offen sc1\n\t"
            "s_add_u32 s49, s70, 11264\n\t"
            "buffer_load_dwordx4 v[44:47], v247, s[28:31], s49 offen sc1\n\t"
            "s_add_u32 s49, s70, 12288\n\t"
            "buffer_load_dwordx4 v[48:51], v247, s[28:31], s49 offen sc1\n\t"
            "s_add_u32 s49, s70, 13312\n\t"
            "buffer_load_dwordx4 v[52:55], v247, s[28:31], s49 offen sc1\n\t"
            "s_add_u32 s49, s70, 14336\n\t"
            "buffer_load_dwordx4 v[56:59], v247, s[28:31], s49 offen sc1\n\t"
            "s_add_u32 s49, s70, 15360\n\t"
            "buffer_load_dwordx4 v[60:63], v247, s[28:31], s49 offen sc1\n\t"
            "s_add_u32 s49, s70, 16384\n\t"
            "buffer_load_dwordx4 v[64:67], v247, s[28:31], s49 offen sc1\n\t"
            "s_add_u32 s49, s70, 17408\n\t"
            "buffer_load_dwordx4 v[68:71], v247, s[28:31], s49 offen sc1\n\t"
            "s_waitcnt lgkmcnt(0)\n\t"
            "v_and_b32 v249, s69, v249\n\t"
            "s_mov_b32 exec_hi, 0x3fffff\n\t"
            "s_add_u32 m0, s66, 0\n\t"
            "s_add_u32 s49, s79, 0\n\t"
            "buffer_load_dwordx4 v249, s[72:75], s49 offen lds\n\t"
            "s_mov_b32 exec_hi, -1\n\t"
            "s_mov_b32 exec_hi, 0x3fffff\n\t"
            "s_add_u32 m0, s66, 872\n\t"
            "s_add_u32 s49, s68, 196224\n\t"
            "buffer_load_dwordx4 v249, s[72:75], s49 offen lds\n\t"
            "s_mov_b32 exec_hi, -1\n\t"
            "s_mov_b32 exec_hi, 0x3fffff\n\t"
            "s_add_u32 m0, s66, 1744\n\t"
            "s_add_u32 s49, s68, 392448\n\t"
            "buffer_load_dwordx4 v249, s[72:75], s49 offen lds\n\t"
            "s_mov_b32 exec_hi, -1\n\t"
            "s_mov_b32 exec_hi, 0x3fffff\n\t"
            "s_add_u32 m0, s66, 2616\n\t"
            "s_add_u32 s49, s68, 588672\n\t"
            "buffer_load_dwordx4 v249, s[72:75], s49 offen lds\n\t"
            "s_mov_b32 exec_hi, -1\n\t"
            "s_mov_b32 exec_hi, 0x3fffff\n\t"
            "s_add_u32 m0, s66, 3488\n\t"
            "s_add_u32 s49, s68, 784896\n\t"
            "buffer_load_dwordx4 v249, s[72:75], s49 offen lds\n\t"
            "s_mov_b32 exec_hi, -1\n\t"
            "s_mov_b32 exec_hi, 0x3fffff\n\t"
            "s_add_u32 m0, s66, 4360\n\t"
            "s_add_u32 s49, s68, 981120\n\t"
            "buffer_load_dwordx4 v249, s[72:75], s49 offen lds\n\t"
            "s_mov_b32 exec_hi, -1\n\t"
            "s_mov_b32 exec_hi, 0x3fffff\n\t"
            "s_add_u32 m0, s66, 5232\n\t"
            "s_add_u32 s49, s68, 1177344\n\t"
            "buffer_load_dwordx4 v249, s[72:75], s49 offen lds\n\t"
            "s_mov_b32 exec_hi, -1\n\t"
            "s_mov_b32 exec_hi, 0x3fffff\n\t"
            "s_add_u32 m0, s66, 6104\n\t"
            "s_add_u32 s49, s68, 1373568\n\t"
            "buffer_load_dwordx4 v249, s[72:75], s49 offen lds\n\t"
            "s_mov_b32 exec_hi, -1\n\t"
            "s_mov_b32 exec_hi, 0x3fffff\n\t"
            "s_add_u32 m0, s66, 6976\n\t"
            "s_add_u32 s49, s68, 1569792\n\t"
            "buffer_load_dwordx4 v249, s[72:75], s49 offen lds\n\t"
            "s_mov_b32 exec_hi, -1\n\t"
            "s_nop 7\n\t"
            "s_setprio 3\n\t"
            "s_nop 7\n\t"
            "v_add_u32 v196, s22, v229\n\t"
            "v_xor_b32 v197, 64, v196\n\t"
            "v_add_u32 v198, s22, v247\n\t"
            "ds_read_b128 v[144:147], v196\n\t"
            "ds_read_b128 v[148:151], v197\n\t"
            "ds_read_b128 v[152:155], v196 offset:128\n\t"
            "ds_read_b128 v[156:159], v197 offset:128\n\t"
            "ds_read_b128 v[160:163], v196 offset:256\n\t"
            "ds_read_b128 v[164:167], v197 offset:256\n\t"
            "ds_read_b128 v[216:219], v198 offset:12288\n\t"
            "ds_read_b128 v[220:223], v198 offset:13312\n\t"
            "v_add_u32 v198, s48, v247\n\t"
            "ds_read_b128 v[168:171], v196 offset:6144\n\t"
            "ds_read_b128 v[172:175], v197 offset:6144\n\t"
            "ds_read_b128 v[176:179], v196 offset:6272\n\t"
            "ds_read_b128 v[180:183], v197 offset:6272\n\t"
            "ds_read_b128 v[184:187], v196 offset:6400\n\t"
            "v_add_u32 v196, s48, v229\n\t"
            "ds_read_b128 v[188:191], v197 offset:6400\n\t"
            "s_waitcnt vmcnt(9)\n\t"
            "s_waitcnt lgkmcnt(13)\n\t"
            "v_mfma_f32_16x16x32_bf16 v[200:203], v[0:3], v[144:147], 0\n\t"
            "s_waitcnt lgkmcnt(12)\n\t"
            "v_xor_b32 v197, 64, v196\n\t"
            "v_mfma_f32_16x16x32_bf16 v[200:203], v[4:7], v[148:151], v[200:203]\n\t"
            "s_add_u32 s49, s33, 18432\n\t"
            "buffer_load_dwordx4 v[72:75], v247, s[28:31], s49 offen sc1\n\t"
            "s_waitcnt lgkmcnt(11)\n\t"
            "v_mfma_f32_16x16x32_bf16 v[200:203], v[8:11], v[152:155], v[200:203]\n\t"
            "s_waitcnt lgkmcnt(7)\n\t"
            "v_add_f32 v216, v216, v218\n\t"
            "s_waitcnt lgkmcnt(6)\n\t"
            "v_add_f32 v220, v220, v222\n\t"
            "v_mfma_f32_16x16x32_bf16 v[200:203], v[12:15], v[156:159], v[200:203]\n\t"
            "v_add_f32 v216, v216, v220\n\t"
            "v_add_f32 v217, v217, v219\n\t"
            "v_mfma_f32_16x16x32_bf16 v[200:203], v[16:19], v[160:163], v[200:203]\n\t"
            "v_add_f32 v221, v221, v223\n\t"
            "v_mul_f32 v216, 0x3baaaaab, v216\n\t"
            "v_mfma_f32_16x16x32_bf16 v[200:203], v[20:23], v[164:167], v[200:203]\n\t"
            "v_add_f32 v217, v217, v221\n\t"
            "v_mul_f32_e64 v216, v216, -v216\n\t"
            "v_mfma_f32_16x16x32_bf16 v[204:207], v[24:27], v[144:147], 0\n\t"
            "v_fmac_f32 v216, 0x3baaaaab, v217\n\t"
            "v_max_f32 v216, 0, v216\n\t"
            "v_mfma_f32_16x16x32_bf16 v[204:207], v[28:31], v[148:151], v[204:207]\n\t"
            "v_add_f32 v216, 0x3727c5ac, v216\n\t"
            "v_rsq_f32 v216, v216\n\t"
            "v_mfma_f32_16x16x32_bf16 v[204:207], v[32:35], v[152:155], v[204:207]\n\t"
            "ds_write_b32 v232, v216\n\t"
            "v_mfma_f32_16x16x32_bf16 v[204:207], v[36:39], v[156:159], v[204:207]\n\t"
            "ds_read_b32 v192, v234\n\t"
            "v_mfma_f32_16x16x32_bf16 v[204:207], v[40:43], v[160:163], v[204:207]\n\t"
            "ds_read_b32 v193, v234 offset:64\n\t"
            "v_mfma_f32_16x16x32_bf16 v[204:207], v[44:47], v[164:167], v[204:207]\n\t"
            "s_add_u32 s49, s33, 19456\n\t"
            "buffer_load_dwordx4 v[76:79], v247, s[28:31], s49 offen sc1\n\t"
            "v_mfma_f32_16x16x32_bf16 v[208:211], v[48:51], v[144:147], 0\n\t"
            "ds_read_b128 v[144:147], v196\n\t"
            "v_mfma_f32_16x16x32_bf16 v[208:211], v[52:55], v[148:151], v[208:211]\n\t"
            "ds_read_b128 v[148:151], v197\n\t"
            "v_mfma_f32_16x16x32_bf16 v[208:211], v[56:59], v[152:155], v[208:211]\n\t"
            "ds_read_b128 v[152:155], v196 offset:128\n\t"
            "v_mfma_f32_16x16x32_bf16 v[208:211], v[60:63], v[156:159], v[208:211]\n\t"
            "s_waitcnt lgkmcnt(4)\n\t"
            "ds_read_b128 v[156:159], v197 offset:128\n\t"
            "v_mul_f32 v200, v192, v200\n\t"
            "v_mfma_f32_16x16x32_bf16 v[208:211], v[64:67], v[160:163], v[208:211]\n\t"
            "ds_read_b128 v[160:163], v196 offset:256\n\t"
            "v_mul_f32 v201, v192, v201\n\t"
            "v_mfma_f32_16x16x32_bf16 v[208:211], v[68:71], v[164:167], v[208:211]\n\t"
            "ds_read_b128 v[164:167], v197 offset:256\n\t"
            "v_mul_f32 v204, v192, v204\n\t"
            "v_mfma_f32_16x16x32_bf16 v[212:215], v[0:3], v[168:171], 0\n\t"
            "v_mul_f32 v205, v192, v205\n\t"
            "v_mul_f32 v202, v192, v202\n\t"
            "v_mfma_f32_16x16x32_bf16 v[212:215], v[4:7], v[172:175], v[212:215]\n\t"
            "v_mul_f32 v203, v192, v203\n\t"
            "v_cvt_pk_bf16_f32 v200, v200, v201\n\t"
            "v_mfma_f32_16x16x32_bf16 v[212:215], v[8:11], v[176:179], v[212:215]\n\t"
            "v_mul_f32 v206, v192, v206\n\t"
            "v_mul_f32 v207, v192, v207\n\t"
            "v_mfma_f32_16x16x32_bf16 v[212:215], v[12:15], v[180:183], v[212:215]\n\t"
            "v_cvt_pk_bf16_f32 v204, v204, v205\n\t"
            "v_cvt_pk_bf16_f32 v201, v202, v203\n\t"
            "v_mfma_f32_16x16x32_bf16 v[212:215], v[16:19], v[184:187], v[212:215]\n\t"
            "v_cvt_pk_bf16_f32 v205, v206, v207\n\t"
            "v_mul_f32 v208, v192, v208\n\t"
            "v_mfma_f32_16x16x32_bf16 v[212:215], v[20:23], v[188:191], v[212:215]\n\t"
            "ds_write_b64 v227, v[200:201]\n\t"
            "v_mfma_f32_16x16x32_bf16 v[216:219], v[24:27], v[168:171], 0\n\t"
            "ds_read_b128 v[200:203], v198 offset:18816\n\t"
            "v_mul_f32 v209, v192, v209\n\t"
            "v_mfma_f32_16x16x32_bf16 v[216:219], v[28:31], v[172:175], v[216:219]\n\t"
            "ds_write_b64 v227, v[204:205] offset:128\n\t"
            "v_mfma_f32_16x16x32_bf16 v[216:219], v[32:35], v[176:179], v[216:219]\n\t"
            "ds_read_b128 v[204:207], v198 offset:19840\n\t"
            "v_mul_f32 v210, v192, v210\n\t"
            "v_mfma_f32_16x16x32_bf16 v[216:219], v[36:39], v[180:183], v[216:219]\n\t"
            "v_mul_f32 v211, v192, v211\n\t"
            "v_cvt_pk_bf16_f32 v208, v208, v209\n\t"
            "v_mfma_f32_16x16x32_bf16 v[216:219], v[40:43], v[184:187], v[216:219]\n\t"
            "s_waitcnt lgkmcnt(10)\n\t"
            "v_cvt_pk_bf16_f32 v209, v210, v211\n\t"
            "v_mul_f32 v212, v193, v212\n\t"
            "v_mfma_f32_16x16x32_bf16 v[216:219], v[44:47], v[188:191], v[216:219]\n\t"
            "ds_write_b64 v227, v[208:209] offset:256\n\t"
            "v_mfma_f32_16x16x32_bf16 v[220:223], v[48:51], v[168:171], 0\n\t"
            "ds_read_b128 v[168:171], v196 offset:6144\n\t"
            "v_mul_f32 v213, v193, v213\n\t"
            "v_mfma_f32_16x16x32_bf16 v[220:223], v[52:55], v[172:175], v[220:223]\n\t"
            "s_waitcnt lgkmcnt(4)\n\t"
            "ds_read_b128 v[172:175], v197 offset:6144\n\t"
            "v_add_f32 v200, v200, v202\n\t"
            "v_mfma_f32_16x16x32_bf16 v[220:223], v[56:59], v[176:179], v[220:223]\n\t"
            "ds_read_b128 v[176:179], v196 offset:6272\n\t"
            "v_add_f32 v201, v201, v203\n\t"
            "v_mfma_f32_16x16x32_bf16 v[220:223], v[60:63], v[180:183], v[220:223]\n\t"
            "ds_read_b128 v[180:183], v197 offset:6272\n\t"
            "v_mul_f32 v214, v193, v214\n\t"
            "v_mfma_f32_16x16x32_bf16 v[220:223], v[64:67], v[184:187], v[220:223]\n\t"
            "s_waitcnt lgkmcnt(5)\n\t"
            "ds_read_b128 v[184:187], v196 offset:6400\n\t"
            "v_add_f32 v204, v204, v206\n\t"
            "v_mfma_f32_16x16x32_bf16 v[220:223], v[68:71], v[188:191], v[220:223]\n\t"
            "ds_read_b128 v[188:191], v197 offset:6400\n\t"
            "v_add_f32 v200, v200, v204\n\t"
            "v_add_f32 v205, v205, v207\n\t"
            "v_mul_f32 v200, 0x3baaaaab, v200\n\t"
            "v_add_f32 v201, v201, v205\n\t"
            "v_mul_f32_e64 v200, v200, -v200\n\t"
            "v_fmac_f32 v200, 0x3baaaaab, v201\n\t"
            "v_max_f32 v200, 0, v200\n\t"
            "v_add_f32 v200, 0x3727c5ac, v200\n\t"
            "v_rsq_f32 v200, v200\n\t"
            "v_mul_f32 v215, v193, v215\n\t"
            "v_cvt_pk_bf16_f32 v212, v212, v213\n\t"
            "v_cvt_pk_bf16_f32 v213, v214, v215\n\t"
            "v_mul_f32 v216, v193, v216\n\t"
            "v_mul_f32 v217, v193, v217\n\t"
            "v_mul_f32 v218, v193, v218\n\t"
            "v_mul_f32 v219, v193, v219\n\t"
            "v_mul_f32 v220, v193, v220\n\t"
            "v_mul_f32 v221, v193, v221\n\t"
            "v_mul_f32 v222, v193, v222\n\t"
            "v_mul_f32 v223, v193, v223\n\t"
            "v_cvt_pk_bf16_f32 v216, v216, v217\n\t"
            "v_cvt_pk_bf16_f32 v217, v218, v219\n\t"
            "v_cvt_pk_bf16_f32 v220, v220, v221\n\t"
            "v_cvt_pk_bf16_f32 v221, v222, v223\n\t"
            "ds_write_b32 v232, v200\n\t"
            "v_mfma_f32_16x16x32_bf16 v[200:203], v[144:147], v[0:3], 0\n\t"
            "ds_read_b128 v[192:195], v233\n\t"
            "v_mfma_f32_16x16x32_bf16 v[200:203], v[148:151], v[4:7], v[200:203]\n\t"
            "ds_write_b64 v228, v[212:213]\n\t"
            "v_mfma_f32_16x16x32_bf16 v[200:203], v[152:155], v[8:11], v[200:203]\n\t"
            "s_add_u32 s49, s33, 20480\n\t"
            "buffer_load_dwordx4 v[80:83], v247, s[28:31], s49 offen sc1\n\t"
            "v_mfma_f32_16x16x32_bf16 v[200:203], v[156:159], v[12:15], v[200:203]\n\t"
            "ds_write_b64 v228, v[216:217] offset:128\n\t"
            "v_mfma_f32_16x16x32_bf16 v[200:203], v[160:163], v[16:19], v[200:203]\n\t"
            "s_add_u32 s49, s33, 21504\n\t"
            "buffer_load_dwordx4 v[84:87], v247, s[28:31], s49 offen sc1\n\t"
            "v_mfma_f32_16x16x32_bf16 v[200:203], v[164:167], v[20:23], v[200:203]\n\t"
            "s_add_u32 s49, s33, 22528\n\t"
            "buffer_load_dwordx4 v[88:91], v247, s[28:31], s49 offen sc1\n\t"
            "v_mfma_f32_16x16x32_bf16 v[204:207], v[144:147], v[24:27], 0\n\t"
            "s_add_u32 s49, s33, 23552\n\t"
            "buffer_load_dwordx4 v[92:95], v247, s[28:31], s49 offen sc1\n\t"
            "v_mfma_f32_16x16x32_bf16 v[204:207], v[148:151], v[28:31], v[204:207]\n\t"
            "ds_write_b64 v228, v[220:221] offset:256\n\t"
            "v_mfma_f32_16x16x32_bf16 v[204:207], v[152:155], v[32:35], v[204:207]\n\t"
            "s_waitcnt lgkmcnt(3)\n\t"
            "v_mul_f32 v200, v192, v200\n\t"
            "v_mul_f32 v201, v193, v201\n\t"
            "v_mfma_f32_16x16x32_bf16 v[204:207], v[156:159], v[36:39], v[204:207]\n\t"
            "v_mul_f32 v202, v194, v202\n\t"
            "v_mul_f32 v203, v195, v203\n\t"
            "v_mfma_f32_16x16x32_bf16 v[204:207], v[160:163], v[40:43], v[204:207]\n\t"
            "s_add_u32 s49, s33, 24576\n\t"
            "buffer_load_dwordx4 v[96:99], v247, s[28:31], s49 offen sc1\n\t"
            "v_mfma_f32_16x16x32_bf16 v[204:207], v[164:167], v[44:47], v[204:207]\n\t"
            "s_add_u32 s49, s33, 25600\n\t"
            "buffer_load_dwordx4 v[100:103], v247, s[28:31], s49 offen sc1\n\t"
            "v_mfma_f32_16x16x32_bf16 v[208:211], v[144:147], v[48:51], 0\n\t"
            "ds_read_b128 v[144:147], v196 offset:12288\n\t"
            "v_cvt_pk_bf16_f32 v200, v200, v201\n\t"
            "v_mfma_f32_16x16x32_bf16 v[208:211], v[148:151], v[52:55], v[208:211]\n\t"
            "ds_read_b128 v[148:151], v197 offset:12288\n\t"
            "v_cvt_pk_bf16_f32 v201, v202, v203\n\t"
            "v_mfma_f32_16x16x32_bf16 v[208:211], v[152:155], v[56:59], v[208:211]\n\t"
            "ds_read_b128 v[152:155], v196 offset:12416\n\t"
            "v_mul_f32 v204, v192, v204\n\t"
            "v_mfma_f32_16x16x32_bf16 v[208:211], v[156:159], v[60:63], v[208:211]\n\t"
            "ds_read_b128 v[156:159], v197 offset:12416\n\t"
            "v_mul_f32 v205, v193, v205\n\t"
            "v_mfma_f32_16x16x32_bf16 v[208:211], v[160:163], v[64:67], v[208:211]\n\t"
            "ds_read_b128 v[160:163], v196 offset:12544\n\t"
            "v_mul_f32 v206, v194, v206\n\t"
            "v_mfma_f32_16x16x32_bf16 v[208:211], v[164:167], v[68:71], v[208:211]\n\t"
            "ds_read_b128 v[164:167], v197 offset:12544\n\t"
            "v_mul_f32 v207, v195, v207\n\t"
            "v_mfma_f32_16x16x32_bf16 v[212:215], v[168:171], v[0:3], 0\n\t"
            "s_add_u32 s49, s33, 26624\n\t"
            "buffer_load_dwordx4 v[104:107], v247, s[28:31], s49 offen sc1\n\t"
            "v_mfma_f32_16x16x32_bf16 v[212:215], v[172:175], v[4:7], v[212:215]\n\t"
            "s_add_u32 s49, s33, 27648\n\t"
            "buffer_load_dwordx4 v[108:111], v247, s[28:31], s49 offen sc1\n\t"
            "v_mfma_f32_16x16x32_bf16 v[212:215], v[176:179], v[8:11], v[212:215]\n\t"
            "v_mul_f32 v208, v192, v208\n\t"
            "v_mul_f32 v209, v193, v209\n\t"
            "v_mfma_f32_16x16x32_bf16 v[212:215], v[180:183], v[12:15], v[212:215]\n\t"
            "v_mul_f32 v210, v194, v210\n\t"
            "v_mul_f32 v211, v195, v211\n\t"
            "v_mfma_f32_16x16x32_bf16 v[212:215], v[184:187], v[16:19], v[212:215]\n\t"
            "ds_read_b128 v[192:195], v233 offset:64\n\t"
            "v_cvt_pk_bf16_f32 v204, v204, v205\n\t"
            "v_mfma_f32_16x16x32_bf16 v[212:215], v[188:191], v[20:23], v[212:215]\n\t"
            "s_add_u32 s49, s33, 28672\n\t"
            "buffer_load_dwordx4 v[112:115], v247, s[28:31], s49 offen sc1\n\t"
            "v_mfma_f32_16x16x32_bf16 v[216:219], v[168:171], v[24:27], 0\n\t"
            "v_cvt_pk_bf16_f32 v205, v206, v207\n\t"
            "v_cvt_pk_bf16_f32 v208, v208, v209\n\t"
            "v_mfma_f32_16x16x32_bf16 v[216:219], v[172:175], v[28:31], v[216:219]\n\t"
            "s_add_u32 s49, s33, 29696\n\t"
            "buffer_load_dwordx4 v[116:119], v247, s[28:31], s49 offen sc1\n\t"
            "v_mfma_f32_16x16x32_bf16 v[216:219], v[176:179], v[32:35], v[216:219]\n\t"
            "s_waitcnt lgkmcnt(0)\n\t"
            "v_mul_f32 v212, v192, v212\n\t"
            "v_mul_f32 v213, v193, v213\n\t"
            "v_mfma_f32_16x16x32_bf16 v[216:219], v[180:183], v[36:39], v[216:219]\n\t"
            "v_mul_f32 v214, v194, v214\n\t"
            "v_mul_f32 v215, v195, v215\n\t"
            "v_mfma_f32_16x16x32_bf16 v[216:219], v[184:187], v[40:43], v[216:219]\n\t"
            "v_cvt_pk_bf16_f32 v202, v212, v213\n\t"
            "v_cvt_pk_bf16_f32 v203, v214, v215\n\t"
            "v_mfma_f32_16x16x32_bf16 v[216:219], v[188:191], v[44:47], v[216:219]\n\t"
            "s_add_u32 s49, s33, 30720\n\t"
            "buffer_load_dwordx4 v[120:123], v247, s[28:31], s49 offen sc1\n\t"
            "v_mfma_f32_16x16x32_bf16 v[220:223], v[168:171], v[48:51], 0\n\t"
            "v_cvt_pk_bf16_f32 v209, v210, v211\n\t"
            "v_mfma_f32_16x16x32_bf16 v[220:223], v[172:175], v[52:55], v[220:223]\n\t"
            "s_add_u32 s49, s33, 31744\n\t"
            "buffer_load_dwordx4 v[124:127], v247, s[28:31], s49 offen sc1\n\t"
            "v_mfma_f32_16x16x32_bf16 v[220:223], v[176:179], v[56:59], v[220:223]\n\t"
            "v_mul_f32 v216, v192, v216\n\t"
            "v_mul_f32 v217, v193, v217\n\t"
            "v_mfma_f32_16x16x32_bf16 v[220:223], v[180:183], v[60:63], v[220:223]\n\t"
            "v_mul_f32 v218, v194, v218\n\t"
            "v_mul_f32 v219, v195, v219\n\t"
            "v_mfma_f32_16x16x32_bf16 v[220:223], v[184:187], v[64:67], v[220:223]\n\t"
            "v_cvt_pk_bf16_f32 v206, v216, v217\n\t"
            "v_cvt_pk_bf16_f32 v207, v218, v219\n\t"
            "v_mfma_f32_16x16x32_bf16 v[220:223], v[188:191], v[68:71], v[220:223]\n\t"
            "s_add_u32 s49, s33, 32768\n\t"
            "buffer_load_dwordx4 v[128:131], v247, s[28:31], s49 offen sc1\n\t"
            "v_mfma_f32_16x16x32_bf16 v[212:215], v[144:147], v[0:3], 0\n\t"
            "s_add_u32 s49, s33, 33792\n\t"
            "buffer_load_dwordx4 v[132:135], v247, s[28:31], s49 offen sc1\n\t"
            "v_mfma_f32_16x16x32_bf16 v[212:215], v[148:151], v[4:7], v[212:215]\n\t"
            "s_add_u32 s49, s33, 34816\n\t"
            "buffer_load_dwordx4 v[136:139], v247, s[28:31], s49 offen sc1\n\t"
            "v_mfma_f32_16x16x32_bf16 v[212:215], v[152:155], v[8:11], v[212:215]\n\t"
            "v_mul_f32 v220, v192, v220\n\t"
            "v_mul_f32 v221, v193, v221\n\t"
            "v_mfma_f32_16x16x32_bf16 v[212:215], v[156:159], v[12:15], v[212:215]\n\t"
            "v_mul_f32 v222, v194, v222\n\t"
            "v_mul_f32 v223, v195, v223\n\t"
            "v_mfma_f32_16x16x32_bf16 v[212:215], v[160:163], v[16:19], v[212:215]\n\t"
            "ds_read_b128 v[192:195], v233 offset:128\n\t"
            "v_cvt_pk_bf16_f32 v210, v220, v221\n\t"
            "v_mfma_f32_16x16x32_bf16 v[212:215], v[164:167], v[20:23], v[212:215]\n\t"
            "v_cvt_pk_bf16_f32 v211, v222, v223\n\t"
            "v_mfma_f32_16x16x32_bf16 v[216:219], v[144:147], v[24:27], 0\n\t"
            "ds_write_b128 v230, v[200:203]\n\t"
            "v_mfma_f32_16x16x32_bf16 v[216:219], v[148:151], v[28:31], v[216:219]\n\t"
            "ds_write_b128 v230, v[204:207] offset:1536\n\t"
            "v_mfma_f32_16x16x32_bf16 v[216:219], v[152:155], v[32:35], v[216:219]\n\t"
            "ds_write_b128 v230, v[208:211] offset:3072\n\t"
            "v_mfma_f32_16x16x32_bf16 v[216:219], v[156:159], v[36:39], v[216:219]\n\t"
            "s_add_u32 s49, s33, 35840\n\t"
            "buffer_load_dwordx4 v[140:143], v247, s[28:31], s49 offen sc1\n\t"
            "v_mfma_f32_16x16x32_bf16 v[216:219], v[160:163], v[40:43], v[216:219]\n\t"
            "s_waitcnt lgkmcnt(3)\n\t"
            "v_mul_f32 v212, v192, v212\n\t"
            "v_mul_f32 v213, v193, v213\n\t"
            "v_mfma_f32_16x16x32_bf16 v[216:219], v[164:167], v[44:47], v[216:219]\n\t"
            "v_mul_f32 v214, v194, v214\n\t"
            "v_mul_f32 v215, v195, v215\n\t"
            "v_mfma_f32_16x16x32_bf16 v[220:223], v[144:147], v[48:51], 0\n\t"
            "v_cvt_pk_bf16_f32 v212, v212, v213\n\t"
            "v_cvt_pk_bf16_f32 v213, v214, v215\n\t"
            "v_mfma_f32_16x16x32_bf16 v[220:223], v[148:151], v[52:55], v[220:223]\n\t"
            "ds_write_b64 v231, v[212:213]\n\t"
            "v_mfma_f32_16x16x32_bf16 v[220:223], v[152:155], v[56:59], v[220:223]\n\t"
            "v_mul_f32 v216, v192, v216\n\t"
            "v_mul_f32 v217, v193, v217\n\t"
            "v_mfma_f32_16x16x32_bf16 v[220:223], v[156:159], v[60:63], v[220:223]\n\t"
            "v_mul_f32 v218, v194, v218\n\t"
            "v_mul_f32 v219, v195, v219\n\t"
            "v_mfma_f32_16x16x32_bf16 v[220:223], v[160:163], v[64:67], v[220:223]\n\t"
            "v_cvt_pk_bf16_f32 v216, v216, v217\n\t"
            "v_cvt_pk_bf16_f32 v217, v218, v219\n\t"
            "v_mfma_f32_16x16x32_bf16 v[220:223], v[164:167], v[68:71], v[220:223]\n\t"
            "ds_write_b64 v231, v[216:217] offset:1536\n\t"
            "s_nop 6\n\t"
            "v_mul_f32 v220, v192, v220\n\t"
            "v_mul_f32 v221, v193, v221\n\t"
            "v_mul_f32 v222, v194, v222\n\t"
            "v_mul_f32 v223, v195, v223\n\t"
            "v_cvt_pk_bf16_f32 v220, v220, v221\n\t"
            "v_cvt_pk_bf16_f32 v221, v222, v223\n\t"
            "ds_write_b64 v231, v[220:221] offset:3072\n\t"
            "s_waitcnt lgkmcnt(0)\n\t"
            "s_nop 7\n\t"
            "v_mov_b32 v144, s44\n\t"
            "v_mov_b32 v145, -1\n\t"
            "ds_write_b32 v144, v145\n\t"
            "s_cmp_ge_u32 s33, 221184\n\t"
            "s_cbranch_scc1 .Lrp%=\n\t"
            "s_barrier\n\t"
            "s_waitcnt vmcnt(0)\n\t"
            "s_nop 7\n\t"
            "v_add_u32 v232, s67, v242\n\t"
            "v_xor_b32 v233, 64, v232\n\t"
            "v_add_u32 v234, s67, v247\n\t"
            "ds_read_b128 v[144:147], v232\n\t"
            "ds_read_b128 v[148:151], v233\n\t"
            "ds_read_b128 v[152:155], v232 offset:128\n\t"
            "ds_read_b128 v[156:159], v233 offset:128\n\t"
            "ds_read_b128 v[160:163], v232 offset:256\n\t"
            "ds_read_b128 v[164:167], v233 offset:256\n\t"
            "ds_read_b128 v[176:179], v234 offset:18816\n\t"
            "ds_read_b128 v[180:183], v234 offset:19840\n\t"
            "s_waitcnt lgkmcnt(7)\n\t"
            "v_mfma_f32_16x16x32_bf16 v[200:203], v[144:147], v[0:3], 0\n\t"
            "s_waitcnt lgkmcnt(6)\n\t"
            "v_mfma_f32_16x16x32_bf16 v[200:203], v[148:151], v[4:7], v[200:203]\n\t"
            "s_waitcnt lgkmcnt(5)\n\t"
            "v_mfma_f32_16x16x32_bf16 v[200:203], v[152:155], v[8:11], v[200:203]\n\t"
            "s_waitcnt lgkmcnt(4)\n\t"
            "v_mfma_f32_16x16x32_bf16 v[200:203], v[156:159], v[12:15], v[200:203]\n\t"
            "s_waitcnt lgkmcnt(1)\n\t"
            "v_add_f32 v176, v176, v178\n\t"
            "s_waitcnt lgkmcnt(0)\n\t"
            "v_add_f32 v180, v180, v182\n\t"
            "v_mfma_f32_16x16x32_bf16 v[200:203], v[160:163], v[16:19], v[200:203]\n\t"
            "v_add_f32 v176, v176, v180\n\t"
            "v_add_f32 v177, v177, v179\n\t"
            "v_mfma_f32_16x16x32_bf16 v[200:203], v[164:167], v[20:23], v[200:203]\n\t"
            "v_add_f32 v181, v181, v183\n\t"
            "v_mul_f32 v176, 0x3baaaaab, v176\n\t"
            "v_mfma_f32_16x16x32_bf16 v[204:207], v[144:147], v[24:27], 0\n\t"
            "v_add_f32 v177, v177, v181\n\t"
            "v_mul_f32_e64 v176, v176, -v176\n\t"
            "v_mfma_f32_16x16x32_bf16 v[204:207], v[148:151], v[28:31], v[204:207]\n\t"
            "v_fmac_f32 v176, 0x3baaaaab, v177\n\t"
            "v_max_f32 v176, 0, v176\n\t"
            "v_mfma_f32_16x16x32_bf16 v[204:207], v[152:155], v[32:35], v[204:207]\n\t"
            "v_add_f32 v176, 0x3727c5ac, v176\n\t"
            "v_rsq_f32 v176, v176\n\t"
            "v_mfma_f32_16x16x32_bf16 v[204:207], v[156:159], v[36:39], v[204:207]\n\t"
            "ds_write_b32 v246, v176\n\t"
            "v_mfma_f32_16x16x32_bf16 v[204:207], v[160:163], v[40:43], v[204:207]\n\t"
            "ds_read_b128 v[228:231], v245\n\t"
            "v_mfma_f32_16x16x32_bf16 v[204:207], v[164:167], v[44:47], v[204:207]\n\t"
            "v_mfma_f32_16x16x32_bf16 v[208:211], v[144:147], v[48:51], 0\n\t"
            "v_mfma_f32_16x16x32_bf16 v[208:211], v[148:151], v[52:55], v[208:211]\n\t"
            "v_mfma_f32_16x16x32_bf16 v[208:211], v[152:155], v[56:59], v[208:211]\n\t"
            "v_mfma_f32_16x16x32_bf16 v[208:211], v[156:159], v[60:63], v[208:211]\n\t"
            "v_mfma_f32_16x16x32_bf16 v[208:211], v[160:163], v[64:67], v[208:211]\n\t"
            "v_mfma_f32_16x16x32_bf16 v[208:211], v[164:167], v[68:71], v[208:211]\n\t"
            "v_mfma_f32_16x16x32_bf16 v[212:215], v[144:147], v[72:75], 0\n\t"
            "s_waitcnt lgkmcnt(0)\n\t"
            "v_mul_f32 v200, v228, v200\n\t"
            "v_mul_f32 v201, v229, v201\n\t"
            "v_mfma_f32_16x16x32_bf16 v[212:215], v[148:151], v[76:79], v[212:215]\n\t"
            "v_mul_f32 v204, v228, v204\n\t"
            "v_mul_f32 v205, v229, v205\n\t"
            "v_mfma_f32_16x16x32_bf16 v[212:215], v[152:155], v[80:83], v[212:215]\n\t"
            "v_mul_f32 v208, v228, v208\n\t"
            "v_mul_f32 v209, v229, v209\n\t"
            "v_mfma_f32_16x16x32_bf16 v[212:215], v[156:159], v[84:87], v[212:215]\n\t"
            "v_mul_f32 v202, v230, v202\n\t"
            "v_mul_f32 v203, v231, v203\n\t"
            "v_mfma_f32_16x16x32_bf16 v[212:215], v[160:163], v[88:91], v[212:215]\n\t"
            "v_cvt_pk_bf16_f32 v200, v200, v201\n\t"
            "v_mul_f32 v206, v230, v206\n\t"
            "v_mfma_f32_16x16x32_bf16 v[212:215], v[164:167], v[92:95], v[212:215]\n\t"
            "v_mul_f32 v207, v231, v207\n\t"
            "v_cvt_pk_bf16_f32 v204, v204, v205\n\t"
            "v_mfma_f32_16x16x32_bf16 v[216:219], v[144:147], v[96:99], 0\n\t"
            "v_mul_f32 v210, v230, v210\n\t"
            "v_mul_f32 v211, v231, v211\n\t"
            "v_mfma_f32_16x16x32_bf16 v[216:219], v[148:151], v[100:103], v[216:219]\n\t"
            "v_cvt_pk_bf16_f32 v208, v208, v209\n\t"
            "v_cvt_pk_bf16_f32 v201, v202, v203\n\t"
            "v_mfma_f32_16x16x32_bf16 v[216:219], v[152:155], v[104:107], v[216:219]\n\t"
            "v_mul_f32 v212, v228, v212\n\t"
            "v_mul_f32 v213, v229, v213\n\t"
            "v_mfma_f32_16x16x32_bf16 v[216:219], v[156:159], v[108:111], v[216:219]\n\t"
            "v_mul_f32 v214, v230, v214\n\t"
            "v_mul_f32 v215, v231, v215\n\t"
            "v_mfma_f32_16x16x32_bf16 v[216:219], v[160:163], v[112:115], v[216:219]\n\t"
            "v_cvt_pk_bf16_f32 v212, v212, v213\n\t"
            "v_cvt_pk_bf16_f32 v205, v206, v207\n\t"
            "v_mfma_f32_16x16x32_bf16 v[216:219], v[164:167], v[116:119], v[216:219]\n\t"
            "v_cvt_pk_bf16_f32 v209, v210, v211\n\t"
            "v_cvt_pk_bf16_f32 v213, v214, v215\n\t"
            "v_mfma_f32_16x16x32_bf16 v[220:223], v[144:147], v[120:123], 0\n\t"
            "v_mfma_f32_16x16x32_bf16 v[220:223], v[148:151], v[124:127], v[220:223]\n\t"
            "v_mfma_f32_16x16x32_bf16 v[220:223], v[152:155], v[128:131], v[220:223]\n\t"
            "v_mfma_f32_16x16x32_bf16 v[220:223], v[156:159], v[132:135], v[220:223]\n\t"
            "s_nop 1\n\t"
            "v_mul_f32 v216, v228, v216\n\t"
            "v_mfma_f32_16x16x32_bf16 v[220:223], v[160:163], v[136:139], v[220:223]\n\t"
            "v_mul_f32 v217, v229, v217\n\t"
            "v_mul_f32 v218, v230, v218\n\t"
            "v_mfma_f32_16x16x32_bf16 v[220:223], v[164:167], v[140:143], v[220:223]\n\t"
            "v_mul_f32 v219, v231, v219\n\t"
            "v_cvt_pk_bf16_f32 v216, v216, v217\n\t"
            "v_cvt_pk_bf16_f32 v217, v218, v219\n\t"
            "s_nop 4\n\t"
            "v_mul_f32 v220, v228, v220\n\t"
            "v_mul_f32 v221, v229, v221\n\t"
            "v_mul_f32 v222, v230, v222\n\t"
            "v_mul_f32 v223, v231, v223\n\t"
            "v_cvt_pk_bf16_f32 v220, v220, v221\n\t"
            "v_cvt_pk_bf16_f32 v221, v222, v223\n\t"
            "s_nop 7\n\t"
            "s_add_u32 s45, s46, s27\n\t"
            "s_mov_b32 s46, s47\n\t"
            "s_mov_b32 s47, s68\n\t"
            "s_xor_b32 s48, s48, s71\n\t"
            "s_xor_b32 s67, s67, s71\n\t"
            "s_add_u32 s76, s76, 4\n\t"
            "s_lshl_b32 s56, s56, 2\n\t"
            "s_lshr_b32 s49, s26, 30\n\t"
            "s_or_b32 s56, s56, s49\n\t"
            "ds_read_b128 v[168:171], v254\n\t"
            "ds_read_b128 v[172:175], v254 offset:64\n\t"
            "s_waitcnt vmcnt(0)\n\t"
            "ds_read_b64 v[144:145], v253\n\t"
            "ds_read_b64 v[146:147], v253 offset:96\n\t"
            "ds_read_b64 v[148:149], v253 offset:1744\n\t"
            "ds_read_b64 v[150:151], v253 offset:1840\n\t"
            "ds_read_b64 v[152:153], v253 offset:8\n\t"
            "ds_read_b64 v[154:155], v253 offset:104\n\t"
            "ds_read_b64 v[156:157], v253 offset:1752\n\t"
            "ds_read_b64 v[158:159], v253 offset:1848\n\t"
            "ds_read_b64 v[160:161], v253 offset:16\n\t"
            "ds_read_b64 v[162:163], v253 offset:112\n\t"
            "ds_read_b64 v[164:165], v253 offset:1760\n\t"
            "ds_read_b64 v[166:167], v253 offset:1856\n\t"
            "s_waitcnt lgkmcnt(0)\n\t"
            "s_nop 7\n\t"
            ".p2align 6\n\t"
            ".Lwc_loop%=:\n\t"
            "s_setprio 0\n\t"
            "ds_read_b128 v[228:231], v245 offset:64\n\t"
            "v_mov_b64 v[180:181], 0\n\t"
            "v_mov_b64 v[182:183], 0\n\t"
            "v_mov_b64 v[176:177], 0\n\t"
            "v_mov_b64 v[178:179], 0\n\t"
            "v_mov_b32 v249, s76\n\t"
            "v_mov_b64 v[184:185], 0\n\t"
            "s_waitcnt lgkmcnt(2)\n\t"
            "v_smfmac_f32_16x16x64_bf16 v[176:179], v[168:171], v[144:151], v255\n\t"
            "v_smfmac_f32_16x16x64_bf16 v[180:183], v[172:175], v[152:159], v255\n\t"
            "ds_read_b64 v[146:147], v253 offset:120\n\t"
            "v_mov_b64 v[188:189], 0\n\t"
            "v_mov_b64 v[186:187], 0\n\t"
            "v_mov_b64 v[190:191], 0\n\t"
            "v_add_u32 v236, s67, v250\n\t"
            "v_add_u32 v232, s48, v242\n\t"
            "v_xor_b32 v233, 64, v232\n\t"
            "v_add_u32 v248, s67, v251\n\t"
            "v_add_u32 v234, s67, v247\n\t"
            "v_cvt_pk_bf16_f32 v176, v176, v177\n\t"
            "v_cvt_pk_bf16_f32 v177, v178, v179\n\t"
            "v_cvt_pk_bf16_f32 v179, v182, v183\n\t"
            "v_cvt_pk_bf16_f32 v178, v180, v181\n\t"
            "v_mov_b64 v[180:181], 0\n\t"
            "v_mov_b64 v[182:183], 0\n\t"
            "ds_read_b64 v[144:145], v253 offset:24\n\t"
            "ds_read_b64 v[148:149], v253 offset:1768\n\t"
            "ds_read_b32 v249, v249\n\t"
            "ds_read_b128 v[172:175], v254 offset:192\n\t"
            "ds_read_b64 v[150:151], v253 offset:1864\n\t"
            "ds_read_b128 v[168:171], v254 offset:128\n\t"
            "ds_read_b64 v[154:155], v253 offset:128\n\t"
            "ds_read_b64 v[152:153], v253 offset:32\n\t"
            "ds_read_b64 v[156:157], v253 offset:1776\n\t"
            "ds_read_b64 v[158:159], v253 offset:1872\n\t"
            "s_waitcnt lgkmcnt(7)\n\t"
            "v_readfirstlane_b32 s26, v249\n\t"
            "ds_read_b32 v249, v252 offset:1024\n\t"
            "s_and_b32 s68, s26, 0x7ffffff\n\t"
            "s_add_u32 s68, s68, s78\n\t"
            "s_and_b32 s69, s26, 0x38000000\n\t"
            "s_or_b32 s69, s69, s39\n\t"
            "s_bitcmp1_b32 s26, 27\n\t"
            "s_cselect_b32 s79, 2.0, s68\n\t"
            "s_waitcnt lgkmcnt(5)\n\t"
            "v_smfmac_f32_16x16x64_bf16 v[184:187], v[168:171], v[160:167], v255\n\t"
            "v_smfmac_f32_16x16x64_bf16 v[188:191], v[172:175], v[144:151], v255\n\t"
            "s_and_b32 s57, s56, 3\n\t"
            "s_lshl_b32 s58, s57, 3\n\t"
            "s_lshr_b32 m0, s54, s58\n\t"
            "ds_read_b128 v[168:171], v254 offset:256\n\t"
            "s_lshl_b32 s58, s57, 4\n\t"
            "s_lshr_b64 s[36:37], s[50:51], s58\n\t"
            "s_and_b32 s57, s36, 0xffff\n\t"
            "s_add_u32 s57, s57, s67\n\t"
            "v_mfma_f32_4x4x4_16b_bf16 v[196:199], v[176:177], v[176:177], 0\n\t"
            "v_mfma_f32_4x4x4_16b_bf16 v[192:195], v[238:239], v[176:177], 0\n\t"
            "s_nop 0\n\t"
            "v_mfma_f32_4x4x4_16b_bf16 v[196:199], v[178:179], v[178:179], v[196:199]\n\t"
            "v_mfma_f32_4x4x4_16b_bf16 v[192:195], v[238:239], v[178:179], v[192:195]\n\t"
            "ds_read_b128 v[172:175], v254 offset:320\n\t"
            "s_lshr_b64 s[36:37], s[52:53], s58\n\t"
            "s_and_b32 s58, s36, 0xffff\n\t"
            "s_add_u32 s58, s58, s67\n\t"
            "ds_read_b64 v[164:165], v253 offset:1784\n\t"
            "s_and_b32 s59, s50, 0xffff\n\t"
            "s_add_u32 s59, s59, s67\n\t"
            "s_and_b32 s38, s52, 0xffff\n\t"
            "s_add_u32 s38, s38, s67\n\t"
            "ds_read_b64 v[166:167], v253 offset:1880\n\t"
            "v_writelane_b32 v236, s59, m0\n\t"
            "v_writelane_b32 v236, s57, 63\n\t"
            "v_cvt_pk_bf16_f32 v184, v184, v185\n\t"
            "v_cvt_pk_bf16_f32 v185, v186, v187\n\t"
            "v_cvt_pk_bf16_f32 v186, v188, v189\n\t"
            "v_cvt_pk_bf16_f32 v187, v190, v191\n\t"
            "v_xor_b32 v237, 16, v236\n\t"
            "v_mov_b64 v[188:189], 0\n\t"
            "v_mov_b64 v[190:191], 0\n\t"
            "v_writelane_b32 v248, s38, m0\n\t"
            "v_writelane_b32 v248, s58, 63\n\t"
            "s_waitcnt lgkmcnt(4)\n\t"
            "v_and_b32 v249, s69, v249\n\t"
            "s_barrier\n\t"
            "s_setprio 3\n\t"
            "ds_read_b64 v[162:163], v253 offset:136\n\t"
            "ds_read_b64 v[160:161], v253 offset:40\n\t"
            "ds_read_b64 v[150:151], v253 offset:1888\n\t"
            "ds_read_b64 v[144:145], v253 offset:48\n\t"
            "ds_read_b64 v[148:149], v253 offset:1792\n\t"
            "ds_read_b64 v[146:147], v253 offset:144\n\t"
            "ds_write_b128 v236, v[176:179]\n\t"
            "v_mov_b64 v[178:179], 0\n\t"
            "v_mov_b64 v[176:177], 0\n\t"
            "ds_write_b128 v237, v[184:187]\n\t"
            "s_waitcnt lgkmcnt(6)\n\t"
            "v_smfmac_f32_16x16x64_bf16 v[176:179], v[168:171], v[152:159], v255\n\t"
            "v_smfmac_f32_16x16x64_bf16 v[180:183], v[172:175], v[160:167], v255\n\t"
            "ds_read_b128 v[168:171], v254 offset:384\n\t"
            "ds_read_b64 v[152:153], v253 offset:56\n\t"
            "v_mfma_f32_4x4x4_16b_bf16 v[196:199], v[184:185], v[184:185], v[196:199]\n\t"
            "v_mfma_f32_4x4x4_16b_bf16 v[192:195], v[238:239], v[184:185], v[192:195]\n\t"
            "s_nop 0\n\t"
            "v_mfma_f32_4x4x4_16b_bf16 v[196:199], v[186:187], v[186:187], v[196:199]\n\t"
            "v_mfma_f32_4x4x4_16b_bf16 v[192:195], v[238:239], v[186:187], v[192:195]\n\t"
            "ds_read_b64 v[158:159], v253 offset:1896\n\t"
            "v_mov_b64 v[186:187], 0\n\t"
            "v_mov_b64 v[184:185], 0\n\t"
            "v_cvt_pk_bf16_f32 v176, v176, v177\n\t"
            "v_cvt_pk_bf16_f32 v177, v178, v179\n\t"
            "v_cvt_pk_bf16_f32 v178, v180, v181\n\t"
            "v_cvt_pk_bf16_f32 v179, v182, v183\n\t"
            "v_mov_b64 v[180:181], 0\n\t"
            "v_mov_b64 v[182:183], 0\n\t"
            "ds_read_b64 v[156:157], v253 offset:1800\n\t"
            "ds_read_b128 v[172:175], v254 offset:448\n\t"
            "ds_read_b64 v[154:155], v253 offset:152\n\t"
            "ds_read_b64 v[166:167], v253 offset:1904\n\t"
            "ds_read_b64 v[160:161], v253 offset:64\n\t"
            "ds_read_b64 v[164:165], v253 offset:1808\n\t"
            "s_waitcnt lgkmcnt(14)\n\t"
            "ds_read_b64 v[162:163], v253 offset:160\n\t"
            "s_waitcnt lgkmcnt(14)\n\t"
            "ds_write_b128 v236, v[176:179] offset:128\n\t"
            "s_waitcnt lgkmcnt(5)\n\t"
            "v_smfmac_f32_16x16x64_bf16 v[184:187], v[168:171], v[144:151], v255\n\t"
            "v_smfmac_f32_16x16x64_bf16 v[188:191], v[172:175], v[152:159], v255\n\t"
            "ds_read_b128 v[172:175], v254 offset:576\n\t"
            "ds_read_b64 v[146:147], v253 offset:168\n\t"
            "v_mfma_f32_4x4x4_16b_bf16 v[196:199], v[176:177], v[176:177], v[196:199]\n\t"
            "v_mfma_f32_4x4x4_16b_bf16 v[192:195], v[238:239], v[176:177], v[192:195]\n\t"
            "s_nop 0\n\t"
            "v_mfma_f32_4x4x4_16b_bf16 v[196:199], v[178:179], v[178:179], v[196:199]\n\t"
            "v_mfma_f32_4x4x4_16b_bf16 v[192:195], v[238:239], v[178:179], v[192:195]\n\t"
            "ds_read_b64 v[148:149], v253 offset:1816\n\t"
            "v_mov_b64 v[176:177], 0\n\t"
            "v_mov_b64 v[178:179], 0\n\t"
            "v_cvt_pk_bf16_f32 v184, v184, v185\n\t"
            "v_cvt_pk_bf16_f32 v185, v186, v187\n\t"
            "v_cvt_pk_bf16_f32 v187, v190, v191\n\t"
            "v_cvt_pk_bf16_f32 v186, v188, v189\n\t"
            "v_mov_b64 v[190:191], 0\n\t"
            "v_mov_b64 v[188:189], 0\n\t"
            "ds_read_b64 v[144:145], v253 offset:72\n\t"
            "ds_read_b64 v[150:151], v253 offset:1912\n\t"
            "ds_read_b128 v[168:171], v254 offset:512\n\t"
            "ds_read_b64 v[158:159], v253 offset:1920\n\t"
            "ds_read_b64 v[156:157], v253 offset:1824\n\t"
            "ds_read_b64 v[154:155], v253 offset:176\n\t"
            "ds_read_b64 v[152:153], v253 offset:80\n\t"
            "s_waitcnt lgkmcnt(14)\n\t"
            "ds_write_b128 v237, v[184:187] offset:128\n\t"
            "s_waitcnt lgkmcnt(5)\n\t"
            "v_smfmac_f32_16x16x64_bf16 v[176:179], v[168:171], v[160:167], v255\n\t"
            "v_smfmac_f32_16x16x64_bf16 v[180:183], v[172:175], v[144:151], v255\n\t"
            "ds_read_b64 v[166:167], v253 offset:1928\n\t"
            "ds_read_b128 v[168:171], v254 offset:640\n\t"
            "v_mfma_f32_4x4x4_16b_bf16 v[196:199], v[184:185], v[184:185], v[196:199]\n\t"
            "v_mfma_f32_4x4x4_16b_bf16 v[192:195], v[238:239], v[184:185], v[192:195]\n\t"
            "s_nop 0\n\t"
            "v_mfma_f32_4x4x4_16b_bf16 v[196:199], v[186:187], v[186:187], v[196:199]\n\t"
            "v_mfma_f32_4x4x4_16b_bf16 v[192:195], v[238:239], v[186:187], v[192:195]\n\t"
            "ds_read_b64 v[160:161], v253 offset:88\n\t"
            "v_mov_b64 v[184:185], 0\n\t"
            "v_mov_b64 v[186:187], 0\n\t"
            "v_cvt_pk_bf16_f32 v176, v176, v177\n\t"
            "v_cvt_pk_bf16_f32 v177, v178, v179\n\t"
            "v_cvt_pk_bf16_f32 v179, v182, v183\n\t"
            "v_cvt_pk_bf16_f32 v178, v180, v181\n\t"
            "ds_read_b128 v[172:175], v254 offset:704\n\t"
            "ds_read_b64 v[162:163], v253 offset:184\n\t"
            "ds_read_b64 v[164:165], v253 offset:1832\n\t"
            "ds_read_b128 v[144:147], v232 offset:6144\n\t"
            "ds_read_b128 v[148:151], v233 offset:6144\n\t"
            "ds_read_b128 v[180:183], v233 offset:12288\n\t"
            "ds_write_b128 v236, v[176:179] offset:256\n\t"
            "s_waitcnt lgkmcnt(4)\n\t"
            "v_smfmac_f32_16x16x64_bf16 v[184:187], v[168:171], v[152:159], v255\n\t"
            "v_smfmac_f32_16x16x64_bf16 v[188:191], v[172:175], v[160:167], v255\n\t"
            "ds_read_b128 v[152:155], v232 offset:6272\n\t"
            "ds_read_b128 v[156:159], v233 offset:6272\n\t"
            "v_mfma_f32_4x4x4_16b_bf16 v[196:199], v[176:177], v[176:177], v[196:199]\n\t"
            "v_mfma_f32_4x4x4_16b_bf16 v[192:195], v[238:239], v[176:177], v[192:195]\n\t"
            "s_nop 0\n\t"
            "v_mfma_f32_4x4x4_16b_bf16 v[196:199], v[178:179], v[178:179], v[196:199]\n\t"
            "v_mfma_f32_4x4x4_16b_bf16 v[192:195], v[238:239], v[178:179], v[192:195]\n\t"
            "ds_read_b128 v[160:163], v232 offset:6400\n\t"
            "v_cvt_pk_bf16_f32 v184, v184, v185\n\t"
            "v_cvt_pk_bf16_f32 v185, v186, v187\n\t"
            "v_cvt_pk_bf16_f32 v186, v188, v189\n\t"
            "v_cvt_pk_bf16_f32 v187, v190, v191\n\t"
            "s_nop 1\n\t"
            "v_mfma_f32_4x4x4_16b_bf16 v[196:199], v[184:185], v[184:185], v[196:199]\n\t"
            "v_mfma_f32_4x4x4_16b_bf16 v[192:195], v[238:239], v[184:185], v[192:195]\n\t"
            "s_nop 0\n\t"
            "v_mfma_f32_4x4x4_16b_bf16 v[196:199], v[186:187], v[186:187], v[196:199]\n\t"
            "v_mfma_f32_4x4x4_16b_bf16 v[192:195], v[238:239], v[186:187], v[192:195]\n\t"
            "ds_read_b128 v[164:167], v233 offset:6400\n\t"
            "s_waitcnt lgkmcnt(7)\n\t"
            "v_mfma_f32_16x16x32_bf16 v[224:227], v[144:147], v[0:3], 0\n\t"
            "s_setprio 3\n\t"
            "ds_read_b128 v[176:179], v232 offset:12288\n\t"
            "s_waitcnt lgkmcnt(7)\n\t"
            "v_mfma_f32_16x16x32_bf16 v[224:227], v[148:151], v[4:7], v[224:227]\n\t"
            "s_waitcnt lgkmcnt(4)\n\t"
            "ds_read_b128 v[188:191], v233 offset:12416\n\t"
            "v_mfma_f32_16x16x32_bf16 v[224:227], v[152:155], v[8:11], v[224:227]\n\t"
            "s_waitcnt lgkmcnt(4)\n\t"
            "v_cndmask_b32_e64 v193, v196, v197, s[60:61]\n\t"
            "v_cndmask_b32_e64 v193, v193, v198, s[62:63]\n\t"
            "v_mfma_f32_16x16x32_bf16 v[224:227], v[156:159], v[12:15], v[224:227]\n\t"
            "ds_write_b128 v237, v[184:187] offset:256\n\t"
            "s_mov_b32 exec_hi, 0x3fffff\n\t"
            "s_add_u32 m0, s66, 0\n\t"
            "s_add_u32 s49, s79, 0\n\t"
            "buffer_load_dwordx4 v249, s[72:75], s49 offen lds\n\t"
            "s_mov_b32 exec_hi, -1\n\t"
            "s_mov_b32 exec_hi, 0x3fffff\n\t"
            "s_add_u32 m0, s66, 872\n\t"
            "s_add_u32 s49, s68, 196224\n\t"
            "buffer_load_dwordx4 v249, s[72:75], s49 offen lds\n\t"
            "s_mov_b32 exec_hi, -1\n\t"
            "s_waitcnt lgkmcnt(4)\n\t"
            "v_mfma_f32_16x16x32_bf16 v[224:227], v[160:163], v[16:19], v[224:227]\n\t"
            "s_waitcnt lgkmcnt(3)\n\t"
            "ds_read_b128 v[184:187], v232 offset:12416\n\t"
            "v_cndmask_b32_e64 v193, v193, v199, s[64:65]\n\t"
            "v_mfma_f32_16x16x32_bf16 v[224:227], v[164:167], v[20:23], v[224:227]\n\t"
            "ds_write_b64 v248, v[192:193] offset:18816\n\t"
            "s_mov_b32 exec_hi, 0x3fffff\n\t"
            "s_add_u32 m0, s66, 1744\n\t"
            "s_add_u32 s49, s68, 392448\n\t"
            "buffer_load_dwordx4 v249, s[72:75], s49 offen lds\n\t"
            "s_mov_b32 exec_hi, -1\n\t"
            "s_mov_b32 exec_hi, 0x3fffff\n\t"
            "s_add_u32 m0, s66, 2616\n\t"
            "s_add_u32 s49, s68, 588672\n\t"
            "buffer_load_dwordx4 v249, s[72:75], s49 offen lds\n\t"
            "s_mov_b32 exec_hi, -1\n\t"
            "v_mfma_f32_16x16x32_bf16 v[168:171], v[144:147], v[24:27], 0\n\t"
            "ds_read_b128 v[192:195], v232 offset:12544\n\t"
            "v_mul_f32 v226, v230, v226\n\t"
            "v_mfma_f32_16x16x32_bf16 v[168:171], v[148:151], v[28:31], v[168:171]\n\t"
            "ds_read_b128 v[196:199], v233 offset:12544\n\t"
            "v_mul_f32 v224, v228, v224\n\t"
            "v_mfma_f32_16x16x32_bf16 v[168:171], v[152:155], v[32:35], v[168:171]\n\t"
            "v_mul_f32 v225, v229, v225\n\t"
            "v_cvt_pk_bf16_f32 v202, v224, v225\n\t"
            "v_mfma_f32_16x16x32_bf16 v[168:171], v[156:159], v[36:39], v[168:171]\n\t"
            "v_mul_f32 v227, v231, v227\n\t"
            "v_cvt_pk_bf16_f32 v203, v226, v227\n\t"
            "v_mfma_f32_16x16x32_bf16 v[168:171], v[160:163], v[40:43], v[168:171]\n\t"
            "v_add_u32 v232, s67, v242\n\t"
            "v_xor_b32 v233, 64, v232\n\t"
            "v_mfma_f32_16x16x32_bf16 v[168:171], v[164:167], v[44:47], v[168:171]\n\t"
            "s_mov_b32 exec_hi, 0x3fffff\n\t"
            "s_add_u32 m0, s66, 3488\n\t"
            "s_add_u32 s49, s68, 784896\n\t"
            "buffer_load_dwordx4 v249, s[72:75], s49 offen lds\n\t"
            "s_mov_b32 exec_hi, -1\n\t"
            "s_mov_b32 exec_hi, 0x3fffff\n\t"
            "s_add_u32 m0, s66, 4360\n\t"
            "s_add_u32 s49, s68, 981120\n\t"
            "buffer_load_dwordx4 v249, s[72:75], s49 offen lds\n\t"
            "s_mov_b32 exec_hi, -1\n\t"
            "ds_write_b128 v243, v[200:203]\n\t"
            "v_mfma_f32_16x16x32_bf16 v[172:175], v[144:147], v[48:51], 0\n\t"
            "v_mul_f32 v170, v230, v170\n\t"
            "v_mul_f32 v168, v228, v168\n\t"
            "v_mfma_f32_16x16x32_bf16 v[172:175], v[148:151], v[52:55], v[172:175]\n\t"
            "v_mul_f32 v171, v231, v171\n\t"
            "v_cvt_pk_bf16_f32 v207, v170, v171\n\t"
            "v_mfma_f32_16x16x32_bf16 v[172:175], v[152:155], v[56:59], v[172:175]\n\t"
            "v_mul_f32 v169, v229, v169\n\t"
            "v_cvt_pk_bf16_f32 v206, v168, v169\n\t"
            "v_mfma_f32_16x16x32_bf16 v[172:175], v[156:159], v[60:63], v[172:175]\n\t"
            "v_mfma_f32_16x16x32_bf16 v[172:175], v[160:163], v[64:67], v[172:175]\n\t"
            "v_mfma_f32_16x16x32_bf16 v[172:175], v[164:167], v[68:71], v[172:175]\n\t"
            "s_mov_b32 exec_hi, 0x3fffff\n\t"
            "s_add_u32 m0, s66, 5232\n\t"
            "s_add_u32 s49, s68, 1177344\n\t"
            "buffer_load_dwordx4 v249, s[72:75], s49 offen lds\n\t"
            "s_mov_b32 exec_hi, -1\n\t"
            "s_mov_b32 exec_hi, 0x3fffff\n\t"
            "s_add_u32 m0, s66, 6104\n\t"
            "s_add_u32 s49, s68, 1373568\n\t"
            "buffer_load_dwordx4 v249, s[72:75], s49 offen lds\n\t"
            "s_mov_b32 exec_hi, -1\n\t"
            "ds_write_b128 v243, v[204:207] offset:1536\n\t"
            "v_mfma_f32_16x16x32_bf16 v[224:227], v[144:147], v[72:75], 0\n\t"
            "v_mul_f32 v174, v230, v174\n\t"
            "v_mul_f32 v175, v231, v175\n\t"
            "v_mfma_f32_16x16x32_bf16 v[224:227], v[148:151], v[76:79], v[224:227]\n\t"
            "v_mul_f32 v172, v228, v172\n\t"
            "v_mul_f32 v173, v229, v173\n\t"
            "v_mfma_f32_16x16x32_bf16 v[224:227], v[152:155], v[80:83], v[224:227]\n\t"
            "v_cvt_pk_bf16_f32 v210, v172, v173\n\t"
            "v_cvt_pk_bf16_f32 v211, v174, v175\n\t"
            "v_mfma_f32_16x16x32_bf16 v[224:227], v[156:159], v[84:87], v[224:227]\n\t"
            "v_mfma_f32_16x16x32_bf16 v[224:227], v[160:163], v[88:91], v[224:227]\n\t"
            "v_mfma_f32_16x16x32_bf16 v[224:227], v[164:167], v[92:95], v[224:227]\n\t"
            "s_mov_b32 exec_hi, 0x3fffff\n\t"
            "s_add_u32 m0, s66, 6976\n\t"
            "s_add_u32 s49, s68, 1569792\n\t"
            "buffer_load_dwordx4 v249, s[72:75], s49 offen lds\n\t"
            "s_mov_b32 exec_hi, -1\n\t"
            "ds_write_b128 v243, v[208:211] offset:3072\n\t"
            "v_mfma_f32_16x16x32_bf16 v[168:171], v[144:147], v[96:99], 0\n\t"
            "s_add_u32 s45, s46, s27\n\t"
            "s_mov_b32 s46, s47\n\t"
            "s_mov_b32 s47, s68\n\t"
            "v_mfma_f32_16x16x32_bf16 v[168:171], v[148:151], v[100:103], v[168:171]\n\t"
            "s_xor_b32 s48, s48, s71\n\t"
            "s_xor_b32 s67, s67, s71\n\t"
            "s_add_u32 s76, s76, 4\n\t"
            "v_mfma_f32_16x16x32_bf16 v[168:171], v[152:155], v[104:107], v[168:171]\n\t"
            "s_lshl_b32 s56, s56, 2\n\t"
            "s_lshr_b32 s49, s26, 30\n\t"
            "s_or_b32 s56, s56, s49\n\t"
            "v_mfma_f32_16x16x32_bf16 v[168:171], v[156:159], v[108:111], v[168:171]\n\t"
            "v_mul_f32 v226, v230, v226\n\t"
            "v_mul_f32 v227, v231, v227\n\t"
            "v_mfma_f32_16x16x32_bf16 v[168:171], v[160:163], v[112:115], v[168:171]\n\t"
            "v_mul_f32 v225, v229, v225\n\t"
            "v_mul_f32 v224, v228, v224\n\t"
            "v_mfma_f32_16x16x32_bf16 v[168:171], v[164:167], v[116:119], v[168:171]\n\t"
            "v_cvt_pk_bf16_f32 v214, v224, v225\n\t"
            "v_cvt_pk_bf16_f32 v215, v226, v227\n\t"
            "ds_write_b128 v243, v[212:215] offset:4608\n\t"
            "v_mfma_f32_16x16x32_bf16 v[172:175], v[144:147], v[120:123], 0\n\t"
            "v_mfma_f32_16x16x32_bf16 v[172:175], v[148:151], v[124:127], v[172:175]\n\t"
            "v_mfma_f32_16x16x32_bf16 v[172:175], v[152:155], v[128:131], v[172:175]\n\t"
            "s_nop 1\n\t"
            "v_mul_f32 v168, v228, v168\n\t"
            "v_mfma_f32_16x16x32_bf16 v[172:175], v[156:159], v[132:135], v[172:175]\n\t"
            "v_mul_f32 v170, v230, v170\n\t"
            "v_mul_f32 v169, v229, v169\n\t"
            "v_mfma_f32_16x16x32_bf16 v[172:175], v[160:163], v[136:139], v[172:175]\n\t"
            "v_mul_f32 v171, v231, v171\n\t"
            "v_cvt_pk_bf16_f32 v219, v170, v171\n\t"
            "v_mfma_f32_16x16x32_bf16 v[172:175], v[164:167], v[140:143], v[172:175]\n\t"
            "v_cvt_pk_bf16_f32 v218, v168, v169\n\t"
            "ds_write_b128 v243, v[216:219] offset:6144\n\t"
            "s_waitcnt lgkmcnt(11)\n\t"
            "v_mfma_f32_16x16x32_bf16 v[224:227], v[176:179], v[0:3], 0\n\t"
            "v_mfma_f32_16x16x32_bf16 v[224:227], v[180:183], v[4:7], v[224:227]\n\t"
            "s_waitcnt lgkmcnt(8)\n\t"
            "v_mfma_f32_16x16x32_bf16 v[224:227], v[184:187], v[8:11], v[224:227]\n\t"
            "v_mfma_f32_16x16x32_bf16 v[224:227], v[188:191], v[12:15], v[224:227]\n\t"
            "s_waitcnt lgkmcnt(6)\n\t"
            "v_mul_f32 v174, v230, v174\n\t"
            "v_mfma_f32_16x16x32_bf16 v[224:227], v[192:195], v[16:19], v[224:227]\n\t"
            "s_waitcnt lgkmcnt(5)\n\t"
            "v_mul_f32 v175, v231, v175\n\t"
            "v_mul_f32 v172, v228, v172\n\t"
            "v_mfma_f32_16x16x32_bf16 v[224:227], v[196:199], v[20:23], v[224:227]\n\t"
            "v_mul_f32 v173, v229, v173\n\t"
            "v_cvt_pk_bf16_f32 v223, v174, v175\n\t"
            "v_mfma_f32_16x16x32_bf16 v[168:171], v[176:179], v[24:27], 0\n\t"
            "ds_read_b128 v[228:231], v245 offset:128\n\t"
            "v_cvt_pk_bf16_f32 v222, v172, v173\n\t"
            "v_mfma_f32_16x16x32_bf16 v[168:171], v[180:183], v[28:31], v[168:171]\n\t"
            "v_mfma_f32_16x16x32_bf16 v[168:171], v[184:187], v[32:35], v[168:171]\n\t"
            "v_mfma_f32_16x16x32_bf16 v[168:171], v[188:191], v[36:39], v[168:171]\n\t"
            "v_mfma_f32_16x16x32_bf16 v[168:171], v[192:195], v[40:43], v[168:171]\n\t"
            "v_mfma_f32_16x16x32_bf16 v[168:171], v[196:199], v[44:47], v[168:171]\n\t"
            "ds_write_b128 v243, v[220:223] offset:7680\n\t"
            "v_mfma_f32_16x16x32_bf16 v[172:175], v[176:179], v[48:51], 0\n\t"
            "v_mfma_f32_16x16x32_bf16 v[172:175], v[180:183], v[52:55], v[172:175]\n\t"
            "s_waitcnt lgkmcnt(1)\n\t"
            "v_mul_f32 v224, v228, v224\n\t"
            "v_mfma_f32_16x16x32_bf16 v[172:175], v[184:187], v[56:59], v[172:175]\n\t"
            "v_mul_f32 v226, v230, v226\n\t"
            "v_mul_f32 v225, v229, v225\n\t"
            "v_mfma_f32_16x16x32_bf16 v[172:175], v[188:191], v[60:63], v[172:175]\n\t"
            "v_mul_f32 v227, v231, v227\n\t"
            "v_cvt_pk_bf16_f32 v224, v224, v225\n\t"
            "v_mfma_f32_16x16x32_bf16 v[172:175], v[192:195], v[64:67], v[172:175]\n\t"
            "v_cvt_pk_bf16_f32 v225, v226, v227\n\t"
            "v_mul_f32 v169, v229, v169\n\t"
            "v_mfma_f32_16x16x32_bf16 v[172:175], v[196:199], v[68:71], v[172:175]\n\t"
            "ds_write_b64 v244, v[224:225]\n\t"
            "v_mfma_f32_16x16x32_bf16 v[224:227], v[176:179], v[72:75], 0\n\t"
            "v_mul_f32 v171, v231, v171\n\t"
            "v_mul_f32 v168, v228, v168\n\t"
            "v_mfma_f32_16x16x32_bf16 v[224:227], v[180:183], v[76:79], v[224:227]\n\t"
            "v_cvt_pk_bf16_f32 v168, v168, v169\n\t"
            "v_mul_f32 v170, v230, v170\n\t"
            "v_mfma_f32_16x16x32_bf16 v[224:227], v[184:187], v[80:83], v[224:227]\n\t"
            "v_cvt_pk_bf16_f32 v169, v170, v171\n\t"
            "v_mul_f32 v173, v229, v173\n\t"
            "v_mfma_f32_16x16x32_bf16 v[224:227], v[188:191], v[84:87], v[224:227]\n\t"
            "v_mul_f32 v175, v231, v175\n\t"
            "v_mul_f32 v174, v230, v174\n\t"
            "v_mfma_f32_16x16x32_bf16 v[224:227], v[192:195], v[88:91], v[224:227]\n\t"
            "v_mul_f32 v172, v228, v172\n\t"
            "v_cvt_pk_bf16_f32 v172, v172, v173\n\t"
            "v_mfma_f32_16x16x32_bf16 v[224:227], v[196:199], v[92:95], v[224:227]\n\t"
            "ds_write_b64 v244, v[168:169] offset:1536\n\t"
            "v_mfma_f32_16x16x32_bf16 v[168:171], v[176:179], v[96:99], 0\n\t"
            "v_cvt_pk_bf16_f32 v173, v174, v175\n\t"
            "v_mfma_f32_16x16x32_bf16 v[168:171], v[180:183], v[100:103], v[168:171]\n\t"
            "v_mfma_f32_16x16x32_bf16 v[168:171], v[184:187], v[104:107], v[168:171]\n\t"
            "v_mfma_f32_16x16x32_bf16 v[168:171], v[188:191], v[108:111], v[168:171]\n\t"
            "s_nop 1\n\t"
            "v_mul_f32 v225, v229, v225\n\t"
            "v_mfma_f32_16x16x32_bf16 v[168:171], v[192:195], v[112:115], v[168:171]\n\t"
            "v_mul_f32 v226, v230, v226\n\t"
            "v_mul_f32 v224, v228, v224\n\t"
            "v_mfma_f32_16x16x32_bf16 v[168:171], v[196:199], v[116:119], v[168:171]\n\t"
            "ds_write_b64 v244, v[172:173] offset:3072\n\t"
            "v_mfma_f32_16x16x32_bf16 v[172:175], v[176:179], v[120:123], 0\n\t"
            "v_mul_f32 v227, v231, v227\n\t"
            "v_cvt_pk_bf16_f32 v224, v224, v225\n\t"
            "v_mfma_f32_16x16x32_bf16 v[172:175], v[180:183], v[124:127], v[172:175]\n\t"
            "v_cvt_pk_bf16_f32 v225, v226, v227\n\t"
            "v_mfma_f32_16x16x32_bf16 v[172:175], v[184:187], v[128:131], v[172:175]\n\t"
            "s_nop 0\n\t"
            "v_mul_f32 v168, v228, v168\n\t"
            "v_mul_f32 v169, v229, v169\n\t"
            "v_mfma_f32_16x16x32_bf16 v[172:175], v[188:191], v[132:135], v[172:175]\n\t"
            "v_mul_f32 v171, v231, v171\n\t"
            "v_cvt_pk_bf16_f32 v168, v168, v169\n\t"
            "v_mfma_f32_16x16x32_bf16 v[172:175], v[192:195], v[136:139], v[172:175]\n\t"
            "v_mul_f32 v170, v230, v170\n\t"
            "v_cvt_pk_bf16_f32 v169, v170, v171\n\t"
            "v_mfma_f32_16x16x32_bf16 v[172:175], v[196:199], v[140:143], v[172:175]\n\t"
            "ds_write_b64 v244, v[224:225] offset:4608\n\t"
            "ds_write_b64 v244, v[168:169] offset:6144\n\t"
            "ds_read_b128 v[168:171], v254\n\t"
            "s_nop 4\n\t"
            "v_mul_f32 v172, v228, v172\n\t"
            "v_mul_f32 v173, v229, v173\n\t"
            "v_cvt_pk_bf16_f32 v172, v172, v173\n\t"
            "v_mul_f32 v174, v230, v174\n\t"
            "v_mul_f32 v175, v231, v175\n\t"
            "v_cvt_pk_bf16_f32 v173, v174, v175\n\t"
            "ds_write_b64 v244, v[172:173] offset:7680\n\t"
            "ds_read_b128 v[172:175], v254 offset:64\n\t"
            "s_waitcnt lgkmcnt(1)\n\t"
            "s_cmp_lt_u32 s46, 0x7000000\n\t"
            "s_barrier\n\t"
            "s_cbranch_scc0 .Lvx%=\n\t"
            "ds_read_b128 v[144:147], v232\n\t"
            "ds_read_b128 v[148:151], v233\n\t"
            "ds_read_b128 v[152:155], v232 offset:128\n\t"
            "ds_read_b128 v[156:159], v233 offset:128\n\t"
            "ds_read_b128 v[160:163], v232 offset:256\n\t"
            "ds_read_b128 v[164:167], v233 offset:256\n\t"
            "ds_read_b128 v[180:183], v234 offset:19840\n\t"
            "ds_read_b128 v[176:179], v234 offset:18816\n\t"
            "s_waitcnt lgkmcnt(7)\n\t"
            "v_mfma_f32_16x16x32_bf16 v[200:203], v[144:147], v[0:3], 0\n\t"
            "s_waitcnt lgkmcnt(6)\n\t"
            "v_mfma_f32_16x16x32_bf16 v[200:203], v[148:151], v[4:7], v[200:203]\n\t"
            "s_waitcnt lgkmcnt(5)\n\t"
            "v_mfma_f32_16x16x32_bf16 v[200:203], v[152:155], v[8:11], v[200:203]\n\t"
            "s_waitcnt lgkmcnt(4)\n\t"
            "v_mfma_f32_16x16x32_bf16 v[200:203], v[156:159], v[12:15], v[200:203]\n\t"
            "s_waitcnt lgkmcnt(1)\n\t"
            "v_add_f32 v181, v181, v183\n\t"
            "s_waitcnt lgkmcnt(0)\n\t"
            "v_add_f32 v176, v176, v178\n\t"
            "v_mfma_f32_16x16x32_bf16 v[200:203], v[160:163], v[16:19], v[200:203]\n\t"
            "v_add_f32 v177, v177, v179\n\t"
            "v_add_f32 v180, v180, v182\n\t"
            "v_mfma_f32_16x16x32_bf16 v[200:203], v[164:167], v[20:23], v[200:203]\n\t"
            "v_add_f32 v176, v176, v180\n\t"
            "v_mul_f32 v176, 0x3baaaaab, v176\n\t"
            "v_mfma_f32_16x16x32_bf16 v[204:207], v[144:147], v[24:27], 0\n\t"
            "v_mul_f32_e64 v176, v176, -v176\n\t"
            "v_add_f32 v177, v177, v181\n\t"
            "v_mfma_f32_16x16x32_bf16 v[204:207], v[148:151], v[28:31], v[204:207]\n\t"
            "v_fmac_f32 v176, 0x3baaaaab, v177\n\t"
            "v_max_f32 v176, 0, v176\n\t"
            "v_mfma_f32_16x16x32_bf16 v[204:207], v[152:155], v[32:35], v[204:207]\n\t"
            "v_add_f32 v176, 0x3727c5ac, v176\n\t"
            "v_mfma_f32_16x16x32_bf16 v[204:207], v[156:159], v[36:39], v[204:207]\n\t"
            "v_rsq_f32 v176, v176\n\t"
            "v_mfma_f32_16x16x32_bf16 v[204:207], v[160:163], v[40:43], v[204:207]\n\t"
            "ds_write_b32 v246, v176\n\t"
            "v_mfma_f32_16x16x32_bf16 v[204:207], v[164:167], v[44:47], v[204:207]\n\t"
            "ds_read_b128 v[228:231], v245\n\t"
            "v_mfma_f32_16x16x32_bf16 v[208:211], v[144:147], v[48:51], 0\n\t"
            "v_mfma_f32_16x16x32_bf16 v[208:211], v[148:151], v[52:55], v[208:211]\n\t"
            "v_mfma_f32_16x16x32_bf16 v[208:211], v[152:155], v[56:59], v[208:211]\n\t"
            "v_mfma_f32_16x16x32_bf16 v[208:211], v[156:159], v[60:63], v[208:211]\n\t"
            "v_mfma_f32_16x16x32_bf16 v[208:211], v[160:163], v[64:67], v[208:211]\n\t"
            "v_mfma_f32_16x16x32_bf16 v[208:211], v[164:167], v[68:71], v[208:211]\n\t"
            "v_mfma_f32_16x16x32_bf16 v[212:215], v[144:147], v[72:75], 0\n\t"
            "v_mfma_f32_16x16x32_bf16 v[212:215], v[148:151], v[76:79], v[212:215]\n\t"
            "s_waitcnt lgkmcnt(0)\n\t"
            "v_mul_f32 v201, v229, v201\n\t"
            "v_mul_f32 v205, v229, v205\n\t"
            "v_mfma_f32_16x16x32_bf16 v[212:215], v[152:155], v[80:83], v[212:215]\n\t"
            "v_mul_f32 v206, v230, v206\n\t"
            "v_mul_f32 v203, v231, v203\n\t"
            "v_mfma_f32_16x16x32_bf16 v[212:215], v[156:159], v[84:87], v[212:215]\n\t"
            "v_mul_f32 v211, v231, v211\n\t"
            "v_mul_f32 v208, v228, v208\n\t"
            "v_mfma_f32_16x16x32_bf16 v[212:215], v[160:163], v[88:91], v[212:215]\n\t"
            "v_mul_f32 v209, v229, v209\n\t"
            "v_cvt_pk_bf16_f32 v208, v208, v209\n\t"
            "v_mfma_f32_16x16x32_bf16 v[212:215], v[164:167], v[92:95], v[212:215]\n\t"
            "v_mul_f32 v204, v228, v204\n\t"
            "v_mul_f32 v207, v231, v207\n\t"
            "v_mfma_f32_16x16x32_bf16 v[216:219], v[144:147], v[96:99], 0\n\t"
            "v_mul_f32 v200, v228, v200\n\t"
            "v_cvt_pk_bf16_f32 v200, v200, v201\n\t"
            "v_mfma_f32_16x16x32_bf16 v[216:219], v[148:151], v[100:103], v[216:219]\n\t"
            "v_cvt_pk_bf16_f32 v204, v204, v205\n\t"
            "v_cvt_pk_bf16_f32 v205, v206, v207\n\t"
            "v_mfma_f32_16x16x32_bf16 v[216:219], v[152:155], v[104:107], v[216:219]\n\t"
            "v_mul_f32 v214, v230, v214\n\t"
            "v_mul_f32 v213, v229, v213\n\t"
            "v_mfma_f32_16x16x32_bf16 v[216:219], v[156:159], v[108:111], v[216:219]\n\t"
            "v_mul_f32 v212, v228, v212\n\t"
            "v_mul_f32 v215, v231, v215\n\t"
            "v_mfma_f32_16x16x32_bf16 v[216:219], v[160:163], v[112:115], v[216:219]\n\t"
            "v_mul_f32 v202, v230, v202\n\t"
            "v_cvt_pk_bf16_f32 v201, v202, v203\n\t"
            "v_mfma_f32_16x16x32_bf16 v[216:219], v[164:167], v[116:119], v[216:219]\n\t"
            "s_waitcnt vmcnt(0)\n\t"
            "v_mul_f32 v210, v230, v210\n\t"
            "v_mfma_f32_16x16x32_bf16 v[220:223], v[144:147], v[120:123], 0\n\t"
            "ds_read_b64 v[144:145], v253\n\t"
            "v_cvt_pk_bf16_f32 v209, v210, v211\n\t"
            "v_mfma_f32_16x16x32_bf16 v[220:223], v[148:151], v[124:127], v[220:223]\n\t"
            "ds_read_b64 v[150:151], v253 offset:1840\n\t"
            "v_cvt_pk_bf16_f32 v212, v212, v213\n\t"
            "v_mfma_f32_16x16x32_bf16 v[220:223], v[152:155], v[128:131], v[220:223]\n\t"
            "ds_read_b64 v[152:153], v253 offset:8\n\t"
            "v_mul_f32 v216, v228, v216\n\t"
            "v_mfma_f32_16x16x32_bf16 v[220:223], v[156:159], v[132:135], v[220:223]\n\t"
            "ds_read_b64 v[148:149], v253 offset:1744\n\t"
            "v_mul_f32 v217, v229, v217\n\t"
            "v_mfma_f32_16x16x32_bf16 v[220:223], v[160:163], v[136:139], v[220:223]\n\t"
            "ds_read_b64 v[146:147], v253 offset:96\n\t"
            "v_cvt_pk_bf16_f32 v216, v216, v217\n\t"
            "v_mfma_f32_16x16x32_bf16 v[220:223], v[164:167], v[140:143], v[220:223]\n\t"
            "s_setprio 0\n\t"
            "ds_read_b64 v[164:165], v253 offset:1760\n\t"
            "v_cvt_pk_bf16_f32 v213, v214, v215\n\t"
            "v_mul_f32 v219, v231, v219\n\t"
            "v_mul_f32 v218, v230, v218\n\t"
            "v_cvt_pk_bf16_f32 v217, v218, v219\n\t"
            "ds_read_b64 v[154:155], v253 offset:104\n\t"
            "ds_read_b64 v[162:163], v253 offset:112\n\t"
            "ds_read_b64 v[158:159], v253 offset:1848\n\t"
            "v_mul_f32 v220, v228, v220\n\t"
            "v_mul_f32 v223, v231, v223\n\t"
            "v_mul_f32 v222, v230, v222\n\t"
            "v_mul_f32 v221, v229, v221\n\t"
            "v_cvt_pk_bf16_f32 v220, v220, v221\n\t"
            "v_cvt_pk_bf16_f32 v221, v222, v223\n\t"
            "ds_read_b64 v[160:161], v253 offset:16\n\t"
            "ds_read_b64 v[156:157], v253 offset:1752\n\t"
            "ds_read_b64 v[166:167], v253 offset:1856\n\t"
            "s_branch .Lwc_loop%=\n\t"
            ".Lvx%=:\n\t"
            "s_waitcnt lgkmcnt(0)\n\t"
            "s_nop 7\n\t"
            "s_setprio 3\n\t"
            "v_add_u32 v236, s19, v252\n\t"
            "ds_read_b128 v[244:247], v236\n\t"
            "s_add_u32 s70, s33, 73728\n\t"
            "s_waitcnt lgkmcnt(0)\n\t"
            "buffer_load_dwordx4 v[200:203], v244, s[28:31], s21 offen\n\t"
            "buffer_load_dwordx4 v[204:207], v245, s[28:31], s21 offen\n\t"
            "buffer_load_dwordx4 v[208:211], v246, s[28:31], s21 offen\n\t"
            "s_add_u32 s21, s21, 6144\n\t"
            "buffer_load_dwordx4 v[212:215], v244, s[28:31], s21 offen\n\t"
            "buffer_load_dwordx4 v[216:219], v245, s[28:31], s21 offen\n\t"
            "buffer_load_dwordx4 v[220:223], v246, s[28:31], s21 offen\n\t"
            "s_nop 7\n\t"
            "s_add_u32 s49, s70, 0\n\t"
            "buffer_load_dwordx4 v[0:3], v247, s[28:31], s49 offen sc1\n\t"
            "s_add_u32 s49, s70, 1024\n\t"
            "buffer_load_dwordx4 v[4:7], v247, s[28:31], s49 offen sc1\n\t"
            "s_add_u32 s49, s70, 2048\n\t"
            "buffer_load_dwordx4 v[8:11], v247, s[28:31], s49 offen sc1\n\t"
            "s_add_u32 s49, s70, 3072\n\t"
            "buffer_load_dwordx4 v[12:15], v247, s[28:31], s49 offen sc1\n\t"
            "s_add_u32 s49, s70, 4096\n\t"
            "buffer_load_dwordx4 v[16:19], v247, s[28:31], s49 offen sc1\n\t"
            "s_add_u32 s49, s70, 5120\n\t"
            "buffer_load_dwordx4 v[20:23], v247, s[28:31], s49 offen sc1\n\t"
            "s_add_u32 s49, s70, 6144\n\t"
            "buffer_load_dwordx4 v[24:27], v247, s[28:31], s49 offen sc1\n\t"
            "s_add_u32 s49, s70, 7168\n\t"
            "buffer_load_dwordx4 v[28:31], v247, s[28:31], s49 offen sc1\n\t"
            "s_add_u32 s49, s70, 8192\n\t"
            "buffer_load_dwordx4 v[32:35], v247, s[28:31], s49 offen sc1\n\t"
            "s_add_u32 s49, s70, 9216\n\t"
            "buffer_load_dwordx4 v[36:39], v247, s[28:31], s49 offen sc1\n\t"
            "s_add_u32 s49, s70, 10240\n\t"
            "buffer_load_dwordx4 v[40:43], v247, s[28:31], s49 offen sc1\n\t"
            "s_add_u32 s49, s70, 11264\n\t"
            "buffer_load_dwordx4 v[44:47], v247, s[28:31], s49 offen sc1\n\t"
            "s_add_u32 s49, s70, 12288\n\t"
            "buffer_load_dwordx4 v[48:51], v247, s[28:31], s49 offen sc1\n\t"
            "s_add_u32 s49, s70, 13312\n\t"
            "buffer_load_dwordx4 v[52:55], v247, s[28:31], s49 offen sc1\n\t"
            "s_add_u32 s49, s70, 14336\n\t"
            "buffer_load_dwordx4 v[56:59], v247, s[28:31], s49 offen sc1\n\t"
            "s_add_u32 s49, s70, 15360\n\t"
            "buffer_load_dwordx4 v[60:63], v247, s[28:31], s49 offen sc1\n\t"
            "s_add_u32 s49, s70, 16384\n\t"
            "buffer_load_dwordx4 v[64:67], v247, s[28:31], s49 offen sc1\n\t"
            "s_add_u32 s49, s70, 17408\n\t"
            "buffer_load_dwordx4 v[68:71], v247, s[28:31], s49 offen sc1\n\t"
            "s_add_u32 s49, s70, 18432\n\t"
            "buffer_load_dwordx4 v[72:75], v247, s[28:31], s49 offen sc1\n\t"
            "s_add_u32 s49, s70, 19456\n\t"
            "buffer_load_dwordx4 v[76:79], v247, s[28:31], s49 offen sc1\n\t"
            "s_add_u32 s49, s70, 20480\n\t"
            "buffer_load_dwordx4 v[80:83], v247, s[28:31], s49 offen sc1\n\t"
            "s_add_u32 s49, s70, 21504\n\t"
            "buffer_load_dwordx4 v[84:87], v247, s[28:31], s49 offen sc1\n\t"
            "s_add_u32 s49, s70, 22528\n\t"
            "buffer_load_dwordx4 v[88:91], v247, s[28:31], s49 offen sc1\n\t"
            "s_add_u32 s49, s70, 23552\n\t"
            "buffer_load_dwordx4 v[92:95], v247, s[28:31], s49 offen sc1\n\t"
            "s_waitcnt vmcnt(18)\n\t"
            "s_add_u32 s49, s70, 24576\n\t"
            "buffer_load_dwordx4 v[96:99], v247, s[28:31], s49 offen sc1\n\t"
            "s_add_u32 s49, s70, 25600\n\t"
            "buffer_load_dwordx4 v[100:103], v247, s[28:31], s49 offen sc1\n\t"
            "v_add_u32 v242, s22, v242\n\t"
            "v_xor_b32 v243, 64, v242\n\t"
            "v_lshlrev_b32 v192, 16, v200\n\t"
            "v_and_b32 v193, 0xffff0000, v200\n\t"
            "v_lshlrev_b32 v194, 16, v201\n\t"
            "v_and_b32 v195, 0xffff0000, v201\n\t"
            "v_lshlrev_b32 v196, 16, v202\n\t"
            "v_and_b32 v197, 0xffff0000, v202\n\t"
            "v_lshlrev_b32 v198, 16, v203\n\t"
            "v_and_b32 v199, 0xffff0000, v203\n\t"
            "v_bfe_u32 v248, v247, 2, 6\n\t"
            "v_add_u32 v248, s23, v248\n\t"
            "v_lshlrev_b32 v224, 16, v204\n\t"
            "v_and_b32 v225, 0xffff0000, v204\n\t"
            "v_lshlrev_b32 v226, 16, v205\n\t"
            "v_and_b32 v227, 0xffff0000, v205\n\t"
            "v_lshrrev_b32 v236, 4, v247\n\t"
            "v_and_b32 v236, 48, v236\n\t"
            "v_add_u32 v236, s20, v236\n\t"
            "v_bfe_u32 v249, v247, 2, 6\n\t"
            "v_lshlrev_b32 v228, 16, v206\n\t"
            "v_and_b32 v229, 0xffff0000, v206\n\t"
            "v_lshlrev_b32 v230, 16, v207\n\t"
            "v_and_b32 v231, 0xffff0000, v207\n\t"
            "v_add_u32 v249, s23, v249\n\t"
            "ds_read_b128 v[144:147], v242 offset:53760\n\t"
            "ds_read_b128 v[148:151], v243 offset:53760\n\t"
            "ds_read_b128 v[152:155], v242 offset:53888\n\t"
            "ds_read_b128 v[156:159], v243 offset:53888\n\t"
            "ds_read_b128 v[160:163], v242 offset:54016\n\t"
            "ds_read_b128 v[164:167], v243 offset:54016\n\t"
            "ds_read_b32 v248, v248\n\t"
            "ds_read_b32 v249, v249 offset:64\n\t"
            "s_add_u32 s49, s70, 26624\n\t"
            "buffer_load_dwordx4 v[104:107], v247, s[28:31], s49 offen sc1\n\t"
            "s_add_u32 s49, s70, 27648\n\t"
            "buffer_load_dwordx4 v[108:111], v247, s[28:31], s49 offen sc1\n\t"
            "s_waitcnt lgkmcnt(7)\n\t"
            "v_mfma_f32_16x16x32_bf16 v[192:195], v[0:3], v[144:147], v[192:195]\n\t"
            "s_add_u32 s49, s70, 28672\n\t"
            "buffer_load_dwordx4 v[112:115], v247, s[28:31], s49 offen sc1\n\t"
            "s_waitcnt lgkmcnt(6)\n\t"
            "v_mfma_f32_16x16x32_bf16 v[192:195], v[4:7], v[148:151], v[192:195]\n\t"
            "s_add_u32 s49, s70, 29696\n\t"
            "buffer_load_dwordx4 v[116:119], v247, s[28:31], s49 offen sc1\n\t"
            "s_waitcnt lgkmcnt(5)\n\t"
            "v_mfma_f32_16x16x32_bf16 v[192:195], v[8:11], v[152:155], v[192:195]\n\t"
            "s_waitcnt vmcnt(18)\n\t"
            "s_add_u32 s49, s70, 30720\n\t"
            "buffer_load_dwordx4 v[120:123], v247, s[28:31], s49 offen sc1\n\t"
            "s_waitcnt lgkmcnt(4)\n\t"
            "v_mfma_f32_16x16x32_bf16 v[192:195], v[12:15], v[156:159], v[192:195]\n\t"
            "s_add_u32 s49, s70, 31744\n\t"
            "buffer_load_dwordx4 v[124:127], v247, s[28:31], s49 offen sc1\n\t"
            "s_waitcnt lgkmcnt(3)\n\t"
            "v_mfma_f32_16x16x32_bf16 v[192:195], v[16:19], v[160:163], v[192:195]\n\t"
            "s_add_u32 s49, s70, 32768\n\t"
            "buffer_load_dwordx4 v[128:131], v247, s[28:31], s49 offen sc1\n\t"
            "s_waitcnt lgkmcnt(2)\n\t"
            "v_mfma_f32_16x16x32_bf16 v[192:195], v[20:23], v[164:167], v[192:195]\n\t"
            "s_add_u32 s49, s70, 33792\n\t"
            "buffer_load_dwordx4 v[132:135], v247, s[28:31], s49 offen sc1\n\t"
            "v_mfma_f32_16x16x32_bf16 v[196:199], v[24:27], v[144:147], v[196:199]\n\t"
            "s_add_u32 s49, s70, 34816\n\t"
            "buffer_load_dwordx4 v[136:139], v247, s[28:31], s49 offen sc1\n\t"
            "v_mfma_f32_16x16x32_bf16 v[196:199], v[28:31], v[148:151], v[196:199]\n\t"
            "s_add_u32 s49, s70, 35840\n\t"
            "buffer_load_dwordx4 v[140:143], v247, s[28:31], s49 offen sc1\n\t"
            "v_mfma_f32_16x16x32_bf16 v[196:199], v[32:35], v[152:155], v[196:199]\n\t"
            "s_waitcnt vmcnt(18)\n\t"
            "s_waitcnt lgkmcnt(1)\n\t"
            "v_ashrrev_i32 v237, 31, v248\n\t"
            "v_mfma_f32_16x16x32_bf16 v[196:199], v[36:39], v[156:159], v[196:199]\n\t"
            "v_and_b32 v237, 1177344, v237\n\t"
            "v_sub_u32 v236, v236, v237\n\t"
            "v_mfma_f32_16x16x32_bf16 v[196:199], v[40:43], v[160:163], v[196:199]\n\t"
            "v_bfe_i32 v237, v248, 30, 1\n\t"
            "v_and_b32 v237, 2304, v237\n\t"
            "v_mfma_f32_16x16x32_bf16 v[196:199], v[44:47], v[164:167], v[196:199]\n\t"
            "v_cvt_pk_bf16_f32 v192, v192, v193\n\t"
            "v_sub_u32 v236, v236, v237\n\t"
            "v_mfma_f32_16x16x32_bf16 v[224:227], v[48:51], v[144:147], v[224:227]\n\t"
            "v_and_b32 v248, 0x7ffffff, v248\n\t"
            "v_cvt_pk_bf16_f32 v193, v194, v195\n\t"
            "v_mfma_f32_16x16x32_bf16 v[224:227], v[52:55], v[148:151], v[224:227]\n\t"
            "v_add_u32 v248, v248, v236\n\t"
            "v_lshrrev_b32 v236, 4, v247\n\t"
            "v_mfma_f32_16x16x32_bf16 v[224:227], v[56:59], v[152:155], v[224:227]\n\t"
            "v_cvt_pk_bf16_f32 v194, v196, v197\n\t"
            "v_cvt_pk_bf16_f32 v195, v198, v199\n\t"
            "v_mfma_f32_16x16x32_bf16 v[224:227], v[60:63], v[156:159], v[224:227]\n\t"
            "buffer_store_dwordx4 v[192:195], v248, s[40:43], 0 offen sc0 sc1\n\t"
            "v_mfma_f32_16x16x32_bf16 v[224:227], v[64:67], v[160:163], v[224:227]\n\t"
            "s_waitcnt vmcnt(13)\n\t"
            "v_and_b32 v236, 48, v236\n\t"
            "s_waitcnt lgkmcnt(0)\n\t"
            "v_ashrrev_i32 v237, 31, v249\n\t"
            "v_mfma_f32_16x16x32_bf16 v[224:227], v[68:71], v[164:167], v[224:227]\n\t"
            "v_add_u32 v236, s20, v236\n\t"
            "v_and_b32 v237, 1177344, v237\n\t"
            "v_mfma_f32_16x16x32_bf16 v[228:231], v[72:75], v[144:147], v[228:231]\n\t"
            "v_sub_u32 v236, v236, v237\n\t"
            "v_bfe_i32 v237, v249, 30, 1\n\t"
            "v_mfma_f32_16x16x32_bf16 v[228:231], v[76:79], v[148:151], v[228:231]\n\t"
            "v_and_b32 v237, 2304, v237\n\t"
            "v_sub_u32 v236, v236, v237\n\t"
            "v_mfma_f32_16x16x32_bf16 v[228:231], v[80:83], v[152:155], v[228:231]\n\t"
            "v_cvt_pk_bf16_f32 v224, v224, v225\n\t"
            "v_cvt_pk_bf16_f32 v225, v226, v227\n\t"
            "v_mfma_f32_16x16x32_bf16 v[228:231], v[84:87], v[156:159], v[228:231]\n\t"
            "v_and_b32 v249, 0x7ffffff, v249\n\t"
            "v_add_u32 v249, v249, v236\n\t"
            "v_mfma_f32_16x16x32_bf16 v[228:231], v[88:91], v[160:163], v[228:231]\n\t"
            "v_mfma_f32_16x16x32_bf16 v[228:231], v[92:95], v[164:167], v[228:231]\n\t"
            "s_nop 7\n\t"
            "v_cvt_pk_bf16_f32 v226, v228, v229\n\t"
            "v_cvt_pk_bf16_f32 v227, v230, v231\n\t"
            "buffer_store_dwordx4 v[224:227], v248, s[40:43], 0 offen offset:64 sc0 sc1\n\t"
            "s_nop 7\n\t"
            "v_mov_b32 v168, s23\n\t"
            ".Ldv%=:\n\t"
            "ds_read_b128 v[172:175], v168 offset:96\n\t"
            "s_waitcnt lgkmcnt(0)\n\t"
            "v_or_b32 v172, v172, v173\n\t"
            "v_or3_b32 v172, v172, v174, v175\n\t"
            "v_cmp_gt_i32 vcc, 0, v172\n\t"
            "s_nop 4\n\t"
            "s_cbranch_vccnz .Ldv%=\n\t"
            "s_nop 7\n\t"
            "ds_read_b128 v[168:171], v242 offset:59904\n\t"
            "v_lshlrev_b32 v232, 16, v212\n\t"
            "v_and_b32 v233, 0xffff0000, v212\n\t"
            "v_lshlrev_b32 v234, 16, v213\n\t"
            "v_and_b32 v235, 0xffff0000, v213\n\t"
            "s_waitcnt vmcnt(32)\n\t"
            "v_lshlrev_b32 v192, 16, v214\n\t"
            "v_and_b32 v193, 0xffff0000, v214\n\t"
            "v_lshlrev_b32 v194, 16, v215\n\t"
            "v_and_b32 v195, 0xffff0000, v215\n\t"
            "v_lshlrev_b32 v196, 16, v216\n\t"
            "v_and_b32 v197, 0xffff0000, v216\n\t"
            "v_lshlrev_b32 v198, 16, v217\n\t"
            "v_and_b32 v199, 0xffff0000, v217\n\t"
            "v_lshlrev_b32 v224, 16, v218\n\t"
            "v_and_b32 v225, 0xffff0000, v218\n\t"
            "v_lshlrev_b32 v226, 16, v219\n\t"
            "v_and_b32 v227, 0xffff0000, v219\n\t"
            "v_lshlrev_b32 v228, 16, v208\n\t"
            "v_and_b32 v229, 0xffff0000, v208\n\t"
            "v_lshlrev_b32 v230, 16, v209\n\t"
            "v_and_b32 v231, 0xffff0000, v209\n\t"
            "ds_read_b128 v[172:175], v243 offset:59904\n\t"
            "ds_read_b128 v[176:179], v242 offset:60032\n\t"
            "ds_read_b128 v[180:183], v243 offset:60032\n\t"
            "ds_read_b128 v[184:187], v242 offset:60160\n\t"
            "s_waitcnt lgkmcnt(4)\n\t"
            "v_mfma_f32_16x16x32_bf16 v[232:235], v[0:3], v[168:171], v[232:235]\n\t"
            "s_waitcnt lgkmcnt(3)\n\t"
            "ds_read_b128 v[188:191], v243 offset:60160\n\t"
            "v_mfma_f32_16x16x32_bf16 v[232:235], v[4:7], v[172:175], v[232:235]\n\t"
            "s_waitcnt lgkmcnt(3)\n\t"
            "v_mfma_f32_16x16x32_bf16 v[232:235], v[8:11], v[176:179], v[232:235]\n\t"
            "s_waitcnt vmcnt(26)\n\t"
            "s_waitcnt lgkmcnt(2)\n\t"
            "v_mfma_f32_16x16x32_bf16 v[232:235], v[12:15], v[180:183], v[232:235]\n\t"
            "s_waitcnt lgkmcnt(1)\n\t"
            "v_mfma_f32_16x16x32_bf16 v[232:235], v[16:19], v[184:187], v[232:235]\n\t"
            "s_waitcnt lgkmcnt(0)\n\t"
            "v_mfma_f32_16x16x32_bf16 v[232:235], v[20:23], v[188:191], v[232:235]\n\t"
            "v_mfma_f32_16x16x32_bf16 v[192:195], v[24:27], v[168:171], v[192:195]\n\t"
            "v_mfma_f32_16x16x32_bf16 v[192:195], v[28:31], v[172:175], v[192:195]\n\t"
            "v_mfma_f32_16x16x32_bf16 v[192:195], v[32:35], v[176:179], v[192:195]\n\t"
            "s_waitcnt vmcnt(20)\n\t"
            "v_mfma_f32_16x16x32_bf16 v[192:195], v[36:39], v[180:183], v[192:195]\n\t"
            "v_mfma_f32_16x16x32_bf16 v[192:195], v[40:43], v[184:187], v[192:195]\n\t"
            "s_nop 1\n\t"
            "v_cvt_pk_bf16_f32 v232, v232, v233\n\t"
            "v_mfma_f32_16x16x32_bf16 v[192:195], v[44:47], v[188:191], v[192:195]\n\t"
            "v_cvt_pk_bf16_f32 v233, v234, v235\n\t"
            "v_mfma_f32_16x16x32_bf16 v[196:199], v[48:51], v[168:171], v[196:199]\n\t"
            "v_mfma_f32_16x16x32_bf16 v[196:199], v[52:55], v[172:175], v[196:199]\n\t"
            "v_mfma_f32_16x16x32_bf16 v[196:199], v[56:59], v[176:179], v[196:199]\n\t"
            "v_mfma_f32_16x16x32_bf16 v[196:199], v[60:63], v[180:183], v[196:199]\n\t"
            "v_mfma_f32_16x16x32_bf16 v[196:199], v[64:67], v[184:187], v[196:199]\n\t"
            "s_nop 1\n\t"
            "v_cvt_pk_bf16_f32 v234, v192, v193\n\t"
            "v_mfma_f32_16x16x32_bf16 v[196:199], v[68:71], v[188:191], v[196:199]\n\t"
            "v_cvt_pk_bf16_f32 v235, v194, v195\n\t"
            "v_lshlrev_b32 v192, 16, v220\n\t"
            "v_and_b32 v193, 0xffff0000, v220\n\t"
            "v_lshlrev_b32 v194, 16, v221\n\t"
            "v_and_b32 v195, 0xffff0000, v221\n\t"
            "buffer_store_dwordx4 v[232:235], v249, s[40:43], 0 offen sc0 sc1\n\t"
            "s_waitcnt vmcnt(15)\n\t"
            "v_mfma_f32_16x16x32_bf16 v[224:227], v[72:75], v[168:171], v[224:227]\n\t"
            "v_cvt_pk_bf16_f32 v196, v196, v197\n\t"
            "v_cvt_pk_bf16_f32 v197, v198, v199\n\t"
            "v_mfma_f32_16x16x32_bf16 v[224:227], v[76:79], v[172:175], v[224:227]\n\t"
            "v_lshlrev_b32 v232, 16, v210\n\t"
            "v_and_b32 v233, 0xffff0000, v210\n\t"
            "v_mfma_f32_16x16x32_bf16 v[224:227], v[80:83], v[176:179], v[224:227]\n\t"
            "s_waitcnt vmcnt(9)\n\t"
            "v_lshlrev_b32 v234, 16, v211\n\t"
            "v_mfma_f32_16x16x32_bf16 v[224:227], v[84:87], v[180:183], v[224:227]\n\t"
            "v_and_b32 v235, 0xffff0000, v211\n\t"
            "v_mfma_f32_16x16x32_bf16 v[224:227], v[88:91], v[184:187], v[224:227]\n\t"
            "v_mfma_f32_16x16x32_bf16 v[224:227], v[92:95], v[188:191], v[224:227]\n\t"
            "v_mfma_f32_16x16x32_bf16 v[228:231], v[96:99], v[144:147], v[228:231]\n\t"
            "v_mfma_f32_16x16x32_bf16 v[228:231], v[100:103], v[148:151], v[228:231]\n\t"
            "v_mfma_f32_16x16x32_bf16 v[228:231], v[104:107], v[152:155], v[228:231]\n\t"
            "v_mfma_f32_16x16x32_bf16 v[228:231], v[108:111], v[156:159], v[228:231]\n\t"
            "v_mfma_f32_16x16x32_bf16 v[228:231], v[112:115], v[160:163], v[228:231]\n\t"
            "v_mfma_f32_16x16x32_bf16 v[228:231], v[116:119], v[164:167], v[228:231]\n\t"
            "s_nop 1\n\t"
            "v_cvt_pk_bf16_f32 v198, v224, v225\n\t"
            "v_cvt_pk_bf16_f32 v199, v226, v227\n\t"
            "buffer_store_dwordx4 v[196:199], v249, s[40:43], 0 offen offset:64 sc0 sc1\n\t"
            "s_waitcnt vmcnt(4)\n\t"
            "v_mfma_f32_16x16x32_bf16 v[232:235], v[120:123], v[144:147], v[232:235]\n\t"
            "v_lshlrev_b32 v196, 16, v222\n\t"
            "v_and_b32 v197, 0xffff0000, v222\n\t"
            "v_mfma_f32_16x16x32_bf16 v[232:235], v[124:127], v[148:151], v[232:235]\n\t"
            "v_cvt_pk_bf16_f32 v228, v228, v229\n\t"
            "v_cvt_pk_bf16_f32 v229, v230, v231\n\t"
            "v_mfma_f32_16x16x32_bf16 v[232:235], v[128:131], v[152:155], v[232:235]\n\t"
            "s_waitcnt vmcnt(10)\n\t"
            "v_lshlrev_b32 v198, 16, v223\n\t"
            "v_mfma_f32_16x16x32_bf16 v[232:235], v[132:135], v[156:159], v[232:235]\n\t"
            "v_and_b32 v199, 0xffff0000, v223\n\t"
            "v_mfma_f32_16x16x32_bf16 v[232:235], v[136:139], v[160:163], v[232:235]\n\t"
            "v_mfma_f32_16x16x32_bf16 v[232:235], v[140:143], v[164:167], v[232:235]\n\t"
            "v_mfma_f32_16x16x32_bf16 v[192:195], v[96:99], v[168:171], v[192:195]\n\t"
            "v_mfma_f32_16x16x32_bf16 v[192:195], v[100:103], v[172:175], v[192:195]\n\t"
            "v_mfma_f32_16x16x32_bf16 v[192:195], v[104:107], v[176:179], v[192:195]\n\t"
            "v_mfma_f32_16x16x32_bf16 v[192:195], v[108:111], v[180:183], v[192:195]\n\t"
            "v_mfma_f32_16x16x32_bf16 v[192:195], v[112:115], v[184:187], v[192:195]\n\t"
            "v_mfma_f32_16x16x32_bf16 v[192:195], v[116:119], v[188:191], v[192:195]\n\t"
            "s_nop 1\n\t"
            "v_cvt_pk_bf16_f32 v230, v232, v233\n\t"
            "v_cvt_pk_bf16_f32 v231, v234, v235\n\t"
            "buffer_store_dwordx4 v[228:231], v248, s[40:43], 0 offen offset:128 sc0 sc1\n\t"
            "s_waitcnt vmcnt(5)\n\t"
            "v_mfma_f32_16x16x32_bf16 v[196:199], v[120:123], v[168:171], v[196:199]\n\t"
            "v_mfma_f32_16x16x32_bf16 v[196:199], v[124:127], v[172:175], v[196:199]\n\t"
            "v_cvt_pk_bf16_f32 v192, v192, v193\n\t"
            "v_mfma_f32_16x16x32_bf16 v[196:199], v[128:131], v[176:179], v[196:199]\n\t"
            "v_cvt_pk_bf16_f32 v193, v194, v195\n\t"
            "v_mfma_f32_16x16x32_bf16 v[196:199], v[132:135], v[180:183], v[196:199]\n\t"
            "v_mfma_f32_16x16x32_bf16 v[196:199], v[136:139], v[184:187], v[196:199]\n\t"
            "v_mfma_f32_16x16x32_bf16 v[196:199], v[140:143], v[188:191], v[196:199]\n\t"
            "s_nop 7\n\t"
            "v_cvt_pk_bf16_f32 v194, v196, v197\n\t"
            "v_cvt_pk_bf16_f32 v195, v198, v199\n\t"
            "buffer_store_dwordx4 v[192:195], v249, s[40:43], 0 offen offset:128 sc0 sc1\n\t"
            "s_nop 7\n\t"
            "s_branch .Lre%=\n\t"
            ".Lrp%=:\n\t"
            "v_add_u32 v236, s19, v252\n\t"
            "ds_write_b128 v236, v[244:247]\n\t"
            "v_add_u32 v236, s48, v244\n\t"
            "v_add_u32 v237, s48, v245\n\t"
            "v_add_u32 v248, s48, v246\n\t"
            "ds_read_b128 v[200:203], v236\n\t"
            "ds_read_b128 v[204:207], v237\n\t"
            "ds_read_b128 v[208:211], v248\n\t"
            "ds_read_b128 v[212:215], v236 offset:6144\n\t"
            "ds_read_b128 v[216:219], v237 offset:6144\n\t"
            "ds_read_b128 v[220:223], v248 offset:6144\n\t"
            "ds_read_b128 v[224:227], v236 offset:12288\n\t"
            "ds_read_b128 v[228:231], v237 offset:12288\n\t"
            "ds_read_b128 v[232:235], v248 offset:12288\n\t"
            "s_waitcnt lgkmcnt(0)\n\t"
            "s_barrier\n\t"
            "s_add_u32 s45, s46, s27\n\t"
            "s_mov_b32 s46, s47\n\t"
            "s_mov_b32 s47, s68\n\t"
            "s_xor_b32 s48, s48, s71\n\t"
            "s_xor_b32 s67, s67, s71\n\t"
            "s_add_u32 s76, s76, 4\n\t"
            "s_lshl_b32 s56, s56, 2\n\t"
            "s_lshr_b32 s49, s26, 30\n\t"
            "s_or_b32 s56, s56, s49\n\t"
            "s_add_u32 s49, s33, 0\n\t"
            "buffer_load_dwordx4 v[0:3], v247, s[28:31], s49 offen sc1\n\t"
            "s_add_u32 s49, s33, 1024\n\t"
            "buffer_load_dwordx4 v[4:7], v247, s[28:31], s49 offen sc1\n\t"
            "s_add_u32 s49, s33, 2048\n\t"
            "buffer_load_dwordx4 v[8:11], v247, s[28:31], s49 offen sc1\n\t"
            "s_add_u32 s49, s33, 3072\n\t"
            "buffer_load_dwordx4 v[12:15], v247, s[28:31], s49 offen sc1\n\t"
            "s_add_u32 s49, s33, 4096\n\t"
            "buffer_load_dwordx4 v[16:19], v247, s[28:31], s49 offen sc1\n\t"
            "s_add_u32 s49, s33, 5120\n\t"
            "buffer_load_dwordx4 v[20:23], v247, s[28:31], s49 offen sc1\n\t"
            "s_add_u32 s49, s33, 6144\n\t"
            "buffer_load_dwordx4 v[24:27], v247, s[28:31], s49 offen sc1\n\t"
            "s_add_u32 s49, s33, 7168\n\t"
            "buffer_load_dwordx4 v[28:31], v247, s[28:31], s49 offen sc1\n\t"
            "s_add_u32 s49, s33, 8192\n\t"
            "buffer_load_dwordx4 v[32:35], v247, s[28:31], s49 offen sc1\n\t"
            "s_add_u32 s49, s33, 9216\n\t"
            "buffer_load_dwordx4 v[36:39], v247, s[28:31], s49 offen sc1\n\t"
            "s_add_u32 s49, s33, 10240\n\t"
            "buffer_load_dwordx4 v[40:43], v247, s[28:31], s49 offen sc1\n\t"
            "s_add_u32 s49, s33, 11264\n\t"
            "buffer_load_dwordx4 v[44:47], v247, s[28:31], s49 offen sc1\n\t"
            "s_add_u32 s49, s33, 12288\n\t"
            "buffer_load_dwordx4 v[48:51], v247, s[28:31], s49 offen sc1\n\t"
            "s_add_u32 s49, s33, 13312\n\t"
            "buffer_load_dwordx4 v[52:55], v247, s[28:31], s49 offen sc1\n\t"
            "s_add_u32 s49, s33, 14336\n\t"
            "buffer_load_dwordx4 v[56:59], v247, s[28:31], s49 offen sc1\n\t"
            "s_add_u32 s49, s33, 15360\n\t"
            "buffer_load_dwordx4 v[60:63], v247, s[28:31], s49 offen sc1\n\t"
            "s_add_u32 s49, s33, 16384\n\t"
            "buffer_load_dwordx4 v[64:67], v247, s[28:31], s49 offen sc1\n\t"
            "s_add_u32 s49, s33, 17408\n\t"
            "buffer_load_dwordx4 v[68:71], v247, s[28:31], s49 offen sc1\n\t"
            "v_mov_b32 v249, s76\n\t"
            "ds_read_b32 v249, v249\n\t"
            "s_waitcnt lgkmcnt(0)\n\t"
            "v_readfirstlane_b32 s44, v249\n\t"
            "s_and_b32 s70, s44, 0x7ffffff\n\t"
            "s_add_u32 s70, s70, s77\n\t"
            "s_and_b32 s44, s44, 0x08000000\n\t"
            "s_or_b32 s44, s44, s39\n\t"
            "ds_read_b32 v249, v252 offset:1028\n\t"
            "s_waitcnt lgkmcnt(0)\n\t"
            "v_and_b32 v249, s44, v249\n\t"
            "s_mov_b32 m0, s32\n\t"
            "s_add_u32 s49, s70, s25\n\t"
            "buffer_load_dword v249, s[72:75], s70 offen lds\n\t"
            "buffer_load_dword v249, s[72:75], s49 offen lds\n\t"
            "ds_read_b128 v[168:171], v254\n\t"
            "ds_read_b128 v[172:175], v254 offset:64\n\t"
            "s_waitcnt vmcnt(2)\n\t"
            "ds_read_b64 v[144:145], v253\n\t"
            "ds_read_b64 v[146:147], v253 offset:96\n\t"
            "ds_read_b64 v[148:149], v253 offset:1744\n\t"
            "ds_read_b64 v[150:151], v253 offset:1840\n\t"
            "ds_read_b64 v[152:153], v253 offset:8\n\t"
            "ds_read_b64 v[154:155], v253 offset:104\n\t"
            "ds_read_b64 v[156:157], v253 offset:1752\n\t"
            "ds_read_b64 v[158:159], v253 offset:1848\n\t"
            "ds_read_b64 v[160:161], v253 offset:16\n\t"
            "ds_read_b64 v[162:163], v253 offset:112\n\t"
            "ds_read_b64 v[164:165], v253 offset:1760\n\t"
            "ds_read_b64 v[166:167], v253 offset:1856\n\t"
            "s_waitcnt lgkmcnt(0)\n\t"
            "s_nop 7\n\t"
            ".p2align 6\n\t"
            ".Lwb_loop%=:\n\t"
            "s_setprio 0\n\t"
            "s_bfe_u32 s92, s56, 0x20004\n\t"
            "s_lshl_b32 s93, s92, 3\n\t"
            "s_lshr_b32 s93, s55, s93\n\t"
            "s_lshl_b64 s[94:95], s[34:35], s93\n\t"
            "s_bitcmp1_b32 s92, 1\n\t"
            "s_cselect_b32 s93, 1177344, 0\n\t"
            "s_bitcmp1_b32 s92, 0\n\t"
            "s_cselect_b32 s92, 2304, 0\n\t"
            "s_add_u32 s92, s92, s93\n\t"
            "v_mov_b64 v[176:177], 0\n\t"
            "v_mov_b64 v[178:179], 0\n\t"
            "v_mov_b64 v[180:181], 0\n\t"
            "v_mov_b64 v[182:183], 0\n\t"
            "v_mov_b32 v249, s76\n\t"
            "v_mov_b64 v[186:187], 0\n\t"
            "s_waitcnt lgkmcnt(0)\n\t"
            "v_smfmac_f32_16x16x64_bf16 v[176:179], v[168:171], v[144:151], v255\n\t"
            "v_smfmac_f32_16x16x64_bf16 v[180:183], v[172:175], v[152:159], v255\n\t"
            "ds_read_b64 v[148:149], v253 offset:1768\n\t"
            "v_mov_b64 v[190:191], 0\n\t"
            "v_mov_b64 v[188:189], 0\n\t"
            "v_mov_b64 v[184:185], 0\n\t"
            "v_add_u32 v236, s67, v250\n\t"
            "v_add_u32 v248, s67, v251\n\t"
            "ds_read_b64 v[150:151], v253 offset:1864\n\t"
            "ds_read_b128 v[168:171], v254 offset:128\n\t"
            "v_cvt_pk_bf16_f32 v176, v176, v177\n\t"
            "v_cvt_pk_bf16_f32 v177, v178, v179\n\t"
            "v_cvt_pk_bf16_f32 v178, v180, v181\n\t"
            "v_cvt_pk_bf16_f32 v179, v182, v183\n\t"
            "v_mov_b64 v[180:181], 0\n\t"
            "v_mov_b64 v[182:183], 0\n\t"
            "ds_read_b64 v[146:147], v253 offset:120\n\t"
            "ds_read_b64 v[144:145], v253 offset:24\n\t"
            "ds_read_b128 v[172:175], v254 offset:192\n\t"
            "ds_read_b32 v249, v249\n\t"
            "ds_read_b64 v[158:159], v253 offset:1872\n\t"
            "ds_read_b64 v[156:157], v253 offset:1776\n\t"
            "ds_read_b64 v[152:153], v253 offset:32\n\t"
            "ds_read_b64 v[154:155], v253 offset:128\n\t"
            "s_waitcnt lgkmcnt(5)\n\t"
            "v_smfmac_f32_16x16x64_bf16 v[184:187], v[168:171], v[160:167], v255\n\t"
            "v_smfmac_f32_16x16x64_bf16 v[188:191], v[172:175], v[144:151], v255\n\t"
            "s_waitcnt lgkmcnt(4)\n\t"
            "ds_read_b128 v[168:171], v254 offset:256\n\t"
            "v_readfirstlane_b32 s26, v249\n\t"
            "v_mfma_f32_4x4x4_16b_bf16 v[196:199], v[176:177], v[176:177], 0\n\t"
            "v_mfma_f32_4x4x4_16b_bf16 v[192:195], v[238:239], v[176:177], 0\n\t"
            "s_nop 0\n\t"
            "v_mfma_f32_4x4x4_16b_bf16 v[196:199], v[178:179], v[178:179], v[196:199]\n\t"
            "v_mfma_f32_4x4x4_16b_bf16 v[192:195], v[238:239], v[178:179], v[192:195]\n\t"
            "ds_read_b64 v[160:161], v253 offset:40\n\t"
            "s_and_b32 s68, s26, 0x7ffffff\n\t"
            "s_add_u32 s68, s68, s78\n\t"
            "s_and_b32 s69, s26, 0x38000000\n\t"
            "s_or_b32 s69, s69, s39\n\t"
            "s_bitcmp1_b32 s26, 27\n\t"
            "s_cselect_b32 s79, 2.0, s68\n\t"
            "ds_read_b64 v[164:165], v253 offset:1784\n\t"
            "s_and_b32 s57, s56, 3\n\t"
            "s_lshl_b32 s58, s57, 3\n\t"
            "s_lshr_b32 m0, s54, s58\n\t"
            "ds_read_b64 v[166:167], v253 offset:1880\n\t"
            "s_lshl_b32 s58, s57, 4\n\t"
            "s_lshr_b64 s[36:37], s[50:51], s58\n\t"
            "s_and_b32 s57, s36, 0xffff\n\t"
            "s_add_u32 s57, s57, s67\n\t"
            "ds_read_b128 v[172:175], v254 offset:320\n\t"
            "s_lshr_b64 s[36:37], s[52:53], s58\n\t"
            "s_and_b32 s58, s36, 0xffff\n\t"
            "s_add_u32 s58, s58, s67\n\t"
            "ds_read_b64 v[162:163], v253 offset:136\n\t"
            "s_and_b32 s59, s50, 0xffff\n\t"
            "s_add_u32 s59, s59, s67\n\t"
            "s_and_b32 s38, s52, 0xffff\n\t"
            "s_add_u32 s38, s38, s67\n\t"
            "ds_read_b64 v[144:145], v253 offset:48\n\t"
            "v_writelane_b32 v236, s59, m0\n\t"
            "v_writelane_b32 v236, s57, 63\n\t"
            "v_cvt_pk_bf16_f32 v184, v184, v185\n\t"
            "v_cvt_pk_bf16_f32 v185, v186, v187\n\t"
            "v_cvt_pk_bf16_f32 v187, v190, v191\n\t"
            "v_cvt_pk_bf16_f32 v186, v188, v189\n\t"
            "v_xor_b32 v237, 16, v236\n\t"
            "v_mov_b64 v[188:189], 0\n\t"
            "v_mov_b64 v[190:191], 0\n\t"
            "v_writelane_b32 v248, s38, m0\n\t"
            "v_writelane_b32 v248, s58, 63\n\t"
            "ds_read_b64 v[148:149], v253 offset:1792\n\t"
            "ds_read_b64 v[146:147], v253 offset:144\n\t"
            "ds_read_b64 v[150:151], v253 offset:1888\n\t"
            "ds_read_b32 v249, v252 offset:1024\n\t"
            "s_waitcnt lgkmcnt(14)\n\t"
            "ds_write_b128 v236, v[176:179]\n\t"
            "v_mov_b64 v[176:177], 0\n\t"
            "v_mov_b64 v[178:179], 0\n\t"
            "s_waitcnt lgkmcnt(6)\n\t"
            "s_nop 0\n\t"
            "v_smfmac_f32_16x16x64_bf16 v[176:179], v[168:171], v[152:159], v255\n\t"
            "v_smfmac_f32_16x16x64_bf16 v[180:183], v[172:175], v[160:167], v255\n\t"
            "ds_read_b64 v[158:159], v253 offset:1896\n\t"
            "ds_read_b64 v[154:155], v253 offset:152\n\t"
            "v_mfma_f32_4x4x4_16b_bf16 v[196:199], v[184:185], v[184:185], v[196:199]\n\t"
            "v_mfma_f32_4x4x4_16b_bf16 v[192:195], v[238:239], v[184:185], v[192:195]\n\t"
            "s_nop 0\n\t"
            "v_mfma_f32_4x4x4_16b_bf16 v[196:199], v[186:187], v[186:187], v[196:199]\n\t"
            "v_mfma_f32_4x4x4_16b_bf16 v[192:195], v[238:239], v[186:187], v[192:195]\n\t"
            "ds_read_b128 v[172:175], v254 offset:448\n\t"
            "v_cvt_pk_bf16_f32 v176, v176, v177\n\t"
            "v_cvt_pk_bf16_f32 v177, v178, v179\n\t"
            "v_cvt_pk_bf16_f32 v178, v180, v181\n\t"
            "v_cvt_pk_bf16_f32 v179, v182, v183\n\t"
            "v_mov_b64 v[180:181], 0\n\t"
            "v_mov_b64 v[182:183], 0\n\t"
            "s_barrier\n\t"
            "s_setprio 3\n\t"
            "s_waitcnt lgkmcnt(4)\n\t"
            "v_and_b32 v249, s69, v249\n\t"
            "ds_read_b128 v[168:171], v254 offset:384\n\t"
            "ds_read_b64 v[156:157], v253 offset:1800\n\t"
            "ds_read_b64 v[152:153], v253 offset:56\n\t"
            "ds_read_b64 v[164:165], v253 offset:1808\n\t"
            "ds_read_b64 v[166:167], v253 offset:1904\n\t"
            "ds_read_b64 v[162:163], v253 offset:160\n\t"
            "ds_read_b64 v[160:161], v253 offset:64\n\t"
            "ds_write_b128 v237, v[184:187]\n\t"
            "v_mov_b64 v[184:185], 0\n\t"
            "v_mov_b64 v[186:187], 0\n\t"
            "ds_write_b128 v236, v[176:179] offset:128\n\t"
            "s_waitcnt lgkmcnt(6)\n\t"
            "v_smfmac_f32_16x16x64_bf16 v[184:187], v[168:171], v[144:151], v255\n\t"
            "v_smfmac_f32_16x16x64_bf16 v[188:191], v[172:175], v[152:159], v255\n\t"
            "ds_read_b64 v[144:145], v253 offset:72\n\t"
            "ds_read_b64 v[148:149], v253 offset:1816\n\t"
            "v_mfma_f32_4x4x4_16b_bf16 v[196:199], v[176:177], v[176:177], v[196:199]\n\t"
            "v_mfma_f32_4x4x4_16b_bf16 v[192:195], v[238:239], v[176:177], v[192:195]\n\t"
            "s_nop 0\n\t"
            "v_mfma_f32_4x4x4_16b_bf16 v[196:199], v[178:179], v[178:179], v[196:199]\n\t"
            "v_mfma_f32_4x4x4_16b_bf16 v[192:195], v[238:239], v[178:179], v[192:195]\n\t"
            "ds_read_b64 v[150:151], v253 offset:1912\n\t"
            "v_mov_b64 v[176:177], 0\n\t"
            "v_mov_b64 v[178:179], 0\n\t"
            "v_cvt_pk_bf16_f32 v184, v184, v185\n\t"
            "v_cvt_pk_bf16_f32 v185, v186, v187\n\t"
            "v_cvt_pk_bf16_f32 v186, v188, v189\n\t"
            "v_cvt_pk_bf16_f32 v187, v190, v191\n\t"
            "v_mov_b64 v[190:191], 0\n\t"
            "v_mov_b64 v[188:189], 0\n\t"
            "ds_read_b128 v[168:171], v254 offset:512\n\t"
            "ds_read_b128 v[172:175], v254 offset:576\n\t"
            "ds_read_b64 v[146:147], v253 offset:168\n\t"
            "ds_read_b64 v[154:155], v253 offset:176\n\t"
            "ds_read_b64 v[152:153], v253 offset:80\n\t"
            "ds_read_b64 v[156:157], v253 offset:1824\n\t"
            "s_waitcnt lgkmcnt(14)\n\t"
            "ds_read_b64 v[158:159], v253 offset:1920\n\t"
            "s_waitcnt lgkmcnt(14)\n\t"
            "ds_write_b128 v237, v[184:187] offset:128\n\t"
            "s_waitcnt lgkmcnt(5)\n\t"
            "v_smfmac_f32_16x16x64_bf16 v[176:179], v[168:171], v[160:167], v255\n\t"
            "v_smfmac_f32_16x16x64_bf16 v[180:183], v[172:175], v[144:151], v255\n\t"
            "ds_read_b64 v[166:167], v253 offset:1928\n\t"
            "ds_read_b64 v[160:161], v253 offset:88\n\t"
            "v_mfma_f32_4x4x4_16b_bf16 v[196:199], v[184:185], v[184:185], v[196:199]\n\t"
            "v_mfma_f32_4x4x4_16b_bf16 v[192:195], v[238:239], v[184:185], v[192:195]\n\t"
            "s_nop 0\n\t"
            "v_mfma_f32_4x4x4_16b_bf16 v[196:199], v[186:187], v[186:187], v[196:199]\n\t"
            "v_mfma_f32_4x4x4_16b_bf16 v[192:195], v[238:239], v[186:187], v[192:195]\n\t"
            "ds_read_b128 v[172:175], v254 offset:704\n\t"
            "v_mov_b64 v[186:187], 0\n\t"
            "v_mov_b64 v[184:185], 0\n\t"
            "v_cvt_pk_bf16_f32 v176, v176, v177\n\t"
            "v_cvt_pk_bf16_f32 v177, v178, v179\n\t"
            "v_cvt_pk_bf16_f32 v178, v180, v181\n\t"
            "v_cvt_pk_bf16_f32 v179, v182, v183\n\t"
            "v_and_b32 v183, 0xffff0000, v203\n\t"
            "v_and_b32 v181, 0xffff0000, v202\n\t"
            "v_lshlrev_b32 v182, 16, v203\n\t"
            "v_lshlrev_b32 v180, 16, v202\n\t"
            "ds_read_b64 v[164:165], v253 offset:1832\n\t"
            "ds_read_b128 v[168:171], v254 offset:640\n\t"
            "ds_read_b64 v[162:163], v253 offset:184\n\t"
            "ds_read_b128 v[144:147], v242\n\t"
            "ds_read_b128 v[148:151], v243\n\t"
            "ds_write_b128 v236, v[176:179] offset:256\n\t"
            "v_add_u32 v236, s48, v244\n\t"
            "s_waitcnt lgkmcnt(3)\n\t"
            "v_smfmac_f32_16x16x64_bf16 v[184:187], v[168:171], v[152:159], v255\n\t"
            "v_smfmac_f32_16x16x64_bf16 v[188:191], v[172:175], v[160:167], v255\n\t"
            "ds_read_b128 v[152:155], v242 offset:128\n\t"
            "ds_read_b128 v[160:163], v242 offset:256\n\t"
            "v_mfma_f32_4x4x4_16b_bf16 v[196:199], v[176:177], v[176:177], v[196:199]\n\t"
            "v_mfma_f32_4x4x4_16b_bf16 v[192:195], v[238:239], v[176:177], v[192:195]\n\t"
            "s_nop 0\n\t"
            "v_mfma_f32_4x4x4_16b_bf16 v[196:199], v[178:179], v[178:179], v[196:199]\n\t"
            "v_mfma_f32_4x4x4_16b_bf16 v[192:195], v[238:239], v[178:179], v[192:195]\n\t"
            "ds_read_b128 v[156:159], v243 offset:128\n\t"
            "v_cvt_pk_bf16_f32 v184, v184, v185\n\t"
            "v_lshlrev_b32 v176, 16, v200\n\t"
            "v_cvt_pk_bf16_f32 v185, v186, v187\n\t"
            "v_cvt_pk_bf16_f32 v187, v190, v191\n\t"
            "v_cvt_pk_bf16_f32 v186, v188, v189\n\t"
            "v_lshlrev_b32 v178, 16, v201\n\t"
            "v_and_b32 v179, 0xffff0000, v201\n\t"
            "v_mfma_f32_4x4x4_16b_bf16 v[196:199], v[184:185], v[184:185], v[196:199]\n\t"
            "v_mfma_f32_4x4x4_16b_bf16 v[192:195], v[238:239], v[184:185], v[192:195]\n\t"
            "s_nop 0\n\t"
            "v_mfma_f32_4x4x4_16b_bf16 v[196:199], v[186:187], v[186:187], v[196:199]\n\t"
            "v_mfma_f32_4x4x4_16b_bf16 v[192:195], v[238:239], v[186:187], v[192:195]\n\t"
            "ds_read_b128 v[164:167], v243 offset:256\n\t"
            "v_and_b32 v177, 0xffff0000, v200\n\t"
            "v_lshlrev_b32 v190, 16, v207\n\t"
            "v_and_b32 v191, 0xffff0000, v207\n\t"
            "s_waitcnt lgkmcnt(6)\n\t"
            "v_mfma_f32_16x16x32_bf16 v[176:179], v[0:3], v[144:147], v[176:179]\n\t"
            "s_setprio 3\n\t"
            "ds_read_b128 v[168:171], v242 offset:6144\n\t"
            "v_and_b32 v189, 0xffff0000, v206\n\t"
            "s_waitcnt lgkmcnt(6)\n\t"
            "v_mfma_f32_16x16x32_bf16 v[176:179], v[4:7], v[148:151], v[176:179]\n\t"
            "s_waitcnt lgkmcnt(4)\n\t"
            "ds_read_b128 v[172:175], v243 offset:6144\n\t"
            "v_lshlrev_b32 v188, 16, v206\n\t"
            "v_mfma_f32_16x16x32_bf16 v[176:179], v[8:11], v[152:155], v[176:179]\n\t"
            "s_mov_b32 exec_hi, 0x3fffff\n\t"
            "s_add_u32 m0, s66, 0\n\t"
            "s_add_u32 s49, s79, 0\n\t"
            "buffer_load_dwordx4 v249, s[72:75], s49 offen lds\n\t"
            "s_mov_b32 exec_hi, -1\n\t"
            "s_mov_b32 exec_hi, 0x3fffff\n\t"
            "s_add_u32 m0, s66, 872\n\t"
            "s_add_u32 s49, s68, 196224\n\t"
            "buffer_load_dwordx4 v249, s[72:75], s49 offen lds\n\t"
            "s_mov_b32 exec_hi, -1\n\t"
            "ds_write_b128 v237, v[184:187] offset:256\n\t"
            "s_waitcnt lgkmcnt(4)\n\t"
            "v_mfma_f32_16x16x32_bf16 v[176:179], v[12:15], v[156:159], v[176:179]\n\t"
            "ds_read_b128 v[200:203], v236\n\t"
            "v_and_b32 v185, 0xffff0000, v204\n\t"
            "v_mfma_f32_16x16x32_bf16 v[176:179], v[16:19], v[160:163], v[176:179]\n\t"
            "s_waitcnt lgkmcnt(4)\n\t"
            "v_lshlrev_b32 v184, 16, v204\n\t"
            "v_lshlrev_b32 v186, 16, v205\n\t"
            "v_mfma_f32_16x16x32_bf16 v[176:179], v[20:23], v[164:167], v[176:179]\n\t"
            "s_mov_b32 exec_hi, 0x3fffff\n\t"
            "s_add_u32 m0, s66, 1744\n\t"
            "s_add_u32 s49, s68, 392448\n\t"
            "buffer_load_dwordx4 v249, s[72:75], s49 offen lds\n\t"
            "s_mov_b32 exec_hi, -1\n\t"
            "s_mov_b32 exec_hi, 0x3fffff\n\t"
            "s_add_u32 m0, s66, 2616\n\t"
            "s_add_u32 s49, s68, 588672\n\t"
            "buffer_load_dwordx4 v249, s[72:75], s49 offen lds\n\t"
            "s_mov_b32 exec_hi, -1\n\t"
            "v_mfma_f32_16x16x32_bf16 v[180:183], v[24:27], v[144:147], v[180:183]\n\t"
            "v_and_b32 v187, 0xffff0000, v205\n\t"
            "v_cndmask_b32_e64 v193, v196, v197, s[60:61]\n\t"
            "v_mfma_f32_16x16x32_bf16 v[180:183], v[28:31], v[148:151], v[180:183]\n\t"
            "v_cndmask_b32_e64 v193, v193, v198, s[62:63]\n\t"
            "v_cndmask_b32_e64 v193, v193, v199, s[64:65]\n\t"
            "v_mfma_f32_16x16x32_bf16 v[180:183], v[32:35], v[152:155], v[180:183]\n\t"
            "ds_read_b128 v[196:199], v252\n\t"
            "v_lshlrev_b32 v194, 16, v209\n\t"
            "v_mfma_f32_16x16x32_bf16 v[180:183], v[36:39], v[156:159], v[180:183]\n\t"
            "v_and_b32 v195, 0xffff0000, v209\n\t"
            "v_cvt_pk_bf16_f32 v176, v176, v177\n\t"
            "v_mfma_f32_16x16x32_bf16 v[180:183], v[40:43], v[160:163], v[180:183]\n\t"
            "v_cvt_pk_bf16_f32 v177, v178, v179\n\t"
            "v_add_u32 v237, s48, v245\n\t"
            "v_mfma_f32_16x16x32_bf16 v[180:183], v[44:47], v[164:167], v[180:183]\n\t"
            "s_mov_b32 exec_hi, 0x3fffff\n\t"
            "s_add_u32 m0, s66, 3488\n\t"
            "s_add_u32 s49, s68, 784896\n\t"
            "buffer_load_dwordx4 v249, s[72:75], s49 offen lds\n\t"
            "s_mov_b32 exec_hi, -1\n\t"
            "s_mov_b32 exec_hi, 0x3fffff\n\t"
            "s_add_u32 m0, s66, 4360\n\t"
            "s_add_u32 s49, s68, 981120\n\t"
            "buffer_load_dwordx4 v249, s[72:75], s49 offen lds\n\t"
            "s_mov_b32 exec_hi, -1\n\t"
            "ds_write_b64 v248, v[192:193] offset:18816\n\t"
            "v_mfma_f32_16x16x32_bf16 v[184:187], v[48:51], v[144:147], v[184:187]\n\t"
            "ds_read_b128 v[204:207], v237\n\t"
            "v_lshlrev_b32 v192, 16, v208\n\t"
            "v_mfma_f32_16x16x32_bf16 v[184:187], v[52:55], v[148:151], v[184:187]\n\t"
            "v_and_b32 v193, 0xffff0000, v208\n\t"
            "v_cvt_pk_bf16_f32 v179, v182, v183\n\t"
            "v_mfma_f32_16x16x32_bf16 v[184:187], v[56:59], v[152:155], v[184:187]\n\t"
            "v_cvt_pk_bf16_f32 v178, v180, v181\n\t"
            "v_and_b32 v183, 0xffff0000, v213\n\t"
            "v_mfma_f32_16x16x32_bf16 v[184:187], v[60:63], v[156:159], v[184:187]\n\t"
            "v_and_b32 v181, 0xffff0000, v212\n\t"
            "v_lshlrev_b32 v182, 16, v213\n\t"
            "v_mfma_f32_16x16x32_bf16 v[184:187], v[64:67], v[160:163], v[184:187]\n\t"
            "v_lshlrev_b32 v180, 16, v212\n\t"
            "v_add_u32 v248, s48, v246\n\t"
            "v_mfma_f32_16x16x32_bf16 v[184:187], v[68:71], v[164:167], v[184:187]\n\t"
            "s_mov_b32 exec_hi, 0x3fffff\n\t"
            "s_add_u32 m0, s66, 5232\n\t"
            "s_add_u32 s49, s68, 1177344\n\t"
            "buffer_load_dwordx4 v249, s[72:75], s49 offen lds\n\t"
            "s_mov_b32 exec_hi, -1\n\t"
            "s_mov_b32 exec_hi, 0x3fffff\n\t"
            "s_add_u32 m0, s66, 6104\n\t"
            "s_add_u32 s49, s68, 1373568\n\t"
            "buffer_load_dwordx4 v249, s[72:75], s49 offen lds\n\t"
            "s_mov_b32 exec_hi, -1\n\t"
            "v_mfma_f32_16x16x32_bf16 v[188:191], v[72:75], v[144:147], v[188:191]\n\t"
            "v_cvt_pk_bf16_f32 v184, v184, v185\n\t"
            "v_cvt_pk_bf16_f32 v185, v186, v187\n\t"
            "v_mfma_f32_16x16x32_bf16 v[188:191], v[76:79], v[148:151], v[188:191]\n\t"
            "v_mfma_f32_16x16x32_bf16 v[188:191], v[80:83], v[152:155], v[188:191]\n\t"
            "v_mfma_f32_16x16x32_bf16 v[188:191], v[84:87], v[156:159], v[188:191]\n\t"
            "v_mfma_f32_16x16x32_bf16 v[188:191], v[88:91], v[160:163], v[188:191]\n\t"
            "v_mfma_f32_16x16x32_bf16 v[188:191], v[92:95], v[164:167], v[188:191]\n\t"
            "s_mov_b32 exec_hi, 0x3fffff\n\t"
            "s_add_u32 m0, s66, 6976\n\t"
            "s_add_u32 s49, s68, 1569792\n\t"
            "buffer_load_dwordx4 v249, s[72:75], s49 offen lds\n\t"
            "s_mov_b32 exec_hi, -1\n\t"
            "v_mfma_f32_16x16x32_bf16 v[192:195], v[96:99], v[144:147], v[192:195]\n\t"
            "v_mov_b32 v249, s76\n\t"
            "v_mfma_f32_16x16x32_bf16 v[192:195], v[100:103], v[148:151], v[192:195]\n\t"
            "ds_read_b32 v249, v249 offset:4\n\t"
            "v_cvt_pk_bf16_f32 v186, v188, v189\n\t"
            "v_mfma_f32_16x16x32_bf16 v[192:195], v[104:107], v[152:155], v[192:195]\n\t"
            "v_cvt_pk_bf16_f32 v187, v190, v191\n\t"
            "v_and_b32 v191, 0xffff0000, v217\n\t"
            "v_mfma_f32_16x16x32_bf16 v[192:195], v[108:111], v[156:159], v[192:195]\n\t"
            "v_and_b32 v189, 0xffff0000, v216\n\t"
            "v_lshlrev_b32 v190, 16, v217\n\t"
            "v_mfma_f32_16x16x32_bf16 v[192:195], v[112:115], v[160:163], v[192:195]\n\t"
            "v_lshlrev_b32 v188, 16, v216\n\t"
            "v_mfma_f32_16x16x32_bf16 v[192:195], v[116:119], v[164:167], v[192:195]\n\t"
            "s_waitcnt lgkmcnt(3)\n\t"
            "s_mov_b64 exec, s[94:95]\n\t"
            "v_add_u32 v196, s92, v196\n\t"
            "s_mov_b64 exec, -1\n\t"
            "buffer_store_dwordx4 v[176:179], v196, s[40:43], s45 offen sc0 sc1\n\t"
            "buffer_store_dwordx4 v[184:187], v196, s[40:43], s45 offen offset:64 sc0 sc1\n\t"
            "s_waitcnt lgkmcnt(0)\n\t"
            "v_readfirstlane_b32 s44, v249\n\t"
            "v_and_b32 v179, 0xffff0000, v211\n\t"
            "v_lshlrev_b32 v176, 16, v210\n\t"
            "v_lshlrev_b32 v178, 16, v211\n\t"
            "v_and_b32 v177, 0xffff0000, v210\n\t"
            "v_and_b32 v185, 0xffff0000, v214\n\t"
            "v_and_b32 v187, 0xffff0000, v215\n\t"
            "v_mfma_f32_16x16x32_bf16 v[176:179], v[120:123], v[144:147], v[176:179]\n\t"
            "ds_read_b128 v[144:147], v242 offset:6272\n\t"
            "v_lshlrev_b32 v184, 16, v214\n\t"
            "v_mfma_f32_16x16x32_bf16 v[176:179], v[124:127], v[148:151], v[176:179]\n\t"
            "ds_read_b128 v[148:151], v243 offset:6272\n\t"
            "v_lshlrev_b32 v186, 16, v215\n\t"
            "v_mfma_f32_16x16x32_bf16 v[176:179], v[128:131], v[152:155], v[176:179]\n\t"
            "ds_read_b128 v[152:155], v242 offset:6400\n\t"
            "v_cvt_pk_bf16_f32 v192, v192, v193\n\t"
            "v_mfma_f32_16x16x32_bf16 v[176:179], v[132:135], v[156:159], v[176:179]\n\t"
            "ds_read_b128 v[156:159], v243 offset:6400\n\t"
            "v_cvt_pk_bf16_f32 v193, v194, v195\n\t"
            "v_mfma_f32_16x16x32_bf16 v[176:179], v[136:139], v[160:163], v[176:179]\n\t"
            "ds_read_b128 v[160:163], v242 offset:12288\n\t"
            "v_mfma_f32_16x16x32_bf16 v[176:179], v[140:143], v[164:167], v[176:179]\n\t"
            "s_and_b32 s70, s44, 0x7ffffff\n\t"
            "s_add_u32 s70, s70, s77\n\t"
            "s_and_b32 s44, s44, 0x08000000\n\t"
            "s_or_b32 s44, s44, s39\n\t"
            "v_mfma_f32_16x16x32_bf16 v[180:183], v[0:3], v[168:171], v[180:183]\n\t"
            "ds_read_b128 v[164:167], v243 offset:12288\n\t"
            "v_mfma_f32_16x16x32_bf16 v[180:183], v[4:7], v[172:175], v[180:183]\n\t"
            "s_waitcnt lgkmcnt(5)\n\t"
            "ds_read_b32 v249, v252 offset:1028\n\t"
            "v_cvt_pk_bf16_f32 v195, v178, v179\n\t"
            "v_mfma_f32_16x16x32_bf16 v[180:183], v[8:11], v[144:147], v[180:183]\n\t"
            "s_waitcnt lgkmcnt(5)\n\t"
            "ds_read_b128 v[208:211], v248\n\t"
            "v_cvt_pk_bf16_f32 v194, v176, v177\n\t"
            "v_mfma_f32_16x16x32_bf16 v[180:183], v[12:15], v[148:151], v[180:183]\n\t"
            "s_waitcnt lgkmcnt(5)\n\t"
            "ds_read_b128 v[212:215], v236 offset:6144\n\t"
            "v_lshlrev_b32 v176, 16, v220\n\t"
            "v_mfma_f32_16x16x32_bf16 v[180:183], v[16:19], v[152:155], v[180:183]\n\t"
            "s_waitcnt lgkmcnt(5)\n\t"
            "v_lshlrev_b32 v178, 16, v221\n\t"
            "v_and_b32 v179, 0xffff0000, v221\n\t"
            "v_mfma_f32_16x16x32_bf16 v[180:183], v[20:23], v[156:159], v[180:183]\n\t"
            "buffer_store_dwordx4 v[192:195], v196, s[40:43], s45 offen offset:128 sc0 sc1\n\t"
            "v_mfma_f32_16x16x32_bf16 v[184:187], v[24:27], v[168:171], v[184:187]\n\t"
            "s_nop 0\n\t"
            "v_and_b32 v195, 0xffff0000, v219\n\t"
            "v_and_b32 v193, 0xffff0000, v218\n\t"
            "v_mfma_f32_16x16x32_bf16 v[184:187], v[28:31], v[172:175], v[184:187]\n\t"
            "v_lshlrev_b32 v192, 16, v218\n\t"
            "v_lshlrev_b32 v194, 16, v219\n\t"
            "v_mfma_f32_16x16x32_bf16 v[184:187], v[32:35], v[144:147], v[184:187]\n\t"
            "ds_read_b128 v[216:219], v237 offset:6144\n\t"
            "v_and_b32 v177, 0xffff0000, v220\n\t"
            "v_mfma_f32_16x16x32_bf16 v[184:187], v[36:39], v[148:151], v[184:187]\n\t"
            "v_cvt_pk_bf16_f32 v180, v180, v181\n\t"
            "v_cvt_pk_bf16_f32 v181, v182, v183\n\t"
            "v_mfma_f32_16x16x32_bf16 v[184:187], v[40:43], v[152:155], v[184:187]\n\t"
            "s_waitcnt lgkmcnt(3)\n\t"
            "v_and_b32 v249, s44, v249\n\t"
            "v_mfma_f32_16x16x32_bf16 v[184:187], v[44:47], v[156:159], v[184:187]\n\t"
            "v_mfma_f32_16x16x32_bf16 v[188:191], v[48:51], v[168:171], v[188:191]\n\t"
            "v_mfma_f32_16x16x32_bf16 v[188:191], v[52:55], v[172:175], v[188:191]\n\t"
            "v_mfma_f32_16x16x32_bf16 v[188:191], v[56:59], v[144:147], v[188:191]\n\t"
            "v_mfma_f32_16x16x32_bf16 v[188:191], v[60:63], v[148:151], v[188:191]\n\t"
            "v_mfma_f32_16x16x32_bf16 v[188:191], v[64:67], v[152:155], v[188:191]\n\t"
            "v_mfma_f32_16x16x32_bf16 v[188:191], v[68:71], v[156:159], v[188:191]\n\t"
            "s_nop 1\n\t"
            "v_cvt_pk_bf16_f32 v183, v186, v187\n\t"
            "v_mfma_f32_16x16x32_bf16 v[192:195], v[72:75], v[168:171], v[192:195]\n\t"
            "v_cvt_pk_bf16_f32 v182, v184, v185\n\t"
            "v_and_b32 v185, 0xffff0000, v224\n\t"
            "v_mfma_f32_16x16x32_bf16 v[192:195], v[76:79], v[172:175], v[192:195]\n\t"
            "v_lshlrev_b32 v186, 16, v225\n\t"
            "v_lshlrev_b32 v184, 16, v224\n\t"
            "v_mfma_f32_16x16x32_bf16 v[192:195], v[80:83], v[144:147], v[192:195]\n\t"
            "v_and_b32 v187, 0xffff0000, v225\n\t"
            "v_cvt_pk_bf16_f32 v188, v188, v189\n\t"
            "v_mfma_f32_16x16x32_bf16 v[192:195], v[84:87], v[148:151], v[192:195]\n\t"
            "v_cvt_pk_bf16_f32 v189, v190, v191\n\t"
            "v_mfma_f32_16x16x32_bf16 v[192:195], v[88:91], v[152:155], v[192:195]\n\t"
            "v_mfma_f32_16x16x32_bf16 v[192:195], v[92:95], v[156:159], v[192:195]\n\t"
            "buffer_store_dwordx4 v[180:183], v197, s[40:43], s45 offen sc0 sc1\n\t"
            "v_mfma_f32_16x16x32_bf16 v[176:179], v[96:99], v[168:171], v[176:179]\n\t"
            "s_nop 0\n\t"
            "v_lshlrev_b32 v182, 16, v223\n\t"
            "v_and_b32 v181, 0xffff0000, v222\n\t"
            "v_mfma_f32_16x16x32_bf16 v[176:179], v[100:103], v[172:175], v[176:179]\n\t"
            "v_lshlrev_b32 v180, 16, v222\n\t"
            "v_and_b32 v183, 0xffff0000, v223\n\t"
            "v_mfma_f32_16x16x32_bf16 v[176:179], v[104:107], v[144:147], v[176:179]\n\t"
            "ds_read_b128 v[220:223], v248 offset:6144\n\t"
            "v_cvt_pk_bf16_f32 v190, v192, v193\n\t"
            "v_mfma_f32_16x16x32_bf16 v[176:179], v[108:111], v[148:151], v[176:179]\n\t"
            "v_cvt_pk_bf16_f32 v191, v194, v195\n\t"
            "v_lshlrev_b32 v192, 16, v228\n\t"
            "v_mfma_f32_16x16x32_bf16 v[176:179], v[112:115], v[152:155], v[176:179]\n\t"
            "v_and_b32 v195, 0xffff0000, v229\n\t"
            "v_and_b32 v193, 0xffff0000, v228\n\t"
            "v_mfma_f32_16x16x32_bf16 v[176:179], v[116:119], v[156:159], v[176:179]\n\t"
            "buffer_store_dwordx4 v[188:191], v197, s[40:43], s45 offen offset:64 sc0 sc1\n\t"
            "v_mfma_f32_16x16x32_bf16 v[180:183], v[120:123], v[168:171], v[180:183]\n\t"
            "ds_read_b128 v[168:171], v242 offset:12416\n\t"
            "v_lshlrev_b32 v188, 16, v226\n\t"
            "v_mfma_f32_16x16x32_bf16 v[180:183], v[124:127], v[172:175], v[180:183]\n\t"
            "ds_read_b128 v[172:175], v243 offset:12416\n\t"
            "v_and_b32 v191, 0xffff0000, v227\n\t"
            "v_mfma_f32_16x16x32_bf16 v[180:183], v[128:131], v[144:147], v[180:183]\n\t"
            "ds_read_b128 v[144:147], v242 offset:12544\n\t"
            "v_and_b32 v189, 0xffff0000, v226\n\t"
            "v_mfma_f32_16x16x32_bf16 v[180:183], v[132:135], v[148:151], v[180:183]\n\t"
            "ds_read_b128 v[148:151], v243 offset:12544\n\t"
            "v_lshlrev_b32 v190, 16, v227\n\t"
            "v_mfma_f32_16x16x32_bf16 v[180:183], v[136:139], v[152:155], v[180:183]\n\t"
            "ds_read_b128 v[224:227], v236 offset:12288\n\t"
            "v_lshlrev_b32 v194, 16, v229\n\t"
            "v_mfma_f32_16x16x32_bf16 v[180:183], v[140:143], v[156:159], v[180:183]\n\t"
            "v_and_b32 v153, 0xffff0000, v230\n\t"
            "v_lshlrev_b32 v152, 16, v230\n\t"
            "v_mfma_f32_16x16x32_bf16 v[184:187], v[0:3], v[160:163], v[184:187]\n\t"
            "v_and_b32 v155, 0xffff0000, v231\n\t"
            "v_lshlrev_b32 v154, 16, v231\n\t"
            "v_mfma_f32_16x16x32_bf16 v[184:187], v[4:7], v[164:167], v[184:187]\n\t"
            "s_waitcnt lgkmcnt(4)\n\t"
            "ds_read_b128 v[228:231], v237 offset:12288\n\t"
            "v_lshlrev_b32 v156, 16, v232\n\t"
            "v_lshlrev_b32 v158, 16, v233\n\t"
            "v_mfma_f32_16x16x32_bf16 v[184:187], v[8:11], v[168:171], v[184:187]\n\t"
            "s_waitcnt lgkmcnt(4)\n\t"
            "v_and_b32 v159, 0xffff0000, v233\n\t"
            "v_and_b32 v157, 0xffff0000, v232\n\t"
            "v_mfma_f32_16x16x32_bf16 v[184:187], v[12:15], v[172:175], v[184:187]\n\t"
            "s_waitcnt lgkmcnt(3)\n\t"
            "v_cvt_pk_bf16_f32 v176, v176, v177\n\t"
            "v_cvt_pk_bf16_f32 v177, v178, v179\n\t"
            "v_mfma_f32_16x16x32_bf16 v[184:187], v[16:19], v[144:147], v[184:187]\n\t"
            "s_waitcnt lgkmcnt(2)\n\t"
            "v_cvt_pk_bf16_f32 v179, v182, v183\n\t"
            "v_lshlrev_b32 v182, 16, v235\n\t"
            "v_mfma_f32_16x16x32_bf16 v[184:187], v[20:23], v[148:151], v[184:187]\n\t"
            "v_and_b32 v183, 0xffff0000, v235\n\t"
            "v_cvt_pk_bf16_f32 v178, v180, v181\n\t"
            "buffer_store_dwordx4 v[176:179], v197, s[40:43], s45 offen offset:128 sc0 sc1\n\t"
            "v_mfma_f32_16x16x32_bf16 v[188:191], v[24:27], v[160:163], v[188:191]\n\t"
            "v_lshlrev_b32 v180, 16, v234\n\t"
            "v_and_b32 v181, 0xffff0000, v234\n\t"
            "v_mfma_f32_16x16x32_bf16 v[188:191], v[28:31], v[164:167], v[188:191]\n\t"
            "ds_read_b128 v[232:235], v248 offset:12288\n\t"
            "v_cvt_pk_bf16_f32 v184, v184, v185\n\t"
            "v_mfma_f32_16x16x32_bf16 v[188:191], v[32:35], v[168:171], v[188:191]\n\t"
            "s_waitcnt lgkmcnt(0)\n\t"
            "s_barrier\n\t"
            "v_cvt_pk_bf16_f32 v185, v186, v187\n\t"
            "v_mfma_f32_16x16x32_bf16 v[188:191], v[36:39], v[172:175], v[188:191]\n\t"
            "v_mfma_f32_16x16x32_bf16 v[188:191], v[40:43], v[144:147], v[188:191]\n\t"
            "v_mfma_f32_16x16x32_bf16 v[188:191], v[44:47], v[148:151], v[188:191]\n\t"
            "v_mfma_f32_16x16x32_bf16 v[192:195], v[48:51], v[160:163], v[192:195]\n\t"
            "v_mfma_f32_16x16x32_bf16 v[192:195], v[52:55], v[164:167], v[192:195]\n\t"
            "v_mfma_f32_16x16x32_bf16 v[192:195], v[56:59], v[168:171], v[192:195]\n\t"
            "v_mfma_f32_16x16x32_bf16 v[192:195], v[60:63], v[172:175], v[192:195]\n\t"
            "v_mfma_f32_16x16x32_bf16 v[192:195], v[64:67], v[144:147], v[192:195]\n\t"
            "v_mfma_f32_16x16x32_bf16 v[192:195], v[68:71], v[148:151], v[192:195]\n\t"
            "s_nop 1\n\t"
            "v_cvt_pk_bf16_f32 v187, v190, v191\n\t"
            "v_mfma_f32_16x16x32_bf16 v[152:155], v[72:75], v[160:163], v[152:155]\n\t"
            "v_cvt_pk_bf16_f32 v186, v188, v189\n\t"
            "v_mfma_f32_16x16x32_bf16 v[152:155], v[76:79], v[164:167], v[152:155]\n\t"
            "v_mfma_f32_16x16x32_bf16 v[152:155], v[80:83], v[168:171], v[152:155]\n\t"
            "v_mfma_f32_16x16x32_bf16 v[152:155], v[84:87], v[172:175], v[152:155]\n\t"
            "v_cvt_pk_bf16_f32 v192, v192, v193\n\t"
            "v_mfma_f32_16x16x32_bf16 v[152:155], v[88:91], v[144:147], v[152:155]\n\t"
            "v_cvt_pk_bf16_f32 v193, v194, v195\n\t"
            "v_mfma_f32_16x16x32_bf16 v[152:155], v[92:95], v[148:151], v[152:155]\n\t"
            "buffer_store_dwordx4 v[184:187], v198, s[40:43], s45 offen sc0 sc1\n\t"
            "v_mfma_f32_16x16x32_bf16 v[156:159], v[96:99], v[160:163], v[156:159]\n\t"
            "v_mfma_f32_16x16x32_bf16 v[156:159], v[100:103], v[164:167], v[156:159]\n\t"
            "v_mfma_f32_16x16x32_bf16 v[156:159], v[104:107], v[168:171], v[156:159]\n\t"
            "v_mfma_f32_16x16x32_bf16 v[156:159], v[108:111], v[172:175], v[156:159]\n\t"
            "v_mfma_f32_16x16x32_bf16 v[156:159], v[112:115], v[144:147], v[156:159]\n\t"
            "s_nop 1\n\t"
            "v_cvt_pk_bf16_f32 v194, v152, v153\n\t"
            "v_mfma_f32_16x16x32_bf16 v[156:159], v[116:119], v[148:151], v[156:159]\n\t"
            "v_cvt_pk_bf16_f32 v195, v154, v155\n\t"
            "buffer_store_dwordx4 v[192:195], v198, s[40:43], s45 offen offset:64 sc0 sc1\n\t"
            "v_mfma_f32_16x16x32_bf16 v[180:183], v[120:123], v[160:163], v[180:183]\n\t"
            "s_waitcnt vmcnt(8)\n\t"
            "ds_read_b64 v[162:163], v253 offset:112\n\t"
            "v_mfma_f32_16x16x32_bf16 v[180:183], v[124:127], v[164:167], v[180:183]\n\t"
            "ds_read_b64 v[160:161], v253 offset:16\n\t"
            "v_mfma_f32_16x16x32_bf16 v[180:183], v[128:131], v[168:171], v[180:183]\n\t"
            "ds_read_b64 v[166:167], v253 offset:1856\n\t"
            "v_cvt_pk_bf16_f32 v156, v156, v157\n\t"
            "v_mfma_f32_16x16x32_bf16 v[180:183], v[132:135], v[172:175], v[180:183]\n\t"
            "ds_read_b128 v[172:175], v254 offset:64\n\t"
            "v_cvt_pk_bf16_f32 v157, v158, v159\n\t"
            "v_mfma_f32_16x16x32_bf16 v[180:183], v[136:139], v[144:147], v[180:183]\n\t"
            "ds_read_b64 v[152:153], v253 offset:8\n\t"
            "v_mfma_f32_16x16x32_bf16 v[180:183], v[140:143], v[148:151], v[180:183]\n\t"
            "s_setprio 0\n\t"
            "ds_read_b64 v[164:165], v253 offset:1760\n\t"
            "ds_read_b64 v[144:145], v253\n\t"
            "ds_read_b64 v[146:147], v253 offset:96\n\t"
            "ds_read_b64 v[148:149], v253 offset:1744\n\t"
            "ds_read_b64 v[154:155], v253 offset:104\n\t"
            "ds_read_b128 v[168:171], v254\n\t"
            "ds_read_b64 v[150:151], v253 offset:1840\n\t"
            "v_cvt_pk_bf16_f32 v159, v182, v183\n\t"
            "v_cvt_pk_bf16_f32 v158, v180, v181\n\t"
            "buffer_store_dwordx4 v[156:159], v198, s[40:43], s45 offen offset:128 sc0 sc1\n\t"
            "s_mov_b32 m0, s32\n\t"
            "s_add_u32 s49, s70, s25\n\t"
            "buffer_load_dword v249, s[72:75], s70 offen lds\n\t"
            "buffer_load_dword v249, s[72:75], s49 offen lds\n\t"
            "ds_read_b64 v[156:157], v253 offset:1752\n\t"
            "s_add_u32 s45, s46, s27\n\t"
            "s_mov_b32 s46, s47\n\t"
            "s_mov_b32 s47, s68\n\t"
            "ds_read_b64 v[158:159], v253 offset:1848\n\t"
            "s_xor_b32 s48, s48, s71\n\t"
            "s_xor_b32 s67, s67, s71\n\t"
            "s_add_u32 s76, s76, 4\n\t"
            "s_lshl_b32 s56, s56, 2\n\t"
            "s_lshr_b32 s49, s26, 30\n\t"
            "s_or_b32 s56, s56, s49\n\t"
            "s_cmp_lt_u32 s46, 0x7000000\n\t"
            "s_cbranch_scc1 .Lwb_loop%=\n\t"
            "s_waitcnt lgkmcnt(0)\n\t"
            "s_nop 7\n\t"
            "s_setprio 3\n\t"
            "s_nop 7\n\t"
            "ds_read_b128 v[196:199], v252\n\t"
            "s_bfe_u32 s57, s56, 0x20004\n\t"
            "s_lshl_b32 s58, s57, 3\n\t"
            "s_lshr_b32 s58, s55, s58\n\t"
            "s_lshl_b64 s[36:37], s[34:35], s58\n\t"
            "s_bitcmp1_b32 s57, 1\n\t"
            "s_cselect_b32 s58, 1177344, 0\n\t"
            "s_bitcmp1_b32 s57, 0\n\t"
            "s_cselect_b32 s57, 2304, 0\n\t"
            "s_add_u32 s57, s57, s58\n\t"
            "s_waitcnt lgkmcnt(0)\n\t"
            "s_mov_b64 exec, s[36:37]\n\t"
            "v_add_u32 v196, s57, v196\n\t"
            "s_mov_b64 exec, -1\n\t"
            "s_nop 7\n\t"
            "v_mov_b32 v144, s23\n\t"
            ".Ldp%=:\n\t"
            "ds_read_b128 v[148:151], v144 offset:96\n\t"
            "s_waitcnt lgkmcnt(0)\n\t"
            "v_or_b32 v148, v148, v149\n\t"
            "v_or3_b32 v148, v148, v150, v151\n\t"
            "v_cmp_gt_i32 vcc, 0, v148\n\t"
            "s_nop 4\n\t"
            "s_cbranch_vccnz .Ldp%=\n\t"
            "s_nop 7\n\t"
            "ds_read_b128 v[184:187], v242\n\t"
            "v_lshlrev_b32 v152, 16, v200\n\t"
            "v_and_b32 v153, 0xffff0000, v200\n\t"
            "v_lshlrev_b32 v154, 16, v201\n\t"
            "v_and_b32 v155, 0xffff0000, v201\n\t"
            "v_lshlrev_b32 v156, 16, v202\n\t"
            "v_and_b32 v157, 0xffff0000, v202\n\t"
            "v_lshlrev_b32 v158, 16, v203\n\t"
            "v_and_b32 v159, 0xffff0000, v203\n\t"
            "v_lshlrev_b32 v160, 16, v204\n\t"
            "v_and_b32 v161, 0xffff0000, v204\n\t"
            "v_lshlrev_b32 v162, 16, v205\n\t"
            "v_and_b32 v163, 0xffff0000, v205\n\t"
            "v_lshlrev_b32 v164, 16, v206\n\t"
            "v_and_b32 v165, 0xffff0000, v206\n\t"
            "v_lshlrev_b32 v166, 16, v207\n\t"
            "v_and_b32 v167, 0xffff0000, v207\n\t"
            "v_lshlrev_b32 v176, 16, v208\n\t"
            "v_and_b32 v177, 0xffff0000, v208\n\t"
            "v_lshlrev_b32 v178, 16, v209\n\t"
            "v_and_b32 v179, 0xffff0000, v209\n\t"
            "v_lshlrev_b32 v180, 16, v210\n\t"
            "v_and_b32 v181, 0xffff0000, v210\n\t"
            "v_lshlrev_b32 v182, 16, v211\n\t"
            "v_and_b32 v183, 0xffff0000, v211\n\t"
            "v_lshlrev_b32 v144, 16, v212\n\t"
            "v_and_b32 v145, 0xffff0000, v212\n\t"
            "v_lshlrev_b32 v146, 16, v213\n\t"
            "v_and_b32 v147, 0xffff0000, v213\n\t"
            "v_lshlrev_b32 v148, 16, v214\n\t"
            "v_and_b32 v149, 0xffff0000, v214\n\t"
            "s_waitcnt lgkmcnt(0)\n\t"
            "v_mfma_f32_16x16x32_bf16 v[152:155], v[0:3], v[184:187], v[152:155]\n\t"
            "ds_read_b128 v[188:191], v243\n\t"
            "v_lshlrev_b32 v150, 16, v215\n\t"
            "v_mfma_f32_16x16x32_bf16 v[156:159], v[24:27], v[184:187], v[156:159]\n\t"
            "ds_read_b128 v[192:195], v242 offset:128\n\t"
            "v_and_b32 v151, 0xffff0000, v215\n\t"
            "v_mfma_f32_16x16x32_bf16 v[160:163], v[48:51], v[184:187], v[160:163]\n\t"
            "v_lshlrev_b32 v168, 16, v216\n\t"
            "v_and_b32 v169, 0xffff0000, v216\n\t"
            "v_mfma_f32_16x16x32_bf16 v[164:167], v[72:75], v[184:187], v[164:167]\n\t"
            "v_lshlrev_b32 v170, 16, v217\n\t"
            "v_and_b32 v171, 0xffff0000, v217\n\t"
            "v_mfma_f32_16x16x32_bf16 v[176:179], v[96:99], v[184:187], v[176:179]\n\t"
            "v_lshlrev_b32 v172, 16, v218\n\t"
            "v_and_b32 v173, 0xffff0000, v218\n\t"
            "v_mfma_f32_16x16x32_bf16 v[180:183], v[120:123], v[184:187], v[180:183]\n\t"
            "s_waitcnt lgkmcnt(1)\n\t"
            "ds_read_b128 v[184:187], v243 offset:128\n\t"
            "v_lshlrev_b32 v174, 16, v219\n\t"
            "v_and_b32 v175, 0xffff0000, v219\n\t"
            "v_mfma_f32_16x16x32_bf16 v[152:155], v[4:7], v[188:191], v[152:155]\n\t"
            "v_mfma_f32_16x16x32_bf16 v[156:159], v[28:31], v[188:191], v[156:159]\n\t"
            "v_mfma_f32_16x16x32_bf16 v[160:163], v[52:55], v[188:191], v[160:163]\n\t"
            "v_mfma_f32_16x16x32_bf16 v[164:167], v[76:79], v[188:191], v[164:167]\n\t"
            "v_mfma_f32_16x16x32_bf16 v[176:179], v[100:103], v[188:191], v[176:179]\n\t"
            "v_mfma_f32_16x16x32_bf16 v[180:183], v[124:127], v[188:191], v[180:183]\n\t"
            "s_waitcnt lgkmcnt(1)\n\t"
            "ds_read_b128 v[188:191], v242 offset:256\n\t"
            "v_mfma_f32_16x16x32_bf16 v[152:155], v[8:11], v[192:195], v[152:155]\n\t"
            "v_mfma_f32_16x16x32_bf16 v[156:159], v[32:35], v[192:195], v[156:159]\n\t"
            "v_mfma_f32_16x16x32_bf16 v[160:163], v[56:59], v[192:195], v[160:163]\n\t"
            "v_mfma_f32_16x16x32_bf16 v[164:167], v[80:83], v[192:195], v[164:167]\n\t"
            "v_mfma_f32_16x16x32_bf16 v[176:179], v[104:107], v[192:195], v[176:179]\n\t"
            "v_mfma_f32_16x16x32_bf16 v[180:183], v[128:131], v[192:195], v[180:183]\n\t"
            "s_waitcnt lgkmcnt(1)\n\t"
            "ds_read_b128 v[192:195], v243 offset:256\n\t"
            "v_mfma_f32_16x16x32_bf16 v[152:155], v[12:15], v[184:187], v[152:155]\n\t"
            "v_mfma_f32_16x16x32_bf16 v[156:159], v[36:39], v[184:187], v[156:159]\n\t"
            "v_mfma_f32_16x16x32_bf16 v[160:163], v[60:63], v[184:187], v[160:163]\n\t"
            "v_mfma_f32_16x16x32_bf16 v[164:167], v[84:87], v[184:187], v[164:167]\n\t"
            "v_mfma_f32_16x16x32_bf16 v[176:179], v[108:111], v[184:187], v[176:179]\n\t"
            "v_mfma_f32_16x16x32_bf16 v[180:183], v[132:135], v[184:187], v[180:183]\n\t"
            "s_waitcnt lgkmcnt(1)\n\t"
            "ds_read_b128 v[184:187], v242 offset:6144\n\t"
            "v_mfma_f32_16x16x32_bf16 v[152:155], v[16:19], v[188:191], v[152:155]\n\t"
            "v_mfma_f32_16x16x32_bf16 v[156:159], v[40:43], v[188:191], v[156:159]\n\t"
            "v_mfma_f32_16x16x32_bf16 v[160:163], v[64:67], v[188:191], v[160:163]\n\t"
            "v_mfma_f32_16x16x32_bf16 v[164:167], v[88:91], v[188:191], v[164:167]\n\t"
            "v_mfma_f32_16x16x32_bf16 v[176:179], v[112:115], v[188:191], v[176:179]\n\t"
            "v_mfma_f32_16x16x32_bf16 v[180:183], v[136:139], v[188:191], v[180:183]\n\t"
            "s_waitcnt lgkmcnt(1)\n\t"
            "ds_read_b128 v[188:191], v243 offset:6144\n\t"
            "v_mfma_f32_16x16x32_bf16 v[152:155], v[20:23], v[192:195], v[152:155]\n\t"
            "v_mfma_f32_16x16x32_bf16 v[156:159], v[44:47], v[192:195], v[156:159]\n\t"
            "v_mfma_f32_16x16x32_bf16 v[160:163], v[68:71], v[192:195], v[160:163]\n\t"
            "v_mfma_f32_16x16x32_bf16 v[164:167], v[92:95], v[192:195], v[164:167]\n\t"
            "v_mfma_f32_16x16x32_bf16 v[176:179], v[116:119], v[192:195], v[176:179]\n\t"
            "v_mfma_f32_16x16x32_bf16 v[180:183], v[140:143], v[192:195], v[180:183]\n\t"
            "s_waitcnt lgkmcnt(1)\n\t"
            "ds_read_b128 v[192:195], v242 offset:6272\n\t"
            "v_mfma_f32_16x16x32_bf16 v[144:147], v[0:3], v[184:187], v[144:147]\n\t"
            "v_cvt_pk_bf16_f32 v152, v152, v153\n\t"
            "v_cvt_pk_bf16_f32 v153, v154, v155\n\t"
            "v_mfma_f32_16x16x32_bf16 v[148:151], v[24:27], v[184:187], v[148:151]\n\t"
            "v_cvt_pk_bf16_f32 v160, v160, v161\n\t"
            "v_cvt_pk_bf16_f32 v161, v162, v163\n\t"
            "v_mfma_f32_16x16x32_bf16 v[168:171], v[48:51], v[184:187], v[168:171]\n\t"
            "v_cvt_pk_bf16_f32 v154, v156, v157\n\t"
            "v_cvt_pk_bf16_f32 v155, v158, v159\n\t"
            "v_mfma_f32_16x16x32_bf16 v[172:175], v[72:75], v[184:187], v[172:175]\n\t"
            "s_waitcnt lgkmcnt(1)\n\t"
            "v_cvt_pk_bf16_f32 v162, v164, v165\n\t"
            "v_cvt_pk_bf16_f32 v163, v166, v167\n\t"
            "v_mfma_f32_16x16x32_bf16 v[144:147], v[4:7], v[188:191], v[144:147]\n\t"
            "v_lshlrev_b32 v156, 16, v220\n\t"
            "v_and_b32 v157, 0xffff0000, v220\n\t"
            "v_mfma_f32_16x16x32_bf16 v[148:151], v[28:31], v[188:191], v[148:151]\n\t"
            "v_lshlrev_b32 v158, 16, v221\n\t"
            "v_and_b32 v159, 0xffff0000, v221\n\t"
            "v_mfma_f32_16x16x32_bf16 v[168:171], v[52:55], v[188:191], v[168:171]\n\t"
            "v_lshlrev_b32 v164, 16, v222\n\t"
            "v_and_b32 v165, 0xffff0000, v222\n\t"
            "v_mfma_f32_16x16x32_bf16 v[156:159], v[96:99], v[184:187], v[156:159]\n\t"
            "v_lshlrev_b32 v166, 16, v223\n\t"
            "v_and_b32 v167, 0xffff0000, v223\n\t"
            "v_mfma_f32_16x16x32_bf16 v[172:175], v[76:79], v[188:191], v[172:175]\n\t"
            "buffer_store_dwordx4 v[152:155], v196, s[40:43], s45 offen sc0 sc1\n\t"
            "v_mfma_f32_16x16x32_bf16 v[164:167], v[120:123], v[184:187], v[164:167]\n\t"
            "ds_read_b128 v[184:187], v243 offset:6272\n\t"
            "v_cvt_pk_bf16_f32 v176, v176, v177\n\t"
            "v_mfma_f32_16x16x32_bf16 v[156:159], v[100:103], v[188:191], v[156:159]\n\t"
            "buffer_store_dwordx4 v[160:163], v196, s[40:43], s45 offen offset:64 sc0 sc1\n\t"
            "v_mfma_f32_16x16x32_bf16 v[164:167], v[124:127], v[188:191], v[164:167]\n\t"
            "s_waitcnt lgkmcnt(1)\n\t"
            "ds_read_b128 v[188:191], v242 offset:6400\n\t"
            "v_cvt_pk_bf16_f32 v177, v178, v179\n\t"
            "v_mfma_f32_16x16x32_bf16 v[144:147], v[8:11], v[192:195], v[144:147]\n\t"
            "v_cvt_pk_bf16_f32 v178, v180, v181\n\t"
            "v_cvt_pk_bf16_f32 v179, v182, v183\n\t"
            "v_mfma_f32_16x16x32_bf16 v[148:151], v[32:35], v[192:195], v[148:151]\n\t"
            "buffer_store_dwordx4 v[176:179], v196, s[40:43], s45 offen offset:128 sc0 sc1\n\t"
            "v_mfma_f32_16x16x32_bf16 v[168:171], v[56:59], v[192:195], v[168:171]\n\t"
            "v_lshlrev_b32 v152, 16, v224\n\t"
            "v_and_b32 v153, 0xffff0000, v224\n\t"
            "v_mfma_f32_16x16x32_bf16 v[172:175], v[80:83], v[192:195], v[172:175]\n\t"
            "v_lshlrev_b32 v154, 16, v225\n\t"
            "v_and_b32 v155, 0xffff0000, v225\n\t"
            "v_mfma_f32_16x16x32_bf16 v[156:159], v[104:107], v[192:195], v[156:159]\n\t"
            "v_lshlrev_b32 v180, 16, v226\n\t"
            "v_and_b32 v181, 0xffff0000, v226\n\t"
            "v_mfma_f32_16x16x32_bf16 v[164:167], v[128:131], v[192:195], v[164:167]\n\t"
            "s_waitcnt lgkmcnt(1)\n\t"
            "ds_read_b128 v[192:195], v243 offset:6400\n\t"
            "v_lshlrev_b32 v182, 16, v227\n\t"
            "v_mfma_f32_16x16x32_bf16 v[144:147], v[12:15], v[184:187], v[144:147]\n\t"
            "v_and_b32 v183, 0xffff0000, v227\n\t"
            "v_lshlrev_b32 v160, 16, v228\n\t"
            "v_mfma_f32_16x16x32_bf16 v[148:151], v[36:39], v[184:187], v[148:151]\n\t"
            "s_waitcnt lgkmcnt(1)\n\t"
            "v_and_b32 v161, 0xffff0000, v228\n\t"
            "v_lshlrev_b32 v162, 16, v229\n\t"
            "v_mfma_f32_16x16x32_bf16 v[144:147], v[16:19], v[188:191], v[144:147]\n\t"
            "v_and_b32 v163, 0xffff0000, v229\n\t"
            "v_lshlrev_b32 v176, 16, v230\n\t"
            "v_mfma_f32_16x16x32_bf16 v[168:171], v[60:63], v[184:187], v[168:171]\n\t"
            "v_and_b32 v177, 0xffff0000, v230\n\t"
            "v_lshlrev_b32 v178, 16, v231\n\t"
            "v_mfma_f32_16x16x32_bf16 v[148:151], v[40:43], v[188:191], v[148:151]\n\t"
            "v_and_b32 v179, 0xffff0000, v231\n\t"
            "v_mfma_f32_16x16x32_bf16 v[172:175], v[84:87], v[184:187], v[172:175]\n\t"
            "v_mfma_f32_16x16x32_bf16 v[168:171], v[64:67], v[188:191], v[168:171]\n\t"
            "v_mfma_f32_16x16x32_bf16 v[156:159], v[108:111], v[184:187], v[156:159]\n\t"
            "s_waitcnt lgkmcnt(0)\n\t"
            "v_mfma_f32_16x16x32_bf16 v[144:147], v[20:23], v[192:195], v[144:147]\n\t"
            "v_mfma_f32_16x16x32_bf16 v[148:151], v[44:47], v[192:195], v[148:151]\n\t"
            "v_mfma_f32_16x16x32_bf16 v[164:167], v[132:135], v[184:187], v[164:167]\n\t"
            "ds_read_b128 v[184:187], v242 offset:12288\n\t"
            "v_mfma_f32_16x16x32_bf16 v[172:175], v[88:91], v[188:191], v[172:175]\n\t"
            "v_mfma_f32_16x16x32_bf16 v[168:171], v[68:71], v[192:195], v[168:171]\n\t"
            "v_mfma_f32_16x16x32_bf16 v[156:159], v[112:115], v[188:191], v[156:159]\n\t"
            "s_nop 1\n\t"
            "v_cvt_pk_bf16_f32 v144, v144, v145\n\t"
            "v_mfma_f32_16x16x32_bf16 v[164:167], v[136:139], v[188:191], v[164:167]\n\t"
            "ds_read_b128 v[188:191], v243 offset:12288\n\t"
            "v_cvt_pk_bf16_f32 v145, v146, v147\n\t"
            "v_mfma_f32_16x16x32_bf16 v[172:175], v[92:95], v[192:195], v[172:175]\n\t"
            "v_cvt_pk_bf16_f32 v146, v148, v149\n\t"
            "v_cvt_pk_bf16_f32 v147, v150, v151\n\t"
            "v_mfma_f32_16x16x32_bf16 v[156:159], v[116:119], v[192:195], v[156:159]\n\t"
            "buffer_store_dwordx4 v[144:147], v197, s[40:43], s45 offen sc0 sc1\n\t"
            "v_mfma_f32_16x16x32_bf16 v[164:167], v[140:143], v[192:195], v[164:167]\n\t"
            "s_waitcnt lgkmcnt(1)\n\t"
            "ds_read_b128 v[192:195], v242 offset:12416\n\t"
            "v_cvt_pk_bf16_f32 v168, v168, v169\n\t"
            "v_mfma_f32_16x16x32_bf16 v[152:155], v[0:3], v[184:187], v[152:155]\n\t"
            "v_cvt_pk_bf16_f32 v169, v170, v171\n\t"
            "v_cvt_pk_bf16_f32 v170, v172, v173\n\t"
            "v_mfma_f32_16x16x32_bf16 v[180:183], v[24:27], v[184:187], v[180:183]\n\t"
            "v_cvt_pk_bf16_f32 v171, v174, v175\n\t"
            "v_cvt_pk_bf16_f32 v156, v156, v157\n\t"
            "v_mfma_f32_16x16x32_bf16 v[160:163], v[48:51], v[184:187], v[160:163]\n\t"
            "buffer_store_dwordx4 v[168:171], v197, s[40:43], s45 offen offset:64 sc0 sc1\n\t"
            "v_mfma_f32_16x16x32_bf16 v[176:179], v[72:75], v[184:187], v[176:179]\n\t"
            "s_waitcnt lgkmcnt(1)\n\t"
            "v_cvt_pk_bf16_f32 v157, v158, v159\n\t"
            "v_cvt_pk_bf16_f32 v158, v164, v165\n\t"
            "v_mfma_f32_16x16x32_bf16 v[152:155], v[4:7], v[188:191], v[152:155]\n\t"
            "v_cvt_pk_bf16_f32 v159, v166, v167\n\t"
            "v_lshlrev_b32 v164, 16, v232\n\t"
            "v_mfma_f32_16x16x32_bf16 v[180:183], v[28:31], v[188:191], v[180:183]\n\t"
            "buffer_store_dwordx4 v[156:159], v197, s[40:43], s45 offen offset:128 sc0 sc1\n\t"
            "v_mfma_f32_16x16x32_bf16 v[160:163], v[52:55], v[188:191], v[160:163]\n\t"
            "v_and_b32 v165, 0xffff0000, v232\n\t"
            "v_lshlrev_b32 v166, 16, v233\n\t"
            "v_mfma_f32_16x16x32_bf16 v[176:179], v[76:79], v[188:191], v[176:179]\n\t"
            "s_waitcnt lgkmcnt(0)\n\t"
            "v_and_b32 v167, 0xffff0000, v233\n\t"
            "v_lshlrev_b32 v156, 16, v234\n\t"
            "v_mfma_f32_16x16x32_bf16 v[152:155], v[8:11], v[192:195], v[152:155]\n\t"
            "v_and_b32 v157, 0xffff0000, v234\n\t"
            "v_lshlrev_b32 v158, 16, v235\n\t"
            "v_mfma_f32_16x16x32_bf16 v[164:167], v[96:99], v[184:187], v[164:167]\n\t"
            "v_and_b32 v159, 0xffff0000, v235\n\t"
            "v_mfma_f32_16x16x32_bf16 v[164:167], v[100:103], v[188:191], v[164:167]\n\t"
            "s_nop 0\n\t"
            "v_mfma_f32_16x16x32_bf16 v[156:159], v[120:123], v[184:187], v[156:159]\n\t"
            "ds_read_b128 v[184:187], v243 offset:12416\n\t"
            "v_mfma_f32_16x16x32_bf16 v[156:159], v[124:127], v[188:191], v[156:159]\n\t"
            "ds_read_b128 v[188:191], v242 offset:12544\n\t"
            "v_mfma_f32_16x16x32_bf16 v[180:183], v[32:35], v[192:195], v[180:183]\n\t"
            "v_mfma_f32_16x16x32_bf16 v[160:163], v[56:59], v[192:195], v[160:163]\n\t"
            "v_mfma_f32_16x16x32_bf16 v[176:179], v[80:83], v[192:195], v[176:179]\n\t"
            "v_mfma_f32_16x16x32_bf16 v[164:167], v[104:107], v[192:195], v[164:167]\n\t"
            "v_mfma_f32_16x16x32_bf16 v[156:159], v[128:131], v[192:195], v[156:159]\n\t"
            "s_waitcnt lgkmcnt(1)\n\t"
            "ds_read_b128 v[192:195], v243 offset:12544\n\t"
            "v_mfma_f32_16x16x32_bf16 v[152:155], v[12:15], v[184:187], v[152:155]\n\t"
            "v_mfma_f32_16x16x32_bf16 v[180:183], v[36:39], v[184:187], v[180:183]\n\t"
            "s_waitcnt lgkmcnt(1)\n\t"
            "v_mfma_f32_16x16x32_bf16 v[152:155], v[16:19], v[188:191], v[152:155]\n\t"
            "v_mfma_f32_16x16x32_bf16 v[160:163], v[60:63], v[184:187], v[160:163]\n\t"
            "v_mfma_f32_16x16x32_bf16 v[180:183], v[40:43], v[188:191], v[180:183]\n\t"
            "v_mfma_f32_16x16x32_bf16 v[176:179], v[84:87], v[184:187], v[176:179]\n\t"
            "s_waitcnt lgkmcnt(0)\n\t"
            "v_mfma_f32_16x16x32_bf16 v[152:155], v[20:23], v[192:195], v[152:155]\n\t"
            "v_mfma_f32_16x16x32_bf16 v[160:163], v[64:67], v[188:191], v[160:163]\n\t"
            "v_mfma_f32_16x16x32_bf16 v[180:183], v[44:47], v[192:195], v[180:183]\n\t"
            "v_mfma_f32_16x16x32_bf16 v[164:167], v[108:111], v[184:187], v[164:167]\n\t"
            "v_mfma_f32_16x16x32_bf16 v[176:179], v[88:91], v[188:191], v[176:179]\n\t"
            "v_mfma_f32_16x16x32_bf16 v[156:159], v[132:135], v[184:187], v[156:159]\n\t"
            "v_mfma_f32_16x16x32_bf16 v[160:163], v[68:71], v[192:195], v[160:163]\n\t"
            "s_nop 1\n\t"
            "v_cvt_pk_bf16_f32 v152, v152, v153\n\t"
            "v_mfma_f32_16x16x32_bf16 v[164:167], v[112:115], v[188:191], v[164:167]\n\t"
            "v_cvt_pk_bf16_f32 v153, v154, v155\n\t"
            "v_mfma_f32_16x16x32_bf16 v[176:179], v[92:95], v[192:195], v[176:179]\n\t"
            "v_cvt_pk_bf16_f32 v154, v180, v181\n\t"
            "v_cvt_pk_bf16_f32 v155, v182, v183\n\t"
            "v_mfma_f32_16x16x32_bf16 v[156:159], v[136:139], v[188:191], v[156:159]\n\t"
            "buffer_store_dwordx4 v[152:155], v198, s[40:43], s45 offen sc0 sc1\n\t"
            "v_mfma_f32_16x16x32_bf16 v[164:167], v[116:119], v[192:195], v[164:167]\n\t"
            "v_cvt_pk_bf16_f32 v160, v160, v161\n\t"
            "v_cvt_pk_bf16_f32 v161, v162, v163\n\t"
            "v_mfma_f32_16x16x32_bf16 v[156:159], v[140:143], v[192:195], v[156:159]\n\t"
            "v_cvt_pk_bf16_f32 v162, v176, v177\n\t"
            "v_cvt_pk_bf16_f32 v163, v178, v179\n\t"
            "buffer_store_dwordx4 v[160:163], v198, s[40:43], s45 offen offset:64 sc0 sc1\n\t"
            "s_nop 1\n\t"
            "v_cvt_pk_bf16_f32 v164, v164, v165\n\t"
            "v_cvt_pk_bf16_f32 v165, v166, v167\n\t"
            "s_nop 0\n\t"
            "v_cvt_pk_bf16_f32 v166, v156, v157\n\t"
            "v_cvt_pk_bf16_f32 v167, v158, v159\n\t"
            "buffer_store_dwordx4 v[164:167], v198, s[40:43], s45 offen offset:128 sc0 sc1\n\t"
            "s_nop 7\n\t"
            ".Lre%=:\n\t"
            : "+{v[224:227]}"(f0), "+{v[228:231]}"(f1), "+{v[232:235]}"(f2), "+{v242}"(r242), "+{v243}"(r243), "+{v[244:247]}"(r244),
              "+{s45}"(s45), "+{s46}"(s46), "+{s47}"(s47), "+{s48}"(s48), "+{s67}"(s67), "+{s76}"(s76), "+{s56}"(s56), "+{s44}"(s44), "+{s70}"(s70), "+{s21}"(s21)
            : B_VIN, B_SIN
            : "v0", "v1", "v2", "v3", "v4", "v5", "v6", "v7", "v8", "v9", "v10", "v11", "v12", "v13", "v14", "v15", "v16", "v17", "v18", "v19", "v20", "v21", "v22", "v23", "v24", "v25", "v26", "v27", "v28", "v29", "v30", "v31", "v32", "v33", "v34", "v35", "v36", "v37", "v38", "v39", "v40", "v41", "v42", "v43", "v44", "v45", "v46", "v47", "v48", "v49", "v50", "v51", "v52", "v53", "v54", "v55", "v56", "v57", "v58", "v59", "v60", "v61", "v62", "v63", "v64", "v65", "v66", "v67", "v68", "v69", "v70", "v71", "v72", "v73", "v74", "v75", "v76", "v77", "v78", "v79", "v80", "v81", "v82", "v83", "v84", "v85", "v86", "v87", "v88", "v89", "v90", "v91", "v92", "v93", "v94", "v95", "v96", "v97", "v98", "v99", "v100", "v101", "v102", "v103", "v104", "v105", "v106", "v107", "v108", "v109", "v110", "v111", "v112", "v113", "v114", "v115", "v116", "v117", "v118", "v119", "v120", "v121", "v122", "v123", "v124", "v125", "v126", "v127", "v128", "v129", "v130", "v131", "v132", "v133", "v134", "v135", "v136", "v137", "v138", "v139", "v140", "v141", "v142", "v143", "v144", "v145", "v146", "v147", "v148", "v149", "v150", "v151", "v152", "v153", "v154", "v155", "v156", "v157", "v158", "v159", "v160", "v161", "v162", "v163", "v164", "v165", "v166", "v167", "v168", "v169", "v170", "v171", "v172", "v173", "v174", "v175", "v176", "v177", "v178", "v179", "v180", "v181", "v182", "v183", "v184", "v185", "v186", "v187", "v188", "v189", "v190", "v191", "v192", "v193", "v194", "v195", "v196", "v197", "v198", "v199", "v200", "v201", "v202", "v203", "v204", "v205", "v206", "v207", "v208", "v209", "v210", "v211", "v212", "v213", "v214", "v215", "v216", "v217", "v218", "v219", "v220", "v221", "v222", "v223", "v236", "v237", "v248", "v249", "s92", "s93", "s94", "s95", "s26", "s68", "s69", "s79", "s36", "s37", "s38", "s57", "s58", "s59", "s49", "vcc", "scc", "m0", "memory");
        }
        asm volatile("wk_code_end1:");
        asm volatile("wk_code_end2:");

    }
}

extern "C" void kernel_launch(void* const* d_in, const int* in_sizes, int n_in,
                              void* d_out, int out_size, void* d_ws, size_t ws_size,
                              hipStream_t stream) {
    const unsigned short* x = (const unsigned short*)d_in[0];
    const unsigned short* dw = (const unsigned short*)d_in[1];
    const unsigned short* wqkv = (const unsigned short*)d_in[2];
    const unsigned short* wo = (const unsigned short*)d_in[3];
    unsigned short* out = (unsigned short*)d_out;
    u32x4* wf = (u32x4*)d_ws;
    win_attn_kernel<<<GRID, 512, 0, stream>>>(x, dw, wqkv, wo, wf, out);
}
```
